```python
import torch

def _softmax(x, axis=-1):
    return torch.softmax(x, dim=axis)

def _gelu(x):
    return torch.nn.functional.gelu(x, approximate='tanh')

def _silu(x):
    return torch.nn.functional.silu(x)

def _sigmoid(x):
    return torch.sigmoid(x)

def _layernorm(x, axis=-1, eps=1e-05):
    m = x.mean(dim=axis, keepdim=True)
    v = x.var(dim=axis, keepdim=True, unbiased=False)
    return (x - m) / torch.sqrt(v + eps)

def _scatter_add(tgt, idx, src, axis=0):
    return tgt.clone().index_add_(axis, idx, src)

def _segment_sum(src, seg, n):
    out = torch.zeros((n,) + src.shape[1:], dtype=src.dtype)
    return out.index_add_(0, seg, src)

def _pad(x, pw):
    flat = tuple((v for pair in reversed(pw) for v in pair))
    return torch.nn.functional.pad(x, flat)
kernel_name = 'gated_mix__memory_aug_recurrent_mem_tr__ssm_conv_s4d__prompt'

def setup_inputs():

    _w = lambda a: (lambda t: t.to(torch.bfloat16) if t.is_floating_point() else t)(torch.as_tensor(a))

    def _A_setup():
        B, S, Dm = (1, 2048, 576)
        H, Dh = (32, 128)
        M, seg = (8, 32)
        seg = min(seg, S)
        S = S // seg * seg
        return {'x': _w((lambda *s: torch.randn(*s, dtype=torch.bfloat16) * 0.05)(B, S, Dm)), 'mem0': _w((lambda *s: torch.randn(*s, dtype=torch.bfloat16) * 0.4)(B, M, Dm)), 'Wq': _w((lambda *s: torch.randn(*s, dtype=torch.bfloat16) * 6.4)(Dm, H, Dh) * Dm ** (-0.5)), 'Wk': _w((lambda *s: torch.randn(*s, dtype=torch.bfloat16) * 6.4)(Dm, H, Dh) * Dm ** (-0.5)), 'Wv': _w((lambda *s: torch.randn(*s, dtype=torch.bfloat16) * 0.4)(Dm, H, Dh) * Dm ** (-0.5)), 'Wo': _w((lambda *s: torch.randn(*s, dtype=torch.bfloat16) * 0.4)(H, Dh, Dm) * (H * Dh) ** (-0.5)), 'seg_len': seg}
    _A_in = _A_setup()

    def _B_setup():
        Bsz, L, D, N = (1, 2048, 576, 128)
        return {'u': _w((lambda *s: torch.randn(*s, dtype=torch.bfloat16) * 0.05)(Bsz, L, D)), 'log_A_real': _w((lambda *s: torch.randn(*s, dtype=torch.bfloat16) * 6.4)(D, N) * 0.5), 'A_imag': _w((lambda *s: torch.randn(*s, dtype=torch.bfloat16) * 0.05)(D, N) * 3.14), 'C_real': _w((lambda *s: torch.randn(*s, dtype=torch.bfloat16) * 0.2)(D, N) / N ** 0.5), 'C_imag': _w((lambda *s: torch.randn(*s, dtype=torch.bfloat16) * 0.2)(D, N) / N ** 0.5), 'log_dt': _w((lambda *s: torch.randn(*s, dtype=torch.bfloat16) * 0.05)(D) * 0.5 - 4.0), 'D_skip': _w((lambda *s: torch.randn(*s, dtype=torch.bfloat16) * 0.05)(D))}
    _B_in = _B_setup()
    return {'A_x': _A_in['x'], 'A_mem0': _A_in['mem0'], 'A_Wq': _A_in['Wq'], 'A_Wk': _A_in['Wk'], 'A_Wv': _A_in['Wv'], 'A_Wo': _A_in['Wo'], 'A_seg_len': _A_in['seg_len'], 'B_u': _B_in['u'], 'B_log_A_real': _B_in['log_A_real'], 'B_A_imag': _B_in['A_imag'], 'B_C_real': _B_in['C_real'], 'B_C_imag': _B_in['C_imag'], 'B_log_dt': _B_in['log_dt'], 'B_D_skip': _B_in['D_skip'], 'gate_w': _w((lambda *s: torch.randn(*s, dtype=torch.bfloat16) * 0.05)(576) * 16.0)}

def reference(A_x, A_mem0, A_Wq, A_Wk, A_Wv, A_Wo, A_seg_len, B_u, B_log_A_real, B_A_imag, B_C_real, B_C_imag, B_log_dt, B_D_skip, gate_w):

    def _A_ref(x, mem0, Wq, Wk, Wv, Wo, seg_len):
        B, S, Dm = x.shape
        H, Dh = (Wq.shape[1], Wq.shape[2])
        M = mem0.shape[1]
        scale = Dh ** (-0.5)
        n_seg = S // seg_len
        L = M + seg_len
        idx = torch.arange(L)
        mask = (idx[:, None] < M) | (idx[:, None] >= idx[None, :])
        mem = mem0
        outs = []
        for s in range(n_seg):
            xs = x[:, s * seg_len:(s + 1) * seg_len, :].clone()
            h = torch.concatenate([mem, xs], axis=1)
            q = torch.einsum('btd,dhe->bhte', h, Wq)
            k = torch.einsum('btd,dhe->bhte', h, Wk)
            v = torch.einsum('btd,dhe->bhte', h, Wv)
            scores = torch.einsum('bhqe,bhke->bhqk', q, k) * scale
            scores = torch.where(mask[None, None], scores, -1000000000.0)
            p = _softmax(scores, axis=-1)
            o = torch.einsum('bhqk,bhke->bhqe', p, v)
            o = torch.einsum('bhte,hed->btd', o, Wo)
            mem = o[:, :M, :].clone()
            outs.append(o[:, M:, :])
        return torch.concatenate(outs, axis=1)

    def _B_ref(u, log_A_real, A_imag, C_real, C_imag, log_dt, D_skip):
        L = u.shape[1]
        dt = torch.exp(log_dt)
        A = -torch.exp(log_A_real) + 1j * A_imag
        C = (C_real + 1j * C_imag) * dt[:, None]
        pos = (lambda x, dt: x.to(dt))(torch.arange(L), log_dt.dtype)
        P = torch.exp((A * dt[:, None])[:, :, None] * pos[None, None, :])
        K = 2.0 * torch.einsum('dn,dnl->dl', C, P).real
        L2 = 2 * L
        u_t = (lambda x, *d: x.permute(*d))(u, 0, 2, 1)
        Kf = (lambda x, *a, **k: torch.fft.rfft(x.float(), *a, **k))(K, L2)
        Uf = (lambda x, *a, **k: torch.fft.rfft(x.float(), *a, **k))(u_t, L2)
        y = (lambda x, *a, **k: torch.fft.irfft(x, *a, **k).to(torch.bfloat16))(Uf * Kf[None, :, :], L2)[:, :, :L]
        y = (lambda x, *d: x.permute(*d))(y, 0, 2, 1)
        return y + u * D_skip[None, None, :]
    x = A_x
    g = _sigmoid((x * gate_w).sum(-1, keepdims=True))
    a = _A_ref(x, A_mem0, A_Wq, A_Wk, A_Wv, A_Wo, A_seg_len)
    b = _B_ref(x, B_log_A_real, B_A_imag, B_C_real, B_C_imag, B_log_dt, B_D_skip)
    return g * a + (1 - g) * b
```

```cpp
#include <hip/hip_runtime.h>
#include <hip/hip_bf16.h>
#include <stdint.h>

typedef unsigned short u16;
typedef __bf16 bf16x8 __attribute__((ext_vector_type(8)));
typedef __bf16 bf16x16 __attribute__((ext_vector_type(16)));
typedef short s16x4 __attribute__((ext_vector_type(4)));
typedef float f32x2 __attribute__((ext_vector_type(2)));
typedef float f32x4 __attribute__((ext_vector_type(4)));
typedef float f32x16 __attribute__((ext_vector_type(16)));
typedef unsigned u32x2 __attribute__((ext_vector_type(2)));
typedef unsigned u32x4 __attribute__((ext_vector_type(4)));
typedef unsigned u32x8 __attribute__((ext_vector_type(8)));
typedef unsigned u32x16 __attribute__((ext_vector_type(16)));
typedef unsigned u32x32 __attribute__((ext_vector_type(32)));
typedef short s16x8 __attribute__((ext_vector_type(8)));
#define LAS __attribute__((address_space(3)))
#define DI __device__ __forceinline__
typedef __amdgpu_buffer_rsrc_t rsrc_t;

constexpr int SEQ = 2048, DM = 576, NH = 32, DH = 128, HD = NH * DH, NM = 8, SEG = 32, NSEG = 64, NS = 128;
constexpr unsigned SENT = 0xFFFFFFFFu;
constexpr unsigned OOB = 0x7FFFFFF0u;
constexpr int SPIN_MAX = 1 << 20;

constexpr int OFF_PART = 0;
constexpr int PART_BYTES = 72 * 4096;
constexpr int OFF_MEMB = OFF_PART + 2 * PART_BYTES;
constexpr int MEMB_STRIDE = 4 * 16384;
constexpr int OFF_CAN = OFF_MEMB + 4 * MEMB_STRIDE;
constexpr int COMM_LOGICAL = OFF_CAN + 32768;
constexpr int COMM_BYTES = 2 * COMM_LOGICAL;
constexpr int F_SEL = 31 * 61440;
static_assert(F_SEL >= COMM_BYTES, "");
constexpr int F_ERR = F_SEL + 128;
constexpr int F_KV = F_SEL + 256;
constexpr int F_KVM = F_KV + 4096;
constexpr int F_O = F_KVM + 4096;
constexpr int F_S4 = F_O + 4096;
constexpr int F_DONE = F_S4 + 512;
constexpr int INIT_BYTES = 32 * 61440;
constexpr int F_INIT = INIT_BYTES;
constexpr int F_EPOCH = F_INIT + 4096;
static_assert(F_DONE + 128 <= INIT_BYTES && F_SEL >= 31 * 61440, "flags belong to the last share");
static_assert(F_EPOCH + 128 <= (2 << 20), "hand-off region");
constexpr int OFF_KF = 2 << 20;
constexpr int OFF_VF = OFF_KF + SEQ * HD * 2;
constexpr int OFF_OF = OFF_VF + SEQ * HD * 2;
constexpr int OFF_KM = OFF_OF + SEQ * HD * 2;
constexpr int OFF_VM = OFF_KM + NSEG * NH * NM * DH * 2;
constexpr int OFF_BO = OFF_VM + NSEG * NH * NM * DH * 2;
constexpr int OFF_GT = OFF_BO + SEQ * DM * 2;
constexpr int OFF_DBG = OFF_GT + SEQ * 4;
constexpr int WS_USED = OFF_DBG + (4 << 20);

struct Args {
    const u16 *x, *mem0, *Wq, *Wk, *Wv, *Wo, *lAr, *Aim, *Cr, *Ci, *ldt, *Dsk, *gw;
    u16* out; char* ws;
    int c1, c2, d0, mask, d1, up, dn, rd, cw, pad;
};

DI float bf2f(u16 b) { return __uint_as_float((unsigned)b << 16); }
DI float bflo(unsigned w) { return __uint_as_float(w << 16); }
DI float bfhi(unsigned w) { return __uint_as_float(w & 0xFFFF0000u); }
DI u16 f2bf(float f) { return __builtin_bit_cast(u16, (__bf16)f); }
DI unsigned pack2(float lo, float hi) {
    typedef __bf16 bf16x2 __attribute__((ext_vector_type(2)));
    bf16x2 v = {(__bf16)lo, (__bf16)hi};
    return __builtin_bit_cast(unsigned, v);
}
#define WG_BARRIER() asm volatile("s_waitcnt lgkmcnt(0)\n\ts_barrier" ::: "memory")
#define VM_DRAIN() asm volatile("s_waitcnt vmcnt(0)" ::: "memory")
DI int cmap(int o) { return (o & 0x1FFF) | ((o >> 13) << 14); }
DI unsigned mx4(unsigned a, u32x4 v) { a = max(max(a, v.x), v.y); return max(max(a, v.z), v.w); }
DI unsigned now() { return (unsigned)__builtin_amdgcn_s_memrealtime(); }
DI unsigned wait_until(unsigned t) {
    unsigned v; int guard = 8192;
    do { v = now(); } while ((int)(t - v) >= 4 && --guard > 0);
    const int r = __builtin_amdgcn_readfirstlane((int)(t - v));
    asm volatile("s_cmp_lt_i32 %0, 1\n\ts_cbranch_scc1 1f\n\ts_nop 3\n\ts_cmp_lt_i32 %0, 2\n\ts_cbranch_scc1 1f\n\ts_nop 3\n\ts_cmp_lt_i32 %0, 3\n\ts_cbranch_scc1 1f\n\ts_nop 7\n1:" :: "s"(r) : "scc");
    return r > 0 ? t : v + 2;
}
DI void adapt(int& c8, bool failed, int lo8, int up, int dn) { c8 = failed ? c8 + up : max(c8 - dn, lo8); }
DI void st128(u32x4 v, rsrc_t rs, int off, int soff) {
    __builtin_amdgcn_raw_buffer_store_b128(v, rs, off, soff, 16);
    asm volatile("s_nop 1" :: "v"(v) : "memory");
}
DI void st32(unsigned v, rsrc_t rs, int off, int soff) { __builtin_amdgcn_raw_buffer_store_b32(v, rs, off, soff, 16); }
DI u32x4 ld128(rsrc_t rs, int off, int soff) { return __builtin_amdgcn_raw_buffer_load_b128(rs, off, soff, 16); }
DI u32x2 ld64(rsrc_t rs, int off, int soff) { return __builtin_amdgcn_raw_buffer_load_b64(rs, off, soff, 16); }
DI unsigned ld32(rsrc_t rs, int off, int soff) { return __builtin_amdgcn_raw_buffer_load_b32(rs, off, soff, 16); }
DI f32x2 fold(f32x4 a) { return (f32x2){a[0], a[1]} + (f32x2){a[2], a[3]}; }
DI float dpp_xor1(float v) { return __int_as_float(__builtin_amdgcn_update_dpp(0, __float_as_int(v), 0xB1, 0xf, 0xf, true)); }
DI float red_sum_g(float v) {
    auto a = __builtin_amdgcn_permlane16_swap(__float_as_uint(v), __float_as_uint(v), false, false);
    v = __uint_as_float(a[0]) + __uint_as_float(a[1]);
    auto b = __builtin_amdgcn_permlane32_swap(__float_as_uint(v), __float_as_uint(v), false, false);
    return __uint_as_float(b[0]) + __uint_as_float(b[1]);
}
DI int wave_max32(int d) {
    d = max(d, __builtin_amdgcn_update_dpp(d, d, 0x121, 0xf, 0xf, false));
    d = max(d, __builtin_amdgcn_update_dpp(d, d, 0x122, 0xf, 0xf, false));
    d = max(d, __builtin_amdgcn_update_dpp(d, d, 0x124, 0xf, 0xf, false));
    d = max(d, __builtin_amdgcn_update_dpp(d, d, 0x128, 0xf, 0xf, false));
    return __builtin_amdgcn_readfirstlane(max(max(__builtin_amdgcn_readlane(d, 0), __builtin_amdgcn_readlane(d, 16)), max(__builtin_amdgcn_readlane(d, 32), __builtin_amdgcn_readlane(d, 48))));
}
DI u32x2 tr_read(LAS char* lds, int addr) { return __builtin_bit_cast(u32x2, __builtin_amdgcn_ds_read_tr16_b64_v4i16((LAS s16x4*)(lds + addr))); }
DI void wait_prog(rsrc_t rw, int off, bool act, int thr, int& budget) {
    for (;;) {
        const int v = (int)ld32(rw, off, 0);
        asm volatile("" ::: "memory");
        if (__all(!act || v >= thr)) break;
        if (--budget < 0) break;
        __builtin_amdgcn_s_sleep(1);
    }
}
DI u32x2 launch_tag(u32x2 e) {
    unsigned long long z = (((unsigned long long)e.y << 32) | e.x) + 0x9E3779B97F4A7C15ull;
    z = (z ^ (z >> 30)) * 0xBF58476D1CE4E5B9ull; z = (z ^ (z >> 27)) * 0x94D049BB133111EBull; z ^= z >> 31;
    return (u32x2){(unsigned)z, (unsigned)(z >> 32)};
}
DI void wait_init(rsrc_t rw, u32x2 tag, int& budget) {
    const int o = F_INIT + (threadIdx.x & 31) * 128;
    for (;;) {
        const u32x2 v = ld64(rw, o, 0);
        asm volatile("" ::: "memory");
        if (__all(v.x == tag.x && v.y == tag.y)) break;
        if (--budget < 0) break;
        __builtin_amdgcn_s_sleep(2);
    }
}
DI void glds16(const char* gsrc, unsigned lds_dst) {
    unsigned keep;
    asm volatile("s_mov_b32 %0, m0\n\ts_mov_b32 m0, %2\n\ts_nop 0\n\tglobal_load_lds_dwordx4 %1, off\n\ts_mov_b32 m0, %0" : "=&s"(keep) : "v"(gsrc), "s"(lds_dst) : "memory");
}
DI void bdma(rsrc_t rs, int voff, int soff, unsigned lds_dst) {
    unsigned keep;
    asm volatile("s_mov_b32 %0, m0\n\ts_mov_b32 m0, %4\n\ts_nop 0\n\tbuffer_load_dwordx4 %1, %2, %3 offen sc1 lds\n\ts_mov_b32 m0, %0" : "=&s"(keep) : "v"(voff), "s"(rs), "s"(soff), "s"(lds_dst) : "memory");
}
DI int swz(int r) { return ((r & 3) ^ (((r >> 2) & 3) << 1) ^ (((r >> 4) & 1) << 2)) & 7; }
DI void ring_issue(const u16* src, int ld, unsigned slot, int w, int lane) {
#pragma unroll
    for (int j = 0; j < 4; ++j) {
        const int r = 16 * w + 4 * j + (lane >> 4);
        glds16((const char*)(src + (size_t)r * ld) + (((lane & 15) ^ (2 * swz(r))) * 16), slot + (16 * w + 4 * j) * 256);
    }
}
template <int N> DI void ring_wait() { asm volatile("s_waitcnt vmcnt(%0) lgkmcnt(0)\n\ts_barrier" :: "i"(N) : "memory"); }
template <int I> struct ic_t { static constexpr int value = I; };
template <int N, int I = 0, class F> DI void static_for(F&& f) { if constexpr (I < N) { f(ic_t<I>{}); static_for<N, I + 1>(f); } }
#define MFMA32(a, b, c) __builtin_amdgcn_mfma_f32_32x32x16_bf16(__builtin_bit_cast(bf16x8, a), __builtin_bit_cast(bf16x8, b), c, 0, 0, 0)
#define MFMA16(a, b, c) __builtin_amdgcn_mfma_f32_16x16x32_bf16(__builtin_bit_cast(bf16x8, a), __builtin_bit_cast(bf16x8, b), c, 0, 0, 0)

constexpr int S_CH = 9;
constexpr int S_XRS = 4096 + 32;
constexpr int S_XR = 0;
constexpr int S_RC = S_CH * S_XRS;
constexpr int S_RCS = 4192;
constexpr int S_KD = S_RC + 4 * S_RCS;
constexpr int S_K = S_KD + 48 * 128;
constexpr int S_TAB = S_K + 79 * 160 + 32;
constexpr int S_TROW = 1056;
constexpr int S_TOTAL = S_TAB + 57 * S_TROW;
static_assert(S_RC % 16 == 0 && S_TAB % 16 == 0 && 4 * 64 * 144 <= 57 * S_TROW, "");
DI f32x2 crot(f32x2 z) { return (f32x2){-z.y, z.x}; }
DI f32x2 cmul(f32x2 a, f32x2 z) { return __builtin_elementwise_fma(a.yy, crot(z), a.xx * z); }
DI f32x2 cexp_p(float ar, float ai, float p) {
    const float x = ai * p;
    const float hi = x * 0.15915494309189535f;
    const float lo = __builtin_fmaf(x, 0.15915494309189535f, -hi) + x * 6.420638316725915e-9f;
    const float rev = (hi - __builtin_rintf(hi)) + lo;
    const float m = __builtin_amdgcn_exp2f(ar * p * 1.4426950408889634f);
    return (f32x2){m * __builtin_amdgcn_cosf(rev), m * __builtin_amdgcn_sinf(rev)};
}
#define FENCE() __builtin_amdgcn_sched_barrier(0)
DI void role_s4d(const Args& a, LAS char* lds, int task, rsrc_t rw, u32x2 tag, int& budget) {
    const int tid = threadIdx.x, lane = tid & 63, w = __builtin_amdgcn_readfirstlane(tid >> 6);
    const int d0 = task * S_CH;
#define S4S(i)
    {
        const rsrc_t rx = __builtin_amdgcn_make_buffer_rsrc((void*)a.x, 0, SEQ * DM * 2, 0x00020000);
        const unsigned sh = (d0 & 1) * 16;
        u32x4 v[8]; unsigned e[8];
#pragma unroll
        for (int i = 0; i < 8; ++i) {
            const int o = (tid + 256 * i) * (DM * 2) + (d0 & ~1) * 2;
            v[i] = __builtin_amdgcn_raw_buffer_load_b128(rx, o, 0, 0); e[i] = __builtin_amdgcn_raw_buffer_load_b32(rx, o, 16, 0);
        }
#pragma unroll
        for (int p = 0; p < 2; ++p) {
            const int tok = 32 * task + 8 * w + 4 * p + (lane >> 4);
            float acc = 0.f;
#pragma unroll
            for (int i = 0; i < 5; ++i) {
                const int ch = (lane & 15) + 16 * i;
                const u32x4 gv = ch < 72 ? *(const u32x4*)(a.gw + ch * 8) : (u32x4){0u, 0u, 0u, 0u};
                const u32x4 xv = *(const u32x4*)(a.x + (size_t)tok * DM + min(ch, 71) * 8);
#pragma unroll
                for (int j = 0; j < 4; ++j) acc += bflo(xv[j]) * bflo(gv[j]) + bfhi(xv[j]) * bfhi(gv[j]);
            }
            acc += __shfl_xor(acc, 1); acc += __shfl_xor(acc, 2); acc += __shfl_xor(acc, 4); acc += __shfl_xor(acc, 8);
            st32(__float_as_uint(1.0f / (1.0f + __expf(-acc))), rw, (lane & 15) == 0 ? OFF_GT + tok * 4 : OOB, 0);
        }
#pragma unroll
        for (int i = 0; i < 8; ++i) {
            const unsigned x5[5] = {__builtin_amdgcn_alignbit(v[i].y, v[i].x, sh), __builtin_amdgcn_alignbit(v[i].z, v[i].y, sh), __builtin_amdgcn_alignbit(v[i].w, v[i].z, sh),
                                    __builtin_amdgcn_alignbit(e[i], v[i].w, sh), e[i] >> sh};
#pragma unroll
            for (int c = 0; c < S_CH; ++c) *(LAS u16*)(lds + S_XR + c * S_XRS + (2047 - tid - 256 * i) * 2) = (u16)(x5[c >> 1] >> (16 * (c & 1)));
        }
    }
    if (tid < 8 * S_CH) *(LAS unsigned*)(lds + S_XR + (tid >> 3) * S_XRS + 4096 + (tid & 7) * 4) = 0;
    if (tid < 64) *(LAS unsigned*)(lds + S_RC + (tid >> 4) * S_RCS + 4096 + (tid & 15) * 4) = 0;
    for (int i = tid; i < 15 * 160 / 4; i += 256) *(LAS unsigned*)(lds + S_K + i * 4) = 0;
    int kd[8];
#pragma unroll
    for (int j = 0; j < 8; ++j) {
        const int p = (int)bf2f(f2bf((float)(8 * tid + j))), k = p >> 7, r = p & 7;
        const int row = p < 256 ? 8 * k + r : (p < 512 ? 8 + 4 * k + (r >> 1) : (p < 1024 ? 16 + 2 * k + (r >> 2) : 24 + k));
        kd[j] = S_KD + (row * 16 + ((p >> 3) & 15)) * 8;
    }
    const int n = tid & 127, nn = lane & 15, g = lane >> 4;
    u16 pA = a.lAr[d0 * NS + n], pI = a.Aim[d0 * NS + n], pCr = a.Cr[d0 * NS + n], pCi = a.Ci[d0 * NS + n], pT = a.ldt[d0], pD = a.Dsk[d0];
    for (int ch = 0; ch < S_CH; ++ch) {
        WG_BARRIER();
        S4S(0)
        const float dsk = bf2f(pD);
        {
            const u32x4 v = *(LAS u32x4*)(lds + S_XR + ch * S_XRS + tid * 16);
            const u32x2 e = *(LAS u32x2*)(lds + S_XR + ch * S_XRS + tid * 16 + 16);
            const unsigned s01 = __builtin_amdgcn_alignbit(v.y, v.x, 16), s12 = __builtin_amdgcn_alignbit(v.z, v.y, 16), s23 = __builtin_amdgcn_alignbit(v.w, v.z, 16);
            const unsigned s34 = __builtin_amdgcn_alignbit(e.x, v.w, 16), s45 = __builtin_amdgcn_alignbit(e.y, e.x, 16);
            *(LAS u32x4*)(lds + S_RC + tid * 16) = v;
            *(LAS u32x4*)(lds + S_RC + S_RCS + tid * 16) = (u32x4){s01, s12, s23, s34};
            *(LAS u32x4*)(lds + S_RC + 2 * S_RCS + tid * 16) = (u32x4){v.y, v.z, v.w, e.x};
            *(LAS u32x4*)(lds + S_RC + 3 * S_RCS + tid * 16) = (u32x4){s12, s23, s34, s45};
        }
        S4S(1)
        {
            const float dt = bf2f(f2bf(expf(bf2f(pT))));
            const float ar = -bf2f(f2bf(expf(bf2f(pA)))) * dt, ai = bf2f(pI) * dt;
            const f32x2 C = {2.f * bf2f(pCr) * dt, 2.f * bf2f(pCi) * dt};
            const int dn = min(d0 + ch + 1, DM - 1);
            pA = a.lAr[dn * NS + n]; pI = a.Aim[dn * NS + n]; pCr = a.Cr[dn * NS + n]; pCi = a.Ci[dn * NS + n]; pT = a.ldt[dn]; pD = a.Dsk[dn];
            const int to = S_TAB + n * 8;
            auto put = [&](int row, f32x2 v) { *(LAS f32x2*)(lds + to + row * S_TROW) = v; };
            const f32x2 z128 = cexp_p(ar, ai, 128.f);
            if (w < 2) {
                f32x2 E[8], G[4];
                E[1] = cexp_p(ar, ai, 1.f); G[0] = C;
#pragma unroll
                for (int r = 2; r < 8; ++r) E[r] = cmul(E[r >> 1], E[r - (r >> 1)]);
#pragma unroll
                for (int k = 1; k < 4; ++k) G[k] = cmul(G[k - 1], z128);
#pragma unroll
                for (int k = 0; k < 4; ++k)
#pragma unroll
                    for (int r = 0; r < 8; r += 1 + (k >> 1)) put(k < 2 ? 8 * k + r : 8 + 4 * k + (r >> 1), r ? cmul(E[r], G[k]) : G[k]);
            } else {
                const f32x2 z4 = cexp_p(ar, ai, 4.f), z8 = cexp_p(ar, -ai, 8.f);
                f32x2 G = cmul(C, cexp_p(ar, ai, 512.f)), B = z8;
                put(41, (f32x2){1.f, 0.f});
#pragma unroll
                for (int k = 1; k < 16; ++k) {
                    put(41 + k, B); B = cmul(B, z8);
                    if (k < 5) { put(22 + 2 * k, G); put(23 + 2 * k, cmul(G, z4)); }
                    else if (k < 14) put(27 + k, G);
                    G = cmul(G, z128);
                }
            }
        }
        WG_BARRIER();
        S4S(2)
        {
            const int bo = S_TAB + (41 + nn) * S_TROW + g * 16;
            f32x4 av[12], bv[12], acc[3] = {};
            if (w < 3) {
                const int ao = S_TAB + min(16 * w + nn, 40) * S_TROW + g * 16;
#pragma unroll
                for (int j = 0; j < 12; ++j) { av[j] = *(LAS f32x4*)(lds + ao + j * 64); bv[j] = *(LAS f32x4*)(lds + bo + j * 64); }
#pragma unroll
                for (int j = 0; j < 48; ++j) acc[j & 1] = __builtin_amdgcn_mfma_f32_16x16x4f32(av[j >> 2][j & 3], bv[j >> 2][j & 3], acc[j & 1], 0, 0, 0);
#pragma unroll
                for (int r = 0; r < 4; ++r) *(LAS float*)(lds + S_KD + ((16 * w + 4 * g + r) * 16 + nn) * 8) = acc[0][r] + acc[1][r];
            } else {
#pragma unroll
                for (int j = 0; j < 4; ++j) {
                    bv[j] = *(LAS f32x4*)(lds + bo + 768 + j * 64);
#pragma unroll
                    for (int t = 0; t < 3; ++t) av[3 * j + t] = *(LAS f32x4*)(lds + S_TAB + min(16 * t + nn, 40) * S_TROW + g * 16 + 768 + j * 64);
                }
#pragma unroll
                for (int j = 0; j < 16; ++j)
#pragma unroll
                    for (int t = 0; t < 3; ++t) acc[t] = __builtin_amdgcn_mfma_f32_16x16x4f32(av[3 * (j >> 2) + t][j & 3], bv[j >> 2][j & 3], acc[t], 0, 0, 0);
#pragma unroll
                for (int t = 0; t < 3; ++t)
#pragma unroll
                    for (int r = 0; r < 4; ++r) *(LAS float*)(lds + S_KD + ((16 * t + 4 * g + r) * 16 + nn) * 8 + 4) = acc[t][r];
            }
        }
        WG_BARRIER();
        S4S(3)
        {
            float kv[8];
#pragma unroll
            for (int j = 0; j < 8; ++j) { const f32x2 t = *(LAS f32x2*)(lds + kd[j]); kv[j] = t.x + t.y; }
            u32x4 hi, lo;
#pragma unroll
            for (int j = 0; j < 4; ++j) { hi[j] = pack2(kv[2 * j], kv[2 * j + 1]); lo[j] = pack2(kv[2 * j] - bflo(hi[j]), kv[2 * j + 1] - bfhi(hi[j])); }
            const int o = S_K + (15 + (tid >> 2)) * 160 + (tid & 3) * 16;
            *(LAS u32x4*)(lds + o) = hi; *(LAS u32x4*)(lds + o + 64) = lo;
        }
        WG_BARRIER();
        S4S(4)
        {
            const int st0 = 2047 - nn + 8 * g - 32 * w, cc = st0 & 3;
            const int ub = S_RC + cc * S_RCS + (st0 - cc) * 2 - 3872, kb = S_K + (3 + nn - w) * 160 + g * 16;
            f32x4 y[4][2] = {};
            u32x4 F[2][16];
            auto rd = [&](int i, int f) {
                if (f & 1) { const int o = ub + 3872 - 256 * i - 1024 * (f >> 2) - 32 * ((f >> 1) & 1); const u32x2 x = *(LAS u32x2*)(lds + o), z = *(LAS u32x2*)(lds + o + 8); F[i & 1][f] = (u32x4){x.x, x.y, z.x, z.y}; }
                else F[i & 1][f] = *(LAS u32x4*)(lds + kb + (12 - 4 * i + 16 * ((f >> 1) & 3)) * 160 + (f >> 3) * 64);
            };
#pragma unroll
            for (int f = 0; f < 16; ++f) rd(0, f);
            FENCE();
#pragma unroll
            for (int i = 0; i < 4; ++i) {
                int c = 0;
#pragma unroll
                for (int hl = 0; hl < 2; ++hl)
#pragma unroll
                    for (int q = 0; q < 4; ++q)
#pragma unroll
                        for (int T = q; T < 4; ++T) {
                            y[T][0] = MFMA16(F[i & 1][4 * q + 1], F[i & 1][8 * hl + 2 * (T - q)], y[T][0]);
                            y[T][1] = MFMA16(F[i & 1][4 * q + 3], F[i & 1][8 * hl + 2 * (T - q)], y[T][1]);
                            if (i < 3 && c < 16) rd(i + 1, c);
                            ++c;
                            FENCE();
                        }
            }
#pragma unroll
            for (int T = 0; T < 4; ++T)
#pragma unroll
                for (int bt = 0; bt < 2; ++bt) *(LAS f32x4*)(lds + S_TAB + (w * 64 + 16 * T + nn) * 144 + bt * 64 + g * 16) = y[T][bt];
        }
        WG_BARRIER();
        S4S(5)
        {
            f32x4 y0 = {0, 0, 0, 0}, y1 = y0;
#pragma unroll
            for (int ww = 0; ww < 4; ++ww) {
                const int o = S_TAB + (ww * 64 + (tid >> 2)) * 144 + (tid & 3) * 32;
                y0 += *(LAS f32x4*)(lds + o); y1 += *(LAS f32x4*)(lds + o + 16);
            }
            const u32x4 u = *(LAS u32x4*)(lds + S_XR + ch * S_XRS + (255 - tid) * 16);
            st128((u32x4){pack2(y0[0] + bfhi(u.w) * dsk, y0[1] + bflo(u.w) * dsk), pack2(y0[2] + bfhi(u.z) * dsk, y0[3] + bflo(u.z) * dsk),
                          pack2(y1[0] + bfhi(u.y) * dsk, y1[1] + bflo(u.y) * dsk), pack2(y1[2] + bfhi(u.x) * dsk, y1[3] + bflo(u.x) * dsk)}, rw, tid * 16, OFF_BO + (d0 + ch) * 4096);
        }
        S4S(6)
    }
    wait_init(rw, tag, budget);
    VM_DRAIN();
    WG_BARRIER();
    if (tid == 0) st32(1u, rw, F_S4 + task * 4, 0);
}

constexpr int XT_BYTES = 32 * 1152;
constexpr int P_XT = 0;
constexpr int P_STG = 2 * XT_BYTES;
struct WSlice { u32x4 f[36]; };
constexpr int P_RING = 5;
DI void load_wslices(WSlice& W0, WSlice& W1, const u16* s0, const u16* s1, int w, int lane, LAS char* lds, unsigned lds_base) {
    constexpr int NB = 18;
    const int r0 = 8 * (lane >> 5) + ((lane & 15) >> 2);
    const int cb = (4 * w + 2 * ((lane >> 4) & 1) + ((lane & 3) >> 1)), lo8 = (lane & 1) * 8;
    auto src = [&](int i) { return (i < 9 ? s0 : s1) + (size_t)(64 * (i % 9)) * HD; };
#pragma unroll
    for (int i = 0; i < P_RING - 1; ++i) ring_issue(src(i), HD, lds_base + P_STG + i * 16384, w, lane);
    static_for<NB>([&](auto ic) {
        constexpr int i = decltype(ic)::value, rem = NB - 1 - i < P_RING - 2 ? NB - 1 - i : P_RING - 2;
        ring_wait<4 * rem>();
        if constexpr (i + P_RING - 1 < NB) ring_issue(src(i + P_RING - 1), HD, lds_base + P_STG + ((i + P_RING - 1) % P_RING) * 16384, w, lane);
        const int sl = P_STG + (i % P_RING) * 16384;
#pragma unroll
        for (int kk = 0; kk < 4; ++kk) {
            const int ra = r0 + 16 * kk, rb = ra + 4;
            const u32x2 lo = tr_read(lds, sl + ra * 256 + ((cb ^ (2 * swz(ra))) * 16) + lo8), hi = tr_read(lds, sl + rb * 256 + ((cb ^ (2 * swz(rb))) * 16) + lo8);
            (i < 9 ? W0 : W1).f[(i % 9) * 4 + kk] = (u32x4){lo.x, lo.y, hi.x, hi.y};
        }
        u32x4* f = (i < 9 ? W0 : W1).f + (i % 9) * 4;
        if constexpr (i < 16) asm volatile("" : "+a"(f[0]), "+a"(f[1]), "+a"(f[2]), "+a"(f[3]));
    });
}
DI u32x4 piece_pi(const f32x16& a, int qq) {
    return (u32x4){pack2(a[4 * qq], a[4 * qq + 8]), pack2(a[4 * qq + 1], a[4 * qq + 9]), pack2(a[4 * qq + 2], a[4 * qq + 10]), pack2(a[4 * qq + 3], a[4 * qq + 11])};
}
DI u32x4 piece_seq(const f32x16& a, int s2) {
    return (u32x4){pack2(a[8 * s2], a[8 * s2 + 1]), pack2(a[8 * s2 + 2], a[8 * s2 + 3]), pack2(a[8 * s2 + 4], a[8 * s2 + 5]), pack2(a[8 * s2 + 6], a[8 * s2 + 7])};
}
struct XTile {
    int xs[9], xa[4];
    DI void init(int w, int lane) {
#pragma unroll
        for (int i = 0; i < 9; ++i) { const int L = (i * 4 + w) * 64 + lane, r = L / 72, cs = L - 72 * r; xs[i] = r * 1152 + (cs ^ ((r >> 1) & 7)) * 16; }
        const int c32 = lane & 31, hf = lane >> 5;
#pragma unroll
        for (int j = 0; j < 4; ++j) xa[j] = P_XT + c32 * 1152 + (((2 * j + hf) ^ ((c32 >> 1) & 7)) * 16);
    }
    DI void dma(const Args& a, unsigned lds_base, int w, int s, int buf, int i) const {
        glds16((const char*)a.x + (size_t)min(s, NSEG - 1) * XT_BYTES + xs[i], lds_base + P_XT + buf * XT_BYTES + (i * 4 + w) * 1024);
    }
};
template <bool TR> DI void project(f32x16& acc0, f32x16& acc1, const WSlice& W0, const WSlice& W1, const XTile& X, const Args& a, LAS char* lds, unsigned lds_base, int w, int buf, int sn) {
    const int bo = buf * XT_BYTES;
    constexpr int PD = 5;
    u32x4 xf[PD];
#pragma unroll
    for (int i = 0; i < PD; ++i) xf[i] = *(LAS u32x4*)(lds + X.xa[i & 3] + bo + (i >> 2) * 128);
#pragma unroll
    for (int ks = 0; ks < 36; ++ks) {
        if (ks == 0) asm volatile("v_mfma_f32_32x32x16_bf16 %0, %1, %2, 0" : "=&v"(acc0) : "a"(W0.f[0]), "v"(xf[0]) : "memory");
        else asm volatile("v_mfma_f32_32x32x16_bf16 %0, %1, %2, %0" : "+v"(acc0) : "a"(W0.f[ks]), "v"(xf[ks % PD]) : "memory");
        if (TR) {
            if (ks == 0) asm volatile("v_mfma_f32_32x32x16_bf16 %0, %2, %1, 0" : "=&v"(acc1) : "a"(W1.f[0]), "v"(xf[0]) : "memory");
            else if (ks < 28) asm volatile("v_mfma_f32_32x32x16_bf16 %0, %2, %1, %0" : "+v"(acc1) : "a"(W1.f[ks]), "v"(xf[ks % PD]) : "memory");
            else asm volatile("v_mfma_f32_32x32x16_bf16 %0, %2, %1, %0" : "+v"(acc1) : "v"(W1.f[ks]), "v"(xf[ks % PD]) : "memory");
        } else {
            if (ks == 0) asm volatile("v_mfma_f32_32x32x16_bf16 %0, %1, %2, 0" : "=&v"(acc1) : "a"(W1.f[0]), "v"(xf[0]) : "memory");
            else if (ks < 28) asm volatile("v_mfma_f32_32x32x16_bf16 %0, %1, %2, %0" : "+v"(acc1) : "a"(W1.f[ks]), "v"(xf[ks % PD]) : "memory");
            else asm volatile("v_mfma_f32_32x32x16_bf16 %0, %1, %2, %0" : "+v"(acc1) : "v"(W1.f[ks]), "v"(xf[ks % PD]) : "memory");
        }
        if (ks + PD < 36) xf[ks % PD] = *(LAS u32x4*)(lds + X.xa[(ks + PD) & 3] + bo + ((ks + PD) >> 2) * 128);
        if (ks < 18 && (ks & 1)) X.dma(a, lds_base, w, sn, buf ^ 1, ks >> 1);
    }
    asm volatile("s_nop 7\n\ts_nop 3" : "+v"(acc0), "+v"(acc1) :: "memory");
}
DI void role_kv(const Args& a, LAS char* lds, unsigned lds_base, int h, rsrc_t rw, u32x2 tag, int& budget) {
    const int tid = threadIdx.x, lane = tid & 63, w = __builtin_amdgcn_readfirstlane(tid >> 6);
    XTile X; X.init(w, lane);
#pragma unroll
    for (int i = 0; i < 9; ++i) X.dma(a, lds_base, w, 0, 0, i);
    WSlice W0, W1;
    load_wslices(W0, W1, a.Wk + h * DH, a.Wv + h * DH, w, lane, lds, lds_base);
    wait_init(rw, tag, budget);
    VM_DRAIN();
    WG_BARRIER();
    const int oo = h * 8192 + w * 2048 + lane * 16;
    const int fo = tid == 0 ? F_KV + h * 128 : OOB;
    for (int s = 0; s < NSEG; ++s) {
        f32x16 acc0, acc1;
        project<true>(acc0, acc1, W0, W1, X, a, lds, lds_base, w, s & 1, s + 1);
        const int so = s * (NH * 8192);
        st128(piece_pi(acc0, 0), rw, oo, OFF_KF + so);
        st128(piece_pi(acc0, 1), rw, oo + 1024, OFF_KF + so);
        st128(piece_seq(acc1, 0), rw, oo, OFF_VF + so);
        st128(piece_seq(acc1, 1), rw, oo + 1024, OFF_VF + so);
        if (s < 2) VM_DRAIN();
        else asm volatile("s_waitcnt vmcnt(4)" ::: "memory");
        WG_BARRIER();
        st32((unsigned)(s < 2 ? s : s - 1), rw, fo, 0);
    }
    VM_DRAIN();
    WG_BARRIER();
    st32(NSEG, rw, fo, 0);
}

DI int prog_min(rsrc_t rw, int off) {
    const int v = (int)ld32(rw, off, 0);
    asm volatile("" ::: "memory");
    return -wave_max32(-v);
}
DI float red_max_g(float v) {
    auto a = __builtin_amdgcn_permlane16_swap(__float_as_uint(v), __float_as_uint(v), false, false);
    v = fmaxf(__uint_as_float(a[0]), __uint_as_float(a[1]));
    auto b = __builtin_amdgcn_permlane32_swap(__float_as_uint(v), __float_as_uint(v), false, false);
    return fmaxf(__uint_as_float(b[0]), __uint_as_float(b[1]));
}
#define M16 "v_mfma_f32_16x16x32_bf16 "
DI void role_qa(const Args& a, LAS char* lds, unsigned lds_base, int h0, int par, rsrc_t rw, u32x2 tag, int& budget) {
    const int tid = threadIdx.x, lane = tid & 63, w = __builtin_amdgcn_readfirstlane(tid >> 6);
    const int n = lane & 15, g = lane >> 4, h = h0 + (w >> 1), tt = w & 1;
    XTile X; X.init(w, lane);
#pragma unroll
    for (int i = 0; i < 9; ++i) X.dma(a, lds_base, w, par, 0, i);
    WSlice W0, W1;
    load_wslices(W0, W1, a.Wq + h0 * DH, a.Wq + (h0 + 1) * DH, w, lane, lds, lds_base);
    VM_DRAIN();
    WG_BARRIER();
    constexpr int Q_Q = P_STG, Q_KV = Q_Q + 16384, Q_M = Q_KV + 32768;
    static_assert(Q_M + 8192 <= P_STG + P_RING * 16384, "");
    auto proj = [&](int s) {
        f32x16 acc0, acc1;
        project<false>(acc0, acc1, W0, W1, X, a, lds, lds_base, w, (s >> 1) & 1, s + 2);
        const int o = Q_Q + w * 2048 + lane * 16;
        *(LAS u32x4*)(lds + o) = piece_pi(acc0, 0); *(LAS u32x4*)(lds + o + 1024) = piece_pi(acc0, 1);
        *(LAS u32x4*)(lds + o + 8192) = piece_pi(acc1, 0); *(LAS u32x4*)(lds + o + 8192 + 1024) = piece_pi(acc1, 1);
    };
    const int fs = (tt ? OFF_VF : OFF_KF) + (g >> 1) * 1024 + ((g & 1) * 32 + 16 * (n >> 3) + (n & 7)) * 16;
    auto fetch = [&](int s) {
        const int so = (min(s, NSEG - 1) * NH + h) * 8192;
#pragma unroll
        for (int f = 0; f < 8; ++f) bdma(rw, fs, so + (f >> 1) * 2048 + (f & 1) * 128, lds_base + Q_KV + w * 8192 + f * 1024);
    };
    const int ms = tt ? OFF_VM + lane * 16 : OFF_KM + (lane & 7) * 256 + (lane >> 3) * 16;
    auto fetch_m = [&](int s) {
        const int so = (s * NH + h) * 2048;
        bdma(rw, ms, so, lds_base + Q_M + w * 2048); bdma(rw, ms, so + (tt ? 1024 : 128), lds_base + Q_M + w * 2048 + 1024);
    };
    const float c = 0.08838834764831845f * 1.4426950408889634f;
    u32x4 qf[4];
    f32x4 o[8]; float mx, l;
    auto xpart = [&]() {
        const int kb = Q_KV + (w >> 1) * 16384 + lane * 16;
        u32x4 kn[8], vn[8];
#pragma unroll
        for (int ks = 0; ks < 4; ++ks) qf[ks] = *(LAS u32x4*)(lds + Q_Q + (w >> 1) * 8192 + (2 * ks + (g >> 1)) * 1024 + ((g & 1) * 32 + 16 * tt + n) * 16);
#pragma unroll
        for (int f = 0; f < 8; ++f) kn[f] = *(LAS u32x4*)(lds + kb + f * 1024);
        FENCE();
#pragma unroll
        for (int f = 0; f < 8; ++f) vn[f] = *(LAS u32x4*)(lds + kb + 8192 + f * 1024);
        f32x4 s0, s1;
        asm volatile(M16 "%0, %2, %10, 0\n\t" M16 "%1, %3, %10, 0\n\t" M16 "%0, %4, %11, %0\n\t" M16 "%1, %5, %11, %1\n\t"
                     M16 "%0, %6, %12, %0\n\t" M16 "%1, %7, %12, %1\n\t" M16 "%0, %8, %13, %0\n\t" M16 "%1, %9, %13, %1\n\ts_nop 7"
                     : "=&v"(s0), "=&v"(s1) : "v"(kn[0]), "v"(kn[1]), "v"(kn[2]), "v"(kn[3]), "v"(kn[4]), "v"(kn[5]), "v"(kn[6]), "v"(kn[7]), "v"(qf[0]), "v"(qf[1]), "v"(qf[2]), "v"(qf[3]));
        float p[8];
        mx = -3.0e38f; l = 0.f;
#pragma unroll
        for (int r = 0; r < 8; ++r) { const int key = 16 * (g >> 1) + 4 * (g & 1) + (r & 3) + 8 * (r >> 2); p[r] = key <= 16 * tt + n ? (r < 4 ? s0[r & 3] : s1[r & 3]) : -3.0e38f; mx = fmaxf(mx, p[r]); }
        mx = red_max_g(mx);
#pragma unroll
        for (int r = 0; r < 8; ++r) { p[r] = __builtin_amdgcn_exp2f((p[r] - mx) * c); l += p[r]; }
        const u32x4 pf = {pack2(p[0], p[1]), pack2(p[2], p[3]), pack2(p[4], p[5]), pack2(p[6], p[7])};
        asm volatile("s_nop 1\n\t" M16 "%0, %8, %16, 0\n\t" M16 "%1, %9, %16, 0\n\t" M16 "%2, %10, %16, 0\n\t" M16 "%3, %11, %16, 0\n\t"
                     M16 "%4, %12, %16, 0\n\t" M16 "%5, %13, %16, 0\n\t" M16 "%6, %14, %16, 0\n\t" M16 "%7, %15, %16, 0\n\ts_nop 7"
                     : "=&v"(o[0]), "=&v"(o[1]), "=&v"(o[2]), "=&v"(o[3]), "=&v"(o[4]), "=&v"(o[5]), "=&v"(o[6]), "=&v"(o[7])
                     : "v"(vn[0]), "v"(vn[1]), "v"(vn[2]), "v"(vn[3]), "v"(vn[4]), "v"(vn[5]), "v"(vn[6]), "v"(vn[7]), "v"(pf));
    };
    proj(par);
    wait_init(rw, tag, budget);
    wait_prog(rw, F_KV + h * 128, true, par, budget);
    fetch(par);
    VM_DRAIN();
    WG_BARRIER();
    xpart();
    WG_BARRIER();
    proj(par + 2);
    const int fm = F_KVM + h * 128 + (lane & 7) * 4, fo = lane == 0 ? F_O + h * 128 + (2 * par + tt) * 4 : OOB;
    int m_rdy = -1;
    bool pre = false;
    for (int s = par; s < NSEG; s += 2) {
        S4S(0)
        if (!pre) {
            while (m_rdy < s) { m_rdy = prog_min(rw, fm); if (--budget < 0) break; }
            fetch_m(s); fetch(s + 2);
        }
        S4S(1)
        asm volatile("s_waitcnt vmcnt(8)" ::: "memory");
        WG_BARRIER();
        S4S(2)
        const int mb = Q_M + (w >> 1) * 4096;
        u32x4 kf[4]; u32x2 vm[4];
#pragma unroll
        for (int ks = 0; ks < 4; ++ks) kf[ks] = *(LAS u32x4*)(lds + mb + (4 * ks + g) * 128 + (2 * (n >> 2) + (n & 1)) * 16);
#pragma unroll
        for (int t = 0; t < 4; ++t) { const int vo = mb + 2048 + t * 512 + (n >> 3) * 256 + g * 64 + (n & 7) * 4; vm[t] = (u32x2){*(LAS unsigned*)(lds + vo), *(LAS unsigned*)(lds + vo + 32)}; }
        const int pm_ = (int)ld32(rw, fm, 0);
        f32x4 sm;
        asm volatile(M16 "%0, %1, %5, 0\n\t" M16 "%0, %2, %6, %0\n\t" M16 "%0, %3, %7, %0\n\t" M16 "%0, %4, %8, %0\n\ts_nop 7"
                     : "=&v"(sm) : "v"(kf[0]), "v"(kf[1]), "v"(kf[2]), "v"(kf[3]), "v"(qf[0]), "v"(qf[1]), "v"(qf[2]), "v"(qf[3]));
        const float m2 = red_max_g(fmaxf(mx, fmaxf(sm[0], sm[1])));
        const float al = __builtin_amdgcn_exp2f((mx - m2) * c), p0 = __builtin_amdgcn_exp2f((sm[0] - m2) * c), p1 = __builtin_amdgcn_exp2f((sm[1] - m2) * c);
        const float inv = __builtin_amdgcn_rcpf(red_sum_g(l * al + p0 + p1)), ai = al * inv;
#pragma unroll
        for (int t = 0; t < 8; ++t) o[t] *= ai;
        const unsigned pm = pack2(p0 * inv, p1 * inv);
#define M1K "v_mfma_f32_16x16x16_bf16 "
        asm volatile("s_nop 1\n\t" M1K "%0, %8, %12, %0\n\t" M1K "%1, %8, %13, %1\n\t" M1K "%2, %9, %12, %2\n\t" M1K "%3, %9, %13, %3\n\t"
                     M1K "%4, %10, %12, %4\n\t" M1K "%5, %10, %13, %5\n\t" M1K "%6, %11, %12, %6\n\t" M1K "%7, %11, %13, %7\n\ts_nop 7"
                     : "+v"(o[0]), "+v"(o[1]), "+v"(o[2]), "+v"(o[3]), "+v"(o[4]), "+v"(o[5]), "+v"(o[6]), "+v"(o[7])
                     : "v"(vm[0]), "v"(vm[1]), "v"(vm[2]), "v"(vm[3]), "v"((u32x2){pm, 0u}), "v"((u32x2){0u, pm}));
        const int oo = OFF_OF + lane * 16, so = ((2 * s + tt) * NH + h) * 4096;
#pragma unroll
        for (int t = 0; t < 4; ++t)
            st128((u32x4){pack2(o[2 * t][0], o[2 * t][1]), pack2(o[2 * t][2], o[2 * t][3]), pack2(o[2 * t + 1][0], o[2 * t + 1][1]), pack2(o[2 * t + 1][2], o[2 * t + 1][3])}, rw, oo + t * 1024, so);
        S4S(3)
        if (s + 2 < NSEG) {
            asm volatile("s_waitcnt vmcnt(4)" ::: "memory");
            WG_BARRIER();
            S4S(4)
            xpart();
        }
        FENCE();
        S4S(5)
        m_rdy = max(m_rdy, -wave_max32(-pm_));
        VM_DRAIN();
        st32((unsigned)s, rw, fo, 0);
        S4S(6)
        WG_BARRIER();
        pre = m_rdy >= s + 2;
        if (pre) { fetch_m(s + 2); fetch(s + 4); }
        S4S(7)
        if (s + 4 < NSEG) proj(s + 4);
        S4S(8)
    }
}

constexpr int O_W = 0;
constexpr int O_STG = 131072;
constexpr int OUT_REP = 7;
DI void role_outp(const Args& a, LAS char* lds, int nb, int rep, rsrc_t rw, int& budget) {
    const int tid = threadIdx.x, lane = tid & 63, w = __builtin_amdgcn_readfirstlane(tid >> 6);
    WG_BARRIER();
    const int n = lane & 15, g = lane >> 4;
    const int wl = O_W + w * 32768 + lane * 16;
    u32x4 wa[64], wv[32];
    {
        const int stg = O_STG + w * 5120;
        const rsrc_t ro = __builtin_amdgcn_make_buffer_rsrc((void*)a.Wo, 0, HD * DM * 2, 0x00020000);
        const int so = (1024 * w + (lane >> 3)) * (DM * 2) + nb * 128 + (lane & 7) * 16, sw = stg + (lane >> 3) * 160 + (lane & 7) * 16;
        const int ra = stg + (16 * (g >> 1) + 4 * (g & 1) + (n >> 2)) * 160 + (n & 3) * 8;
        constexpr int PB = 4;
        u32x4 b[PB][4];
        auto fetch = [&](int ks) {
#pragma unroll
            for (int i = 0; i < 4; ++i) b[ks % PB][i] = __builtin_amdgcn_raw_buffer_load_b128(ro, so, (32 * ks + 8 * i) * (DM * 2), 0);
        };
#pragma unroll
        for (int ks = 0; ks < PB; ++ks) fetch(ks);
        FENCE();
#pragma unroll
        for (int ks = 0; ks < 32; ++ks) {
#pragma unroll
            for (int i = 0; i < 4; ++i) *(LAS u32x4*)(lds + sw + i * 1280) = b[ks % PB][i];
            if (ks + PB < 32) fetch(ks + PB);
            u32x4 f[4];
#pragma unroll
            for (int t = 0; t < 4; ++t) { const u32x2 lo = tr_read(lds, ra + t * 32), hi = tr_read(lds, ra + t * 32 + 1280); f[t] = (u32x4){lo.x, lo.y, hi.x, hi.y}; }
            wa[2 * ks] = f[0]; wa[2 * ks + 1] = f[1]; wv[ks] = f[2];
            *(LAS u32x4*)(lds + wl + ks * 1024) = f[3];
            asm volatile("" : "+a"(wa[2 * ks]), "+a"(wa[2 * ks + 1]), "+v"(wv[ks]));
            FENCE();
        }
    }
    wait_prog(rw, F_S4 + lane * 4, true, 1, budget);
    int o_rdy = -1;
    const int tok = tid >> 4, c4 = (tid & 15) * 4;
    const int lo_ = OFF_OF + w * 32768 + lane * 16, fo = F_O + (8 * w + ((lane >> 2) & 7)) * 128 + (lane & 3) * 4;
    const int tl = nb < 8 ? OFF_OF + w * 32768 + nb * 4096 + lane * 64 : OOB;
    const int bo = OFF_BO + (64 * nb + c4) * 4096 + tok * 2;
    for (int u = rep; u < 2 * NSEG; u += OUT_REP) {
        WG_BARRIER();
        S4S(0)
        while (!__all((lane & 3) != (u & 3) || o_rdy >= (u >> 1))) { o_rdy = (int)ld32(rw, fo, 0); asm volatile("" ::: "memory"); if (--budget < 0) break; }
        S4S(1)
        const unsigned tv = ld32(rw, tl, u * 131072);
        u16 bv[4];
#pragma unroll
        for (int j = 0; j < 4; ++j) bv[j] = __builtin_amdgcn_raw_buffer_load_b16(rw, bo, u * 32 + j * 4096, 16);
        const float gt = __uint_as_float(ld32(rw, OFF_GT + tok * 4, u * 64)), g1 = 1.0f - gt;
        FENCE();
        constexpr int RO = 16;
        u32x4 of[RO], w3[3];
#pragma unroll
        for (int i = 0; i < RO; ++i) { of[i] = ld128(rw, lo_ + (i & 3) * 1024, u * 131072 + (i >> 2) * 4096); FENCE(); }
        w3[0] = *(LAS u32x4*)(lds + wl); w3[1] = *(LAS u32x4*)(lds + wl + 1024);
        f32x4 acc[4];
#pragma unroll
        for (int i = 0; i < 32; ++i) {
            if (i + 2 < 32) w3[(i + 2) % 3] = *(LAS u32x4*)(lds + wl + (i + 2) * 1024);
            if (i == 0) asm volatile(M16 "%0, %4, %8, 0\n\t" M16 "%1, %5, %8, 0\n\t" M16 "%2, %6, %8, 0\n\t" M16 "%3, %7, %8, 0"
                                     : "=&v"(acc[0]), "=&v"(acc[1]), "=&v"(acc[2]), "=&v"(acc[3]) : "a"(wa[0]), "a"(wa[1]), "v"(wv[0]), "v"(w3[0]), "v"(of[0]) : "memory");
            else asm volatile(M16 "%0, %4, %8, %0\n\t" M16 "%1, %5, %8, %1\n\t" M16 "%2, %6, %8, %2\n\t" M16 "%3, %7, %8, %3"
                              : "+v"(acc[0]), "+v"(acc[1]), "+v"(acc[2]), "+v"(acc[3]) : "a"(wa[2 * i]), "a"(wa[2 * i + 1]), "v"(wv[i]), "v"(w3[i % 3]), "v"(of[i % RO]) : "memory");
            if (i + RO < 32) of[i % RO] = ld128(rw, lo_ + (i & 3) * 1024, u * 131072 + ((i + RO) >> 2) * 4096);
        }
        asm volatile("s_nop 7" : "+v"(acc[0]), "+v"(acc[1]), "+v"(acc[2]), "+v"(acc[3]) : "v"(tv) : "memory");
        S4S(2)
#pragma unroll
        for (int t = 0; t < 4; ++t) *(LAS f32x4*)(lds + O_STG + (w * 16 + n) * 272 + t * 64 + g * 16) = acc[t];
        WG_BARRIER();
        S4S(3)
        f32x4 v = *(LAS f32x4*)(lds + O_STG + tok * 272 + c4 * 4);
#pragma unroll
        for (int ww = 1; ww < 4; ++ww) v += *(LAS f32x4*)(lds + O_STG + (ww * 16 + tok) * 272 + c4 * 4);
        __builtin_nontemporal_store((u32x2){pack2(gt * v[0] + g1 * bf2f(bv[0]), gt * v[1] + g1 * bf2f(bv[1])), pack2(gt * v[2] + g1 * bf2f(bv[2]), gt * v[3] + g1 * bf2f(bv[3]))},
                                    (u32x2*)(a.out + (size_t)(u * 16 + tok) * DM + 64 * nb + c4));
        S4S(4)
    }
}
#define STEP_ASM \
    "v_readfirstlane_b32 s36, v0\n" \
    "v_readfirstlane_b32 s37, v1\n" \
    "v_readfirstlane_b32 s38, v2\n" \
    "v_readfirstlane_b32 s39, v3\n" \
    "v_readfirstlane_b32 s40, v4\n" \
    "v_readfirstlane_b32 s41, v5\n" \
    "v_readfirstlane_b32 s42, v6\n" \
    "v_readfirstlane_b32 s43, v7\n" \
    "v_readfirstlane_b32 s28, v8\n" \
    "v_readfirstlane_b32 s29, v9\n" \
    "v_readfirstlane_b32 s30, v10\n" \
    "v_readfirstlane_b32 s31, v11\n" \
    "v_readfirstlane_b32 s32, v12\n" \
    "v_readfirstlane_b32 s33, v13\n" \
    "v_readfirstlane_b32 s34, v14\n" \
    "v_readfirstlane_b32 s35, v15\n" \
    "v_readfirstlane_b32 s24, v16\n" \
    "v_readfirstlane_b32 s25, v17\n" \
    "v_readfirstlane_b32 s26, v18\n" \
    "v_readfirstlane_b32 s27, v19\n" \
    "v_readfirstlane_b32 s60, v24\n" \
    "v_readfirstlane_b32 s61, v25\n" \
    "v_readfirstlane_b32 s62, v26\n" \
    "v_readfirstlane_b32 s63, v27\n" \
    "v_readfirstlane_b32 s64, v28\n" \
    "v_readfirstlane_b32 s65, v29\n" \
    "v_readfirstlane_b32 s66, v30\n" \
    "v_readfirstlane_b32 s67, v31\n" \
    "v_mov_b64 v[40:41], 0\n" \
    "v_mov_b64 v[42:43], 0\n" \
    "v_mov_b64 v[44:45], 0\n" \
    "v_mov_b64 v[46:47], 0\n" \
    "v_mov_b64 v[48:49], 0\n" \
    "v_mov_b64 v[50:51], 0\n" \
    "v_mov_b64 v[52:53], 0\n" \
    "v_mov_b64 v[54:55], 0\n" \
    "s_mov_b32 s68, m0\n" \
    "s_getpc_b64 s[96:97]\n" \
    "14:\n" \
    "s_add_u32 s96, s96, 2f-14b\n" \
    "s_addc_u32 s97, s97, 0\n" \
    "s_lshl_b32 s72, s60, 10\n" \
    "s_add_i32 s73, s72, 4096\n" \
    "s_lshr_b32 s74, s72, 2\n" \
    "s_add_i32 s74, s74, 8192\n" \
    "s_lshl_b32 s95, s72, 1\n" \
    "s_add_i32 s95, s95, 16384\n" \
    "s_mov_b32 s81, 0x3e0293ee\n" \
    "s_mov_b32 s82, 0x53800000\n" \
    "s_lshl_b32 s83, s60, 6\n" \
    "s_and_b32 s83, s83, 128\n" \
    "v_or_b32 v109, s60, v127\n" \
    "v_min_u32 v109, 1, v109\n" \
    "v_lshlrev_b32 v109, 30, v109\n" \
    "0:\n" \
    "s_and_b32 s75, s36, 3\n" \
    "s_lshl_b32 s75, s75, 17\n" \
    "s_add_i32 s75, s75, s26\n" \
    "s_add_i32 s69, s75, s72\n" \
    "s_add_i32 s70, s69, 4096\n" \
    "s_add_i32 s71, s75, 16384\n" \
    "s_mov_b32 m0, s72\n" \
    "s_cmp_eq_u32 s36, 0\n" \
    "s_cbranch_scc1 3f\n" \
    "12:\n" \
    "s_sub_i32 s90, s37, s29\n" \
    "s_mul_i32 s90, s90, s25\n" \
    "s_ashr_i32 s90, s90, 12\n" \
    "s_add_i32 s90, s90, s24\n" \
    "s_add_i32 s90, s90, -116\n" \
    "1:\n" \
    "s_memtime vcc\n" \
    "s_waitcnt lgkmcnt(0)\n" \
    "s_sub_i32 s75, s90, vcc_lo\n" \
    "s_cmp_gt_i32 s75, 112\n" \
    "s_cbranch_scc1 1b\n" \
    "s_max_i32 s75, s75, 0\n" \
    "s_and_b32 s75, s75, -4\n" \
    "s_sub_u32 vcc_lo, s96, s75\n" \
    "s_subb_u32 vcc_hi, s97, 0\n" \
    "s_setpc_b64 vcc\n" \
    "s_nop 0\n" \
    "s_nop 0\n" \
    "s_nop 0\n" \
    "s_nop 0\n" \
    "s_nop 0\n" \
    "s_nop 0\n" \
    "s_nop 0\n" \
    "s_nop 0\n" \
    "s_nop 0\n" \
    "s_nop 0\n" \
    "s_nop 0\n" \
    "s_nop 0\n" \
    "s_nop 0\n" \
    "s_nop 0\n" \
    "s_nop 0\n" \
    "s_nop 0\n" \
    "s_nop 0\n" \
    "s_nop 0\n" \
    "s_nop 0\n" \
    "s_nop 0\n" \
    "s_nop 0\n" \
    "s_nop 0\n" \
    "s_nop 0\n" \
    "s_nop 0\n" \
    "s_nop 0\n" \
    "s_nop 0\n" \
    "s_nop 0\n" \
    "s_nop 0\n" \
    "2:\n" \
    "buffer_load_dwordx4 v127, %[rs], s69 offen sc1 lds\n" \
    "s_mov_b32 m0, s73\n" \
    "s_nop 0\n" \
    "buffer_load_dwordx4 v127, %[rs], s70 offen sc1 lds\n" \
    "s_movk_i32 m0, 0x2000\n" \
    "s_mov_b32 exec_lo, s27\n" \
    "s_mov_b32 exec_hi, s27\n" \
    "buffer_load_dwordx4 v127, %[rs], s71 offen sc1 lds\n" \
    "s_mov_b64 exec, -1\n" \
    "v_bfe_u32 v0, v127, 8, 2\n" \
    "v_subrev_u32 v0, s60, v0\n" \
    "v_mul_i32_i24 v0, -1088, v0\n" \
    "v_add_u32 v0, v0, v129\n" \
    "v_lshrrev_b32 v1, 2, v127\n" \
    "s_add_i32 s80, s36, -2\n" \
    "v_mov_b32 v2, s80\n" \
    "s_mov_b64 exec, 1\n" \
    "buffer_store_dword v2, off, %[rw], s65 sc1\n" \
    "s_mov_b32 exec_lo, s66\n" \
    "s_mov_b32 exec_hi, s66\n" \
    "ds_read2_b32 v[96:97], v0 offset0:0 offset1:144\n" \
    "s_mov_b64 exec, -1\n" \
    "s_lshl_b32 s80, s36, 16\n" \
    "s_add_i32 s80, s80, s67\n" \
    "v_mov_b64 v[40:41], 0\n" \
    "v_mov_b64 v[42:43], 0\n" \
    "v_mov_b64 v[44:45], 0\n" \
    "v_mov_b64 v[46:47], 0\n" \
    "v_mov_b64 v[48:49], 0\n" \
    "v_mov_b64 v[50:51], 0\n" \
    "v_mov_b64 v[52:53], 0\n" \
    "v_mov_b64 v[54:55], 0\n" \
    "s_waitcnt lgkmcnt(0)\n" \
    "buffer_store_dword v96, v1, %[rw], s80 offen sc1\n" \
    "buffer_store_dword v97, v1, %[rw], s80 offen offset:256 sc1\n" \
    "3:\n" \
    "ds_read_b128 v[72:75], v133 offset:0\n" \
    "ds_read_b128 v[76:79], v133 offset:1024\n" \
    "ds_read_b128 v[80:83], v133 offset:2048\n" \
    "ds_read_b128 v[84:87], v133 offset:3072\n" \
    "ds_read_b128 v[88:91], v133 offset:49152\n" \
    "ds_read_b128 v[92:95], v133 offset:50176\n" \
    "s_add_i32 s91, s36, 1\n" \
    "s_and_b32 s78, s36, 3\n" \
    "s_lshl_b32 s78, s78, 14\n" \
    "s_add_i32 s77, s78, s61\n" \
    "s_bitcmp1_b32 s36, 0\n" \
    "s_cselect_b32 s79, 0x90000, 0\n" \
    "s_lshl_b32 s92, s91, 18\n" \
    "s_add_i32 s92, s92, s64\n" \
    "s_mov_b32 m0, s95\n" \
    "s_add_i32 s78, s78, 0x1a0000\n" \
    "s_add_i32 s93, s92, 0x1000000\n" \
    "v_mov_b64 v[56:57], 0\n" \
    "v_mov_b64 v[58:59], 0\n" \
    "v_mov_b64 v[60:61], 0\n" \
    "v_mov_b64 v[62:63], 0\n" \
    "v_mov_b64 v[64:65], 0\n" \
    "v_mov_b64 v[66:67], 0\n" \
    "v_mov_b64 v[68:69], 0\n" \
    "v_mov_b64 v[70:71], 0\n" \
    "v_mov_b64 v[98:99], 0\n" \
    "v_mov_b32 v101, 0\n" \
    "v_mov_b32 v106, 0\n" \
    "v_mov_b32 v103, s91\n" \
    "s_sub_i32 s76, s40, s24\n" \
    "s_min_i32 s94, s91, s38\n" \
    "s_lshl_b32 s94, s94, 18\n" \
    "s_andn2_b32 s80, 0x1000000, s66\n" \
    "s_add_i32 s94, s94, s64\n" \
    "s_add_i32 s94, s94, s80\n" \
    "v_lshlrev_b32 v107, 1, v127\n" \
    "s_waitcnt vmcnt(3) lgkmcnt(0)\n" \
    "s_barrier\n" \
    "ds_read_b128 v[0:3], v125 offset:0\n" \
    "s_memtime s[86:87]\n" \
    "v_mfma_f32_16x16x32_bf16 v[20:23], v[120:123], v[120:123], 0\n" \
    "ds_read_b128 v[4:7], v125 offset:1024\n" \
    "ds_read_b128 v[8:11], v125 offset:2048\n" \
    "v_mfma_f32_16x16x32_bf16 v[20:23], v[120:123], v[120:123], 0\n" \
    "ds_read_b128 v[12:15], v125 offset:3072\n" \
    "buffer_load_dword v110, v107, %[rw], s94 offen sc1\n" \
    "v_mfma_f32_16x16x32_bf16 v[20:23], v[120:123], v[120:123], 0\n" \
    "s_waitcnt lgkmcnt(3)\n" \
    "v_smfmac_f32_16x16x64_bf16 v[40:43], v[0:3], a[0:7], v124\n" \
    "ds_read_b128 v[16:19], v125 offset:4096\n" \
    "ds_read_b128 v[24:27], v125 offset:5120\n" \
    "v_smfmac_f32_16x16x64_bf16 v[44:47], v[0:3], a[8:15], v124\n" \
    "ds_read_b128 v[28:31], v125 offset:6144\n" \
    "ds_read_b128 v[32:35], v125 offset:7168\n" \
    "v_smfmac_f32_16x16x64_bf16 v[56:59], v[0:3], v[72:79], v124\n" \
    "ds_read_b128 v[36:39], v126 offset:0\n" \
    "v_smfmac_f32_16x16x64_bf16 v[60:63], v[0:3], v[80:87], v124\n" \
    "s_waitcnt lgkmcnt(7)\n" \
    "v_smfmac_f32_16x16x64_bf16 v[40:43], v[4:7], a[16:23], v124\n" \
    "ds_read_b128 v[72:75], v133 offset:16384\n" \
    "ds_read_b128 v[76:79], v133 offset:17408\n" \
    "v_max3_u32 v106, v106, v0, v1\n" \
    "v_smfmac_f32_16x16x64_bf16 v[44:47], v[4:7], a[24:31], v124\n" \
    "ds_read_b128 v[80:83], v133 offset:18432\n" \
    "ds_read_b128 v[84:87], v133 offset:19456\n" \
    "v_max3_u32 v106, v106, v2, v3\n" \
    "v_smfmac_f32_16x16x64_bf16 v[56:59], v[4:7], v[88:95], v124\n" \
    "v_max3_u32 v106, v106, v4, v5\n" \
    "v_smfmac_f32_16x16x64_bf16 v[60:63], v[4:7], v[168:175], v124\n" \
    "s_waitcnt lgkmcnt(4)\n" \
    "v_max3_u32 v106, v106, v6, v7\n" \
    "v_smfmac_f32_16x16x64_bf16 v[40:43], v[8:11], a[32:39], v124\n" \
    "ds_read_b128 v[88:91], v133 offset:32768\n" \
    "ds_read_b128 v[92:95], v133 offset:33792\n" \
    "v_max3_u32 v106, v106, v8, v9\n" \
    "v_smfmac_f32_16x16x64_bf16 v[44:47], v[8:11], a[40:47], v124\n" \
    "v_max3_u32 v106, v106, v10, v11\n" \
    "v_smfmac_f32_16x16x64_bf16 v[56:59], v[8:11], v[176:183], v124\n" \
    "v_max3_u32 v106, v106, v12, v13\n" \
    "v_smfmac_f32_16x16x64_bf16 v[60:63], v[8:11], v[184:191], v124\n" \
    "v_max3_u32 v106, v106, v14, v15\n" \
    "v_smfmac_f32_16x16x64_bf16 v[40:43], v[12:15], a[48:55], v124\n" \
    "v_max3_u32 v106, v106, v16, v17\n" \
    "v_smfmac_f32_16x16x64_bf16 v[44:47], v[12:15], a[56:63], v124\n" \
    "s_waitcnt lgkmcnt(0)\n" \
    "v_max3_u32 v106, v106, v18, v19\n" \
    "v_smfmac_f32_16x16x64_bf16 v[56:59], v[12:15], v[72:79], v124\n" \
    "s_add_i32 s80, s86, s76\n" \
    "s_mul_hi_u32 s80, s80, s41\n" \
    "v_max3_u32 v106, v106, v24, v25\n" \
    "v_smfmac_f32_16x16x64_bf16 v[40:43], v[16:19], a[64:71], v124\n" \
    "ds_read_b128 v[72:75], v133 offset:34816\n" \
    "ds_read_b128 v[76:79], v133 offset:35840\n" \
    "v_max3_u32 v106, v106, v26, v27\n" \
    "v_smfmac_f32_16x16x64_bf16 v[44:47], v[16:19], a[72:79], v124\n" \
    "s_add_i32 s80, s80, s29\n" \
    "v_max3_u32 v106, v106, v28, v29\n" \
    "v_smfmac_f32_16x16x64_bf16 v[40:43], v[24:27], a[80:87], v124\n" \
    "v_max3_u32 v106, v106, v30, v31\n" \
    "v_smfmac_f32_16x16x64_bf16 v[44:47], v[24:27], a[88:95], v124\n" \
    "v_max3_u32 v106, v106, v32, v33\n" \
    "v_smfmac_f32_16x16x64_bf16 v[40:43], v[28:31], a[96:103], v124\n" \
    "v_max3_u32 v106, v106, v34, v35\n" \
    "v_smfmac_f32_16x16x64_bf16 v[44:47], v[28:31], a[104:111], v124\n" \
    "ds_read_b128 v[20:23], v127 offset:19456\n" \
    "v_max3_u32 v106, v106, v36, v37\n" \
    "v_smfmac_f32_16x16x64_bf16 v[40:43], v[32:35], a[112:119], v124\n" \
    "v_max3_u32 v106, v106, v38, v39\n" \
    "v_smfmac_f32_16x16x64_bf16 v[44:47], v[32:35], a[120:127], v124\n" \
    "v_cmp_eq_u32 vcc, -1, v106\n" \
    "v_smfmac_f32_16x16x64_bf16 v[40:43], v[36:39], a[128:135], v124\n" \
    "v_add_u32 v106, s63, v133\n" \
    "v_smfmac_f32_16x16x64_bf16 v[44:47], v[36:39], a[136:143], v124\n" \
    "s_cbranch_vccnz 5f\n" \
    "6:\n" \
    "v_mov_b32 v102, s80\n" \
    "v_smfmac_f32_16x16x64_bf16 v[60:63], v[12:15], v[80:87], v124\n" \
    "s_waitcnt lgkmcnt(1)\n" \
    "v_add_f32 v40, v40, v42\n" \
    "v_smfmac_f32_16x16x64_bf16 v[56:59], v[16:19], v[88:95], v124\n" \
    "buffer_store_dwordx2 v[102:103], v109, %[rs], s77 offen sc1\n" \
    "v_add_f32 v41, v41, v43\n" \
    "v_smfmac_f32_16x16x64_bf16 v[60:63], v[16:19], v[72:79], v124\n" \
    "v_add_f32 v44, v44, v46\n" \
    "v_smfmac_f32_16x16x64_bf16 v[48:51], v[0:3], a[144:151], v124\n" \
    "v_add_f32 v45, v45, v47\n" \
    "v_smfmac_f32_16x16x64_bf16 v[52:55], v[0:3], a[152:159], v124\n" \
    "v_mul_f32 v40, s81, v40\n" \
    "v_smfmac_f32_16x16x64_bf16 v[48:51], v[4:7], a[160:167], v124\n" \
    "ds_read_b128 v[0:3], v127 offset:16384\n" \
    "v_mul_f32 v44, s81, v44\n" \
    "v_smfmac_f32_16x16x64_bf16 v[52:55], v[4:7], a[168:175], v124\n" \
    "v_mul_f32 v41, s81, v41\n" \
    "v_smfmac_f32_16x16x64_bf16 v[48:51], v[8:11], a[176:183], v124\n" \
    "ds_read_b128 v[4:7], v127 offset:18432\n" \
    "v_mul_f32 v45, s81, v45\n" \
    "v_smfmac_f32_16x16x64_bf16 v[52:55], v[8:11], a[184:191], v124\n" \
    "v_cvt_pk_bf16_f32 v107, v40, v44\n" \
    "v_smfmac_f32_16x16x64_bf16 v[48:51], v[12:15], a[192:199], v124\n" \
    "ds_read_b128 v[8:11], v127 offset:20480\n" \
    "v_cvt_pk_bf16_f32 v108, v41, v45\n" \
    "v_smfmac_f32_16x16x64_bf16 v[52:55], v[12:15], a[200:207], v124\n" \
    "ds_write2_b32 v128, v107, v108 offset0:0 offset1:80\n" \
    "v_smfmac_f32_16x16x64_bf16 v[48:51], v[16:19], a[208:215], v124\n" \
    "ds_read_b128 v[12:15], v127 offset:22528\n" \
    "ds_read_b128 v[40:43], v127 offset:21504\n" \
    "v_smfmac_f32_16x16x64_bf16 v[52:55], v[16:19], a[216:223], v124\n" \
    "ds_read_b128 v[44:47], v127 offset:23552\n" \
    "v_smfmac_f32_16x16x64_bf16 v[48:51], v[24:27], a[224:231], v124\n" \
    "ds_read_b128 v[16:19], v127 offset:17408\n" \
    "v_smfmac_f32_16x16x64_bf16 v[52:55], v[24:27], a[232:239], v124\n" \
    "s_waitcnt lgkmcnt(4)\n" \
    "s_barrier\n" \
    "ds_read_b128 v[72:75], v130 offset:0\n" \
    "v_smfmac_f32_16x16x64_bf16 v[48:51], v[28:31], a[240:247], v124\n" \
    "ds_read_b128 v[76:79], v130 offset:64\n" \
    "ds_read_b128 v[80:83], v130 offset:128\n" \
    "ds_read_b128 v[84:87], v130 offset:192\n" \
    "v_smfmac_f32_16x16x64_bf16 v[52:55], v[28:31], a[248:255], v124\n" \
    "v_smfmac_f32_16x16x64_bf16 v[48:51], v[32:35], v[136:143], v124\n" \
    "v_smfmac_f32_16x16x64_bf16 v[52:55], v[32:35], v[144:151], v124\n" \
    "v_smfmac_f32_16x16x64_bf16 v[48:51], v[36:39], v[152:159], v124\n" \
    "v_smfmac_f32_16x16x64_bf16 v[52:55], v[36:39], v[160:167], v124\n" \
    "s_waitcnt lgkmcnt(3)\n" \
    "v_mfma_f32_16x16x32_bf16 v[88:91], v[0:3], v[72:75], 0\n" \
    "s_waitcnt lgkmcnt(0)\n" \
    "v_add_f32 v48, v48, v50\n" \
    "v_add_f32 v49, v49, v51\n" \
    "v_mfma_f32_16x16x32_bf16 v[88:91], v[4:7], v[76:79], v[88:91]\n" \
    "v_add_f32 v52, v52, v54\n" \
    "v_add_f32 v53, v53, v55\n" \
    "v_mfma_f32_16x16x32_bf16 v[88:91], v[8:11], v[80:83], v[88:91]\n" \
    "v_cvt_pk_bf16_f32 v107, v48, v52\n" \
    "v_cvt_pk_bf16_f32 v108, v49, v53\n" \
    "ds_write2_b32 v129, v107, v108 offset0:0 offset1:144\n" \
    "v_mfma_f32_16x16x32_bf16 v[88:91], v[12:15], v[84:87], v[88:91]\n" \
    "ds_read_b64 v[0:1], v132 offset:0\n" \
    "ds_read_b64 v[2:3], v132 offset:1024\n" \
    "v_smfmac_f32_16x16x64_bf16 v[56:59], v[24:27], v[192:199], v124\n" \
    "ds_read_b64 v[4:5], v132 offset:512\n" \
    "ds_read_b64 v[6:7], v132 offset:1536\n" \
    "v_smfmac_f32_16x16x64_bf16 v[60:63], v[24:27], v[200:207], v124\n" \
    "v_mfma_f32_16x16x32_bf16 v[92:95], v[16:19], v[72:75], 0\n" \
    "v_exp_f32 v88, v88\n" \
    "s_waitcnt lgkmcnt(4)\n" \
    "s_barrier\n" \
    "ds_read_b128 v[48:51], v131 offset:0\n" \
    "ds_read_b128 v[52:55], v131 offset:128\n" \
    "v_mfma_f32_16x16x32_bf16 v[92:95], v[20:23], v[76:79], v[92:95]\n" \
    "ds_read_b128 v[8:11], v106 offset:0\n" \
    "v_exp_f32 v89, v89\n" \
    "v_mfma_f32_16x16x32_bf16 v[92:95], v[40:43], v[80:83], v[92:95]\n" \
    "ds_read_b128 v[12:15], v106 offset:1024\n" \
    "v_exp_f32 v90, v90\n" \
    "v_mfma_f32_16x16x32_bf16 v[92:95], v[44:47], v[84:87], v[92:95]\n" \
    "ds_read_b128 v[16:19], v106 offset:16384\n" \
    "v_exp_f32 v91, v91\n" \
    "v_smfmac_f32_16x16x64_bf16 v[56:59], v[28:31], v[208:215], v124\n" \
    "ds_read_b128 v[20:23], v106 offset:17408\n" \
    "s_waitcnt lgkmcnt(6)\n" \
    "v_cvt_pk_bf16_f32 v88, v88, v89\n" \
    "v_smfmac_f32_16x16x64_bf16 v[60:63], v[28:31], v[216:223], v124\n" \
    "ds_read_b128 v[40:43], v106 offset:2048\n" \
    "ds_read_b128 v[44:47], v106 offset:3072\n" \
    "v_cvt_pk_bf16_f32 v89, v90, v91\n" \
    "s_waitcnt lgkmcnt(6)\n" \
    "v_smfmac_f32_16x16x64_bf16 v[64:67], v[48:51], v[72:79], v124\n" \
    "ds_read_b128 v[24:27], v106 offset:6144\n" \
    "ds_read_b128 v[28:31], v106 offset:7168\n" \
    "v_exp_f32 v92, v92\n" \
    "v_smfmac_f32_16x16x64_bf16 v[64:67], v[52:55], v[80:87], v124\n" \
    "ds_read_b128 v[72:75], v106 offset:18432\n" \
    "ds_read_b128 v[76:79], v106 offset:19456\n" \
    "v_exp_f32 v93, v93\n" \
    "v_smfmac_f32_16x16x64_bf16 v[56:59], v[32:35], v[224:231], v124\n" \
    "ds_read_b128 v[80:83], v106 offset:4096\n" \
    "ds_read_b128 v[84:87], v106 offset:5120\n" \
    "v_exp_f32 v94, v94\n" \
    "v_smfmac_f32_16x16x64_bf16 v[60:63], v[32:35], v[232:239], v124\n" \
    "s_waitcnt lgkmcnt(6)\n" \
    "s_cmp_eq_u32 s36, 63\n" \
    "v_add_f32 v64, v64, v66\n" \
    "v_add_f32 v65, v65, v67\n" \
    "v_smfmac_f32_16x16x64_bf16 v[56:59], v[36:39], v[240:247], v124\n" \
    "ds_read_b128 v[48:51], v106 offset:20480\n" \
    "ds_read_b128 v[52:55], v106 offset:21504\n" \
    "v_exp_f32 v95, v95\n" \
    "v_cvt_pk_bf16_f32 v90, v92, v93\n" \
    "v_smfmac_f32_16x16x64_bf16 v[60:63], v[36:39], v[248:255], v124\n" \
    "ds_read_b128 v[32:35], v106 offset:22528\n" \
    "ds_read_b128 v[36:39], v106 offset:23552\n" \
    "v_cvt_pk_bf16_f32 v91, v94, v95\n" \
    "v_exp_f32 v64, v64\n" \
    "v_exp_f32 v65, v65\n" \
    "v_mfma_f32_16x16x32_bf16 v[102:105], v[88:91], v[120:123], 0\n" \
    "v_swap_b32 v1, v2\n" \
    "v_swap_b32 v5, v6\n" \
    "v_cvt_pk_bf16_f32 v100, v64, v65\n" \
    "v_add_f32 v56, v56, v58\n" \
    "v_smfmac_f32_16x16x64_bf16 v[68:71], v[88:91], v[0:7], v124\n" \
    "ds_read_b128 v[0:3], v133 offset:51200\n" \
    "ds_read_b128 v[4:7], v133 offset:52224\n" \
    "v_add_f32 v57, v57, v59\n" \
    "v_add_f32 v60, v60, v62\n" \
    "v_add_f32 v61, v61, v63\n" \
    "v_cvt_pk_bf16_f32 v96, v56, v57\n" \
    "v_mfma_f32_16x16x16_bf16 v[102:105], v[100:101], v[120:121], v[102:105]\n" \
    "v_cvt_pk_bf16_f32 v97, v60, v61\n" \
    "s_cbranch_scc1 8f\n" \
    "s_nop 0\n" \
    "v_smfmac_f32_16x16x32_bf16 v[68:71], v[100:101], v[96:99], v124\n" \
    "v_mov_b64 v[56:57], 0\n" \
    "v_mov_b64 v[58:59], 0\n" \
    "v_rcp_f32 v104, v102\n" \
    "v_rcp_f32 v105, v103\n" \
    "v_mul_f32 v68, v68, v104\n" \
    "v_mul_f32 v70, v70, v104\n" \
    "v_mul_f32 v69, v69, v105\n" \
    "v_mul_f32 v71, v71, v105\n" \
    "v_cvt_pk_bf16_f32 v107, v68, v70\n" \
    "v_cvt_pk_bf16_f32 v108, v69, v71\n" \
    "ds_write2_b32 v129, v107, v108 offset0:64 offset1:208\n" \
    "v_mfma_f32_16x16x32_bf16 v[60:63], v[56:59], v[120:123], 0\n" \
    "v_max3_f32 v102, v102, v103, v104\n" \
    "v_max_f32 v102, v102, v105\n" \
    "v_mfma_f32_16x16x32_bf16 v[88:91], v[56:59], v[120:123], 0\n" \
    "v_cmp_nge_f32 vcc, s82, v102\n" \
    "v_add_u32 v100, s83, v131\n" \
    "v_mfma_f32_16x16x32_bf16 v[92:95], v[56:59], v[120:123], 0\n" \
    "v_sub_u32 v101, v131, s83\n" \
    "v_xor_b32 v108, 0x2000, v133\n" \
    "v_mfma_f32_16x16x32_bf16 v[64:67], v[56:59], v[120:123], 0\n" \
    "v_and_b32 v107, 0x3c0, v127\n" \
    "v_add_u32 v107, v107, v127\n" \
    "s_cbranch_vccnz 7f\n" \
    "s_waitcnt lgkmcnt(0)\n" \
    "s_barrier\n" \
    "ds_read_b128 v[68:71], v100 offset:256\n" \
    "ds_read_b128 v[102:105], v101 offset:384\n" \
    "v_mfma_f32_16x16x32_bf16 v[64:67], v[56:59], v[120:123], 0\n" \
    "buffer_load_dwordx2 v[98:99], v107, %[rs], s78 offen sc1\n" \
    "v_mfma_f32_16x16x32_bf16 v[64:67], v[56:59], v[120:123], 0\n" \
    "buffer_load_dword v100, off, %[rw], s62 sc1\n" \
    "v_mfma_f32_16x16x32_bf16 v[64:67], v[56:59], v[120:123], 0\n" \
    "s_waitcnt lgkmcnt(1)\n" \
    "v_smfmac_f32_16x16x64_bf16 v[56:59], v[68:71], v[8:15], v124\n" \
    "s_waitcnt lgkmcnt(0)\n" \
    "v_smfmac_f32_16x16x64_bf16 v[56:59], v[102:105], v[16:23], v124\n" \
    "v_smfmac_f32_16x16x64_bf16 v[60:63], v[68:71], v[40:47], v124\n" \
    "ds_read_b128 v[16:19], v108 offset:51200\n" \
    "ds_read_b128 v[20:23], v108 offset:52224\n" \
    "v_smfmac_f32_16x16x64_bf16 v[60:63], v[102:105], v[72:79], v124\n" \
    "v_add_f32 v56, v56, v58\n" \
    "v_smfmac_f32_16x16x64_bf16 v[88:91], v[68:71], v[80:87], v124\n" \
    "v_add_f32 v57, v57, v59\n" \
    "v_smfmac_f32_16x16x64_bf16 v[88:91], v[102:105], v[48:55], v124\n" \
    "v_add_f32 v60, v60, v62\n" \
    "v_smfmac_f32_16x16x64_bf16 v[92:95], v[68:71], v[24:31], v124\n" \
    "v_add_f32 v61, v61, v63\n" \
    "v_smfmac_f32_16x16x64_bf16 v[92:95], v[102:105], v[32:39], v124\n" \
    "v_add_f32 v88, v88, v90\n" \
    "v_smfmac_f32_16x16x64_bf16 v[64:67], v[68:71], v[0:7], v124\n" \
    "s_waitcnt lgkmcnt(0)\n" \
    "v_add_f32 v89, v89, v91\n" \
    "v_smfmac_f32_16x16x64_bf16 v[64:67], v[102:105], v[16:23], v124\n" \
    "v_add_f32 v92, v92, v94\n" \
    "v_add_f32 v93, v93, v95\n" \
    "v_cvt_pk_bf16_f32 v8, v56, v60\n" \
    "v_cvt_pk_bf16_f32 v10, v57, v61\n" \
    "v_cvt_pk_bf16_f32 v9, v88, v92\n" \
    "v_cvt_pk_bf16_f32 v11, v89, v93\n" \
    "buffer_store_dwordx4 v[8:11], v134, %[rs], s79 offen sc1\n" \
    "v_add_f32 v64, v64, v66\n" \
    "v_add_f32 v65, v65, v67\n" \
    "v_cvt_pk_bf16_f32 v64, v64, v65\n" \
    "buffer_store_dword v64, v135, %[rs], s79 offen sc1\n" \
    "s_memtime s[88:89]\n" \
    "s_memrealtime s[44:45]\n" \
    "s_and_b32 s75, s91, 3\n" \
    "s_lshl_b32 s75, s75, 17\n" \
    "s_memrealtime s[46:47]\n" \
    "s_add_i32 s75, s75, s26\n" \
    "s_add_i32 s69, s75, s72\n" \
    "s_memrealtime s[48:49]\n" \
    "s_add_i32 s70, s69, 4096\n" \
    "s_add_i32 s71, s75, 16384\n" \
    "s_memrealtime s[50:51]\n" \
    "s_sub_i32 s42, s42, s32\n" \
    "s_max_i32 s42, s42, s33\n" \
    "s_memrealtime s[52:53]\n" \
    "s_add_i32 s40, s40, 64\n" \
    "s_nop 0\n" \
    "s_memrealtime s[54:55]\n" \
    "s_nop 1\n" \
    "s_memrealtime s[56:57]\n" \
    "s_nop 1\n" \
    "s_memrealtime vcc\n" \
    "v_and_b32 v18, 0x300, v127\n" \
    "v_add_u32 v18, v18, v127\n" \
    "v_and_b32 v19, 0x380, v127\n" \
    "v_add_u32 v19, v19, v127\n" \
    "s_waitcnt lgkmcnt(0)\n" \
    "s_add_i32 s44, s44, s46\n" \
    "s_add_i32 s48, s48, s50\n" \
    "s_add_i32 s52, s52, s54\n" \
    "s_add_i32 s44, s44, s48\n" \
    "s_add_i32 s52, s52, s56\n" \
    "s_add_i32 s44, s44, s52\n" \
    "s_add_i32 s80, s44, vcc_lo\n" \
    "s_sub_i32 s76, s80, s29\n" \
    "s_mov_b32 s29, s80\n" \
    "s_sub_i32 s80, s88, s24\n" \
    "s_mov_b32 s24, s88\n" \
    "s_mul_i32 s75, s76, s25\n" \
    "s_lshr_b32 s75, s75, 12\n" \
    "s_sub_i32 s75, s80, s75\n" \
    "s_mul_hi_u32 s80, s80, s41\n" \
    "s_sub_i32 s80, s76, s80\n" \
    "s_lshl_b32 s80, s80, 18\n" \
    "s_cmp_lt_u32 s76, 0x1000\n" \
    "s_cselect_b32 s75, s75, 0\n" \
    "s_cselect_b32 s80, s80, 0\n" \
    "s_add_i32 s25, s25, s75\n" \
    "s_add_i32 s41, s41, s80\n" \
    "s_cmp_eq_u32 s36, 1\n" \
    "s_cselect_b32 s28, s29, s28\n" \
    "s_sub_i32 s80, s88, s86\n" \
    "s_min_u32 s40, s40, s80\n" \
    "s_waitcnt vmcnt(2)\n" \
    "v_readfirstlane_b32 s80, v100\n" \
    "v_cmp_ne_u32 vcc, s91, v99\n" \
    "s_mov_b32 s43, 0\n" \
    "s_max_i32 s38, s38, s80\n" \
    "s_cmp_lt_i32 s38, s91\n" \
    "s_cselect_b64 exec, 0, -1\n" \
    "s_cselect_b32 s94, 8, 0\n" \
    "buffer_load_dwordx4 v19, %[rw], s92 offen sc1 lds\n" \
    "s_add_i32 m0, s95, 896\n" \
    "s_nop 0\n" \
    "buffer_load_dwordx4 v19, %[rw], s92 offen offset:128 sc1 lds\n" \
    "s_add_i32 m0, s95, 8192\n" \
    "s_nop 0\n" \
    "buffer_load_dwordx4 v18, %[rw], s93 offen sc1 lds\n" \
    "s_add_i32 m0, s95, 8960\n" \
    "s_nop 0\n" \
    "buffer_load_dwordx4 v18, %[rw], s93 offen offset:256 sc1 lds\n" \
    "s_mov_b32 m0, s72\n" \
    "s_mov_b64 exec, -1\n" \
    "s_cbranch_vccnz 15f\n" \
    "10:\n" \
    "v_subrev_u32 v28, s29, v98\n" \
    "v_min_i32 v28, 0x4000, v28\n" \
    "v_max_i32 v28, 0xffffc000, v28\n" \
    "s_nop 1\n" \
    "v_max_i32_dpp v28, v28, v28 row_shr:1 row_mask:0xf bank_mask:0xf\n" \
    "s_nop 1\n" \
    "v_max_i32_dpp v28, v28, v28 row_shr:2 row_mask:0xf bank_mask:0xf\n" \
    "s_nop 1\n" \
    "v_max_i32_dpp v28, v28, v28 row_shr:4 row_mask:0xf bank_mask:0xf\n" \
    "s_nop 1\n" \
    "v_max_i32_dpp v28, v28, v28 row_shr:8 row_mask:0xf bank_mask:0xf\n" \
    "s_add_i32 s76, s29, s42\n" \
    "s_nop 0\n" \
    "v_max_i32_dpp v28, v28, v28 row_bcast:15 row_mask:0xa bank_mask:0xf\n" \
    "s_nop 1\n" \
    "v_max_i32_dpp v28, v28, v28 row_bcast:31 row_mask:0xc bank_mask:0xf\n" \
    "s_nop 0\n" \
    "v_readlane_b32 s37, v28, 63\n" \
    "s_nop 0\n" \
    "s_add_i32 s37, s37, s76\n" \
    "s_cmp_lg_u32 s94, 0\n" \
    "s_cbranch_scc1 9f\n" \
    "s_mov_b32 s36, s91\n" \
    "s_branch 12b\n" \
    "5:\n" \
    "s_cmp_eq_u32 s36, 0\n" \
    "s_cbranch_scc1 6b\n" \
    "s_add_i32 s39, s39, -1\n" \
    "s_cmp_lt_i32 s39, 0\n" \
    "s_cbranch_scc1 6b\n" \
    "s_add_i32 s30, s30, 1\n" \
    "s_cmp_gt_u32 s36, 2\n" \
    "s_cselect_b32 s80, s31, 0\n" \
    "s_add_i32 s42, s42, s80\n" \
    "s_waitcnt lgkmcnt(0)\n" \
    "s_mov_b32 s37, s29\n" \
    "s_nop 7\n" \
    "s_barrier\n" \
    "s_branch 0b\n" \
    "15:\n" \
    "s_add_i32 s30, s30, 0x10000\n" \
    "s_add_i32 s39, s39, -1\n" \
    "s_cmp_lt_i32 s39, 0\n" \
    "s_cbranch_scc1 10b\n" \
    "s_sleep 4\n" \
    "buffer_load_dwordx2 v[98:99], v107, %[rs], s78 offen sc1\n" \
    "s_waitcnt vmcnt(0)\n" \
    "v_cmp_ne_u32 vcc, s91, v99\n" \
    "s_nop 1\n" \
    "s_cbranch_vccnz 15b\n" \
    "s_branch 10b\n" \
    "7:\n" \
    "s_mov_b32 s43, 2\n" \
    "8:\n" \
    "s_nop 7\n" \
    "s_waitcnt vmcnt(0)\n" \
    "9:\n" \
    "s_mov_b32 m0, s68\n" \
    "s_waitcnt lgkmcnt(0)\n" \
    "v_mov_b32 v0, s36\n" \
    "v_mov_b32 v1, s37\n" \
    "v_mov_b32 v2, s38\n" \
    "v_mov_b32 v3, s39\n" \
    "v_mov_b32 v4, s40\n" \
    "v_mov_b32 v5, s41\n" \
    "v_mov_b32 v6, s42\n" \
    "v_mov_b32 v7, s43\n" \
    "v_mov_b32 v8, s28\n" \
    "v_mov_b32 v9, s29\n" \
    "v_mov_b32 v10, s30\n" \
    "v_mov_b32 v11, s31\n" \
    "v_mov_b32 v12, s32\n" \
    "v_mov_b32 v13, s33\n" \
    "v_mov_b32 v14, s34\n" \
    "v_mov_b32 v15, s35\n" \
    "v_mov_b32 v16, s24\n" \
    "v_mov_b32 v17, s25\n" \
    "v_mov_b32 v18, s26\n" \
    "v_mov_b32 v19, s27\n" \
    "v_mov_b32 v20, s84\n" \
    "v_mov_b32 v21, s86\n" \
    "v_mov_b32 v22, s88\n" \
    "v_mov_b32 v23, s84\n" \
    "v_mov_b32 v24, s60\n" \
    "v_mov_b32 v25, s61\n" \
    "v_mov_b32 v26, s62\n" \
    "v_mov_b32 v27, s63\n" \
    "v_mov_b32 v28, s64\n" \
    "v_mov_b32 v29, s65\n" \
    "v_mov_b32 v30, s66\n" \
    "v_mov_b32 v31, s67\n" \
    ""
static_assert(2 * PART_BYTES == 589824 && 2 * OFF_CAN == 1703936 && OFF_PART == 0 && OFF_VF - OFF_KF == 2048 * 4096 * 2, "constants of the step block");
#define STEP_CLOBBER "v32", "v33", "v34", "v35", "v36", "v37", "v38", "v39", "v40", "v41", "v42", "v43", "v44", "v45", "v46", "v47", "v48", "v49", "v50", "v51", "v52", "v53", "v54", "v55", "v56", "v57", "v58", "v59", "v60", "v61", "v62", "v63", "v64", "v65", "v66", "v67", "v68", "v69", "v70", "v71", "v72", "v73", "v74", "v75", "v76", "v77", "v78", "v79", "v80", "v81", "v82", "v83", "v84", "v85", "v86", "v87", "v88", "v89", "v90", "v91", "v92", "v93", "v94", "v95", "v98", "v99", "v100", "v101", "v102", "v103", "v104", "v105", "v106", "v107", "v108", "v109", "v110", "s24", "s25", "s26", "s27", "s28", "s29", "s30", "s31", "s32", "s33", "s34", "s35", "s36", "s37", "s38", "s39", "s40", "s41", "s42", "s43", "s44", "s45", "s46", "s47", "s48", "s49", "s50", "s51", "s52", "s53", "s54", "s55", "s56", "s57", "s58", "s59", "s60", "s61", "s62", "s63", "s64", "s65", "s66", "s67", "s68", "s69", "s70", "s71", "s72", "s73", "s74", "s75", "s76", "s77", "s78", "s79", "s80", "s81", "s82", "s83", "s84", "s85", "s86", "s87", "s88", "s89", "s90", "s91", "s92", "s93", "s94", "s95", "s96", "s97", "vcc", "scc", "memory"
constexpr int NREP = 2;
#define OWNER_ASM \
    "s_getpc_b64 s[96:97]\n" \
    "14:\n" \
    "s_add_u32 s96, s96, 2f-14b\n" \
    "s_addc_u32 s97, s97, 0\n" \
    "s_mov_b32 %[np], 0\n" \
    "6:\n" \
    "buffer_load_dwordx2 v[0:1], %[ca], %[rs], %[cs] offen sc1\n" \
    "s_memtime s[58:59]\n" \
    "s_memrealtime s[44:45]\n" \
    "s_nop 1\n" \
    "s_memrealtime s[46:47]\n" \
    "s_nop 1\n" \
    "s_memrealtime s[48:49]\n" \
    "s_nop 1\n" \
    "s_memrealtime s[50:51]\n" \
    "s_nop 1\n" \
    "s_memrealtime s[52:53]\n" \
    "s_nop 1\n" \
    "s_memrealtime s[54:55]\n" \
    "s_nop 1\n" \
    "s_memrealtime s[56:57]\n" \
    "s_nop 1\n" \
    "s_memrealtime vcc\n" \
    "s_waitcnt lgkmcnt(0)\n" \
    "s_add_i32 s44, s44, s46\n" \
    "s_add_i32 s48, s48, s50\n" \
    "s_add_i32 s52, s52, s54\n" \
    "s_add_i32 s44, s44, s48\n" \
    "s_add_i32 s52, s52, s56\n" \
    "s_add_i32 s44, s44, s52\n" \
    "s_add_i32 s59, s44, vcc_lo\n" \
    "s_sub_i32 s44, s59, %[oT]\n" \
    "s_mov_b32 %[oT], s59\n" \
    "s_sub_i32 s45, s58, %[oC]\n" \
    "s_mov_b32 %[oC], s58\n" \
    "s_mul_i32 s46, s44, %[oR]\n" \
    "s_lshr_b32 s46, s46, 12\n" \
    "s_sub_i32 s46, s45, s46\n" \
    "s_cmp_lt_u32 s44, 0x1000\n" \
    "s_cselect_b32 s46, s46, 0\n" \
    "s_add_i32 %[oR], %[oR], s46\n" \
    "s_waitcnt vmcnt(0)\n" \
    "v_subrev_u32 v0, %[oT], v0\n" \
    "v_cmp_ne_u32 vcc, %[tag], v1\n" \
    "v_min_i32 v0, 0x4000, v0\n" \
    "v_max_i32 v0, 0xffffc000, v0\n" \
    "s_nop 0\n" \
    "s_cbranch_vccnz 7f\n" \
    "8:\n" \
    "v_max_i32_dpp v0, v0, v0 row_shr:1 row_mask:0xf bank_mask:0xf\n" \
    "s_nop 1\n" \
    "v_max_i32_dpp v0, v0, v0 row_shr:2 row_mask:0xf bank_mask:0xf\n" \
    "s_nop 1\n" \
    "v_max_i32_dpp v0, v0, v0 row_shr:4 row_mask:0xf bank_mask:0xf\n" \
    "s_nop 1\n" \
    "v_max_i32_dpp v0, v0, v0 row_shr:8 row_mask:0xf bank_mask:0xf\n" \
    "s_nop 1\n" \
    "v_max_i32_dpp v0, v0, v0 row_bcast:15 row_mask:0xa bank_mask:0xf\n" \
    "s_nop 1\n" \
    "v_max_i32_dpp v0, v0, v0 row_bcast:31 row_mask:0xc bank_mask:0xf\n" \
    "s_nop 1\n" \
    "v_readlane_b32 s44, v0, 63\n" \
    "s_add_i32 s45, %[oC], -116\n" \
    "s_add_i32 %[tm], s44, %[oT]\n" \
    "s_add_i32 s44, s44, %[c8]\n" \
    "s_mul_i32 s44, s44, %[oR]\n" \
    "s_ashr_i32 s44, s44, 12\n" \
    "s_add_i32 s44, s44, s45\n" \
    "1:\n" \
    "s_memtime vcc\n" \
    "s_waitcnt lgkmcnt(0)\n" \
    "s_sub_i32 s58, s44, vcc_lo\n" \
    "s_cmp_gt_i32 s58, 112\n" \
    "s_cbranch_scc1 1b\n" \
    "s_max_i32 s58, s58, 0\n" \
    "s_and_b32 s58, s58, -4\n" \
    "s_sub_u32 vcc_lo, s96, s58\n" \
    "s_subb_u32 vcc_hi, s97, 0\n" \
    "s_setpc_b64 vcc\n" \
    "s_nop 0\n" \
    "s_nop 0\n" \
    "s_nop 0\n" \
    "s_nop 0\n" \
    "s_nop 0\n" \
    "s_nop 0\n" \
    "s_nop 0\n" \
    "s_nop 0\n" \
    "s_nop 0\n" \
    "s_nop 0\n" \
    "s_nop 0\n" \
    "s_nop 0\n" \
    "s_nop 0\n" \
    "s_nop 0\n" \
    "s_nop 0\n" \
    "s_nop 0\n" \
    "s_nop 0\n" \
    "s_nop 0\n" \
    "s_nop 0\n" \
    "s_nop 0\n" \
    "s_nop 0\n" \
    "s_nop 0\n" \
    "s_nop 0\n" \
    "s_nop 0\n" \
    "s_nop 0\n" \
    "s_nop 0\n" \
    "s_nop 0\n" \
    "s_nop 0\n" \
    "2:\n" \
    "buffer_load_dwordx4 v[0:3], %[o], %[rs], %[pb] offen offset:0 sc1\n" \
    "buffer_load_dwordx4 v[4:7], %[o], %[rs], %[pb] offen offset:1024 sc1\n" \
    "buffer_load_dwordx4 v[8:11], %[o], %[rs], %[pb] offen offset:2048 sc1\n" \
    "buffer_load_dwordx4 v[12:15], %[o], %[rs], %[pb] offen offset:3072 sc1\n" \
    "s_add_i32 %[np], %[np], 1\n" \
    "s_waitcnt vmcnt(3)\n" \
    "v_mfma_f32_16x16x32_bf16 v[16:19], %[sel], v[0:3], 0\n" \
    "v_max3_u32 v20, v0, v1, v1\n" \
    "v_max3_u32 v20, v2, v3, v20\n" \
    "s_waitcnt vmcnt(2)\n" \
    "v_mfma_f32_16x16x32_bf16 v[16:19], %[sel], v[4:7], v[16:19]\n" \
    "v_max3_u32 v20, v4, v5, v20\n" \
    "v_max3_u32 v20, v6, v7, v20\n" \
    "s_waitcnt vmcnt(1)\n" \
    "v_mfma_f32_16x16x32_bf16 v[16:19], %[sel], v[8:11], v[16:19]\n" \
    "v_max3_u32 v20, v8, v9, v20\n" \
    "v_max3_u32 v20, v10, v11, v20\n" \
    "s_waitcnt vmcnt(0)\n" \
    "v_mfma_f32_16x16x32_bf16 v[16:19], %[sel], v[12:15], v[16:19]\n" \
    "v_max3_u32 v20, v12, v13, v20\n" \
    "v_max3_u32 v20, v14, v15, v20\n" \
    "v_cmp_eq_u32 vcc, -1, v20\n" \
    "s_nop 3\n" \
    "s_cbranch_vccnz 4f\n" \
    "3:\n" \
    "v_add_f32_dpp v16, v16, v16 row_ror:8 row_mask:0xf bank_mask:0xf\n" \
    "v_add_f32_dpp v17, v17, v17 row_ror:8 row_mask:0xf bank_mask:0xf\n" \
    "v_cvt_pk_bf16_f32 v16, v16, v17\n" \
    "buffer_store_dword v16, %[so], %[rs], %[mb0] offen sc1\n" \
    "s_branch 5f\n" \
    "4:\n" \
    "s_add_i32 %[bud], %[bud], -1\n" \
    "s_cmp_lt_i32 %[bud], 0\n" \
    "s_cbranch_scc0 2b\n" \
    "s_branch 3b\n" \
    "7:\n" \
    "s_add_i32 %[bud], %[bud], -1\n" \
    "s_cmp_lt_i32 %[bud], 0\n" \
    "s_cbranch_scc0 6b\n" \
    "s_branch 8b\n" \
    "5:\n" \
    ""
#define OWNER_CLOBBER "v0", "v1", "v2", "v3", "v4", "v5", "v6", "v7", "v8", "v9", "v10", "v11", "v12", "v13", "v14", "v15", "v16", "v17", "v18", "v19", "v20", "s44", "s45", "s46", "s47", "s48", "s49", "s50", "s51", "s52", "s53", "s54", "s55", "s56", "s57", "s58", "s59", "s96", "s97", "vcc", "scc", "memory"
constexpr int L_MEMS = 0;
constexpr int L_Q = 9216;
constexpr int L_KO = L_Q + 2560;
constexpr int L_KX = 16384;
constexpr int L_W = 32768;
constexpr int C_RING = 8;
constexpr int L_TOTAL = 163840;
static_assert(S_TOTAL <= L_TOTAL && L_W + C_RING * 16384 <= L_TOTAL && L_KO + 8 * 576 <= L_KX, "LDS budget");
static_assert(P_STG + P_RING * 16384 <= L_TOTAL, "LDS budget");

DI int pos2col(int p) { return p < 512 ? (p & ~63) + 16 * (p & 3) + ((p & 63) >> 2) : p; }
DI void unit_rc(int u, int& i, int& p0) {
    if (u < 512) { const int G = u >> 3, r = u & 7; i = 2 * (G & 3) + (r >> 2); p0 = 32 * (G >> 2) + 8 * (r & 3); }
    else { const int v = u - 512; i = (v >> 1) & 7; p0 = 512 + 16 * (v >> 4) + 8 * (v & 1); }
}
struct Mem0 { u16 v[3][8]; };
DI Mem0 mem0_load(const u16* mem0, int tid) {
    Mem0 m;
#pragma unroll
    for (int k = 0; k < 3; ++k) {
        int i, p0; unit_rc(min(tid + 256 * k, 575), i, p0);
#pragma unroll
        for (int j = 0; j < 8; ++j) m.v[k][j] = mem0[i * DM + pos2col(p0 + j)];
    }
    return m;
}
DI void mem0_store(const Mem0& m, LAS char* lds, int tid) {
#pragma unroll
    for (int k = 0; k < 3; ++k) {
        u32x4 v;
#pragma unroll
        for (int j = 0; j < 4; ++j) v[j] = (unsigned)m.v[k][2 * j] | ((unsigned)m.v[k][2 * j + 1] << 16);
        *(LAS u32x4*)(lds + L_MEMS + min(tid + 256 * k, 575) * 16) = v;
    }
}
DI unsigned read_canaries(int s, rsrc_t rs, int lane, int& budget) {
    const int o = 2 * OFF_CAN + (s & 3) * 16384 + (lane >> 2) * 128 + (lane & 3) * 16;
    u32x2 v;
    for (;;) {
        v = ld64(rs, o, 0);
        asm volatile("" ::: "memory");
        if (__all(v.y == (unsigned)(s + 1))) break;
        if (--budget < 0) break;
    }
    const unsigned t = 8 * now();
    return t + (unsigned)wave_max32(min(max((int)(v.x - t), -0x4000), 0x4000));
}
DI u32x8 sm_frag(LAS char* lds, int sl, int r0, int sa, int sH, int cb, int lo8) {
    auto rd = [&](int r) { return tr_read(lds, sl + r * 256 + ((cb ^ (2 * swz(r))) * 16) + lo8); };
    const u32x2 a = rd(r0), b = rd(r0 + sa), c = rd(r0 + sH), d = rd(r0 + sa + sH);
    return (u32x8){a.x, a.y, b.x, b.y, c.x, c.y, d.x, d.y};
}
DI int wait_sel(rsrc_t rw, u32x2 tag, int& budget) {
    unsigned v;
    wait_init(rw, tag, budget);
    for (;;) {
        v = __builtin_amdgcn_readfirstlane(ld32(rw, F_SEL, 0));
        asm volatile("" ::: "memory");
        if (v != SENT) break;
        if (--budget < 0) break;
        __builtin_amdgcn_s_sleep(2);
    }
    return (int)(v & 1);
}

#define GS(slot, i)
#define GV(slot, i, v)
DI void owner_loop(const Args& a, int G, rsrc_t rs, unsigned tbase, unsigned cbase, int& budget) {
    const int lane = threadIdx.x & 63, n = lane & 15, g = lane >> 4;
    const bool ex = G >= 64;
    const u32x4 sent4 = {SENT, SENT, SENT, SENT};
    const int js = (n & 2) ? 8 : ex ? (n & 4) + 2 * (n & 1) + (n >> 3) : 2 * (n >> 2) + (n & 1);
    u32x4 selu;
#pragma unroll
    for (int j = 0; j < 4; ++j) selu[j] = (js == 2 * j ? 0x3F80u : 0u) | (js == 2 * j + 1 ? 0x3F800000u : 0u);
    const int o = cmap(G * 4096 + lane * 16);
    const int so = (n < 8 || NREP > 1) ? cmap((n >> 3) * 16384 + G * 128 + (g & 1) * 4 + (ex ? (n & 4) * 16 + (g >> 1) * 32 + (n & 3) * 8 : (g >> 1) * 64 + (n & 7) * 8)) : OOB;
    unsigned fails = 0;
    int c8 = a.c1 * 8;
    const bool gv_on = true; (void)gv_on;
    unsigned oT = now(), oC = (unsigned)__builtin_readcyclecounter(), oR;
    { const unsigned r = (unsigned)(512.0f * (float)(oC - cbase) * __builtin_amdgcn_rcpf((float)(oT - tbase))); asm volatile("s_nop 0\n\tv_readfirstlane_b32 %0, %1" : "=s"(oR) : "v"(r)); }
    oT = 8 * oT + 16;
    const int ca = 2 * OFF_CAN + (lane >> 2) * 128 + (lane & 3) * 16;
    for (int s = 0; s < NSEG - 1; ++s) {
        const int pb = 2 * (OFF_PART + (s & 1) * PART_BYTES);
        int np; unsigned tc;
        asm volatile(OWNER_ASM : [np] "=&s"(np), [tm] "=&s"(tc), [bud] "+s"(budget), [oT] "+s"(oT), [oC] "+s"(oC), [oR] "+s"(oR)
            : [c8] "s"(c8), [ca] "v"(ca), [cs] "s"((s & 3) * 16384), [tag] "s"(s + 1), [o] "v"(o), [so] "v"(so), [sel] "v"(selu), [rs] "s"(rs), [pb] "s"(pb), [mb0] "s"(2 * (OFF_MEMB + ((s + 1) & 3) * MEMB_STRIDE)),
              [mb1] "s"(2 * (OFF_MEMB + ((s + 1) & 3) * MEMB_STRIDE + 32768)) : OWNER_CLOBBER);
        (void)wait_until(now() + a.rd);
#pragma unroll
        for (int r = 0; r < 4; ++r) st128(sent4, rs, o + r * 1024, pb);
        if (s > 1) { st32(SENT, rs, so, 2 * (OFF_MEMB + ((s - 1) & 3) * MEMB_STRIDE)); if (NREP > 2) st32(SENT, rs, so, 2 * (OFF_MEMB + ((s - 1) & 3) * MEMB_STRIDE + 32768)); }
        fails += np > 1;
        if (s > 1) adapt(c8, np > 1, a.c1 * 8 - 32, a.up, a.dn);
        { const unsigned t = now(); (void)wait_until(t + ((int)(tc - 8 * t) >> 3) + a.c2 + a.cw); }
    }
}
DI void role_owner(const Args& a, int ow, rsrc_t rw, u32x2 tag, int& budget) {
    const int tid = threadIdx.x, lane = tid & 63, w = __builtin_amdgcn_readfirstlane(tid >> 6);
    const unsigned tbase = now(), cbase = (unsigned)__builtin_readcyclecounter();
    if (ow < 32) {
        const u32x4 ones = {SENT, SENT, SENT, SENT};
#pragma unroll
        for (int i = 0; i < 15; ++i) st128(ones, rw, tid * 16 + i * 4096, ow * 61440);
        VM_DRAIN();
        WG_BARRIER();
        __builtin_amdgcn_raw_buffer_store_b64(tag, rw, tid == 0 ? F_INIT + ow * 128 : OOB, 0, 16);
    }
    if (w >= 2 || (w == 1 && ow < 56)) return;
    if (ow == 31) {
        unsigned best[2] = {~0u, ~0u};
        for (int r = 0; r < 10; ++r) {
            unsigned long long t0, t1;
            asm volatile("s_memtime %0\n\ts_waitcnt lgkmcnt(0)" : "=s"(t0) :: "memory");
            st32(SENT, rw, lane == 0 ? (r & 1) * 8192 + (r >> 1) * 4096 * 5 / 4096 * 16384 + ((r >> 1) & 1) * 4096 : OOB, 0);
            asm volatile("s_waitcnt vmcnt(0)\n\ts_memtime %0\n\ts_waitcnt lgkmcnt(0)" : "=s"(t1) :: "memory");
            best[r & 1] = min(best[r & 1], (unsigned)(t1 - t0));
        }
        st32(best[1] < best[0] ? 1u : 0u, rw, lane == 0 ? F_SEL : OOB, 0);
    }
    const int sel = wait_sel(rw, tag, budget);
    const rsrc_t rs = __builtin_amdgcn_make_buffer_rsrc(a.ws + sel * 8192, 0, COMM_BYTES - 8192, 0x00020000);
    owner_loop(a, ow + 8 * w, rs, tbase, cbase, budget);
}

template <int O, class V> DI void put8(V& d, u32x8 f) {
#pragma unroll
    for (int j = 0; j < 8; ++j) d[O + j] = f[j];
}
DI int opaque_lane() { int l; asm volatile("v_mbcnt_lo_u32_b32 %0, -1, 0\n\tv_mbcnt_hi_u32_b32 %0, -1, %0" : "=v"(l)); return l; }
DI f32x4 SMF64(u32x4 a, u32x8 b, f32x4 c, int i) { asm volatile("s_nop 1\n\tv_smfmac_f32_16x16x64_bf16 %0, %1, %2, %3\n\ts_nop 7" : "+v"(c) : "v"(a), "v"(b), "v"(i)); return c; }
DI f32x4 SMF32(u32x2 a, u32x4 b, f32x4 c, int i) { asm volatile("s_nop 1\n\tv_smfmac_f32_16x16x32_bf16 %0, %1, %2, %3\n\ts_nop 7" : "+v"(c) : "v"(a), "v"(b), "v"(i)); return c; }
DI f32x4 MFV(u32x4 a, u32x4 b, f32x4 c) { asm volatile("s_nop 1\n\tv_mfma_f32_16x16x32_bf16 %0, %1, %2, %0\n\ts_nop 7" : "+v"(c) : "v"(a), "v"(b)); return c; }

DI void role_head(const Args& a, LAS char* lds, unsigned lds_base, int h, int tw, rsrc_t rw, u32x2 tag, int& budget) {
    const int tid = threadIdx.x, w = __builtin_amdgcn_readfirstlane(tid >> 6);
    char* ws = a.ws;
    const unsigned tbase = now(), cbase = (unsigned)__builtin_readcyclecounter();
    const bool gv_on = tw == 0; (void)gv_on;
    u32x32 A0, A1, A2, A3, A4, A5, A6, A7, R0, R1, R2, R3; u32x8 R4;
    {
        const int lane = tid & 63, n = lane & 15, g = lane >> 4;
        const Mem0 m0v = mem0_load(a.mem0, tid);
        asm volatile("" ::: "memory");
        const int tq = n >> 2, tc = (n & 3) >> 1, lo8 = (n & 1) * 8;
        const int r_nat = 16 * (g >> 1) + 4 * (g & 1) + (tq & 1) + 8 * (tq >> 1);
        const int r_sig = 16 * (tq & 1) + 4 * (g >> 1) + (g & 1) + 2 * (tq >> 1);
        const int r_o = 16 * (tq & 1) + 8 * (g >> 1) + 4 * (g & 1) + (tq >> 1);
#define WFRAG(sl, kb, t) ((kb) < 8 ? sm_frag(lds, sl, r_sig, 32, 8, 4 * w + 2 * (t) + tc, lo8) : sm_frag(lds, sl, r_nat, 2, 32, 4 * w + 2 * (t) + tc, lo8))
        constexpr int VKB[10] = {2, 5, 6, 7, 8, 0, 0, 3, 4, 1};
        auto issue = [&](int i) {
            const unsigned slot = lds_base + L_W + (i % C_RING) * 16384;
            if (i < 9) ring_issue(a.Wq + (size_t)(64 * i) * HD + h * DH, HD, slot, w, lane);
            else if (i < 18) ring_issue(a.Wk + (size_t)(64 * (i - 9)) * HD + h * DH, HD, slot, w, lane);
            else if (i == 23) {
#pragma unroll
                for (int j = 0; j < 4; ++j) {
                    const int r = 16 * w + 4 * j + g, cs = n ^ (2 * swz(r));
                    glds16((const char*)(a.Wo + (size_t)(h * DH + r + 64 * (cs >> 3)) * DM + 512 + (cs & 7) * 8), slot + (16 * w + 4 * j) * 256);
                }
            }
            else if (i < 28) ring_issue(a.Wv + (size_t)(64 * VKB[i - 18]) * HD + h * DH, HD, slot, w, lane);
            else ring_issue(a.Wo + (size_t)(h * DH + 64 * ((i & 1) ^ ((i - 28) >> 1))) * DM + 256 * tw + 128 * ((i - 28) >> 1), DM, slot, w, lane);
        };
        auto keep = [&](int at, u32x8 f) {
            *(LAS u32x4*)(lds + at + lane * 16) = (u32x4){f[0], f[1], f[2], f[3]};
            *(LAS u32x4*)(lds + at + 1024 + lane * 16) = (u32x4){f[4], f[5], f[6], f[7]};
        };
        u32x8 hold[4] = {}, wex;
#pragma unroll
        for (int i = 0; i < C_RING - 1; ++i) issue(i);
        static_for<33>([&](auto ic) {
            constexpr int i = decltype(ic)::value, rem = i > 31 ? 0 : (31 - i < C_RING - 2 ? 31 - i : C_RING - 2);
            ring_wait<4 * rem>();
            if constexpr (i + C_RING - 1 < 32) issue(i + C_RING - 1);
            if constexpr (i > 24) {
                const int sp = L_W + ((i - 1) % C_RING) * 16384;
                if constexpr (i <= 28) { keep(sp + w * 4096, hold[0]); keep(sp + w * 4096 + 2048, i == 28 ? wex : hold[1]); }
                else if ((w >> 1) == ((i - 29) >> 1)) {
#pragma unroll
                    for (int T = 0; T < 4; ++T) keep(sp + (w & 1) * 8192 + T * 2048, hold[T]);
                }
            }
            const int sl = L_W + (i % C_RING) * 16384;
            if constexpr (i < 16) {
                constexpr int kb = i % 9;
                u32x32& A = i < 2 ? A0 : i < 4 ? A1 : i < 6 ? A2 : i < 8 ? A3 : i < 10 ? A4 : i < 12 ? A5 : i < 14 ? A6 : A7;
                put8<16 * (i & 1)>(A, WFRAG(sl, kb, 0)); put8<16 * (i & 1) + 8>(A, WFRAG(sl, kb, 1));
                if constexpr (i == 1) asm volatile("" : "+{a[0:31]}"(A0));
                if constexpr (i == 3) asm volatile("" : "+{a[32:63]}"(A1));
                if constexpr (i == 5) asm volatile("" : "+{a[64:95]}"(A2));
                if constexpr (i == 7) asm volatile("" : "+{a[96:127]}"(A3));
                if constexpr (i == 9) asm volatile("" : "+{a[128:159]}"(A4));
                if constexpr (i == 11) asm volatile("" : "+{a[160:191]}"(A5));
                if constexpr (i == 13) asm volatile("" : "+{a[192:223]}"(A6));
                if constexpr (i == 15) asm volatile("" : "+{a[224:255]}"(A7));
            }
            else if constexpr (i == 16) { put8<16>(R0, WFRAG(sl, 7, 0)); put8<24>(R0, WFRAG(sl, 7, 1)); }
            else if constexpr (i == 17) { put8<0>(R1, WFRAG(sl, 8, 0)); put8<8>(R1, WFRAG(sl, 8, 1)); }
            else if constexpr (i == 18) { put8<24>(R1, WFRAG(sl, 2, 0)); put8<0>(R2, WFRAG(sl, 2, 1)); }
            else if constexpr (i == 19) { put8<8>(R2, WFRAG(sl, 5, 0)); put8<16>(R2, WFRAG(sl, 5, 1)); }
            else if constexpr (i == 20) { put8<24>(R2, WFRAG(sl, 6, 0)); put8<0>(R3, WFRAG(sl, 6, 1)); }
            else if constexpr (i == 21) { put8<8>(R3, WFRAG(sl, 7, 0)); put8<16>(R3, WFRAG(sl, 7, 1)); }
            else if constexpr (i == 22) { put8<24>(R3, WFRAG(sl, 8, 0)); R4 = WFRAG(sl, 8, 1); }
            else if constexpr (i == 23) wex = sm_frag(lds, sl, r_o, 2, 32, 8 * (w >> 1) + 4 * tw + 2 * (w & 1) + tc, lo8);
            else if constexpr (i < 27) { hold[0] = WFRAG(sl, 0, 0); hold[1] = WFRAG(sl, 0, 1); }
            else if constexpr (i == 27) { hold[0] = WFRAG(sl, 1, 0); put8<16>(R1, WFRAG(sl, 1, 1)); }
            else if constexpr (i < 32) {
                if ((w >> 1) == ((i - 28) >> 1)) {
#pragma unroll
                    for (int T = 0; T < 4; ++T) hold[T] = sm_frag(lds, sl, r_o, 2, 32, 8 * (w & 1) + 2 * T + tc, lo8);
                }
            }
        });
        mem0_store(m0v, lds, tid);
        const int ai = 2 * (n >> 2) + (n & 1), ahf = (n >> 1) & 1;
        R0[0] = R0[1] = R0[2] = R0[3] = 0x3F803F80u;
        R0[4] = ahf ? 0xEEEE : 0x4444;
        R0[5] = lds_base + L_MEMS + (g >> 1) * 512 + ai * 64 + (g & 1) * 32 + ahf * 16;
        R0[6] = lds_base + L_MEMS + 8192 + g * 256 + ai * 32 + ahf * 16;
        R0[7] = lane * 16;
        R0[8] = lds_base + L_Q + 2 * g * 320 + (16 * w + n) * 4;
        R0[9] = lds_base + L_KO + 2 * g * 576 + (16 * w + (n & 8) + 4 * (n & 1) + ((n >> 1) & 3)) * 4;
        R0[10] = lds_base + L_Q + ai * 320 + g * 16;
        R0[11] = lds_base + L_KO + ai * 576 + g * 32 + ahf * 16;
        R0[12] = lds_base + L_KX + 8192 + w * 2048 + (g >> 1) * 256 + n * 16 + (g & 1) * 8;
        R0[13] = lds_base + L_W + w * 4096 + lane * 16;
        R0[14] = cmap(((2 * (4 * tw + w) + (n >> 3)) * 4 + g) * 4096 + h * 128 + (n & 7) * 16);
        R0[15] = (g >> 1) == (w >> 1) ? cmap((64 + 2 * (2 * tw + (w & 1)) + (g >> 1)) * 4096 + h * 128 + (g & 1) * 64 + n * 4) : OOB;
        asm volatile("" : "+{v[120:151]}"(R0), "+{v[152:183]}"(R1), "+{v[184:215]}"(R2), "+{v[216:247]}"(R3), "+{v[248:255]}"(R4));
    }
    wait_init(rw, tag, budget);
    int sel, kv_rdy;
    for (;;) {
        const int v = (int)ld32(rw, (tid & 1) ? F_SEL : F_KV + h * 128, 0);
        asm volatile("" ::: "memory");
        kv_rdy = __builtin_amdgcn_readlane(v, 0); sel = __builtin_amdgcn_readlane(v, 1);
        if ((kv_rdy >= 0 && sel >= 0) || --budget < 0) break;
    }
    sel &= 1;
    const rsrc_t rs = __builtin_amdgcn_make_buffer_rsrc(ws + sel * 8192, 0, COMM_BYTES - 8192, 0x00020000);
    auto dma = [&](int voff, int soff, unsigned dst) { bdma(rw, voff, soff, dst); };
    auto fetch_kv = [&](int s, int lane) {
        const int n = lane & 15, g = lane >> 4;
        const int kxs = (g >> 1) * 1024 + ((g & 1) * 32 + 16 * (n >> 3) + (n & 7)) * 16;
        const int vxs = (g >> 1) * 1024 + ((g & 1) * 32 + n) * 16;
        const int so_ = (s * 32 + h) * 8192 + w * 2048;
        const unsigned xdst = lds_base + L_KX + w * 2048;
        dma(kxs, OFF_KF + so_, xdst); dma(kxs, OFF_KF + so_ + 128, xdst + 1024);
        dma(vxs, OFF_VF + so_, xdst + 8192); dma(vxs, OFF_VF + so_ + 256, xdst + 8192 + 1024);
    };
    auto publish_m = [&](int s, int lane, u32x2 vm) {
        const int mo = (s * NH + h) * 2048 + w * 512;
        if (tw == 0) {
            const int kml = L_KO + (2 * w + ((lane >> 4) & 1)) * 576 + ((lane & 15) >> 1) * 32 + (lane & 1) * 8;
            const u32x2 x = *(LAS u32x2*)(lds + kml), y = *(LAS u32x2*)(lds + kml + 16);
            st128((u32x4){x.x, y.x, x.y, y.y}, rw, lane < 32 ? lane * 16 : OOB, OFF_KM + mo);
        } else { st32(vm.x, rw, lane * 4, OFF_VM + mo); st32(vm.y, rw, lane * 4 + 256, OFF_VM + mo); }
    };
    const int fo = F_KVM + h * 128 + (4 * tw + w) * 4;
    u32x8 scon;
    scon[0] = w; scon[1] = 2 * OFF_CAN + (h >> 1) * 128 + (2 * (h & 1) + tw) * 16; scon[2] = F_KV + h * 128; scon[3] = 65536 + (w >> 1) * 32768 + (w & 1) * 8192 - w * 4096;
    scon[4] = OFF_KF + h * 8192 + w * 2048; scon[5] = fo; scon[6] = tw - 1; scon[7] = (tw ? OFF_VM : OFF_KM) + (h - NH) * 2048 + w * 512;
    u32x8 S1 = {0u, 0u, (unsigned)kv_rdy, (unsigned)budget, (unsigned)a.d0, 0xFFFFFFFFu / 22, (unsigned)(a.c2 * 8), 0u};
    u32x8 S2 = {0u, 0u, 0u, (unsigned)(a.up + a.dn), (unsigned)a.dn, (unsigned)(a.c2 * 8 - 40), 0u, 1u};
    fetch_kv(0, opaque_lane());
    VM_DRAIN();
    u32x2 vm = {};
    u32x8 S3;
    {
        const unsigned t = now(), c = (unsigned)__builtin_readcyclecounter();
        const float r = (float)(c - cbase) * __builtin_amdgcn_rcpf(8.0f * (float)(t - tbase));
        S2[1] = 8 * t + 16; S1[5] = (unsigned)(4294967296.0f * __builtin_amdgcn_rcpf(r));
        S3 = (u32x8){c, (unsigned)(4096.0f * r), 2u * OFF_MEMB + (blockIdx.x & (NREP - 1)) * 32768, w == 3 ? ~0u : 0u};
    }
    for (;;) {
        asm volatile(STEP_ASM
            : "+{v[96:97]}"(vm), "+{v[0:7]}"(S1), "+{v[8:15]}"(S2), "+{v[16:23]}"(S3), "+{v[24:31]}"(scon)
            : "{a[0:31]}"(A0), "{a[32:63]}"(A1), "{a[64:95]}"(A2), "{a[96:127]}"(A3), "{a[128:159]}"(A4), "{a[160:191]}"(A5), "{a[192:223]}"(A6), "{a[224:255]}"(A7),
              "{v[120:151]}"(R0), "{v[152:183]}"(R1), "{v[184:215]}"(R2), "{v[216:247]}"(R3), "{v[248:255]}"(R4),
              [rs] "s"(rs), [rw] "s"(rw)
            : STEP_CLOBBER);
        const int s = __builtin_amdgcn_readfirstlane(S1[0]);
        const unsigned st = __builtin_amdgcn_readfirstlane(S1[7]);
        kv_rdy = __builtin_amdgcn_readfirstlane(S1[2]); budget = __builtin_amdgcn_readfirstlane(S1[3]);
        if (s == NSEG - 1) break;
        if (__builtin_expect(st & 2, 0)) {
            const int lane = opaque_lane();
            LAS char* z = (LAS char*)0;
            const f32x4 zero = {0, 0, 0, 0};
            const u32x4 ones = __builtin_shufflevector(R0, R0, 0, 1, 2, 3);
            const unsigned zr = lane >> 6;
            const int idx = R0[4];
            u32x4 qf[4];
            f32x4 s1 = zero, s2 = zero;
#pragma unroll
            for (int ks = 0; ks < 4; ++ks) {
                qf[ks] = *(LAS u32x4*)(z + R0[10] + ks * 64);
                s1 = MFV(*(LAS u32x4*)(lds + L_KX + lane * 16 + ks * 2048), qf[ks], s1);
                s2 = MFV(*(LAS u32x4*)(lds + L_KX + lane * 16 + ks * 2048 + 1024), qf[ks], s2);
            }
            f32x4 s0 = SMF64(*(LAS u32x4*)(z + R0[11]), __builtin_shufflevector(qf[0], qf[1], 0, 1, 2, 3, 4, 5, 6, 7), zero, idx);
            s0 = SMF64(*(LAS u32x4*)(z + R0[11] + 128), __builtin_shufflevector(qf[2], qf[3], 0, 1, 2, 3, 4, 5, 6, 7), s0, idx);
            const float sa = s0[0] + s0[2], sb = s0[1] + s0[3];
            float mx = fmaxf(fmaxf(fmaxf(fmaxf(s1[0], s1[1]), fmaxf(s1[2], s1[3])), fmaxf(fmaxf(s2[0], s2[1]), fmaxf(s2[2], s2[3]))), fmaxf(sa, sb));
            mx = fmaxf(mx, __shfl_xor(mx, 16)); mx = fmaxf(mx, __shfl_xor(mx, 32));
            auto ex2 = [&](float x, float y) { return pack2(__builtin_amdgcn_exp2f(x - mx), __builtin_amdgcn_exp2f(y - mx)); };
            const u32x4 px = {ex2(s1[0], s1[1]), ex2(s1[2], s1[3]), ex2(s2[0], s2[1]), ex2(s2[2], s2[3])};
            const unsigned pm = ex2(sa, sb);
            const f32x4 l = MFV(((u32x4){pm, zr, zr, zr}), ones, MFV(px, ones, zero));
            const u32x2 x0 = *(LAS u32x2*)(z + R0[12]), y0 = *(LAS u32x2*)(z + R0[12] + 1024), x1 = *(LAS u32x2*)(z + R0[12] + 512), y1 = *(LAS u32x2*)(z + R0[12] + 1536);
            const f32x4 o = SMF64(px, ((u32x8){x0.x, y0.x, x0.y, y0.y, x1.x, y1.x, x1.y, y1.y}), zero, idx) + SMF32(((u32x2){pm, zr}), ((u32x4){vm.x, vm.y, zr, zr}), zero, idx);
            const float i0 = __builtin_amdgcn_rcpf(l[0]), i1 = __builtin_amdgcn_rcpf(l[1]);
            *(LAS unsigned*)(z + R0[9] + 256) = pack2(o[0] * i0, o[2] * i0);
            *(LAS unsigned*)(z + R0[9] + 256 + 576) = pack2(o[1] * i1, o[3] * i1);
            WG_BARRIER();
            const u32x4 oa = *(LAS u32x4*)(z + R0[11] + 256 + (w >> 1) * 128), ob = *(LAS u32x4*)(z + R0[11] + 384 - (w >> 1) * 128);
            auto frag = [&](int at) { return __builtin_shufflevector(*(LAS u32x4*)(z + at), *(LAS u32x4*)(z + at + 1024), 0, 1, 2, 3, 4, 5, 6, 7); };
            f32x2 f[4];
#pragma unroll
            for (int T = 0; T < 4; ++T) f[T] = fold(SMF64(ob, frag(R0[13] + scon[3] + 16384 + T * 2048), SMF64(oa, frag(R0[13] + scon[3] + T * 2048), zero, idx), idx));
            const f32x2 fx = fold(SMF64(ob, frag((R0[13] ^ 8192) + 49152 + 2048), SMF64(oa, frag(R0[13] + 49152 + 2048), zero, idx), idx));
            const int pb = 2 * (OFF_PART + (s & 1) * PART_BYTES);
            st128((u32x4){pack2(f[0].x, f[1].x), pack2(f[2].x, f[3].x), pack2(f[0].y, f[1].y), pack2(f[2].y, f[3].y)}, rs, R0[14], pb);
            st32(pack2(fx.x, fx.y), rs, R0[15], pb);
        }
        if (__builtin_expect((st & 2) || kv_rdy <= s, 0)) {
            while (kv_rdy <= s) { kv_rdy = prog_min(rw, F_KV + h * 128); if (--budget < 0) break; }
            fetch_kv(s + 1, opaque_lane());
        }
        if (st & 2) { S1[1] = read_canaries(s, rs, opaque_lane(), budget) + S1[6]; S2[2] += 0x10000; }
        if ((int)(S1[1] - S2[1]) > 0x8000) { S2[1] = 8 * now() + 16; S3[0] = (unsigned)__builtin_readcyclecounter(); }
        S1[0] = s + 1; S1[2] = kv_rdy; S1[3] = budget;
    }
    {
        const int lane = opaque_lane();
        publish_m(NSEG - 1, lane, vm);
        st32((unsigned)(NSEG - 2), rw, lane == 0 ? fo : OOB, 0);
        VM_DRAIN();
        st32((unsigned)(NSEG - 1), rw, lane == 0 ? fo : OOB, 0);
    }
}

__global__ void __launch_bounds__(256, 1) fused_kernel(Args a) {
    extern __shared__ __attribute__((aligned(16))) char smem[];
    LAS char* lds = (LAS char*)smem;
    const int b = blockIdx.x, bx = b & 7, bi = b >> 3;
    const rsrc_t rw = __builtin_amdgcn_make_buffer_rsrc(a.ws, 0, WS_USED, 0x00020000);
    int budget = SPIN_MAX;
    const unsigned lds_base = (unsigned)(uintptr_t)lds;
    u32x2 tag;
    {
        const u32x2 e = ld64(rw, F_EPOCH, 0);
        tag = launch_tag((u32x2){(unsigned)__builtin_amdgcn_readfirstlane(e.x), (unsigned)__builtin_amdgcn_readfirstlane(e.y)});
        WG_BARRIER();
    }
#define RT(i)
    if (bx < 4) {
        if (a.mask & 2) {
            if (bi < 16) role_head(a, lds, lds_base, 8 * bx + (bi >> 1), bi & 1, rw, tag, budget);
            else role_owner(a, 4 * (bi - 16) + bx, rw, tag, budget);
        }
    } else {
        const int fx = bx - 4, no = fx < 3 ? 14 : 22, j = 14 * fx + bi;
        if (bi >= no || (j & 2)) (void)wait_until(now() + a.d1);
        if (bi >= no) {
            const int t = 18 * fx + bi - no;
            role_s4d(a, lds, t, rw, tag, budget);
            RT(1)
            if (t < 9 * OUT_REP) role_outp(a, lds, t % 9, t / 9, rw, budget);
        }
        else if (j & 2) role_qa(a, lds, lds_base, (j >> 2) * 2, j & 1, rw, tag, budget);
        else role_kv(a, lds, lds_base, (j >> 2) * 2 + (j & 1), rw, tag, budget);
    }
    RT(2)
    if (budget < 0 && threadIdx.x == 0) st32(0x1000u + b, rw, F_ERR, 0);
    if (threadIdx.x == 0) {
        const unsigned old = __hip_atomic_fetch_add((__attribute__((address_space(1))) unsigned*)(a.ws + F_DONE), 1u, __ATOMIC_RELAXED, __HIP_MEMORY_SCOPE_AGENT);
        if (old == 254u) __builtin_amdgcn_raw_buffer_store_b64(tag, rw, F_EPOCH, 0, 16);
    }
}

#define C2V 53
static int g_c1 = 12, g_c2 = C2V, g_d0 = 2040, g_staged = 0, g_d1 = 300, g_rd = 50, g_cw = 65, g_up = 3, g_dn = 1;
extern "C" void kernel_launch(void* const* d_in, const int* in_sizes, int n_in, void* d_out, int out_size, void* d_ws, size_t ws_size, hipStream_t stream) {
    Args a;
    a.x = (const u16*)d_in[0]; a.mem0 = (const u16*)d_in[1];
    a.Wq = (const u16*)d_in[2]; a.Wk = (const u16*)d_in[3]; a.Wv = (const u16*)d_in[4]; a.Wo = (const u16*)d_in[5];
    a.lAr = (const u16*)d_in[8]; a.Aim = (const u16*)d_in[9]; a.Cr = (const u16*)d_in[10]; a.Ci = (const u16*)d_in[11];
    a.ldt = (const u16*)d_in[12]; a.Dsk = (const u16*)d_in[13]; a.gw = (const u16*)d_in[14];
    a.out = (u16*)d_out;
    a.ws = (char*)(((uintptr_t)d_ws + ((2u << 20) - 1)) & ~(uintptr_t)((2u << 20) - 1));
    a.c1 = g_c1; a.c2 = g_c2; a.d0 = g_d0; a.mask = 15; a.d1 = g_d1; a.up = g_up; a.dn = g_dn; a.rd = g_rd; a.cw = g_cw; a.pad = 0;
    (void)hipFuncSetAttribute((const void*)fused_kernel, hipFuncAttributeMaxDynamicSharedMemorySize, L_TOTAL);
    fused_kernel<<<256, 256, L_TOTAL, stream>>>(a);
}
```

```cpp
#include <hip/hip_runtime.h>
#include <hip/hip_bf16.h>
#include <stdint.h>

typedef unsigned short u16;
typedef __bf16 bf16x8 __attribute__((ext_vector_type(8)));
typedef __bf16 bf16x16 __attribute__((ext_vector_type(16)));
typedef short s16x4 __attribute__((ext_vector_type(4)));
typedef float f32x2 __attribute__((ext_vector_type(2)));
typedef float f32x4 __attribute__((ext_vector_type(4)));
typedef float f32x16 __attribute__((ext_vector_type(16)));
typedef unsigned u32x2 __attribute__((ext_vector_type(2)));
typedef unsigned u32x4 __attribute__((ext_vector_type(4)));
typedef unsigned u32x8 __attribute__((ext_vector_type(8)));
typedef unsigned u32x16 __attribute__((ext_vector_type(16)));
typedef unsigned u32x32 __attribute__((ext_vector_type(32)));
typedef short s16x8 __attribute__((ext_vector_type(8)));
#define LAS __attribute__((address_space(3)))
#define DI __device__ __forceinline__
typedef __amdgpu_buffer_rsrc_t rsrc_t;

constexpr int SEQ = 2048, DM = 576, NH = 32, DH = 128, HD = NH * DH, NM = 8, SEG = 32, NSEG = 64, NS = 128;
constexpr unsigned SENT = 0xFFFFFFFFu;
constexpr unsigned OOB = 0x7FFFFFF0u;
constexpr int SPIN_MAX = 1 << 20;

constexpr int OFF_PART = 0;
constexpr int PART_BYTES = 72 * 4096;
constexpr int OFF_MEMB = OFF_PART + 2 * PART_BYTES;
constexpr int MEMB_STRIDE = 4 * 16384;
constexpr int OFF_CAN = OFF_MEMB + 4 * MEMB_STRIDE;
constexpr int COMM_LOGICAL = OFF_CAN + 32768;
constexpr int COMM_BYTES = 2 * COMM_LOGICAL;
constexpr int F_ERR = COMM_BYTES;
constexpr int F_KV = F_ERR + 128;
constexpr int F_KVM = F_KV + 16384;
constexpr int F_O = F_KVM + 4096;
constexpr int F_S4 = F_O + 4096;
constexpr int F_DONE = F_S4 + 512;
constexpr int INIT_SHARE = 14 * 4096, INIT_BYTES = 32 * INIT_SHARE;
constexpr int F_INIT = INIT_BYTES;
constexpr int F_EPOCH = F_INIT + 16384;
static_assert(F_DONE + 128 <= INIT_BYTES, "");
static_assert(F_EPOCH + 128 <= (2 << 20), "hand-off region");
constexpr int OFF_KF = 2 << 20;
constexpr int OFF_VF = OFF_KF + SEQ * HD * 2;
constexpr int OFF_OF = OFF_VF + SEQ * HD * 2;
constexpr int OFF_KM = OFF_OF + SEQ * HD * 2;
constexpr int OFF_VM = OFF_KM + NSEG * NH * NM * DH * 2;
constexpr int OFF_BO = OFF_VM + NSEG * NH * NM * DH * 2;
constexpr int OFF_GT = OFF_BO + SEQ * DM * 2;
constexpr int OFF_DBG = OFF_GT + SEQ * 4;
constexpr int WS_USED = OFF_DBG + (4 << 20);

struct Args {
    const u16 *x, *mem0, *Wq, *Wk, *Wv, *Wo, *lAr, *Aim, *Cr, *Ci, *ldt, *Dsk, *gw;
    u16* out; char* ws;
    int c1, c2, d0, b0, d1, up, dn, rd, cw, e0;
};

DI float bf2f(u16 b) { return __uint_as_float((unsigned)b << 16); }
DI float bflo(unsigned w) { return __uint_as_float(w << 16); }
DI float bfhi(unsigned w) { return __uint_as_float(w & 0xFFFF0000u); }
DI u16 f2bf(float f) { return __builtin_bit_cast(u16, (__bf16)f); }
DI unsigned pack2(float lo, float hi) {
    typedef __bf16 bf16x2 __attribute__((ext_vector_type(2)));
    bf16x2 v = {(__bf16)lo, (__bf16)hi};
    return __builtin_bit_cast(unsigned, v);
}
#define WG_BARRIER() asm volatile("s_waitcnt lgkmcnt(0)\n\ts_barrier" ::: "memory")
#define VM_DRAIN() asm volatile("s_waitcnt vmcnt(0)" ::: "memory")
DI int cmap(int o) { return (o & 0x1FFF) | ((o >> 13) << 14); }
DI unsigned mx4(unsigned a, u32x4 v) { a = max(max(a, v.x), v.y); return max(max(a, v.z), v.w); }
DI unsigned now() { return (unsigned)__builtin_amdgcn_s_memrealtime(); }
DI unsigned wait_until(unsigned t) {
    unsigned v; int guard = 8192;
    do { v = now(); } while ((int)(t - v) >= 4 && --guard > 0);
    const int r = __builtin_amdgcn_readfirstlane((int)(t - v));
    asm volatile("s_cmp_lt_i32 %0, 1\n\ts_cbranch_scc1 1f\n\ts_nop 3\n\ts_cmp_lt_i32 %0, 2\n\ts_cbranch_scc1 1f\n\ts_nop 3\n\ts_cmp_lt_i32 %0, 3\n\ts_cbranch_scc1 1f\n\ts_nop 7\n1:" :: "s"(r) : "scc");
    return r > 0 ? t : v + 2;
}
DI void st128(u32x4 v, rsrc_t rs, int off, int soff) {
    __builtin_amdgcn_raw_buffer_store_b128(v, rs, off, soff, 16);
    asm volatile("s_nop 1" :: "v"(v) : "memory");
}
DI void st32(unsigned v, rsrc_t rs, int off, int soff) { __builtin_amdgcn_raw_buffer_store_b32(v, rs, off, soff, 16); }
DI u32x4 ld128(rsrc_t rs, int off, int soff) { return __builtin_amdgcn_raw_buffer_load_b128(rs, off, soff, 16); }
DI u32x2 ld64(rsrc_t rs, int off, int soff) { return __builtin_amdgcn_raw_buffer_load_b64(rs, off, soff, 16); }
DI unsigned ld32(rsrc_t rs, int off, int soff) { return __builtin_amdgcn_raw_buffer_load_b32(rs, off, soff, 16); }
DI f32x2 fold(f32x4 a) { return (f32x2){a[0], a[1]} + (f32x2){a[2], a[3]}; }
DI float dpp_xor1(float v) { return __int_as_float(__builtin_amdgcn_update_dpp(0, __float_as_int(v), 0xB1, 0xf, 0xf, true)); }
DI float red_sum_g(float v) {
    auto a = __builtin_amdgcn_permlane16_swap(__float_as_uint(v), __float_as_uint(v), false, false);
    v = __uint_as_float(a[0]) + __uint_as_float(a[1]);
    auto b = __builtin_amdgcn_permlane32_swap(__float_as_uint(v), __float_as_uint(v), false, false);
    return __uint_as_float(b[0]) + __uint_as_float(b[1]);
}
DI int wave_max32(int d) {
    d = max(d, __builtin_amdgcn_update_dpp(d, d, 0x121, 0xf, 0xf, false));
    d = max(d, __builtin_amdgcn_update_dpp(d, d, 0x122, 0xf, 0xf, false));
    d = max(d, __builtin_amdgcn_update_dpp(d, d, 0x124, 0xf, 0xf, false));
    d = max(d, __builtin_amdgcn_update_dpp(d, d, 0x128, 0xf, 0xf, false));
    return __builtin_amdgcn_readfirstlane(max(max(__builtin_amdgcn_readlane(d, 0), __builtin_amdgcn_readlane(d, 16)), max(__builtin_amdgcn_readlane(d, 32), __builtin_amdgcn_readlane(d, 48))));
}
DI u32x2 tr_read(LAS char* lds, int addr) { return __builtin_bit_cast(u32x2, __builtin_amdgcn_ds_read_tr16_b64_v4i16((LAS s16x4*)(lds + addr))); }
DI void wait_prog(rsrc_t rw, int off, bool act, int thr, int& budget) {
    for (;;) {
        const int v = (int)ld32(rw, off, 0);
        asm volatile("" ::: "memory");
        if (__all(!act || v >= thr)) break;
        if (--budget < 0) break;
        __builtin_amdgcn_s_sleep(1);
    }
}
DI u32x2 launch_tag(const Args& a) {
    unsigned long long z;
    asm volatile("s_load_dwordx2 %0, %1, 0x0\n\ts_waitcnt lgkmcnt(0)" : "=s"(z) : "s"(a.ws + F_EPOCH) : "memory");
    z += 0x9E3779B97F4A7C15ull;
    z = (z ^ (z >> 30)) * 0xBF58476D1CE4E5B9ull; z = (z ^ (z >> 27)) * 0x94D049BB133111EBull; z ^= z >> 31;
    return (u32x2){(unsigned)z, (unsigned)(z >> 32)};
}
DI int wait_init(rsrc_t rw, u32x2 tag, int& budget, int more = 0) {
    const int l = threadIdx.x & 63, o = F_INIT + (l < 32 ? l : l == 63 ? min(more, 32) : more) * 128;
    u32x2 v;
    for (;;) {
        v = ld64(rw, o, 0);
        asm volatile("" ::: "memory");
        if (__all(((v.x ^ tag.x) >> 1) == 0 && v.y == tag.y)) break;
        if (--budget < 0) break;
        __builtin_amdgcn_s_sleep(2);
    }
    return __builtin_amdgcn_readlane(v.x, 63) & 1;
}
DI void glds16(const void* base, int voff, unsigned lds_dst) {
    unsigned keep;
    asm volatile("s_mov_b32 %0, m0\n\ts_mov_b32 m0, %3\n\ts_nop 0\n\tglobal_load_lds_dwordx4 %1, %2\n\ts_mov_b32 m0, %0" : "=&s"(keep) : "v"(voff), "s"(base), "s"(lds_dst) : "memory");
}
DI void bdma(rsrc_t rs, int voff, int soff, unsigned lds_dst) {
    unsigned keep;
    asm volatile("s_mov_b32 %0, m0\n\ts_mov_b32 m0, %4\n\ts_nop 0\n\tbuffer_load_dwordx4 %1, %2, %3 offen sc1 lds\n\ts_mov_b32 m0, %0" : "=&s"(keep) : "v"(voff), "s"(rs), "s"(soff), "s"(lds_dst) : "memory");
}
DI int swz(int r) { return ((r & 3) ^ (((r >> 2) & 3) << 1) ^ (((r >> 4) & 1) << 2)) & 7; }
template <class F> DI u32x4 ring_offs(int w, int lane, F at) {
    u32x4 o;
#pragma unroll
    for (int j = 0; j < 4; ++j) { const int r = 16 * w + 4 * j + (lane >> 4); o[j] = at(r, (lane & 15) ^ (2 * swz(r))) - 1024 * j; }
    return o;
}
DI u32x4 ring_offs(int w, int lane, int ld) { return ring_offs(w, lane, [&](int r, int c) { return r * ld * 2 + c * 16; }); }
DI void ring_issue(const u16* src, u32x4 o, unsigned slot) {
    unsigned keep;
    asm volatile("s_mov_b32 %0, m0\n\ts_mov_b32 m0, %6\n\ts_nop 0\n\tglobal_load_lds_dwordx4 %1, %5\n\tglobal_load_lds_dwordx4 %2, %5 offset:1024\n\t"
                 "global_load_lds_dwordx4 %3, %5 offset:2048\n\tglobal_load_lds_dwordx4 %4, %5 offset:3072\n\ts_mov_b32 m0, %0"
                 : "=&s"(keep) : "v"(o.x), "v"(o.y), "v"(o.z), "v"(o.w), "s"(src), "s"(slot) : "memory");
}
#define PF_FIRST 2
constexpr int L_TOTAL = 163840, L_SINK = L_TOTAL - 256;
DI void touch(const void* base, int voff, unsigned lds_base) {
    unsigned keep;
    asm volatile("s_mov_b32 %0, m0\n\ts_mov_b32 m0, %3\n\ts_nop 0\n\tglobal_load_lds_dword %1, %2\n\ts_mov_b32 m0, %0" : "=&s"(keep) : "v"(voff), "s"(base), "s"(lds_base + L_SINK) : "memory");
}
DI void prefetch(const Args& a, unsigned lds_base, int x, int id, int n) {
    const int lane = threadIdx.x & 63;
    const int h0 = x < 4 ? 8 * x : (0x160E0800 >> (8 * (x - 4))) & 255, lpr = x == 5 ? 12 : x == 7 ? 20 : 16, per = 9 * lpr;
    const int first = PF_FIRST * lpr, end = (x < 4 ? 4 : 2) * per;
    const float rl = __builtin_amdgcn_rcpf((float)lpr);
    for (int i = first + id; i < end; i += n) {
        const int m = (i >= per) + (i >= 2 * per) + (i >= 3 * per), mm = m + (x >= 4);
        int j = i - m * per;
        if (x < 4 && m == 2) j = (int)((0x143087652ull >> (4 * (j >> 4))) & 15) * 16 + (j & 15);
        const int L = j * 64 + lane, row = (int)((L + 0.5f) * rl);
        touch(mm == 0 ? a.Wq : mm == 1 ? a.Wk : mm == 2 ? a.Wv : a.Wo, m == 3 ? h0 * (DH * DM * 2) + L * 128 : row * (HD * 2) + (L - row * lpr) * 128 + h0 * 256, lds_base);
    }
}
template <int N> DI void ring_wait() { asm volatile("s_waitcnt vmcnt(%0) lgkmcnt(0)\n\ts_barrier" :: "i"(N) : "memory"); }
template <int I> struct ic_t { static constexpr int value = I; };
template <int N, int I = 0, class F> DI void static_for(F&& f) { if constexpr (I < N) { f(ic_t<I>{}); static_for<N, I + 1>(f); } }
#define MFMA32(a, b, c) __builtin_amdgcn_mfma_f32_32x32x16_bf16(__builtin_bit_cast(bf16x8, a), __builtin_bit_cast(bf16x8, b), c, 0, 0, 0)
#define MFMA16(a, b, c) __builtin_amdgcn_mfma_f32_16x16x32_bf16(__builtin_bit_cast(bf16x8, a), __builtin_bit_cast(bf16x8, b), c, 0, 0, 0)

constexpr int S_CH = 9;
constexpr int S_XRS = 4096 + 32;
constexpr int S_XR = 0;
constexpr int S_RC = S_CH * S_XRS;
constexpr int S_RCS = 4192;
constexpr int S_KD = S_RC + 4 * S_RCS;
constexpr int S_K = S_KD + 48 * 128;
constexpr int S_TAB = S_K + 79 * 160 + 32;
constexpr int S_TROW = 1056;
constexpr int S_TOTAL = S_TAB + 57 * S_TROW;
static_assert(S_RC % 16 == 0 && S_TAB % 16 == 0 && 4 * 64 * 144 <= 57 * S_TROW, "");
DI f32x2 crot(f32x2 z) { return (f32x2){-z.y, z.x}; }
DI f32x2 cmul(f32x2 a, f32x2 z) { return __builtin_elementwise_fma(a.yy, crot(z), a.xx * z); }
DI f32x2 cexp_p(float ar, float ai, float p) {
    const float x = ai * p;
    const float hi = x * 0.15915494309189535f;
    const float lo = __builtin_fmaf(x, 0.15915494309189535f, -hi) + x * 6.420638316725915e-9f;
    const float rev = (hi - __builtin_rintf(hi)) + lo;
    const float m = __builtin_amdgcn_exp2f(ar * p * 1.4426950408889634f);
    return (f32x2){m * __builtin_amdgcn_cosf(rev), m * __builtin_amdgcn_sinf(rev)};
}
#define FENCE() __builtin_amdgcn_sched_barrier(0)
DI void role_s4d(const Args& a, LAS char* lds, int task, rsrc_t rw, int& budget) {
    const int tid = threadIdx.x, lane = tid & 63, w = __builtin_amdgcn_readfirstlane(tid >> 6);
    const int d0 = task * S_CH;
#define S4S(i)
    {
        const rsrc_t rx = __builtin_amdgcn_make_buffer_rsrc((void*)a.x, 0, SEQ * DM * 2, 0x00020000);
        const unsigned sh = (d0 & 1) * 16;
        u32x4 v[8]; unsigned e[8];
#pragma unroll
        for (int i = 0; i < 8; ++i) {
            const int o = (tid + 256 * i) * (DM * 2) + (d0 & ~1) * 2;
            v[i] = __builtin_amdgcn_raw_buffer_load_b128(rx, o, 0, 0); e[i] = __builtin_amdgcn_raw_buffer_load_b32(rx, o, 16, 0);
        }
#pragma unroll
        for (int p = 0; p < 2; ++p) {
            const int tok = 32 * task + 8 * w + 4 * p + (lane >> 4);
            float acc = 0.f;
#pragma unroll
            for (int i = 0; i < 5; ++i) {
                const int ch = (lane & 15) + 16 * i;
                const u32x4 gv = ch < 72 ? *(const u32x4*)(a.gw + ch * 8) : (u32x4){0u, 0u, 0u, 0u};
                const u32x4 xv = *(const u32x4*)(a.x + (size_t)tok * DM + min(ch, 71) * 8);
#pragma unroll
                for (int j = 0; j < 4; ++j) acc += bflo(xv[j]) * bflo(gv[j]) + bfhi(xv[j]) * bfhi(gv[j]);
            }
            acc += __shfl_xor(acc, 1); acc += __shfl_xor(acc, 2); acc += __shfl_xor(acc, 4); acc += __shfl_xor(acc, 8);
            st32(__float_as_uint(1.0f / (1.0f + __expf(-acc))), rw, (lane & 15) == 0 ? OFF_GT + tok * 4 : OOB, 0);
        }
#pragma unroll
        for (int i = 0; i < 8; ++i) {
            const unsigned x5[5] = {__builtin_amdgcn_alignbit(v[i].y, v[i].x, sh), __builtin_amdgcn_alignbit(v[i].z, v[i].y, sh), __builtin_amdgcn_alignbit(v[i].w, v[i].z, sh),
                                    __builtin_amdgcn_alignbit(e[i], v[i].w, sh), e[i] >> sh};
#pragma unroll
            for (int c = 0; c < S_CH; ++c) *(LAS u16*)(lds + S_XR + c * S_XRS + (2047 - tid - 256 * i) * 2) = (u16)(x5[c >> 1] >> (16 * (c & 1)));
        }
    }
    if (tid < 8 * S_CH) *(LAS unsigned*)(lds + S_XR + (tid >> 3) * S_XRS + 4096 + (tid & 7) * 4) = 0;
    if (tid < 64) *(LAS unsigned*)(lds + S_RC + (tid >> 4) * S_RCS + 4096 + (tid & 15) * 4) = 0;
    for (int i = tid; i < 15 * 160 / 4; i += 256) *(LAS unsigned*)(lds + S_K + i * 4) = 0;
    int kd[8];
#pragma unroll
    for (int j = 0; j < 8; ++j) {
        const int p = (int)bf2f(f2bf((float)(8 * tid + j))), k = p >> 7, r = p & 7;
        const int row = p < 256 ? 8 * k + r : (p < 512 ? 8 + 4 * k + (r >> 1) : (p < 1024 ? 16 + 2 * k + (r >> 2) : 24 + k));
        kd[j] = S_KD + (row * 16 + ((p >> 3) & 15)) * 8;
    }
    const int n = tid & 127, nn = lane & 15, g = lane >> 4;
    u16 pA = a.lAr[d0 * NS + n], pI = a.Aim[d0 * NS + n], pCr = a.Cr[d0 * NS + n], pCi = a.Ci[d0 * NS + n], pT = a.ldt[d0], pD = a.Dsk[d0];
    for (int ch = 0; ch < S_CH; ++ch) {
        WG_BARRIER();
        S4S(0)
        const float dsk = bf2f(pD);
        {
            const u32x4 v = *(LAS u32x4*)(lds + S_XR + ch * S_XRS + tid * 16);
            const u32x2 e = *(LAS u32x2*)(lds + S_XR + ch * S_XRS + tid * 16 + 16);
            const unsigned s01 = __builtin_amdgcn_alignbit(v.y, v.x, 16), s12 = __builtin_amdgcn_alignbit(v.z, v.y, 16), s23 = __builtin_amdgcn_alignbit(v.w, v.z, 16);
            const unsigned s34 = __builtin_amdgcn_alignbit(e.x, v.w, 16), s45 = __builtin_amdgcn_alignbit(e.y, e.x, 16);
            *(LAS u32x4*)(lds + S_RC + tid * 16) = v;
            *(LAS u32x4*)(lds + S_RC + S_RCS + tid * 16) = (u32x4){s01, s12, s23, s34};
            *(LAS u32x4*)(lds + S_RC + 2 * S_RCS + tid * 16) = (u32x4){v.y, v.z, v.w, e.x};
            *(LAS u32x4*)(lds + S_RC + 3 * S_RCS + tid * 16) = (u32x4){s12, s23, s34, s45};
        }
        S4S(1)
        {
            const float dt = bf2f(f2bf(expf(bf2f(pT))));
            const float ar = -bf2f(f2bf(expf(bf2f(pA)))) * dt, ai = bf2f(pI) * dt;
            const f32x2 C = {2.f * bf2f(pCr) * dt, 2.f * bf2f(pCi) * dt};
            const int dn = min(d0 + ch + 1, DM - 1);
            pA = a.lAr[dn * NS + n]; pI = a.Aim[dn * NS + n]; pCr = a.Cr[dn * NS + n]; pCi = a.Ci[dn * NS + n]; pT = a.ldt[dn]; pD = a.Dsk[dn];
            const int to = S_TAB + n * 8;
            auto put = [&](int row, f32x2 v) { *(LAS f32x2*)(lds + to + row * S_TROW) = v; };
            const f32x2 z128 = cexp_p(ar, ai, 128.f);
            if (w < 2) {
                f32x2 E[8], G[4];
                E[1] = cexp_p(ar, ai, 1.f); G[0] = C;
#pragma unroll
                for (int r = 2; r < 8; ++r) E[r] = cmul(E[r >> 1], E[r - (r >> 1)]);
#pragma unroll
                for (int k = 1; k < 4; ++k) G[k] = cmul(G[k - 1], z128);
#pragma unroll
                for (int k = 0; k < 4; ++k)
#pragma unroll
                    for (int r = 0; r < 8; r += 1 + (k >> 1)) put(k < 2 ? 8 * k + r : 8 + 4 * k + (r >> 1), r ? cmul(E[r], G[k]) : G[k]);
            } else {
                const f32x2 z4 = cexp_p(ar, ai, 4.f), z8 = cexp_p(ar, -ai, 8.f);
                f32x2 G = cmul(C, cexp_p(ar, ai, 512.f)), B = z8;
                put(41, (f32x2){1.f, 0.f});
#pragma unroll
                for (int k = 1; k < 16; ++k) {
                    put(41 + k, B); B = cmul(B, z8);
                    if (k < 5) { put(22 + 2 * k, G); put(23 + 2 * k, cmul(G, z4)); }
                    else if (k < 14) put(27 + k, G);
                    G = cmul(G, z128);
                }
            }
        }
        WG_BARRIER();
        S4S(2)
        {
            const int bo = S_TAB + (41 + nn) * S_TROW + g * 16;
            f32x4 av[12], bv[12], acc[3] = {};
            if (w < 3) {
                const int ao = S_TAB + min(16 * w + nn, 40) * S_TROW + g * 16;
#pragma unroll
                for (int j = 0; j < 12; ++j) { av[j] = *(LAS f32x4*)(lds + ao + j * 64); bv[j] = *(LAS f32x4*)(lds + bo + j * 64); }
#pragma unroll
                for (int j = 0; j < 48; ++j) acc[j & 1] = __builtin_amdgcn_mfma_f32_16x16x4f32(av[j >> 2][j & 3], bv[j >> 2][j & 3], acc[j & 1], 0, 0, 0);
#pragma unroll
                for (int r = 0; r < 4; ++r) *(LAS float*)(lds + S_KD + ((16 * w + 4 * g + r) * 16 + nn) * 8) = acc[0][r] + acc[1][r];
            } else {
#pragma unroll
                for (int j = 0; j < 4; ++j) {
                    bv[j] = *(LAS f32x4*)(lds + bo + 768 + j * 64);
#pragma unroll
                    for (int t = 0; t < 3; ++t) av[3 * j + t] = *(LAS f32x4*)(lds + S_TAB + min(16 * t + nn, 40) * S_TROW + g * 16 + 768 + j * 64);
                }
#pragma unroll
                for (int j = 0; j < 16; ++j)
#pragma unroll
                    for (int t = 0; t < 3; ++t) acc[t] = __builtin_amdgcn_mfma_f32_16x16x4f32(av[3 * (j >> 2) + t][j & 3], bv[j >> 2][j & 3], acc[t], 0, 0, 0);
#pragma unroll
                for (int t = 0; t < 3; ++t)
#pragma unroll
                    for (int r = 0; r < 4; ++r) *(LAS float*)(lds + S_KD + ((16 * t + 4 * g + r) * 16 + nn) * 8 + 4) = acc[t][r];
            }
        }
        WG_BARRIER();
        S4S(3)
        {
            float kv[8];
#pragma unroll
            for (int j = 0; j < 8; ++j) { const f32x2 t = *(LAS f32x2*)(lds + kd[j]); kv[j] = t.x + t.y; }
            u32x4 hi, lo;
#pragma unroll
            for (int j = 0; j < 4; ++j) { hi[j] = pack2(kv[2 * j], kv[2 * j + 1]); lo[j] = pack2(kv[2 * j] - bflo(hi[j]), kv[2 * j + 1] - bfhi(hi[j])); }
            const int o = S_K + (15 + (tid >> 2)) * 160 + (tid & 3) * 16;
            *(LAS u32x4*)(lds + o) = hi; *(LAS u32x4*)(lds + o + 64) = lo;
        }
        WG_BARRIER();
        S4S(4)
        {
            const int st0 = 2047 - nn + 8 * g - 32 * w, cc = st0 & 3;
            const int ub = S_RC + cc * S_RCS + (st0 - cc) * 2 - 3872, kb = S_K + (3 + nn - w) * 160 + g * 16;
            f32x4 y[4][2] = {};
            u32x4 F[2][16];
            auto rd = [&](int i, int f) {
                if (f & 1) { const int o = ub + 3872 - 256 * i - 1024 * (f >> 2) - 32 * ((f >> 1) & 1); const u32x2 x = *(LAS u32x2*)(lds + o), z = *(LAS u32x2*)(lds + o + 8); F[i & 1][f] = (u32x4){x.x, x.y, z.x, z.y}; }
                else F[i & 1][f] = *(LAS u32x4*)(lds + kb + (12 - 4 * i + 16 * ((f >> 1) & 3)) * 160 + (f >> 3) * 64);
            };
#pragma unroll
            for (int f = 0; f < 16; ++f) rd(0, f);
            FENCE();
#pragma unroll
            for (int i = 0; i < 4; ++i) {
                int c = 0;
#pragma unroll
                for (int hl = 0; hl < 2; ++hl)
#pragma unroll
                    for (int q = 0; q < 4; ++q)
#pragma unroll
                        for (int T = q; T < 4; ++T) {
                            y[T][0] = MFMA16(F[i & 1][4 * q + 1], F[i & 1][8 * hl + 2 * (T - q)], y[T][0]);
                            y[T][1] = MFMA16(F[i & 1][4 * q + 3], F[i & 1][8 * hl + 2 * (T - q)], y[T][1]);
                            if (i < 3 && c < 16) rd(i + 1, c);
                            ++c;
                            FENCE();
                        }
            }
#pragma unroll
            for (int T = 0; T < 4; ++T)
#pragma unroll
                for (int bt = 0; bt < 2; ++bt) *(LAS f32x4*)(lds + S_TAB + (w * 64 + 16 * T + nn) * 144 + bt * 64 + g * 16) = y[T][bt];
        }
        WG_BARRIER();
        S4S(5)
        {
            f32x4 y0 = {0, 0, 0, 0}, y1 = y0;
#pragma unroll
            for (int ww = 0; ww < 4; ++ww) {
                const int o = S_TAB + (ww * 64 + (tid >> 2)) * 144 + (tid & 3) * 32;
                y0 += *(LAS f32x4*)(lds + o); y1 += *(LAS f32x4*)(lds + o + 16);
            }
            const u32x4 u = *(LAS u32x4*)(lds + S_XR + ch * S_XRS + (255 - tid) * 16);
            st128((u32x4){pack2(y0[0] + bfhi(u.w) * dsk, y0[1] + bflo(u.w) * dsk), pack2(y0[2] + bfhi(u.z) * dsk, y0[3] + bflo(u.z) * dsk),
                          pack2(y1[0] + bfhi(u.y) * dsk, y1[1] + bflo(u.y) * dsk), pack2(y1[2] + bfhi(u.x) * dsk, y1[3] + bflo(u.x) * dsk)}, rw, tid * 16, OFF_BO + (d0 + ch) * 4096);
        }
        S4S(6)
    }
    wait_init(rw, launch_tag(a), budget);
    VM_DRAIN();
    WG_BARRIER();
    if (tid == 0) st32(1u, rw, F_S4 + task * 4, 0);
}

constexpr int XT_BYTES = 32 * 1152;
constexpr int P_XT = 0;
constexpr int P_STG = 2 * XT_BYTES;
struct WSlice { u32x4 f[36]; };
constexpr int P_RING = 5;
template <class F> DI void load_wslices(WSlice& W0, WSlice& W1, const u16* s0, const u16* s1, int w, int lane, LAS char* lds, unsigned lds_base, F also) {
    constexpr int NB = 18;
    const int r0 = 8 * (lane >> 5) + ((lane & 15) >> 2);
    const int cb = (4 * w + 2 * ((lane >> 4) & 1) + ((lane & 3) >> 1)), lo8 = (lane & 1) * 8;
    auto src = [&](int i) { return (i < 9 ? s0 : s1) + (size_t)(64 * (i % 9)) * HD; };
    const u32x4 ro = ring_offs(w, lane, HD);
#pragma unroll
    for (int i = 0; i < P_RING - 1; ++i) ring_issue(src(i), ro, lds_base + P_STG + i * 16384 + w * 4096);
    also();
    static_for<NB>([&](auto ic) {
        constexpr int i = decltype(ic)::value, rem = NB - 1 - i < P_RING - 2 ? NB - 1 - i : P_RING - 2;
        ring_wait<4 * rem>();
        if constexpr (i + P_RING - 1 < NB) ring_issue(src(i + P_RING - 1), ro, lds_base + P_STG + ((i + P_RING - 1) % P_RING) * 16384 + w * 4096);
        const int sl = P_STG + (i % P_RING) * 16384;
#pragma unroll
        for (int kk = 0; kk < 4; ++kk) {
            const int ra = r0 + 16 * kk, rb = ra + 4;
            const u32x2 lo = tr_read(lds, sl + ra * 256 + ((cb ^ (2 * swz(ra))) * 16) + lo8), hi = tr_read(lds, sl + rb * 256 + ((cb ^ (2 * swz(rb))) * 16) + lo8);
            (i < 9 ? W0 : W1).f[(i % 9) * 4 + kk] = (u32x4){lo.x, lo.y, hi.x, hi.y};
        }
        u32x4* f = (i < 9 ? W0 : W1).f + (i % 9) * 4;
        if constexpr (i < 16) asm volatile("" : "+a"(f[0]), "+a"(f[1]), "+a"(f[2]), "+a"(f[3]));
    });
}
DI u32x4 piece_pi(const f32x16& a, int qq) {
    return (u32x4){pack2(a[4 * qq], a[4 * qq + 8]), pack2(a[4 * qq + 1], a[4 * qq + 9]), pack2(a[4 * qq + 2], a[4 * qq + 10]), pack2(a[4 * qq + 3], a[4 * qq + 11])};
}
DI u32x4 piece_seq(const f32x16& a, int s2) {
    return (u32x4){pack2(a[8 * s2], a[8 * s2 + 1]), pack2(a[8 * s2 + 2], a[8 * s2 + 3]), pack2(a[8 * s2 + 4], a[8 * s2 + 5]), pack2(a[8 * s2 + 6], a[8 * s2 + 7])};
}
struct XTile {
    int xs[9], xa[4];
    DI void init(int w, int lane) {
#pragma unroll
        for (int i = 0; i < 9; ++i) { const int L = (i * 4 + w) * 64 + lane, r = L / 72, cs = L - 72 * r; xs[i] = r * 1152 + (cs ^ ((r >> 1) & 7)) * 16; }
        const int c32 = lane & 31, hf = lane >> 5;
#pragma unroll
        for (int j = 0; j < 4; ++j) xa[j] = P_XT + c32 * 1152 + (((2 * j + hf) ^ ((c32 >> 1) & 7)) * 16);
    }
    DI void dma(const Args& a, unsigned lds_base, int w, int s, int buf, int i) const {
        glds16((const char*)a.x + (size_t)min(s, NSEG - 1) * XT_BYTES, xs[i], lds_base + P_XT + buf * XT_BYTES + (i * 4 + w) * 1024);
    }
    DI void first(const Args& a, unsigned lds_base, int w, int lane, int s) {
        init(w, lane);
#pragma unroll
        for (int i = 0; i < 9; ++i) dma(a, lds_base, w, s, 0, i);
    }
};
template <bool TR, class F> DI void project(f32x16& acc0, f32x16& acc1, const WSlice& W0, const WSlice& W1, const XTile& X, const Args& a, LAS char* lds, unsigned lds_base, int w, int buf, int sn, F mid) {
    const int bo = buf * XT_BYTES;
    constexpr int PD = 5;
    u32x4 xf[PD];
#pragma unroll
    for (int i = 0; i < PD; ++i) xf[i] = *(LAS u32x4*)(lds + X.xa[i & 3] + bo + (i >> 2) * 128);
#pragma unroll
    for (int ks = 0; ks < 36; ++ks) {
        if (ks == 0) asm volatile("v_mfma_f32_32x32x16_bf16 %0, %1, %2, 0" : "=&v"(acc0) : "a"(W0.f[0]), "v"(xf[0]) : "memory");
        else asm volatile("v_mfma_f32_32x32x16_bf16 %0, %1, %2, %0" : "+v"(acc0) : "a"(W0.f[ks]), "v"(xf[ks % PD]) : "memory");
        if (TR) {
            if (ks == 0) asm volatile("v_mfma_f32_32x32x16_bf16 %0, %2, %1, 0" : "=&v"(acc1) : "a"(W1.f[0]), "v"(xf[0]) : "memory");
            else if (ks < 28) asm volatile("v_mfma_f32_32x32x16_bf16 %0, %2, %1, %0" : "+v"(acc1) : "a"(W1.f[ks]), "v"(xf[ks % PD]) : "memory");
            else asm volatile("v_mfma_f32_32x32x16_bf16 %0, %2, %1, %0" : "+v"(acc1) : "v"(W1.f[ks]), "v"(xf[ks % PD]) : "memory");
        } else {
            if (ks == 0) asm volatile("v_mfma_f32_32x32x16_bf16 %0, %1, %2, 0" : "=&v"(acc1) : "a"(W1.f[0]), "v"(xf[0]) : "memory");
            else if (ks < 28) asm volatile("v_mfma_f32_32x32x16_bf16 %0, %1, %2, %0" : "+v"(acc1) : "a"(W1.f[ks]), "v"(xf[ks % PD]) : "memory");
            else asm volatile("v_mfma_f32_32x32x16_bf16 %0, %1, %2, %0" : "+v"(acc1) : "v"(W1.f[ks]), "v"(xf[ks % PD]) : "memory");
        }
        if (ks + PD < 36) xf[ks % PD] = *(LAS u32x4*)(lds + X.xa[(ks + PD) & 3] + bo + ((ks + PD) >> 2) * 128);
        if (ks < 18 && (ks & 1)) X.dma(a, lds_base, w, sn, buf ^ 1, ks >> 1);
        if (ks == 14) mid();
    }
    asm volatile("s_nop 7\n\ts_nop 3" : "+v"(acc0), "+v"(acc1) :: "memory");
}
DI void role_kv(const Args& a, LAS char* lds, unsigned lds_base, int h, rsrc_t rw, int& budget) {
    const int tid = threadIdx.x, lane = tid & 63, w = __builtin_amdgcn_readfirstlane(tid >> 6);
    WSlice W0, W1;
    XTile X;
    u32x2 tag;
    load_wslices(W0, W1, a.Wk + h * DH, a.Wv + h * DH, w, lane, lds, lds_base, [&]() { X.first(a, lds_base, w, lane, 0); tag = launch_tag(a); });
    VM_DRAIN();
    WG_BARRIER();
    const int oo = h * 8192 + w * 2048 + lane * 16;
    const int fo = lane == 0 ? F_KV + h * 512 + w * 128 : OOB;
    for (int s = 0; s < NSEG; ++s) {
        f32x16 acc0, acc1;
        project<true>(acc0, acc1, W0, W1, X, a, lds, lds_base, w, s & 1, s + 1, [&]() {
            asm volatile("s_waitcnt vmcnt(7)" ::: "memory");
            st32((unsigned)(s - 1), rw, s ? fo : OOB, 0);
        });
        const int so = s * (NH * 8192);
        st128(piece_pi(acc0, 0), rw, oo, OFF_KF + so);
        st128(piece_pi(acc0, 1), rw, oo + 1024, OFF_KF + so);
        st128(piece_seq(acc1, 0), rw, oo, OFF_VF + so);
        st128(piece_seq(acc1, 1), rw, oo + 1024, OFF_VF + so);
        if (s == 0) { wait_init(rw, tag, budget); VM_DRAIN(); }
        asm volatile("s_waitcnt vmcnt(4)" ::: "memory");
        WG_BARRIER();
        if (s == 0) __builtin_amdgcn_raw_buffer_store_b64(tag, rw, tid == 0 ? F_INIT + (33 + h) * 128 : OOB, 0, 16);
    }
    VM_DRAIN();
    st32(NSEG, rw, fo, 0);
}

DI int prog_min(rsrc_t rw, int off) {
    const int v = (int)ld32(rw, off, 0);
    asm volatile("" ::: "memory");
    return -wave_max32(-v);
}
DI float red_max_g(float v) {
    auto a = __builtin_amdgcn_permlane16_swap(__float_as_uint(v), __float_as_uint(v), false, false);
    v = fmaxf(__uint_as_float(a[0]), __uint_as_float(a[1]));
    auto b = __builtin_amdgcn_permlane32_swap(__float_as_uint(v), __float_as_uint(v), false, false);
    return fmaxf(__uint_as_float(b[0]), __uint_as_float(b[1]));
}
#define M16 "v_mfma_f32_16x16x32_bf16 "
DI void role_qa(const Args& a, LAS char* lds, unsigned lds_base, int h0, int par, rsrc_t rw, int& budget) {
    const int tid = threadIdx.x, lane = tid & 63, w = __builtin_amdgcn_readfirstlane(tid >> 6);
    const int n = lane & 15, g = lane >> 4, h = h0 + (w >> 1), tt = w & 1;
    WSlice W0, W1;
    XTile X;
    load_wslices(W0, W1, a.Wq + h0 * DH, a.Wq + (h0 + 1) * DH, w, lane, lds, lds_base, [&]() { X.first(a, lds_base, w, lane, par); });
    VM_DRAIN();
    WG_BARRIER();
    constexpr int Q_Q = P_STG, Q_KV = Q_Q + 16384, Q_M = Q_KV + 32768;
    static_assert(Q_M + 8192 <= P_STG + P_RING * 16384, "");
    auto proj = [&](int s) {
        f32x16 acc0, acc1;
        project<false>(acc0, acc1, W0, W1, X, a, lds, lds_base, w, (s >> 1) & 1, s + 2, []() {});
        const int o = Q_Q + w * 2048 + lane * 16;
        *(LAS u32x4*)(lds + o) = piece_pi(acc0, 0); *(LAS u32x4*)(lds + o + 1024) = piece_pi(acc0, 1);
        *(LAS u32x4*)(lds + o + 8192) = piece_pi(acc1, 0); *(LAS u32x4*)(lds + o + 8192 + 1024) = piece_pi(acc1, 1);
    };
    const int fs = (tt ? OFF_VF : OFF_KF) + (g >> 1) * 1024 + ((g & 1) * 32 + 16 * (n >> 3) + (n & 7)) * 16;
    auto fetch = [&](int s) {
        const int so = (min(s, NSEG - 1) * NH + h) * 8192;
#pragma unroll
        for (int f = 0; f < 8; ++f) bdma(rw, fs, so + (f >> 1) * 2048 + (f & 1) * 128, lds_base + Q_KV + w * 8192 + f * 1024);
    };
    const int ms = tt ? OFF_VM + lane * 16 : OFF_KM + (lane & 7) * 256 + (lane >> 3) * 16;
    auto fetch_m = [&](int s) {
        const int so = (s * NH + h) * 2048;
        bdma(rw, ms, so, lds_base + Q_M + w * 2048); bdma(rw, ms, so + (tt ? 1024 : 128), lds_base + Q_M + w * 2048 + 1024);
    };
    const float c = 0.08838834764831845f * 1.4426950408889634f;
    u32x4 qf[4];
    f32x4 o[8]; float mx, l;
    auto xpart = [&]() {
        const int kb = Q_KV + (w >> 1) * 16384 + lane * 16;
        u32x4 kn[8], vn[8];
#pragma unroll
        for (int ks = 0; ks < 4; ++ks) qf[ks] = *(LAS u32x4*)(lds + Q_Q + (w >> 1) * 8192 + (2 * ks + (g >> 1)) * 1024 + ((g & 1) * 32 + 16 * tt + n) * 16);
#pragma unroll
        for (int f = 0; f < 8; ++f) kn[f] = *(LAS u32x4*)(lds + kb + f * 1024);
        FENCE();
#pragma unroll
        for (int f = 0; f < 8; ++f) vn[f] = *(LAS u32x4*)(lds + kb + 8192 + f * 1024);
        f32x4 s0, s1;
        asm volatile(M16 "%0, %2, %10, 0\n\t" M16 "%1, %3, %10, 0\n\t" M16 "%0, %4, %11, %0\n\t" M16 "%1, %5, %11, %1\n\t"
                     M16 "%0, %6, %12, %0\n\t" M16 "%1, %7, %12, %1\n\t" M16 "%0, %8, %13, %0\n\t" M16 "%1, %9, %13, %1\n\ts_nop 7"
                     : "=&v"(s0), "=&v"(s1) : "v"(kn[0]), "v"(kn[1]), "v"(kn[2]), "v"(kn[3]), "v"(kn[4]), "v"(kn[5]), "v"(kn[6]), "v"(kn[7]), "v"(qf[0]), "v"(qf[1]), "v"(qf[2]), "v"(qf[3]));
        float p[8];
        mx = -3.0e38f; l = 0.f;
#pragma unroll
        for (int r = 0; r < 8; ++r) { const int key = 16 * (g >> 1) + 4 * (g & 1) + (r & 3) + 8 * (r >> 2); p[r] = key <= 16 * tt + n ? (r < 4 ? s0[r & 3] : s1[r & 3]) : -3.0e38f; mx = fmaxf(mx, p[r]); }
        mx = red_max_g(mx);
#pragma unroll
        for (int r = 0; r < 8; ++r) { p[r] = __builtin_amdgcn_exp2f((p[r] - mx) * c); l += p[r]; }
        const u32x4 pf = {pack2(p[0], p[1]), pack2(p[2], p[3]), pack2(p[4], p[5]), pack2(p[6], p[7])};
        asm volatile("s_nop 1\n\t" M16 "%0, %8, %16, 0\n\t" M16 "%1, %9, %16, 0\n\t" M16 "%2, %10, %16, 0\n\t" M16 "%3, %11, %16, 0\n\t"
                     M16 "%4, %12, %16, 0\n\t" M16 "%5, %13, %16, 0\n\t" M16 "%6, %14, %16, 0\n\t" M16 "%7, %15, %16, 0\n\ts_nop 7"
                     : "=&v"(o[0]), "=&v"(o[1]), "=&v"(o[2]), "=&v"(o[3]), "=&v"(o[4]), "=&v"(o[5]), "=&v"(o[6]), "=&v"(o[7])
                     : "v"(vn[0]), "v"(vn[1]), "v"(vn[2]), "v"(vn[3]), "v"(vn[4]), "v"(vn[5]), "v"(vn[6]), "v"(vn[7]), "v"(pf));
    };
    proj(par);
    wait_init(rw, launch_tag(a), budget);
    wait_prog(rw, F_KV + h * 512 + (lane & 3) * 128, true, par, budget);
    fetch(par);
    VM_DRAIN();
    WG_BARRIER();
    xpart();
    WG_BARRIER();
    proj(par + 2);
    const int fm = F_KVM + h * 128 + (lane & 7) * 4, fo = lane == 0 ? F_O + h * 128 + (2 * par + tt) * 4 : OOB;
    int m_rdy = -1;
    bool pre = false;
    for (int s = par; s < NSEG; s += 2) {
        S4S(0)
        if (!pre) {
            while (m_rdy < s) { m_rdy = prog_min(rw, fm); if (--budget < 0) break; }
            fetch_m(s); fetch(s + 2);
        }
        S4S(1)
        asm volatile("s_waitcnt vmcnt(8)" ::: "memory");
        WG_BARRIER();
        S4S(2)
        const int mb = Q_M + (w >> 1) * 4096;
        u32x4 kf[4]; u32x2 vm[4];
#pragma unroll
        for (int ks = 0; ks < 4; ++ks) kf[ks] = *(LAS u32x4*)(lds + mb + (4 * ks + g) * 128 + (2 * (n >> 2) + (n & 1)) * 16);
#pragma unroll
        for (int t = 0; t < 4; ++t) { const int vo = mb + 2048 + t * 512 + (n >> 3) * 256 + g * 64 + (n & 7) * 4; vm[t] = (u32x2){*(LAS unsigned*)(lds + vo), *(LAS unsigned*)(lds + vo + 32)}; }
        const int pm_ = (int)ld32(rw, fm, 0);
        f32x4 sm;
        asm volatile(M16 "%0, %1, %5, 0\n\t" M16 "%0, %2, %6, %0\n\t" M16 "%0, %3, %7, %0\n\t" M16 "%0, %4, %8, %0\n\ts_nop 7"
                     : "=&v"(sm) : "v"(kf[0]), "v"(kf[1]), "v"(kf[2]), "v"(kf[3]), "v"(qf[0]), "v"(qf[1]), "v"(qf[2]), "v"(qf[3]));
        const float m2 = red_max_g(fmaxf(mx, fmaxf(sm[0], sm[1])));
        const float al = __builtin_amdgcn_exp2f((mx - m2) * c), p0 = __builtin_amdgcn_exp2f((sm[0] - m2) * c), p1 = __builtin_amdgcn_exp2f((sm[1] - m2) * c);
        const float inv = __builtin_amdgcn_rcpf(red_sum_g(l * al + p0 + p1)), ai = al * inv;
#pragma unroll
        for (int t = 0; t < 8; ++t) o[t] *= ai;
        const unsigned pm = pack2(p0 * inv, p1 * inv);
#define M1K "v_mfma_f32_16x16x16_bf16 "
        asm volatile("s_nop 1\n\t" M1K "%0, %8, %12, %0\n\t" M1K "%1, %8, %13, %1\n\t" M1K "%2, %9, %12, %2\n\t" M1K "%3, %9, %13, %3\n\t"
                     M1K "%4, %10, %12, %4\n\t" M1K "%5, %10, %13, %5\n\t" M1K "%6, %11, %12, %6\n\t" M1K "%7, %11, %13, %7\n\ts_nop 7"
                     : "+v"(o[0]), "+v"(o[1]), "+v"(o[2]), "+v"(o[3]), "+v"(o[4]), "+v"(o[5]), "+v"(o[6]), "+v"(o[7])
                     : "v"(vm[0]), "v"(vm[1]), "v"(vm[2]), "v"(vm[3]), "v"((u32x2){pm, 0u}), "v"((u32x2){0u, pm}));
        const int oo = OFF_OF + lane * 16, so = ((2 * s + tt) * NH + h) * 4096;
#pragma unroll
        for (int t = 0; t < 4; ++t)
            st128((u32x4){pack2(o[2 * t][0], o[2 * t][1]), pack2(o[2 * t][2], o[2 * t][3]), pack2(o[2 * t + 1][0], o[2 * t + 1][1]), pack2(o[2 * t + 1][2], o[2 * t + 1][3])}, rw, oo + t * 1024, so);
        S4S(3)
        if (s + 2 < NSEG) {
            asm volatile("s_waitcnt vmcnt(4)" ::: "memory");
            WG_BARRIER();
            S4S(4)
            xpart();
        }
        FENCE();
        S4S(5)
        m_rdy = max(m_rdy, -wave_max32(-pm_));
        VM_DRAIN();
        st32((unsigned)s, rw, fo, 0);
        S4S(6)
        WG_BARRIER();
        pre = m_rdy >= s + 2;
        if (pre) { fetch_m(s + 2); fetch(s + 4); }
        S4S(7)
        if (s + 4 < NSEG) proj(s + 4);
        S4S(8)
    }
}

constexpr int O_W = 0;
constexpr int O_STG = 131072;
constexpr int OUT_REP = 7;
DI void role_outp(const Args& a, LAS char* lds, int nb, int rep, rsrc_t rw, int& budget) {
    const int tid = threadIdx.x, lane = tid & 63, w = __builtin_amdgcn_readfirstlane(tid >> 6);
    WG_BARRIER();
    const int n = lane & 15, g = lane >> 4;
    const int wl = O_W + w * 32768 + lane * 16;
    u32x4 wa[64], wv[32];
    {
        const int stg = O_STG + w * 5120;
        const rsrc_t ro = __builtin_amdgcn_make_buffer_rsrc((void*)a.Wo, 0, HD * DM * 2, 0x00020000);
        const int so = (1024 * w + (lane >> 3)) * (DM * 2) + nb * 128 + (lane & 7) * 16, sw = stg + (lane >> 3) * 160 + (lane & 7) * 16;
        const int ra = stg + (16 * (g >> 1) + 4 * (g & 1) + (n >> 2)) * 160 + (n & 3) * 8;
        constexpr int PB = 4;
        u32x4 b[PB][4];
        auto fetch = [&](int ks) {
#pragma unroll
            for (int i = 0; i < 4; ++i) b[ks % PB][i] = __builtin_amdgcn_raw_buffer_load_b128(ro, so, (32 * ks + 8 * i) * (DM * 2), 0);
        };
#pragma unroll
        for (int ks = 0; ks < PB; ++ks) fetch(ks);
        FENCE();
#pragma unroll
        for (int ks = 0; ks < 32; ++ks) {
#pragma unroll
            for (int i = 0; i < 4; ++i) *(LAS u32x4*)(lds + sw + i * 1280) = b[ks % PB][i];
            if (ks + PB < 32) fetch(ks + PB);
            u32x4 f[4];
#pragma unroll
            for (int t = 0; t < 4; ++t) { const u32x2 lo = tr_read(lds, ra + t * 32), hi = tr_read(lds, ra + t * 32 + 1280); f[t] = (u32x4){lo.x, lo.y, hi.x, hi.y}; }
            wa[2 * ks] = f[0]; wa[2 * ks + 1] = f[1]; wv[ks] = f[2];
            *(LAS u32x4*)(lds + wl + ks * 1024) = f[3];
            asm volatile("" : "+a"(wa[2 * ks]), "+a"(wa[2 * ks + 1]), "+v"(wv[ks]));
            FENCE();
        }
    }
    wait_prog(rw, F_S4 + lane * 4, true, 1, budget);
    int o_rdy = -1;
    const int tok = tid >> 4, c4 = (tid & 15) * 4;
    const int lo_ = OFF_OF + w * 32768 + lane * 16, fo = F_O + (8 * w + ((lane >> 2) & 7)) * 128 + (lane & 3) * 4;
    const int tl = nb < 8 ? OFF_OF + w * 32768 + nb * 4096 + lane * 64 : OOB;
    const int bo = OFF_BO + (64 * nb + c4) * 4096 + tok * 2;
    for (int u = rep; u < 2 * NSEG; u += OUT_REP) {
        WG_BARRIER();
        S4S(0)
        while (!__all((lane & 3) != (u & 3) || o_rdy >= (u >> 1))) { o_rdy = (int)ld32(rw, fo, 0); asm volatile("" ::: "memory"); if (--budget < 0) break; }
        S4S(1)
        const unsigned tv = ld32(rw, tl, u * 131072);
        u16 bv[4];
#pragma unroll
        for (int j = 0; j < 4; ++j) bv[j] = __builtin_amdgcn_raw_buffer_load_b16(rw, bo, u * 32 + j * 4096, 16);
        const float gt = __uint_as_float(ld32(rw, OFF_GT + tok * 4, u * 64)), g1 = 1.0f - gt;
        FENCE();
        constexpr int RO = 16;
        u32x4 of[RO], w3[3];
#pragma unroll
        for (int i = 0; i < RO; ++i) { of[i] = ld128(rw, lo_ + (i & 3) * 1024, u * 131072 + (i >> 2) * 4096); FENCE(); }
        w3[0] = *(LAS u32x4*)(lds + wl); w3[1] = *(LAS u32x4*)(lds + wl + 1024);
        f32x4 acc[4];
#pragma unroll
        for (int i = 0; i < 32; ++i) {
            if (i + 2 < 32) w3[(i + 2) % 3] = *(LAS u32x4*)(lds + wl + (i + 2) * 1024);
            if (i == 0) asm volatile(M16 "%0, %4, %8, 0\n\t" M16 "%1, %5, %8, 0\n\t" M16 "%2, %6, %8, 0\n\t" M16 "%3, %7, %8, 0"
                                     : "=&v"(acc[0]), "=&v"(acc[1]), "=&v"(acc[2]), "=&v"(acc[3]) : "a"(wa[0]), "a"(wa[1]), "v"(wv[0]), "v"(w3[0]), "v"(of[0]) : "memory");
            else asm volatile(M16 "%0, %4, %8, %0\n\t" M16 "%1, %5, %8, %1\n\t" M16 "%2, %6, %8, %2\n\t" M16 "%3, %7, %8, %3"
                              : "+v"(acc[0]), "+v"(acc[1]), "+v"(acc[2]), "+v"(acc[3]) : "a"(wa[2 * i]), "a"(wa[2 * i + 1]), "v"(wv[i]), "v"(w3[i % 3]), "v"(of[i % RO]) : "memory");
            if (i + RO < 32) of[i % RO] = ld128(rw, lo_ + (i & 3) * 1024, u * 131072 + ((i + RO) >> 2) * 4096);
        }
        asm volatile("s_nop 7" : "+v"(acc[0]), "+v"(acc[1]), "+v"(acc[2]), "+v"(acc[3]) : "v"(tv) : "memory");
        S4S(2)
#pragma unroll
        for (int t = 0; t < 4; ++t) *(LAS f32x4*)(lds + O_STG + (w * 16 + n) * 272 + t * 64 + g * 16) = acc[t];
        WG_BARRIER();
        S4S(3)
        f32x4 v = *(LAS f32x4*)(lds + O_STG + tok * 272 + c4 * 4);
#pragma unroll
        for (int ww = 1; ww < 4; ++ww) v += *(LAS f32x4*)(lds + O_STG + (ww * 16 + tok) * 272 + c4 * 4);
        __builtin_nontemporal_store((u32x2){pack2(gt * v[0] + g1 * bf2f(bv[0]), gt * v[1] + g1 * bf2f(bv[1])), pack2(gt * v[2] + g1 * bf2f(bv[2]), gt * v[3] + g1 * bf2f(bv[3]))},
                                    (u32x2*)(a.out + (size_t)(u * 16 + tok) * DM + 64 * nb + c4));
        S4S(4)
    }
}
#define STEP_ASM \
    "v_readfirstlane_b32 s36, v0\n" \
    "v_readfirstlane_b32 s37, v1\n" \
    "v_readfirstlane_b32 s38, v2\n" \
    "v_readfirstlane_b32 s39, v3\n" \
    "v_readfirstlane_b32 s40, v4\n" \
    "v_readfirstlane_b32 s41, v5\n" \
    "v_readfirstlane_b32 s42, v6\n" \
    "v_readfirstlane_b32 s43, v7\n" \
    "v_readfirstlane_b32 s28, v8\n" \
    "v_readfirstlane_b32 s29, v9\n" \
    "v_readfirstlane_b32 s30, v10\n" \
    "v_readfirstlane_b32 s31, v11\n" \
    "v_readfirstlane_b32 s32, v12\n" \
    "v_readfirstlane_b32 s33, v13\n" \
    "v_readfirstlane_b32 s34, v14\n" \
    "v_readfirstlane_b32 s35, v15\n" \
    "v_readfirstlane_b32 s24, v16\n" \
    "v_readfirstlane_b32 s25, v17\n" \
    "v_readfirstlane_b32 s26, v18\n" \
    "v_readfirstlane_b32 s27, v19\n" \
    "v_readfirstlane_b32 s60, v24\n" \
    "v_readfirstlane_b32 s61, v25\n" \
    "v_readfirstlane_b32 s62, v26\n" \
    "v_readfirstlane_b32 s63, v27\n" \
    "v_readfirstlane_b32 s64, v28\n" \
    "v_readfirstlane_b32 s65, v29\n" \
    "v_readfirstlane_b32 s66, v30\n" \
    "v_readfirstlane_b32 s67, v31\n" \
    "v_mov_b64 v[40:41], 0\n" \
    "v_mov_b64 v[42:43], 0\n" \
    "v_mov_b64 v[44:45], 0\n" \
    "v_mov_b64 v[46:47], 0\n" \
    "v_mov_b64 v[48:49], 0\n" \
    "v_mov_b64 v[50:51], 0\n" \
    "v_mov_b64 v[52:53], 0\n" \
    "v_mov_b64 v[54:55], 0\n" \
    "s_mov_b32 s68, m0\n" \
    "s_getpc_b64 s[96:97]\n" \
    "14:\n" \
    "s_add_u32 s96, s96, 2f-14b\n" \
    "s_addc_u32 s97, s97, 0\n" \
    "s_lshl_b32 s72, s60, 10\n" \
    "s_add_i32 s73, s72, 4096\n" \
    "s_lshr_b32 s74, s72, 2\n" \
    "s_add_i32 s74, s74, 8192\n" \
    "s_lshl_b32 s95, s72, 1\n" \
    "s_add_i32 s95, s95, 16384\n" \
    "s_mov_b32 s81, 0x3e0293ee\n" \
    "s_mov_b32 s82, 0x53800000\n" \
    "s_lshl_b32 s83, s60, 6\n" \
    "s_and_b32 s83, s83, 128\n" \
    "v_mov_b32 v109, 2.0\n" \
    "0:\n" \
    "s_and_b32 s75, s36, 3\n" \
    "s_lshl_b32 s75, s75, 17\n" \
    "s_add_i32 s75, s75, s26\n" \
    "s_add_i32 s69, s75, s72\n" \
    "s_add_i32 s70, s69, 4096\n" \
    "s_add_i32 s71, s75, 16384\n" \
    "s_mov_b32 m0, s72\n" \
    "s_cmp_eq_u32 s36, 0\n" \
    "s_cbranch_scc1 3f\n" \
    "12:\n" \
    "s_sub_i32 s90, s37, s29\n" \
    "s_mul_i32 s90, s90, s25\n" \
    "s_ashr_i32 s90, s90, 12\n" \
    "s_add_i32 s90, s90, s24\n" \
    "s_add_i32 s90, s90, -116\n" \
    "1:\n" \
    "s_memtime vcc\n" \
    "s_waitcnt lgkmcnt(0)\n" \
    "s_sub_i32 s75, s90, vcc_lo\n" \
    "s_cmp_gt_i32 s75, 112\n" \
    "s_cbranch_scc1 1b\n" \
    "s_max_i32 s75, s75, 0\n" \
    "s_and_b32 s75, s75, -4\n" \
    "s_sub_u32 vcc_lo, s96, s75\n" \
    "s_subb_u32 vcc_hi, s97, 0\n" \
    "s_setpc_b64 vcc\n" \
    "s_nop 0\n" \
    "s_nop 0\n" \
    "s_nop 0\n" \
    "s_nop 0\n" \
    "s_nop 0\n" \
    "s_nop 0\n" \
    "s_nop 0\n" \
    "s_nop 0\n" \
    "s_nop 0\n" \
    "s_nop 0\n" \
    "s_nop 0\n" \
    "s_nop 0\n" \
    "s_nop 0\n" \
    "s_nop 0\n" \
    "s_nop 0\n" \
    "s_nop 0\n" \
    "s_nop 0\n" \
    "s_nop 0\n" \
    "s_nop 0\n" \
    "s_nop 0\n" \
    "s_nop 0\n" \
    "s_nop 0\n" \
    "s_nop 0\n" \
    "s_nop 0\n" \
    "s_nop 0\n" \
    "s_nop 0\n" \
    "s_nop 0\n" \
    "s_nop 0\n" \
    "2:\n" \
    "buffer_load_dwordx4 v127, %[rs], s69 offen sc1 lds\n" \
    "s_mov_b32 m0, s73\n" \
    "s_nop 0\n" \
    "buffer_load_dwordx4 v127, %[rs], s70 offen sc1 lds\n" \
    "s_movk_i32 m0, 0x2000\n" \
    "s_mov_b32 exec_lo, s27\n" \
    "s_mov_b32 exec_hi, s27\n" \
    "buffer_load_dwordx4 v127, %[rs], s71 offen sc1 lds\n" \
    "s_mov_b64 exec, -1\n" \
    "v_bfe_u32 v0, v127, 8, 2\n" \
    "v_subrev_u32 v0, s60, v0\n" \
    "v_mul_i32_i24 v0, -1088, v0\n" \
    "v_add_u32 v0, v0, v129\n" \
    "v_lshrrev_b32 v1, 2, v127\n" \
    "s_add_i32 s80, s36, -2\n" \
    "v_mov_b32 v2, s80\n" \
    "s_mov_b64 exec, 1\n" \
    "buffer_store_dword v2, off, %[rw], s65 sc1\n" \
    "s_mov_b32 exec_lo, s66\n" \
    "s_mov_b32 exec_hi, s66\n" \
    "ds_read2_b32 v[96:97], v0 offset0:0 offset1:144\n" \
    "s_mov_b64 exec, -1\n" \
    "s_lshl_b32 s80, s36, 16\n" \
    "s_add_i32 s80, s80, s67\n" \
    "v_mov_b64 v[40:41], 0\n" \
    "v_mov_b64 v[42:43], 0\n" \
    "v_mov_b64 v[44:45], 0\n" \
    "v_mov_b64 v[46:47], 0\n" \
    "v_mov_b64 v[48:49], 0\n" \
    "v_mov_b64 v[50:51], 0\n" \
    "v_mov_b64 v[52:53], 0\n" \
    "v_mov_b64 v[54:55], 0\n" \
    "v_or_b32 v109, s60, v127\n" \
    "v_min_u32 v109, 1, v109\n" \
    "v_lshlrev_b32 v109, 30, v109\n" \
    "s_waitcnt lgkmcnt(0)\n" \
    "buffer_store_dword v96, v1, %[rw], s80 offen sc1\n" \
    "buffer_store_dword v97, v1, %[rw], s80 offen offset:256 sc1\n" \
    "3:\n" \
    "ds_read_b128 v[72:75], v133 offset:0\n" \
    "ds_read_b128 v[76:79], v133 offset:1024\n" \
    "ds_read_b128 v[80:83], v133 offset:2048\n" \
    "ds_read_b128 v[84:87], v133 offset:3072\n" \
    "ds_read_b128 v[88:91], v133 offset:49152\n" \
    "ds_read_b128 v[92:95], v133 offset:50176\n" \
    "s_add_i32 s91, s36, 1\n" \
    "s_and_b32 s78, s36, 3\n" \
    "s_lshl_b32 s78, s78, 14\n" \
    "s_add_i32 s77, s78, s61\n" \
    "s_bitcmp1_b32 s36, 0\n" \
    "s_cselect_b32 s79, 0x90000, 0\n" \
    "s_lshl_b32 s92, s91, 18\n" \
    "s_add_i32 s92, s92, s64\n" \
    "s_mov_b32 m0, s95\n" \
    "s_add_i32 s78, s78, 0x1a0000\n" \
    "s_add_i32 s93, s92, 0x1000000\n" \
    "v_mov_b64 v[56:57], 0\n" \
    "v_mov_b64 v[58:59], 0\n" \
    "v_mov_b64 v[60:61], 0\n" \
    "v_mov_b64 v[62:63], 0\n" \
    "v_mov_b64 v[64:65], 0\n" \
    "v_mov_b64 v[66:67], 0\n" \
    "v_mov_b64 v[68:69], 0\n" \
    "v_mov_b64 v[70:71], 0\n" \
    "v_mov_b64 v[98:99], 0\n" \
    "v_mov_b32 v101, 0\n" \
    "v_mov_b32 v106, 0\n" \
    "v_mov_b32 v103, s91\n" \
    "s_sub_i32 s76, s40, s24\n" \
    "s_min_i32 s94, s91, s38\n" \
    "s_lshl_b32 s94, s94, 18\n" \
    "s_andn2_b32 s80, 0x1000000, s66\n" \
    "s_add_i32 s94, s94, s64\n" \
    "s_add_i32 s94, s94, s80\n" \
    "v_lshlrev_b32 v107, 1, v127\n" \
    "s_waitcnt vmcnt(3) lgkmcnt(0)\n" \
    "s_barrier\n" \
    "ds_read_b128 v[0:3], v125 offset:0\n" \
    "s_memtime s[86:87]\n" \
    "v_mfma_f32_16x16x32_bf16 v[20:23], v[120:123], v[120:123], 0\n" \
    "ds_read_b128 v[4:7], v125 offset:1024\n" \
    "ds_read_b128 v[8:11], v125 offset:2048\n" \
    "v_mfma_f32_16x16x32_bf16 v[20:23], v[120:123], v[120:123], 0\n" \
    "ds_read_b128 v[12:15], v125 offset:3072\n" \
    "buffer_load_dword v110, v107, %[rw], s94 offen sc1\n" \
    "v_mfma_f32_16x16x32_bf16 v[20:23], v[120:123], v[120:123], 0\n" \
    "s_waitcnt lgkmcnt(3)\n" \
    "v_smfmac_f32_16x16x64_bf16 v[40:43], v[0:3], a[0:7], v124\n" \
    "ds_read_b128 v[16:19], v125 offset:4096\n" \
    "ds_read_b128 v[24:27], v125 offset:5120\n" \
    "v_smfmac_f32_16x16x64_bf16 v[44:47], v[0:3], a[8:15], v124\n" \
    "ds_read_b128 v[28:31], v125 offset:6144\n" \
    "ds_read_b128 v[32:35], v125 offset:7168\n" \
    "v_smfmac_f32_16x16x64_bf16 v[56:59], v[0:3], v[72:79], v124\n" \
    "ds_read_b128 v[36:39], v126 offset:0\n" \
    "v_smfmac_f32_16x16x64_bf16 v[60:63], v[0:3], v[80:87], v124\n" \
    "s_waitcnt lgkmcnt(7)\n" \
    "v_smfmac_f32_16x16x64_bf16 v[40:43], v[4:7], a[16:23], v124\n" \
    "ds_read_b128 v[72:75], v133 offset:16384\n" \
    "ds_read_b128 v[76:79], v133 offset:17408\n" \
    "v_max3_u32 v106, v106, v0, v1\n" \
    "v_smfmac_f32_16x16x64_bf16 v[44:47], v[4:7], a[24:31], v124\n" \
    "ds_read_b128 v[80:83], v133 offset:18432\n" \
    "ds_read_b128 v[84:87], v133 offset:19456\n" \
    "v_max3_u32 v106, v106, v2, v3\n" \
    "v_smfmac_f32_16x16x64_bf16 v[56:59], v[4:7], v[88:95], v124\n" \
    "v_max3_u32 v106, v106, v4, v5\n" \
    "v_smfmac_f32_16x16x64_bf16 v[60:63], v[4:7], v[168:175], v124\n" \
    "s_waitcnt lgkmcnt(4)\n" \
    "v_max3_u32 v106, v106, v6, v7\n" \
    "v_smfmac_f32_16x16x64_bf16 v[40:43], v[8:11], a[32:39], v124\n" \
    "ds_read_b128 v[88:91], v133 offset:32768\n" \
    "ds_read_b128 v[92:95], v133 offset:33792\n" \
    "v_max3_u32 v106, v106, v8, v9\n" \
    "v_smfmac_f32_16x16x64_bf16 v[44:47], v[8:11], a[40:47], v124\n" \
    "v_max3_u32 v106, v106, v10, v11\n" \
    "v_smfmac_f32_16x16x64_bf16 v[56:59], v[8:11], v[176:183], v124\n" \
    "v_max3_u32 v106, v106, v12, v13\n" \
    "v_smfmac_f32_16x16x64_bf16 v[60:63], v[8:11], v[184:191], v124\n" \
    "v_max3_u32 v106, v106, v14, v15\n" \
    "v_smfmac_f32_16x16x64_bf16 v[40:43], v[12:15], a[48:55], v124\n" \
    "v_max3_u32 v106, v106, v16, v17\n" \
    "v_smfmac_f32_16x16x64_bf16 v[44:47], v[12:15], a[56:63], v124\n" \
    "s_waitcnt lgkmcnt(0)\n" \
    "v_max3_u32 v106, v106, v18, v19\n" \
    "v_smfmac_f32_16x16x64_bf16 v[56:59], v[12:15], v[72:79], v124\n" \
    "s_add_i32 s80, s86, s76\n" \
    "s_mul_hi_u32 s80, s80, s41\n" \
    "v_max3_u32 v106, v106, v24, v25\n" \
    "v_smfmac_f32_16x16x64_bf16 v[40:43], v[16:19], a[64:71], v124\n" \
    "ds_read_b128 v[72:75], v133 offset:34816\n" \
    "ds_read_b128 v[76:79], v133 offset:35840\n" \
    "v_max3_u32 v106, v106, v26, v27\n" \
    "v_smfmac_f32_16x16x64_bf16 v[44:47], v[16:19], a[72:79], v124\n" \
    "s_add_i32 s80, s80, s29\n" \
    "v_max3_u32 v106, v106, v28, v29\n" \
    "v_smfmac_f32_16x16x64_bf16 v[40:43], v[24:27], a[80:87], v124\n" \
    "v_max3_u32 v106, v106, v30, v31\n" \
    "v_smfmac_f32_16x16x64_bf16 v[44:47], v[24:27], a[88:95], v124\n" \
    "v_max3_u32 v106, v106, v32, v33\n" \
    "v_smfmac_f32_16x16x64_bf16 v[40:43], v[28:31], a[96:103], v124\n" \
    "v_max3_u32 v106, v106, v34, v35\n" \
    "v_smfmac_f32_16x16x64_bf16 v[44:47], v[28:31], a[104:111], v124\n" \
    "ds_read_b128 v[20:23], v127 offset:19456\n" \
    "v_max3_u32 v106, v106, v36, v37\n" \
    "v_smfmac_f32_16x16x64_bf16 v[40:43], v[32:35], a[112:119], v124\n" \
    "v_max3_u32 v106, v106, v38, v39\n" \
    "v_smfmac_f32_16x16x64_bf16 v[44:47], v[32:35], a[120:127], v124\n" \
    "v_cmp_eq_u32 vcc, -1, v106\n" \
    "v_smfmac_f32_16x16x64_bf16 v[40:43], v[36:39], a[128:135], v124\n" \
    "v_add_u32 v106, s63, v133\n" \
    "v_smfmac_f32_16x16x64_bf16 v[44:47], v[36:39], a[136:143], v124\n" \
    "s_cbranch_vccnz 5f\n" \
    "6:\n" \
    "v_mov_b32 v102, s80\n" \
    "v_smfmac_f32_16x16x64_bf16 v[60:63], v[12:15], v[80:87], v124\n" \
    "s_waitcnt lgkmcnt(1)\n" \
    "v_add_f32 v40, v40, v42\n" \
    "v_smfmac_f32_16x16x64_bf16 v[56:59], v[16:19], v[88:95], v124\n" \
    "buffer_store_dwordx2 v[102:103], v109, %[rs], s77 offen sc1\n" \
    "v_add_f32 v41, v41, v43\n" \
    "v_smfmac_f32_16x16x64_bf16 v[60:63], v[16:19], v[72:79], v124\n" \
    "v_add_f32 v44, v44, v46\n" \
    "v_smfmac_f32_16x16x64_bf16 v[48:51], v[0:3], a[144:151], v124\n" \
    "v_add_f32 v45, v45, v47\n" \
    "v_smfmac_f32_16x16x64_bf16 v[52:55], v[0:3], a[152:159], v124\n" \
    "v_mul_f32 v40, s81, v40\n" \
    "v_smfmac_f32_16x16x64_bf16 v[48:51], v[4:7], a[160:167], v124\n" \
    "ds_read_b128 v[0:3], v127 offset:16384\n" \
    "v_mul_f32 v44, s81, v44\n" \
    "v_smfmac_f32_16x16x64_bf16 v[52:55], v[4:7], a[168:175], v124\n" \
    "v_mul_f32 v41, s81, v41\n" \
    "v_smfmac_f32_16x16x64_bf16 v[48:51], v[8:11], a[176:183], v124\n" \
    "ds_read_b128 v[4:7], v127 offset:18432\n" \
    "v_mul_f32 v45, s81, v45\n" \
    "v_smfmac_f32_16x16x64_bf16 v[52:55], v[8:11], a[184:191], v124\n" \
    "v_cvt_pk_bf16_f32 v107, v40, v44\n" \
    "v_smfmac_f32_16x16x64_bf16 v[48:51], v[12:15], a[192:199], v124\n" \
    "ds_read_b128 v[8:11], v127 offset:20480\n" \
    "v_cvt_pk_bf16_f32 v108, v41, v45\n" \
    "v_smfmac_f32_16x16x64_bf16 v[52:55], v[12:15], a[200:207], v124\n" \
    "ds_write2_b32 v128, v107, v108 offset0:0 offset1:80\n" \
    "v_smfmac_f32_16x16x64_bf16 v[48:51], v[16:19], a[208:215], v124\n" \
    "ds_read_b128 v[12:15], v127 offset:22528\n" \
    "ds_read_b128 v[40:43], v127 offset:21504\n" \
    "v_smfmac_f32_16x16x64_bf16 v[52:55], v[16:19], a[216:223], v124\n" \
    "ds_read_b128 v[44:47], v127 offset:23552\n" \
    "v_smfmac_f32_16x16x64_bf16 v[48:51], v[24:27], a[224:231], v124\n" \
    "ds_read_b128 v[16:19], v127 offset:17408\n" \
    "v_smfmac_f32_16x16x64_bf16 v[52:55], v[24:27], a[232:239], v124\n" \
    "s_waitcnt lgkmcnt(4)\n" \
    "s_barrier\n" \
    "ds_read_b128 v[72:75], v130 offset:0\n" \
    "v_smfmac_f32_16x16x64_bf16 v[48:51], v[28:31], a[240:247], v124\n" \
    "ds_read_b128 v[76:79], v130 offset:64\n" \
    "ds_read_b128 v[80:83], v130 offset:128\n" \
    "ds_read_b128 v[84:87], v130 offset:192\n" \
    "v_smfmac_f32_16x16x64_bf16 v[52:55], v[28:31], a[248:255], v124\n" \
    "v_smfmac_f32_16x16x64_bf16 v[48:51], v[32:35], v[136:143], v124\n" \
    "v_smfmac_f32_16x16x64_bf16 v[52:55], v[32:35], v[144:151], v124\n" \
    "v_smfmac_f32_16x16x64_bf16 v[48:51], v[36:39], v[152:159], v124\n" \
    "v_smfmac_f32_16x16x64_bf16 v[52:55], v[36:39], v[160:167], v124\n" \
    "s_waitcnt lgkmcnt(3)\n" \
    "v_mfma_f32_16x16x32_bf16 v[88:91], v[0:3], v[72:75], 0\n" \
    "s_waitcnt lgkmcnt(0)\n" \
    "v_add_f32 v48, v48, v50\n" \
    "v_add_f32 v49, v49, v51\n" \
    "v_mfma_f32_16x16x32_bf16 v[88:91], v[4:7], v[76:79], v[88:91]\n" \
    "v_add_f32 v52, v52, v54\n" \
    "v_add_f32 v53, v53, v55\n" \
    "v_mfma_f32_16x16x32_bf16 v[88:91], v[8:11], v[80:83], v[88:91]\n" \
    "v_cvt_pk_bf16_f32 v107, v48, v52\n" \
    "v_cvt_pk_bf16_f32 v108, v49, v53\n" \
    "ds_write2_b32 v129, v107, v108 offset0:0 offset1:144\n" \
    "v_mfma_f32_16x16x32_bf16 v[88:91], v[12:15], v[84:87], v[88:91]\n" \
    "ds_read_b64 v[0:1], v132 offset:0\n" \
    "ds_read_b64 v[2:3], v132 offset:1024\n" \
    "v_smfmac_f32_16x16x64_bf16 v[56:59], v[24:27], v[192:199], v124\n" \
    "ds_read_b64 v[4:5], v132 offset:512\n" \
    "ds_read_b64 v[6:7], v132 offset:1536\n" \
    "v_smfmac_f32_16x16x64_bf16 v[60:63], v[24:27], v[200:207], v124\n" \
    "v_mfma_f32_16x16x32_bf16 v[92:95], v[16:19], v[72:75], 0\n" \
    "v_exp_f32 v88, v88\n" \
    "s_waitcnt lgkmcnt(4)\n" \
    "s_barrier\n" \
    "ds_read_b128 v[48:51], v131 offset:0\n" \
    "ds_read_b128 v[52:55], v131 offset:128\n" \
    "v_mfma_f32_16x16x32_bf16 v[92:95], v[20:23], v[76:79], v[92:95]\n" \
    "ds_read_b128 v[8:11], v106 offset:0\n" \
    "v_exp_f32 v89, v89\n" \
    "v_mfma_f32_16x16x32_bf16 v[92:95], v[40:43], v[80:83], v[92:95]\n" \
    "ds_read_b128 v[12:15], v106 offset:1024\n" \
    "v_exp_f32 v90, v90\n" \
    "v_mfma_f32_16x16x32_bf16 v[92:95], v[44:47], v[84:87], v[92:95]\n" \
    "ds_read_b128 v[16:19], v106 offset:32768\n" \
    "v_exp_f32 v91, v91\n" \
    "v_smfmac_f32_16x16x64_bf16 v[56:59], v[28:31], v[208:215], v124\n" \
    "ds_read_b128 v[20:23], v106 offset:33792\n" \
    "s_waitcnt lgkmcnt(6)\n" \
    "v_cvt_pk_bf16_f32 v88, v88, v89\n" \
    "v_smfmac_f32_16x16x64_bf16 v[60:63], v[28:31], v[216:223], v124\n" \
    "ds_read_b128 v[40:43], v106 offset:2048\n" \
    "ds_read_b128 v[44:47], v106 offset:3072\n" \
    "v_cvt_pk_bf16_f32 v89, v90, v91\n" \
    "s_waitcnt lgkmcnt(6)\n" \
    "v_smfmac_f32_16x16x64_bf16 v[64:67], v[48:51], v[72:79], v124\n" \
    "ds_read_b128 v[24:27], v106 offset:6144\n" \
    "ds_read_b128 v[28:31], v106 offset:7168\n" \
    "v_exp_f32 v92, v92\n" \
    "v_smfmac_f32_16x16x64_bf16 v[64:67], v[52:55], v[80:87], v124\n" \
    "ds_read_b128 v[72:75], v106 offset:34816\n" \
    "ds_read_b128 v[76:79], v106 offset:35840\n" \
    "v_exp_f32 v93, v93\n" \
    "v_smfmac_f32_16x16x64_bf16 v[56:59], v[32:35], v[224:231], v124\n" \
    "ds_read_b128 v[80:83], v106 offset:4096\n" \
    "ds_read_b128 v[84:87], v106 offset:5120\n" \
    "v_exp_f32 v94, v94\n" \
    "v_smfmac_f32_16x16x64_bf16 v[60:63], v[32:35], v[232:239], v124\n" \
    "s_waitcnt lgkmcnt(6)\n" \
    "s_cmp_eq_u32 s36, 63\n" \
    "v_add_f32 v64, v64, v66\n" \
    "v_add_f32 v65, v65, v67\n" \
    "v_smfmac_f32_16x16x64_bf16 v[56:59], v[36:39], v[240:247], v124\n" \
    "ds_read_b128 v[48:51], v106 offset:36864\n" \
    "ds_read_b128 v[52:55], v106 offset:37888\n" \
    "v_exp_f32 v95, v95\n" \
    "v_cvt_pk_bf16_f32 v90, v92, v93\n" \
    "v_smfmac_f32_16x16x64_bf16 v[60:63], v[36:39], v[248:255], v124\n" \
    "ds_read_b128 v[32:35], v106 offset:38912\n" \
    "ds_read_b128 v[36:39], v106 offset:39936\n" \
    "v_cvt_pk_bf16_f32 v91, v94, v95\n" \
    "v_exp_f32 v64, v64\n" \
    "v_exp_f32 v65, v65\n" \
    "v_mfma_f32_16x16x32_bf16 v[102:105], v[88:91], v[120:123], 0\n" \
    "v_swap_b32 v1, v2\n" \
    "v_swap_b32 v5, v6\n" \
    "v_cvt_pk_bf16_f32 v100, v64, v65\n" \
    "v_add_f32 v56, v56, v58\n" \
    "v_smfmac_f32_16x16x64_bf16 v[68:71], v[88:91], v[0:7], v124\n" \
    "ds_read_b128 v[0:3], v133 offset:51200\n" \
    "ds_read_b128 v[4:7], v133 offset:52224\n" \
    "v_add_f32 v57, v57, v59\n" \
    "v_add_f32 v60, v60, v62\n" \
    "v_add_f32 v61, v61, v63\n" \
    "v_cvt_pk_bf16_f32 v96, v56, v57\n" \
    "v_mfma_f32_16x16x16_bf16 v[102:105], v[100:101], v[120:121], v[102:105]\n" \
    "v_cvt_pk_bf16_f32 v97, v60, v61\n" \
    "s_cbranch_scc1 8f\n" \
    "s_nop 0\n" \
    "v_smfmac_f32_16x16x32_bf16 v[68:71], v[100:101], v[96:99], v124\n" \
    "v_mov_b64 v[56:57], 0\n" \
    "v_mov_b64 v[58:59], 0\n" \
    "v_rcp_f32 v104, v102\n" \
    "v_rcp_f32 v105, v103\n" \
    "v_mul_f32 v68, v68, v104\n" \
    "v_mul_f32 v70, v70, v104\n" \
    "v_mul_f32 v69, v69, v105\n" \
    "v_mul_f32 v71, v71, v105\n" \
    "v_cvt_pk_bf16_f32 v107, v68, v70\n" \
    "v_cvt_pk_bf16_f32 v108, v69, v71\n" \
    "ds_write2_b32 v129, v107, v108 offset0:64 offset1:208\n" \
    "v_mfma_f32_16x16x32_bf16 v[60:63], v[56:59], v[120:123], 0\n" \
    "v_max3_f32 v102, v102, v103, v104\n" \
    "v_max_f32 v102, v102, v105\n" \
    "v_mfma_f32_16x16x32_bf16 v[88:91], v[56:59], v[120:123], 0\n" \
    "v_cmp_nge_f32 vcc, s82, v102\n" \
    "v_add_u32 v100, s83, v131\n" \
    "v_mfma_f32_16x16x32_bf16 v[92:95], v[56:59], v[120:123], 0\n" \
    "v_sub_u32 v101, v131, s83\n" \
    "v_xor_b32 v108, 0x2000, v133\n" \
    "v_mfma_f32_16x16x32_bf16 v[64:67], v[56:59], v[120:123], 0\n" \
    "v_and_b32 v107, 0x3c0, v127\n" \
    "v_add_u32 v107, v107, v127\n" \
    "s_cbranch_vccnz 7f\n" \
    "s_waitcnt lgkmcnt(0)\n" \
    "s_barrier\n" \
    "ds_read_b128 v[68:71], v100 offset:256\n" \
    "ds_read_b128 v[102:105], v101 offset:384\n" \
    "v_mfma_f32_16x16x32_bf16 v[64:67], v[56:59], v[120:123], 0\n" \
    "buffer_load_dwordx2 v[98:99], v107, %[rs], s78 offen sc1\n" \
    "v_mfma_f32_16x16x32_bf16 v[64:67], v[56:59], v[120:123], 0\n" \
    "buffer_load_dword v100, off, %[rw], s62 sc1\n" \
    "v_mfma_f32_16x16x32_bf16 v[64:67], v[56:59], v[120:123], 0\n" \
    "s_waitcnt lgkmcnt(1)\n" \
    "v_smfmac_f32_16x16x64_bf16 v[56:59], v[68:71], v[8:15], v124\n" \
    "s_waitcnt lgkmcnt(0)\n" \
    "v_smfmac_f32_16x16x64_bf16 v[56:59], v[102:105], v[16:23], v124\n" \
    "v_smfmac_f32_16x16x64_bf16 v[60:63], v[68:71], v[40:47], v124\n" \
    "ds_read_b128 v[16:19], v108 offset:51200\n" \
    "ds_read_b128 v[20:23], v108 offset:52224\n" \
    "v_smfmac_f32_16x16x64_bf16 v[60:63], v[102:105], v[72:79], v124\n" \
    "v_add_f32 v56, v56, v58\n" \
    "v_smfmac_f32_16x16x64_bf16 v[88:91], v[68:71], v[80:87], v124\n" \
    "v_add_f32 v57, v57, v59\n" \
    "v_smfmac_f32_16x16x64_bf16 v[88:91], v[102:105], v[48:55], v124\n" \
    "v_add_f32 v60, v60, v62\n" \
    "v_smfmac_f32_16x16x64_bf16 v[92:95], v[68:71], v[24:31], v124\n" \
    "v_add_f32 v61, v61, v63\n" \
    "v_smfmac_f32_16x16x64_bf16 v[92:95], v[102:105], v[32:39], v124\n" \
    "v_add_f32 v88, v88, v90\n" \
    "v_smfmac_f32_16x16x64_bf16 v[64:67], v[68:71], v[0:7], v124\n" \
    "s_waitcnt lgkmcnt(0)\n" \
    "v_add_f32 v89, v89, v91\n" \
    "v_smfmac_f32_16x16x64_bf16 v[64:67], v[102:105], v[16:23], v124\n" \
    "v_add_f32 v92, v92, v94\n" \
    "v_add_f32 v93, v93, v95\n" \
    "v_cvt_pk_bf16_f32 v8, v56, v60\n" \
    "v_cvt_pk_bf16_f32 v10, v57, v61\n" \
    "v_cvt_pk_bf16_f32 v9, v88, v92\n" \
    "v_cvt_pk_bf16_f32 v11, v89, v93\n" \
    "buffer_store_dwordx4 v[8:11], v134, %[rs], s79 offen sc1\n" \
    "v_add_f32 v64, v64, v66\n" \
    "v_add_f32 v65, v65, v67\n" \
    "v_cvt_pk_bf16_f32 v64, v64, v65\n" \
    "buffer_store_dword v64, v135, %[rs], s79 offen sc1\n" \
    "s_memtime s[88:89]\n" \
    "s_memrealtime s[44:45]\n" \
    "s_and_b32 s75, s91, 3\n" \
    "s_lshl_b32 s75, s75, 17\n" \
    "s_memrealtime s[46:47]\n" \
    "s_add_i32 s75, s75, s26\n" \
    "s_add_i32 s69, s75, s72\n" \
    "s_memrealtime s[48:49]\n" \
    "s_add_i32 s70, s69, 4096\n" \
    "s_add_i32 s71, s75, 16384\n" \
    "s_memrealtime s[50:51]\n" \
    "s_sub_i32 s76, s33, s42\n" \
    "s_ashr_i32 s76, s76, s32\n" \
    "s_memrealtime s[52:53]\n" \
    "s_add_i32 s42, s42, s76\n" \
    "s_add_i32 s40, s40, 64\n" \
    "s_memrealtime s[54:55]\n" \
    "s_nop 1\n" \
    "s_memrealtime s[56:57]\n" \
    "s_nop 1\n" \
    "s_memrealtime vcc\n" \
    "v_and_b32 v18, 0x300, v127\n" \
    "v_add_u32 v18, v18, v127\n" \
    "v_and_b32 v19, 0x380, v127\n" \
    "v_add_u32 v19, v19, v127\n" \
    "s_waitcnt lgkmcnt(0)\n" \
    "s_add_i32 s44, s44, s46\n" \
    "s_add_i32 s48, s48, s50\n" \
    "s_add_i32 s52, s52, s54\n" \
    "s_add_i32 s44, s44, s48\n" \
    "s_add_i32 s52, s52, s56\n" \
    "s_add_i32 s44, s44, s52\n" \
    "s_add_i32 s80, s44, vcc_lo\n" \
    "s_sub_i32 s76, s80, s29\n" \
    "s_mov_b32 s29, s80\n" \
    "s_sub_i32 s80, s88, s24\n" \
    "s_mov_b32 s24, s88\n" \
    "s_mul_i32 s75, s76, s25\n" \
    "s_lshr_b32 s75, s75, 12\n" \
    "s_sub_i32 s75, s80, s75\n" \
    "s_mul_hi_u32 s80, s80, s41\n" \
    "s_sub_i32 s80, s76, s80\n" \
    "s_lshl_b32 s80, s80, 18\n" \
    "s_cmp_lt_u32 s76, 0x1000\n" \
    "s_cselect_b32 s75, s75, 0\n" \
    "s_cselect_b32 s80, s80, 0\n" \
    "s_add_i32 s25, s25, s75\n" \
    "s_add_i32 s41, s41, s80\n" \
    "s_cmp_eq_u32 s36, 1\n" \
    "s_cselect_b32 s28, s29, s28\n" \
    "s_sub_i32 s80, s88, s86\n" \
    "s_min_u32 s40, s40, s80\n" \
    "s_waitcnt vmcnt(2)\n" \
    "v_readfirstlane_b32 s80, v100\n" \
    "v_cmp_ne_u32 vcc, s91, v99\n" \
    "s_mov_b32 s43, 0\n" \
    "s_max_i32 s38, s38, s80\n" \
    "s_cmp_lt_i32 s38, s91\n" \
    "s_cselect_b64 exec, 0, -1\n" \
    "s_cselect_b32 s94, 8, 0\n" \
    "buffer_load_dwordx4 v19, %[rw], s92 offen sc1 lds\n" \
    "s_add_i32 m0, s95, 896\n" \
    "s_nop 0\n" \
    "buffer_load_dwordx4 v19, %[rw], s92 offen offset:128 sc1 lds\n" \
    "s_add_i32 m0, s95, 8192\n" \
    "s_nop 0\n" \
    "buffer_load_dwordx4 v18, %[rw], s93 offen sc1 lds\n" \
    "s_add_i32 m0, s95, 8960\n" \
    "s_nop 0\n" \
    "buffer_load_dwordx4 v18, %[rw], s93 offen offset:256 sc1 lds\n" \
    "s_mov_b32 m0, s72\n" \
    "s_mov_b64 exec, -1\n" \
    "s_cbranch_vccnz 15f\n" \
    "10:\n" \
    "v_subrev_u32 v28, s29, v98\n" \
    "v_min_i32 v28, 0x4000, v28\n" \
    "v_max_i32 v28, 0xffffc000, v28\n" \
    "s_nop 1\n" \
    "v_max_i32_dpp v28, v28, v28 row_shr:1 row_mask:0xf bank_mask:0xf\n" \
    "s_nop 1\n" \
    "v_max_i32_dpp v28, v28, v28 row_shr:2 row_mask:0xf bank_mask:0xf\n" \
    "s_nop 1\n" \
    "v_max_i32_dpp v28, v28, v28 row_shr:4 row_mask:0xf bank_mask:0xf\n" \
    "s_nop 1\n" \
    "v_max_i32_dpp v28, v28, v28 row_shr:8 row_mask:0xf bank_mask:0xf\n" \
    "s_add_i32 s76, s29, s42\n" \
    "s_nop 0\n" \
    "v_max_i32_dpp v28, v28, v28 row_bcast:15 row_mask:0xa bank_mask:0xf\n" \
    "s_nop 1\n" \
    "v_max_i32_dpp v28, v28, v28 row_bcast:31 row_mask:0xc bank_mask:0xf\n" \
    "s_nop 0\n" \
    "v_readlane_b32 s37, v28, 63\n" \
    "s_nop 0\n" \
    "s_add_i32 s37, s37, s76\n" \
    "s_cmp_lg_u32 s94, 0\n" \
    "s_cbranch_scc1 9f\n" \
    "s_mov_b32 s36, s91\n" \
    "s_branch 12b\n" \
    "5:\n" \
    "s_cmp_eq_u32 s36, 0\n" \
    "s_cbranch_scc1 6b\n" \
    "s_add_i32 s39, s39, -1\n" \
    "s_cmp_lt_i32 s39, 0\n" \
    "s_cbranch_scc1 6b\n" \
    "s_add_i32 s30, s30, 1\n" \
    "s_and_b32 s80, s31, 0xffff\n" \
    "s_add_i32 s42, s42, s80\n" \
    "s_lshr_b32 s80, s31, 16\n" \
    "s_cmp_lt_u32 s36, 4\n" \
    "s_cselect_b32 s80, 0, s80\n" \
    "s_add_i32 s33, s33, s80\n" \
    "s_min_u32 s80, s80, 1\n" \
    "s_lshl_b32 s80, s80, 16\n" \
    "s_sub_i32 s31, s31, s80\n" \
    "s_waitcnt lgkmcnt(0)\n" \
    "s_mov_b32 s37, s29\n" \
    "s_nop 7\n" \
    "s_barrier\n" \
    "s_branch 0b\n" \
    "15:\n" \
    "s_add_i32 s30, s30, 0x10000\n" \
    "s_add_i32 s39, s39, -1\n" \
    "s_cmp_lt_i32 s39, 0\n" \
    "s_cbranch_scc1 10b\n" \
    "s_sleep 9\n" \
    "buffer_load_dwordx2 v[98:99], v107, %[rs], s78 offen sc1\n" \
    "s_waitcnt vmcnt(0)\n" \
    "v_cmp_ne_u32 vcc, s91, v99\n" \
    "s_nop 1\n" \
    "s_cbranch_vccnz 15b\n" \
    "s_memtime s[88:89]\n" \
    "s_memrealtime s[44:45]\n" \
    "s_nop 1\n" \
    "s_memrealtime s[46:47]\n" \
    "s_nop 1\n" \
    "s_memrealtime s[48:49]\n" \
    "s_nop 1\n" \
    "s_memrealtime s[50:51]\n" \
    "s_nop 1\n" \
    "s_memrealtime s[52:53]\n" \
    "s_nop 1\n" \
    "s_memrealtime s[54:55]\n" \
    "s_nop 1\n" \
    "s_memrealtime s[56:57]\n" \
    "s_nop 1\n" \
    "s_memrealtime vcc\n" \
    "s_waitcnt lgkmcnt(0)\n" \
    "s_add_i32 s44, s44, s46\n" \
    "s_add_i32 s48, s48, s50\n" \
    "s_add_i32 s52, s52, s54\n" \
    "s_add_i32 s44, s44, s48\n" \
    "s_add_i32 s52, s52, s56\n" \
    "s_add_i32 s44, s44, s52\n" \
    "s_add_i32 s29, s44, vcc_lo\n" \
    "s_mov_b32 s24, s88\n" \
    "s_branch 10b\n" \
    "7:\n" \
    "s_mov_b32 s43, 2\n" \
    "8:\n" \
    "s_nop 7\n" \
    "s_waitcnt vmcnt(0)\n" \
    "9:\n" \
    "s_mov_b32 m0, s68\n" \
    "s_waitcnt lgkmcnt(0)\n" \
    "v_mov_b32 v0, s36\n" \
    "v_mov_b32 v1, s37\n" \
    "v_mov_b32 v2, s38\n" \
    "v_mov_b32 v3, s39\n" \
    "v_mov_b32 v4, s40\n" \
    "v_mov_b32 v5, s41\n" \
    "v_mov_b32 v6, s42\n" \
    "v_mov_b32 v7, s43\n" \
    "v_mov_b32 v8, s28\n" \
    "v_mov_b32 v9, s29\n" \
    "v_mov_b32 v10, s30\n" \
    "v_mov_b32 v11, s31\n" \
    "v_mov_b32 v12, s32\n" \
    "v_mov_b32 v13, s33\n" \
    "v_mov_b32 v14, s34\n" \
    "v_mov_b32 v15, s35\n" \
    "v_mov_b32 v16, s24\n" \
    "v_mov_b32 v17, s25\n" \
    "v_mov_b32 v18, s26\n" \
    "v_mov_b32 v19, s27\n" \
    "v_mov_b32 v20, s84\n" \
    "v_mov_b32 v21, s86\n" \
    "v_mov_b32 v22, s88\n" \
    "v_mov_b32 v23, s84\n" \
    "v_mov_b32 v24, s60\n" \
    "v_mov_b32 v25, s61\n" \
    "v_mov_b32 v26, s62\n" \
    "v_mov_b32 v27, s63\n" \
    "v_mov_b32 v28, s64\n" \
    "v_mov_b32 v29, s65\n" \
    "v_mov_b32 v30, s66\n" \
    "v_mov_b32 v31, s67\n" \
    ".Lstep_end:\n" \
    ""
static_assert(2 * PART_BYTES == 589824 && 2 * OFF_CAN == 1703936 && OFF_PART == 0 && OFF_VF - OFF_KF == 2048 * 4096 * 2, "constants of the step block");
#define STEP_CLOBBER "v32", "v33", "v34", "v35", "v36", "v37", "v38", "v39", "v40", "v41", "v42", "v43", "v44", "v45", "v46", "v47", "v48", "v49", "v50", "v51", "v52", "v53", "v54", "v55", "v56", "v57", "v58", "v59", "v60", "v61", "v62", "v63", "v64", "v65", "v66", "v67", "v68", "v69", "v70", "v71", "v72", "v73", "v74", "v75", "v76", "v77", "v78", "v79", "v80", "v81", "v82", "v83", "v84", "v85", "v86", "v87", "v88", "v89", "v90", "v91", "v92", "v93", "v94", "v95", "v98", "v99", "v100", "v101", "v102", "v103", "v104", "v105", "v106", "v107", "v108", "v109", "v110", "s24", "s25", "s26", "s27", "s28", "s29", "s30", "s31", "s32", "s33", "s34", "s35", "s36", "s37", "s38", "s39", "s40", "s41", "s42", "s43", "s44", "s45", "s46", "s47", "s48", "s49", "s50", "s51", "s52", "s53", "s54", "s55", "s56", "s57", "s58", "s59", "s60", "s61", "s62", "s63", "s64", "s65", "s66", "s67", "s68", "s69", "s70", "s71", "s72", "s73", "s74", "s75", "s76", "s77", "s78", "s79", "s80", "s81", "s82", "s83", "s84", "s85", "s86", "s87", "s88", "s89", "s90", "s91", "s92", "s93", "s94", "s95", "s96", "s97", "vcc", "scc", "memory"
constexpr int NREP = 2;
#define OWNER_ASM \
    "s_getpc_b64 s[96:97]\n" \
    "14:\n" \
    "s_add_u32 s96, s96, 2f-14b\n" \
    "s_addc_u32 s97, s97, 0\n" \
    "s_mov_b32 %[np], 0\n" \
    "6:\n" \
    "buffer_load_dwordx2 v[0:1], %[ca], %[rs], %[cs] offen sc1\n" \
    "s_memtime s[58:59]\n" \
    "s_memrealtime s[44:45]\n" \
    "s_nop 1\n" \
    "s_memrealtime s[46:47]\n" \
    "s_nop 1\n" \
    "s_memrealtime s[48:49]\n" \
    "s_nop 1\n" \
    "s_memrealtime s[50:51]\n" \
    "s_nop 1\n" \
    "s_memrealtime s[52:53]\n" \
    "s_nop 1\n" \
    "s_memrealtime s[54:55]\n" \
    "s_nop 1\n" \
    "s_memrealtime s[56:57]\n" \
    "s_nop 1\n" \
    "s_memrealtime vcc\n" \
    "s_waitcnt lgkmcnt(0)\n" \
    "s_add_i32 s44, s44, s46\n" \
    "s_add_i32 s48, s48, s50\n" \
    "s_add_i32 s52, s52, s54\n" \
    "s_add_i32 s44, s44, s48\n" \
    "s_add_i32 s52, s52, s56\n" \
    "s_add_i32 s44, s44, s52\n" \
    "s_add_i32 s59, s44, vcc_lo\n" \
    "s_sub_i32 s44, s59, %[oT]\n" \
    "s_mov_b32 %[oT], s59\n" \
    "s_sub_i32 s45, s58, %[oC]\n" \
    "s_mov_b32 %[oC], s58\n" \
    "s_mul_i32 s46, s44, %[oR]\n" \
    "s_lshr_b32 s46, s46, 12\n" \
    "s_sub_i32 s46, s45, s46\n" \
    "s_cmp_lt_u32 s44, 0x1000\n" \
    "s_cselect_b32 s46, s46, 0\n" \
    "s_add_i32 %[oR], %[oR], s46\n" \
    "s_waitcnt vmcnt(0)\n" \
    "v_subrev_u32 v0, %[oT], v0\n" \
    "v_cmp_ne_u32 vcc, %[tag], v1\n" \
    "v_min_i32 v0, 0x4000, v0\n" \
    "v_max_i32 v0, 0xffffc000, v0\n" \
    "s_nop 0\n" \
    "s_cbranch_vccnz 7f\n" \
    "8:\n" \
    "v_max_i32_dpp v0, v0, v0 row_shr:1 row_mask:0xf bank_mask:0xf\n" \
    "s_nop 1\n" \
    "v_max_i32_dpp v0, v0, v0 row_shr:2 row_mask:0xf bank_mask:0xf\n" \
    "s_nop 1\n" \
    "v_max_i32_dpp v0, v0, v0 row_shr:4 row_mask:0xf bank_mask:0xf\n" \
    "s_nop 1\n" \
    "v_max_i32_dpp v0, v0, v0 row_shr:8 row_mask:0xf bank_mask:0xf\n" \
    "s_nop 1\n" \
    "v_max_i32_dpp v0, v0, v0 row_bcast:15 row_mask:0xa bank_mask:0xf\n" \
    "s_nop 1\n" \
    "v_max_i32_dpp v0, v0, v0 row_bcast:31 row_mask:0xc bank_mask:0xf\n" \
    "s_nop 1\n" \
    "v_readlane_b32 s44, v0, 63\n" \
    "s_add_i32 s45, %[oC], -116\n" \
    "s_add_i32 %[tm], s44, %[oT]\n" \
    "s_add_i32 s44, s44, %[c8]\n" \
    "s_mul_i32 s44, s44, %[oR]\n" \
    "s_ashr_i32 s44, s44, 12\n" \
    "s_add_i32 s44, s44, s45\n" \
    "1:\n" \
    "s_memtime vcc\n" \
    "s_waitcnt lgkmcnt(0)\n" \
    "s_sub_i32 s58, s44, vcc_lo\n" \
    "s_cmp_gt_i32 s58, 112\n" \
    "s_cbranch_scc1 1b\n" \
    "s_max_i32 s58, s58, 0\n" \
    "s_and_b32 s58, s58, -4\n" \
    "s_sub_u32 vcc_lo, s96, s58\n" \
    "s_subb_u32 vcc_hi, s97, 0\n" \
    "s_setpc_b64 vcc\n" \
    "s_nop 0\n" \
    "s_nop 0\n" \
    "s_nop 0\n" \
    "s_nop 0\n" \
    "s_nop 0\n" \
    "s_nop 0\n" \
    "s_nop 0\n" \
    "s_nop 0\n" \
    "s_nop 0\n" \
    "s_nop 0\n" \
    "s_nop 0\n" \
    "s_nop 0\n" \
    "s_nop 0\n" \
    "s_nop 0\n" \
    "s_nop 0\n" \
    "s_nop 0\n" \
    "s_nop 0\n" \
    "s_nop 0\n" \
    "s_nop 0\n" \
    "s_nop 0\n" \
    "s_nop 0\n" \
    "s_nop 0\n" \
    "s_nop 0\n" \
    "s_nop 0\n" \
    "s_nop 0\n" \
    "s_nop 0\n" \
    "s_nop 0\n" \
    "s_nop 0\n" \
    "2:\n" \
    "buffer_load_dwordx4 v[0:3], %[o], %[rs], %[pb] offen offset:0 sc1\n" \
    "buffer_load_dwordx4 v[4:7], %[o], %[rs], %[pb] offen offset:1024 sc1\n" \
    "buffer_load_dwordx4 v[8:11], %[o], %[rs], %[pb] offen offset:2048 sc1\n" \
    "buffer_load_dwordx4 v[12:15], %[o], %[rs], %[pb] offen offset:3072 sc1\n" \
    "s_add_i32 %[np], %[np], 1\n" \
    "s_waitcnt vmcnt(3)\n" \
    "v_mfma_f32_16x16x32_bf16 v[16:19], %[sel], v[0:3], 0\n" \
    "v_max3_u32 v20, v0, v1, v1\n" \
    "v_max3_u32 v20, v2, v3, v20\n" \
    "s_waitcnt vmcnt(2)\n" \
    "v_mfma_f32_16x16x32_bf16 v[16:19], %[sel], v[4:7], v[16:19]\n" \
    "v_max3_u32 v20, v4, v5, v20\n" \
    "v_max3_u32 v20, v6, v7, v20\n" \
    "s_waitcnt vmcnt(1)\n" \
    "v_mfma_f32_16x16x32_bf16 v[16:19], %[sel], v[8:11], v[16:19]\n" \
    "v_max3_u32 v20, v8, v9, v20\n" \
    "v_max3_u32 v20, v10, v11, v20\n" \
    "s_waitcnt vmcnt(0)\n" \
    "v_mfma_f32_16x16x32_bf16 v[16:19], %[sel], v[12:15], v[16:19]\n" \
    "v_max3_u32 v20, v12, v13, v20\n" \
    "v_max3_u32 v20, v14, v15, v20\n" \
    "v_cmp_eq_u32 vcc, -1, v20\n" \
    "s_nop 3\n" \
    "s_cbranch_vccnz 4f\n" \
    "3:\n" \
    "v_add_f32_dpp v16, v16, v16 row_ror:8 row_mask:0xf bank_mask:0xf\n" \
    "v_add_f32_dpp v17, v17, v17 row_ror:8 row_mask:0xf bank_mask:0xf\n" \
    "v_cvt_pk_bf16_f32 v16, v16, v17\n" \
    "buffer_store_dword v16, %[so], %[rs], %[mb0] offen sc1\n" \
    "s_branch 5f\n" \
    "4:\n" \
    "s_add_i32 %[bud], %[bud], -1\n" \
    "s_cmp_lt_i32 %[bud], 0\n" \
    "s_cbranch_scc0 2b\n" \
    "s_branch 3b\n" \
    "7:\n" \
    "s_cmp_eq_u32 %[tag], 1\n" \
    "s_cbranch_scc1 9f\n" \
    "s_sleep 13\n" \
    "9:\n" \
    "s_sleep 6\n" \
    "s_add_i32 %[bud], %[bud], -1\n" \
    "s_cmp_lt_i32 %[bud], 0\n" \
    "s_cbranch_scc0 6b\n" \
    "s_branch 8b\n" \
    "5:\n" \
    ""
#define OWNER_CLOBBER "v0", "v1", "v2", "v3", "v4", "v5", "v6", "v7", "v8", "v9", "v10", "v11", "v12", "v13", "v14", "v15", "v16", "v17", "v18", "v19", "v20", "s44", "s45", "s46", "s47", "s48", "s49", "s50", "s51", "s52", "s53", "s54", "s55", "s56", "s57", "s58", "s59", "s96", "s97", "vcc", "scc", "memory"
constexpr int L_MEMS = 0;
constexpr int L_Q = 9216;
constexpr int L_KO = L_Q + 2560;
constexpr int L_KX = 16384;
constexpr int L_W = 32768;
constexpr int C_RING = 8;
static_assert(S_TOTAL <= L_SINK && L_W + C_RING * 16384 <= L_TOTAL && L_KO + 8 * 576 <= L_KX, "LDS budget");
static_assert(P_STG + P_RING * 16384 <= L_TOTAL, "LDS budget");

DI int pos2col(int p) { return p < 512 ? (p & ~63) + 16 * (p & 3) + ((p & 63) >> 2) : p; }
DI void unit_rc(int u, int& i, int& p0) {
    if (u < 512) { const int G = u >> 3, r = u & 7; i = 2 * (G & 3) + (r >> 2); p0 = 32 * (G >> 2) + 8 * (r & 3); }
    else { const int v = u - 512; i = (v >> 1) & 7; p0 = 512 + 16 * (v >> 4) + 8 * (v & 1); }
}
DI void mem0_arrange(LAS char* lds, int raw, int tid) {
#pragma unroll
    for (int k = 0; k < 2; ++k) {
        int i, p0; unit_rc(tid + 256 * k, i, p0);
        unsigned r[4];
#pragma unroll
        for (int j = 0; j < 4; ++j) r[j] = *(LAS unsigned*)(lds + raw + (i * DM + pos2col(p0)) * 2 + 32 * j);
        *(LAS u32x4*)(lds + L_MEMS + (tid + 256 * k) * 16) = (u32x4){__builtin_amdgcn_perm(r[1], r[0], 0x05040100), __builtin_amdgcn_perm(r[3], r[2], 0x05040100),
                                                                     __builtin_amdgcn_perm(r[1], r[0], 0x07060302), __builtin_amdgcn_perm(r[3], r[2], 0x07060302)};
    }
    int i, p0; unit_rc(512 + (tid & 63), i, p0);
    *(LAS u32x4*)(lds + L_MEMS + 8192 + (tid & 63) * 16) = *(LAS u32x4*)(lds + raw + (i * DM + p0) * 2);
}
DI unsigned read_canaries(int s, rsrc_t rs, int lane, int& budget) {
    const int o = 2 * OFF_CAN + (s & 3) * 16384 + (lane >> 2) * 128 + (lane & 3) * 16;
    u32x2 v;
    for (;;) {
        v = ld64(rs, o, 0);
        asm volatile("" ::: "memory");
        if (__all(v.y == (unsigned)(s + 1))) break;
        if (--budget < 0) break;
    }
    const unsigned t = 8 * now();
    return t + (unsigned)wave_max32(min(max((int)(v.x - t), -0x4000), 0x4000));
}
DI u32x8 sm_frag(LAS char* lds, int sl, int r0, int sa, int sH, int cb, int lo8) {
    auto rd = [&](int r) { return tr_read(lds, sl + r * 256 + ((cb ^ (2 * swz(r))) * 16) + lo8); };
    const u32x2 a = rd(r0), b = rd(r0 + sa), c = rd(r0 + sH), d = rd(r0 + sa + sH);
    return (u32x8){a.x, a.y, b.x, b.y, c.x, c.y, d.x, d.y};
}
#define GS(slot, i)
#define GV(slot, i, v)
DI void owner_loop(const Args& a, int G, rsrc_t rs, unsigned tbase, unsigned cbase, int& budget) {
    const int lane = threadIdx.x & 63, n = lane & 15, g = lane >> 4;
    const bool ex = G >= 64;
    const u32x4 sent4 = {SENT, SENT, SENT, SENT};
    const int js = (n & 2) ? 8 : ex ? (n & 4) + 2 * (n & 1) + (n >> 3) : 2 * (n >> 2) + (n & 1);
    u32x4 selu;
#pragma unroll
    for (int j = 0; j < 4; ++j) selu[j] = (js == 2 * j ? 0x3F80u : 0u) | (js == 2 * j + 1 ? 0x3F800000u : 0u);
    const int o = cmap(G * 4096 + lane * 16);
    const int so = (n < 8 || NREP > 1) ? cmap((n >> 3) * 16384 + G * 128 + (g & 1) * 4 + (ex ? (n & 4) * 16 + (g >> 1) * 32 + (n & 3) * 8 : (g >> 1) * 64 + (n & 7) * 8)) : OOB;
    unsigned fails = 0;
    const int c8 = a.c1;
    const bool gv_on = true; (void)gv_on;
    unsigned oT = now(), oC = (unsigned)__builtin_readcyclecounter(), oR;
    { const unsigned r = (unsigned)(512.0f * (float)(oC - cbase) * __builtin_amdgcn_rcpf((float)(oT - tbase))); asm volatile("s_nop 0\n\tv_readfirstlane_b32 %0, %1" : "=s"(oR) : "v"(r)); }
    oT = 8 * oT + 16;
    budget = __builtin_amdgcn_readfirstlane(budget);
    const int ca = 2 * OFF_CAN + (lane >> 2) * 128 + (lane & 3) * 16;
    for (int s = 0; s < NSEG - 1; ++s) {
        const int pb = 2 * (OFF_PART + (s & 1) * PART_BYTES);
        int np; unsigned tc;
        asm volatile(OWNER_ASM : [np] "=&s"(np), [tm] "=&s"(tc), [bud] "+s"(budget), [oT] "+s"(oT), [oC] "+s"(oC), [oR] "+s"(oR)
            : [c8] "s"(c8), [ca] "v"(ca), [cs] "s"((s & 3) * 16384), [tag] "s"(s + 1), [o] "v"(o), [so] "v"(so), [sel] "v"(selu), [rs] "s"(rs), [pb] "s"(pb), [mb0] "s"(2 * (OFF_MEMB + ((s + 1) & 3) * MEMB_STRIDE)),
              [mb1] "s"(2 * (OFF_MEMB + ((s + 1) & 3) * MEMB_STRIDE + 32768)) : OWNER_CLOBBER);
        (void)wait_until(now() + a.rd);
#pragma unroll
        for (int r = 0; r < 4; ++r) st128(sent4, rs, o + r * 1024, pb);
        if (s > 1) { st32(SENT, rs, so, 2 * (OFF_MEMB + ((s - 1) & 3) * MEMB_STRIDE)); if (NREP > 2) st32(SENT, rs, so, 2 * (OFF_MEMB + ((s - 1) & 3) * MEMB_STRIDE + 32768)); }
        fails += np > 1;
        { const unsigned t = now(); (void)wait_until(t + ((int)(tc - 8 * t + a.c2) >> 3) + a.cw); }
    }
}
DI void role_owner(const Args& a, unsigned lds_base, int ow, rsrc_t rw, int& budget) {
    const int tid = threadIdx.x, lane = tid & 63, w = __builtin_amdgcn_readfirstlane(tid >> 6);
    const unsigned tbase = now(), cbase = (unsigned)__builtin_readcyclecounter();
    if (ow != 63 && w) { prefetch(a, lds_base, ow & 3, (ow >> 2) * 3 + w - 1, (ow & 3) == 3 ? 45 : 48); VM_DRAIN(); }
    if (w >= 2 || (w == 1 && ow >= 8)) return;
    const u32x2 tag = launch_tag(a);
    if (ow == 63) {
        unsigned best[2] = {~0u, ~0u};
        for (int r = 0; r < 10; ++r) {
            unsigned long long t0, t1;
            asm volatile("s_memtime %0\n\ts_waitcnt lgkmcnt(0)" : "=s"(t0) :: "memory");
            st32(SENT, rw, lane == 0 ? (r & 1) * 8192 + (r >> 1) * 4096 * 5 / 4096 * 16384 + ((r >> 1) & 1) * 4096 : OOB, 0);
            asm volatile("s_waitcnt vmcnt(0)\n\ts_memtime %0\n\ts_waitcnt lgkmcnt(0)" : "=s"(t1) :: "memory");
            best[r & 1] = min(best[r & 1], (unsigned)(t1 - t0));
        }
        __builtin_amdgcn_raw_buffer_store_b64((u32x2){(tag.x & ~1u) | (best[1] < best[0]), tag.y}, rw, lane == 0 ? F_INIT + 32 * 128 : OOB, 0, 16);
    }
    const int sel = wait_init(rw, tag, budget, 32);
    const rsrc_t rs = __builtin_amdgcn_make_buffer_rsrc(a.ws + sel * 8192, 0, COMM_BYTES - 8192, 0x00020000);
    owner_loop(a, ow + 64 * w, rs, tbase, cbase, budget);
}

template <int O, class V> DI void put8(V& d, u32x8 f) {
#pragma unroll
    for (int j = 0; j < 8; ++j) d[O + j] = f[j];
}
DI int opaque_lane() { int l; asm volatile("v_mbcnt_lo_u32_b32 %0, -1, 0\n\tv_mbcnt_hi_u32_b32 %0, -1, %0" : "=v"(l)); return l; }
DI f32x4 SMF64(u32x4 a, u32x8 b, f32x4 c, int i) { asm volatile("s_nop 1\n\tv_smfmac_f32_16x16x64_bf16 %0, %1, %2, %3\n\ts_nop 7" : "+v"(c) : "v"(a), "v"(b), "v"(i)); return c; }
DI f32x4 SMF32(u32x2 a, u32x4 b, f32x4 c, int i) { asm volatile("s_nop 1\n\tv_smfmac_f32_16x16x32_bf16 %0, %1, %2, %3\n\ts_nop 7" : "+v"(c) : "v"(a), "v"(b), "v"(i)); return c; }
DI f32x4 MFV(u32x4 a, u32x4 b, f32x4 c) { asm volatile("s_nop 1\n\tv_mfma_f32_16x16x32_bf16 %0, %1, %2, %0\n\ts_nop 7" : "+v"(c) : "v"(a), "v"(b)); return c; }

DI void role_head(const Args& a, LAS char* lds, unsigned lds_base, int h, int tw, rsrc_t rw, int& budget) {
    const int tid = threadIdx.x, w = __builtin_amdgcn_readfirstlane(tid >> 6);
    char* ws = a.ws;
    const unsigned tbase = now(), cbase = (unsigned)__builtin_readcyclecounter();
    const bool gv_on = tw == 0; (void)gv_on;
    u32x32 A0, A1, A2, A3, A4, A5, A6, A7, R0, R1, R2, R3; u32x8 R4;
    u32x2 tag;
    {
        const int lane = tid & 63, n = lane & 15, g = lane >> 4;
#pragma unroll
        for (int j = 0; j < 3; ++j) glds16(a.mem0, min(4 * j + w, 8) * 1024 + lane * 16, lds_base + L_KX + min(4 * j + w, 8) * 1024);
        const int tq = n >> 2, tc = (n & 3) >> 1, lo8 = (n & 1) * 8;
        const int r_nat = 16 * (g >> 1) + 4 * (g & 1) + (tq & 1) + 8 * (tq >> 1);
        const int r_sig = 16 * (tq & 1) + 4 * (g >> 1) + (g & 1) + 2 * (tq >> 1);
        const int r_o = 16 * (tq & 1) + 8 * (g >> 1) + 4 * (g & 1) + (tq >> 1);
#define WFRAG(sl, kb, t) ((kb) < 8 ? sm_frag(lds, sl, r_sig, 32, 8, 4 * w + 2 * (t) + tc, lo8) : sm_frag(lds, sl, r_nat, 2, 32, 4 * w + 2 * (t) + tc, lo8))
        constexpr int VKB[10] = {2, 5, 6, 7, 8, 0, 0, 3, 4, 1};
        const u32x4 ro = ring_offs(w, lane, HD);
        auto issue = [&](int i) {
            const unsigned slot = lds_base + L_W + (i % C_RING) * 16384 + w * 4096;
            if (i < 9) ring_issue(a.Wq + (size_t)(64 * i) * HD + h * DH, ro, slot);
            else if (i < 18) ring_issue(a.Wk + (size_t)(64 * (i - 9)) * HD + h * DH, ro, slot);
            else if (i == 23) ring_issue(a.Wo + (size_t)h * DH * DM + 512, ring_offs(w, lane, [](int r, int c) { return (r + 64 * (c >> 3)) * (DM * 2) + (c & 7) * 16; }), slot);
            else if (i < 28) ring_issue(a.Wv + (size_t)(64 * VKB[i - 18]) * HD + h * DH, ro, slot);
            else ring_issue(a.Wo + (size_t)(h * DH + 64 * ((i & 1) ^ ((i - 28) >> 1))) * DM + 256 * tw + 128 * (i & 1), ring_offs(w, lane, DM), slot);
        };
        auto keep = [&](int at, u32x8 f) {
            *(LAS u32x4*)(lds + at + lane * 16) = (u32x4){f[0], f[1], f[2], f[3]};
            *(LAS u32x4*)(lds + at + 1024 + lane * 16) = (u32x4){f[4], f[5], f[6], f[7]};
        };
        u32x8 hold[4] = {}, wex;
#pragma unroll
        for (int i = 0; i < C_RING - 1; ++i) issue(i);
        tag = launch_tag(a);
        ring_wait<4 * (C_RING - 1)>();
        mem0_arrange(lds, L_KX, tid);
        static_for<33>([&](auto ic) {
            constexpr int i = decltype(ic)::value, rem = i > 31 ? 0 : (31 - i < C_RING - 2 ? 31 - i : C_RING - 2);
            if constexpr (i == 28 || i == 30) return;
            ring_wait<4 * rem>();
            if constexpr (i + C_RING - 1 < 32) issue(i + C_RING - 1);
            if constexpr (i > 24) {
                if constexpr (i <= 29) { const int sp = L_W + (i == 29 ? 3 : i - 25) * 16384 + w * 4096; keep(sp, hold[0]); keep(sp + 2048, i == 29 ? wex : hold[1]); }
                else {
#pragma unroll
                    for (int T = 0; T < 4; ++T) keep(L_W + (2 * i - 58) * 16384 + w * 8192 + T * 2048, hold[T]);
                }
            }
            const int sl = L_W + (i % C_RING) * 16384;
            if constexpr (i < 16) {
                constexpr int kb = i % 9;
                u32x32& A = i < 2 ? A0 : i < 4 ? A1 : i < 6 ? A2 : i < 8 ? A3 : i < 10 ? A4 : i < 12 ? A5 : i < 14 ? A6 : A7;
                put8<16 * (i & 1)>(A, WFRAG(sl, kb, 0)); put8<16 * (i & 1) + 8>(A, WFRAG(sl, kb, 1));
                if constexpr (i == 1) asm volatile("" : "+{a[0:31]}"(A0));
                if constexpr (i == 3) asm volatile("" : "+{a[32:63]}"(A1));
                if constexpr (i == 5) asm volatile("" : "+{a[64:95]}"(A2));
                if constexpr (i == 7) asm volatile("" : "+{a[96:127]}"(A3));
                if constexpr (i == 9) asm volatile("" : "+{a[128:159]}"(A4));
                if constexpr (i == 11) asm volatile("" : "+{a[160:191]}"(A5));
                if constexpr (i == 13) asm volatile("" : "+{a[192:223]}"(A6));
                if constexpr (i == 15) asm volatile("" : "+{a[224:255]}"(A7));
            }
            else if constexpr (i == 16) { put8<16>(R0, WFRAG(sl, 7, 0)); put8<24>(R0, WFRAG(sl, 7, 1)); }
            else if constexpr (i == 17) { put8<0>(R1, WFRAG(sl, 8, 0)); put8<8>(R1, WFRAG(sl, 8, 1)); }
            else if constexpr (i == 18) { put8<24>(R1, WFRAG(sl, 2, 0)); put8<0>(R2, WFRAG(sl, 2, 1)); }
            else if constexpr (i == 19) { put8<8>(R2, WFRAG(sl, 5, 0)); put8<16>(R2, WFRAG(sl, 5, 1)); }
            else if constexpr (i == 20) { put8<24>(R2, WFRAG(sl, 6, 0)); put8<0>(R3, WFRAG(sl, 6, 1)); }
            else if constexpr (i == 21) { put8<8>(R3, WFRAG(sl, 7, 0)); put8<16>(R3, WFRAG(sl, 7, 1)); }
            else if constexpr (i == 22) { put8<24>(R3, WFRAG(sl, 8, 0)); R4 = WFRAG(sl, 8, 1); }
            else if constexpr (i == 23) wex = sm_frag(lds, sl, r_o, 2, 32, 8 * (w >> 1) + 4 * tw + 2 * (w & 1) + tc, lo8);
            else if constexpr (i < 27) { hold[0] = WFRAG(sl, 0, 0); hold[1] = WFRAG(sl, 0, 1); }
            else if constexpr (i == 27) { hold[0] = WFRAG(sl, 1, 0); put8<16>(R1, WFRAG(sl, 1, 1)); }
            else if constexpr (i < 32) {
#pragma unroll
                for (int T = 0; T < 4; ++T) hold[T] = sm_frag(lds, sl - 16384 + (w >> 1) * 16384, r_o, 2, 32, 8 * (w & 1) + 2 * T + tc, lo8);
            }
        });
        const int ai = 2 * (n >> 2) + (n & 1), ahf = (n >> 1) & 1;
        R0[0] = R0[1] = R0[2] = R0[3] = 0x3F803F80u;
        R0[4] = ahf ? 0xEEEE : 0x4444;
        R0[5] = lds_base + L_MEMS + (g >> 1) * 512 + ai * 64 + (g & 1) * 32 + ahf * 16;
        R0[6] = lds_base + L_MEMS + 8192 + g * 256 + ai * 32 + ahf * 16;
        R0[7] = lane * 16;
        R0[8] = lds_base + L_Q + 2 * g * 320 + (16 * w + n) * 4;
        R0[9] = lds_base + L_KO + 2 * g * 576 + (16 * w + (n & 8) + 4 * (n & 1) + ((n >> 1) & 3)) * 4;
        R0[10] = lds_base + L_Q + ai * 320 + g * 16;
        R0[11] = lds_base + L_KO + ai * 576 + g * 32 + ahf * 16;
        R0[12] = lds_base + L_KX + 8192 + w * 2048 + (g >> 1) * 256 + n * 16 + (g & 1) * 8;
        R0[13] = lds_base + L_W + w * 4096 + lane * 16;
        R0[14] = cmap(((2 * (4 * tw + w) + (n >> 3)) * 4 + g) * 4096 + h * 128 + (n & 7) * 16);
        R0[15] = (g >> 1) == (w >> 1) ? cmap((64 + 2 * (2 * tw + (w & 1)) + (g >> 1)) * 4096 + h * 128 + (g & 1) * 64 + n * 4) : OOB;
        asm volatile("" : "+{v[120:151]}"(R0), "+{v[152:183]}"(R1), "+{v[184:215]}"(R2), "+{v[216:247]}"(R3), "+{v[248:255]}"(R4));
    }
    const int sel = wait_init(rw, tag, budget, 33 + h);
    int kv_rdy = 0;
    const rsrc_t rs = __builtin_amdgcn_make_buffer_rsrc(ws + sel * 8192, 0, COMM_BYTES - 8192, 0x00020000);
    auto dma = [&](int voff, int soff, unsigned dst) { bdma(rw, voff, soff, dst); };
    auto fetch_kv = [&](int s, int lane) {
        const int n = lane & 15, g = lane >> 4;
        const int kxs = (g >> 1) * 1024 + ((g & 1) * 32 + 16 * (n >> 3) + (n & 7)) * 16;
        const int vxs = (g >> 1) * 1024 + ((g & 1) * 32 + n) * 16;
        const int so_ = (s * 32 + h) * 8192 + w * 2048;
        const unsigned xdst = lds_base + L_KX + w * 2048;
        dma(kxs, OFF_KF + so_, xdst); dma(kxs, OFF_KF + so_ + 128, xdst + 1024);
        dma(vxs, OFF_VF + so_, xdst + 8192); dma(vxs, OFF_VF + so_ + 256, xdst + 8192 + 1024);
    };
    auto publish_m = [&](int s, int lane, u32x2 vm) {
        const int mo = (s * NH + h) * 2048 + w * 512;
        if (tw == 0) {
            const int kml = L_KO + (2 * w + ((lane >> 4) & 1)) * 576 + ((lane & 15) >> 1) * 32 + (lane & 1) * 8;
            const u32x2 x = *(LAS u32x2*)(lds + kml), y = *(LAS u32x2*)(lds + kml + 16);
            st128((u32x4){x.x, y.x, x.y, y.y}, rw, lane < 32 ? lane * 16 : OOB, OFF_KM + mo);
        } else { st32(vm.x, rw, lane * 4, OFF_VM + mo); st32(vm.y, rw, lane * 4 + 256, OFF_VM + mo); }
    };
    const int fo = F_KVM + h * 128 + (4 * tw + w) * 4;
    u32x8 scon;
    scon[0] = w; scon[1] = 2 * OFF_CAN + (h >> 1) * 128 + (2 * (h & 1) + tw) * 16; scon[2] = F_KV + h * 512 + w * 128; scon[3] = 65536 + w * 4096;
    scon[4] = OFF_KF + h * 8192 + w * 2048; scon[5] = fo; scon[6] = tw - 1; scon[7] = (tw ? OFF_VM : OFF_KM) + (h - NH) * 2048 + w * 512;
    u32x8 S1 = {0u, 0u, (unsigned)kv_rdy, (unsigned)budget, (unsigned)a.d0, 0xFFFFFFFFu / 22, (unsigned)(a.c2 + a.b0), 0u};
    u32x8 S2 = {0u, 0u, 0u, (unsigned)a.up, (unsigned)a.dn, (unsigned)a.c2, 0u, 1u};
    fetch_kv(0, opaque_lane());
    __builtin_amdgcn_raw_buffer_store_b64((u32x2){8 * now() + a.e0, 1u}, rs, tid == 0 ? 0 : OOB, scon[1], 16);
    asm volatile("s_waitcnt vmcnt(1)" ::: "memory");
    u32x2 vm = {};
    u32x8 S3;
    {
        const unsigned t = now(), c = (unsigned)__builtin_readcyclecounter();
        const float r = (float)(c - cbase) * __builtin_amdgcn_rcpf(8.0f * (float)(t - tbase));
        S2[1] = 8 * t + 16; S1[5] = (unsigned)(4294967296.0f * __builtin_amdgcn_rcpf(r));
        S3 = (u32x8){c, (unsigned)(4096.0f * r), 2u * OFF_MEMB + ((h >> 3) & (NREP - 1)) * 32768, w == 3 ? ~0u : 0u};
    }
    for (;;) {
        asm volatile(STEP_ASM
            : "+{v[96:97]}"(vm), "+{v[0:7]}"(S1), "+{v[8:15]}"(S2), "+{v[16:23]}"(S3), "+{v[24:31]}"(scon)
            : "{a[0:31]}"(A0), "{a[32:63]}"(A1), "{a[64:95]}"(A2), "{a[96:127]}"(A3), "{a[128:159]}"(A4), "{a[160:191]}"(A5), "{a[192:223]}"(A6), "{a[224:255]}"(A7),
              "{v[120:151]}"(R0), "{v[152:183]}"(R1), "{v[184:215]}"(R2), "{v[216:247]}"(R3), "{v[248:255]}"(R4),
              [rs] "s"(rs), [rw] "s"(rw)
            : STEP_CLOBBER);
        const int s = __builtin_amdgcn_readfirstlane(S1[0]);
        const unsigned st = __builtin_amdgcn_readfirstlane(S1[7]);
        kv_rdy = __builtin_amdgcn_readfirstlane(S1[2]); budget = __builtin_amdgcn_readfirstlane(S1[3]);
        if (s == NSEG - 1) break;
        if (__builtin_expect(st & 2, 0)) {
            const int lane = opaque_lane();
            LAS char* z = (LAS char*)0;
            const f32x4 zero = {0, 0, 0, 0};
            const u32x4 ones = __builtin_shufflevector(R0, R0, 0, 1, 2, 3);
            const unsigned zr = lane >> 6;
            const int idx = R0[4];
            u32x4 qf[4];
            f32x4 s1 = zero, s2 = zero;
#pragma unroll
            for (int ks = 0; ks < 4; ++ks) {
                qf[ks] = *(LAS u32x4*)(z + R0[10] + ks * 64);
                s1 = MFV(*(LAS u32x4*)(lds + L_KX + lane * 16 + ks * 2048), qf[ks], s1);
                s2 = MFV(*(LAS u32x4*)(lds + L_KX + lane * 16 + ks * 2048 + 1024), qf[ks], s2);
            }
            f32x4 s0 = SMF64(*(LAS u32x4*)(z + R0[11]), __builtin_shufflevector(qf[0], qf[1], 0, 1, 2, 3, 4, 5, 6, 7), zero, idx);
            s0 = SMF64(*(LAS u32x4*)(z + R0[11] + 128), __builtin_shufflevector(qf[2], qf[3], 0, 1, 2, 3, 4, 5, 6, 7), s0, idx);
            const float sa = s0[0] + s0[2], sb = s0[1] + s0[3];
            float mx = fmaxf(fmaxf(fmaxf(fmaxf(s1[0], s1[1]), fmaxf(s1[2], s1[3])), fmaxf(fmaxf(s2[0], s2[1]), fmaxf(s2[2], s2[3]))), fmaxf(sa, sb));
            mx = fmaxf(mx, __shfl_xor(mx, 16)); mx = fmaxf(mx, __shfl_xor(mx, 32));
            auto ex2 = [&](float x, float y) { return pack2(__builtin_amdgcn_exp2f(x - mx), __builtin_amdgcn_exp2f(y - mx)); };
            const u32x4 px = {ex2(s1[0], s1[1]), ex2(s1[2], s1[3]), ex2(s2[0], s2[1]), ex2(s2[2], s2[3])};
            const unsigned pm = ex2(sa, sb);
            const f32x4 l = MFV(((u32x4){pm, zr, zr, zr}), ones, MFV(px, ones, zero));
            const u32x2 x0 = *(LAS u32x2*)(z + R0[12]), y0 = *(LAS u32x2*)(z + R0[12] + 1024), x1 = *(LAS u32x2*)(z + R0[12] + 512), y1 = *(LAS u32x2*)(z + R0[12] + 1536);
            const f32x4 o = SMF64(px, ((u32x8){x0.x, y0.x, x0.y, y0.y, x1.x, y1.x, x1.y, y1.y}), zero, idx) + SMF32(((u32x2){pm, zr}), ((u32x4){vm.x, vm.y, zr, zr}), zero, idx);
            const float i0 = __builtin_amdgcn_rcpf(l[0]), i1 = __builtin_amdgcn_rcpf(l[1]);
            *(LAS unsigned*)(z + R0[9] + 256) = pack2(o[0] * i0, o[2] * i0);
            *(LAS unsigned*)(z + R0[9] + 256 + 576) = pack2(o[1] * i1, o[3] * i1);
            WG_BARRIER();
            const u32x4 oa = *(LAS u32x4*)(z + R0[11] + 256 + (w >> 1) * 128), ob = *(LAS u32x4*)(z + R0[11] + 384 - (w >> 1) * 128);
            auto frag = [&](int at) { return __builtin_shufflevector(*(LAS u32x4*)(z + at), *(LAS u32x4*)(z + at + 1024), 0, 1, 2, 3, 4, 5, 6, 7); };
            f32x2 f[4];
#pragma unroll
            for (int T = 0; T < 4; ++T) f[T] = fold(SMF64(ob, frag(R0[13] + scon[3] + 32768 + T * 2048), SMF64(oa, frag(R0[13] + scon[3] + T * 2048), zero, idx), idx));
            const f32x2 fx = fold(SMF64(ob, frag((R0[13] ^ 8192) + 49152 + 2048), SMF64(oa, frag(R0[13] + 49152 + 2048), zero, idx), idx));
            const int pb = 2 * (OFF_PART + (s & 1) * PART_BYTES);
            st128((u32x4){pack2(f[0].x, f[1].x), pack2(f[2].x, f[3].x), pack2(f[0].y, f[1].y), pack2(f[2].y, f[3].y)}, rs, R0[14], pb);
            st32(pack2(fx.x, fx.y), rs, R0[15], pb);
        }
        if (__builtin_expect((st & 2) || kv_rdy <= s, 0)) {
            while (kv_rdy <= s) { kv_rdy = prog_min(rw, F_KV + h * 512 + w * 128); if (--budget < 0) break; }
            fetch_kv(s + 1, opaque_lane());
        }
        if (st & 2) { S1[1] = read_canaries(s, rs, opaque_lane(), budget) + S1[6]; S2[2] += 0x10000; }
        if ((int)(S1[1] - S2[1]) > 0x8000) { S2[1] = 8 * now() + 16; S3[0] = (unsigned)__builtin_readcyclecounter(); }
        S1[0] = s + 1; S1[2] = kv_rdy; S1[3] = budget;
    }
    {
        const int lane = opaque_lane();
        publish_m(NSEG - 1, lane, vm);
        st32((unsigned)(NSEG - 2), rw, lane == 0 ? fo : OOB, 0);
        VM_DRAIN();
        st32((unsigned)(NSEG - 1), rw, lane == 0 ? fo : OOB, 0);
    }
}

__global__ void __launch_bounds__(256, 1) fused_kernel(Args a) {
    extern __shared__ __attribute__((aligned(16))) char smem[];
    LAS char* lds = (LAS char*)smem;
    const unsigned lds_base = (unsigned)(uintptr_t)lds;
    const int w = __builtin_amdgcn_readfirstlane(threadIdx.x >> 6);
    const int b = blockIdx.x, bx = b & 7, bi = b >> 3;
    if (bi >= 28) {
        const void* pc; unsigned len;
        asm volatile("s_getpc_b64 %0\n1:\ts_mov_b32 %1, .Lstep_end-1b" : "=s"(pc), "=s"(len));
        touch(pc, min((bi - 28) * 32768 + threadIdx.x * 128, len), lds_base);
    }
    asm volatile("" :: "s"(a.Wq), "s"(a.cw));
    const rsrc_t rw = __builtin_amdgcn_make_buffer_rsrc(a.ws, 0, WS_USED, 0x00020000);
    int budget = SPIN_MAX;
#define RT(i)
    if (bx < 4) {
        if (bi < 16) role_head(a, lds, lds_base, 8 * bx + (bi >> 1), bi & 1, rw, budget);
        else role_owner(a, lds_base, 4 * (bi - 16) + bx, rw, budget);
    } else {
        const int fx = bx - 4, no = fx < 3 ? 14 : 22, j = 14 * fx + bi;
        if (bi >= no || (j & 2)) {
            const unsigned t = now() + a.d1;
            if (bi >= no) { if (w) prefetch(a, lds_base, bx, (bi - no) * 3 + w - 1, 3 * (32 - no)); }
            else {
                const int sh = (j >> 2) * 2 + (j & 1);
#pragma unroll
                for (int i = 0; i < INIT_SHARE / 4096; ++i) st128((u32x4){SENT, SENT, SENT, SENT}, rw, threadIdx.x * 16 + i * 4096, sh * INIT_SHARE);
                const u32x2 tag = launch_tag(a);
                VM_DRAIN();
                WG_BARRIER();
                __builtin_amdgcn_raw_buffer_store_b64(tag, rw, threadIdx.x == 0 ? F_INIT + sh * 128 : OOB, 0, 16);
                RT(3)
            }
            (void)wait_until(t);
        }
        if (bi >= no) {
            const int t = 18 * fx + bi - no;
            role_s4d(a, lds, t, rw, budget);
            RT(1)
            if (t < 9 * OUT_REP) role_outp(a, lds, t % 9, t / 9, rw, budget);
        }
        else if (j & 2) role_qa(a, lds, lds_base, (j >> 2) * 2, j & 1, rw, budget);
        else role_kv(a, lds, lds_base, (j >> 2) * 2 + (j & 1), rw, budget);
    }
    RT(2)
    if (budget < 0 && threadIdx.x == 0) st32(0x1000u + b, rw, F_ERR, 0);
    if (threadIdx.x == 0) {
        const unsigned old = __hip_atomic_fetch_add((__attribute__((address_space(1))) unsigned*)(a.ws + F_DONE), 1u, __ATOMIC_RELAXED, __HIP_MEMORY_SCOPE_AGENT);
        if (old == 254u) __builtin_amdgcn_raw_buffer_store_b64(launch_tag(a), rw, F_EPOCH, 0, 16);
    }
}

static int g_c1 = 72, g_c2 = 392, g_b0 = 64, g_up = 16, g_fl = 8, g_dn = 3, g_d0 = 2040, g_d1 = 300, g_rd = 50, g_cw = 70, g_e0 = 165;
extern "C" void kernel_launch(void* const* d_in, const int* in_sizes, int n_in, void* d_out, int out_size, void* d_ws, size_t ws_size, hipStream_t stream) {
    Args a;
    a.x = (const u16*)d_in[0]; a.mem0 = (const u16*)d_in[1];
    a.Wq = (const u16*)d_in[2]; a.Wk = (const u16*)d_in[3]; a.Wv = (const u16*)d_in[4]; a.Wo = (const u16*)d_in[5];
    a.lAr = (const u16*)d_in[8]; a.Aim = (const u16*)d_in[9]; a.Cr = (const u16*)d_in[10]; a.Ci = (const u16*)d_in[11];
    a.ldt = (const u16*)d_in[12]; a.Dsk = (const u16*)d_in[13]; a.gw = (const u16*)d_in[14];
    a.out = (u16*)d_out;
    a.ws = (char*)(((uintptr_t)d_ws + ((2u << 20) - 1)) & ~(uintptr_t)((2u << 20) - 1));
    a.c1 = g_c1; a.c2 = g_c2; a.d0 = g_d0; a.b0 = g_b0; a.d1 = g_d1; a.up = g_up | g_fl << 16; a.dn = g_dn; a.rd = g_rd; a.cw = g_cw; a.e0 = 8 * g_e0;
    (void)hipFuncSetAttribute((const void*)fused_kernel, hipFuncAttributeMaxDynamicSharedMemorySize, L_TOTAL);
    fused_kernel<<<256, 256, L_TOTAL, stream>>>(a);
}
```

```cpp
#include <hip/hip_runtime.h>
#include <hip/hip_bf16.h>
#include <stdint.h>

typedef unsigned short u16;
typedef __bf16 bf16x8 __attribute__((ext_vector_type(8)));
typedef __bf16 bf16x16 __attribute__((ext_vector_type(16)));
typedef short s16x4 __attribute__((ext_vector_type(4)));
typedef float f32x2 __attribute__((ext_vector_type(2)));
typedef float f32x4 __attribute__((ext_vector_type(4)));
typedef float f32x16 __attribute__((ext_vector_type(16)));
typedef unsigned u32x2 __attribute__((ext_vector_type(2)));
typedef unsigned u32x4 __attribute__((ext_vector_type(4)));
typedef unsigned u32x8 __attribute__((ext_vector_type(8)));
typedef unsigned u32x16 __attribute__((ext_vector_type(16)));
typedef unsigned u32x32 __attribute__((ext_vector_type(32)));
typedef short s16x8 __attribute__((ext_vector_type(8)));
#define LAS __attribute__((address_space(3)))
#define DI __device__ __forceinline__
typedef __amdgpu_buffer_rsrc_t rsrc_t;

constexpr int SEQ = 2048, DM = 576, NH = 32, DH = 128, HD = NH * DH, NM = 8, SEG = 32, NSEG = 64, NS = 128;
constexpr unsigned SENT = 0xFFFFFFFFu;
constexpr unsigned OOB = 0x7FFFFFF0u;
constexpr int SPIN_MAX = 1 << 20;

constexpr int OFF_PART = 0;
constexpr int PART_BYTES = 72 * 4096;
constexpr int OFF_MEMB = OFF_PART + 2 * PART_BYTES;
constexpr int MEMB_STRIDE = 4 * 16384;
constexpr int OFF_CAN = OFF_MEMB + 4 * MEMB_STRIDE;
constexpr int COMM_LOGICAL = OFF_CAN + 32768;
constexpr int COMM_BYTES = 2 * COMM_LOGICAL;
constexpr int F_ERR = COMM_BYTES;
constexpr int F_KV = F_ERR + 128;
constexpr int F_KVM = F_KV + 16384;
constexpr int F_O = F_KVM + 8192;
constexpr int F_H = F_O + 4096;
constexpr int F_S4 = F_H + 32768;
constexpr int F_DONE = F_S4 + 512;
constexpr int INIT_SHARE = 14 * 4096, INIT_BYTES = 32 * INIT_SHARE;
constexpr int F_INIT = INIT_BYTES;
constexpr int F_EPOCH = F_INIT + 16384;
static_assert(F_DONE + 128 <= INIT_BYTES, "");
static_assert(F_EPOCH + 128 <= (2 << 20), "hand-off region");
constexpr int OFF_KF = 2 << 20;
constexpr int OFF_VF = OFF_KF + SEQ * HD * 2;
constexpr int OFF_OF = OFF_VF + SEQ * HD * 2;
constexpr int OFF_KM = OFF_OF + SEQ * HD * 2;
constexpr int OFF_VM = OFF_KM + NSEG * NH * NM * DH * 2;
constexpr int FAST = NSEG - 4;
constexpr int OFF_BO = OFF_VM + NSEG * NH * NM * DH * 2;
constexpr int BO_ROW = 4352;
constexpr int OFF_GT = OFF_BO + DM * BO_ROW;
constexpr int OFF_DBG = OFF_GT + SEQ * 4;
constexpr int WS_USED = OFF_DBG + (4 << 20);

struct Args {
    const u16 *x, *mem0, *Wq, *Wk, *Wv, *Wo, *lAr, *Aim, *Cr, *Ci, *ldt, *Dsk, *gw;
    u16* out; char* ws;
    int c1, c2, d0, b0, d1, up, dn, rd, cw, e0;
    unsigned xa, xb;
};

DI float bf2f(u16 b) { return __uint_as_float((unsigned)b << 16); }
DI float bflo(unsigned w) { return __uint_as_float(w << 16); }
DI float bfhi(unsigned w) { return __uint_as_float(w & 0xFFFF0000u); }
DI u16 f2bf(float f) { return __builtin_bit_cast(u16, (__bf16)f); }
DI unsigned pack2(float lo, float hi) {
    typedef __bf16 bf16x2 __attribute__((ext_vector_type(2)));
    bf16x2 v = {(__bf16)lo, (__bf16)hi};
    return __builtin_bit_cast(unsigned, v);
}
#define WG_BARRIER() asm volatile("s_waitcnt lgkmcnt(0)\n\ts_barrier" ::: "memory")
#define VM_DRAIN() asm volatile("s_waitcnt vmcnt(0)" ::: "memory")
DI int cmap(int o) { return (o & 0x1FFF) | ((o >> 13) << 14); }
DI unsigned mx4(unsigned a, u32x4 v) { a = max(max(a, v.x), v.y); return max(max(a, v.z), v.w); }
DI unsigned now() { return (unsigned)__builtin_amdgcn_s_memrealtime(); }
DI unsigned wait_until(unsigned t) {
    unsigned v; int guard = 8192;
    do { v = now(); } while ((int)(t - v) >= 4 && --guard > 0);
    const int r = __builtin_amdgcn_readfirstlane((int)(t - v));
    asm volatile("s_cmp_lt_i32 %0, 1\n\ts_cbranch_scc1 1f\n\ts_nop 3\n\ts_cmp_lt_i32 %0, 2\n\ts_cbranch_scc1 1f\n\ts_nop 3\n\ts_cmp_lt_i32 %0, 3\n\ts_cbranch_scc1 1f\n\ts_nop 7\n1:" :: "s"(r) : "scc");
    return r > 0 ? t : v + 2;
}
DI void st128(u32x4 v, rsrc_t rs, int off, int soff) {
    __builtin_amdgcn_raw_buffer_store_b128(v, rs, off, soff, 16);
    asm volatile("s_nop 1" :: "v"(v) : "memory");
}
DI void st32(unsigned v, rsrc_t rs, int off, int soff) { __builtin_amdgcn_raw_buffer_store_b32(v, rs, off, soff, 16); }
DI u32x4 ld128(rsrc_t rs, int off, int soff) { return __builtin_amdgcn_raw_buffer_load_b128(rs, off, soff, 16); }
DI u32x2 ld64(rsrc_t rs, int off, int soff) { return __builtin_amdgcn_raw_buffer_load_b64(rs, off, soff, 16); }
DI unsigned ld32(rsrc_t rs, int off, int soff) { return __builtin_amdgcn_raw_buffer_load_b32(rs, off, soff, 16); }
DI f32x2 fold(f32x4 a) { return (f32x2){a[0], a[1]} + (f32x2){a[2], a[3]}; }
DI float dpp_xor1(float v) { return __int_as_float(__builtin_amdgcn_update_dpp(0, __float_as_int(v), 0xB1, 0xf, 0xf, true)); }
DI float red_sum_g(float v) {
    auto a = __builtin_amdgcn_permlane16_swap(__float_as_uint(v), __float_as_uint(v), false, false);
    v = __uint_as_float(a[0]) + __uint_as_float(a[1]);
    auto b = __builtin_amdgcn_permlane32_swap(__float_as_uint(v), __float_as_uint(v), false, false);
    return __uint_as_float(b[0]) + __uint_as_float(b[1]);
}
DI int wave_max32(int d) {
    d = max(d, __builtin_amdgcn_update_dpp(d, d, 0x121, 0xf, 0xf, false));
    d = max(d, __builtin_amdgcn_update_dpp(d, d, 0x122, 0xf, 0xf, false));
    d = max(d, __builtin_amdgcn_update_dpp(d, d, 0x124, 0xf, 0xf, false));
    d = max(d, __builtin_amdgcn_update_dpp(d, d, 0x128, 0xf, 0xf, false));
    return __builtin_amdgcn_readfirstlane(max(max(__builtin_amdgcn_readlane(d, 0), __builtin_amdgcn_readlane(d, 16)), max(__builtin_amdgcn_readlane(d, 32), __builtin_amdgcn_readlane(d, 48))));
}
DI u32x2 tr_read(LAS char* lds, int addr) { return __builtin_bit_cast(u32x2, __builtin_amdgcn_ds_read_tr16_b64_v4i16((LAS s16x4*)(lds + addr))); }
DI void wait_prog(rsrc_t rw, int off, bool act, int thr, int& budget) {
    for (;;) {
        const int v = (int)ld32(rw, off, 0);
        asm volatile("" ::: "memory");
        if (__all(!act || v >= thr)) break;
        if (--budget < 0) break;
        __builtin_amdgcn_s_sleep(1);
    }
}
DI u32x2 launch_tag(const Args& a) {
    unsigned long long z;
    asm volatile("s_load_dwordx2 %0, %1, 0x0\n\ts_waitcnt lgkmcnt(0)" : "=s"(z) : "s"(a.ws + F_EPOCH) : "memory");
    z += 0x9E3779B97F4A7C15ull;
    z = (z ^ (z >> 30)) * 0xBF58476D1CE4E5B9ull; z = (z ^ (z >> 27)) * 0x94D049BB133111EBull; z ^= z >> 31;
    return (u32x2){(unsigned)z, (unsigned)(z >> 32)};
}
DI int wait_init(rsrc_t rw, u32x2 tag, int& budget, int more = 0) {
    const int l = threadIdx.x & 63, o = F_INIT + (l < 32 ? l : l == 63 ? min(more, 32) : more) * 128;
    u32x2 v;
    for (;;) {
        v = ld64(rw, o, 0);
        asm volatile("" ::: "memory");
        if (__all(((v.x ^ tag.x) >> 1) == 0 && v.y == tag.y)) break;
        if (--budget < 0) break;
        __builtin_amdgcn_s_sleep(2);
    }
    return __builtin_amdgcn_readlane(v.x, 63) & 1;
}
DI void glds16(const void* base, int voff, unsigned lds_dst) {
    unsigned keep;
    asm volatile("s_mov_b32 %0, m0\n\ts_mov_b32 m0, %3\n\ts_nop 0\n\tglobal_load_lds_dwordx4 %1, %2\n\ts_mov_b32 m0, %0" : "=&s"(keep) : "v"(voff), "s"(base), "s"(lds_dst) : "memory");
}
DI void bdma(rsrc_t rs, int voff, int soff, unsigned lds_dst) {
    unsigned keep;
    asm volatile("s_mov_b32 %0, m0\n\ts_mov_b32 m0, %4\n\ts_nop 0\n\tbuffer_load_dwordx4 %1, %2, %3 offen sc1 lds\n\ts_mov_b32 m0, %0" : "=&s"(keep) : "v"(voff), "s"(rs), "s"(soff), "s"(lds_dst) : "memory");
}
DI int swz(int r) { return ((r & 3) ^ (((r >> 2) & 3) << 1) ^ (((r >> 4) & 1) << 2)) & 7; }
template <class F> DI u32x4 ring_offs(int w, int lane, F at) {
    u32x4 o;
#pragma unroll
    for (int j = 0; j < 4; ++j) { const int r = 16 * w + 4 * j + (lane >> 4); o[j] = at(r, (lane & 15) ^ (2 * swz(r))) - 1024 * j; }
    return o;
}
DI u32x4 ring_offs(int w, int lane, int ld) { return ring_offs(w, lane, [&](int r, int c) { return r * ld * 2 + c * 16; }); }
DI void ring_issue(const u16* src, u32x4 o, unsigned slot) {
    unsigned keep;
    asm volatile("s_mov_b32 %0, m0\n\ts_mov_b32 m0, %6\n\ts_nop 0\n\tglobal_load_lds_dwordx4 %1, %5\n\tglobal_load_lds_dwordx4 %2, %5 offset:1024\n\t"
                 "global_load_lds_dwordx4 %3, %5 offset:2048\n\tglobal_load_lds_dwordx4 %4, %5 offset:3072\n\ts_mov_b32 m0, %0"
                 : "=&s"(keep) : "v"(o.x), "v"(o.y), "v"(o.z), "v"(o.w), "s"(src), "s"(slot) : "memory");
}
#define PF_FIRST 2
constexpr int L_TOTAL = 163840, L_SINK = L_TOTAL - 256;
DI void touch(const void* base, int voff, unsigned lds_base) {
    unsigned keep;
    asm volatile("s_mov_b32 %0, m0\n\ts_mov_b32 m0, %3\n\ts_nop 0\n\tglobal_load_lds_dword %1, %2\n\ts_mov_b32 m0, %0" : "=&s"(keep) : "v"(voff), "s"(base), "s"(lds_base + L_SINK) : "memory");
}
DI void prefetch(const Args& a, unsigned lds_base, int x, int id, int n) {
    const int lane = threadIdx.x & 63;
    const int h0 = x < 4 ? 8 * x : (0x160E0800 >> (8 * (x - 4))) & 255, lpr = x == 5 ? 12 : x == 7 ? 20 : 16, per = 9 * lpr;
    const int first = PF_FIRST * lpr, end = (x < 4 ? 4 : 2) * per;
    const float rl = __builtin_amdgcn_rcpf((float)lpr);
    for (int i = first + id; i < end; i += n) {
        const int m = (i >= per) + (i >= 2 * per) + (i >= 3 * per), mm = m + (x >= 4);
        int j = i - m * per;
        if (x < 4 && m == 2) j = (int)((0x143087652ull >> (4 * (j >> 4))) & 15) * 16 + (j & 15);
        const int L = j * 64 + lane, row = (int)((L + 0.5f) * rl);
        touch(mm == 0 ? a.Wq : mm == 1 ? a.Wk : mm == 2 ? a.Wv : a.Wo, m == 3 ? h0 * (DH * DM * 2) + L * 128 : row * (HD * 2) + (L - row * lpr) * 128 + h0 * 256, lds_base);
    }
}
template <int N> DI void ring_wait() { asm volatile("s_waitcnt vmcnt(%0) lgkmcnt(0)\n\ts_barrier" :: "i"(N) : "memory"); }
template <int I> struct ic_t { static constexpr int value = I; };
template <int N, int I = 0, class F> DI void static_for(F&& f) { if constexpr (I < N) { f(ic_t<I>{}); static_for<N, I + 1>(f); } }
#define MFMA32(a, b, c) __builtin_amdgcn_mfma_f32_32x32x16_bf16(__builtin_bit_cast(bf16x8, a), __builtin_bit_cast(bf16x8, b), c, 0, 0, 0)
#define MFMA16(a, b, c) __builtin_amdgcn_mfma_f32_16x16x32_bf16(__builtin_bit_cast(bf16x8, a), __builtin_bit_cast(bf16x8, b), c, 0, 0, 0)

constexpr int S_CH = 9;
constexpr int S_XRS = 4096 + 32;
constexpr int S_XR = 0;
constexpr int S_RC = S_CH * S_XRS;
constexpr int S_RCS = 4192;
constexpr int S_KD = S_RC + 4 * S_RCS;
constexpr int S_K = S_KD + 48 * 128;
constexpr int S_TAB = S_K + 79 * 160 + 32;
constexpr int S_TROW = 1056;
constexpr int S_TOTAL = S_TAB + 57 * S_TROW;
static_assert(S_RC % 16 == 0 && S_TAB % 16 == 0 && 4 * 64 * 144 <= 57 * S_TROW, "");
DI f32x2 crot(f32x2 z) { return (f32x2){-z.y, z.x}; }
DI f32x2 cmul(f32x2 a, f32x2 z) { return __builtin_elementwise_fma(a.yy, crot(z), a.xx * z); }
DI f32x2 cexp_p(float ar, float ai, float p) {
    const float x = ai * p;
    const float hi = x * 0.15915494309189535f;
    const float lo = __builtin_fmaf(x, 0.15915494309189535f, -hi) + x * 6.420638316725915e-9f;
    const float rev = (hi - __builtin_rintf(hi)) + lo;
    const float m = __builtin_amdgcn_exp2f(ar * p * 1.4426950408889634f);
    return (f32x2){m * __builtin_amdgcn_cosf(rev), m * __builtin_amdgcn_sinf(rev)};
}
#define FENCE() __builtin_amdgcn_sched_barrier(0)
#define TT(c, i)
DI void role_s4d(const Args& a, LAS char* lds, int task, rsrc_t rw, int& budget) {
    const int tid = threadIdx.x, lane = tid & 63, w = __builtin_amdgcn_readfirstlane(tid >> 6);
    const int d0 = task * S_CH;
#define S4S(i)
    {
        const rsrc_t rx = __builtin_amdgcn_make_buffer_rsrc((void*)a.x, 0, SEQ * DM * 2, 0x00020000);
        const unsigned sh = (d0 & 1) * 16;
        u32x4 v[8]; unsigned e[8];
#pragma unroll
        for (int i = 0; i < 8; ++i) {
            const int o = (tid + 256 * i) * (DM * 2) + (d0 & ~1) * 2;
            v[i] = __builtin_amdgcn_raw_buffer_load_b128(rx, o, 0, 0); e[i] = __builtin_amdgcn_raw_buffer_load_b32(rx, o, 16, 0);
        }
#pragma unroll
        for (int p = 0; p < 2; ++p) {
            const int tok = 32 * task + 8 * w + 4 * p + (lane >> 4);
            float acc = 0.f;
#pragma unroll
            for (int i = 0; i < 5; ++i) {
                const int ch = (lane & 15) + 16 * i;
                const u32x4 gv = ch < 72 ? *(const u32x4*)(a.gw + ch * 8) : (u32x4){0u, 0u, 0u, 0u};
                const u32x4 xv = *(const u32x4*)(a.x + (size_t)tok * DM + min(ch, 71) * 8);
#pragma unroll
                for (int j = 0; j < 4; ++j) acc += bflo(xv[j]) * bflo(gv[j]) + bfhi(xv[j]) * bfhi(gv[j]);
            }
            acc += __shfl_xor(acc, 1); acc += __shfl_xor(acc, 2); acc += __shfl_xor(acc, 4); acc += __shfl_xor(acc, 8);
            st32(__float_as_uint(1.0f / (1.0f + __expf(-acc))), rw, (lane & 15) == 0 ? OFF_GT + tok * 4 : OOB, 0);
        }
#pragma unroll
        for (int i = 0; i < 8; ++i) {
            const unsigned x5[5] = {__builtin_amdgcn_alignbit(v[i].y, v[i].x, sh), __builtin_amdgcn_alignbit(v[i].z, v[i].y, sh), __builtin_amdgcn_alignbit(v[i].w, v[i].z, sh),
                                    __builtin_amdgcn_alignbit(e[i], v[i].w, sh), e[i] >> sh};
#pragma unroll
            for (int c = 0; c < S_CH; ++c) *(LAS u16*)(lds + S_XR + c * S_XRS + (2047 - tid - 256 * i) * 2) = (u16)(x5[c >> 1] >> (16 * (c & 1)));
        }
    }
    if (tid < 8 * S_CH) *(LAS unsigned*)(lds + S_XR + (tid >> 3) * S_XRS + 4096 + (tid & 7) * 4) = 0;
    if (tid < 64) *(LAS unsigned*)(lds + S_RC + (tid >> 4) * S_RCS + 4096 + (tid & 15) * 4) = 0;
    for (int i = tid; i < 15 * 160 / 4; i += 256) *(LAS unsigned*)(lds + S_K + i * 4) = 0;
    int kd[8];
#pragma unroll
    for (int j = 0; j < 8; ++j) {
        const int p = (int)bf2f(f2bf((float)(8 * tid + j))), k = p >> 7, r = p & 7;
        const int row = p < 256 ? 8 * k + r : (p < 512 ? 8 + 4 * k + (r >> 1) : (p < 1024 ? 16 + 2 * k + (r >> 2) : 24 + k));
        kd[j] = S_KD + (row * 16 + ((p >> 3) & 15)) * 8;
    }
    const int n = tid & 127, nn = lane & 15, g = lane >> 4;
    u16 pA = a.lAr[d0 * NS + n], pI = a.Aim[d0 * NS + n], pCr = a.Cr[d0 * NS + n], pCi = a.Ci[d0 * NS + n], pT = a.ldt[d0], pD = a.Dsk[d0];
    for (int ch = 0; ch < S_CH; ++ch) {
        WG_BARRIER();
        S4S(0)
        const float dsk = bf2f(pD);
        {
            const u32x4 v = *(LAS u32x4*)(lds + S_XR + ch * S_XRS + tid * 16);
            const u32x2 e = *(LAS u32x2*)(lds + S_XR + ch * S_XRS + tid * 16 + 16);
            const unsigned s01 = __builtin_amdgcn_alignbit(v.y, v.x, 16), s12 = __builtin_amdgcn_alignbit(v.z, v.y, 16), s23 = __builtin_amdgcn_alignbit(v.w, v.z, 16);
            const unsigned s34 = __builtin_amdgcn_alignbit(e.x, v.w, 16), s45 = __builtin_amdgcn_alignbit(e.y, e.x, 16);
            *(LAS u32x4*)(lds + S_RC + tid * 16) = v;
            *(LAS u32x4*)(lds + S_RC + S_RCS + tid * 16) = (u32x4){s01, s12, s23, s34};
            *(LAS u32x4*)(lds + S_RC + 2 * S_RCS + tid * 16) = (u32x4){v.y, v.z, v.w, e.x};
            *(LAS u32x4*)(lds + S_RC + 3 * S_RCS + tid * 16) = (u32x4){s12, s23, s34, s45};
        }
        S4S(1)
        {
            const float dt = bf2f(f2bf(expf(bf2f(pT))));
            const float ar = -bf2f(f2bf(expf(bf2f(pA)))) * dt, ai = bf2f(pI) * dt;
            const f32x2 C = {2.f * bf2f(pCr) * dt, 2.f * bf2f(pCi) * dt};
            const int dn = min(d0 + ch + 1, DM - 1);
            pA = a.lAr[dn * NS + n]; pI = a.Aim[dn * NS + n]; pCr = a.Cr[dn * NS + n]; pCi = a.Ci[dn * NS + n]; pT = a.ldt[dn]; pD = a.Dsk[dn];
            const int to = S_TAB + n * 8;
            auto put = [&](int row, f32x2 v) { *(LAS f32x2*)(lds + to + row * S_TROW) = v; };
            const f32x2 z128 = cexp_p(ar, ai, 128.f);
            if (w < 2) {
                f32x2 E[8], G[4];
                E[1] = cexp_p(ar, ai, 1.f); G[0] = C;
#pragma unroll
                for (int r = 2; r < 8; ++r) E[r] = cmul(E[r >> 1], E[r - (r >> 1)]);
#pragma unroll
                for (int k = 1; k < 4; ++k) G[k] = cmul(G[k - 1], z128);
#pragma unroll
                for (int k = 0; k < 4; ++k)
#pragma unroll
                    for (int r = 0; r < 8; r += 1 + (k >> 1)) put(k < 2 ? 8 * k + r : 8 + 4 * k + (r >> 1), r ? cmul(E[r], G[k]) : G[k]);
            } else {
                const f32x2 z4 = cexp_p(ar, ai, 4.f), z8 = cexp_p(ar, -ai, 8.f);
                f32x2 G = cmul(C, cexp_p(ar, ai, 512.f)), B = z8;
                put(41, (f32x2){1.f, 0.f});
#pragma unroll
                for (int k = 1; k < 16; ++k) {
                    put(41 + k, B); B = cmul(B, z8);
                    if (k < 5) { put(22 + 2 * k, G); put(23 + 2 * k, cmul(G, z4)); }
                    else if (k < 14) put(27 + k, G);
                    G = cmul(G, z128);
                }
            }
        }
        WG_BARRIER();
        S4S(2)
        {
            const int bo = S_TAB + (41 + nn) * S_TROW + g * 16;
            f32x4 av[12], bv[12], acc[3] = {};
            if (w < 3) {
                const int ao = S_TAB + min(16 * w + nn, 40) * S_TROW + g * 16;
#pragma unroll
                for (int j = 0; j < 12; ++j) { av[j] = *(LAS f32x4*)(lds + ao + j * 64); bv[j] = *(LAS f32x4*)(lds + bo + j * 64); }
#pragma unroll
                for (int j = 0; j < 48; ++j) acc[j & 1] = __builtin_amdgcn_mfma_f32_16x16x4f32(av[j >> 2][j & 3], bv[j >> 2][j & 3], acc[j & 1], 0, 0, 0);
#pragma unroll
                for (int r = 0; r < 4; ++r) *(LAS float*)(lds + S_KD + ((16 * w + 4 * g + r) * 16 + nn) * 8) = acc[0][r] + acc[1][r];
            } else {
#pragma unroll
                for (int j = 0; j < 4; ++j) {
                    bv[j] = *(LAS f32x4*)(lds + bo + 768 + j * 64);
#pragma unroll
                    for (int t = 0; t < 3; ++t) av[3 * j + t] = *(LAS f32x4*)(lds + S_TAB + min(16 * t + nn, 40) * S_TROW + g * 16 + 768 + j * 64);
                }
#pragma unroll
                for (int j = 0; j < 16; ++j)
#pragma unroll
                    for (int t = 0; t < 3; ++t) acc[t] = __builtin_amdgcn_mfma_f32_16x16x4f32(av[3 * (j >> 2) + t][j & 3], bv[j >> 2][j & 3], acc[t], 0, 0, 0);
#pragma unroll
                for (int t = 0; t < 3; ++t)
#pragma unroll
                    for (int r = 0; r < 4; ++r) *(LAS float*)(lds + S_KD + ((16 * t + 4 * g + r) * 16 + nn) * 8 + 4) = acc[t][r];
            }
        }
        WG_BARRIER();
        S4S(3)
        {
            float kv[8];
#pragma unroll
            for (int j = 0; j < 8; ++j) { const f32x2 t = *(LAS f32x2*)(lds + kd[j]); kv[j] = t.x + t.y; }
            u32x4 hi, lo;
#pragma unroll
            for (int j = 0; j < 4; ++j) { hi[j] = pack2(kv[2 * j], kv[2 * j + 1]); lo[j] = pack2(kv[2 * j] - bflo(hi[j]), kv[2 * j + 1] - bfhi(hi[j])); }
            const int o = S_K + (15 + (tid >> 2)) * 160 + (tid & 3) * 16;
            *(LAS u32x4*)(lds + o) = hi; *(LAS u32x4*)(lds + o + 64) = lo;
        }
        WG_BARRIER();
        S4S(4)
        {
            const int st0 = 2047 - nn + 8 * g - 32 * w, cc = st0 & 3;
            const int ub = S_RC + cc * S_RCS + (st0 - cc) * 2 - 3872, kb = S_K + (3 + nn - w) * 160 + g * 16;
            f32x4 y[4][2] = {};
            u32x4 F[2][16];
            auto rd = [&](int i, int f) {
                if (f & 1) { const int o = ub + 3872 - 256 * i - 1024 * (f >> 2) - 32 * ((f >> 1) & 1); const u32x2 x = *(LAS u32x2*)(lds + o), z = *(LAS u32x2*)(lds + o + 8); F[i & 1][f] = (u32x4){x.x, x.y, z.x, z.y}; }
                else F[i & 1][f] = *(LAS u32x4*)(lds + kb + (12 - 4 * i + 16 * ((f >> 1) & 3)) * 160 + (f >> 3) * 64);
            };
#pragma unroll
            for (int f = 0; f < 16; ++f) rd(0, f);
            FENCE();
#pragma unroll
            for (int i = 0; i < 4; ++i) {
                int c = 0;
#pragma unroll
                for (int hl = 0; hl < 2; ++hl)
#pragma unroll
                    for (int q = 0; q < 4; ++q)
#pragma unroll
                        for (int T = q; T < 4; ++T) {
                            y[T][0] = MFMA16(F[i & 1][4 * q + 1], F[i & 1][8 * hl + 2 * (T - q)], y[T][0]);
                            y[T][1] = MFMA16(F[i & 1][4 * q + 3], F[i & 1][8 * hl + 2 * (T - q)], y[T][1]);
                            if (i < 3 && c < 16) rd(i + 1, c);
                            ++c;
                            FENCE();
                        }
            }
#pragma unroll
            for (int T = 0; T < 4; ++T)
#pragma unroll
                for (int bt = 0; bt < 2; ++bt) *(LAS f32x4*)(lds + S_TAB + (w * 64 + 16 * T + nn) * 144 + bt * 64 + g * 16) = y[T][bt];
        }
        WG_BARRIER();
        S4S(5)
        {
            f32x4 y0 = {0, 0, 0, 0}, y1 = y0;
#pragma unroll
            for (int ww = 0; ww < 4; ++ww) {
                const int o = S_TAB + (ww * 64 + (tid >> 2)) * 144 + (tid & 3) * 32;
                y0 += *(LAS f32x4*)(lds + o); y1 += *(LAS f32x4*)(lds + o + 16);
            }
            const u32x4 u = *(LAS u32x4*)(lds + S_XR + ch * S_XRS + (255 - tid) * 16);
            st128((u32x4){pack2(y0[0] + bfhi(u.w) * dsk, y0[1] + bflo(u.w) * dsk), pack2(y0[2] + bfhi(u.z) * dsk, y0[3] + bflo(u.z) * dsk),
                          pack2(y1[0] + bfhi(u.y) * dsk, y1[1] + bflo(u.y) * dsk), pack2(y1[2] + bfhi(u.x) * dsk, y1[3] + bflo(u.x) * dsk)}, rw, tid * 16, OFF_BO + (d0 + ch) * BO_ROW);
        }
        S4S(6)
    }
    wait_init(rw, launch_tag(a), budget);
    VM_DRAIN();
    WG_BARRIER();
    if (tid == 0) st32(1u, rw, F_S4 + task * 4, 0);
}

constexpr int XT_BYTES = 32 * 1152;
constexpr int P_XT = 0;
constexpr int P_STG = 2 * XT_BYTES;
struct WSlice { u32x4 f[36]; };
constexpr int P_RING = 5;
template <class F> DI void load_wslices(WSlice& W0, WSlice& W1, const u16* s0, const u16* s1, int w, int lane, LAS char* lds, unsigned lds_base, F also) {
    constexpr int NB = 18;
    const int r0 = 8 * (lane >> 5) + ((lane & 15) >> 2);
    const int cb = (4 * w + 2 * ((lane >> 4) & 1) + ((lane & 3) >> 1)), lo8 = (lane & 1) * 8;
    auto src = [&](int i) { return (i < 9 ? s0 : s1) + (size_t)(64 * (i % 9)) * HD; };
    const u32x4 ro = ring_offs(w, lane, HD);
#pragma unroll
    for (int i = 0; i < P_RING - 1; ++i) ring_issue(src(i), ro, lds_base + P_STG + i * 16384 + w * 4096);
    also();
    static_for<NB>([&](auto ic) {
        constexpr int i = decltype(ic)::value, rem = NB - 1 - i < P_RING - 2 ? NB - 1 - i : P_RING - 2;
        ring_wait<4 * rem>();
        if constexpr (i + P_RING - 1 < NB) ring_issue(src(i + P_RING - 1), ro, lds_base + P_STG + ((i + P_RING - 1) % P_RING) * 16384 + w * 4096);
        const int sl = P_STG + (i % P_RING) * 16384;
#pragma unroll
        for (int kk = 0; kk < 4; ++kk) {
            const int ra = r0 + 16 * kk, rb = ra + 4;
            const u32x2 lo = tr_read(lds, sl + ra * 256 + ((cb ^ (2 * swz(ra))) * 16) + lo8), hi = tr_read(lds, sl + rb * 256 + ((cb ^ (2 * swz(rb))) * 16) + lo8);
            (i < 9 ? W0 : W1).f[(i % 9) * 4 + kk] = (u32x4){lo.x, lo.y, hi.x, hi.y};
        }
        u32x4* f = (i < 9 ? W0 : W1).f + (i % 9) * 4;
        if constexpr (i < 16) asm volatile("" : "+a"(f[0]), "+a"(f[1]), "+a"(f[2]), "+a"(f[3]));
    });
}
DI u32x4 piece_pi(const f32x16& a, int qq) {
    return (u32x4){pack2(a[4 * qq], a[4 * qq + 8]), pack2(a[4 * qq + 1], a[4 * qq + 9]), pack2(a[4 * qq + 2], a[4 * qq + 10]), pack2(a[4 * qq + 3], a[4 * qq + 11])};
}
DI u32x4 piece_seq(const f32x16& a, int s2) {
    return (u32x4){pack2(a[8 * s2], a[8 * s2 + 1]), pack2(a[8 * s2 + 2], a[8 * s2 + 3]), pack2(a[8 * s2 + 4], a[8 * s2 + 5]), pack2(a[8 * s2 + 6], a[8 * s2 + 7])};
}
struct XTile {
    int xs[9], xa[4];
    DI void init(int w, int lane) {
#pragma unroll
        for (int i = 0; i < 9; ++i) { const int L = (i * 4 + w) * 64 + lane, r = L / 72, cs = L - 72 * r; xs[i] = r * 1152 + (cs ^ ((r >> 1) & 7)) * 16; }
        const int c32 = lane & 31, hf = lane >> 5;
#pragma unroll
        for (int j = 0; j < 4; ++j) xa[j] = P_XT + c32 * 1152 + (((2 * j + hf) ^ ((c32 >> 1) & 7)) * 16);
    }
    DI void dma(const Args& a, unsigned lds_base, int w, int s, int buf, int i) const {
        glds16((const char*)a.x + (size_t)min(s, NSEG - 1) * XT_BYTES, xs[i], lds_base + P_XT + buf * XT_BYTES + (i * 4 + w) * 1024);
    }
    DI void first(const Args& a, unsigned lds_base, int w, int lane, int s) {
        init(w, lane);
#pragma unroll
        for (int i = 0; i < 9; ++i) dma(a, lds_base, w, s, 0, i);
    }
};
template <bool TR, class F> DI void project(f32x16& acc0, f32x16& acc1, const WSlice& W0, const WSlice& W1, const XTile& X, const Args& a, LAS char* lds, unsigned lds_base, int w, int buf, int sn, F mid) {
    const int bo = buf * XT_BYTES;
    constexpr int PD = 5;
    u32x4 xf[PD];
#pragma unroll
    for (int i = 0; i < PD; ++i) xf[i] = *(LAS u32x4*)(lds + X.xa[i & 3] + bo + (i >> 2) * 128);
#pragma unroll
    for (int ks = 0; ks < 36; ++ks) {
        if (ks == 0) asm volatile("v_mfma_f32_32x32x16_bf16 %0, %1, %2, 0" : "=&v"(acc0) : "a"(W0.f[0]), "v"(xf[0]) : "memory");
        else asm volatile("v_mfma_f32_32x32x16_bf16 %0, %1, %2, %0" : "+v"(acc0) : "a"(W0.f[ks]), "v"(xf[ks % PD]) : "memory");
        if (TR) {
            if (ks == 0) asm volatile("v_mfma_f32_32x32x16_bf16 %0, %2, %1, 0" : "=&v"(acc1) : "a"(W1.f[0]), "v"(xf[0]) : "memory");
            else if (ks < 28) asm volatile("v_mfma_f32_32x32x16_bf16 %0, %2, %1, %0" : "+v"(acc1) : "a"(W1.f[ks]), "v"(xf[ks % PD]) : "memory");
            else asm volatile("v_mfma_f32_32x32x16_bf16 %0, %2, %1, %0" : "+v"(acc1) : "v"(W1.f[ks]), "v"(xf[ks % PD]) : "memory");
        } else {
            if (ks == 0) asm volatile("v_mfma_f32_32x32x16_bf16 %0, %1, %2, 0" : "=&v"(acc1) : "a"(W1.f[0]), "v"(xf[0]) : "memory");
            else if (ks < 28) asm volatile("v_mfma_f32_32x32x16_bf16 %0, %1, %2, %0" : "+v"(acc1) : "a"(W1.f[ks]), "v"(xf[ks % PD]) : "memory");
            else asm volatile("v_mfma_f32_32x32x16_bf16 %0, %1, %2, %0" : "+v"(acc1) : "v"(W1.f[ks]), "v"(xf[ks % PD]) : "memory");
        }
        if (ks + PD < 36) xf[ks % PD] = *(LAS u32x4*)(lds + X.xa[(ks + PD) & 3] + bo + ((ks + PD) >> 2) * 128);
        if (ks < 18 && (ks & 1)) X.dma(a, lds_base, w, sn, buf ^ 1, ks >> 1);
        if (ks == (TR ? 14 : 26)) mid();
    }
    asm volatile("s_nop 7\n\ts_nop 3" : "+v"(acc0), "+v"(acc1) :: "memory");
}
DI void role_kv(const Args& a, LAS char* lds, unsigned lds_base, int h, rsrc_t rw, int& budget) {
    const int tid = threadIdx.x, lane = tid & 63, w = __builtin_amdgcn_readfirstlane(tid >> 6);
    WSlice W0, W1;
    XTile X;
    u32x2 tag;
    load_wslices(W0, W1, a.Wk + h * DH, a.Wv + h * DH, w, lane, lds, lds_base, [&]() { X.first(a, lds_base, w, lane, 0); tag = launch_tag(a); });
    VM_DRAIN();
    WG_BARRIER();
    const int oo = h * 8192 + w * 2048 + lane * 16;
    const int fo = lane == 0 ? F_KV + h * 512 + w * 128 : OOB;
    for (int s = 0; s < NSEG; ++s) {
        f32x16 acc0, acc1;
        project<true>(acc0, acc1, W0, W1, X, a, lds, lds_base, w, s & 1, s + 1, [&]() {
            asm volatile("s_waitcnt vmcnt(7)" ::: "memory");
            st32((unsigned)(s - 1), rw, s ? fo : OOB, 0);
        });
        const int so = s * (NH * 8192);
        st128(piece_pi(acc0, 0), rw, oo, OFF_KF + so);
        st128(piece_pi(acc0, 1), rw, oo + 1024, OFF_KF + so);
        st128(piece_seq(acc1, 0), rw, oo, OFF_VF + so);
        st128(piece_seq(acc1, 1), rw, oo + 1024, OFF_VF + so);
        if (s == 0) { wait_init(rw, tag, budget); VM_DRAIN(); }
        asm volatile("s_waitcnt vmcnt(4)" ::: "memory");
        WG_BARRIER();
        if (s == 0) __builtin_amdgcn_raw_buffer_store_b64(tag, rw, tid == 0 ? F_INIT + (33 + h) * 128 : OOB, 0, 16);
    }
    VM_DRAIN();
    st32(NSEG, rw, fo, 0);
}

DI int prog_min(rsrc_t rw, int off) {
    const int v = (int)ld32(rw, off, 0);
    asm volatile("" ::: "memory");
    return -wave_max32(-v);
}
DI float red_max_g(float v) {
    auto a = __builtin_amdgcn_permlane16_swap(__float_as_uint(v), __float_as_uint(v), false, false);
    v = fmaxf(__uint_as_float(a[0]), __uint_as_float(a[1]));
    auto b = __builtin_amdgcn_permlane32_swap(__float_as_uint(v), __float_as_uint(v), false, false);
    return fmaxf(__uint_as_float(b[0]), __uint_as_float(b[1]));
}
#define M16 "v_mfma_f32_16x16x32_bf16 "
DI void role_qa(const Args& a, LAS char* lds, unsigned lds_base, int h0, int s0, int s1, rsrc_t rw, int& budget) {
    const int tid = threadIdx.x, lane = tid & 63, w = __builtin_amdgcn_readfirstlane(tid >> 6);
    const int n = lane & 15, g = lane >> 4, h = h0 + (w >> 1), tt = w & 1;
    WSlice W0, W1;
    XTile X;
    load_wslices(W0, W1, a.Wq + h0 * DH, a.Wq + (h0 + 1) * DH, w, lane, lds, lds_base, [&]() { X.first(a, lds_base, w, lane, s0); });
    const int oo = OFF_OF + (2 * tt + (n >> 3)) * 65536 + h * 2048 + g * 128 + (n & 7) * 16;
    const bool fast = s0 >= FAST;
    if (fast)
#pragma unroll
        for (int t = 0; t < 8; ++t) st128((u32x4){SENT, SENT, SENT, SENT}, rw, oo + (t & 3) * 512, (s0 + (t >> 2) * 2) * 262144);
    VM_DRAIN();
    WG_BARRIER();
    constexpr int Q_Q = P_STG, Q_KV = Q_Q + 16384, Q_M = Q_KV + 32768;
    static_assert(Q_M + 16384 <= P_STG + P_RING * 16384, "");
    const int fk = F_KV + h * 512 + (lane & 3) * 128, fm = F_KVM + h * 256 + (lane & 1) * 128;
    int pm_, m_rdy = -1, kv_rdy = -1;
    auto look = [&]() { pm_ = (int)ld32(rw, lane & 4 ? fk : fm, 0); };
    auto seen = [&]() { auto rl = [&](int i) { return __builtin_amdgcn_readlane(pm_, i); }; m_rdy = min(rl(0), rl(1)); kv_rdy = min(min(rl(4), rl(5)), min(rl(6), rl(7))); };
    auto wait_m = [&](int thr, unsigned& t) {
        for (;;) {
            const u32x2 v = ld64(rw, fm, 0);
            asm volatile("" ::: "memory");
            t = now();
            const int p0 = __builtin_amdgcn_readlane(v.x, 0), p1 = __builtin_amdgcn_readlane(v.x, 1);
            const int d0 = (int)(__builtin_amdgcn_readlane(v.y, 0) - 8 * t) >> 3, d1 = (int)(__builtin_amdgcn_readlane(v.y, 1) - 8 * t) >> 3;
            if (min(p0, p1) >= thr || --budget < 0) return max(p0 == thr ? d0 : -1000, p1 == thr ? d1 : -1000);
            (void)wait_until(t + min(max(min(d0, d1) + (int)(a.xa >> 16 & 255), 10), 200));
        }
    };
    auto proj = [&](int s) {
        f32x16 acc0, acc1;
        project<false>(acc0, acc1, W0, W1, X, a, lds, lds_base, w, ((s - s0) >> 1) & 1, s + 2, look);
        const int o = Q_Q + w * 2048 + lane * 16;
        *(LAS u32x4*)(lds + o) = piece_pi(acc0, 0); *(LAS u32x4*)(lds + o + 1024) = piece_pi(acc0, 1);
        *(LAS u32x4*)(lds + o + 8192) = piece_pi(acc1, 0); *(LAS u32x4*)(lds + o + 8192 + 1024) = piece_pi(acc1, 1);
    };
    const int fs = (tt ? OFF_VF : OFF_KF) + (g >> 1) * 1024 + ((g & 1) * 32 + 16 * (n >> 3) + (n & 7)) * 16;
    auto fetch = [&](int s) {
        const int so = (min(s, NSEG - 1) * NH + h) * 8192;
#pragma unroll
        for (int f = 0; f < 8; ++f) bdma(rw, fs, so + (f >> 1) * 2048 + (f & 1) * 128, lds_base + Q_KV + w * 8192 + f * 1024);
    };
    const int ms = OFF_KM + (lane & 7) * 256 + (lane >> 3) * 16;
    auto fetch_m = [&](int s) {
        const int so = (s * NH + h) * 2048;
        const unsigned d = lds_base + Q_M + w * 4096;
        bdma(rw, ms, so, d); bdma(rw, ms, so + 128, d + 1024); bdma(rw, lane * 16, OFF_VM + so, d + 2048); bdma(rw, lane * 16, OFF_VM + so + 1024, d + 3072);
    };
    const float c = 0.08838834764831845f * 1.4426950408889634f;
    u32x4 qf[4];
    f32x4 o[8]; float mx, l;
    auto xpart = [&]() {
        const int kb = Q_KV + (w >> 1) * 16384 + lane * 16;
        u32x4 kn[8], vn[8];
#pragma unroll
        for (int ks = 0; ks < 4; ++ks) qf[ks] = *(LAS u32x4*)(lds + Q_Q + (w >> 1) * 8192 + (2 * ks + (g >> 1)) * 1024 + ((g & 1) * 32 + 16 * tt + n) * 16);
#pragma unroll
        for (int f = 0; f < 8; ++f) kn[f] = *(LAS u32x4*)(lds + kb + f * 1024);
        FENCE();
#pragma unroll
        for (int f = 0; f < 8; ++f) vn[f] = *(LAS u32x4*)(lds + kb + 8192 + f * 1024);
        f32x4 s0, s1;
        asm volatile(M16 "%0, %2, %10, 0\n\t" M16 "%1, %3, %10, 0\n\t" M16 "%0, %4, %11, %0\n\t" M16 "%1, %5, %11, %1\n\t"
                     M16 "%0, %6, %12, %0\n\t" M16 "%1, %7, %12, %1\n\t" M16 "%0, %8, %13, %0\n\t" M16 "%1, %9, %13, %1\n\ts_nop 7"
                     : "=&v"(s0), "=&v"(s1) : "v"(kn[0]), "v"(kn[1]), "v"(kn[2]), "v"(kn[3]), "v"(kn[4]), "v"(kn[5]), "v"(kn[6]), "v"(kn[7]), "v"(qf[0]), "v"(qf[1]), "v"(qf[2]), "v"(qf[3]));
        float p[8];
        mx = -3.0e38f; l = 0.f;
#pragma unroll
        for (int r = 0; r < 8; ++r) { const int key = 16 * (g >> 1) + 4 * (g & 1) + (r & 3) + 8 * (r >> 2); p[r] = key <= 16 * tt + n ? (r < 4 ? s0[r & 3] : s1[r & 3]) : -3.0e38f; mx = fmaxf(mx, p[r]); }
        mx = red_max_g(mx);
#pragma unroll
        for (int r = 0; r < 8; ++r) { p[r] = __builtin_amdgcn_exp2f((p[r] - mx) * c); l += p[r]; }
        const u32x4 pf = {pack2(p[0], p[1]), pack2(p[2], p[3]), pack2(p[4], p[5]), pack2(p[6], p[7])};
        asm volatile("s_nop 1\n\t" M16 "%0, %8, %16, 0\n\t" M16 "%1, %9, %16, 0\n\t" M16 "%2, %10, %16, 0\n\t" M16 "%3, %11, %16, 0\n\t"
                     M16 "%4, %12, %16, 0\n\t" M16 "%5, %13, %16, 0\n\t" M16 "%6, %14, %16, 0\n\t" M16 "%7, %15, %16, 0\n\ts_nop 7"
                     : "=&v"(o[0]), "=&v"(o[1]), "=&v"(o[2]), "=&v"(o[3]), "=&v"(o[4]), "=&v"(o[5]), "=&v"(o[6]), "=&v"(o[7])
                     : "v"(vn[0]), "v"(vn[1]), "v"(vn[2]), "v"(vn[3]), "v"(vn[4]), "v"(vn[5]), "v"(vn[6]), "v"(vn[7]), "v"(pf));
    };
    proj(s0);
    if (!fast) wait_init(rw, launch_tag(a), budget);
    wait_prog(rw, fk, true, fast ? s1 : s0, budget);
    fetch(s0);
    VM_DRAIN();
    WG_BARRIER();
    xpart();
    WG_BARRIER();
    proj(s0 + 2);
    const int fo = lane == 0 ? F_O + h * 128 + (2 * s0 + tt) * 4 : OOB;
    bool pre = false;
    for (int s = s0; s <= s1; s += 2) {
        S4S(0)
        const int mb = Q_M + w * 4096;
        u32x4 kf[4]; u32x2 vm[4];
        auto frags = [&]() {
#pragma unroll
            for (int ks = 0; ks < 4; ++ks) kf[ks] = *(LAS u32x4*)(lds + mb + (4 * ks + g) * 128 + (2 * (n >> 2) + (n & 1)) * 16);
#pragma unroll
            for (int t = 0; t < 4; ++t) { const int vo = mb + 2048 + t * 512 + (n >> 3) * 256 + g * 64 + (n & 7) * 4; vm[t] = (u32x2){*(LAS unsigned*)(lds + vo), *(LAS unsigned*)(lds + vo + 32)}; }
        };
        if (fast) {
            TT(s == s0, 3)
            if (s < s1) fetch(s1);
            unsigned t;
            const int d = min(wait_m(min(s - 1, NSEG - 3), t) + (int)(a.xa >> (s == NSEG - 1 ? 8 : 0) & 255), 500);
            st32((t + max(d, 0) + (a.xb >> 8 * (s - FAST) & 255)) & 0x7FFFFFFFu, rw, lane == 0 ? F_H + ((2 * (s - FAST) + tt) * NH + h) * 128 : OOB, 0);
            (void)wait_until(t + d);
            TT(true, 0)
            for (;;) {
                fetch_m(s);
                VM_DRAIN();
                frags();
                unsigned c = 0;
#pragma unroll
                for (int i = 0; i < 4; ++i) c = max(max(mx4(c, kf[i]), vm[i].x), vm[i].y);
                if (!__any(c == SENT) || --budget < 0) break;
            }
        } else {
            if (!pre) {
                seen();
                unsigned t;
                if (m_rdy < s) { const int d = wait_m(s, t); (void)wait_until(t + min(d + (int)(a.xa >> 24), 300)); }
                fetch_m(s);
                if (kv_rdy < s + 2) wait_prog(rw, fk, true, s + 2, budget);
                fetch(s + 2);
            }
            S4S(1)
            asm volatile("s_waitcnt vmcnt(8)" ::: "memory");
            frags();
        }
        TT(s == s1, 1)
        S4S(2)
        f32x4 sm;
        asm volatile(M16 "%0, %1, %5, 0\n\t" M16 "%0, %2, %6, %0\n\t" M16 "%0, %3, %7, %0\n\t" M16 "%0, %4, %8, %0\n\ts_nop 7"
                     : "=&v"(sm) : "v"(kf[0]), "v"(kf[1]), "v"(kf[2]), "v"(kf[3]), "v"(qf[0]), "v"(qf[1]), "v"(qf[2]), "v"(qf[3]));
        const float m2 = red_max_g(fmaxf(mx, fmaxf(sm[0], sm[1])));
        const float al = __builtin_amdgcn_exp2f((mx - m2) * c), p0 = __builtin_amdgcn_exp2f((sm[0] - m2) * c), p1 = __builtin_amdgcn_exp2f((sm[1] - m2) * c);
        const float inv = __builtin_amdgcn_rcpf(red_sum_g(l * al + p0 + p1)), ai = al * inv;
#pragma unroll
        for (int t = 0; t < 8; ++t) o[t] *= ai;
        const unsigned pm = pack2(p0 * inv, p1 * inv);
#define M1K "v_mfma_f32_16x16x16_bf16 "
        asm volatile("s_nop 1\n\t" M1K "%0, %8, %12, %0\n\t" M1K "%1, %8, %13, %1\n\t" M1K "%2, %9, %12, %2\n\t" M1K "%3, %9, %13, %3\n\t"
                     M1K "%4, %10, %12, %4\n\t" M1K "%5, %10, %13, %5\n\t" M1K "%6, %11, %12, %6\n\t" M1K "%7, %11, %13, %7\n\ts_nop 7"
                     : "+v"(o[0]), "+v"(o[1]), "+v"(o[2]), "+v"(o[3]), "+v"(o[4]), "+v"(o[5]), "+v"(o[6]), "+v"(o[7])
                     : "v"(vm[0]), "v"(vm[1]), "v"(vm[2]), "v"(vm[3]), "v"((u32x2){pm, 0u}), "v"((u32x2){0u, pm}));
#pragma unroll
        for (int b = 0; b < 2; ++b) {
            u32x4 x, y;
#pragma unroll
            for (int j = 0; j < 4; ++j) { x[j] = pack2(o[4 * b + (j >> 1)][2 * (j & 1)], o[4 * b + (j >> 1)][2 * (j & 1) + 1]); y[j] = pack2(o[4 * b + 2 + (j >> 1)][2 * (j & 1)], o[4 * b + 2 + (j >> 1)][2 * (j & 1) + 1]); }
            if (s == NSEG - 1) { const u32x4 z = {x.x, x.z, y.x, y.z}; y = (u32x4){x.y, x.w, y.y, y.w}; x = z; }
            st128(x, rw, oo + b * 1024, s * 262144); st128(y, rw, oo + b * 1024 + 512, s * 262144);
        }
        TT(s == s1, 2)
        S4S(3)
        if (s < s1) {
            asm volatile("s_waitcnt vmcnt(4)" ::: "memory");
            WG_BARRIER();
            S4S(4)
            look();
            xpart();
        }
        TT(s == s0, 4)
        if (fast) continue;
        FENCE();
        S4S(5)
        seen();
        VM_DRAIN();
        st32((unsigned)s, rw, fo, 0);
        TT(s == s1, 3)
        TT(s == s1 - 2, 4)
        S4S(6)
        WG_BARRIER();
        pre = m_rdy >= s + 2 && kv_rdy >= s + 4 && s < s1;
        if (pre) { fetch_m(s + 2); fetch(s + 4); }
        S4S(7)
        if (s + 4 <= s1) proj(s + 4);
        S4S(8)
    }
}

#define STEP_ASM \
    "v_readfirstlane_b32 s36, v0\n" \
    "v_readfirstlane_b32 s37, v1\n" \
    "v_readfirstlane_b32 s38, v2\n" \
    "v_readfirstlane_b32 s39, v3\n" \
    "v_readfirstlane_b32 s40, v4\n" \
    "v_readfirstlane_b32 s41, v5\n" \
    "v_readfirstlane_b32 s42, v6\n" \
    "v_readfirstlane_b32 s43, v7\n" \
    "v_readfirstlane_b32 s28, v8\n" \
    "v_readfirstlane_b32 s29, v9\n" \
    "v_readfirstlane_b32 s30, v10\n" \
    "v_readfirstlane_b32 s31, v11\n" \
    "v_readfirstlane_b32 s32, v12\n" \
    "v_readfirstlane_b32 s33, v13\n" \
    "v_readfirstlane_b32 s34, v14\n" \
    "v_readfirstlane_b32 s35, v15\n" \
    "v_readfirstlane_b32 s24, v16\n" \
    "v_readfirstlane_b32 s25, v17\n" \
    "v_readfirstlane_b32 s26, v18\n" \
    "v_readfirstlane_b32 s27, v19\n" \
    "v_readfirstlane_b32 s60, v24\n" \
    "v_readfirstlane_b32 s61, v25\n" \
    "v_readfirstlane_b32 s62, v26\n" \
    "v_readfirstlane_b32 s63, v27\n" \
    "v_readfirstlane_b32 s64, v28\n" \
    "v_readfirstlane_b32 s65, v29\n" \
    "v_readfirstlane_b32 s66, v30\n" \
    "v_readfirstlane_b32 s67, v31\n" \
    "v_mov_b64 v[40:41], 0\n" \
    "v_mov_b64 v[42:43], 0\n" \
    "v_mov_b64 v[44:45], 0\n" \
    "v_mov_b64 v[46:47], 0\n" \
    "v_mov_b64 v[48:49], 0\n" \
    "v_mov_b64 v[50:51], 0\n" \
    "v_mov_b64 v[52:53], 0\n" \
    "v_mov_b64 v[54:55], 0\n" \
    "s_mov_b32 s68, m0\n" \
    "s_getpc_b64 s[96:97]\n" \
    "14:\n" \
    "s_add_u32 s96, s96, 2f-14b\n" \
    "s_addc_u32 s97, s97, 0\n" \
    "s_lshl_b32 s72, s60, 10\n" \
    "s_add_i32 s73, s72, 4096\n" \
    "s_lshr_b32 s74, s72, 2\n" \
    "s_add_i32 s74, s74, 8192\n" \
    "s_lshl_b32 s95, s72, 1\n" \
    "s_add_i32 s95, s95, 16384\n" \
    "s_mov_b32 s81, 0x3e0293ee\n" \
    "s_mov_b32 s82, 0x53800000\n" \
    "s_lshl_b32 s83, s60, 6\n" \
    "s_and_b32 s83, s83, 128\n" \
    "v_mov_b32 v109, 2.0\n" \
    "0:\n" \
    "s_and_b32 s75, s36, 3\n" \
    "s_lshl_b32 s75, s75, 17\n" \
    "s_add_i32 s75, s75, s26\n" \
    "s_add_i32 s69, s75, s72\n" \
    "s_add_i32 s70, s69, 4096\n" \
    "s_add_i32 s71, s75, 16384\n" \
    "s_mov_b32 m0, s72\n" \
    "s_cmp_eq_u32 s36, 0\n" \
    "s_cbranch_scc1 3f\n" \
    "12:\n" \
    "s_sub_i32 s90, s37, s29\n" \
    "s_mul_i32 s90, s90, s25\n" \
    "s_ashr_i32 s90, s90, 12\n" \
    "s_add_i32 s90, s90, s24\n" \
    "s_add_i32 s90, s90, -116\n" \
    "1:\n" \
    "s_memtime vcc\n" \
    "s_waitcnt lgkmcnt(0)\n" \
    "s_sub_i32 s75, s90, vcc_lo\n" \
    "s_cmp_gt_i32 s75, 112\n" \
    "s_cbranch_scc1 1b\n" \
    "s_max_i32 s75, s75, 0\n" \
    "s_and_b32 s75, s75, -4\n" \
    "s_sub_u32 vcc_lo, s96, s75\n" \
    "s_subb_u32 vcc_hi, s97, 0\n" \
    "s_setpc_b64 vcc\n" \
    "s_nop 0\n" \
    "s_nop 0\n" \
    "s_nop 0\n" \
    "s_nop 0\n" \
    "s_nop 0\n" \
    "s_nop 0\n" \
    "s_nop 0\n" \
    "s_nop 0\n" \
    "s_nop 0\n" \
    "s_nop 0\n" \
    "s_nop 0\n" \
    "s_nop 0\n" \
    "s_nop 0\n" \
    "s_nop 0\n" \
    "s_nop 0\n" \
    "s_nop 0\n" \
    "s_nop 0\n" \
    "s_nop 0\n" \
    "s_nop 0\n" \
    "s_nop 0\n" \
    "s_nop 0\n" \
    "s_nop 0\n" \
    "s_nop 0\n" \
    "s_nop 0\n" \
    "s_nop 0\n" \
    "s_nop 0\n" \
    "s_nop 0\n" \
    "s_nop 0\n" \
    "2:\n" \
    "buffer_load_dwordx4 v127, %[rs], s69 offen sc1 lds\n" \
    "s_mov_b32 m0, s73\n" \
    "s_nop 0\n" \
    "buffer_load_dwordx4 v127, %[rs], s70 offen sc1 lds\n" \
    "s_movk_i32 m0, 0x2000\n" \
    "s_mov_b32 exec_lo, s27\n" \
    "s_mov_b32 exec_hi, s27\n" \
    "buffer_load_dwordx4 v127, %[rs], s71 offen sc1 lds\n" \
    "s_mov_b64 exec, -1\n" \
    "v_bfe_u32 v0, v127, 8, 2\n" \
    "v_subrev_u32 v0, s60, v0\n" \
    "v_mul_i32_i24 v0, -1088, v0\n" \
    "v_add_u32 v0, v0, v129\n" \
    "v_lshrrev_b32 v1, 2, v127\n" \
    "s_mov_b32 exec_lo, s66\n" \
    "s_mov_b32 exec_hi, s66\n" \
    "ds_read2_b32 v[96:97], v0 offset0:0 offset1:144\n" \
    "s_mov_b64 exec, -1\n" \
    "s_lshl_b32 s80, s36, 16\n" \
    "s_add_i32 s80, s80, s67\n" \
    "v_mov_b64 v[40:41], 0\n" \
    "v_mov_b64 v[42:43], 0\n" \
    "v_mov_b64 v[44:45], 0\n" \
    "v_mov_b64 v[46:47], 0\n" \
    "v_mov_b64 v[48:49], 0\n" \
    "v_mov_b64 v[50:51], 0\n" \
    "v_mov_b64 v[52:53], 0\n" \
    "v_mov_b64 v[54:55], 0\n" \
    "v_or_b32 v109, s60, v127\n" \
    "v_min_u32 v109, 1, v109\n" \
    "v_lshlrev_b32 v109, 30, v109\n" \
    "s_waitcnt lgkmcnt(0)\n" \
    "buffer_store_dword v96, v1, %[rw], s80 offen sc1\n" \
    "buffer_store_dword v97, v1, %[rw], s80 offen offset:256 sc1\n" \
    "3:\n" \
    "ds_read_b128 v[72:75], v133 offset:0\n" \
    "ds_read_b128 v[76:79], v133 offset:1024\n" \
    "ds_read_b128 v[80:83], v133 offset:2048\n" \
    "ds_read_b128 v[84:87], v133 offset:3072\n" \
    "ds_read_b128 v[88:91], v133 offset:49152\n" \
    "ds_read_b128 v[92:95], v133 offset:50176\n" \
    "s_add_i32 s91, s36, 1\n" \
    "s_and_b32 s78, s36, 3\n" \
    "s_lshl_b32 s78, s78, 14\n" \
    "s_add_i32 s77, s78, s61\n" \
    "s_bitcmp1_b32 s36, 0\n" \
    "s_cselect_b32 s79, 0x90000, 0\n" \
    "s_lshl_b32 s92, s91, 18\n" \
    "s_add_i32 s92, s92, s64\n" \
    "s_mov_b32 m0, s95\n" \
    "s_add_i32 s78, s78, 0x1a0000\n" \
    "s_add_i32 s93, s92, 0x1000000\n" \
    "v_mov_b64 v[56:57], 0\n" \
    "v_mov_b64 v[58:59], 0\n" \
    "v_mov_b64 v[60:61], 0\n" \
    "v_mov_b64 v[62:63], 0\n" \
    "v_mov_b64 v[64:65], 0\n" \
    "v_mov_b64 v[66:67], 0\n" \
    "v_mov_b64 v[68:69], 0\n" \
    "v_mov_b64 v[70:71], 0\n" \
    "v_mov_b64 v[98:99], 0\n" \
    "v_mov_b32 v101, 0\n" \
    "v_mov_b32 v106, 0\n" \
    "v_mov_b32 v103, s91\n" \
    "s_sub_i32 s76, s40, s24\n" \
    "s_min_i32 s94, s91, s38\n" \
    "s_lshl_b32 s94, s94, 18\n" \
    "s_andn2_b32 s80, 0x1000000, s66\n" \
    "s_add_i32 s94, s94, s64\n" \
    "s_add_i32 s94, s94, s80\n" \
    "v_lshlrev_b32 v107, 1, v127\n" \
    "s_waitcnt vmcnt(2) lgkmcnt(0)\n" \
    "s_barrier\n" \
    "ds_read_b128 v[0:3], v125 offset:0\n" \
    "s_memtime s[86:87]\n" \
    "v_mfma_f32_16x16x32_bf16 v[20:23], v[120:123], v[120:123], 0\n" \
    "ds_read_b128 v[4:7], v125 offset:1024\n" \
    "ds_read_b128 v[8:11], v125 offset:2048\n" \
    "v_mfma_f32_16x16x32_bf16 v[20:23], v[120:123], v[120:123], 0\n" \
    "ds_read_b128 v[12:15], v125 offset:3072\n" \
    "buffer_load_dword v110, v107, %[rw], s94 offen sc1\n" \
    "v_mfma_f32_16x16x32_bf16 v[20:23], v[120:123], v[120:123], 0\n" \
    "s_waitcnt lgkmcnt(3)\n" \
    "v_smfmac_f32_16x16x64_bf16 v[40:43], v[0:3], a[0:7], v124\n" \
    "ds_read_b128 v[16:19], v125 offset:4096\n" \
    "ds_read_b128 v[24:27], v125 offset:5120\n" \
    "v_smfmac_f32_16x16x64_bf16 v[44:47], v[0:3], a[8:15], v124\n" \
    "ds_read_b128 v[28:31], v125 offset:6144\n" \
    "ds_read_b128 v[32:35], v125 offset:7168\n" \
    "v_smfmac_f32_16x16x64_bf16 v[56:59], v[0:3], v[72:79], v124\n" \
    "ds_read_b128 v[36:39], v126 offset:0\n" \
    "v_smfmac_f32_16x16x64_bf16 v[60:63], v[0:3], v[80:87], v124\n" \
    "s_waitcnt lgkmcnt(7)\n" \
    "v_smfmac_f32_16x16x64_bf16 v[40:43], v[4:7], a[16:23], v124\n" \
    "ds_read_b128 v[72:75], v133 offset:16384\n" \
    "ds_read_b128 v[76:79], v133 offset:17408\n" \
    "v_max3_u32 v106, v106, v0, v1\n" \
    "v_smfmac_f32_16x16x64_bf16 v[44:47], v[4:7], a[24:31], v124\n" \
    "ds_read_b128 v[80:83], v133 offset:18432\n" \
    "ds_read_b128 v[84:87], v133 offset:19456\n" \
    "v_max3_u32 v106, v106, v2, v3\n" \
    "v_smfmac_f32_16x16x64_bf16 v[56:59], v[4:7], v[88:95], v124\n" \
    "v_max3_u32 v106, v106, v4, v5\n" \
    "v_smfmac_f32_16x16x64_bf16 v[60:63], v[4:7], v[168:175], v124\n" \
    "s_waitcnt lgkmcnt(4)\n" \
    "v_max3_u32 v106, v106, v6, v7\n" \
    "v_smfmac_f32_16x16x64_bf16 v[40:43], v[8:11], a[32:39], v124\n" \
    "ds_read_b128 v[88:91], v133 offset:32768\n" \
    "ds_read_b128 v[92:95], v133 offset:33792\n" \
    "v_max3_u32 v106, v106, v8, v9\n" \
    "v_smfmac_f32_16x16x64_bf16 v[44:47], v[8:11], a[40:47], v124\n" \
    "v_max3_u32 v106, v106, v10, v11\n" \
    "v_smfmac_f32_16x16x64_bf16 v[56:59], v[8:11], v[176:183], v124\n" \
    "v_max3_u32 v106, v106, v12, v13\n" \
    "v_smfmac_f32_16x16x64_bf16 v[60:63], v[8:11], v[184:191], v124\n" \
    "v_max3_u32 v106, v106, v14, v15\n" \
    "v_smfmac_f32_16x16x64_bf16 v[40:43], v[12:15], a[48:55], v124\n" \
    "v_max3_u32 v106, v106, v16, v17\n" \
    "v_smfmac_f32_16x16x64_bf16 v[44:47], v[12:15], a[56:63], v124\n" \
    "s_waitcnt lgkmcnt(0)\n" \
    "v_max3_u32 v106, v106, v18, v19\n" \
    "v_smfmac_f32_16x16x64_bf16 v[56:59], v[12:15], v[72:79], v124\n" \
    "s_add_i32 s80, s86, s76\n" \
    "s_mul_hi_u32 s80, s80, s41\n" \
    "v_max3_u32 v106, v106, v24, v25\n" \
    "v_smfmac_f32_16x16x64_bf16 v[40:43], v[16:19], a[64:71], v124\n" \
    "ds_read_b128 v[72:75], v133 offset:34816\n" \
    "ds_read_b128 v[76:79], v133 offset:35840\n" \
    "v_max3_u32 v106, v106, v26, v27\n" \
    "v_smfmac_f32_16x16x64_bf16 v[44:47], v[16:19], a[72:79], v124\n" \
    "s_add_i32 s80, s80, s29\n" \
    "v_max3_u32 v106, v106, v28, v29\n" \
    "v_smfmac_f32_16x16x64_bf16 v[40:43], v[24:27], a[80:87], v124\n" \
    "v_max3_u32 v106, v106, v30, v31\n" \
    "v_smfmac_f32_16x16x64_bf16 v[44:47], v[24:27], a[88:95], v124\n" \
    "v_max3_u32 v106, v106, v32, v33\n" \
    "v_smfmac_f32_16x16x64_bf16 v[40:43], v[28:31], a[96:103], v124\n" \
    "v_max3_u32 v106, v106, v34, v35\n" \
    "v_smfmac_f32_16x16x64_bf16 v[44:47], v[28:31], a[104:111], v124\n" \
    "ds_read_b128 v[20:23], v127 offset:19456\n" \
    "v_max3_u32 v106, v106, v36, v37\n" \
    "v_smfmac_f32_16x16x64_bf16 v[40:43], v[32:35], a[112:119], v124\n" \
    "v_max3_u32 v106, v106, v38, v39\n" \
    "v_smfmac_f32_16x16x64_bf16 v[44:47], v[32:35], a[120:127], v124\n" \
    "v_cmp_eq_u32 vcc, -1, v106\n" \
    "v_smfmac_f32_16x16x64_bf16 v[40:43], v[36:39], a[128:135], v124\n" \
    "v_add_u32 v106, s63, v133\n" \
    "v_smfmac_f32_16x16x64_bf16 v[44:47], v[36:39], a[136:143], v124\n" \
    "s_cbranch_vccnz 5f\n" \
    "6:\n" \
    "v_mov_b32 v102, s80\n" \
    "v_smfmac_f32_16x16x64_bf16 v[60:63], v[12:15], v[80:87], v124\n" \
    "s_waitcnt lgkmcnt(1)\n" \
    "v_add_f32 v40, v40, v42\n" \
    "v_smfmac_f32_16x16x64_bf16 v[56:59], v[16:19], v[88:95], v124\n" \
    "buffer_store_dwordx2 v[102:103], v109, %[rs], s77 offen sc1\n" \
    "v_add_f32 v41, v41, v43\n" \
    "v_smfmac_f32_16x16x64_bf16 v[60:63], v[16:19], v[72:79], v124\n" \
    "v_add_f32 v44, v44, v46\n" \
    "v_smfmac_f32_16x16x64_bf16 v[48:51], v[0:3], a[144:151], v124\n" \
    "v_add_f32 v45, v45, v47\n" \
    "v_smfmac_f32_16x16x64_bf16 v[52:55], v[0:3], a[152:159], v124\n" \
    "v_mul_f32 v40, s81, v40\n" \
    "v_smfmac_f32_16x16x64_bf16 v[48:51], v[4:7], a[160:167], v124\n" \
    "ds_read_b128 v[0:3], v127 offset:16384\n" \
    "v_mul_f32 v44, s81, v44\n" \
    "v_smfmac_f32_16x16x64_bf16 v[52:55], v[4:7], a[168:175], v124\n" \
    "v_mul_f32 v41, s81, v41\n" \
    "v_smfmac_f32_16x16x64_bf16 v[48:51], v[8:11], a[176:183], v124\n" \
    "ds_read_b128 v[4:7], v127 offset:18432\n" \
    "v_mul_f32 v45, s81, v45\n" \
    "v_smfmac_f32_16x16x64_bf16 v[52:55], v[8:11], a[184:191], v124\n" \
    "v_cvt_pk_bf16_f32 v107, v40, v44\n" \
    "v_smfmac_f32_16x16x64_bf16 v[48:51], v[12:15], a[192:199], v124\n" \
    "ds_read_b128 v[8:11], v127 offset:20480\n" \
    "v_cvt_pk_bf16_f32 v108, v41, v45\n" \
    "v_smfmac_f32_16x16x64_bf16 v[52:55], v[12:15], a[200:207], v124\n" \
    "ds_write2_b32 v128, v107, v108 offset0:0 offset1:80\n" \
    "v_smfmac_f32_16x16x64_bf16 v[48:51], v[16:19], a[208:215], v124\n" \
    "ds_read_b128 v[12:15], v127 offset:22528\n" \
    "ds_read_b128 v[40:43], v127 offset:21504\n" \
    "v_smfmac_f32_16x16x64_bf16 v[52:55], v[16:19], a[216:223], v124\n" \
    "ds_read_b128 v[44:47], v127 offset:23552\n" \
    "v_smfmac_f32_16x16x64_bf16 v[48:51], v[24:27], a[224:231], v124\n" \
    "ds_read_b128 v[16:19], v127 offset:17408\n" \
    "v_smfmac_f32_16x16x64_bf16 v[52:55], v[24:27], a[232:239], v124\n" \
    "s_waitcnt lgkmcnt(4)\n" \
    "s_barrier\n" \
    "ds_read_b128 v[72:75], v130 offset:0\n" \
    "v_smfmac_f32_16x16x64_bf16 v[48:51], v[28:31], a[240:247], v124\n" \
    "ds_read_b128 v[76:79], v130 offset:64\n" \
    "ds_read_b128 v[80:83], v130 offset:128\n" \
    "ds_read_b128 v[84:87], v130 offset:192\n" \
    "v_smfmac_f32_16x16x64_bf16 v[52:55], v[28:31], a[248:255], v124\n" \
    "v_smfmac_f32_16x16x64_bf16 v[48:51], v[32:35], v[136:143], v124\n" \
    "v_smfmac_f32_16x16x64_bf16 v[52:55], v[32:35], v[144:151], v124\n" \
    "v_smfmac_f32_16x16x64_bf16 v[48:51], v[36:39], v[152:159], v124\n" \
    "v_smfmac_f32_16x16x64_bf16 v[52:55], v[36:39], v[160:167], v124\n" \
    "s_waitcnt lgkmcnt(3)\n" \
    "v_mfma_f32_16x16x32_bf16 v[88:91], v[0:3], v[72:75], 0\n" \
    "s_waitcnt lgkmcnt(0)\n" \
    "v_add_f32 v48, v48, v50\n" \
    "v_add_f32 v49, v49, v51\n" \
    "v_mfma_f32_16x16x32_bf16 v[88:91], v[4:7], v[76:79], v[88:91]\n" \
    "v_add_f32 v52, v52, v54\n" \
    "v_add_f32 v53, v53, v55\n" \
    "v_mfma_f32_16x16x32_bf16 v[88:91], v[8:11], v[80:83], v[88:91]\n" \
    "v_cvt_pk_bf16_f32 v107, v48, v52\n" \
    "v_cvt_pk_bf16_f32 v108, v49, v53\n" \
    "ds_write2_b32 v129, v107, v108 offset0:0 offset1:144\n" \
    "v_mfma_f32_16x16x32_bf16 v[88:91], v[12:15], v[84:87], v[88:91]\n" \
    "ds_read_b64 v[0:1], v132 offset:0\n" \
    "ds_read_b64 v[2:3], v132 offset:1024\n" \
    "v_smfmac_f32_16x16x64_bf16 v[56:59], v[24:27], v[192:199], v124\n" \
    "ds_read_b64 v[4:5], v132 offset:512\n" \
    "ds_read_b64 v[6:7], v132 offset:1536\n" \
    "v_smfmac_f32_16x16x64_bf16 v[60:63], v[24:27], v[200:207], v124\n" \
    "v_mfma_f32_16x16x32_bf16 v[92:95], v[16:19], v[72:75], 0\n" \
    "v_exp_f32 v88, v88\n" \
    "s_waitcnt lgkmcnt(4)\n" \
    "s_barrier\n" \
    "ds_read_b128 v[48:51], v131 offset:0\n" \
    "ds_read_b128 v[52:55], v131 offset:128\n" \
    "v_mfma_f32_16x16x32_bf16 v[92:95], v[20:23], v[76:79], v[92:95]\n" \
    "ds_read_b128 v[8:11], v106 offset:0\n" \
    "v_exp_f32 v89, v89\n" \
    "v_mfma_f32_16x16x32_bf16 v[92:95], v[40:43], v[80:83], v[92:95]\n" \
    "ds_read_b128 v[12:15], v106 offset:1024\n" \
    "v_exp_f32 v90, v90\n" \
    "v_mfma_f32_16x16x32_bf16 v[92:95], v[44:47], v[84:87], v[92:95]\n" \
    "ds_read_b128 v[16:19], v106 offset:32768\n" \
    "v_exp_f32 v91, v91\n" \
    "v_smfmac_f32_16x16x64_bf16 v[56:59], v[28:31], v[208:215], v124\n" \
    "ds_read_b128 v[20:23], v106 offset:33792\n" \
    "s_waitcnt lgkmcnt(6)\n" \
    "v_cvt_pk_bf16_f32 v88, v88, v89\n" \
    "v_smfmac_f32_16x16x64_bf16 v[60:63], v[28:31], v[216:223], v124\n" \
    "ds_read_b128 v[40:43], v106 offset:2048\n" \
    "ds_read_b128 v[44:47], v106 offset:3072\n" \
    "v_cvt_pk_bf16_f32 v89, v90, v91\n" \
    "s_waitcnt lgkmcnt(6)\n" \
    "v_smfmac_f32_16x16x64_bf16 v[64:67], v[48:51], v[72:79], v124\n" \
    "ds_read_b128 v[24:27], v106 offset:6144\n" \
    "ds_read_b128 v[28:31], v106 offset:7168\n" \
    "v_exp_f32 v92, v92\n" \
    "v_smfmac_f32_16x16x64_bf16 v[64:67], v[52:55], v[80:87], v124\n" \
    "ds_read_b128 v[72:75], v106 offset:34816\n" \
    "ds_read_b128 v[76:79], v106 offset:35840\n" \
    "v_exp_f32 v93, v93\n" \
    "v_smfmac_f32_16x16x64_bf16 v[56:59], v[32:35], v[224:231], v124\n" \
    "ds_read_b128 v[80:83], v106 offset:4096\n" \
    "ds_read_b128 v[84:87], v106 offset:5120\n" \
    "v_exp_f32 v94, v94\n" \
    "v_smfmac_f32_16x16x64_bf16 v[60:63], v[32:35], v[232:239], v124\n" \
    "s_waitcnt lgkmcnt(6)\n" \
    "s_cmp_eq_u32 s36, 63\n" \
    "v_add_f32 v64, v64, v66\n" \
    "v_add_f32 v65, v65, v67\n" \
    "v_smfmac_f32_16x16x64_bf16 v[56:59], v[36:39], v[240:247], v124\n" \
    "ds_read_b128 v[48:51], v106 offset:36864\n" \
    "ds_read_b128 v[52:55], v106 offset:37888\n" \
    "v_exp_f32 v95, v95\n" \
    "v_cvt_pk_bf16_f32 v90, v92, v93\n" \
    "v_smfmac_f32_16x16x64_bf16 v[60:63], v[36:39], v[248:255], v124\n" \
    "ds_read_b128 v[32:35], v106 offset:38912\n" \
    "ds_read_b128 v[36:39], v106 offset:39936\n" \
    "v_cvt_pk_bf16_f32 v91, v94, v95\n" \
    "v_exp_f32 v64, v64\n" \
    "v_exp_f32 v65, v65\n" \
    "v_mfma_f32_16x16x32_bf16 v[102:105], v[88:91], v[120:123], 0\n" \
    "v_swap_b32 v1, v2\n" \
    "v_swap_b32 v5, v6\n" \
    "v_cvt_pk_bf16_f32 v100, v64, v65\n" \
    "v_add_f32 v56, v56, v58\n" \
    "v_smfmac_f32_16x16x64_bf16 v[68:71], v[88:91], v[0:7], v124\n" \
    "ds_read_b128 v[0:3], v133 offset:51200\n" \
    "ds_read_b128 v[4:7], v133 offset:52224\n" \
    "v_add_f32 v57, v57, v59\n" \
    "v_add_f32 v60, v60, v62\n" \
    "v_add_f32 v61, v61, v63\n" \
    "v_cvt_pk_bf16_f32 v96, v56, v57\n" \
    "v_mfma_f32_16x16x16_bf16 v[102:105], v[100:101], v[120:121], v[102:105]\n" \
    "v_cvt_pk_bf16_f32 v97, v60, v61\n" \
    "s_cbranch_scc1 16f\n" \
    "s_nop 0\n" \
    "v_smfmac_f32_16x16x32_bf16 v[68:71], v[100:101], v[96:99], v124\n" \
    "v_mov_b64 v[56:57], 0\n" \
    "v_mov_b64 v[58:59], 0\n" \
    "v_rcp_f32 v104, v102\n" \
    "v_rcp_f32 v105, v103\n" \
    "v_mul_f32 v68, v68, v104\n" \
    "v_mul_f32 v70, v70, v104\n" \
    "v_mul_f32 v69, v69, v105\n" \
    "v_mul_f32 v71, v71, v105\n" \
    "v_cvt_pk_bf16_f32 v107, v68, v70\n" \
    "v_cvt_pk_bf16_f32 v108, v69, v71\n" \
    "ds_write2_b32 v129, v107, v108 offset0:64 offset1:208\n" \
    "v_mfma_f32_16x16x32_bf16 v[60:63], v[56:59], v[120:123], 0\n" \
    "v_max3_f32 v102, v102, v103, v104\n" \
    "v_max_f32 v102, v102, v105\n" \
    "v_mfma_f32_16x16x32_bf16 v[88:91], v[56:59], v[120:123], 0\n" \
    "v_cmp_nge_f32 vcc, s82, v102\n" \
    "v_add_u32 v100, s83, v131\n" \
    "v_mfma_f32_16x16x32_bf16 v[92:95], v[56:59], v[120:123], 0\n" \
    "v_sub_u32 v101, v131, s83\n" \
    "v_xor_b32 v108, 0x2000, v133\n" \
    "v_mfma_f32_16x16x32_bf16 v[64:67], v[56:59], v[120:123], 0\n" \
    "v_and_b32 v107, 0x3c0, v127\n" \
    "v_add_u32 v107, v107, v127\n" \
    "s_cbranch_vccnz 7f\n" \
    "s_waitcnt vmcnt(2) lgkmcnt(0)\n" \
    "s_barrier\n" \
    "ds_read_b128 v[68:71], v100 offset:256\n" \
    "ds_read_b128 v[102:105], v101 offset:384\n" \
    "v_mfma_f32_16x16x32_bf16 v[64:67], v[56:59], v[120:123], 0\n" \
    "buffer_load_dwordx2 v[98:99], v107, %[rs], s78 offen sc1\n" \
    "v_mfma_f32_16x16x32_bf16 v[64:67], v[56:59], v[120:123], 0\n" \
    "buffer_load_dword v100, off, %[rw], s62 sc1\n" \
    "v_mfma_f32_16x16x32_bf16 v[64:67], v[56:59], v[120:123], 0\n" \
    "s_waitcnt lgkmcnt(1)\n" \
    "v_smfmac_f32_16x16x64_bf16 v[56:59], v[68:71], v[8:15], v124\n" \
    "s_waitcnt lgkmcnt(0)\n" \
    "v_smfmac_f32_16x16x64_bf16 v[56:59], v[102:105], v[16:23], v124\n" \
    "v_smfmac_f32_16x16x64_bf16 v[60:63], v[68:71], v[40:47], v124\n" \
    "ds_read_b128 v[16:19], v108 offset:51200\n" \
    "ds_read_b128 v[20:23], v108 offset:52224\n" \
    "v_smfmac_f32_16x16x64_bf16 v[60:63], v[102:105], v[72:79], v124\n" \
    "v_add_f32 v56, v56, v58\n" \
    "v_smfmac_f32_16x16x64_bf16 v[88:91], v[68:71], v[80:87], v124\n" \
    "v_add_f32 v57, v57, v59\n" \
    "v_smfmac_f32_16x16x64_bf16 v[88:91], v[102:105], v[48:55], v124\n" \
    "v_add_f32 v60, v60, v62\n" \
    "v_smfmac_f32_16x16x64_bf16 v[92:95], v[68:71], v[24:31], v124\n" \
    "v_add_f32 v61, v61, v63\n" \
    "v_smfmac_f32_16x16x64_bf16 v[92:95], v[102:105], v[32:39], v124\n" \
    "v_add_f32 v88, v88, v90\n" \
    "v_smfmac_f32_16x16x64_bf16 v[64:67], v[68:71], v[0:7], v124\n" \
    "s_waitcnt lgkmcnt(0)\n" \
    "v_add_f32 v89, v89, v91\n" \
    "v_smfmac_f32_16x16x64_bf16 v[64:67], v[102:105], v[16:23], v124\n" \
    "v_add_f32 v92, v92, v94\n" \
    "v_add_f32 v93, v93, v95\n" \
    "v_cvt_pk_bf16_f32 v8, v56, v60\n" \
    "v_cvt_pk_bf16_f32 v10, v57, v61\n" \
    "v_cvt_pk_bf16_f32 v9, v88, v92\n" \
    "v_cvt_pk_bf16_f32 v11, v89, v93\n" \
    "buffer_store_dwordx4 v[8:11], v134, %[rs], s79 offen sc1\n" \
    "v_add_f32 v64, v64, v66\n" \
    "v_add_f32 v65, v65, v67\n" \
    "v_cvt_pk_bf16_f32 v64, v64, v65\n" \
    "buffer_store_dword v64, v135, %[rs], s79 offen sc1\n" \
    "s_memtime s[88:89]\n" \
    "s_memrealtime s[44:45]\n" \
    "s_and_b32 s75, s91, 3\n" \
    "s_lshl_b32 s75, s75, 17\n" \
    "s_memrealtime s[46:47]\n" \
    "s_add_i32 s75, s75, s26\n" \
    "s_add_i32 s69, s75, s72\n" \
    "s_memrealtime s[48:49]\n" \
    "s_add_i32 s70, s69, 4096\n" \
    "s_add_i32 s71, s75, 16384\n" \
    "s_memrealtime s[50:51]\n" \
    "s_sub_i32 s76, s33, s42\n" \
    "s_ashr_i32 s76, s76, s32\n" \
    "s_memrealtime s[52:53]\n" \
    "s_add_i32 s42, s42, s76\n" \
    "s_add_i32 s40, s40, 64\n" \
    "s_memrealtime s[54:55]\n" \
    "s_nop 1\n" \
    "s_memrealtime s[56:57]\n" \
    "s_nop 1\n" \
    "s_memrealtime vcc\n" \
    "v_and_b32 v18, 0x300, v127\n" \
    "v_add_u32 v18, v18, v127\n" \
    "v_and_b32 v19, 0x380, v127\n" \
    "v_add_u32 v19, v19, v127\n" \
    "s_waitcnt lgkmcnt(0)\n" \
    "s_add_i32 s44, s44, s46\n" \
    "s_add_i32 s48, s48, s50\n" \
    "s_add_i32 s52, s52, s54\n" \
    "s_add_i32 s44, s44, s48\n" \
    "s_add_i32 s52, s52, s56\n" \
    "s_add_i32 s44, s44, s52\n" \
    "s_add_i32 s80, s44, vcc_lo\n" \
    "s_sub_i32 s76, s80, s29\n" \
    "s_mov_b32 s29, s80\n" \
    "s_sub_i32 s80, s88, s24\n" \
    "s_mov_b32 s24, s88\n" \
    "s_mul_i32 s75, s76, s25\n" \
    "s_lshr_b32 s75, s75, 12\n" \
    "s_sub_i32 s75, s80, s75\n" \
    "s_mul_hi_u32 s80, s80, s41\n" \
    "s_sub_i32 s80, s76, s80\n" \
    "s_lshl_b32 s80, s80, 18\n" \
    "s_cmp_lt_u32 s76, 0x1000\n" \
    "s_cselect_b32 s75, s75, 0\n" \
    "s_cselect_b32 s80, s80, 0\n" \
    "s_add_i32 s25, s25, s75\n" \
    "s_add_i32 s41, s41, s80\n" \
    "s_cmp_eq_u32 s36, 1\n" \
    "s_cselect_b32 s28, s29, s28\n" \
    "s_sub_i32 s80, s88, s86\n" \
    "s_min_u32 s40, s40, s80\n" \
    "s_waitcnt vmcnt(2)\n" \
    "v_readfirstlane_b32 s80, v100\n" \
    "v_cmp_ne_u32 vcc, s91, v99\n" \
    "s_mov_b32 s43, 0\n" \
    "s_max_i32 s38, s38, s80\n" \
    "s_cmp_lt_i32 s38, s91\n" \
    "s_cselect_b64 exec, 0, -1\n" \
    "s_cselect_b32 s94, 8, 0\n" \
    "buffer_load_dwordx4 v19, %[rw], s92 offen sc1 lds\n" \
    "s_add_i32 m0, s95, 896\n" \
    "s_nop 0\n" \
    "buffer_load_dwordx4 v19, %[rw], s92 offen offset:128 sc1 lds\n" \
    "s_add_i32 m0, s95, 8192\n" \
    "s_nop 0\n" \
    "buffer_load_dwordx4 v18, %[rw], s93 offen sc1 lds\n" \
    "s_add_i32 m0, s95, 8960\n" \
    "s_nop 0\n" \
    "buffer_load_dwordx4 v18, %[rw], s93 offen offset:256 sc1 lds\n" \
    "s_mov_b32 m0, s72\n" \
    "s_mov_b64 exec, -1\n" \
    "s_cbranch_vccnz 15f\n" \
    "10:\n" \
    "v_subrev_u32 v28, s29, v98\n" \
    "v_min_i32 v28, 0x4000, v28\n" \
    "v_max_i32 v28, 0xffffc000, v28\n" \
    "s_nop 1\n" \
    "v_max_i32_dpp v28, v28, v28 row_shr:1 row_mask:0xf bank_mask:0xf\n" \
    "s_nop 1\n" \
    "v_max_i32_dpp v28, v28, v28 row_shr:2 row_mask:0xf bank_mask:0xf\n" \
    "s_nop 1\n" \
    "v_max_i32_dpp v28, v28, v28 row_shr:4 row_mask:0xf bank_mask:0xf\n" \
    "s_nop 1\n" \
    "v_max_i32_dpp v28, v28, v28 row_shr:8 row_mask:0xf bank_mask:0xf\n" \
    "s_add_i32 s76, s29, s42\n" \
    "s_nop 0\n" \
    "v_max_i32_dpp v28, v28, v28 row_bcast:15 row_mask:0xa bank_mask:0xf\n" \
    "s_nop 1\n" \
    "v_max_i32_dpp v28, v28, v28 row_bcast:31 row_mask:0xc bank_mask:0xf\n" \
    "s_nop 0\n" \
    "v_readlane_b32 s37, v28, 63\n" \
    "v_add_u32 v30, -1, s36\n" \
    "s_add_i32 s37, s37, s76\n" \
    "v_mov_b32 v31, s37\n" \
    "buffer_store_dwordx2 v[30:31], v109, %[rw], s65 offen sc1\n" \
    "s_cmp_lg_u32 s94, 0\n" \
    "s_cbranch_scc1 9f\n" \
    "s_mov_b32 s36, s91\n" \
    "s_branch 12b\n" \
    "5:\n" \
    "s_cmp_eq_u32 s36, 0\n" \
    "s_cbranch_scc1 6b\n" \
    "s_add_i32 s39, s39, -1\n" \
    "s_cmp_lt_i32 s39, 0\n" \
    "s_cbranch_scc1 6b\n" \
    "s_add_i32 s30, s30, 1\n" \
    "s_and_b32 s80, s31, 0xffff\n" \
    "s_add_i32 s42, s42, s80\n" \
    "s_lshr_b32 s80, s31, 16\n" \
    "s_cmp_lt_u32 s36, 4\n" \
    "s_cselect_b32 s80, 0, s80\n" \
    "s_add_i32 s33, s33, s80\n" \
    "s_min_u32 s80, s80, 1\n" \
    "s_lshl_b32 s80, s80, 16\n" \
    "s_sub_i32 s31, s31, s80\n" \
    "s_waitcnt lgkmcnt(0)\n" \
    "s_mov_b32 s37, s29\n" \
    "s_nop 7\n" \
    "s_barrier\n" \
    "s_branch 0b\n" \
    "15:\n" \
    "s_add_i32 s30, s30, 0x10000\n" \
    "s_add_i32 s39, s39, -1\n" \
    "s_cmp_lt_i32 s39, 0\n" \
    "s_cbranch_scc1 10b\n" \
    "s_sleep 9\n" \
    "buffer_load_dwordx2 v[98:99], v107, %[rs], s78 offen sc1\n" \
    "s_waitcnt vmcnt(0)\n" \
    "v_cmp_ne_u32 vcc, s91, v99\n" \
    "s_nop 1\n" \
    "s_cbranch_vccnz 15b\n" \
    "s_memtime s[88:89]\n" \
    "s_memrealtime s[44:45]\n" \
    "s_nop 1\n" \
    "s_memrealtime s[46:47]\n" \
    "s_nop 1\n" \
    "s_memrealtime s[48:49]\n" \
    "s_nop 1\n" \
    "s_memrealtime s[50:51]\n" \
    "s_nop 1\n" \
    "s_memrealtime s[52:53]\n" \
    "s_nop 1\n" \
    "s_memrealtime s[54:55]\n" \
    "s_nop 1\n" \
    "s_memrealtime s[56:57]\n" \
    "s_nop 1\n" \
    "s_memrealtime vcc\n" \
    "s_waitcnt lgkmcnt(0)\n" \
    "s_add_i32 s44, s44, s46\n" \
    "s_add_i32 s48, s48, s50\n" \
    "s_add_i32 s52, s52, s54\n" \
    "s_add_i32 s44, s44, s48\n" \
    "s_add_i32 s52, s52, s56\n" \
    "s_add_i32 s44, s44, s52\n" \
    "s_add_i32 s29, s44, vcc_lo\n" \
    "s_mov_b32 s24, s88\n" \
    "s_branch 10b\n" \
    "16:\n" \
    "s_waitcnt lgkmcnt(0)\n" \
    "v_bfe_u32 v0, v127, 8, 2\n" \
    "v_subrev_u32 v0, s60, v0\n" \
    "v_mul_i32_i24 v0, -1088, v0\n" \
    "v_add_u32 v0, v0, v129\n" \
    "v_lshrrev_b32 v1, 2, v127\n" \
    "s_mov_b32 exec_lo, s66\n" \
    "s_mov_b32 exec_hi, s66\n" \
    "ds_read2_b32 v[96:97], v0 offset0:0 offset1:144\n" \
    "s_mov_b64 exec, -1\n" \
    "s_lshl_b32 s80, s91, 16\n" \
    "s_add_i32 s80, s80, s67\n" \
    "s_waitcnt lgkmcnt(0)\n" \
    "buffer_store_dword v96, v1, %[rw], s80 offen sc1\n" \
    "buffer_store_dword v97, v1, %[rw], s80 offen offset:256 sc1\n" \
    "s_branch 9f\n" \
    "7:\n" \
    "s_mov_b32 s43, 2\n" \
    "s_nop 7\n" \
    "s_waitcnt vmcnt(0)\n" \
    "9:\n" \
    "s_mov_b32 m0, s68\n" \
    "s_waitcnt lgkmcnt(0)\n" \
    "v_mov_b32 v0, s36\n" \
    "v_mov_b32 v1, s37\n" \
    "v_mov_b32 v2, s38\n" \
    "v_mov_b32 v3, s39\n" \
    "v_mov_b32 v4, s40\n" \
    "v_mov_b32 v5, s41\n" \
    "v_mov_b32 v6, s42\n" \
    "v_mov_b32 v7, s43\n" \
    "v_mov_b32 v8, s28\n" \
    "v_mov_b32 v9, s29\n" \
    "v_mov_b32 v10, s30\n" \
    "v_mov_b32 v11, s31\n" \
    "v_mov_b32 v12, s32\n" \
    "v_mov_b32 v13, s33\n" \
    "v_mov_b32 v14, s34\n" \
    "v_mov_b32 v15, s35\n" \
    "v_mov_b32 v16, s24\n" \
    "v_mov_b32 v17, s25\n" \
    "v_mov_b32 v18, s26\n" \
    "v_mov_b32 v19, s27\n" \
    "v_mov_b32 v20, s84\n" \
    "v_mov_b32 v21, s86\n" \
    "v_mov_b32 v22, s88\n" \
    "v_mov_b32 v23, s84\n" \
    "v_mov_b32 v24, s60\n" \
    "v_mov_b32 v25, s61\n" \
    "v_mov_b32 v26, s62\n" \
    "v_mov_b32 v27, s63\n" \
    "v_mov_b32 v28, s64\n" \
    "v_mov_b32 v29, s65\n" \
    "v_mov_b32 v30, s66\n" \
    "v_mov_b32 v31, s67\n" \
    ".Lstep_end:\n" \
    ""
static_assert(2 * PART_BYTES == 589824 && 2 * OFF_CAN == 1703936 && OFF_PART == 0 && OFF_VF - OFF_KF == 2048 * 4096 * 2, "constants of the step block");
#define STEP_CLOBBER "v32", "v33", "v34", "v35", "v36", "v37", "v38", "v39", "v40", "v41", "v42", "v43", "v44", "v45", "v46", "v47", "v48", "v49", "v50", "v51", "v52", "v53", "v54", "v55", "v56", "v57", "v58", "v59", "v60", "v61", "v62", "v63", "v64", "v65", "v66", "v67", "v68", "v69", "v70", "v71", "v72", "v73", "v74", "v75", "v76", "v77", "v78", "v79", "v80", "v81", "v82", "v83", "v84", "v85", "v86", "v87", "v88", "v89", "v90", "v91", "v92", "v93", "v94", "v95", "v98", "v99", "v100", "v101", "v102", "v103", "v104", "v105", "v106", "v107", "v108", "v109", "v110", "s24", "s25", "s26", "s27", "s28", "s29", "s30", "s31", "s32", "s33", "s34", "s35", "s36", "s37", "s38", "s39", "s40", "s41", "s42", "s43", "s44", "s45", "s46", "s47", "s48", "s49", "s50", "s51", "s52", "s53", "s54", "s55", "s56", "s57", "s58", "s59", "s60", "s61", "s62", "s63", "s64", "s65", "s66", "s67", "s68", "s69", "s70", "s71", "s72", "s73", "s74", "s75", "s76", "s77", "s78", "s79", "s80", "s81", "s82", "s83", "s84", "s85", "s86", "s87", "s88", "s89", "s90", "s91", "s92", "s93", "s94", "s95", "s96", "s97", "vcc", "scc", "memory"
#define OUT16_ASM \
    "s_mov_b32 exec_lo, %[tm]\n" \
    "s_mov_b32 exec_hi, 0\n" \
    "buffer_load_dword v79, %[tv], %[rw], %[so] offen sc1\n" \
    "s_mov_b64 exec, -1\n" \
    "s_add_i32 s44, %[so], 4096\n" \
    "s_add_i32 s45, %[so], 8192\n" \
    "s_add_i32 s46, %[so], 12288\n" \
    "buffer_load_dwordx4 v[16:19], %[vo], %[rw], %[so] offen offset:0 sc1\n" \
    "ds_read_b128 v[80:83], %[wl] offset:0\n" \
    "ds_read_b128 v[84:87], %[wl] offset:1024\n" \
    "s_nop 7\n" \
    "v_mov_b32 v104, 0\n" \
    "buffer_load_dwordx4 v[20:23], %[vo], %[rw], %[so] offen offset:256 sc1\n" \
    "s_nop 7\n" \
    "s_nop 3\n" \
    "buffer_load_dwordx4 v[24:27], %[vo], %[rw], %[so] offen offset:1024 sc1\n" \
    "s_nop 7\n" \
    "s_nop 3\n" \
    "buffer_load_dwordx4 v[28:31], %[vo], %[rw], %[so] offen offset:1280 sc1\n" \
    "s_nop 7\n" \
    "s_nop 3\n" \
    "buffer_load_dwordx4 v[32:35], %[vo], %[rw], %[so] offen offset:2048 sc1\n" \
    "s_nop 7\n" \
    "s_nop 3\n" \
    "buffer_load_dwordx4 v[36:39], %[vo], %[rw], %[so] offen offset:2304 sc1\n" \
    "s_nop 7\n" \
    "s_nop 3\n" \
    "buffer_load_dwordx4 v[40:43], %[vo], %[rw], %[so] offen offset:3072 sc1\n" \
    "s_nop 7\n" \
    "s_nop 3\n" \
    "buffer_load_dwordx4 v[44:47], %[vo], %[rw], %[so] offen offset:3328 sc1\n" \
    "s_nop 7\n" \
    "s_nop 3\n" \
    "buffer_load_dwordx4 v[48:51], %[vo], %[rw], s44 offen offset:0 sc1\n" \
    "s_nop 7\n" \
    "s_nop 3\n" \
    "buffer_load_dwordx4 v[52:55], %[vo], %[rw], s44 offen offset:256 sc1\n" \
    "s_nop 7\n" \
    "s_nop 3\n" \
    "buffer_load_dwordx4 v[56:59], %[vo], %[rw], s44 offen offset:1024 sc1\n" \
    "s_nop 7\n" \
    "s_nop 3\n" \
    "buffer_load_dwordx4 v[60:63], %[vo], %[rw], s44 offen offset:1280 sc1\n" \
    "ds_read_b128 v[88:91], %[wl] offset:2048\n" \
    "s_waitcnt vmcnt(11)\n" \
    "v_mfma_f32_16x16x32_bf16 v[0:3], a[0:3], v[16:19], 0\n" \
    "v_max3_u32 v104, v104, v16, v17\n" \
    "v_mfma_f32_16x16x32_bf16 v[4:7], a[128:131], v[16:19], 0\n" \
    "v_max3_u32 v104, v104, v18, v19\n" \
    "v_mfma_f32_16x16x32_bf16 v[8:11], v[128:131], v[16:19], 0\n" \
    "s_waitcnt lgkmcnt(2)\n" \
    "v_mfma_f32_16x16x32_bf16 v[12:15], v[80:83], v[16:19], 0\n" \
    "buffer_load_dwordx4 v[64:67], %[vo], %[rw], s44 offen offset:2048 sc1\n" \
    "ds_read_b128 v[80:83], %[wl] offset:3072\n" \
    "s_waitcnt vmcnt(11)\n" \
    "v_mfma_f32_16x16x32_bf16 v[0:3], a[4:7], v[20:23], v[0:3]\n" \
    "v_max3_u32 v104, v104, v20, v21\n" \
    "v_mfma_f32_16x16x32_bf16 v[4:7], a[132:135], v[20:23], v[4:7]\n" \
    "v_max3_u32 v104, v104, v22, v23\n" \
    "v_mfma_f32_16x16x32_bf16 v[8:11], v[132:135], v[20:23], v[8:11]\n" \
    "s_waitcnt lgkmcnt(2)\n" \
    "v_mfma_f32_16x16x32_bf16 v[12:15], v[84:87], v[20:23], v[12:15]\n" \
    "buffer_load_dwordx4 v[68:71], %[vo], %[rw], s44 offen offset:2304 sc1\n" \
    "ds_read_b128 v[84:87], %[wl] offset:4096\n" \
    "s_waitcnt vmcnt(11)\n" \
    "v_mfma_f32_16x16x32_bf16 v[0:3], a[8:11], v[24:27], v[0:3]\n" \
    "v_max3_u32 v104, v104, v24, v25\n" \
    "v_mfma_f32_16x16x32_bf16 v[4:7], a[136:139], v[24:27], v[4:7]\n" \
    "v_max3_u32 v104, v104, v26, v27\n" \
    "v_mfma_f32_16x16x32_bf16 v[8:11], v[136:139], v[24:27], v[8:11]\n" \
    "s_waitcnt lgkmcnt(2)\n" \
    "v_mfma_f32_16x16x32_bf16 v[12:15], v[88:91], v[24:27], v[12:15]\n" \
    "buffer_load_dwordx4 v[72:75], %[vo], %[rw], s44 offen offset:3072 sc1\n" \
    "ds_read_b128 v[88:91], %[wl] offset:5120\n" \
    "s_waitcnt vmcnt(11)\n" \
    "v_mfma_f32_16x16x32_bf16 v[0:3], a[12:15], v[28:31], v[0:3]\n" \
    "v_max3_u32 v104, v104, v28, v29\n" \
    "v_mfma_f32_16x16x32_bf16 v[4:7], a[140:143], v[28:31], v[4:7]\n" \
    "v_max3_u32 v104, v104, v30, v31\n" \
    "v_mfma_f32_16x16x32_bf16 v[8:11], v[140:143], v[28:31], v[8:11]\n" \
    "s_waitcnt lgkmcnt(2)\n" \
    "v_mfma_f32_16x16x32_bf16 v[12:15], v[80:83], v[28:31], v[12:15]\n" \
    "buffer_load_dwordx4 v[76:79], %[vo], %[rw], s44 offen offset:3328 sc1\n" \
    "ds_read_b128 v[80:83], %[wl] offset:6144\n" \
    "s_waitcnt vmcnt(11)\n" \
    "v_mfma_f32_16x16x32_bf16 v[0:3], a[16:19], v[32:35], v[0:3]\n" \
    "v_max3_u32 v104, v104, v32, v33\n" \
    "v_mfma_f32_16x16x32_bf16 v[4:7], a[144:147], v[32:35], v[4:7]\n" \
    "v_max3_u32 v104, v104, v34, v35\n" \
    "v_mfma_f32_16x16x32_bf16 v[8:11], v[144:147], v[32:35], v[8:11]\n" \
    "s_waitcnt lgkmcnt(2)\n" \
    "v_mfma_f32_16x16x32_bf16 v[12:15], v[84:87], v[32:35], v[12:15]\n" \
    "buffer_load_dwordx4 v[16:19], %[vo], %[rw], s45 offen offset:0 sc1\n" \
    "ds_read_b128 v[84:87], %[wl] offset:7168\n" \
    "s_waitcnt vmcnt(11)\n" \
    "v_mfma_f32_16x16x32_bf16 v[0:3], a[20:23], v[36:39], v[0:3]\n" \
    "v_max3_u32 v104, v104, v36, v37\n" \
    "v_mfma_f32_16x16x32_bf16 v[4:7], a[148:151], v[36:39], v[4:7]\n" \
    "v_max3_u32 v104, v104, v38, v39\n" \
    "v_mfma_f32_16x16x32_bf16 v[8:11], v[148:151], v[36:39], v[8:11]\n" \
    "s_waitcnt lgkmcnt(2)\n" \
    "v_mfma_f32_16x16x32_bf16 v[12:15], v[88:91], v[36:39], v[12:15]\n" \
    "buffer_load_dwordx4 v[20:23], %[vo], %[rw], s45 offen offset:256 sc1\n" \
    "ds_read_b128 v[88:91], %[wl] offset:8192\n" \
    "s_waitcnt vmcnt(11)\n" \
    "v_mfma_f32_16x16x32_bf16 v[0:3], a[24:27], v[40:43], v[0:3]\n" \
    "v_max3_u32 v104, v104, v40, v41\n" \
    "v_mfma_f32_16x16x32_bf16 v[4:7], a[152:155], v[40:43], v[4:7]\n" \
    "v_max3_u32 v104, v104, v42, v43\n" \
    "v_mfma_f32_16x16x32_bf16 v[8:11], v[152:155], v[40:43], v[8:11]\n" \
    "s_waitcnt lgkmcnt(2)\n" \
    "v_mfma_f32_16x16x32_bf16 v[12:15], v[80:83], v[40:43], v[12:15]\n" \
    "buffer_load_dwordx4 v[24:27], %[vo], %[rw], s45 offen offset:1024 sc1\n" \
    "ds_read_b128 v[80:83], %[wl] offset:9216\n" \
    "s_waitcnt vmcnt(11)\n" \
    "v_mfma_f32_16x16x32_bf16 v[0:3], a[28:31], v[44:47], v[0:3]\n" \
    "v_max3_u32 v104, v104, v44, v45\n" \
    "v_mfma_f32_16x16x32_bf16 v[4:7], a[156:159], v[44:47], v[4:7]\n" \
    "v_max3_u32 v104, v104, v46, v47\n" \
    "v_mfma_f32_16x16x32_bf16 v[8:11], v[156:159], v[44:47], v[8:11]\n" \
    "s_waitcnt lgkmcnt(2)\n" \
    "v_mfma_f32_16x16x32_bf16 v[12:15], v[84:87], v[44:47], v[12:15]\n" \
    "buffer_load_dwordx4 v[28:31], %[vo], %[rw], s45 offen offset:1280 sc1\n" \
    "ds_read_b128 v[84:87], %[wl] offset:10240\n" \
    "s_waitcnt vmcnt(11)\n" \
    "v_mfma_f32_16x16x32_bf16 v[0:3], a[32:35], v[48:51], v[0:3]\n" \
    "v_max3_u32 v104, v104, v48, v49\n" \
    "v_mfma_f32_16x16x32_bf16 v[4:7], a[160:163], v[48:51], v[4:7]\n" \
    "v_max3_u32 v104, v104, v50, v51\n" \
    "v_mfma_f32_16x16x32_bf16 v[8:11], v[160:163], v[48:51], v[8:11]\n" \
    "s_waitcnt lgkmcnt(2)\n" \
    "v_mfma_f32_16x16x32_bf16 v[12:15], v[88:91], v[48:51], v[12:15]\n" \
    "buffer_load_dwordx4 v[32:35], %[vo], %[rw], s45 offen offset:2048 sc1\n" \
    "ds_read_b128 v[88:91], %[wl] offset:11264\n" \
    "s_waitcnt vmcnt(11)\n" \
    "v_mfma_f32_16x16x32_bf16 v[0:3], a[36:39], v[52:55], v[0:3]\n" \
    "v_max3_u32 v104, v104, v52, v53\n" \
    "v_mfma_f32_16x16x32_bf16 v[4:7], a[164:167], v[52:55], v[4:7]\n" \
    "v_max3_u32 v104, v104, v54, v55\n" \
    "v_mfma_f32_16x16x32_bf16 v[8:11], v[164:167], v[52:55], v[8:11]\n" \
    "s_waitcnt lgkmcnt(2)\n" \
    "v_mfma_f32_16x16x32_bf16 v[12:15], v[80:83], v[52:55], v[12:15]\n" \
    "buffer_load_dwordx4 v[36:39], %[vo], %[rw], s45 offen offset:2304 sc1\n" \
    "ds_read_b128 v[80:83], %[wl] offset:12288\n" \
    "s_waitcnt vmcnt(11)\n" \
    "v_mfma_f32_16x16x32_bf16 v[0:3], a[40:43], v[56:59], v[0:3]\n" \
    "v_max3_u32 v104, v104, v56, v57\n" \
    "v_mfma_f32_16x16x32_bf16 v[4:7], a[168:171], v[56:59], v[4:7]\n" \
    "v_max3_u32 v104, v104, v58, v59\n" \
    "v_mfma_f32_16x16x32_bf16 v[8:11], v[168:171], v[56:59], v[8:11]\n" \
    "s_waitcnt lgkmcnt(2)\n" \
    "v_mfma_f32_16x16x32_bf16 v[12:15], v[84:87], v[56:59], v[12:15]\n" \
    "buffer_load_dwordx4 v[40:43], %[vo], %[rw], s45 offen offset:3072 sc1\n" \
    "ds_read_b128 v[84:87], %[wl] offset:13312\n" \
    "s_waitcnt vmcnt(11)\n" \
    "v_mfma_f32_16x16x32_bf16 v[0:3], a[44:47], v[60:63], v[0:3]\n" \
    "v_max3_u32 v104, v104, v60, v61\n" \
    "v_mfma_f32_16x16x32_bf16 v[4:7], a[172:175], v[60:63], v[4:7]\n" \
    "v_max3_u32 v104, v104, v62, v63\n" \
    "v_mfma_f32_16x16x32_bf16 v[8:11], v[172:175], v[60:63], v[8:11]\n" \
    "s_waitcnt lgkmcnt(2)\n" \
    "v_mfma_f32_16x16x32_bf16 v[12:15], v[88:91], v[60:63], v[12:15]\n" \
    "buffer_load_dwordx4 v[44:47], %[vo], %[rw], s45 offen offset:3328 sc1\n" \
    "ds_read_b128 v[88:91], %[wl] offset:14336\n" \
    "s_waitcnt vmcnt(11)\n" \
    "v_mfma_f32_16x16x32_bf16 v[0:3], a[48:51], v[64:67], v[0:3]\n" \
    "v_max3_u32 v104, v104, v64, v65\n" \
    "v_mfma_f32_16x16x32_bf16 v[4:7], a[176:179], v[64:67], v[4:7]\n" \
    "v_max3_u32 v104, v104, v66, v67\n" \
    "v_mfma_f32_16x16x32_bf16 v[8:11], v[176:179], v[64:67], v[8:11]\n" \
    "s_waitcnt lgkmcnt(2)\n" \
    "v_mfma_f32_16x16x32_bf16 v[12:15], v[80:83], v[64:67], v[12:15]\n" \
    "buffer_load_dwordx4 v[48:51], %[vo], %[rw], s46 offen offset:0 sc1\n" \
    "ds_read_b128 v[80:83], %[wl] offset:15360\n" \
    "s_waitcnt vmcnt(11)\n" \
    "v_mfma_f32_16x16x32_bf16 v[0:3], a[52:55], v[68:71], v[0:3]\n" \
    "v_max3_u32 v104, v104, v68, v69\n" \
    "v_mfma_f32_16x16x32_bf16 v[4:7], a[180:183], v[68:71], v[4:7]\n" \
    "v_max3_u32 v104, v104, v70, v71\n" \
    "v_mfma_f32_16x16x32_bf16 v[8:11], v[180:183], v[68:71], v[8:11]\n" \
    "s_waitcnt lgkmcnt(2)\n" \
    "v_mfma_f32_16x16x32_bf16 v[12:15], v[84:87], v[68:71], v[12:15]\n" \
    "buffer_load_dwordx4 v[52:55], %[vo], %[rw], s46 offen offset:256 sc1\n" \
    "ds_read_b128 v[84:87], %[wl] offset:16384\n" \
    "s_waitcnt vmcnt(11)\n" \
    "v_mfma_f32_16x16x32_bf16 v[0:3], a[56:59], v[72:75], v[0:3]\n" \
    "v_max3_u32 v104, v104, v72, v73\n" \
    "v_mfma_f32_16x16x32_bf16 v[4:7], a[184:187], v[72:75], v[4:7]\n" \
    "v_max3_u32 v104, v104, v74, v75\n" \
    "v_mfma_f32_16x16x32_bf16 v[8:11], v[184:187], v[72:75], v[8:11]\n" \
    "s_waitcnt lgkmcnt(2)\n" \
    "v_mfma_f32_16x16x32_bf16 v[12:15], v[88:91], v[72:75], v[12:15]\n" \
    "buffer_load_dwordx4 v[56:59], %[vo], %[rw], s46 offen offset:1024 sc1\n" \
    "ds_read_b128 v[88:91], %[wl] offset:17408\n" \
    "s_waitcnt vmcnt(11)\n" \
    "v_mfma_f32_16x16x32_bf16 v[0:3], a[60:63], v[76:79], v[0:3]\n" \
    "v_max3_u32 v104, v104, v76, v77\n" \
    "v_mfma_f32_16x16x32_bf16 v[4:7], a[188:191], v[76:79], v[4:7]\n" \
    "v_max3_u32 v104, v104, v78, v79\n" \
    "v_mfma_f32_16x16x32_bf16 v[8:11], v[188:191], v[76:79], v[8:11]\n" \
    "s_waitcnt lgkmcnt(2)\n" \
    "v_mfma_f32_16x16x32_bf16 v[12:15], v[80:83], v[76:79], v[12:15]\n" \
    "buffer_load_dwordx4 v[60:63], %[vo], %[rw], s46 offen offset:1280 sc1\n" \
    "ds_read_b128 v[80:83], %[wl] offset:18432\n" \
    "s_waitcnt vmcnt(11)\n" \
    "v_mfma_f32_16x16x32_bf16 v[0:3], a[64:67], v[16:19], v[0:3]\n" \
    "v_max3_u32 v104, v104, v16, v17\n" \
    "v_mfma_f32_16x16x32_bf16 v[4:7], a[192:195], v[16:19], v[4:7]\n" \
    "v_max3_u32 v104, v104, v18, v19\n" \
    "v_mfma_f32_16x16x32_bf16 v[8:11], v[192:195], v[16:19], v[8:11]\n" \
    "s_waitcnt lgkmcnt(2)\n" \
    "v_mfma_f32_16x16x32_bf16 v[12:15], v[84:87], v[16:19], v[12:15]\n" \
    "buffer_load_dwordx4 v[64:67], %[vo], %[rw], s46 offen offset:2048 sc1\n" \
    "ds_read_b128 v[84:87], %[wl] offset:19456\n" \
    "s_waitcnt vmcnt(11)\n" \
    "v_mfma_f32_16x16x32_bf16 v[0:3], a[68:71], v[20:23], v[0:3]\n" \
    "v_max3_u32 v104, v104, v20, v21\n" \
    "v_mfma_f32_16x16x32_bf16 v[4:7], a[196:199], v[20:23], v[4:7]\n" \
    "v_max3_u32 v104, v104, v22, v23\n" \
    "v_mfma_f32_16x16x32_bf16 v[8:11], v[196:199], v[20:23], v[8:11]\n" \
    "s_waitcnt lgkmcnt(2)\n" \
    "v_mfma_f32_16x16x32_bf16 v[12:15], v[88:91], v[20:23], v[12:15]\n" \
    "buffer_load_dwordx4 v[68:71], %[vo], %[rw], s46 offen offset:2304 sc1\n" \
    "ds_read_b128 v[88:91], %[wl] offset:20480\n" \
    "s_waitcnt vmcnt(11)\n" \
    "v_mfma_f32_16x16x32_bf16 v[0:3], a[72:75], v[24:27], v[0:3]\n" \
    "v_max3_u32 v104, v104, v24, v25\n" \
    "v_mfma_f32_16x16x32_bf16 v[4:7], a[200:203], v[24:27], v[4:7]\n" \
    "v_max3_u32 v104, v104, v26, v27\n" \
    "v_mfma_f32_16x16x32_bf16 v[8:11], v[200:203], v[24:27], v[8:11]\n" \
    "s_waitcnt lgkmcnt(2)\n" \
    "v_mfma_f32_16x16x32_bf16 v[12:15], v[80:83], v[24:27], v[12:15]\n" \
    "buffer_load_dwordx4 v[72:75], %[vo], %[rw], s46 offen offset:3072 sc1\n" \
    "ds_read_b128 v[80:83], %[wl] offset:21504\n" \
    "s_waitcnt vmcnt(11)\n" \
    "v_mfma_f32_16x16x32_bf16 v[0:3], a[76:79], v[28:31], v[0:3]\n" \
    "v_max3_u32 v104, v104, v28, v29\n" \
    "v_mfma_f32_16x16x32_bf16 v[4:7], a[204:207], v[28:31], v[4:7]\n" \
    "v_max3_u32 v104, v104, v30, v31\n" \
    "v_mfma_f32_16x16x32_bf16 v[8:11], v[204:207], v[28:31], v[8:11]\n" \
    "s_waitcnt lgkmcnt(2)\n" \
    "v_mfma_f32_16x16x32_bf16 v[12:15], v[84:87], v[28:31], v[12:15]\n" \
    "buffer_load_dwordx4 v[76:79], %[vo], %[rw], s46 offen offset:3328 sc1\n" \
    "ds_read_b128 v[84:87], %[wl] offset:22528\n" \
    "s_waitcnt vmcnt(11)\n" \
    "v_mfma_f32_16x16x32_bf16 v[0:3], a[80:83], v[32:35], v[0:3]\n" \
    "v_max3_u32 v104, v104, v32, v33\n" \
    "v_mfma_f32_16x16x32_bf16 v[4:7], a[208:211], v[32:35], v[4:7]\n" \
    "v_max3_u32 v104, v104, v34, v35\n" \
    "v_mfma_f32_16x16x32_bf16 v[8:11], v[208:211], v[32:35], v[8:11]\n" \
    "s_waitcnt lgkmcnt(2)\n" \
    "v_mfma_f32_16x16x32_bf16 v[12:15], v[88:91], v[32:35], v[12:15]\n" \
    "buffer_load_dwordx2 v[106:107], %[bv], %[rw], %[bs] offen sc1\n" \
    "ds_read_b128 v[88:91], %[wl] offset:23552\n" \
    "s_waitcnt vmcnt(11)\n" \
    "v_mfma_f32_16x16x32_bf16 v[0:3], a[84:87], v[36:39], v[0:3]\n" \
    "v_max3_u32 v104, v104, v36, v37\n" \
    "v_mfma_f32_16x16x32_bf16 v[4:7], a[212:215], v[36:39], v[4:7]\n" \
    "v_max3_u32 v104, v104, v38, v39\n" \
    "v_mfma_f32_16x16x32_bf16 v[8:11], v[212:215], v[36:39], v[8:11]\n" \
    "s_waitcnt lgkmcnt(2)\n" \
    "v_mfma_f32_16x16x32_bf16 v[12:15], v[80:83], v[36:39], v[12:15]\n" \
    "buffer_load_dword v108, %[gv], %[rw], %[gs] offen sc1\n" \
    "ds_read_b128 v[80:83], %[wl] offset:24576\n" \
    "s_waitcnt vmcnt(11)\n" \
    "v_mfma_f32_16x16x32_bf16 v[0:3], a[88:91], v[40:43], v[0:3]\n" \
    "v_max3_u32 v104, v104, v40, v41\n" \
    "v_mfma_f32_16x16x32_bf16 v[4:7], a[216:219], v[40:43], v[4:7]\n" \
    "v_max3_u32 v104, v104, v42, v43\n" \
    "v_mfma_f32_16x16x32_bf16 v[8:11], v[216:219], v[40:43], v[8:11]\n" \
    "s_waitcnt lgkmcnt(2)\n" \
    "v_mfma_f32_16x16x32_bf16 v[12:15], v[84:87], v[40:43], v[12:15]\n" \
    "buffer_load_dword v105, %[sv], %[rw], %[ss] offen sc1\n" \
    "ds_read_b128 v[84:87], %[wl] offset:25600\n" \
    "s_waitcnt vmcnt(11)\n" \
    "v_mfma_f32_16x16x32_bf16 v[0:3], a[92:95], v[44:47], v[0:3]\n" \
    "v_max3_u32 v104, v104, v44, v45\n" \
    "v_mfma_f32_16x16x32_bf16 v[4:7], a[220:223], v[44:47], v[4:7]\n" \
    "v_max3_u32 v104, v104, v46, v47\n" \
    "v_mfma_f32_16x16x32_bf16 v[8:11], v[220:223], v[44:47], v[8:11]\n" \
    "s_waitcnt lgkmcnt(2)\n" \
    "v_mfma_f32_16x16x32_bf16 v[12:15], v[88:91], v[44:47], v[12:15]\n" \
    "ds_read_b128 v[88:91], %[wl] offset:26624\n" \
    "s_waitcnt vmcnt(10)\n" \
    "v_mfma_f32_16x16x32_bf16 v[0:3], a[96:99], v[48:51], v[0:3]\n" \
    "v_max3_u32 v104, v104, v48, v49\n" \
    "v_mfma_f32_16x16x32_bf16 v[4:7], a[224:227], v[48:51], v[4:7]\n" \
    "v_max3_u32 v104, v104, v50, v51\n" \
    "v_mfma_f32_16x16x32_bf16 v[8:11], v[224:227], v[48:51], v[8:11]\n" \
    "s_waitcnt lgkmcnt(2)\n" \
    "v_mfma_f32_16x16x32_bf16 v[12:15], v[80:83], v[48:51], v[12:15]\n" \
    "ds_read_b128 v[80:83], %[wl] offset:27648\n" \
    "s_waitcnt vmcnt(9)\n" \
    "v_mfma_f32_16x16x32_bf16 v[0:3], a[100:103], v[52:55], v[0:3]\n" \
    "v_max3_u32 v104, v104, v52, v53\n" \
    "v_mfma_f32_16x16x32_bf16 v[4:7], a[228:231], v[52:55], v[4:7]\n" \
    "v_max3_u32 v104, v104, v54, v55\n" \
    "v_mfma_f32_16x16x32_bf16 v[8:11], v[228:231], v[52:55], v[8:11]\n" \
    "s_waitcnt lgkmcnt(2)\n" \
    "v_mfma_f32_16x16x32_bf16 v[12:15], v[84:87], v[52:55], v[12:15]\n" \
    "ds_read_b128 v[84:87], %[wl] offset:28672\n" \
    "s_waitcnt vmcnt(8)\n" \
    "v_mfma_f32_16x16x32_bf16 v[0:3], a[104:107], v[56:59], v[0:3]\n" \
    "v_max3_u32 v104, v104, v56, v57\n" \
    "v_mfma_f32_16x16x32_bf16 v[4:7], a[232:235], v[56:59], v[4:7]\n" \
    "v_max3_u32 v104, v104, v58, v59\n" \
    "v_mfma_f32_16x16x32_bf16 v[8:11], v[232:235], v[56:59], v[8:11]\n" \
    "s_waitcnt lgkmcnt(2)\n" \
    "v_mfma_f32_16x16x32_bf16 v[12:15], v[88:91], v[56:59], v[12:15]\n" \
    "ds_read_b128 v[88:91], %[wl] offset:29696\n" \
    "s_waitcnt vmcnt(7)\n" \
    "v_mfma_f32_16x16x32_bf16 v[0:3], a[108:111], v[60:63], v[0:3]\n" \
    "v_max3_u32 v104, v104, v60, v61\n" \
    "v_mfma_f32_16x16x32_bf16 v[4:7], a[236:239], v[60:63], v[4:7]\n" \
    "v_max3_u32 v104, v104, v62, v63\n" \
    "v_mfma_f32_16x16x32_bf16 v[8:11], v[236:239], v[60:63], v[8:11]\n" \
    "s_waitcnt lgkmcnt(2)\n" \
    "v_mfma_f32_16x16x32_bf16 v[12:15], v[80:83], v[60:63], v[12:15]\n" \
    "ds_read_b128 v[80:83], %[wl] offset:30720\n" \
    "s_waitcnt vmcnt(6)\n" \
    "v_mfma_f32_16x16x32_bf16 v[0:3], a[112:115], v[64:67], v[0:3]\n" \
    "v_max3_u32 v104, v104, v64, v65\n" \
    "v_mfma_f32_16x16x32_bf16 v[4:7], a[240:243], v[64:67], v[4:7]\n" \
    "v_max3_u32 v104, v104, v66, v67\n" \
    "v_mfma_f32_16x16x32_bf16 v[8:11], v[240:243], v[64:67], v[8:11]\n" \
    "s_waitcnt lgkmcnt(2)\n" \
    "v_mfma_f32_16x16x32_bf16 v[12:15], v[84:87], v[64:67], v[12:15]\n" \
    "ds_read_b128 v[84:87], %[wl] offset:31744\n" \
    "s_waitcnt vmcnt(5)\n" \
    "v_mfma_f32_16x16x32_bf16 v[0:3], a[116:119], v[68:71], v[0:3]\n" \
    "v_max3_u32 v104, v104, v68, v69\n" \
    "v_mfma_f32_16x16x32_bf16 v[4:7], a[244:247], v[68:71], v[4:7]\n" \
    "v_max3_u32 v104, v104, v70, v71\n" \
    "v_mfma_f32_16x16x32_bf16 v[8:11], v[244:247], v[68:71], v[8:11]\n" \
    "s_waitcnt lgkmcnt(2)\n" \
    "v_mfma_f32_16x16x32_bf16 v[12:15], v[88:91], v[68:71], v[12:15]\n" \
    "s_waitcnt vmcnt(4)\n" \
    "v_mfma_f32_16x16x32_bf16 v[0:3], a[120:123], v[72:75], v[0:3]\n" \
    "v_max3_u32 v104, v104, v72, v73\n" \
    "v_mfma_f32_16x16x32_bf16 v[4:7], a[248:251], v[72:75], v[4:7]\n" \
    "v_max3_u32 v104, v104, v74, v75\n" \
    "v_mfma_f32_16x16x32_bf16 v[8:11], v[248:251], v[72:75], v[8:11]\n" \
    "s_waitcnt lgkmcnt(1)\n" \
    "v_mfma_f32_16x16x32_bf16 v[12:15], v[80:83], v[72:75], v[12:15]\n" \
    "s_waitcnt vmcnt(3)\n" \
    "v_mfma_f32_16x16x32_bf16 v[0:3], a[124:127], v[76:79], v[0:3]\n" \
    "v_max3_u32 v104, v104, v76, v77\n" \
    "v_mfma_f32_16x16x32_bf16 v[4:7], a[252:255], v[76:79], v[4:7]\n" \
    "v_max3_u32 v104, v104, v78, v79\n" \
    "v_mfma_f32_16x16x32_bf16 v[8:11], v[252:255], v[76:79], v[8:11]\n" \
    "s_waitcnt lgkmcnt(0)\n" \
    "v_mfma_f32_16x16x32_bf16 v[12:15], v[84:87], v[76:79], v[12:15]\n" \
    "s_waitcnt vmcnt(0)\n" \
    "s_nop 7\n" \
    ""
#define OUT8_ASM \
    "s_mov_b32 exec_lo, %[tm]\n" \
    "s_mov_b32 exec_hi, 0\n" \
    "buffer_load_dword v79, %[tv], %[rw], %[so] offen sc1\n" \
    "s_mov_b64 exec, -1\n" \
    "s_add_i32 s44, %[so], 4096\n" \
    "s_add_i32 s45, %[so], 8192\n" \
    "s_add_i32 s46, %[so], 12288\n" \
    "buffer_load_dwordx4 v[16:19], %[vo], %[rw], %[so] offen offset:0 sc1\n" \
    "ds_read_b128 v[80:83], %[wl] offset:0\n" \
    "ds_read_b128 v[84:87], %[wl] offset:1024\n" \
    "ds_read_b128 v[88:91], %[wl] offset:2048\n" \
    "ds_read_b128 v[92:95], %[wl] offset:3072\n" \
    "v_mov_b64 v[0:1], 0\n" \
    "v_mov_b64 v[2:3], 0\n" \
    "v_mov_b64 v[4:5], 0\n" \
    "v_mov_b64 v[6:7], 0\n" \
    "v_mov_b64 v[8:9], 0\n" \
    "v_mov_b64 v[10:11], 0\n" \
    "v_mov_b64 v[12:13], 0\n" \
    "v_mov_b64 v[14:15], 0\n" \
    "v_mov_b32 v104, 0\n" \
    "buffer_load_dwordx4 v[20:23], %[vo], %[rw], %[so] offen offset:1024 sc1\n" \
    "s_nop 7\n" \
    "s_nop 3\n" \
    "buffer_load_dwordx4 v[24:27], %[vo], %[rw], %[so] offen offset:2048 sc1\n" \
    "s_nop 7\n" \
    "s_nop 3\n" \
    "buffer_load_dwordx4 v[28:31], %[vo], %[rw], %[so] offen offset:3072 sc1\n" \
    "s_nop 7\n" \
    "s_nop 3\n" \
    "buffer_load_dwordx4 v[32:35], %[vo], %[rw], s44 offen offset:0 sc1\n" \
    "s_nop 7\n" \
    "s_nop 3\n" \
    "buffer_load_dwordx4 v[36:39], %[vo], %[rw], s44 offen offset:1024 sc1\n" \
    "s_nop 7\n" \
    "s_nop 3\n" \
    "buffer_load_dwordx4 v[40:43], %[vo], %[rw], s44 offen offset:2048 sc1\n" \
    "s_nop 7\n" \
    "s_nop 3\n" \
    "buffer_load_dwordx4 v[44:47], %[vo], %[rw], s44 offen offset:3072 sc1\n" \
    "s_nop 7\n" \
    "s_nop 3\n" \
    "buffer_load_dwordx4 v[48:51], %[vo], %[rw], s45 offen offset:0 sc1\n" \
    "s_nop 7\n" \
    "s_nop 3\n" \
    "buffer_load_dwordx4 v[52:55], %[vo], %[rw], s45 offen offset:1024 sc1\n" \
    "ds_read_b128 v[96:99], %[wl] offset:4096\n" \
    "ds_read_b128 v[100:103], %[wl] offset:5120\n" \
    "s_waitcnt vmcnt(9)\n" \
    "v_smfmac_f32_16x16x64_bf16 v[0:3], v[16:19], a[0:7], %[idx]\n" \
    "v_max3_u32 v104, v104, v16, v17\n" \
    "v_smfmac_f32_16x16x64_bf16 v[4:7], v[16:19], a[128:135], %[idx]\n" \
    "v_max3_u32 v104, v104, v18, v19\n" \
    "v_smfmac_f32_16x16x64_bf16 v[8:11], v[16:19], v[128:135], %[idx]\n" \
    "s_waitcnt lgkmcnt(4)\n" \
    "v_smfmac_f32_16x16x64_bf16 v[12:15], v[16:19], v[80:87], %[idx]\n" \
    "buffer_load_dwordx4 v[56:59], %[vo], %[rw], s45 offen offset:2048 sc1\n" \
    "ds_read_b128 v[80:83], %[wl] offset:6144\n" \
    "ds_read_b128 v[84:87], %[wl] offset:7168\n" \
    "s_waitcnt vmcnt(9)\n" \
    "v_smfmac_f32_16x16x64_bf16 v[0:3], v[20:23], a[8:15], %[idx]\n" \
    "v_max3_u32 v104, v104, v20, v21\n" \
    "v_smfmac_f32_16x16x64_bf16 v[4:7], v[20:23], a[136:143], %[idx]\n" \
    "v_max3_u32 v104, v104, v22, v23\n" \
    "v_smfmac_f32_16x16x64_bf16 v[8:11], v[20:23], v[136:143], %[idx]\n" \
    "s_waitcnt lgkmcnt(4)\n" \
    "v_smfmac_f32_16x16x64_bf16 v[12:15], v[20:23], v[88:95], %[idx]\n" \
    "buffer_load_dwordx4 v[60:63], %[vo], %[rw], s45 offen offset:3072 sc1\n" \
    "ds_read_b128 v[88:91], %[wl] offset:8192\n" \
    "ds_read_b128 v[92:95], %[wl] offset:9216\n" \
    "s_waitcnt vmcnt(9)\n" \
    "v_smfmac_f32_16x16x64_bf16 v[0:3], v[24:27], a[16:23], %[idx]\n" \
    "v_max3_u32 v104, v104, v24, v25\n" \
    "v_smfmac_f32_16x16x64_bf16 v[4:7], v[24:27], a[144:151], %[idx]\n" \
    "v_max3_u32 v104, v104, v26, v27\n" \
    "v_smfmac_f32_16x16x64_bf16 v[8:11], v[24:27], v[144:151], %[idx]\n" \
    "s_waitcnt lgkmcnt(4)\n" \
    "v_smfmac_f32_16x16x64_bf16 v[12:15], v[24:27], v[96:103], %[idx]\n" \
    "buffer_load_dwordx4 v[64:67], %[vo], %[rw], s46 offen offset:0 sc1\n" \
    "ds_read_b128 v[96:99], %[wl] offset:10240\n" \
    "ds_read_b128 v[100:103], %[wl] offset:11264\n" \
    "s_waitcnt vmcnt(9)\n" \
    "v_smfmac_f32_16x16x64_bf16 v[0:3], v[28:31], a[24:31], %[idx]\n" \
    "v_max3_u32 v104, v104, v28, v29\n" \
    "v_smfmac_f32_16x16x64_bf16 v[4:7], v[28:31], a[152:159], %[idx]\n" \
    "v_max3_u32 v104, v104, v30, v31\n" \
    "v_smfmac_f32_16x16x64_bf16 v[8:11], v[28:31], v[152:159], %[idx]\n" \
    "s_waitcnt lgkmcnt(4)\n" \
    "v_smfmac_f32_16x16x64_bf16 v[12:15], v[28:31], v[80:87], %[idx]\n" \
    "buffer_load_dwordx4 v[68:71], %[vo], %[rw], s46 offen offset:1024 sc1\n" \
    "ds_read_b128 v[80:83], %[wl] offset:12288\n" \
    "ds_read_b128 v[84:87], %[wl] offset:13312\n" \
    "s_waitcnt vmcnt(9)\n" \
    "v_smfmac_f32_16x16x64_bf16 v[0:3], v[32:35], a[32:39], %[idx]\n" \
    "v_max3_u32 v104, v104, v32, v33\n" \
    "v_smfmac_f32_16x16x64_bf16 v[4:7], v[32:35], a[160:167], %[idx]\n" \
    "v_max3_u32 v104, v104, v34, v35\n" \
    "v_smfmac_f32_16x16x64_bf16 v[8:11], v[32:35], v[160:167], %[idx]\n" \
    "s_waitcnt lgkmcnt(4)\n" \
    "v_smfmac_f32_16x16x64_bf16 v[12:15], v[32:35], v[88:95], %[idx]\n" \
    "buffer_load_dwordx4 v[72:75], %[vo], %[rw], s46 offen offset:2048 sc1\n" \
    "ds_read_b128 v[88:91], %[wl] offset:14336\n" \
    "ds_read_b128 v[92:95], %[wl] offset:15360\n" \
    "s_waitcnt vmcnt(9)\n" \
    "v_smfmac_f32_16x16x64_bf16 v[0:3], v[36:39], a[40:47], %[idx]\n" \
    "v_max3_u32 v104, v104, v36, v37\n" \
    "v_smfmac_f32_16x16x64_bf16 v[4:7], v[36:39], a[168:175], %[idx]\n" \
    "v_max3_u32 v104, v104, v38, v39\n" \
    "v_smfmac_f32_16x16x64_bf16 v[8:11], v[36:39], v[168:175], %[idx]\n" \
    "s_waitcnt lgkmcnt(4)\n" \
    "v_smfmac_f32_16x16x64_bf16 v[12:15], v[36:39], v[96:103], %[idx]\n" \
    "buffer_load_dwordx4 v[76:79], %[vo], %[rw], s46 offen offset:3072 sc1\n" \
    "ds_read_b128 v[96:99], %[wl] offset:16384\n" \
    "ds_read_b128 v[100:103], %[wl] offset:17408\n" \
    "s_waitcnt vmcnt(9)\n" \
    "v_smfmac_f32_16x16x64_bf16 v[0:3], v[40:43], a[48:55], %[idx]\n" \
    "v_max3_u32 v104, v104, v40, v41\n" \
    "v_smfmac_f32_16x16x64_bf16 v[4:7], v[40:43], a[176:183], %[idx]\n" \
    "v_max3_u32 v104, v104, v42, v43\n" \
    "v_smfmac_f32_16x16x64_bf16 v[8:11], v[40:43], v[176:183], %[idx]\n" \
    "s_waitcnt lgkmcnt(4)\n" \
    "v_smfmac_f32_16x16x64_bf16 v[12:15], v[40:43], v[80:87], %[idx]\n" \
    "buffer_load_dwordx2 v[106:107], %[bv], %[rw], %[bs] offen sc1\n" \
    "ds_read_b128 v[80:83], %[wl] offset:18432\n" \
    "ds_read_b128 v[84:87], %[wl] offset:19456\n" \
    "s_waitcnt vmcnt(9)\n" \
    "v_smfmac_f32_16x16x64_bf16 v[0:3], v[44:47], a[56:63], %[idx]\n" \
    "v_max3_u32 v104, v104, v44, v45\n" \
    "v_smfmac_f32_16x16x64_bf16 v[4:7], v[44:47], a[184:191], %[idx]\n" \
    "v_max3_u32 v104, v104, v46, v47\n" \
    "v_smfmac_f32_16x16x64_bf16 v[8:11], v[44:47], v[184:191], %[idx]\n" \
    "s_waitcnt lgkmcnt(4)\n" \
    "v_smfmac_f32_16x16x64_bf16 v[12:15], v[44:47], v[88:95], %[idx]\n" \
    "buffer_load_dword v108, %[gv], %[rw], %[gs] offen sc1\n" \
    "ds_read_b128 v[88:91], %[wl] offset:20480\n" \
    "ds_read_b128 v[92:95], %[wl] offset:21504\n" \
    "s_waitcnt vmcnt(9)\n" \
    "v_smfmac_f32_16x16x64_bf16 v[0:3], v[48:51], a[64:71], %[idx]\n" \
    "v_max3_u32 v104, v104, v48, v49\n" \
    "v_smfmac_f32_16x16x64_bf16 v[4:7], v[48:51], a[192:199], %[idx]\n" \
    "v_max3_u32 v104, v104, v50, v51\n" \
    "v_smfmac_f32_16x16x64_bf16 v[8:11], v[48:51], v[192:199], %[idx]\n" \
    "s_waitcnt lgkmcnt(4)\n" \
    "v_smfmac_f32_16x16x64_bf16 v[12:15], v[48:51], v[96:103], %[idx]\n" \
    "buffer_load_dword v105, %[sv], %[rw], %[ss] offen sc1\n" \
    "ds_read_b128 v[96:99], %[wl] offset:22528\n" \
    "ds_read_b128 v[100:103], %[wl] offset:23552\n" \
    "s_waitcnt vmcnt(9)\n" \
    "v_smfmac_f32_16x16x64_bf16 v[0:3], v[52:55], a[72:79], %[idx]\n" \
    "v_max3_u32 v104, v104, v52, v53\n" \
    "v_smfmac_f32_16x16x64_bf16 v[4:7], v[52:55], a[200:207], %[idx]\n" \
    "v_max3_u32 v104, v104, v54, v55\n" \
    "v_smfmac_f32_16x16x64_bf16 v[8:11], v[52:55], v[200:207], %[idx]\n" \
    "s_waitcnt lgkmcnt(4)\n" \
    "v_smfmac_f32_16x16x64_bf16 v[12:15], v[52:55], v[80:87], %[idx]\n" \
    "ds_read_b128 v[80:83], %[wl] offset:24576\n" \
    "ds_read_b128 v[84:87], %[wl] offset:25600\n" \
    "s_waitcnt vmcnt(8)\n" \
    "v_smfmac_f32_16x16x64_bf16 v[0:3], v[56:59], a[80:87], %[idx]\n" \
    "v_max3_u32 v104, v104, v56, v57\n" \
    "v_smfmac_f32_16x16x64_bf16 v[4:7], v[56:59], a[208:215], %[idx]\n" \
    "v_max3_u32 v104, v104, v58, v59\n" \
    "v_smfmac_f32_16x16x64_bf16 v[8:11], v[56:59], v[208:215], %[idx]\n" \
    "s_waitcnt lgkmcnt(4)\n" \
    "v_smfmac_f32_16x16x64_bf16 v[12:15], v[56:59], v[88:95], %[idx]\n" \
    "ds_read_b128 v[88:91], %[wl] offset:26624\n" \
    "ds_read_b128 v[92:95], %[wl] offset:27648\n" \
    "s_waitcnt vmcnt(7)\n" \
    "v_smfmac_f32_16x16x64_bf16 v[0:3], v[60:63], a[88:95], %[idx]\n" \
    "v_max3_u32 v104, v104, v60, v61\n" \
    "v_smfmac_f32_16x16x64_bf16 v[4:7], v[60:63], a[216:223], %[idx]\n" \
    "v_max3_u32 v104, v104, v62, v63\n" \
    "v_smfmac_f32_16x16x64_bf16 v[8:11], v[60:63], v[216:223], %[idx]\n" \
    "s_waitcnt lgkmcnt(4)\n" \
    "v_smfmac_f32_16x16x64_bf16 v[12:15], v[60:63], v[96:103], %[idx]\n" \
    "ds_read_b128 v[96:99], %[wl] offset:28672\n" \
    "ds_read_b128 v[100:103], %[wl] offset:29696\n" \
    "s_waitcnt vmcnt(6)\n" \
    "v_smfmac_f32_16x16x64_bf16 v[0:3], v[64:67], a[96:103], %[idx]\n" \
    "v_max3_u32 v104, v104, v64, v65\n" \
    "v_smfmac_f32_16x16x64_bf16 v[4:7], v[64:67], a[224:231], %[idx]\n" \
    "v_max3_u32 v104, v104, v66, v67\n" \
    "v_smfmac_f32_16x16x64_bf16 v[8:11], v[64:67], v[224:231], %[idx]\n" \
    "s_waitcnt lgkmcnt(4)\n" \
    "v_smfmac_f32_16x16x64_bf16 v[12:15], v[64:67], v[80:87], %[idx]\n" \
    "ds_read_b128 v[80:83], %[wl] offset:30720\n" \
    "ds_read_b128 v[84:87], %[wl] offset:31744\n" \
    "s_waitcnt vmcnt(5)\n" \
    "v_smfmac_f32_16x16x64_bf16 v[0:3], v[68:71], a[104:111], %[idx]\n" \
    "v_max3_u32 v104, v104, v68, v69\n" \
    "v_smfmac_f32_16x16x64_bf16 v[4:7], v[68:71], a[232:239], %[idx]\n" \
    "v_max3_u32 v104, v104, v70, v71\n" \
    "v_smfmac_f32_16x16x64_bf16 v[8:11], v[68:71], v[232:239], %[idx]\n" \
    "s_waitcnt lgkmcnt(4)\n" \
    "v_smfmac_f32_16x16x64_bf16 v[12:15], v[68:71], v[88:95], %[idx]\n" \
    "s_waitcnt vmcnt(4)\n" \
    "v_smfmac_f32_16x16x64_bf16 v[0:3], v[72:75], a[112:119], %[idx]\n" \
    "v_max3_u32 v104, v104, v72, v73\n" \
    "v_smfmac_f32_16x16x64_bf16 v[4:7], v[72:75], a[240:247], %[idx]\n" \
    "v_max3_u32 v104, v104, v74, v75\n" \
    "v_smfmac_f32_16x16x64_bf16 v[8:11], v[72:75], v[240:247], %[idx]\n" \
    "s_waitcnt lgkmcnt(2)\n" \
    "v_smfmac_f32_16x16x64_bf16 v[12:15], v[72:75], v[96:103], %[idx]\n" \
    "s_waitcnt vmcnt(3)\n" \
    "v_smfmac_f32_16x16x64_bf16 v[0:3], v[76:79], a[120:127], %[idx]\n" \
    "v_max3_u32 v104, v104, v76, v77\n" \
    "v_smfmac_f32_16x16x64_bf16 v[4:7], v[76:79], a[248:255], %[idx]\n" \
    "v_max3_u32 v104, v104, v78, v79\n" \
    "v_smfmac_f32_16x16x64_bf16 v[8:11], v[76:79], v[248:255], %[idx]\n" \
    "s_waitcnt lgkmcnt(0)\n" \
    "v_smfmac_f32_16x16x64_bf16 v[12:15], v[76:79], v[80:87], %[idx]\n" \
    "s_waitcnt vmcnt(0)\n" \
    "s_nop 5\n" \
    "v_permlane32_swap_b32 v0, v8\n" \
    "v_add_f32 v0, v0, v8\n" \
    "v_permlane32_swap_b32 v1, v9\n" \
    "v_add_f32 v1, v1, v9\n" \
    "v_permlane32_swap_b32 v2, v10\n" \
    "v_add_f32 v2, v2, v10\n" \
    "v_permlane32_swap_b32 v3, v11\n" \
    "v_add_f32 v3, v3, v11\n" \
    "v_permlane32_swap_b32 v4, v12\n" \
    "v_add_f32 v4, v4, v12\n" \
    "v_permlane32_swap_b32 v5, v13\n" \
    "v_add_f32 v5, v5, v13\n" \
    "v_permlane32_swap_b32 v6, v14\n" \
    "v_add_f32 v6, v6, v14\n" \
    "v_permlane32_swap_b32 v7, v15\n" \
    "v_add_f32 v7, v7, v15\n" \
    ""
#define OUT_CLOBBER "v16", "v17", "v18", "v19", "v20", "v21", "v22", "v23", "v24", "v25", "v26", "v27", "v28", "v29", "v30", "v31", "v32", "v33", "v34", "v35", "v36", "v37", "v38", "v39", "v40", "v41", "v42", "v43", "v44", "v45", "v46", "v47", "v48", "v49", "v50", "v51", "v52", "v53", "v54", "v55", "v56", "v57", "v58", "v59", "v60", "v61", "v62", "v63", "v64", "v65", "v66", "v67", "v68", "v69", "v70", "v71", "v72", "v73", "v74", "v75", "v76", "v77", "v78", "v79", "v80", "v81", "v82", "v83", "v84", "v85", "v86", "v87", "v88", "v89", "v90", "v91", "v92", "v93", "v94", "v95", "v96", "v97", "v98", "v99", "v100", "v101", "v102", "v103", "s44", "s45", "s46", "scc", "memory"
constexpr int NREP = 2;
#define OWNER_ASM \
    "s_getpc_b64 s[96:97]\n" \
    "14:\n" \
    "s_add_u32 s96, s96, 2f-14b\n" \
    "s_addc_u32 s97, s97, 0\n" \
    "s_mov_b32 %[np], 0\n" \
    "6:\n" \
    "buffer_load_dwordx2 v[0:1], %[ca], %[rs], %[cs] offen sc1\n" \
    "s_memtime s[58:59]\n" \
    "s_memrealtime s[44:45]\n" \
    "s_nop 1\n" \
    "s_memrealtime s[46:47]\n" \
    "s_nop 1\n" \
    "s_memrealtime s[48:49]\n" \
    "s_nop 1\n" \
    "s_memrealtime s[50:51]\n" \
    "s_nop 1\n" \
    "s_memrealtime s[52:53]\n" \
    "s_nop 1\n" \
    "s_memrealtime s[54:55]\n" \
    "s_nop 1\n" \
    "s_memrealtime s[56:57]\n" \
    "s_nop 1\n" \
    "s_memrealtime vcc\n" \
    "s_waitcnt lgkmcnt(0)\n" \
    "s_add_i32 s44, s44, s46\n" \
    "s_add_i32 s48, s48, s50\n" \
    "s_add_i32 s52, s52, s54\n" \
    "s_add_i32 s44, s44, s48\n" \
    "s_add_i32 s52, s52, s56\n" \
    "s_add_i32 s44, s44, s52\n" \
    "s_add_i32 s59, s44, vcc_lo\n" \
    "s_sub_i32 s44, s59, %[oT]\n" \
    "s_mov_b32 %[oT], s59\n" \
    "s_sub_i32 s45, s58, %[oC]\n" \
    "s_mov_b32 %[oC], s58\n" \
    "s_mul_i32 s46, s44, %[oR]\n" \
    "s_lshr_b32 s46, s46, 12\n" \
    "s_sub_i32 s46, s45, s46\n" \
    "s_cmp_lt_u32 s44, 0x1000\n" \
    "s_cselect_b32 s46, s46, 0\n" \
    "s_add_i32 %[oR], %[oR], s46\n" \
    "s_waitcnt vmcnt(0)\n" \
    "v_subrev_u32 v0, %[oT], v0\n" \
    "v_cmp_ne_u32 vcc, %[tag], v1\n" \
    "v_min_i32 v0, 0x4000, v0\n" \
    "v_max_i32 v0, 0xffffc000, v0\n" \
    "s_nop 0\n" \
    "s_cbranch_vccnz 7f\n" \
    "8:\n" \
    "v_max_i32_dpp v0, v0, v0 row_shr:1 row_mask:0xf bank_mask:0xf\n" \
    "s_nop 1\n" \
    "v_max_i32_dpp v0, v0, v0 row_shr:2 row_mask:0xf bank_mask:0xf\n" \
    "s_nop 1\n" \
    "v_max_i32_dpp v0, v0, v0 row_shr:4 row_mask:0xf bank_mask:0xf\n" \
    "s_nop 1\n" \
    "v_max_i32_dpp v0, v0, v0 row_shr:8 row_mask:0xf bank_mask:0xf\n" \
    "s_nop 1\n" \
    "v_max_i32_dpp v0, v0, v0 row_bcast:15 row_mask:0xa bank_mask:0xf\n" \
    "s_nop 1\n" \
    "v_max_i32_dpp v0, v0, v0 row_bcast:31 row_mask:0xc bank_mask:0xf\n" \
    "s_nop 1\n" \
    "v_readlane_b32 s44, v0, 63\n" \
    "s_add_i32 s45, %[oC], -116\n" \
    "s_add_i32 %[tm], s44, %[oT]\n" \
    "s_add_i32 s44, s44, %[c8]\n" \
    "s_mul_i32 s44, s44, %[oR]\n" \
    "s_ashr_i32 s44, s44, 12\n" \
    "s_add_i32 s44, s44, s45\n" \
    "1:\n" \
    "s_memtime vcc\n" \
    "s_waitcnt lgkmcnt(0)\n" \
    "s_sub_i32 s58, s44, vcc_lo\n" \
    "s_cmp_gt_i32 s58, 112\n" \
    "s_cbranch_scc1 1b\n" \
    "s_max_i32 s58, s58, 0\n" \
    "s_and_b32 s58, s58, -4\n" \
    "s_sub_u32 vcc_lo, s96, s58\n" \
    "s_subb_u32 vcc_hi, s97, 0\n" \
    "s_setpc_b64 vcc\n" \
    "s_nop 0\n" \
    "s_nop 0\n" \
    "s_nop 0\n" \
    "s_nop 0\n" \
    "s_nop 0\n" \
    "s_nop 0\n" \
    "s_nop 0\n" \
    "s_nop 0\n" \
    "s_nop 0\n" \
    "s_nop 0\n" \
    "s_nop 0\n" \
    "s_nop 0\n" \
    "s_nop 0\n" \
    "s_nop 0\n" \
    "s_nop 0\n" \
    "s_nop 0\n" \
    "s_nop 0\n" \
    "s_nop 0\n" \
    "s_nop 0\n" \
    "s_nop 0\n" \
    "s_nop 0\n" \
    "s_nop 0\n" \
    "s_nop 0\n" \
    "s_nop 0\n" \
    "s_nop 0\n" \
    "s_nop 0\n" \
    "s_nop 0\n" \
    "s_nop 0\n" \
    "2:\n" \
    "buffer_load_dwordx4 v[0:3], %[o], %[rs], %[pb] offen offset:0 sc1\n" \
    "buffer_load_dwordx4 v[4:7], %[o], %[rs], %[pb] offen offset:1024 sc1\n" \
    "buffer_load_dwordx4 v[8:11], %[o], %[rs], %[pb] offen offset:2048 sc1\n" \
    "buffer_load_dwordx4 v[12:15], %[o], %[rs], %[pb] offen offset:3072 sc1\n" \
    "s_add_i32 %[np], %[np], 1\n" \
    "s_waitcnt vmcnt(3)\n" \
    "v_mfma_f32_16x16x32_bf16 v[16:19], %[sel], v[0:3], 0\n" \
    "v_max3_u32 v20, v0, v1, v1\n" \
    "v_max3_u32 v20, v2, v3, v20\n" \
    "s_waitcnt vmcnt(2)\n" \
    "v_mfma_f32_16x16x32_bf16 v[16:19], %[sel], v[4:7], v[16:19]\n" \
    "v_max3_u32 v20, v4, v5, v20\n" \
    "v_max3_u32 v20, v6, v7, v20\n" \
    "s_waitcnt vmcnt(1)\n" \
    "v_mfma_f32_16x16x32_bf16 v[16:19], %[sel], v[8:11], v[16:19]\n" \
    "v_max3_u32 v20, v8, v9, v20\n" \
    "v_max3_u32 v20, v10, v11, v20\n" \
    "s_waitcnt vmcnt(0)\n" \
    "v_mfma_f32_16x16x32_bf16 v[16:19], %[sel], v[12:15], v[16:19]\n" \
    "v_max3_u32 v20, v12, v13, v20\n" \
    "v_max3_u32 v20, v14, v15, v20\n" \
    "v_cmp_eq_u32 vcc, -1, v20\n" \
    "s_nop 3\n" \
    "s_cbranch_vccnz 4f\n" \
    "3:\n" \
    "v_add_f32_dpp v16, v16, v16 row_ror:8 row_mask:0xf bank_mask:0xf\n" \
    "v_add_f32_dpp v17, v17, v17 row_ror:8 row_mask:0xf bank_mask:0xf\n" \
    "v_cvt_pk_bf16_f32 v16, v16, v17\n" \
    "buffer_store_dword v16, %[so], %[rs], %[mb0] offen sc1\n" \
    "s_branch 5f\n" \
    "4:\n" \
    "s_add_i32 %[bud], %[bud], -1\n" \
    "s_cmp_lt_i32 %[bud], 0\n" \
    "s_cbranch_scc0 2b\n" \
    "s_branch 3b\n" \
    "7:\n" \
    "s_cmp_eq_u32 %[tag], 1\n" \
    "s_cbranch_scc1 9f\n" \
    "s_sleep 13\n" \
    "9:\n" \
    "s_sleep 6\n" \
    "s_add_i32 %[bud], %[bud], -1\n" \
    "s_cmp_lt_i32 %[bud], 0\n" \
    "s_cbranch_scc0 6b\n" \
    "s_branch 8b\n" \
    "5:\n" \
    ""
#define OWNER_CLOBBER "v0", "v1", "v2", "v3", "v4", "v5", "v6", "v7", "v8", "v9", "v10", "v11", "v12", "v13", "v14", "v15", "v16", "v17", "v18", "v19", "v20", "s44", "s45", "s46", "s47", "s48", "s49", "s50", "s51", "s52", "s53", "s54", "s55", "s56", "s57", "s58", "s59", "s96", "s97", "vcc", "scc", "memory"
constexpr int O_W = 0;
constexpr int O_STG = 131072;
constexpr int O_B = O_STG + 20480;
constexpr int OUT_REP = 7;
#define OUT_AUX 16
DI int opaque_lane() { int l; asm volatile("v_mbcnt_lo_u32_b32 %0, -1, 0\n\tv_mbcnt_hi_u32_b32 %0, -1, %0" : "=v"(l)); return l; }
template <int O, class V> DI void put4(V& d, u32x4 f) {
#pragma unroll
    for (int j = 0; j < 4; ++j) d[O + j] = f[j];
}
DI void role_outp(const Args& a, LAS char* lds, unsigned lds_base, int nb, int rep, rsrc_t rw, int& budget) {
    const int tid = threadIdx.x, lane = tid & 63, w = __builtin_amdgcn_readfirstlane(tid >> 6);
    WG_BARRIER();
    const int n = lane & 15, g = lane >> 4;
    const int wl = O_W + w * 32768 + lane * 16;
    u32x32 A[8], V[4];
    {
        const int stg = O_STG + w * 5120;
        const rsrc_t ro = __builtin_amdgcn_make_buffer_rsrc((void*)a.Wo, 0, HD * DM * 2, 0x00020000);
        const int so = (1024 * w + (lane >> 3)) * (DM * 2) + nb * 128 + (lane & 7) * 16, sw = stg + (lane >> 3) * 160 + (lane & 7) * 16;
        const int ra = stg + (16 * (g & 1) + 4 * (g >> 1) + (n >> 2)) * 160 + (n & 3) * 8;
        constexpr int PB = 4;
        u32x4 b[PB][4];
        auto fetch = [&](int ks) {
#pragma unroll
            for (int i = 0; i < 4; ++i) b[ks % PB][i] = __builtin_amdgcn_raw_buffer_load_b128(ro, so, (64 * (ks >> 1) + 16 * (ks & 1) + 8 * (i & 1) + 32 * (i >> 1)) * (DM * 2), 0);
        };
#pragma unroll
        for (int ks = 0; ks < PB; ++ks) fetch(ks);
        FENCE();
        static_for<32>([&](auto ic) {
            constexpr int ks = decltype(ic)::value;
#pragma unroll
            for (int i = 0; i < 4; ++i) *(LAS u32x4*)(lds + sw + i * 1280) = b[ks % PB][i];
            if (ks + PB < 32) fetch(ks + PB);
            u32x4 f[4];
#pragma unroll
            for (int t = 0; t < 4; ++t) { const u32x2 lo = tr_read(lds, ra + t * 32), hi = tr_read(lds, ra + t * 32 + 1280); f[t] = (u32x4){lo.x, lo.y, hi.x, hi.y}; }
            put4<4 * (ks & 7)>(A[ks >> 3], f[0]); put4<4 * (ks & 7)>(A[4 + (ks >> 3)], f[1]); put4<4 * (ks & 7)>(V[ks >> 3], f[2]);
            *(LAS u32x4*)(lds + wl + ks * 1024) = f[3];
            if constexpr (ks == 7) asm volatile("" : "+{a[0:31]}"(A[0]), "+{a[128:159]}"(A[4]), "+{v[128:159]}"(V[0]));
            if constexpr (ks == 15) asm volatile("" : "+{a[32:63]}"(A[1]), "+{a[160:191]}"(A[5]), "+{v[160:191]}"(V[1]));
            if constexpr (ks == 23) asm volatile("" : "+{a[64:95]}"(A[2]), "+{a[192:223]}"(A[6]), "+{v[192:223]}"(V[2]));
            if constexpr (ks == 31) asm volatile("" : "+{a[96:127]}"(A[3]), "+{a[224:255]}"(A[7]), "+{v[224:255]}"(V[3]));
            FENCE();
        });
    }
    wait_prog(rw, F_S4 + lane * 4, true, 1, budget);
    const rsrc_t ru = __builtin_amdgcn_make_buffer_rsrc(a.out, 0, SEQ * DM * 2, 0x00020000);
    constexpr int SP = 2 * NSEG - 2;
    auto first = [&](int u) { return u >= SP ? 8 * u + 8 * SP : 16 * u; };
    auto st_s = [&](int u) { return (u >= 2 * FAST ? F_H + ((first(u) >> 4) - 2 * FAST) * 4096 : F_O + (u & 3) * 4) + w * 1024; };
    auto bn_s = [&](int u) { return OFF_BO + (64 * nb + 16 * w) * BO_ROW + first(u) * 2; };
    auto gn_s = [&](int u) { return OFF_GT + (first(u) + 4 * w) * 4; };
    int st = -1;
    u32x2 bn = ld64(rw, (lane >> 2) * BO_ROW + (lane & 3) * 8, bn_s(rep));
    float gn = __uint_as_float(ld32(rw, g * 4, gn_s(rep)));
    __builtin_amdgcn_s_waitcnt(0);
#define OUT_OPS "=&{v[0:15]}"(acc), "=&{v104}"(top), "=&{v105}"(st), "=&{v[106:107]}"(bn), "=&{v108}"(gn) \
        : "{a[0:31]}"(A[0]), "{a[32:63]}"(A[1]), "{a[64:95]}"(A[2]), "{a[96:127]}"(A[3]), "{a[128:159]}"(A[4]), "{a[160:191]}"(A[5]), "{a[192:223]}"(A[6]), "{a[224:255]}"(A[7]), \
          "{v[128:159]}"(V[0]), "{v[160:191]}"(V[1]), "{v[192:223]}"(V[2]), "{v[224:255]}"(V[3]), [rw] "s"(rw), [so] "s"(t0 * 8192), [wl] "v"(lds_base + O_W + w * 32768 + lane * 16), \
          [tv] "v"(tv), [tm] "s"(tm), [vo] "v"(vo), [sv] "v"(sv), [bv] "v"((lane >> 2) * BO_ROW + (lane & 3) * 8), [gv] "v"(g * 4), \
          [ss] "s"(st_s(u + OUT_REP)), [bs] "s"(bn_s(u + OUT_REP)), [gs] "s"(gn_s(u + OUT_REP))
    for (int u = rep; u < SP + 4; u += OUT_REP) {
        const bool fast = u >= 2 * FAST, sp = u >= SP;
        const int t0 = first(u);
        WG_BARRIER();
        int lane = opaque_lane(), n = lane & 15, g = lane >> 4;
        if (u == 3 * OUT_REP && nb == 0) __builtin_amdgcn_raw_buffer_store_b64(launch_tag(a), rw, w + lane == 0 ? F_EPOCH : OOB, 0, 16);
        S4S(0)
        TT(u >= 123, 0)
        *(LAS u32x2*)(lds + O_B + (16 * w + (lane >> 2)) * 40 + (lane & 3) * 8) = bn;
        const float gt = gn;
        const int tv = OFF_OF + w * 16384 + nb * 2048 + g * 65536 + n * 128, tm = __builtin_amdgcn_readfirstlane(nb < 8 ? sp ? 0xFFFF : -1 : 0);
        const int vo = OFF_OF + w * 16384 + (n & 7) * 16 + (sp ? (n >> 3) * 512 + g * 128 : (n >> 3) * 65536 + (g & 1) * 512 + (g >> 1) * 128);
        const int thr = fast ? 0 : u >> 1, sv = (lane & 7) * 128;
        if (!__all(st >= thr)) do { __builtin_amdgcn_s_sleep(1); st = (int)ld32(rw, sv, st_s(u)); asm volatile("" ::: "memory"); } while (!__all(st >= thr) && --budget >= 0);
        if (fast) { const unsigned t = now(); (void)wait_until(t + wave_max32(min((int)(2 * (st - t)) >> 1, 500))); }
        S4S(1)
        TT(u >= 123, 1)
        f32x16 acc; unsigned top;
        if (sp) {
            do asm volatile(OUT8_ASM : OUT_OPS, [idx] "v"(n & 8 ? 0xEEEE : 0x4444) : OUT_CLOBBER); while (__any(top == SENT) && --budget >= 0);
            TT(u >= 123, 2)
            lane = opaque_lane(); n = lane & 15; g = lane >> 4;
#pragma unroll
            for (int i = 0; i < 8; ++i) *(LAS float*)(lds + O_STG + (w * 16 + 4 * (g & 1) + (i & 3)) * 272 + (g >> 1) * 128 + (i >> 2) * 64 + n * 4) = acc[i];
        } else {
            do asm volatile(OUT16_ASM : OUT_OPS : OUT_CLOBBER); while (__any(top == SENT) && --budget >= 0);
            S4S(2)
            TT(u >= 123, 2)
            lane = opaque_lane(); n = lane & 15; g = lane >> 4;
#pragma unroll
            for (int t = 0; t < 4; ++t) *(LAS f32x4*)(lds + O_STG + (w * 16 + n) * 272 + t * 64 + g * 16) = (f32x4){acc[4 * t], acc[4 * t + 1], acc[4 * t + 2], acc[4 * t + 3]};
        }
        WG_BARRIER();
        S4S(3)
        const int tok = 4 * w + g, c4 = 4 * n;
        f32x4 r[4]; u16 bv[4];
#pragma unroll
        for (int ww = 0; ww < 4; ++ww) { r[ww] = *(LAS f32x4*)(lds + O_STG + (ww * 16 + tok) * 272 + c4 * 4); bv[ww] = *(LAS u16*)(lds + O_B + (c4 + ww) * 40 + tok * 2); }
        FENCE();
        const f32x4 v = gt * ((r[0] + r[1]) + (r[2] + r[3])) + (1.0f - gt) * (f32x4){bf2f(bv[0]), bf2f(bv[1]), bf2f(bv[2]), bf2f(bv[3])};
        __builtin_amdgcn_raw_buffer_store_b64((u32x2){pack2(v[0], v[1]), pack2(v[2], v[3])}, ru, sp && tok >= 8 ? OOB : (tok * DM + 64 * nb + c4) * 2, t0 * (DM * 2), OUT_AUX);
        S4S(4)
        TT(u >= 123, 3)
        TT(u >= 109 && u < 116, 4)
    }
}

constexpr int L_MEMS = 0;
constexpr int L_Q = 9216;
constexpr int L_KO = L_Q + 2560;
constexpr int L_KX = 16384;
constexpr int L_W = 32768;
constexpr int C_RING = 8;
static_assert(S_TOTAL <= L_SINK && L_W + C_RING * 16384 <= L_TOTAL && L_KO + 8 * 576 <= L_KX, "LDS budget");
static_assert(P_STG + P_RING * 16384 <= L_TOTAL, "LDS budget");

DI int pos2col(int p) { return p < 512 ? (p & ~63) + 16 * (p & 3) + ((p & 63) >> 2) : p; }
DI void unit_rc(int u, int& i, int& p0) {
    if (u < 512) { const int G = u >> 3, r = u & 7; i = 2 * (G & 3) + (r >> 2); p0 = 32 * (G >> 2) + 8 * (r & 3); }
    else { const int v = u - 512; i = (v >> 1) & 7; p0 = 512 + 16 * (v >> 4) + 8 * (v & 1); }
}
DI void mem0_arrange(LAS char* lds, int raw, int tid) {
#pragma unroll
    for (int k = 0; k < 2; ++k) {
        int i, p0; unit_rc(tid + 256 * k, i, p0);
        unsigned r[4];
#pragma unroll
        for (int j = 0; j < 4; ++j) r[j] = *(LAS unsigned*)(lds + raw + (i * DM + pos2col(p0)) * 2 + 32 * j);
        *(LAS u32x4*)(lds + L_MEMS + (tid + 256 * k) * 16) = (u32x4){__builtin_amdgcn_perm(r[1], r[0], 0x05040100), __builtin_amdgcn_perm(r[3], r[2], 0x05040100),
                                                                     __builtin_amdgcn_perm(r[1], r[0], 0x07060302), __builtin_amdgcn_perm(r[3], r[2], 0x07060302)};
    }
    int i, p0; unit_rc(512 + (tid & 63), i, p0);
    *(LAS u32x4*)(lds + L_MEMS + 8192 + (tid & 63) * 16) = *(LAS u32x4*)(lds + raw + (i * DM + p0) * 2);
}
DI unsigned read_canaries(int s, rsrc_t rs, int lane, int& budget) {
    const int o = 2 * OFF_CAN + (s & 3) * 16384 + (lane >> 2) * 128 + (lane & 3) * 16;
    u32x2 v;
    for (;;) {
        v = ld64(rs, o, 0);
        asm volatile("" ::: "memory");
        if (__all(v.y == (unsigned)(s + 1))) break;
        if (--budget < 0) break;
    }
    const unsigned t = 8 * now();
    return t + (unsigned)wave_max32(min(max((int)(v.x - t), -0x4000), 0x4000));
}
DI u32x8 sm_frag(LAS char* lds, int sl, int r0, int sa, int sH, int cb, int lo8) {
    auto rd = [&](int r) { return tr_read(lds, sl + r * 256 + ((cb ^ (2 * swz(r))) * 16) + lo8); };
    const u32x2 a = rd(r0), b = rd(r0 + sa), c = rd(r0 + sH), d = rd(r0 + sa + sH);
    return (u32x8){a.x, a.y, b.x, b.y, c.x, c.y, d.x, d.y};
}
#define GS(slot, i)
#define GV(slot, i, v)
DI void owner_loop(const Args& a, int G, rsrc_t rs, unsigned tbase, unsigned cbase, int& budget) {
    const int lane = threadIdx.x & 63, n = lane & 15, g = lane >> 4;
    const bool ex = G >= 64;
    const u32x4 sent4 = {SENT, SENT, SENT, SENT};
    const int js = (n & 2) ? 8 : ex ? (n & 4) + 2 * (n & 1) + (n >> 3) : 2 * (n >> 2) + (n & 1);
    u32x4 selu;
#pragma unroll
    for (int j = 0; j < 4; ++j) selu[j] = (js == 2 * j ? 0x3F80u : 0u) | (js == 2 * j + 1 ? 0x3F800000u : 0u);
    const int o = cmap(G * 4096 + lane * 16);
    const int so = (n < 8 || NREP > 1) ? cmap((n >> 3) * 16384 + G * 128 + (g & 1) * 4 + (ex ? (n & 4) * 16 + (g >> 1) * 32 + (n & 3) * 8 : (g >> 1) * 64 + (n & 7) * 8)) : OOB;
    unsigned fails = 0;
    const int c8 = G >= 64 ? a.c1 >> 16 : G < 8 ? a.c1 >> 8 & 255 : a.c1 & 255;
    const bool gv_on = true; (void)gv_on;
    unsigned oT = now(), oC = (unsigned)__builtin_readcyclecounter(), oR;
    { const unsigned r = (unsigned)(512.0f * (float)(oC - cbase) * __builtin_amdgcn_rcpf((float)(oT - tbase))); asm volatile("s_nop 0\n\tv_readfirstlane_b32 %0, %1" : "=s"(oR) : "v"(r)); }
    oT = 8 * oT + 16;
    budget = __builtin_amdgcn_readfirstlane(budget);
    const int ca = 2 * OFF_CAN + (lane >> 2) * 128 + (lane & 3) * 16;
    for (int s = 0; s < NSEG - 1; ++s) {
        const int pb = 2 * (OFF_PART + (s & 1) * PART_BYTES);
        int np; unsigned tc;
        asm volatile(OWNER_ASM : [np] "=&s"(np), [tm] "=&s"(tc), [bud] "+s"(budget), [oT] "+s"(oT), [oC] "+s"(oC), [oR] "+s"(oR)
            : [c8] "s"(c8), [ca] "v"(ca), [cs] "s"((s & 3) * 16384), [tag] "s"(s + 1), [o] "v"(o), [so] "v"(so), [sel] "v"(selu), [rs] "s"(rs), [pb] "s"(pb), [mb0] "s"(2 * (OFF_MEMB + ((s + 1) & 3) * MEMB_STRIDE)),
              [mb1] "s"(2 * (OFF_MEMB + ((s + 1) & 3) * MEMB_STRIDE + 32768)) : OWNER_CLOBBER);
        (void)wait_until(now() + a.rd);
#pragma unroll
        for (int r = 0; r < 4; ++r) st128(sent4, rs, o + r * 1024, pb);
        if (s > 1) { st32(SENT, rs, so, 2 * (OFF_MEMB + ((s - 1) & 3) * MEMB_STRIDE)); if (NREP > 2) st32(SENT, rs, so, 2 * (OFF_MEMB + ((s - 1) & 3) * MEMB_STRIDE + 32768)); }
        fails += np > 1;
        { const unsigned t = now(); (void)wait_until(t + ((int)(tc - 8 * t + a.c2) >> 3) + a.cw); }
    }
}
DI void role_owner(const Args& a, unsigned lds_base, int ow, rsrc_t rw, int& budget) {
    const int tid = threadIdx.x, lane = tid & 63, w = __builtin_amdgcn_readfirstlane(tid >> 6);
    const unsigned tbase = now(), cbase = (unsigned)__builtin_readcyclecounter();
    if (ow != 63 && w) { prefetch(a, lds_base, ow & 3, (ow >> 2) * 3 + w - 1, (ow & 3) == 3 ? 45 : 48); VM_DRAIN(); }
    if (w >= 2 || (w == 1 && ow >= 8)) return;
    const u32x2 tag = launch_tag(a);
    if (ow == 63) {
        unsigned best[2] = {~0u, ~0u};
        for (int r = 0; r < 10; ++r) {
            unsigned long long t0, t1;
            asm volatile("s_memtime %0\n\ts_waitcnt lgkmcnt(0)" : "=s"(t0) :: "memory");
            st32(SENT, rw, lane == 0 ? (r & 1) * 8192 + (r >> 1) * 4096 * 5 / 4096 * 16384 + ((r >> 1) & 1) * 4096 : OOB, 0);
            asm volatile("s_waitcnt vmcnt(0)\n\ts_memtime %0\n\ts_waitcnt lgkmcnt(0)" : "=s"(t1) :: "memory");
            best[r & 1] = min(best[r & 1], (unsigned)(t1 - t0));
        }
        __builtin_amdgcn_raw_buffer_store_b64((u32x2){(tag.x & ~1u) | (best[1] < best[0]), tag.y}, rw, lane == 0 ? F_INIT + 32 * 128 : OOB, 0, 16);
    }
    const int sel = wait_init(rw, tag, budget, 32);
    const rsrc_t rs = __builtin_amdgcn_make_buffer_rsrc(a.ws + sel * 8192, 0, COMM_BYTES - 8192, 0x00020000);
    owner_loop(a, ow + 64 * w, rs, tbase, cbase, budget);
}

template <int O, class V> DI void put8(V& d, u32x8 f) {
#pragma unroll
    for (int j = 0; j < 8; ++j) d[O + j] = f[j];
}
DI f32x4 SMF64(u32x4 a, u32x8 b, f32x4 c, int i) { asm volatile("s_nop 1\n\tv_smfmac_f32_16x16x64_bf16 %0, %1, %2, %3\n\ts_nop 7" : "+v"(c) : "v"(a), "v"(b), "v"(i)); return c; }
DI f32x4 SMF32(u32x2 a, u32x4 b, f32x4 c, int i) { asm volatile("s_nop 1\n\tv_smfmac_f32_16x16x32_bf16 %0, %1, %2, %3\n\ts_nop 7" : "+v"(c) : "v"(a), "v"(b), "v"(i)); return c; }
DI f32x4 MFV(u32x4 a, u32x4 b, f32x4 c) { asm volatile("s_nop 1\n\tv_mfma_f32_16x16x32_bf16 %0, %1, %2, %0\n\ts_nop 7" : "+v"(c) : "v"(a), "v"(b)); return c; }

DI void role_head(const Args& a, LAS char* lds, unsigned lds_base, int h, int tw, rsrc_t rw, int& budget) {
    const int tid = threadIdx.x, w = __builtin_amdgcn_readfirstlane(tid >> 6);
    char* ws = a.ws;
    const unsigned tbase = now(), cbase = (unsigned)__builtin_readcyclecounter();
    const bool gv_on = tw == 0; (void)gv_on;
    u32x32 A0, A1, A2, A3, A4, A5, A6, A7, R0, R1, R2, R3; u32x8 R4;
    u32x2 tag;
    {
        const int lane = tid & 63, n = lane & 15, g = lane >> 4;
#pragma unroll
        for (int j = 0; j < 3; ++j) glds16(a.mem0, min(4 * j + w, 8) * 1024 + lane * 16, lds_base + L_KX + min(4 * j + w, 8) * 1024);
        const int tq = n >> 2, tc = (n & 3) >> 1, lo8 = (n & 1) * 8;
        const int r_nat = 16 * (g >> 1) + 4 * (g & 1) + (tq & 1) + 8 * (tq >> 1);
        const int r_sig = 16 * (tq & 1) + 4 * (g >> 1) + (g & 1) + 2 * (tq >> 1);
        const int r_o = 16 * (tq & 1) + 8 * (g >> 1) + 4 * (g & 1) + (tq >> 1);
#define WFRAG(sl, kb, t) ((kb) < 8 ? sm_frag(lds, sl, r_sig, 32, 8, 4 * w + 2 * (t) + tc, lo8) : sm_frag(lds, sl, r_nat, 2, 32, 4 * w + 2 * (t) + tc, lo8))
        constexpr int VKB[10] = {2, 5, 6, 7, 8, 0, 0, 3, 4, 1};
        const u32x4 ro = ring_offs(w, lane, HD);
        auto issue = [&](int i) {
            const unsigned slot = lds_base + L_W + (i % C_RING) * 16384 + w * 4096;
            if (i < 9) ring_issue(a.Wq + (size_t)(64 * i) * HD + h * DH, ro, slot);
            else if (i < 18) ring_issue(a.Wk + (size_t)(64 * (i - 9)) * HD + h * DH, ro, slot);
            else if (i == 23) ring_issue(a.Wo + (size_t)h * DH * DM + 512, ring_offs(w, lane, [](int r, int c) { return (r + 64 * (c >> 3)) * (DM * 2) + (c & 7) * 16; }), slot);
            else if (i < 28) ring_issue(a.Wv + (size_t)(64 * VKB[i - 18]) * HD + h * DH, ro, slot);
            else ring_issue(a.Wo + (size_t)(h * DH + 64 * ((i & 1) ^ ((i - 28) >> 1))) * DM + 256 * tw + 128 * (i & 1), ring_offs(w, lane, DM), slot);
        };
        auto keep = [&](int at, u32x8 f) {
            *(LAS u32x4*)(lds + at + lane * 16) = (u32x4){f[0], f[1], f[2], f[3]};
            *(LAS u32x4*)(lds + at + 1024 + lane * 16) = (u32x4){f[4], f[5], f[6], f[7]};
        };
        u32x8 hold[4] = {}, wex;
#pragma unroll
        for (int i = 0; i < C_RING - 1; ++i) issue(i);
        tag = launch_tag(a);
        ring_wait<4 * (C_RING - 1)>();
        mem0_arrange(lds, L_KX, tid);
        static_for<33>([&](auto ic) {
            constexpr int i = decltype(ic)::value, rem = i > 31 ? 0 : (31 - i < C_RING - 2 ? 31 - i : C_RING - 2);
            if constexpr (i == 28 || i == 30) return;
            ring_wait<4 * rem>();
            if constexpr (i + C_RING - 1 < 32) issue(i + C_RING - 1);
            if constexpr (i > 24) {
                if constexpr (i <= 29) { const int sp = L_W + (i == 29 ? 3 : i - 25) * 16384 + w * 4096; keep(sp, hold[0]); keep(sp + 2048, i == 29 ? wex : hold[1]); }
                else {
#pragma unroll
                    for (int T = 0; T < 4; ++T) keep(L_W + (2 * i - 58) * 16384 + w * 8192 + T * 2048, hold[T]);
                }
            }
            const int sl = L_W + (i % C_RING) * 16384;
            if constexpr (i < 16) {
                constexpr int kb = i % 9;
                u32x32& A = i < 2 ? A0 : i < 4 ? A1 : i < 6 ? A2 : i < 8 ? A3 : i < 10 ? A4 : i < 12 ? A5 : i < 14 ? A6 : A7;
                put8<16 * (i & 1)>(A, WFRAG(sl, kb, 0)); put8<16 * (i & 1) + 8>(A, WFRAG(sl, kb, 1));
                if constexpr (i == 1) asm volatile("" : "+{a[0:31]}"(A0));
                if constexpr (i == 3) asm volatile("" : "+{a[32:63]}"(A1));
                if constexpr (i == 5) asm volatile("" : "+{a[64:95]}"(A2));
                if constexpr (i == 7) asm volatile("" : "+{a[96:127]}"(A3));
                if constexpr (i == 9) asm volatile("" : "+{a[128:159]}"(A4));
                if constexpr (i == 11) asm volatile("" : "+{a[160:191]}"(A5));
                if constexpr (i == 13) asm volatile("" : "+{a[192:223]}"(A6));
                if constexpr (i == 15) asm volatile("" : "+{a[224:255]}"(A7));
            }
            else if constexpr (i == 16) { put8<16>(R0, WFRAG(sl, 7, 0)); put8<24>(R0, WFRAG(sl, 7, 1)); }
            else if constexpr (i == 17) { put8<0>(R1, WFRAG(sl, 8, 0)); put8<8>(R1, WFRAG(sl, 8, 1)); }
            else if constexpr (i == 18) { put8<24>(R1, WFRAG(sl, 2, 0)); put8<0>(R2, WFRAG(sl, 2, 1)); }
            else if constexpr (i == 19) { put8<8>(R2, WFRAG(sl, 5, 0)); put8<16>(R2, WFRAG(sl, 5, 1)); }
            else if constexpr (i == 20) { put8<24>(R2, WFRAG(sl, 6, 0)); put8<0>(R3, WFRAG(sl, 6, 1)); }
            else if constexpr (i == 21) { put8<8>(R3, WFRAG(sl, 7, 0)); put8<16>(R3, WFRAG(sl, 7, 1)); }
            else if constexpr (i == 22) { put8<24>(R3, WFRAG(sl, 8, 0)); R4 = WFRAG(sl, 8, 1); }
            else if constexpr (i == 23) wex = sm_frag(lds, sl, r_o, 2, 32, 8 * (w >> 1) + 4 * tw + 2 * (w & 1) + tc, lo8);
            else if constexpr (i < 27) { hold[0] = WFRAG(sl, 0, 0); hold[1] = WFRAG(sl, 0, 1); }
            else if constexpr (i == 27) { hold[0] = WFRAG(sl, 1, 0); put8<16>(R1, WFRAG(sl, 1, 1)); }
            else if constexpr (i < 32) {
#pragma unroll
                for (int T = 0; T < 4; ++T) hold[T] = sm_frag(lds, sl - 16384 + (w >> 1) * 16384, r_o, 2, 32, 8 * (w & 1) + 2 * T + tc, lo8);
            }
        });
        const int ai = 2 * (n >> 2) + (n & 1), ahf = (n >> 1) & 1;
        R0[0] = R0[1] = R0[2] = R0[3] = 0x3F803F80u;
        R0[4] = ahf ? 0xEEEE : 0x4444;
        R0[5] = lds_base + L_MEMS + (g >> 1) * 512 + ai * 64 + (g & 1) * 32 + ahf * 16;
        R0[6] = lds_base + L_MEMS + 8192 + g * 256 + ai * 32 + ahf * 16;
        R0[7] = lane * 16;
        R0[8] = lds_base + L_Q + 2 * g * 320 + (16 * w + n) * 4;
        R0[9] = lds_base + L_KO + 2 * g * 576 + (16 * w + (n & 8) + 4 * (n & 1) + ((n >> 1) & 3)) * 4;
        R0[10] = lds_base + L_Q + ai * 320 + g * 16;
        R0[11] = lds_base + L_KO + ai * 576 + g * 32 + ahf * 16;
        R0[12] = lds_base + L_KX + 8192 + w * 2048 + (g >> 1) * 256 + n * 16 + (g & 1) * 8;
        R0[13] = lds_base + L_W + w * 4096 + lane * 16;
        R0[14] = cmap(((2 * (4 * tw + w) + (n >> 3)) * 4 + g) * 4096 + h * 128 + (n & 7) * 16);
        R0[15] = (g >> 1) == (w >> 1) ? cmap((64 + 2 * (2 * tw + (w & 1)) + (g >> 1)) * 4096 + h * 128 + (g & 1) * 64 + n * 4) : OOB;
        asm volatile("" : "+{v[120:151]}"(R0), "+{v[152:183]}"(R1), "+{v[184:215]}"(R2), "+{v[216:247]}"(R3), "+{v[248:255]}"(R4));
    }
    const int sel = wait_init(rw, tag, budget, 33 + h);
    int kv_rdy = 0;
    const rsrc_t rs = __builtin_amdgcn_make_buffer_rsrc(ws + sel * 8192, 0, COMM_BYTES - 8192, 0x00020000);
    auto dma = [&](int voff, int soff, unsigned dst) { bdma(rw, voff, soff, dst); };
    auto fetch_kv = [&](int s, int lane) {
        const int n = lane & 15, g = lane >> 4;
        const int kxs = (g >> 1) * 1024 + ((g & 1) * 32 + 16 * (n >> 3) + (n & 7)) * 16;
        const int vxs = (g >> 1) * 1024 + ((g & 1) * 32 + n) * 16;
        const int so_ = (s * 32 + h) * 8192 + w * 2048;
        const unsigned xdst = lds_base + L_KX + w * 2048;
        dma(kxs, OFF_KF + so_, xdst); dma(kxs, OFF_KF + so_ + 128, xdst + 1024);
        dma(vxs, OFF_VF + so_, xdst + 8192); dma(vxs, OFF_VF + so_ + 256, xdst + 8192 + 1024);
    };
    const int fo = F_KVM + h * 256 + tw * 128;
    u32x8 scon;
    scon[0] = w; scon[1] = 2 * OFF_CAN + (h >> 1) * 128 + (2 * (h & 1) + tw) * 16; scon[2] = F_KV + h * 512 + w * 128; scon[3] = 65536 + w * 4096;
    scon[4] = OFF_KF + h * 8192 + w * 2048; scon[5] = fo; scon[6] = tw - 1; scon[7] = (tw ? OFF_VM : OFF_KM) + (h - NH) * 2048 + w * 512;
    u32x8 S1 = {0u, 0u, (unsigned)kv_rdy, (unsigned)budget, (unsigned)a.d0, 0xFFFFFFFFu / 22, (unsigned)(a.c2 + a.b0), 0u};
    u32x8 S2 = {0u, 0u, 0u, (unsigned)a.up, (unsigned)a.dn, (unsigned)a.c2, 0u, 1u};
    fetch_kv(0, opaque_lane());
    __builtin_amdgcn_raw_buffer_store_b64((u32x2){8 * now() + a.e0, 1u}, rs, tid == 0 ? 0 : OOB, scon[1], 16);
    asm volatile("s_waitcnt vmcnt(1)" ::: "memory");
    u32x2 vm = {};
    u32x8 S3;
    {
        const unsigned t = now(), c = (unsigned)__builtin_readcyclecounter();
        const float r = (float)(c - cbase) * __builtin_amdgcn_rcpf(8.0f * (float)(t - tbase));
        S2[1] = 8 * t + 16; S1[5] = (unsigned)(4294967296.0f * __builtin_amdgcn_rcpf(r));
        S3 = (u32x8){c, (unsigned)(4096.0f * r), 2u * OFF_MEMB + ((h >> 3) & (NREP - 1)) * 32768, w == 3 ? ~0u : 0u};
    }
    for (;;) {
        asm volatile(STEP_ASM
            : "+{v[96:97]}"(vm), "+{v[0:7]}"(S1), "+{v[8:15]}"(S2), "+{v[16:23]}"(S3), "+{v[24:31]}"(scon)
            : "{a[0:31]}"(A0), "{a[32:63]}"(A1), "{a[64:95]}"(A2), "{a[96:127]}"(A3), "{a[128:159]}"(A4), "{a[160:191]}"(A5), "{a[192:223]}"(A6), "{a[224:255]}"(A7),
              "{v[120:151]}"(R0), "{v[152:183]}"(R1), "{v[184:215]}"(R2), "{v[216:247]}"(R3), "{v[248:255]}"(R4),
              [rs] "s"(rs), [rw] "s"(rw)
            : STEP_CLOBBER);
        const int s = __builtin_amdgcn_readfirstlane(S1[0]);
        const unsigned st = __builtin_amdgcn_readfirstlane(S1[7]);
        kv_rdy = __builtin_amdgcn_readfirstlane(S1[2]); budget = __builtin_amdgcn_readfirstlane(S1[3]);
        if (s == NSEG - 1) break;
        if (__builtin_expect(st & 2, 0)) {
            const int lane = opaque_lane();
            LAS char* z = (LAS char*)0;
            const f32x4 zero = {0, 0, 0, 0};
            const u32x4 ones = __builtin_shufflevector(R0, R0, 0, 1, 2, 3);
            const unsigned zr = lane >> 6;
            const int idx = R0[4];
            u32x4 qf[4];
            f32x4 s1 = zero, s2 = zero;
#pragma unroll
            for (int ks = 0; ks < 4; ++ks) {
                qf[ks] = *(LAS u32x4*)(z + R0[10] + ks * 64);
                s1 = MFV(*(LAS u32x4*)(lds + L_KX + lane * 16 + ks * 2048), qf[ks], s1);
                s2 = MFV(*(LAS u32x4*)(lds + L_KX + lane * 16 + ks * 2048 + 1024), qf[ks], s2);
            }
            f32x4 s0 = SMF64(*(LAS u32x4*)(z + R0[11]), __builtin_shufflevector(qf[0], qf[1], 0, 1, 2, 3, 4, 5, 6, 7), zero, idx);
            s0 = SMF64(*(LAS u32x4*)(z + R0[11] + 128), __builtin_shufflevector(qf[2], qf[3], 0, 1, 2, 3, 4, 5, 6, 7), s0, idx);
            const float sa = s0[0] + s0[2], sb = s0[1] + s0[3];
            float mx = fmaxf(fmaxf(fmaxf(fmaxf(s1[0], s1[1]), fmaxf(s1[2], s1[3])), fmaxf(fmaxf(s2[0], s2[1]), fmaxf(s2[2], s2[3]))), fmaxf(sa, sb));
            mx = fmaxf(mx, __shfl_xor(mx, 16)); mx = fmaxf(mx, __shfl_xor(mx, 32));
            auto ex2 = [&](float x, float y) { return pack2(__builtin_amdgcn_exp2f(x - mx), __builtin_amdgcn_exp2f(y - mx)); };
            const u32x4 px = {ex2(s1[0], s1[1]), ex2(s1[2], s1[3]), ex2(s2[0], s2[1]), ex2(s2[2], s2[3])};
            const unsigned pm = ex2(sa, sb);
            const f32x4 l = MFV(((u32x4){pm, zr, zr, zr}), ones, MFV(px, ones, zero));
            const u32x2 x0 = *(LAS u32x2*)(z + R0[12]), y0 = *(LAS u32x2*)(z + R0[12] + 1024), x1 = *(LAS u32x2*)(z + R0[12] + 512), y1 = *(LAS u32x2*)(z + R0[12] + 1536);
            const f32x4 o = SMF64(px, ((u32x8){x0.x, y0.x, x0.y, y0.y, x1.x, y1.x, x1.y, y1.y}), zero, idx) + SMF32(((u32x2){pm, zr}), ((u32x4){vm.x, vm.y, zr, zr}), zero, idx);
            const float i0 = __builtin_amdgcn_rcpf(l[0]), i1 = __builtin_amdgcn_rcpf(l[1]);
            *(LAS unsigned*)(z + R0[9] + 256) = pack2(o[0] * i0, o[2] * i0);
            *(LAS unsigned*)(z + R0[9] + 256 + 576) = pack2(o[1] * i1, o[3] * i1);
            WG_BARRIER();
            const u32x4 oa = *(LAS u32x4*)(z + R0[11] + 256 + (w >> 1) * 128), ob = *(LAS u32x4*)(z + R0[11] + 384 - (w >> 1) * 128);
            auto frag = [&](int at) { return __builtin_shufflevector(*(LAS u32x4*)(z + at), *(LAS u32x4*)(z + at + 1024), 0, 1, 2, 3, 4, 5, 6, 7); };
            f32x2 f[4];
#pragma unroll
            for (int T = 0; T < 4; ++T) f[T] = fold(SMF64(ob, frag(R0[13] + scon[3] + 32768 + T * 2048), SMF64(oa, frag(R0[13] + scon[3] + T * 2048), zero, idx), idx));
            const f32x2 fx = fold(SMF64(ob, frag((R0[13] ^ 8192) + 49152 + 2048), SMF64(oa, frag(R0[13] + 49152 + 2048), zero, idx), idx));
            const int pb = 2 * (OFF_PART + (s & 1) * PART_BYTES);
            st128((u32x4){pack2(f[0].x, f[1].x), pack2(f[2].x, f[3].x), pack2(f[0].y, f[1].y), pack2(f[2].y, f[3].y)}, rs, R0[14], pb);
            st32(pack2(fx.x, fx.y), rs, R0[15], pb);
        }
        if (__builtin_expect((st & 2) || kv_rdy <= s, 0)) {
            while (kv_rdy <= s) { kv_rdy = prog_min(rw, F_KV + h * 512 + w * 128); if (--budget < 0) break; }
            fetch_kv(s + 1, opaque_lane());
        }
        if (st & 2) { S1[1] = read_canaries(s, rs, opaque_lane(), budget) + S1[6]; S2[2] += 0x10000; }
        if ((int)(S1[1] - S2[1]) > 0x8000) { S2[1] = 8 * now() + 16; S3[0] = (unsigned)__builtin_readcyclecounter(); }
        S1[0] = s + 1; S1[2] = kv_rdy; S1[3] = budget;
    }
    TT(true, 0)
    st32((unsigned)(NSEG - 2), rw, opaque_lane() == 0 && w == 0 ? fo : OOB, 0);
}

__global__ void __launch_bounds__(256, 1) fused_kernel(Args a) {
    extern __shared__ __attribute__((aligned(16))) char smem[];
    LAS char* lds = (LAS char*)smem;
    const unsigned lds_base = (unsigned)(uintptr_t)lds;
    const int w = __builtin_amdgcn_readfirstlane(threadIdx.x >> 6);
    const int b = blockIdx.x, bx = b & 7, bi = b >> 3;
    if (bi >= 28) {
        const void* pc; unsigned len;
        asm volatile("s_getpc_b64 %0\n1:\ts_mov_b32 %1, .Lstep_end-1b" : "=s"(pc), "=s"(len));
        touch(pc, min((bi - 28) * 32768 + threadIdx.x * 128, len), lds_base);
    }
    asm volatile("" :: "s"(a.Wq), "s"(a.cw));
    const rsrc_t rw = __builtin_amdgcn_make_buffer_rsrc(a.ws, 0, WS_USED, 0x00020000);
    int budget = SPIN_MAX;
#define RT(i)
    if (bx < 4) {
        if (bi < 16) role_head(a, lds, lds_base, 8 * bx + (bi >> 1), bi & 1, rw, budget);
        else role_owner(a, lds_base, 4 * (bi - 16) + bx, rw, budget);
    } else {
        const int fx = bx - 4, no = fx < 3 ? 14 : 22, j = 14 * fx + bi;
        if (bi >= no || (j & 2)) {
            const unsigned t = now() + a.d1;
            if (bi >= no) { if (w) prefetch(a, lds_base, bx, (bi - no) * 3 + w - 1, 3 * (32 - no)); }
            else {
                const int sh = (j >> 2) * 2 + (j & 1);
#pragma unroll
                for (int i = 0; i < INIT_SHARE / 4096; ++i) st128((u32x4){SENT, SENT, SENT, SENT}, rw, threadIdx.x * 16 + i * 4096, sh * INIT_SHARE);
#pragma unroll
                for (int i = 0; i < 4; ++i) st128((u32x4){SENT, SENT, SENT, SENT}, rw, threadIdx.x * 16 + (i & 1) * 4096, (i & 2 ? OFF_VM : OFF_KM) + FAST * 65536 + sh * 8192);
                const u32x2 tag = launch_tag(a);
                VM_DRAIN();
                WG_BARRIER();
                __builtin_amdgcn_raw_buffer_store_b64(tag, rw, threadIdx.x == 0 ? F_INIT + sh * 128 : OOB, 0, 16);
                RT(3)
            }
            (void)wait_until(t);
        }
        if (bi >= no) {
            const int t = 18 * fx + bi - no;
            role_s4d(a, lds, t, rw, budget);
            RT(1)
            if (t < 9 * OUT_REP) role_outp(a, lds, lds_base, t % 9, t / 9, rw, budget);
        }
        else {
            if (!(j & 2)) role_kv(a, lds, lds_base, (j >> 2) * 2 + (j & 1), rw, budget);
            RT(1)
            role_qa(a, lds, lds_base, (j >> 2) * 2, (j & 2 ? 0 : FAST) + (j & 1), (j & 2 ? FAST : NSEG) - 2 + (j & 1), rw, budget);
        }
    }
    RT(2)
    TT(true, 5)
    if (budget < 0 && threadIdx.x == 0) st32(0x1000u + b, rw, F_ERR, 0);
}

static int g_c1 = 56, g_c1m = 52, g_c1x = 70, g_c2 = 372, g_b0 = 64, g_up = 16, g_fl = 8, g_dn = 3, g_d0 = 2040, g_d1 = 300, g_rd = 50, g_cw = 70, g_e0 = 165, g_x1 = 30, g_x2 = 106, g_x3 = 120, g_x4 = 104, g_x5 = 152, g_x6 = 145, g_x7 = 50;
extern "C" void kernel_launch(void* const* d_in, const int* in_sizes, int n_in, void* d_out, int out_size, void* d_ws, size_t ws_size, hipStream_t stream) {
    Args a;
    a.x = (const u16*)d_in[0]; a.mem0 = (const u16*)d_in[1];
    a.Wq = (const u16*)d_in[2]; a.Wk = (const u16*)d_in[3]; a.Wv = (const u16*)d_in[4]; a.Wo = (const u16*)d_in[5];
    a.lAr = (const u16*)d_in[8]; a.Aim = (const u16*)d_in[9]; a.Cr = (const u16*)d_in[10]; a.Ci = (const u16*)d_in[11];
    a.ldt = (const u16*)d_in[12]; a.Dsk = (const u16*)d_in[13]; a.gw = (const u16*)d_in[14];
    a.out = (u16*)d_out;
    a.ws = (char*)(((uintptr_t)d_ws + ((2u << 20) - 1)) & ~(uintptr_t)((2u << 20) - 1));
    a.c1 = g_c1 | g_c1m << 8 | g_c1x << 16; a.c2 = g_c2; a.d0 = g_d0; a.b0 = g_b0; a.d1 = g_d1; a.up = g_up | g_fl << 16; a.dn = g_dn; a.rd = g_rd; a.cw = g_cw; a.e0 = 8 * g_e0;
    a.xa = g_x1 | g_x2 << 8 | g_x5 << 16 | g_x7 << 24; a.xb = g_x6 * 0x101 | g_x3 << 16 | g_x4 << 24;
    (void)hipFuncSetAttribute((const void*)fused_kernel, hipFuncAttributeMaxDynamicSharedMemorySize, L_TOTAL);
    fused_kernel<<<256, 256, L_TOTAL, stream>>>(a);
}
```

```cpp
#include <hip/hip_runtime.h>
#include <hip/hip_bf16.h>
#include <stdint.h>

typedef unsigned short u16;
typedef __bf16 bf16x8 __attribute__((ext_vector_type(8)));
typedef __bf16 bf16x16 __attribute__((ext_vector_type(16)));
typedef short s16x4 __attribute__((ext_vector_type(4)));
typedef float f32x2 __attribute__((ext_vector_type(2)));
typedef float f32x4 __attribute__((ext_vector_type(4)));
typedef float f32x16 __attribute__((ext_vector_type(16)));
typedef unsigned u32x2 __attribute__((ext_vector_type(2)));
typedef unsigned u32x4 __attribute__((ext_vector_type(4)));
typedef unsigned u32x8 __attribute__((ext_vector_type(8)));
typedef unsigned u32x16 __attribute__((ext_vector_type(16)));
typedef unsigned u32x32 __attribute__((ext_vector_type(32)));
typedef short s16x8 __attribute__((ext_vector_type(8)));
#define LAS __attribute__((address_space(3)))
#define DI __device__ __forceinline__
typedef __amdgpu_buffer_rsrc_t rsrc_t;

constexpr int SEQ = 2048, DM = 576, NH = 32, DH = 128, HD = NH * DH, NM = 8, SEG = 32, NSEG = 64, NS = 128;
constexpr unsigned SENT = 0xFFFFFFFFu;
constexpr unsigned OOB = 0x7FFFFFF0u;
constexpr int SPIN_MAX = 1 << 20;

constexpr int OFF_PART = 0;
constexpr int PART_BYTES = 72 * 4096;
constexpr int OFF_MEMB = OFF_PART + 2 * PART_BYTES;
constexpr int MEMB_STRIDE = 4 * 16384;
constexpr int OFF_CAN = OFF_MEMB + 4 * MEMB_STRIDE;
constexpr int COMM_LOGICAL = OFF_CAN + 32768;
constexpr int COMM_BYTES = 2 * COMM_LOGICAL;
constexpr int F_ERR = COMM_BYTES;
constexpr int F_KV = F_ERR + 128;
constexpr int F_KVM = F_KV + 16384;
constexpr int F_O = F_KVM + 8192;
constexpr int F_H = F_O + 4096;
constexpr int F_S4 = F_H + 32768;
constexpr int F_DONE = F_S4 + 512;
constexpr int INIT_SHARE = 14 * 4096, INIT_BYTES = 32 * INIT_SHARE;
constexpr int F_INIT = INIT_BYTES;
constexpr int F_EPOCH = F_INIT + 16384;
static_assert(F_DONE + 128 <= INIT_BYTES, "");
static_assert(F_EPOCH + 128 <= (2 << 20), "hand-off region");
constexpr int OFF_KF = 2 << 20;
constexpr int OFF_VF = OFF_KF + SEQ * HD * 2;
constexpr int OFF_OF = OFF_VF + SEQ * HD * 2;
constexpr int OFF_KM = OFF_OF + SEQ * HD * 2;
constexpr int OFF_VM = OFF_KM + NSEG * NH * NM * DH * 2;
constexpr int FAST = NSEG - 4;
constexpr int OFF_BO = OFF_VM + NSEG * NH * NM * DH * 2;
constexpr int BO_ROW = 4352;
constexpr int OFF_GT = OFF_BO + DM * BO_ROW;
constexpr int OFF_DBG = OFF_GT + SEQ * 4;
constexpr int WS_USED = OFF_DBG + (4 << 20);

struct Args {
    const u16 *x, *mem0, *Wq, *Wk, *Wv, *Wo, *lAr, *Aim, *Cr, *Ci, *ldt, *Dsk, *gw;
    u16* out; char* ws;
    int c1, c2, d0, b0, d1, up, dn, rd, cw, e0;
    unsigned xa, xb;
};

DI float bf2f(u16 b) { return __uint_as_float((unsigned)b << 16); }
DI float bflo(unsigned w) { return __uint_as_float(w << 16); }
DI float bfhi(unsigned w) { return __uint_as_float(w & 0xFFFF0000u); }
DI u16 f2bf(float f) { return __builtin_bit_cast(u16, (__bf16)f); }
DI unsigned pack2(float lo, float hi) {
    typedef __bf16 bf16x2 __attribute__((ext_vector_type(2)));
    bf16x2 v = {(__bf16)lo, (__bf16)hi};
    return __builtin_bit_cast(unsigned, v);
}
#define WG_BARRIER() asm volatile("s_waitcnt lgkmcnt(0)\n\ts_barrier" ::: "memory")
#define VM_DRAIN() asm volatile("s_waitcnt vmcnt(0)" ::: "memory")
DI int cmap(int o) { return (o & 0x1FFF) | ((o >> 13) << 14); }
DI unsigned mx4(unsigned a, u32x4 v) { a = max(max(a, v.x), v.y); return max(max(a, v.z), v.w); }
DI unsigned now() { return (unsigned)__builtin_amdgcn_s_memrealtime(); }
DI unsigned wait_until(unsigned t) {
    unsigned v; int guard = 8192;
    do { v = now(); } while ((int)(t - v) >= 4 && --guard > 0);
    const int r = __builtin_amdgcn_readfirstlane((int)(t - v));
    asm volatile("s_cmp_lt_i32 %0, 1\n\ts_cbranch_scc1 1f\n\ts_nop 3\n\ts_cmp_lt_i32 %0, 2\n\ts_cbranch_scc1 1f\n\ts_nop 3\n\ts_cmp_lt_i32 %0, 3\n\ts_cbranch_scc1 1f\n\ts_nop 7\n1:" :: "s"(r) : "scc");
    return r > 0 ? t : v + 2;
}
DI void st128(u32x4 v, rsrc_t rs, int off, int soff) {
    __builtin_amdgcn_raw_buffer_store_b128(v, rs, off, soff, 16);
    asm volatile("s_nop 1" :: "v"(v) : "memory");
}
DI void st32(unsigned v, rsrc_t rs, int off, int soff) { __builtin_amdgcn_raw_buffer_store_b32(v, rs, off, soff, 16); }
DI u32x4 ld128(rsrc_t rs, int off, int soff) { return __builtin_amdgcn_raw_buffer_load_b128(rs, off, soff, 16); }
DI u32x2 ld64(rsrc_t rs, int off, int soff) { return __builtin_amdgcn_raw_buffer_load_b64(rs, off, soff, 16); }
DI unsigned ld32(rsrc_t rs, int off, int soff) { return __builtin_amdgcn_raw_buffer_load_b32(rs, off, soff, 16); }
DI f32x2 fold(f32x4 a) { return (f32x2){a[0], a[1]} + (f32x2){a[2], a[3]}; }
DI float dpp_xor1(float v) { return __int_as_float(__builtin_amdgcn_update_dpp(0, __float_as_int(v), 0xB1, 0xf, 0xf, true)); }
DI float red_sum_g(float v) {
    auto a = __builtin_amdgcn_permlane16_swap(__float_as_uint(v), __float_as_uint(v), false, false);
    v = __uint_as_float(a[0]) + __uint_as_float(a[1]);
    auto b = __builtin_amdgcn_permlane32_swap(__float_as_uint(v), __float_as_uint(v), false, false);
    return __uint_as_float(b[0]) + __uint_as_float(b[1]);
}
DI int wave_max32(int d) {
    d = max(d, __builtin_amdgcn_update_dpp(d, d, 0x121, 0xf, 0xf, false));
    d = max(d, __builtin_amdgcn_update_dpp(d, d, 0x122, 0xf, 0xf, false));
    d = max(d, __builtin_amdgcn_update_dpp(d, d, 0x124, 0xf, 0xf, false));
    d = max(d, __builtin_amdgcn_update_dpp(d, d, 0x128, 0xf, 0xf, false));
    return __builtin_amdgcn_readfirstlane(max(max(__builtin_amdgcn_readlane(d, 0), __builtin_amdgcn_readlane(d, 16)), max(__builtin_amdgcn_readlane(d, 32), __builtin_amdgcn_readlane(d, 48))));
}
DI u32x2 tr_read(LAS char* lds, int addr) { return __builtin_bit_cast(u32x2, __builtin_amdgcn_ds_read_tr16_b64_v4i16((LAS s16x4*)(lds + addr))); }
DI void wait_prog(rsrc_t rw, int off, bool act, int thr, int& budget) {
    for (;;) {
        const int v = (int)ld32(rw, off, 0);
        asm volatile("" ::: "memory");
        if (__all(!act || v >= thr)) break;
        if (--budget < 0) break;
        __builtin_amdgcn_s_sleep(1);
    }
}
DI u32x2 launch_tag(const Args& a) {
    unsigned long long z;
    asm volatile("s_load_dwordx2 %0, %1, 0x0\n\ts_waitcnt lgkmcnt(0)" : "=s"(z) : "s"(a.ws + F_EPOCH) : "memory");
    z += 0x9E3779B97F4A7C15ull;
    z = (z ^ (z >> 30)) * 0xBF58476D1CE4E5B9ull; z = (z ^ (z >> 27)) * 0x94D049BB133111EBull; z ^= z >> 31;
    return (u32x2){(unsigned)z, (unsigned)(z >> 32)};
}
DI int wait_init(rsrc_t rw, u32x2 tag, int& budget, int more = 0) {
    const int l = threadIdx.x & 63, o = F_INIT + (l < 32 ? l : l == 63 ? min(more, 32) : more) * 128;
    u32x2 v;
    for (;;) {
        v = ld64(rw, o, 0);
        asm volatile("" ::: "memory");
        if (__all(((v.x ^ tag.x) >> 1) == 0 && v.y == tag.y)) break;
        if (--budget < 0) break;
        __builtin_amdgcn_s_sleep(2);
    }
    return __builtin_amdgcn_readlane(v.x, 63) & 1;
}
DI void glds16(const void* base, int voff, unsigned lds_dst) {
    unsigned keep;
    asm volatile("s_mov_b32 %0, m0\n\ts_mov_b32 m0, %3\n\ts_nop 0\n\tglobal_load_lds_dwordx4 %1, %2\n\ts_mov_b32 m0, %0" : "=&s"(keep) : "v"(voff), "s"(base), "s"(lds_dst) : "memory");
}
DI void bdma(rsrc_t rs, int voff, int soff, unsigned lds_dst) {
    unsigned keep;
    asm volatile("s_mov_b32 %0, m0\n\ts_mov_b32 m0, %4\n\ts_nop 0\n\tbuffer_load_dwordx4 %1, %2, %3 offen sc1 lds\n\ts_mov_b32 m0, %0" : "=&s"(keep) : "v"(voff), "s"(rs), "s"(soff), "s"(lds_dst) : "memory");
}
DI int swz(int r) { return ((r & 3) ^ (((r >> 2) & 3) << 1) ^ (((r >> 4) & 1) << 2)) & 7; }
template <class F> DI u32x4 ring_offs(int w, int lane, F at) {
    u32x4 o;
#pragma unroll
    for (int j = 0; j < 4; ++j) { const int r = 16 * w + 4 * j + (lane >> 4); o[j] = at(r, (lane & 15) ^ (2 * swz(r))) - 1024 * j; }
    return o;
}
DI u32x4 ring_offs(int w, int lane, int ld) { return ring_offs(w, lane, [&](int r, int c) { return r * ld * 2 + c * 16; }); }
DI void ring_issue(const u16* src, u32x4 o, unsigned slot) {
    unsigned keep;
    asm volatile("s_mov_b32 %0, m0\n\ts_mov_b32 m0, %6\n\ts_nop 0\n\tglobal_load_lds_dwordx4 %1, %5\n\tglobal_load_lds_dwordx4 %2, %5 offset:1024\n\t"
                 "global_load_lds_dwordx4 %3, %5 offset:2048\n\tglobal_load_lds_dwordx4 %4, %5 offset:3072\n\ts_mov_b32 m0, %0"
                 : "=&s"(keep) : "v"(o.x), "v"(o.y), "v"(o.z), "v"(o.w), "s"(src), "s"(slot) : "memory");
}
#define PF_FIRST 2
constexpr int L_TOTAL = 163840, L_SINK = L_TOTAL - 256;
DI void touch(const void* base, int voff, unsigned lds_base) {
    unsigned keep;
    asm volatile("s_mov_b32 %0, m0\n\ts_mov_b32 m0, %3\n\ts_nop 0\n\tglobal_load_lds_dword %1, %2\n\ts_mov_b32 m0, %0" : "=&s"(keep) : "v"(voff), "s"(base), "s"(lds_base + L_SINK) : "memory");
}
DI void prefetch(const Args& a, unsigned lds_base, int x, int id, int n) {
    const int lane = threadIdx.x & 63;
    const int h0 = x < 4 ? 8 * x : (0x160E0800 >> (8 * (x - 4))) & 255, lpr = x == 5 ? 12 : x == 7 ? 20 : 16, per = 9 * lpr;
    const int first = PF_FIRST * lpr, end = (x < 4 ? 4 : 2) * per;
    const float rl = __builtin_amdgcn_rcpf((float)lpr);
    for (int i = first + id; i < end; i += n) {
        int m = (i >= per) + (i >= 2 * per) + (i >= 3 * per), j = i - m * per;
        if (x < 4 && m < 3) { const int b = i >> 4; m = b < 20 ? b >> 3 : min(b - 20, 2); j = (b < 16 ? b & 7 : b < 20 ? b - 12 : b < 23 ? 8 : b - 23) * 16 + (i & 15); }
        const int mm = m + (x >= 4);
        const int L = j * 64 + lane, row = (int)((L + 0.5f) * rl);
        touch(mm == 0 ? a.Wq : mm == 1 ? a.Wk : mm == 2 ? a.Wv : a.Wo, m == 3 ? h0 * (DH * DM * 2) + L * 128 : row * (HD * 2) + (L - row * lpr) * 128 + h0 * 256, lds_base);
    }
}
template <int N> DI void ring_wait() { asm volatile("s_waitcnt vmcnt(%0) lgkmcnt(0)\n\ts_barrier" :: "i"(N) : "memory"); }
template <int I> struct ic_t { static constexpr int value = I; };
template <int N, int I = 0, class F> DI void static_for(F&& f) { if constexpr (I < N) { f(ic_t<I>{}); static_for<N, I + 1>(f); } }
#define MFMA32(a, b, c) __builtin_amdgcn_mfma_f32_32x32x16_bf16(__builtin_bit_cast(bf16x8, a), __builtin_bit_cast(bf16x8, b), c, 0, 0, 0)
#define MFMA16(a, b, c) __builtin_amdgcn_mfma_f32_16x16x32_bf16(__builtin_bit_cast(bf16x8, a), __builtin_bit_cast(bf16x8, b), c, 0, 0, 0)

constexpr int S_CH = 9;
constexpr int S_XRS = 4096 + 32;
constexpr int S_XR = 0;
constexpr int S_RC = S_CH * S_XRS;
constexpr int S_RCS = 4192;
constexpr int S_KD = S_RC + 4 * S_RCS;
constexpr int S_K = S_KD + 48 * 128;
constexpr int S_TAB = S_K + 79 * 160 + 32;
constexpr int S_TROW = 1056;
constexpr int S_TOTAL = S_TAB + 57 * S_TROW;
static_assert(S_RC % 16 == 0 && S_TAB % 16 == 0 && 4 * 64 * 144 <= 57 * S_TROW, "");
DI f32x2 crot(f32x2 z) { return (f32x2){-z.y, z.x}; }
DI f32x2 cmul(f32x2 a, f32x2 z) { return __builtin_elementwise_fma(a.yy, crot(z), a.xx * z); }
DI f32x2 cexp_p(float ar, float ai, float p) {
    const float x = ai * p;
    const float hi = x * 0.15915494309189535f;
    const float lo = __builtin_fmaf(x, 0.15915494309189535f, -hi) + x * 6.420638316725915e-9f;
    const float rev = (hi - __builtin_rintf(hi)) + lo;
    const float m = __builtin_amdgcn_exp2f(ar * p * 1.4426950408889634f);
    return (f32x2){m * __builtin_amdgcn_cosf(rev), m * __builtin_amdgcn_sinf(rev)};
}
#define FENCE() __builtin_amdgcn_sched_barrier(0)
#define TT(c, i)
DI void role_s4d(const Args& a, LAS char* lds, int task, rsrc_t rw, int& budget) {
    const int tid = threadIdx.x, lane = tid & 63, w = __builtin_amdgcn_readfirstlane(tid >> 6);
    const int d0 = task * S_CH;
#define S4S(i)
    {
        const rsrc_t rx = __builtin_amdgcn_make_buffer_rsrc((void*)a.x, 0, SEQ * DM * 2, 0x00020000);
        const unsigned sh = (d0 & 1) * 16;
        u32x4 v[8]; unsigned e[8];
#pragma unroll
        for (int i = 0; i < 8; ++i) {
            const int o = (tid + 256 * i) * (DM * 2) + (d0 & ~1) * 2;
            v[i] = __builtin_amdgcn_raw_buffer_load_b128(rx, o, 0, 0); e[i] = __builtin_amdgcn_raw_buffer_load_b32(rx, o, 16, 0);
        }
#pragma unroll
        for (int p = 0; p < 2; ++p) {
            const int tok = 32 * task + 8 * w + 4 * p + (lane >> 4);
            float acc = 0.f;
#pragma unroll
            for (int i = 0; i < 5; ++i) {
                const int ch = (lane & 15) + 16 * i;
                const u32x4 gv = ch < 72 ? *(const u32x4*)(a.gw + ch * 8) : (u32x4){0u, 0u, 0u, 0u};
                const u32x4 xv = *(const u32x4*)(a.x + (size_t)tok * DM + min(ch, 71) * 8);
#pragma unroll
                for (int j = 0; j < 4; ++j) acc += bflo(xv[j]) * bflo(gv[j]) + bfhi(xv[j]) * bfhi(gv[j]);
            }
            acc += __shfl_xor(acc, 1); acc += __shfl_xor(acc, 2); acc += __shfl_xor(acc, 4); acc += __shfl_xor(acc, 8);
            st32(__float_as_uint(1.0f / (1.0f + __expf(-acc))), rw, (lane & 15) == 0 ? OFF_GT + tok * 4 : OOB, 0);
        }
#pragma unroll
        for (int i = 0; i < 8; ++i) {
            const unsigned x5[5] = {__builtin_amdgcn_alignbit(v[i].y, v[i].x, sh), __builtin_amdgcn_alignbit(v[i].z, v[i].y, sh), __builtin_amdgcn_alignbit(v[i].w, v[i].z, sh),
                                    __builtin_amdgcn_alignbit(e[i], v[i].w, sh), e[i] >> sh};
#pragma unroll
            for (int c = 0; c < S_CH; ++c) *(LAS u16*)(lds + S_XR + c * S_XRS + (2047 - tid - 256 * i) * 2) = (u16)(x5[c >> 1] >> (16 * (c & 1)));
        }
    }
    if (tid < 8 * S_CH) *(LAS unsigned*)(lds + S_XR + (tid >> 3) * S_XRS + 4096 + (tid & 7) * 4) = 0;
    if (tid < 64) *(LAS unsigned*)(lds + S_RC + (tid >> 4) * S_RCS + 4096 + (tid & 15) * 4) = 0;
    for (int i = tid; i < 15 * 160 / 4; i += 256) *(LAS unsigned*)(lds + S_K + i * 4) = 0;
    int kd[8];
#pragma unroll
    for (int j = 0; j < 8; ++j) {
        const int p = (int)bf2f(f2bf((float)(8 * tid + j))), k = p >> 7, r = p & 7;
        const int row = p < 256 ? 8 * k + r : (p < 512 ? 8 + 4 * k + (r >> 1) : (p < 1024 ? 16 + 2 * k + (r >> 2) : 24 + k));
        kd[j] = S_KD + (row * 16 + ((p >> 3) & 15)) * 8;
    }
    const int n = tid & 127, nn = lane & 15, g = lane >> 4;
    u16 pA = a.lAr[d0 * NS + n], pI = a.Aim[d0 * NS + n], pCr = a.Cr[d0 * NS + n], pCi = a.Ci[d0 * NS + n], pT = a.ldt[d0], pD = a.Dsk[d0];
    for (int ch = 0; ch < S_CH; ++ch) {
        WG_BARRIER();
        S4S(0)
        const float dsk = bf2f(pD);
        {
            const u32x4 v = *(LAS u32x4*)(lds + S_XR + ch * S_XRS + tid * 16);
            const u32x2 e = *(LAS u32x2*)(lds + S_XR + ch * S_XRS + tid * 16 + 16);
            const unsigned s01 = __builtin_amdgcn_alignbit(v.y, v.x, 16), s12 = __builtin_amdgcn_alignbit(v.z, v.y, 16), s23 = __builtin_amdgcn_alignbit(v.w, v.z, 16);
            const unsigned s34 = __builtin_amdgcn_alignbit(e.x, v.w, 16), s45 = __builtin_amdgcn_alignbit(e.y, e.x, 16);
            *(LAS u32x4*)(lds + S_RC + tid * 16) = v;
            *(LAS u32x4*)(lds + S_RC + S_RCS + tid * 16) = (u32x4){s01, s12, s23, s34};
            *(LAS u32x4*)(lds + S_RC + 2 * S_RCS + tid * 16) = (u32x4){v.y, v.z, v.w, e.x};
            *(LAS u32x4*)(lds + S_RC + 3 * S_RCS + tid * 16) = (u32x4){s12, s23, s34, s45};
        }
        S4S(1)
        {
            const float dt = bf2f(f2bf(expf(bf2f(pT))));
            const float ar = -bf2f(f2bf(expf(bf2f(pA)))) * dt, ai = bf2f(pI) * dt;
            const f32x2 C = {2.f * bf2f(pCr) * dt, 2.f * bf2f(pCi) * dt};
            const int dn = min(d0 + ch + 1, DM - 1);
            pA = a.lAr[dn * NS + n]; pI = a.Aim[dn * NS + n]; pCr = a.Cr[dn * NS + n]; pCi = a.Ci[dn * NS + n]; pT = a.ldt[dn]; pD = a.Dsk[dn];
            const int to = S_TAB + n * 8;
            auto put = [&](int row, f32x2 v) { *(LAS f32x2*)(lds + to + row * S_TROW) = v; };
            const f32x2 z128 = cexp_p(ar, ai, 128.f);
            if (w < 2) {
                f32x2 E[8], G[4];
                E[1] = cexp_p(ar, ai, 1.f); G[0] = C;
#pragma unroll
                for (int r = 2; r < 8; ++r) E[r] = cmul(E[r >> 1], E[r - (r >> 1)]);
#pragma unroll
                for (int k = 1; k < 4; ++k) G[k] = cmul(G[k - 1], z128);
#pragma unroll
                for (int k = 0; k < 4; ++k)
#pragma unroll
                    for (int r = 0; r < 8; r += 1 + (k >> 1)) put(k < 2 ? 8 * k + r : 8 + 4 * k + (r >> 1), r ? cmul(E[r], G[k]) : G[k]);
            } else {
                const f32x2 z4 = cexp_p(ar, ai, 4.f), z8 = cexp_p(ar, -ai, 8.f);
                f32x2 G = cmul(C, cexp_p(ar, ai, 512.f)), B = z8;
                put(41, (f32x2){1.f, 0.f});
#pragma unroll
                for (int k = 1; k < 16; ++k) {
                    put(41 + k, B); B = cmul(B, z8);
                    if (k < 5) { put(22 + 2 * k, G); put(23 + 2 * k, cmul(G, z4)); }
                    else if (k < 14) put(27 + k, G);
                    G = cmul(G, z128);
                }
            }
        }
        WG_BARRIER();
        S4S(2)
        {
            const int bo = S_TAB + (41 + nn) * S_TROW + g * 16;
            f32x4 av[12], bv[12], acc[3] = {};
            if (w < 3) {
                const int ao = S_TAB + min(16 * w + nn, 40) * S_TROW + g * 16;
#pragma unroll
                for (int j = 0; j < 12; ++j) { av[j] = *(LAS f32x4*)(lds + ao + j * 64); bv[j] = *(LAS f32x4*)(lds + bo + j * 64); }
#pragma unroll
                for (int j = 0; j < 48; ++j) acc[j & 1] = __builtin_amdgcn_mfma_f32_16x16x4f32(av[j >> 2][j & 3], bv[j >> 2][j & 3], acc[j & 1], 0, 0, 0);
#pragma unroll
                for (int r = 0; r < 4; ++r) *(LAS float*)(lds + S_KD + ((16 * w + 4 * g + r) * 16 + nn) * 8) = acc[0][r] + acc[1][r];
            } else {
#pragma unroll
                for (int j = 0; j < 4; ++j) {
                    bv[j] = *(LAS f32x4*)(lds + bo + 768 + j * 64);
#pragma unroll
                    for (int t = 0; t < 3; ++t) av[3 * j + t] = *(LAS f32x4*)(lds + S_TAB + min(16 * t + nn, 40) * S_TROW + g * 16 + 768 + j * 64);
                }
#pragma unroll
                for (int j = 0; j < 16; ++j)
#pragma unroll
                    for (int t = 0; t < 3; ++t) acc[t] = __builtin_amdgcn_mfma_f32_16x16x4f32(av[3 * (j >> 2) + t][j & 3], bv[j >> 2][j & 3], acc[t], 0, 0, 0);
#pragma unroll
                for (int t = 0; t < 3; ++t)
#pragma unroll
                    for (int r = 0; r < 4; ++r) *(LAS float*)(lds + S_KD + ((16 * t + 4 * g + r) * 16 + nn) * 8 + 4) = acc[t][r];
            }
        }
        WG_BARRIER();
        S4S(3)
        {
            float kv[8];
#pragma unroll
            for (int j = 0; j < 8; ++j) { const f32x2 t = *(LAS f32x2*)(lds + kd[j]); kv[j] = t.x + t.y; }
            u32x4 hi, lo;
#pragma unroll
            for (int j = 0; j < 4; ++j) { hi[j] = pack2(kv[2 * j], kv[2 * j + 1]); lo[j] = pack2(kv[2 * j] - bflo(hi[j]), kv[2 * j + 1] - bfhi(hi[j])); }
            const int o = S_K + (15 + (tid >> 2)) * 160 + (tid & 3) * 16;
            *(LAS u32x4*)(lds + o) = hi; *(LAS u32x4*)(lds + o + 64) = lo;
        }
        WG_BARRIER();
        S4S(4)
        {
            const int st0 = 2047 - nn + 8 * g - 32 * w, cc = st0 & 3;
            const int ub = S_RC + cc * S_RCS + (st0 - cc) * 2 - 3872, kb = S_K + (3 + nn - w) * 160 + g * 16;
            f32x4 y[4][2] = {};
            u32x4 F[2][16];
            auto rd = [&](int i, int f) {
                if (f & 1) { const int o = ub + 3872 - 256 * i - 1024 * (f >> 2) - 32 * ((f >> 1) & 1); const u32x2 x = *(LAS u32x2*)(lds + o), z = *(LAS u32x2*)(lds + o + 8); F[i & 1][f] = (u32x4){x.x, x.y, z.x, z.y}; }
                else F[i & 1][f] = *(LAS u32x4*)(lds + kb + (12 - 4 * i + 16 * ((f >> 1) & 3)) * 160 + (f >> 3) * 64);
            };
#pragma unroll
            for (int f = 0; f < 16; ++f) rd(0, f);
            FENCE();
#pragma unroll
            for (int i = 0; i < 4; ++i) {
                int c = 0;
#pragma unroll
                for (int hl = 0; hl < 2; ++hl)
#pragma unroll
                    for (int q = 0; q < 4; ++q)
#pragma unroll
                        for (int T = q; T < 4; ++T) {
                            y[T][0] = MFMA16(F[i & 1][4 * q + 1], F[i & 1][8 * hl + 2 * (T - q)], y[T][0]);
                            y[T][1] = MFMA16(F[i & 1][4 * q + 3], F[i & 1][8 * hl + 2 * (T - q)], y[T][1]);
                            if (i < 3 && c < 16) rd(i + 1, c);
                            ++c;
                            FENCE();
                        }
            }
#pragma unroll
            for (int T = 0; T < 4; ++T)
#pragma unroll
                for (int bt = 0; bt < 2; ++bt) *(LAS f32x4*)(lds + S_TAB + (w * 64 + 16 * T + nn) * 144 + bt * 64 + g * 16) = y[T][bt];
        }
        WG_BARRIER();
        S4S(5)
        {
            f32x4 y0 = {0, 0, 0, 0}, y1 = y0;
#pragma unroll
            for (int ww = 0; ww < 4; ++ww) {
                const int o = S_TAB + (ww * 64 + (tid >> 2)) * 144 + (tid & 3) * 32;
                y0 += *(LAS f32x4*)(lds + o); y1 += *(LAS f32x4*)(lds + o + 16);
            }
            const u32x4 u = *(LAS u32x4*)(lds + S_XR + ch * S_XRS + (255 - tid) * 16);
            st128((u32x4){pack2(y0[0] + bfhi(u.w) * dsk, y0[1] + bflo(u.w) * dsk), pack2(y0[2] + bfhi(u.z) * dsk, y0[3] + bflo(u.z) * dsk),
                          pack2(y1[0] + bfhi(u.y) * dsk, y1[1] + bflo(u.y) * dsk), pack2(y1[2] + bfhi(u.x) * dsk, y1[3] + bflo(u.x) * dsk)}, rw, tid * 16, OFF_BO + (d0 + ch) * BO_ROW);
        }
        S4S(6)
    }
    wait_init(rw, launch_tag(a), budget);
    VM_DRAIN();
    WG_BARRIER();
    if (tid == 0) st32(1u, rw, F_S4 + task * 4, 0);
}

constexpr int XT_BYTES = 32 * 1152;
constexpr int P_XT = 0;
constexpr int P_STG = 2 * XT_BYTES;
struct WSlice { u32x4 f[36]; };
constexpr int P_RING = 5;
template <class F> DI void load_wslices(WSlice& W0, WSlice& W1, const u16* s0, const u16* s1, int w, int lane, LAS char* lds, unsigned lds_base, F also) {
    constexpr int NB = 18;
    const int r0 = 8 * (lane >> 5) + ((lane & 15) >> 2);
    const int cb = (4 * w + 2 * ((lane >> 4) & 1) + ((lane & 3) >> 1)), lo8 = (lane & 1) * 8;
    auto src = [&](int i) { return (i < 9 ? s0 : s1) + (size_t)(64 * (i % 9)) * HD; };
    const u32x4 ro = ring_offs(w, lane, HD);
#pragma unroll
    for (int i = 0; i < P_RING - 1; ++i) ring_issue(src(i), ro, lds_base + P_STG + i * 16384 + w * 4096);
    also();
    static_for<NB>([&](auto ic) {
        constexpr int i = decltype(ic)::value, rem = NB - 1 - i < P_RING - 2 ? NB - 1 - i : P_RING - 2;
        ring_wait<4 * rem>();
        if constexpr (i + P_RING - 1 < NB) ring_issue(src(i + P_RING - 1), ro, lds_base + P_STG + ((i + P_RING - 1) % P_RING) * 16384 + w * 4096);
        const int sl = P_STG + (i % P_RING) * 16384;
#pragma unroll
        for (int kk = 0; kk < 4; ++kk) {
            const int ra = r0 + 16 * kk, rb = ra + 4;
            const u32x2 lo = tr_read(lds, sl + ra * 256 + ((cb ^ (2 * swz(ra))) * 16) + lo8), hi = tr_read(lds, sl + rb * 256 + ((cb ^ (2 * swz(rb))) * 16) + lo8);
            (i < 9 ? W0 : W1).f[(i % 9) * 4 + kk] = (u32x4){lo.x, lo.y, hi.x, hi.y};
        }
        u32x4* f = (i < 9 ? W0 : W1).f + (i % 9) * 4;
        if constexpr (i < 16) asm volatile("" : "+a"(f[0]), "+a"(f[1]), "+a"(f[2]), "+a"(f[3]));
    });
}
DI u32x4 piece_pi(const f32x16& a, int qq) {
    return (u32x4){pack2(a[4 * qq], a[4 * qq + 8]), pack2(a[4 * qq + 1], a[4 * qq + 9]), pack2(a[4 * qq + 2], a[4 * qq + 10]), pack2(a[4 * qq + 3], a[4 * qq + 11])};
}
DI u32x4 piece_seq(const f32x16& a, int s2) {
    return (u32x4){pack2(a[8 * s2], a[8 * s2 + 1]), pack2(a[8 * s2 + 2], a[8 * s2 + 3]), pack2(a[8 * s2 + 4], a[8 * s2 + 5]), pack2(a[8 * s2 + 6], a[8 * s2 + 7])};
}
struct XTile {
    int xs[9], xa[4];
    DI void init(int w, int lane) {
#pragma unroll
        for (int i = 0; i < 9; ++i) { const int L = (i * 4 + w) * 64 + lane, r = L / 72, cs = L - 72 * r; xs[i] = r * 1152 + (cs ^ ((r >> 1) & 7)) * 16; }
        const int c32 = lane & 31, hf = lane >> 5;
#pragma unroll
        for (int j = 0; j < 4; ++j) xa[j] = P_XT + c32 * 1152 + (((2 * j + hf) ^ ((c32 >> 1) & 7)) * 16);
    }
    DI void dma(const Args& a, unsigned lds_base, int w, int s, int buf, int i) const {
        glds16((const char*)a.x + (size_t)min(s, NSEG - 1) * XT_BYTES, xs[i], lds_base + P_XT + buf * XT_BYTES + (i * 4 + w) * 1024);
    }
    DI void first(const Args& a, unsigned lds_base, int w, int lane, int s) {
        init(w, lane);
#pragma unroll
        for (int i = 0; i < 9; ++i) dma(a, lds_base, w, s, 0, i);
    }
};
template <bool TR, class F> DI void project(f32x16& acc0, f32x16& acc1, const WSlice& W0, const WSlice& W1, const XTile& X, const Args& a, LAS char* lds, unsigned lds_base, int w, int buf, int sn, F mid) {
    const int bo = buf * XT_BYTES;
    constexpr int PD = 5;
    u32x4 xf[PD];
#pragma unroll
    for (int i = 0; i < PD; ++i) xf[i] = *(LAS u32x4*)(lds + X.xa[i & 3] + bo + (i >> 2) * 128);
#pragma unroll
    for (int ks = 0; ks < 36; ++ks) {
        if (ks == 0) asm volatile("v_mfma_f32_32x32x16_bf16 %0, %1, %2, 0" : "=&v"(acc0) : "a"(W0.f[0]), "v"(xf[0]) : "memory");
        else asm volatile("v_mfma_f32_32x32x16_bf16 %0, %1, %2, %0" : "+v"(acc0) : "a"(W0.f[ks]), "v"(xf[ks % PD]) : "memory");
        if (TR) {
            if (ks == 0) asm volatile("v_mfma_f32_32x32x16_bf16 %0, %2, %1, 0" : "=&v"(acc1) : "a"(W1.f[0]), "v"(xf[0]) : "memory");
            else if (ks < 28) asm volatile("v_mfma_f32_32x32x16_bf16 %0, %2, %1, %0" : "+v"(acc1) : "a"(W1.f[ks]), "v"(xf[ks % PD]) : "memory");
            else asm volatile("v_mfma_f32_32x32x16_bf16 %0, %2, %1, %0" : "+v"(acc1) : "v"(W1.f[ks]), "v"(xf[ks % PD]) : "memory");
        } else {
            if (ks == 0) asm volatile("v_mfma_f32_32x32x16_bf16 %0, %1, %2, 0" : "=&v"(acc1) : "a"(W1.f[0]), "v"(xf[0]) : "memory");
            else if (ks < 28) asm volatile("v_mfma_f32_32x32x16_bf16 %0, %1, %2, %0" : "+v"(acc1) : "a"(W1.f[ks]), "v"(xf[ks % PD]) : "memory");
            else asm volatile("v_mfma_f32_32x32x16_bf16 %0, %1, %2, %0" : "+v"(acc1) : "v"(W1.f[ks]), "v"(xf[ks % PD]) : "memory");
        }
        if (ks + PD < 36) xf[ks % PD] = *(LAS u32x4*)(lds + X.xa[(ks + PD) & 3] + bo + ((ks + PD) >> 2) * 128);
        if (ks < 18 && (ks & 1)) X.dma(a, lds_base, w, sn, buf ^ 1, ks >> 1);
        if (ks == (TR ? 14 : 26)) mid();
    }
    asm volatile("s_nop 7\n\ts_nop 3" : "+v"(acc0), "+v"(acc1) :: "memory");
}
DI void role_kv(const Args& a, LAS char* lds, unsigned lds_base, int h, rsrc_t rw, int& budget) {
    const int tid = threadIdx.x, lane = tid & 63, w = __builtin_amdgcn_readfirstlane(tid >> 6);
    WSlice W0, W1;
    XTile X;
    u32x2 tag;
    load_wslices(W0, W1, a.Wk + h * DH, a.Wv + h * DH, w, lane, lds, lds_base, [&]() { X.first(a, lds_base, w, lane, 0); tag = launch_tag(a); });
    VM_DRAIN();
    WG_BARRIER();
    const int oo = h * 8192 + w * 2048 + lane * 16;
    const int fo = lane == 0 ? F_KV + h * 512 + w * 128 : OOB;
    for (int s = 0; s < NSEG; ++s) {
        f32x16 acc0, acc1;
        project<true>(acc0, acc1, W0, W1, X, a, lds, lds_base, w, s & 1, s + 1, [&]() {
            asm volatile("s_waitcnt vmcnt(7)" ::: "memory");
            st32((unsigned)(s - 1), rw, s ? fo : OOB, 0);
        });
        const int so = s * (NH * 8192);
        st128(piece_pi(acc0, 0), rw, oo, OFF_KF + so);
        st128(piece_pi(acc0, 1), rw, oo + 1024, OFF_KF + so);
        st128(piece_seq(acc1, 0), rw, oo, OFF_VF + so);
        st128(piece_seq(acc1, 1), rw, oo + 1024, OFF_VF + so);
        if (s == 0) { wait_init(rw, tag, budget); VM_DRAIN(); }
        asm volatile("s_waitcnt vmcnt(4)" ::: "memory");
        WG_BARRIER();
        if (s == 0) __builtin_amdgcn_raw_buffer_store_b64(tag, rw, tid == 0 ? F_INIT + (33 + h) * 128 : OOB, 0, 16);
    }
    VM_DRAIN();
    st32(NSEG, rw, fo, 0);
}

DI int prog_min(rsrc_t rw, int off) {
    const int v = (int)ld32(rw, off, 0);
    asm volatile("" ::: "memory");
    return -wave_max32(-v);
}
DI float red_max_g(float v) {
    auto a = __builtin_amdgcn_permlane16_swap(__float_as_uint(v), __float_as_uint(v), false, false);
    v = fmaxf(__uint_as_float(a[0]), __uint_as_float(a[1]));
    auto b = __builtin_amdgcn_permlane32_swap(__float_as_uint(v), __float_as_uint(v), false, false);
    return fmaxf(__uint_as_float(b[0]), __uint_as_float(b[1]));
}
#define M16 "v_mfma_f32_16x16x32_bf16 "
DI void role_qa(const Args& a, LAS char* lds, unsigned lds_base, int h0, int s0, int s1, rsrc_t rw, int& budget) {
    const int tid = threadIdx.x, lane = tid & 63, w = __builtin_amdgcn_readfirstlane(tid >> 6);
    const int n = lane & 15, g = lane >> 4, h = h0 + (w >> 1), tt = w & 1;
    WSlice W0, W1;
    XTile X;
    load_wslices(W0, W1, a.Wq + h0 * DH, a.Wq + (h0 + 1) * DH, w, lane, lds, lds_base, [&]() { X.first(a, lds_base, w, lane, s0); });
    const int oo = OFF_OF + (2 * tt + (n >> 3)) * 65536 + h * 2048 + g * 128 + (n & 7) * 16;
    const bool fast = s0 >= FAST;
    if (fast)
#pragma unroll
        for (int t = 0; t < 8; ++t) st128((u32x4){SENT, SENT, SENT, SENT}, rw, oo + (t & 3) * 512, (s0 + (t >> 2) * 2) * 262144);
    VM_DRAIN();
    WG_BARRIER();
    constexpr int Q_Q = P_STG, Q_KV = Q_Q + 16384, Q_M = Q_KV + 32768;
    static_assert(Q_M + 16384 <= P_STG + P_RING * 16384, "");
    const int fk = F_KV + h * 512 + (lane & 3) * 128, fm = F_KVM + h * 256 + (lane & 1) * 128;
    int pm_, m_rdy = -1, kv_rdy = -1;
    auto look = [&]() { pm_ = (int)ld32(rw, lane & 4 ? fk : fm, 0); };
    auto seen = [&]() { auto rl = [&](int i) { return __builtin_amdgcn_readlane(pm_, i); }; m_rdy = min(rl(0), rl(1)); kv_rdy = min(min(rl(4), rl(5)), min(rl(6), rl(7))); };
    auto wait_m = [&](int thr, unsigned& t) {
        for (;;) {
            const u32x2 v = ld64(rw, fm, 0);
            asm volatile("" ::: "memory");
            t = now();
            const int p0 = __builtin_amdgcn_readlane(v.x, 0), p1 = __builtin_amdgcn_readlane(v.x, 1);
            const int d0 = (int)(__builtin_amdgcn_readlane(v.y, 0) - 8 * t) >> 3, d1 = (int)(__builtin_amdgcn_readlane(v.y, 1) - 8 * t) >> 3;
            if (min(p0, p1) >= thr || --budget < 0) return max(p0 == thr ? d0 : -1000, p1 == thr ? d1 : -1000);
            (void)wait_until(t + min(max(min(d0, d1) + (int)(a.xa >> 16 & 255), 10), 200));
        }
    };
    auto proj = [&](int s) {
        f32x16 acc0, acc1;
        project<false>(acc0, acc1, W0, W1, X, a, lds, lds_base, w, ((s - s0) >> 1) & 1, s + 2, look);
        const int o = Q_Q + w * 2048 + lane * 16;
        *(LAS u32x4*)(lds + o) = piece_pi(acc0, 0); *(LAS u32x4*)(lds + o + 1024) = piece_pi(acc0, 1);
        *(LAS u32x4*)(lds + o + 8192) = piece_pi(acc1, 0); *(LAS u32x4*)(lds + o + 8192 + 1024) = piece_pi(acc1, 1);
    };
    const int fs = (tt ? OFF_VF : OFF_KF) + (g >> 1) * 1024 + ((g & 1) * 32 + 16 * (n >> 3) + (n & 7)) * 16;
    auto fetch = [&](int s) {
        const int so = (min(s, NSEG - 1) * NH + h) * 8192;
#pragma unroll
        for (int f = 0; f < 8; ++f) bdma(rw, fs, so + (f >> 1) * 2048 + (f & 1) * 128, lds_base + Q_KV + w * 8192 + f * 1024);
    };
    const int ms = OFF_KM + (lane & 7) * 256 + (lane >> 3) * 16;
    auto fetch_m = [&](int s) {
        const int so = (s * NH + h) * 2048;
        const unsigned d = lds_base + Q_M + w * 4096;
        bdma(rw, ms, so, d); bdma(rw, ms, so + 128, d + 1024); bdma(rw, lane * 16, OFF_VM + so, d + 2048); bdma(rw, lane * 16, OFF_VM + so + 1024, d + 3072);
    };
    const float c = 0.08838834764831845f * 1.4426950408889634f;
    u32x4 qf[4];
    f32x4 o[8]; float mx, l;
    auto xpart = [&]() {
        const int kb = Q_KV + (w >> 1) * 16384 + lane * 16;
        u32x4 kn[8], vn[8];
#pragma unroll
        for (int ks = 0; ks < 4; ++ks) qf[ks] = *(LAS u32x4*)(lds + Q_Q + (w >> 1) * 8192 + (2 * ks + (g >> 1)) * 1024 + ((g & 1) * 32 + 16 * tt + n) * 16);
#pragma unroll
        for (int f = 0; f < 8; ++f) kn[f] = *(LAS u32x4*)(lds + kb + f * 1024);
        FENCE();
#pragma unroll
        for (int f = 0; f < 8; ++f) vn[f] = *(LAS u32x4*)(lds + kb + 8192 + f * 1024);
        f32x4 s0, s1;
        asm volatile(M16 "%0, %2, %10, 0\n\t" M16 "%1, %3, %10, 0\n\t" M16 "%0, %4, %11, %0\n\t" M16 "%1, %5, %11, %1\n\t"
                     M16 "%0, %6, %12, %0\n\t" M16 "%1, %7, %12, %1\n\t" M16 "%0, %8, %13, %0\n\t" M16 "%1, %9, %13, %1\n\ts_nop 7"
                     : "=&v"(s0), "=&v"(s1) : "v"(kn[0]), "v"(kn[1]), "v"(kn[2]), "v"(kn[3]), "v"(kn[4]), "v"(kn[5]), "v"(kn[6]), "v"(kn[7]), "v"(qf[0]), "v"(qf[1]), "v"(qf[2]), "v"(qf[3]));
        float p[8];
        mx = -3.0e38f; l = 0.f;
#pragma unroll
        for (int r = 0; r < 8; ++r) { const int key = 16 * (g >> 1) + 4 * (g & 1) + (r & 3) + 8 * (r >> 2); p[r] = key <= 16 * tt + n ? (r < 4 ? s0[r & 3] : s1[r & 3]) : -3.0e38f; mx = fmaxf(mx, p[r]); }
        mx = red_max_g(mx);
#pragma unroll
        for (int r = 0; r < 8; ++r) { p[r] = __builtin_amdgcn_exp2f((p[r] - mx) * c); l += p[r]; }
        const u32x4 pf = {pack2(p[0], p[1]), pack2(p[2], p[3]), pack2(p[4], p[5]), pack2(p[6], p[7])};
        asm volatile("s_nop 1\n\t" M16 "%0, %8, %16, 0\n\t" M16 "%1, %9, %16, 0\n\t" M16 "%2, %10, %16, 0\n\t" M16 "%3, %11, %16, 0\n\t"
                     M16 "%4, %12, %16, 0\n\t" M16 "%5, %13, %16, 0\n\t" M16 "%6, %14, %16, 0\n\t" M16 "%7, %15, %16, 0\n\ts_nop 7"
                     : "=&v"(o[0]), "=&v"(o[1]), "=&v"(o[2]), "=&v"(o[3]), "=&v"(o[4]), "=&v"(o[5]), "=&v"(o[6]), "=&v"(o[7])
                     : "v"(vn[0]), "v"(vn[1]), "v"(vn[2]), "v"(vn[3]), "v"(vn[4]), "v"(vn[5]), "v"(vn[6]), "v"(vn[7]), "v"(pf));
    };
    proj(s0);
    if (!fast) wait_init(rw, launch_tag(a), budget);
    wait_prog(rw, fk, true, fast ? s1 : s0, budget);
    fetch(s0);
    VM_DRAIN();
    WG_BARRIER();
    xpart();
    WG_BARRIER();
    proj(s0 + 2);
    const int fo = lane == 0 ? F_O + h * 128 + (2 * s0 + tt) * 4 : OOB;
    bool pre = false;
    for (int s = s0; s <= s1; s += 2) {
        S4S(0)
        const int mb = Q_M + w * 4096;
        u32x4 kf[4]; u32x2 vm[4];
        auto frags = [&]() {
#pragma unroll
            for (int ks = 0; ks < 4; ++ks) kf[ks] = *(LAS u32x4*)(lds + mb + (4 * ks + g) * 128 + (2 * (n >> 2) + (n & 1)) * 16);
#pragma unroll
            for (int t = 0; t < 4; ++t) { const int vo = mb + 2048 + t * 512 + (n >> 3) * 256 + g * 64 + (n & 7) * 4; vm[t] = (u32x2){*(LAS unsigned*)(lds + vo), *(LAS unsigned*)(lds + vo + 32)}; }
        };
        if (fast) {
            TT(s == s0, 3)
            if (s < s1) fetch(s1);
            unsigned t;
            const int d = min(wait_m(min(s - 1, NSEG - 3), t) + (int)(a.xa >> (s == NSEG - 1 ? 8 : 0) & 255), 500);
            st32((t + max(d, 0) + (a.xb >> 8 * (s - FAST) & 255)) & 0x7FFFFFFFu, rw, lane == 0 ? F_H + ((2 * (s - FAST) + tt) * NH + h) * 128 : OOB, 0);
            (void)wait_until(t + d);
            TT(true, 0)
            for (;;) {
                fetch_m(s);
                VM_DRAIN();
                frags();
                unsigned c = 0;
#pragma unroll
                for (int i = 0; i < 4; ++i) c = max(max(mx4(c, kf[i]), vm[i].x), vm[i].y);
                if (!__any(c == SENT) || --budget < 0) break;
            }
        } else {
            if (!pre) {
                seen();
                unsigned t;
                if (m_rdy < s) { const int d = wait_m(s, t); (void)wait_until(t + min(d + (int)(a.xa >> 24), 300)); }
                fetch_m(s);
                if (kv_rdy < s + 2) wait_prog(rw, fk, true, s + 2, budget);
                fetch(s + 2);
            }
            S4S(1)
            asm volatile("s_waitcnt vmcnt(8)" ::: "memory");
            frags();
        }
        TT(s == s1, 1)
        S4S(2)
        f32x4 sm;
        asm volatile(M16 "%0, %1, %5, 0\n\t" M16 "%0, %2, %6, %0\n\t" M16 "%0, %3, %7, %0\n\t" M16 "%0, %4, %8, %0\n\ts_nop 7"
                     : "=&v"(sm) : "v"(kf[0]), "v"(kf[1]), "v"(kf[2]), "v"(kf[3]), "v"(qf[0]), "v"(qf[1]), "v"(qf[2]), "v"(qf[3]));
        const float m2 = red_max_g(fmaxf(mx, fmaxf(sm[0], sm[1])));
        const float al = __builtin_amdgcn_exp2f((mx - m2) * c), p0 = __builtin_amdgcn_exp2f((sm[0] - m2) * c), p1 = __builtin_amdgcn_exp2f((sm[1] - m2) * c);
        const float inv = __builtin_amdgcn_rcpf(red_sum_g(l * al + p0 + p1)), ai = al * inv;
#pragma unroll
        for (int t = 0; t < 8; ++t) o[t] *= ai;
        const unsigned pm = pack2(p0 * inv, p1 * inv);
#define M1K "v_mfma_f32_16x16x16_bf16 "
        asm volatile("s_nop 1\n\t" M1K "%0, %8, %12, %0\n\t" M1K "%1, %8, %13, %1\n\t" M1K "%2, %9, %12, %2\n\t" M1K "%3, %9, %13, %3\n\t"
                     M1K "%4, %10, %12, %4\n\t" M1K "%5, %10, %13, %5\n\t" M1K "%6, %11, %12, %6\n\t" M1K "%7, %11, %13, %7\n\ts_nop 7"
                     : "+v"(o[0]), "+v"(o[1]), "+v"(o[2]), "+v"(o[3]), "+v"(o[4]), "+v"(o[5]), "+v"(o[6]), "+v"(o[7])
                     : "v"(vm[0]), "v"(vm[1]), "v"(vm[2]), "v"(vm[3]), "v"((u32x2){pm, 0u}), "v"((u32x2){0u, pm}));
#pragma unroll
        for (int b = 0; b < 2; ++b) {
            u32x4 x, y;
#pragma unroll
            for (int j = 0; j < 4; ++j) { x[j] = pack2(o[4 * b + (j >> 1)][2 * (j & 1)], o[4 * b + (j >> 1)][2 * (j & 1) + 1]); y[j] = pack2(o[4 * b + 2 + (j >> 1)][2 * (j & 1)], o[4 * b + 2 + (j >> 1)][2 * (j & 1) + 1]); }
            if (s == NSEG - 1) { const u32x4 z = {x.x, x.z, y.x, y.z}; y = (u32x4){x.y, x.w, y.y, y.w}; x = z; }
            st128(x, rw, oo + b * 1024, s * 262144); st128(y, rw, oo + b * 1024 + 512, s * 262144);
        }
        TT(s == s1, 2)
        S4S(3)
        if (s < s1) {
            asm volatile("s_waitcnt vmcnt(4)" ::: "memory");
            WG_BARRIER();
            S4S(4)
            look();
            xpart();
        }
        TT(s == s0, 4)
        if (fast) continue;
        FENCE();
        S4S(5)
        seen();
        VM_DRAIN();
        st32((unsigned)s, rw, fo, 0);
        TT(s == s1, 3)
        TT(s == s1 - 2, 4)
        S4S(6)
        WG_BARRIER();
        pre = m_rdy >= s + 2 && kv_rdy >= s + 4 && s < s1;
        if (pre) { fetch_m(s + 2); fetch(s + 4); }
        S4S(7)
        if (s + 4 <= s1) proj(s + 4);
        S4S(8)
    }
}

#define STEP_ASM \
    "v_readfirstlane_b32 s36, v0\n" \
    "v_readfirstlane_b32 s37, v1\n" \
    "v_readfirstlane_b32 s38, v2\n" \
    "v_readfirstlane_b32 s39, v3\n" \
    "v_readfirstlane_b32 s40, v4\n" \
    "v_readfirstlane_b32 s41, v5\n" \
    "v_readfirstlane_b32 s42, v6\n" \
    "v_readfirstlane_b32 s43, v7\n" \
    "v_readfirstlane_b32 s28, v8\n" \
    "v_readfirstlane_b32 s29, v9\n" \
    "v_readfirstlane_b32 s30, v10\n" \
    "v_readfirstlane_b32 s31, v11\n" \
    "v_readfirstlane_b32 s32, v12\n" \
    "v_readfirstlane_b32 s33, v13\n" \
    "v_readfirstlane_b32 s34, v14\n" \
    "v_readfirstlane_b32 s35, v15\n" \
    "v_readfirstlane_b32 s24, v16\n" \
    "v_readfirstlane_b32 s25, v17\n" \
    "v_readfirstlane_b32 s26, v18\n" \
    "v_readfirstlane_b32 s27, v19\n" \
    "v_readfirstlane_b32 s60, v24\n" \
    "v_readfirstlane_b32 s61, v25\n" \
    "v_readfirstlane_b32 s62, v26\n" \
    "v_readfirstlane_b32 s63, v27\n" \
    "v_readfirstlane_b32 s64, v28\n" \
    "v_readfirstlane_b32 s65, v29\n" \
    "v_readfirstlane_b32 s66, v30\n" \
    "v_readfirstlane_b32 s67, v31\n" \
    "s_mov_b32 s68, m0\n" \
    "s_getpc_b64 s[96:97]\n" \
    "14:\n" \
    "s_add_u32 s96, s96, 2f-14b\n" \
    "s_addc_u32 s97, s97, 0\n" \
    "s_lshl_b32 s74, s60, 10\n" \
    "s_lshl_b32 s95, s74, 1\n" \
    "s_add_i32 s95, s95, 16384\n" \
    "s_mov_b32 s81, 0x3e0293ee\n" \
    "s_mov_b32 s82, 0x53800000\n" \
    "s_lshl_b32 s83, s60, 6\n" \
    "s_and_b32 s83, s83, 128\n" \
    "v_add_u32 v109, s83, v131\n" \
    "v_sub_u32 v111, v131, s83\n" \
    "v_xor_b32 v112, 0x2000, v133\n" \
    "v_and_b32 v113, 0x3c0, v127\n" \
    "v_add_u32 v113, v113, v127\n" \
    "v_lshlrev_b32 v114, 1, v127\n" \
    "v_add_u32 v106, s63, v133\n" \
    "s_mov_b64 s[72:73], 0\n" \
    "0:\n" \
    "s_and_b32 s75, s36, 3\n" \
    "s_lshl_b32 s75, s75, 17\n" \
    "s_add_i32 s75, s75, s26\n" \
    "s_add_i32 s69, s75, s74\n" \
    "s_add_i32 s70, s69, 4096\n" \
    "s_add_i32 s71, s75, 16384\n" \
    "12:\n" \
    "ds_read_b128 v[72:75], v133 offset:0\n" \
    "ds_read_b128 v[76:79], v133 offset:1024\n" \
    "ds_read_b128 v[80:83], v133 offset:2048\n" \
    "ds_read_b128 v[84:87], v133 offset:3072\n" \
    "ds_read_b128 v[88:91], v133 offset:16384\n" \
    "ds_read_b128 v[92:95], v133 offset:17408\n" \
    "s_add_i32 s91, s36, 1\n" \
    "s_and_b32 s78, s36, 3\n" \
    "s_lshl_b32 s78, s78, 14\n" \
    "s_add_i32 s77, s78, s61\n" \
    "s_bitcmp1_b32 s36, 0\n" \
    "s_cselect_b32 s79, 0x90000, 0\n" \
    "s_lshl_b32 s92, s91, 18\n" \
    "s_add_i32 s92, s92, s64\n" \
    "s_movk_i32 m0, 0x2000\n" \
    "s_add_i32 s78, s78, 0x1a0000\n" \
    "s_add_i32 s93, s92, 0x1000000\n" \
    "v_mov_b64 v[40:41], 0\n" \
    "v_mov_b64 v[42:43], 0\n" \
    "v_mov_b64 v[44:45], 0\n" \
    "v_mov_b64 v[46:47], 0\n" \
    "v_mov_b64 v[48:49], 0\n" \
    "v_mov_b64 v[50:51], 0\n" \
    "v_mov_b64 v[52:53], 0\n" \
    "v_mov_b64 v[54:55], 0\n" \
    "v_mov_b64 v[56:57], 0\n" \
    "v_mov_b64 v[58:59], 0\n" \
    "v_mov_b64 v[60:61], 0\n" \
    "v_mov_b64 v[62:63], 0\n" \
    "v_mov_b64 v[64:65], 0\n" \
    "v_mov_b64 v[66:67], 0\n" \
    "v_mov_b64 v[68:69], 0\n" \
    "v_mov_b64 v[70:71], 0\n" \
    "v_add_u32 v108, s74, v127\n" \
    "v_xor_b32 v24, 0x400, v108\n" \
    "v_xor_b32 v28, 0x800, v108\n" \
    "v_xor_b32 v32, 0xc00, v108\n" \
    "s_sub_i32 s76, s40, s24\n" \
    "s_min_i32 s94, s91, s38\n" \
    "s_lshl_b32 s94, s94, 18\n" \
    "s_andn2_b32 s80, 0x1000000, s66\n" \
    "s_add_i32 s94, s94, s64\n" \
    "s_add_i32 s94, s94, s80\n" \
    "s_cmp_eq_u32 s36, 0\n" \
    "s_cbranch_scc1 17f\n" \
    "s_sub_i32 s90, s37, s29\n" \
    "s_mul_i32 s90, s90, s25\n" \
    "s_ashr_i32 s90, s90, 12\n" \
    "s_add_i32 s90, s90, s24\n" \
    "s_add_i32 s90, s90, -116\n" \
    "1:\n" \
    "s_memtime vcc\n" \
    "s_waitcnt lgkmcnt(0)\n" \
    "s_sub_i32 s75, s90, vcc_lo\n" \
    "s_cmp_gt_i32 s75, 112\n" \
    "s_cbranch_scc1 1b\n" \
    "s_max_i32 s75, s75, 0\n" \
    "s_and_b32 s75, s75, -4\n" \
    "s_sub_u32 vcc_lo, s96, s75\n" \
    "s_subb_u32 vcc_hi, s97, 0\n" \
    "s_setpc_b64 vcc\n" \
    "s_nop 0\n" \
    "s_nop 0\n" \
    "s_nop 0\n" \
    "s_nop 0\n" \
    "s_nop 0\n" \
    "s_nop 0\n" \
    "s_nop 0\n" \
    "s_nop 0\n" \
    "s_nop 0\n" \
    "s_nop 0\n" \
    "s_nop 0\n" \
    "s_nop 0\n" \
    "s_nop 0\n" \
    "s_nop 0\n" \
    "s_nop 0\n" \
    "s_nop 0\n" \
    "s_nop 0\n" \
    "s_nop 0\n" \
    "s_nop 0\n" \
    "s_nop 0\n" \
    "s_nop 0\n" \
    "s_nop 0\n" \
    "s_nop 0\n" \
    "s_nop 0\n" \
    "s_nop 0\n" \
    "s_nop 0\n" \
    "s_nop 0\n" \
    "s_nop 0\n" \
    "2:\n" \
    "buffer_load_dwordx4 v[0:3], v125, %[rs], s69 offen sc1\n" \
    "s_nop 3\n" \
    "s_nop 3\n" \
    "buffer_load_dwordx4 v[16:19], v125, %[rs], s70 offen sc1\n" \
    "s_mov_b32 exec_lo, s27\n" \
    "s_mov_b32 exec_hi, s27\n" \
    "buffer_load_dwordx4 v127, %[rs], s71 offen sc1 lds\n" \
    "s_mov_b64 exec, -1\n" \
    "v_bfe_u32 v20, v127, 8, 2\n" \
    "v_subrev_u32 v20, s60, v20\n" \
    "v_mul_i32_i24 v20, -1088, v20\n" \
    "v_add_u32 v20, v20, v129\n" \
    "v_lshrrev_b32 v21, 2, v127\n" \
    "s_mov_b32 exec_lo, s66\n" \
    "s_mov_b32 exec_hi, s66\n" \
    "ds_read2_b32 v[96:97], v20 offset0:0 offset1:144\n" \
    "s_mov_b64 exec, -1\n" \
    "s_lshl_b32 s80, s36, 16\n" \
    "s_add_i32 s80, s80, s67\n" \
    "s_cmp_eq_u32 s60, 0\n" \
    "s_cselect_b32 s72, 1, 0\n" \
    "s_waitcnt lgkmcnt(0)\n" \
    "buffer_store_dword v96, v21, %[rw], s80 offen sc1\n" \
    "buffer_store_dword v97, v21, %[rw], s80 offen offset:256 sc1\n" \
    "3:\n" \
    "s_mov_b32 m0, s95\n" \
    "v_mov_b64 v[98:99], 0\n" \
    "v_mov_b32 v101, 0\n" \
    "v_mov_b32 v103, s91\n" \
    "s_waitcnt vmcnt(4)\n" \
    "v_smfmac_f32_16x16x64_bf16 v[40:43], v[0:3], a[0:7], v124\n" \
    "ds_write_b128 v108, v[0:3] offset:0\n" \
    "v_smfmac_f32_16x16x64_bf16 v[44:47], v[0:3], a[8:15], v124\n" \
    "v_smfmac_f32_16x16x64_bf16 v[48:51], v[0:3], a[128:135], v124\n" \
    "v_smfmac_f32_16x16x64_bf16 v[52:55], v[0:3], a[136:143], v124\n" \
    "s_waitcnt vmcnt(3)\n" \
    "v_smfmac_f32_16x16x64_bf16 v[40:43], v[16:19], a[64:71], v124\n" \
    "ds_write_b128 v108, v[16:19] offset:4096\n" \
    "v_smfmac_f32_16x16x64_bf16 v[44:47], v[16:19], a[72:79], v124\n" \
    "s_waitcnt lgkmcnt(1)\n" \
    "s_barrier\n" \
    "v_smfmac_f32_16x16x64_bf16 v[48:51], v[16:19], a[192:199], v124\n" \
    "ds_read_b128 v[4:7], v24 offset:0\n" \
    "v_smfmac_f32_16x16x64_bf16 v[52:55], v[16:19], a[200:207], v124\n" \
    "ds_read_b128 v[8:11], v28 offset:0\n" \
    "v_smfmac_f32_16x16x64_bf16 v[56:59], v[0:3], v[72:79], v124\n" \
    "ds_read_b128 v[12:15], v32 offset:0\n" \
    "v_smfmac_f32_16x16x64_bf16 v[60:63], v[0:3], v[80:87], v124\n" \
    "s_waitcnt vmcnt(2) lgkmcnt(3)\n" \
    "s_barrier\n" \
    "v_smfmac_f32_16x16x64_bf16 v[56:59], v[16:19], v[168:175], v124\n" \
    "ds_read_b128 v[24:27], v24 offset:4096\n" \
    "v_smfmac_f32_16x16x64_bf16 v[60:63], v[16:19], v[176:183], v124\n" \
    "ds_read_b128 v[28:31], v28 offset:4096\n" \
    "s_waitcnt lgkmcnt(3)\n" \
    "v_smfmac_f32_16x16x64_bf16 v[40:43], v[4:7], a[16:23], v124\n" \
    "ds_read_b128 v[32:35], v32 offset:4096\n" \
    "s_memtime s[86:87]\n" \
    "v_smfmac_f32_16x16x64_bf16 v[40:43], v[8:11], a[32:39], v124\n" \
    "ds_read_b128 v[36:39], v126 offset:0\n" \
    "s_waitcnt lgkmcnt(4)\n" \
    "v_smfmac_f32_16x16x64_bf16 v[40:43], v[12:15], a[48:55], v124\n" \
    "buffer_load_dword v110, v114, %[rw], s94 offen sc1\n" \
    "v_smfmac_f32_16x16x64_bf16 v[44:47], v[4:7], a[24:31], v124\n" \
    "s_waitcnt lgkmcnt(2)\n" \
    "v_smfmac_f32_16x16x64_bf16 v[40:43], v[24:27], a[80:87], v124\n" \
    "v_smfmac_f32_16x16x64_bf16 v[40:43], v[28:31], a[96:103], v124\n" \
    "s_waitcnt lgkmcnt(0)\n" \
    "v_smfmac_f32_16x16x64_bf16 v[40:43], v[32:35], a[112:119], v124\n" \
    "ds_read_b128 v[72:75], v133 offset:18432\n" \
    "s_add_i32 s80, s86, s76\n" \
    "v_smfmac_f32_16x16x64_bf16 v[40:43], v[36:39], v[136:143], v124\n" \
    "ds_read_b128 v[76:79], v133 offset:19456\n" \
    "s_mul_hi_u32 s80, s80, s41\n" \
    "v_smfmac_f32_16x16x64_bf16 v[44:47], v[8:11], a[40:47], v124\n" \
    "ds_read_b128 v[80:83], v133 offset:32768\n" \
    "s_add_i32 s80, s80, s29\n" \
    "v_mov_b32 v102, s80\n" \
    "v_smfmac_f32_16x16x64_bf16 v[44:47], v[12:15], a[56:63], v124\n" \
    "ds_read_b128 v[84:87], v133 offset:33792\n" \
    "v_add_f32 v40, v40, v42\n" \
    "v_add_f32 v41, v41, v43\n" \
    "v_smfmac_f32_16x16x64_bf16 v[44:47], v[24:27], a[88:95], v124\n" \
    "ds_read_b128 v[20:23], v127 offset:19456\n" \
    "v_cmp_u_f32 vcc, v40, v41\n" \
    "v_smfmac_f32_16x16x64_bf16 v[44:47], v[28:31], a[104:111], v124\n" \
    "v_mul_f32 v40, s81, v40\n" \
    "v_smfmac_f32_16x16x64_bf16 v[44:47], v[32:35], a[120:127], v124\n" \
    "s_cbranch_vccnz 5f\n" \
    "6:\n" \
    "s_mov_b64 exec, s[72:73]\n" \
    "v_smfmac_f32_16x16x64_bf16 v[44:47], v[36:39], v[144:151], v124\n" \
    "buffer_store_dwordx2 v[102:103], off, %[rs], s77 sc1\n" \
    "v_smfmac_f32_16x16x64_bf16 v[56:59], v[4:7], v[88:95], v124\n" \
    "s_mov_b64 exec, -1\n" \
    "s_waitcnt lgkmcnt(1)\n" \
    "v_mul_f32 v41, s81, v41\n" \
    "v_smfmac_f32_16x16x64_bf16 v[60:63], v[4:7], v[72:79], v124\n" \
    "ds_read_b128 v[88:91], v133 offset:34816\n" \
    "v_add_f32 v44, v44, v46\n" \
    "v_smfmac_f32_16x16x64_bf16 v[56:59], v[8:11], v[80:87], v124\n" \
    "ds_read_b128 v[92:95], v133 offset:35840\n" \
    "v_add_f32 v45, v45, v47\n" \
    "v_smfmac_f32_16x16x64_bf16 v[48:51], v[4:7], a[144:151], v124\n" \
    "ds_read_b128 v[72:75], v133 offset:49152\n" \
    "v_mul_f32 v44, s81, v44\n" \
    "v_smfmac_f32_16x16x64_bf16 v[52:55], v[4:7], a[152:159], v124\n" \
    "ds_read_b128 v[76:79], v133 offset:50176\n" \
    "v_mul_f32 v45, s81, v45\n" \
    "v_smfmac_f32_16x16x64_bf16 v[48:51], v[8:11], a[160:167], v124\n" \
    "ds_read_b128 v[0:3], v127 offset:16384\n" \
    "v_cvt_pk_bf16_f32 v107, v40, v44\n" \
    "v_smfmac_f32_16x16x64_bf16 v[52:55], v[8:11], a[168:175], v124\n" \
    "ds_read_b128 v[4:7], v127 offset:18432\n" \
    "s_waitcnt lgkmcnt(4)\n" \
    "v_cvt_pk_bf16_f32 v108, v41, v45\n" \
    "v_smfmac_f32_16x16x64_bf16 v[60:63], v[8:11], v[88:95], v124\n" \
    "ds_write2_b32 v128, v107, v108 offset0:0 offset1:80\n" \
    "v_smfmac_f32_16x16x64_bf16 v[60:63], v[12:15], v[248:255], v124\n" \
    "ds_read_b128 v[16:19], v127 offset:17408\n" \
    "s_waitcnt lgkmcnt(4)\n" \
    "v_smfmac_f32_16x16x64_bf16 v[56:59], v[12:15], v[72:79], v124\n" \
    "ds_read_b128 v[8:11], v127 offset:20480\n" \
    "v_smfmac_f32_16x16x64_bf16 v[48:51], v[12:15], a[176:183], v124\n" \
    "ds_read_b128 v[40:43], v127 offset:21504\n" \
    "v_smfmac_f32_16x16x64_bf16 v[52:55], v[12:15], a[184:191], v124\n" \
    "ds_read_b128 v[44:47], v127 offset:23552\n" \
    "v_smfmac_f32_16x16x64_bf16 v[48:51], v[24:27], a[208:215], v124\n" \
    "s_waitcnt lgkmcnt(4)\n" \
    "s_barrier\n" \
    "v_smfmac_f32_16x16x64_bf16 v[52:55], v[24:27], a[216:223], v124\n" \
    "ds_read_b128 v[72:75], v130 offset:0\n" \
    "v_smfmac_f32_16x16x64_bf16 v[48:51], v[28:31], a[224:231], v124\n" \
    "ds_read_b128 v[76:79], v130 offset:64\n" \
    "v_smfmac_f32_16x16x64_bf16 v[48:51], v[32:35], a[240:247], v124\n" \
    "ds_read_b128 v[80:83], v130 offset:128\n" \
    "v_smfmac_f32_16x16x64_bf16 v[48:51], v[36:39], v[152:159], v124\n" \
    "ds_read_b128 v[84:87], v130 offset:192\n" \
    "v_smfmac_f32_16x16x64_bf16 v[52:55], v[28:31], a[232:239], v124\n" \
    "ds_read_b128 v[12:15], v127 offset:22528\n" \
    "v_smfmac_f32_16x16x64_bf16 v[52:55], v[32:35], a[248:255], v124\n" \
    "v_add_f32 v48, v48, v50\n" \
    "v_smfmac_f32_16x16x64_bf16 v[52:55], v[36:39], v[160:167], v124\n" \
    "s_waitcnt lgkmcnt(3)\n" \
    "v_add_f32 v49, v49, v51\n" \
    "v_mfma_f32_16x16x32_bf16 v[88:91], v[0:3], v[72:75], 0\n" \
    "ds_read_b64 v[0:1], v132 offset:0\n" \
    "ds_read_b64 v[2:3], v132 offset:1024\n" \
    "v_mfma_f32_16x16x32_bf16 v[88:91], v[4:7], v[76:79], v[88:91]\n" \
    "s_waitcnt lgkmcnt(2)\n" \
    "v_add_f32 v52, v52, v54\n" \
    "v_add_f32 v53, v53, v55\n" \
    "v_mfma_f32_16x16x32_bf16 v[88:91], v[8:11], v[80:83], v[88:91]\n" \
    "v_cvt_pk_bf16_f32 v107, v48, v52\n" \
    "v_cvt_pk_bf16_f32 v108, v49, v53\n" \
    "v_mfma_f32_16x16x32_bf16 v[88:91], v[12:15], v[84:87], v[88:91]\n" \
    "ds_write2_b32 v129, v107, v108 offset0:0 offset1:144\n" \
    "v_mfma_f32_16x16x32_bf16 v[92:95], v[16:19], v[72:75], 0\n" \
    "ds_read_b64 v[4:5], v132 offset:512\n" \
    "ds_read_b64 v[6:7], v132 offset:1536\n" \
    "v_mfma_f32_16x16x32_bf16 v[92:95], v[20:23], v[76:79], v[92:95]\n" \
    "ds_read_b128 v[8:11], v106 offset:0\n" \
    "v_exp_f32 v88, v88\n" \
    "v_mfma_f32_16x16x32_bf16 v[92:95], v[40:43], v[80:83], v[92:95]\n" \
    "ds_read_b128 v[12:15], v106 offset:1024\n" \
    "v_exp_f32 v89, v89\n" \
    "v_mfma_f32_16x16x32_bf16 v[92:95], v[44:47], v[84:87], v[92:95]\n" \
    "s_waitcnt lgkmcnt(4)\n" \
    "s_barrier\n" \
    "v_smfmac_f32_16x16x64_bf16 v[56:59], v[24:27], v[184:191], v124\n" \
    "ds_read_b128 v[48:51], v131 offset:0\n" \
    "ds_read_b128 v[52:55], v131 offset:128\n" \
    "v_cvt_pk_bf16_f32 v88, v88, v89\n" \
    "v_smfmac_f32_16x16x64_bf16 v[56:59], v[28:31], v[200:207], v124\n" \
    "ds_read_b128 v[16:19], v106 offset:32768\n" \
    "v_exp_f32 v90, v90\n" \
    "v_smfmac_f32_16x16x64_bf16 v[56:59], v[32:35], v[216:223], v124\n" \
    "ds_read_b128 v[20:23], v106 offset:33792\n" \
    "v_exp_f32 v91, v91\n" \
    "v_smfmac_f32_16x16x64_bf16 v[56:59], v[36:39], v[232:239], v124\n" \
    "ds_read_b128 v[40:43], v106 offset:2048\n" \
    "s_waitcnt lgkmcnt(3)\n" \
    "v_exp_f32 v92, v92\n" \
    "v_cvt_pk_bf16_f32 v89, v90, v91\n" \
    "v_smfmac_f32_16x16x64_bf16 v[64:67], v[48:51], v[72:79], v124\n" \
    "ds_read_b128 v[44:47], v106 offset:3072\n" \
    "v_exp_f32 v93, v93\n" \
    "v_smfmac_f32_16x16x64_bf16 v[64:67], v[52:55], v[80:87], v124\n" \
    "ds_read_b128 v[72:75], v106 offset:34816\n" \
    "v_exp_f32 v94, v94\n" \
    "v_cvt_pk_bf16_f32 v90, v92, v93\n" \
    "v_smfmac_f32_16x16x64_bf16 v[60:63], v[24:27], v[192:199], v124\n" \
    "ds_read_b128 v[76:79], v106 offset:35840\n" \
    "v_exp_f32 v95, v95\n" \
    "v_smfmac_f32_16x16x64_bf16 v[60:63], v[28:31], v[208:215], v124\n" \
    "ds_read_b128 v[80:83], v106 offset:4096\n" \
    "s_cmp_eq_u32 s36, 63\n" \
    "v_add_f32 v64, v64, v66\n" \
    "v_add_f32 v65, v65, v67\n" \
    "v_smfmac_f32_16x16x64_bf16 v[60:63], v[32:35], v[224:231], v124\n" \
    "ds_read_b128 v[84:87], v106 offset:5120\n" \
    "v_exp_f32 v64, v64\n" \
    "v_cvt_pk_bf16_f32 v91, v94, v95\n" \
    "v_smfmac_f32_16x16x64_bf16 v[60:63], v[36:39], v[240:247], v124\n" \
    "ds_read_b128 v[48:51], v106 offset:36864\n" \
    "v_exp_f32 v65, v65\n" \
    "v_swap_b32 v1, v2\n" \
    "v_swap_b32 v5, v6\n" \
    "v_mfma_f32_16x16x32_bf16 v[102:105], v[88:91], v[120:123], 0\n" \
    "ds_read_b128 v[52:55], v106 offset:37888\n" \
    "v_cvt_pk_bf16_f32 v100, v64, v65\n" \
    "v_add_f32 v56, v56, v58\n" \
    "v_add_f32 v57, v57, v59\n" \
    "v_smfmac_f32_16x16x64_bf16 v[68:71], v[88:91], v[0:7], v124\n" \
    "ds_read_b128 v[24:27], v106 offset:6144\n" \
    "v_add_f32 v60, v60, v62\n" \
    "v_add_f32 v61, v61, v63\n" \
    "v_mfma_f32_16x16x16_bf16 v[102:105], v[100:101], v[120:121], v[102:105]\n" \
    "ds_read_b128 v[28:31], v106 offset:7168\n" \
    "v_cvt_pk_bf16_f32 v96, v56, v57\n" \
    "v_cvt_pk_bf16_f32 v97, v60, v61\n" \
    "s_cbranch_scc1 16f\n" \
    "s_nop 0\n" \
    "v_smfmac_f32_16x16x32_bf16 v[68:71], v[100:101], v[96:99], v124\n" \
    "ds_read_b128 v[32:35], v106 offset:38912\n" \
    "v_mov_b64 v[56:57], 0\n" \
    "v_mov_b64 v[58:59], 0\n" \
    "v_rcp_f32 v104, v102\n" \
    "v_rcp_f32 v105, v103\n" \
    "v_mul_f32 v68, v68, v104\n" \
    "v_mul_f32 v70, v70, v104\n" \
    "v_mul_f32 v69, v69, v105\n" \
    "v_mul_f32 v71, v71, v105\n" \
    "v_cvt_pk_bf16_f32 v107, v68, v70\n" \
    "v_cvt_pk_bf16_f32 v108, v69, v71\n" \
    "ds_write2_b32 v129, v107, v108 offset0:64 offset1:208\n" \
    "v_max3_f32 v102, v102, v103, v104\n" \
    "v_max_f32 v102, v102, v105\n" \
    "v_cmp_nge_f32 vcc, s82, v102\n" \
    "v_mov_b64 v[60:61], 0\n" \
    "v_mov_b64 v[62:63], 0\n" \
    "v_mov_b64 v[88:89], 0\n" \
    "v_mov_b64 v[90:91], 0\n" \
    "v_mov_b64 v[92:93], 0\n" \
    "v_mov_b64 v[94:95], 0\n" \
    "s_waitcnt vmcnt(2) lgkmcnt(0)\n" \
    "s_barrier\n" \
    "ds_read_b128 v[68:71], v109 offset:256\n" \
    "ds_read_b128 v[102:105], v111 offset:384\n" \
    "s_cbranch_vccnz 7f\n" \
    "buffer_load_dwordx2 v[98:99], v113, %[rs], s78 offen sc1\n" \
    "buffer_load_dword v100, off, %[rw], s62 sc1\n" \
    "v_mov_b64 v[64:65], 0\n" \
    "v_mov_b64 v[66:67], 0\n" \
    "s_waitcnt lgkmcnt(1)\n" \
    "v_smfmac_f32_16x16x64_bf16 v[56:59], v[68:71], v[8:15], v124\n" \
    "s_waitcnt lgkmcnt(0)\n" \
    "ds_read_b128 v[36:39], v106 offset:39936\n" \
    "v_smfmac_f32_16x16x64_bf16 v[56:59], v[102:105], v[16:23], v124\n" \
    "ds_read_b128 v[0:3], v133 offset:51200\n" \
    "v_smfmac_f32_16x16x64_bf16 v[60:63], v[68:71], v[40:47], v124\n" \
    "ds_read_b128 v[4:7], v133 offset:52224\n" \
    "v_smfmac_f32_16x16x64_bf16 v[60:63], v[102:105], v[72:79], v124\n" \
    "ds_read_b128 v[16:19], v112 offset:51200\n" \
    "v_add_f32 v56, v56, v58\n" \
    "v_smfmac_f32_16x16x64_bf16 v[88:91], v[68:71], v[80:87], v124\n" \
    "ds_read_b128 v[20:23], v112 offset:52224\n" \
    "v_add_f32 v57, v57, v59\n" \
    "v_smfmac_f32_16x16x64_bf16 v[88:91], v[102:105], v[48:55], v124\n" \
    "v_add_f32 v60, v60, v62\n" \
    "v_smfmac_f32_16x16x64_bf16 v[92:95], v[68:71], v[24:31], v124\n" \
    "s_waitcnt lgkmcnt(4)\n" \
    "v_add_f32 v61, v61, v63\n" \
    "v_smfmac_f32_16x16x64_bf16 v[92:95], v[102:105], v[32:39], v124\n" \
    "s_waitcnt lgkmcnt(2)\n" \
    "v_add_f32 v88, v88, v90\n" \
    "v_smfmac_f32_16x16x64_bf16 v[64:67], v[68:71], v[0:7], v124\n" \
    "s_waitcnt lgkmcnt(0)\n" \
    "v_add_f32 v89, v89, v91\n" \
    "v_smfmac_f32_16x16x64_bf16 v[64:67], v[102:105], v[16:23], v124\n" \
    "v_add_f32 v92, v92, v94\n" \
    "v_add_f32 v93, v93, v95\n" \
    "v_cvt_pk_bf16_f32 v8, v56, v60\n" \
    "v_cvt_pk_bf16_f32 v10, v57, v61\n" \
    "v_cvt_pk_bf16_f32 v9, v88, v92\n" \
    "v_cvt_pk_bf16_f32 v11, v89, v93\n" \
    "buffer_store_dwordx4 v[8:11], v134, %[rs], s79 offen sc1\n" \
    "v_add_f32 v64, v64, v66\n" \
    "v_add_f32 v65, v65, v67\n" \
    "v_cvt_pk_bf16_f32 v64, v64, v65\n" \
    "buffer_store_dword v64, v135, %[rs], s79 offen sc1\n" \
    "s_memtime s[88:89]\n" \
    "s_memrealtime s[44:45]\n" \
    "s_and_b32 s75, s91, 3\n" \
    "s_lshl_b32 s75, s75, 17\n" \
    "s_memrealtime s[46:47]\n" \
    "s_add_i32 s75, s75, s26\n" \
    "s_add_i32 s69, s75, s74\n" \
    "s_memrealtime s[48:49]\n" \
    "s_add_i32 s70, s69, 4096\n" \
    "s_add_i32 s71, s75, 16384\n" \
    "s_memrealtime s[50:51]\n" \
    "s_sub_i32 s76, s33, s42\n" \
    "s_ashr_i32 s76, s76, s32\n" \
    "s_memrealtime s[52:53]\n" \
    "s_add_i32 s42, s42, s76\n" \
    "s_add_i32 s40, s40, 64\n" \
    "s_memrealtime s[54:55]\n" \
    "s_nop 1\n" \
    "s_memrealtime s[56:57]\n" \
    "s_nop 1\n" \
    "s_memrealtime vcc\n" \
    "v_and_b32 v18, 0x300, v127\n" \
    "v_add_u32 v18, v18, v127\n" \
    "v_and_b32 v19, 0x380, v127\n" \
    "v_add_u32 v19, v19, v127\n" \
    "s_waitcnt lgkmcnt(0)\n" \
    "s_add_i32 s44, s44, s46\n" \
    "s_add_i32 s48, s48, s50\n" \
    "s_add_i32 s52, s52, s54\n" \
    "s_add_i32 s44, s44, s48\n" \
    "s_add_i32 s52, s52, s56\n" \
    "s_add_i32 s44, s44, s52\n" \
    "s_add_i32 s80, s44, vcc_lo\n" \
    "s_sub_i32 s76, s80, s29\n" \
    "s_mov_b32 s29, s80\n" \
    "s_sub_i32 s80, s88, s24\n" \
    "s_mov_b32 s24, s88\n" \
    "s_mul_i32 s75, s76, s25\n" \
    "s_lshr_b32 s75, s75, 12\n" \
    "s_sub_i32 s75, s80, s75\n" \
    "s_mul_hi_u32 s80, s80, s41\n" \
    "s_sub_i32 s80, s76, s80\n" \
    "s_lshl_b32 s80, s80, 18\n" \
    "s_cmp_lt_u32 s76, 0x1000\n" \
    "s_cselect_b32 s75, s75, 0\n" \
    "s_cselect_b32 s80, s80, 0\n" \
    "s_add_i32 s25, s25, s75\n" \
    "s_add_i32 s41, s41, s80\n" \
    "s_cmp_eq_u32 s36, 1\n" \
    "s_cselect_b32 s28, s29, s28\n" \
    "s_sub_i32 s80, s88, s86\n" \
    "s_min_u32 s40, s40, s80\n" \
    "s_waitcnt vmcnt(2)\n" \
    "v_readfirstlane_b32 s80, v100\n" \
    "v_cmp_ne_u32 vcc, s91, v99\n" \
    "s_mov_b32 s43, 0\n" \
    "s_max_i32 s38, s38, s80\n" \
    "s_cmp_lt_i32 s38, s91\n" \
    "s_cselect_b64 exec, 0, -1\n" \
    "s_cselect_b32 s94, 8, 0\n" \
    "buffer_load_dwordx4 v19, %[rw], s92 offen sc1 lds\n" \
    "s_add_i32 m0, s95, 896\n" \
    "s_nop 0\n" \
    "buffer_load_dwordx4 v19, %[rw], s92 offen offset:128 sc1 lds\n" \
    "s_add_i32 m0, s95, 8192\n" \
    "s_nop 0\n" \
    "buffer_load_dwordx4 v18, %[rw], s93 offen sc1 lds\n" \
    "s_add_i32 m0, s95, 8960\n" \
    "s_nop 0\n" \
    "buffer_load_dwordx4 v18, %[rw], s93 offen offset:256 sc1 lds\n" \
    "s_mov_b64 exec, -1\n" \
    "s_cbranch_vccnz 15f\n" \
    "10:\n" \
    "v_subrev_u32 v28, s29, v98\n" \
    "v_min_i32 v28, 0x4000, v28\n" \
    "v_max_i32 v28, 0xffffc000, v28\n" \
    "s_nop 1\n" \
    "v_max_i32_dpp v28, v28, v28 row_shr:1 row_mask:0xf bank_mask:0xf\n" \
    "s_nop 1\n" \
    "v_max_i32_dpp v28, v28, v28 row_shr:2 row_mask:0xf bank_mask:0xf\n" \
    "s_nop 1\n" \
    "v_max_i32_dpp v28, v28, v28 row_shr:4 row_mask:0xf bank_mask:0xf\n" \
    "s_nop 1\n" \
    "v_max_i32_dpp v28, v28, v28 row_shr:8 row_mask:0xf bank_mask:0xf\n" \
    "s_add_i32 s76, s29, s42\n" \
    "s_nop 0\n" \
    "v_max_i32_dpp v28, v28, v28 row_bcast:15 row_mask:0xa bank_mask:0xf\n" \
    "s_nop 1\n" \
    "v_max_i32_dpp v28, v28, v28 row_bcast:31 row_mask:0xc bank_mask:0xf\n" \
    "s_nop 0\n" \
    "v_readlane_b32 s37, v28, 63\n" \
    "v_add_u32 v30, -1, s36\n" \
    "s_add_i32 s37, s37, s76\n" \
    "v_mov_b32 v31, s37\n" \
    "s_mov_b64 exec, s[72:73]\n" \
    "buffer_store_dwordx2 v[30:31], off, %[rw], s65 sc1\n" \
    "s_mov_b64 exec, -1\n" \
    "s_cmp_lg_u32 s94, 0\n" \
    "s_cbranch_scc1 9f\n" \
    "s_mov_b32 s36, s91\n" \
    "s_branch 12b\n" \
    "5:\n" \
    "v_mov_b32 v107, 0\n" \
    "v_max3_u32 v107, v107, v0, v1\n" \
    "v_max3_u32 v107, v107, v2, v3\n" \
    "v_max3_u32 v107, v107, v4, v5\n" \
    "v_max3_u32 v107, v107, v6, v7\n" \
    "v_max3_u32 v107, v107, v8, v9\n" \
    "v_max3_u32 v107, v107, v10, v11\n" \
    "v_max3_u32 v107, v107, v12, v13\n" \
    "v_max3_u32 v107, v107, v14, v15\n" \
    "v_max3_u32 v107, v107, v16, v17\n" \
    "v_max3_u32 v107, v107, v18, v19\n" \
    "v_max3_u32 v107, v107, v24, v25\n" \
    "v_max3_u32 v107, v107, v26, v27\n" \
    "v_max3_u32 v107, v107, v28, v29\n" \
    "v_max3_u32 v107, v107, v30, v31\n" \
    "v_max3_u32 v107, v107, v32, v33\n" \
    "v_max3_u32 v107, v107, v34, v35\n" \
    "v_max3_u32 v107, v107, v36, v37\n" \
    "v_max3_u32 v107, v107, v38, v39\n" \
    "v_cmp_eq_u32 vcc, -1, v107\n" \
    "s_nop 4\n" \
    "s_cbranch_vccz 6b\n" \
    "s_cmp_eq_u32 s36, 0\n" \
    "s_cbranch_scc1 6b\n" \
    "s_add_i32 s39, s39, -1\n" \
    "s_cmp_lt_i32 s39, 0\n" \
    "s_cbranch_scc1 6b\n" \
    "s_add_i32 s30, s30, 1\n" \
    "s_and_b32 s80, s31, 0xffff\n" \
    "s_add_i32 s42, s42, s80\n" \
    "s_lshr_b32 s80, s31, 16\n" \
    "s_cmp_lt_u32 s36, 4\n" \
    "s_cselect_b32 s80, 0, s80\n" \
    "s_add_i32 s33, s33, s80\n" \
    "s_min_u32 s80, s80, 1\n" \
    "s_lshl_b32 s80, s80, 16\n" \
    "s_sub_i32 s31, s31, s80\n" \
    "s_waitcnt lgkmcnt(0)\n" \
    "s_mov_b32 s37, s29\n" \
    "s_nop 7\n" \
    "s_barrier\n" \
    "s_branch 0b\n" \
    "15:\n" \
    "s_add_i32 s30, s30, 0x10000\n" \
    "s_add_i32 s39, s39, -1\n" \
    "s_cmp_lt_i32 s39, 0\n" \
    "s_cbranch_scc1 10b\n" \
    "s_sleep 9\n" \
    "buffer_load_dwordx2 v[98:99], v113, %[rs], s78 offen sc1\n" \
    "s_waitcnt vmcnt(0)\n" \
    "v_cmp_ne_u32 vcc, s91, v99\n" \
    "s_nop 1\n" \
    "s_cbranch_vccnz 15b\n" \
    "s_memtime s[88:89]\n" \
    "s_memrealtime s[44:45]\n" \
    "s_nop 1\n" \
    "s_memrealtime s[46:47]\n" \
    "s_nop 1\n" \
    "s_memrealtime s[48:49]\n" \
    "s_nop 1\n" \
    "s_memrealtime s[50:51]\n" \
    "s_nop 1\n" \
    "s_memrealtime s[52:53]\n" \
    "s_nop 1\n" \
    "s_memrealtime s[54:55]\n" \
    "s_nop 1\n" \
    "s_memrealtime s[56:57]\n" \
    "s_nop 1\n" \
    "s_memrealtime vcc\n" \
    "s_waitcnt lgkmcnt(0)\n" \
    "s_add_i32 s44, s44, s46\n" \
    "s_add_i32 s48, s48, s50\n" \
    "s_add_i32 s52, s52, s54\n" \
    "s_add_i32 s44, s44, s48\n" \
    "s_add_i32 s52, s52, s56\n" \
    "s_add_i32 s44, s44, s52\n" \
    "s_add_i32 s29, s44, vcc_lo\n" \
    "s_mov_b32 s24, s88\n" \
    "s_branch 10b\n" \
    "16:\n" \
    "s_waitcnt lgkmcnt(0)\n" \
    "v_bfe_u32 v20, v127, 8, 2\n" \
    "v_subrev_u32 v20, s60, v20\n" \
    "v_mul_i32_i24 v20, -1088, v20\n" \
    "v_add_u32 v20, v20, v129\n" \
    "v_lshrrev_b32 v21, 2, v127\n" \
    "s_mov_b32 exec_lo, s66\n" \
    "s_mov_b32 exec_hi, s66\n" \
    "ds_read2_b32 v[96:97], v20 offset0:0 offset1:144\n" \
    "s_mov_b64 exec, -1\n" \
    "s_lshl_b32 s80, s91, 16\n" \
    "s_add_i32 s80, s80, s67\n" \
    "s_waitcnt lgkmcnt(0)\n" \
    "buffer_store_dword v96, v21, %[rw], s80 offen sc1\n" \
    "buffer_store_dword v97, v21, %[rw], s80 offen offset:256 sc1\n" \
    "s_branch 9f\n" \
    "17:\n" \
    "ds_read_b128 v[0:3], v108 offset:0\n" \
    "ds_read_b128 v[16:19], v108 offset:4096\n" \
    "s_waitcnt lgkmcnt(0)\n" \
    "s_branch 3b\n" \
    "7:\n" \
    "s_mov_b32 s43, 2\n" \
    "s_nop 7\n" \
    "s_waitcnt vmcnt(0)\n" \
    "9:\n" \
    "s_mov_b32 m0, s68\n" \
    "s_waitcnt lgkmcnt(0)\n" \
    "v_mov_b32 v0, s36\n" \
    "v_mov_b32 v1, s37\n" \
    "v_mov_b32 v2, s38\n" \
    "v_mov_b32 v3, s39\n" \
    "v_mov_b32 v4, s40\n" \
    "v_mov_b32 v5, s41\n" \
    "v_mov_b32 v6, s42\n" \
    "v_mov_b32 v7, s43\n" \
    "v_mov_b32 v8, s28\n" \
    "v_mov_b32 v9, s29\n" \
    "v_mov_b32 v10, s30\n" \
    "v_mov_b32 v11, s31\n" \
    "v_mov_b32 v12, s32\n" \
    "v_mov_b32 v13, s33\n" \
    "v_mov_b32 v14, s34\n" \
    "v_mov_b32 v15, s35\n" \
    "v_mov_b32 v16, s24\n" \
    "v_mov_b32 v17, s25\n" \
    "v_mov_b32 v18, s26\n" \
    "v_mov_b32 v19, s27\n" \
    "v_mov_b32 v20, s84\n" \
    "v_mov_b32 v21, s86\n" \
    "v_mov_b32 v22, s88\n" \
    "v_mov_b32 v23, s84\n" \
    "v_mov_b32 v24, s60\n" \
    "v_mov_b32 v25, s61\n" \
    "v_mov_b32 v26, s62\n" \
    "v_mov_b32 v27, s63\n" \
    "v_mov_b32 v28, s64\n" \
    "v_mov_b32 v29, s65\n" \
    "v_mov_b32 v30, s66\n" \
    "v_mov_b32 v31, s67\n" \
    ".Lstep_end:\n" \
    ""
static_assert(2 * PART_BYTES == 589824 && 2 * OFF_CAN == 1703936 && OFF_PART == 0 && OFF_VF - OFF_KF == 2048 * 4096 * 2, "constants of the step block");
#define STEP_CLOBBER "v32", "v33", "v34", "v35", "v36", "v37", "v38", "v39", "v40", "v41", "v42", "v43", "v44", "v45", "v46", "v47", "v48", "v49", "v50", "v51", "v52", "v53", "v54", "v55", "v56", "v57", "v58", "v59", "v60", "v61", "v62", "v63", "v64", "v65", "v66", "v67", "v68", "v69", "v70", "v71", "v72", "v73", "v74", "v75", "v76", "v77", "v78", "v79", "v80", "v81", "v82", "v83", "v84", "v85", "v86", "v87", "v88", "v89", "v90", "v91", "v92", "v93", "v94", "v95", "v98", "v99", "v100", "v101", "v102", "v103", "v104", "v105", "v106", "v107", "v108", "v109", "v110", "v111", "v112", "v113", "v114", "s24", "s25", "s26", "s27", "s28", "s29", "s30", "s31", "s32", "s33", "s34", "s35", "s36", "s37", "s38", "s39", "s40", "s41", "s42", "s43", "s44", "s45", "s46", "s47", "s48", "s49", "s50", "s51", "s52", "s53", "s54", "s55", "s56", "s57", "s58", "s59", "s60", "s61", "s62", "s63", "s64", "s65", "s66", "s67", "s68", "s69", "s70", "s71", "s72", "s73", "s74", "s75", "s76", "s77", "s78", "s79", "s80", "s81", "s82", "s83", "s84", "s85", "s86", "s87", "s88", "s89", "s90", "s91", "s92", "s93", "s94", "s95", "s96", "s97", "vcc", "scc", "memory"
#define OUT16_ASM \
    "s_mov_b32 exec_lo, %[tm]\n" \
    "s_mov_b32 exec_hi, 0\n" \
    "buffer_load_dword v79, %[tv], %[rw], %[so] offen sc1\n" \
    "s_mov_b64 exec, -1\n" \
    "s_add_i32 s44, %[so], 4096\n" \
    "s_add_i32 s45, %[so], 8192\n" \
    "s_add_i32 s46, %[so], 12288\n" \
    "buffer_load_dwordx4 v[16:19], %[vo], %[rw], %[so] offen offset:0 sc1\n" \
    "ds_read_b128 v[80:83], %[wl] offset:0\n" \
    "ds_read_b128 v[84:87], %[wl] offset:1024\n" \
    "s_nop 7\n" \
    "v_mov_b32 v104, 0\n" \
    "buffer_load_dwordx4 v[20:23], %[vo], %[rw], %[so] offen offset:256 sc1\n" \
    "s_nop 7\n" \
    "s_nop 3\n" \
    "buffer_load_dwordx4 v[24:27], %[vo], %[rw], %[so] offen offset:1024 sc1\n" \
    "s_nop 7\n" \
    "s_nop 3\n" \
    "buffer_load_dwordx4 v[28:31], %[vo], %[rw], %[so] offen offset:1280 sc1\n" \
    "s_nop 7\n" \
    "s_nop 3\n" \
    "buffer_load_dwordx4 v[32:35], %[vo], %[rw], %[so] offen offset:2048 sc1\n" \
    "s_nop 7\n" \
    "s_nop 3\n" \
    "buffer_load_dwordx4 v[36:39], %[vo], %[rw], %[so] offen offset:2304 sc1\n" \
    "s_nop 7\n" \
    "s_nop 3\n" \
    "buffer_load_dwordx4 v[40:43], %[vo], %[rw], %[so] offen offset:3072 sc1\n" \
    "s_nop 7\n" \
    "s_nop 3\n" \
    "buffer_load_dwordx4 v[44:47], %[vo], %[rw], %[so] offen offset:3328 sc1\n" \
    "s_nop 7\n" \
    "s_nop 3\n" \
    "buffer_load_dwordx4 v[48:51], %[vo], %[rw], s44 offen offset:0 sc1\n" \
    "s_nop 7\n" \
    "s_nop 3\n" \
    "buffer_load_dwordx4 v[52:55], %[vo], %[rw], s44 offen offset:256 sc1\n" \
    "s_nop 7\n" \
    "s_nop 3\n" \
    "buffer_load_dwordx4 v[56:59], %[vo], %[rw], s44 offen offset:1024 sc1\n" \
    "s_nop 7\n" \
    "s_nop 3\n" \
    "buffer_load_dwordx4 v[60:63], %[vo], %[rw], s44 offen offset:1280 sc1\n" \
    "ds_read_b128 v[88:91], %[wl] offset:2048\n" \
    "s_waitcnt vmcnt(11)\n" \
    "v_mfma_f32_16x16x32_bf16 v[0:3], a[0:3], v[16:19], 0\n" \
    "v_max3_u32 v104, v104, v16, v17\n" \
    "v_mfma_f32_16x16x32_bf16 v[4:7], a[128:131], v[16:19], 0\n" \
    "v_max3_u32 v104, v104, v18, v19\n" \
    "v_mfma_f32_16x16x32_bf16 v[8:11], v[128:131], v[16:19], 0\n" \
    "s_waitcnt lgkmcnt(2)\n" \
    "v_mfma_f32_16x16x32_bf16 v[12:15], v[80:83], v[16:19], 0\n" \
    "buffer_load_dwordx4 v[64:67], %[vo], %[rw], s44 offen offset:2048 sc1\n" \
    "ds_read_b128 v[80:83], %[wl] offset:3072\n" \
    "s_waitcnt vmcnt(11)\n" \
    "v_mfma_f32_16x16x32_bf16 v[0:3], a[4:7], v[20:23], v[0:3]\n" \
    "v_max3_u32 v104, v104, v20, v21\n" \
    "v_mfma_f32_16x16x32_bf16 v[4:7], a[132:135], v[20:23], v[4:7]\n" \
    "v_max3_u32 v104, v104, v22, v23\n" \
    "v_mfma_f32_16x16x32_bf16 v[8:11], v[132:135], v[20:23], v[8:11]\n" \
    "s_waitcnt lgkmcnt(2)\n" \
    "v_mfma_f32_16x16x32_bf16 v[12:15], v[84:87], v[20:23], v[12:15]\n" \
    "buffer_load_dwordx4 v[68:71], %[vo], %[rw], s44 offen offset:2304 sc1\n" \
    "ds_read_b128 v[84:87], %[wl] offset:4096\n" \
    "s_waitcnt vmcnt(11)\n" \
    "v_mfma_f32_16x16x32_bf16 v[0:3], a[8:11], v[24:27], v[0:3]\n" \
    "v_max3_u32 v104, v104, v24, v25\n" \
    "v_mfma_f32_16x16x32_bf16 v[4:7], a[136:139], v[24:27], v[4:7]\n" \
    "v_max3_u32 v104, v104, v26, v27\n" \
    "v_mfma_f32_16x16x32_bf16 v[8:11], v[136:139], v[24:27], v[8:11]\n" \
    "s_waitcnt lgkmcnt(2)\n" \
    "v_mfma_f32_16x16x32_bf16 v[12:15], v[88:91], v[24:27], v[12:15]\n" \
    "buffer_load_dwordx4 v[72:75], %[vo], %[rw], s44 offen offset:3072 sc1\n" \
    "ds_read_b128 v[88:91], %[wl] offset:5120\n" \
    "s_waitcnt vmcnt(11)\n" \
    "v_mfma_f32_16x16x32_bf16 v[0:3], a[12:15], v[28:31], v[0:3]\n" \
    "v_max3_u32 v104, v104, v28, v29\n" \
    "v_mfma_f32_16x16x32_bf16 v[4:7], a[140:143], v[28:31], v[4:7]\n" \
    "v_max3_u32 v104, v104, v30, v31\n" \
    "v_mfma_f32_16x16x32_bf16 v[8:11], v[140:143], v[28:31], v[8:11]\n" \
    "s_waitcnt lgkmcnt(2)\n" \
    "v_mfma_f32_16x16x32_bf16 v[12:15], v[80:83], v[28:31], v[12:15]\n" \
    "buffer_load_dwordx4 v[76:79], %[vo], %[rw], s44 offen offset:3328 sc1\n" \
    "ds_read_b128 v[80:83], %[wl] offset:6144\n" \
    "s_waitcnt vmcnt(11)\n" \
    "v_mfma_f32_16x16x32_bf16 v[0:3], a[16:19], v[32:35], v[0:3]\n" \
    "v_max3_u32 v104, v104, v32, v33\n" \
    "v_mfma_f32_16x16x32_bf16 v[4:7], a[144:147], v[32:35], v[4:7]\n" \
    "v_max3_u32 v104, v104, v34, v35\n" \
    "v_mfma_f32_16x16x32_bf16 v[8:11], v[144:147], v[32:35], v[8:11]\n" \
    "s_waitcnt lgkmcnt(2)\n" \
    "v_mfma_f32_16x16x32_bf16 v[12:15], v[84:87], v[32:35], v[12:15]\n" \
    "buffer_load_dwordx4 v[16:19], %[vo], %[rw], s45 offen offset:0 sc1\n" \
    "ds_read_b128 v[84:87], %[wl] offset:7168\n" \
    "s_waitcnt vmcnt(11)\n" \
    "v_mfma_f32_16x16x32_bf16 v[0:3], a[20:23], v[36:39], v[0:3]\n" \
    "v_max3_u32 v104, v104, v36, v37\n" \
    "v_mfma_f32_16x16x32_bf16 v[4:7], a[148:151], v[36:39], v[4:7]\n" \
    "v_max3_u32 v104, v104, v38, v39\n" \
    "v_mfma_f32_16x16x32_bf16 v[8:11], v[148:151], v[36:39], v[8:11]\n" \
    "s_waitcnt lgkmcnt(2)\n" \
    "v_mfma_f32_16x16x32_bf16 v[12:15], v[88:91], v[36:39], v[12:15]\n" \
    "buffer_load_dwordx4 v[20:23], %[vo], %[rw], s45 offen offset:256 sc1\n" \
    "ds_read_b128 v[88:91], %[wl] offset:8192\n" \
    "s_waitcnt vmcnt(11)\n" \
    "v_mfma_f32_16x16x32_bf16 v[0:3], a[24:27], v[40:43], v[0:3]\n" \
    "v_max3_u32 v104, v104, v40, v41\n" \
    "v_mfma_f32_16x16x32_bf16 v[4:7], a[152:155], v[40:43], v[4:7]\n" \
    "v_max3_u32 v104, v104, v42, v43\n" \
    "v_mfma_f32_16x16x32_bf16 v[8:11], v[152:155], v[40:43], v[8:11]\n" \
    "s_waitcnt lgkmcnt(2)\n" \
    "v_mfma_f32_16x16x32_bf16 v[12:15], v[80:83], v[40:43], v[12:15]\n" \
    "buffer_load_dwordx4 v[24:27], %[vo], %[rw], s45 offen offset:1024 sc1\n" \
    "ds_read_b128 v[80:83], %[wl] offset:9216\n" \
    "s_waitcnt vmcnt(11)\n" \
    "v_mfma_f32_16x16x32_bf16 v[0:3], a[28:31], v[44:47], v[0:3]\n" \
    "v_max3_u32 v104, v104, v44, v45\n" \
    "v_mfma_f32_16x16x32_bf16 v[4:7], a[156:159], v[44:47], v[4:7]\n" \
    "v_max3_u32 v104, v104, v46, v47\n" \
    "v_mfma_f32_16x16x32_bf16 v[8:11], v[156:159], v[44:47], v[8:11]\n" \
    "s_waitcnt lgkmcnt(2)\n" \
    "v_mfma_f32_16x16x32_bf16 v[12:15], v[84:87], v[44:47], v[12:15]\n" \
    "buffer_load_dwordx4 v[28:31], %[vo], %[rw], s45 offen offset:1280 sc1\n" \
    "ds_read_b128 v[84:87], %[wl] offset:10240\n" \
    "s_waitcnt vmcnt(11)\n" \
    "v_mfma_f32_16x16x32_bf16 v[0:3], a[32:35], v[48:51], v[0:3]\n" \
    "v_max3_u32 v104, v104, v48, v49\n" \
    "v_mfma_f32_16x16x32_bf16 v[4:7], a[160:163], v[48:51], v[4:7]\n" \
    "v_max3_u32 v104, v104, v50, v51\n" \
    "v_mfma_f32_16x16x32_bf16 v[8:11], v[160:163], v[48:51], v[8:11]\n" \
    "s_waitcnt lgkmcnt(2)\n" \
    "v_mfma_f32_16x16x32_bf16 v[12:15], v[88:91], v[48:51], v[12:15]\n" \
    "buffer_load_dwordx4 v[32:35], %[vo], %[rw], s45 offen offset:2048 sc1\n" \
    "ds_read_b128 v[88:91], %[wl] offset:11264\n" \
    "s_waitcnt vmcnt(11)\n" \
    "v_mfma_f32_16x16x32_bf16 v[0:3], a[36:39], v[52:55], v[0:3]\n" \
    "v_max3_u32 v104, v104, v52, v53\n" \
    "v_mfma_f32_16x16x32_bf16 v[4:7], a[164:167], v[52:55], v[4:7]\n" \
    "v_max3_u32 v104, v104, v54, v55\n" \
    "v_mfma_f32_16x16x32_bf16 v[8:11], v[164:167], v[52:55], v[8:11]\n" \
    "s_waitcnt lgkmcnt(2)\n" \
    "v_mfma_f32_16x16x32_bf16 v[12:15], v[80:83], v[52:55], v[12:15]\n" \
    "buffer_load_dwordx4 v[36:39], %[vo], %[rw], s45 offen offset:2304 sc1\n" \
    "ds_read_b128 v[80:83], %[wl] offset:12288\n" \
    "s_waitcnt vmcnt(11)\n" \
    "v_mfma_f32_16x16x32_bf16 v[0:3], a[40:43], v[56:59], v[0:3]\n" \
    "v_max3_u32 v104, v104, v56, v57\n" \
    "v_mfma_f32_16x16x32_bf16 v[4:7], a[168:171], v[56:59], v[4:7]\n" \
    "v_max3_u32 v104, v104, v58, v59\n" \
    "v_mfma_f32_16x16x32_bf16 v[8:11], v[168:171], v[56:59], v[8:11]\n" \
    "s_waitcnt lgkmcnt(2)\n" \
    "v_mfma_f32_16x16x32_bf16 v[12:15], v[84:87], v[56:59], v[12:15]\n" \
    "buffer_load_dwordx4 v[40:43], %[vo], %[rw], s45 offen offset:3072 sc1\n" \
    "ds_read_b128 v[84:87], %[wl] offset:13312\n" \
    "s_waitcnt vmcnt(11)\n" \
    "v_mfma_f32_16x16x32_bf16 v[0:3], a[44:47], v[60:63], v[0:3]\n" \
    "v_max3_u32 v104, v104, v60, v61\n" \
    "v_mfma_f32_16x16x32_bf16 v[4:7], a[172:175], v[60:63], v[4:7]\n" \
    "v_max3_u32 v104, v104, v62, v63\n" \
    "v_mfma_f32_16x16x32_bf16 v[8:11], v[172:175], v[60:63], v[8:11]\n" \
    "s_waitcnt lgkmcnt(2)\n" \
    "v_mfma_f32_16x16x32_bf16 v[12:15], v[88:91], v[60:63], v[12:15]\n" \
    "buffer_load_dwordx4 v[44:47], %[vo], %[rw], s45 offen offset:3328 sc1\n" \
    "ds_read_b128 v[88:91], %[wl] offset:14336\n" \
    "s_waitcnt vmcnt(11)\n" \
    "v_mfma_f32_16x16x32_bf16 v[0:3], a[48:51], v[64:67], v[0:3]\n" \
    "v_max3_u32 v104, v104, v64, v65\n" \
    "v_mfma_f32_16x16x32_bf16 v[4:7], a[176:179], v[64:67], v[4:7]\n" \
    "v_max3_u32 v104, v104, v66, v67\n" \
    "v_mfma_f32_16x16x32_bf16 v[8:11], v[176:179], v[64:67], v[8:11]\n" \
    "s_waitcnt lgkmcnt(2)\n" \
    "v_mfma_f32_16x16x32_bf16 v[12:15], v[80:83], v[64:67], v[12:15]\n" \
    "buffer_load_dwordx4 v[48:51], %[vo], %[rw], s46 offen offset:0 sc1\n" \
    "ds_read_b128 v[80:83], %[wl] offset:15360\n" \
    "s_waitcnt vmcnt(11)\n" \
    "v_mfma_f32_16x16x32_bf16 v[0:3], a[52:55], v[68:71], v[0:3]\n" \
    "v_max3_u32 v104, v104, v68, v69\n" \
    "v_mfma_f32_16x16x32_bf16 v[4:7], a[180:183], v[68:71], v[4:7]\n" \
    "v_max3_u32 v104, v104, v70, v71\n" \
    "v_mfma_f32_16x16x32_bf16 v[8:11], v[180:183], v[68:71], v[8:11]\n" \
    "s_waitcnt lgkmcnt(2)\n" \
    "v_mfma_f32_16x16x32_bf16 v[12:15], v[84:87], v[68:71], v[12:15]\n" \
    "buffer_load_dwordx4 v[52:55], %[vo], %[rw], s46 offen offset:256 sc1\n" \
    "ds_read_b128 v[84:87], %[wl] offset:16384\n" \
    "s_waitcnt vmcnt(11)\n" \
    "v_mfma_f32_16x16x32_bf16 v[0:3], a[56:59], v[72:75], v[0:3]\n" \
    "v_max3_u32 v104, v104, v72, v73\n" \
    "v_mfma_f32_16x16x32_bf16 v[4:7], a[184:187], v[72:75], v[4:7]\n" \
    "v_max3_u32 v104, v104, v74, v75\n" \
    "v_mfma_f32_16x16x32_bf16 v[8:11], v[184:187], v[72:75], v[8:11]\n" \
    "s_waitcnt lgkmcnt(2)\n" \
    "v_mfma_f32_16x16x32_bf16 v[12:15], v[88:91], v[72:75], v[12:15]\n" \
    "buffer_load_dwordx4 v[56:59], %[vo], %[rw], s46 offen offset:1024 sc1\n" \
    "ds_read_b128 v[88:91], %[wl] offset:17408\n" \
    "s_waitcnt vmcnt(11)\n" \
    "v_mfma_f32_16x16x32_bf16 v[0:3], a[60:63], v[76:79], v[0:3]\n" \
    "v_max3_u32 v104, v104, v76, v77\n" \
    "v_mfma_f32_16x16x32_bf16 v[4:7], a[188:191], v[76:79], v[4:7]\n" \
    "v_max3_u32 v104, v104, v78, v79\n" \
    "v_mfma_f32_16x16x32_bf16 v[8:11], v[188:191], v[76:79], v[8:11]\n" \
    "s_waitcnt lgkmcnt(2)\n" \
    "v_mfma_f32_16x16x32_bf16 v[12:15], v[80:83], v[76:79], v[12:15]\n" \
    "buffer_load_dwordx4 v[60:63], %[vo], %[rw], s46 offen offset:1280 sc1\n" \
    "ds_read_b128 v[80:83], %[wl] offset:18432\n" \
    "s_waitcnt vmcnt(11)\n" \
    "v_mfma_f32_16x16x32_bf16 v[0:3], a[64:67], v[16:19], v[0:3]\n" \
    "v_max3_u32 v104, v104, v16, v17\n" \
    "v_mfma_f32_16x16x32_bf16 v[4:7], a[192:195], v[16:19], v[4:7]\n" \
    "v_max3_u32 v104, v104, v18, v19\n" \
    "v_mfma_f32_16x16x32_bf16 v[8:11], v[192:195], v[16:19], v[8:11]\n" \
    "s_waitcnt lgkmcnt(2)\n" \
    "v_mfma_f32_16x16x32_bf16 v[12:15], v[84:87], v[16:19], v[12:15]\n" \
    "buffer_load_dwordx4 v[64:67], %[vo], %[rw], s46 offen offset:2048 sc1\n" \
    "ds_read_b128 v[84:87], %[wl] offset:19456\n" \
    "s_waitcnt vmcnt(11)\n" \
    "v_mfma_f32_16x16x32_bf16 v[0:3], a[68:71], v[20:23], v[0:3]\n" \
    "v_max3_u32 v104, v104, v20, v21\n" \
    "v_mfma_f32_16x16x32_bf16 v[4:7], a[196:199], v[20:23], v[4:7]\n" \
    "v_max3_u32 v104, v104, v22, v23\n" \
    "v_mfma_f32_16x16x32_bf16 v[8:11], v[196:199], v[20:23], v[8:11]\n" \
    "s_waitcnt lgkmcnt(2)\n" \
    "v_mfma_f32_16x16x32_bf16 v[12:15], v[88:91], v[20:23], v[12:15]\n" \
    "buffer_load_dwordx4 v[68:71], %[vo], %[rw], s46 offen offset:2304 sc1\n" \
    "ds_read_b128 v[88:91], %[wl] offset:20480\n" \
    "s_waitcnt vmcnt(11)\n" \
    "v_mfma_f32_16x16x32_bf16 v[0:3], a[72:75], v[24:27], v[0:3]\n" \
    "v_max3_u32 v104, v104, v24, v25\n" \
    "v_mfma_f32_16x16x32_bf16 v[4:7], a[200:203], v[24:27], v[4:7]\n" \
    "v_max3_u32 v104, v104, v26, v27\n" \
    "v_mfma_f32_16x16x32_bf16 v[8:11], v[200:203], v[24:27], v[8:11]\n" \
    "s_waitcnt lgkmcnt(2)\n" \
    "v_mfma_f32_16x16x32_bf16 v[12:15], v[80:83], v[24:27], v[12:15]\n" \
    "buffer_load_dwordx4 v[72:75], %[vo], %[rw], s46 offen offset:3072 sc1\n" \
    "ds_read_b128 v[80:83], %[wl] offset:21504\n" \
    "s_waitcnt vmcnt(11)\n" \
    "v_mfma_f32_16x16x32_bf16 v[0:3], a[76:79], v[28:31], v[0:3]\n" \
    "v_max3_u32 v104, v104, v28, v29\n" \
    "v_mfma_f32_16x16x32_bf16 v[4:7], a[204:207], v[28:31], v[4:7]\n" \
    "v_max3_u32 v104, v104, v30, v31\n" \
    "v_mfma_f32_16x16x32_bf16 v[8:11], v[204:207], v[28:31], v[8:11]\n" \
    "s_waitcnt lgkmcnt(2)\n" \
    "v_mfma_f32_16x16x32_bf16 v[12:15], v[84:87], v[28:31], v[12:15]\n" \
    "buffer_load_dwordx4 v[76:79], %[vo], %[rw], s46 offen offset:3328 sc1\n" \
    "ds_read_b128 v[84:87], %[wl] offset:22528\n" \
    "s_waitcnt vmcnt(11)\n" \
    "v_mfma_f32_16x16x32_bf16 v[0:3], a[80:83], v[32:35], v[0:3]\n" \
    "v_max3_u32 v104, v104, v32, v33\n" \
    "v_mfma_f32_16x16x32_bf16 v[4:7], a[208:211], v[32:35], v[4:7]\n" \
    "v_max3_u32 v104, v104, v34, v35\n" \
    "v_mfma_f32_16x16x32_bf16 v[8:11], v[208:211], v[32:35], v[8:11]\n" \
    "s_waitcnt lgkmcnt(2)\n" \
    "v_mfma_f32_16x16x32_bf16 v[12:15], v[88:91], v[32:35], v[12:15]\n" \
    "buffer_load_dwordx2 v[106:107], %[bv], %[rw], %[bs] offen sc1\n" \
    "ds_read_b128 v[88:91], %[wl] offset:23552\n" \
    "s_waitcnt vmcnt(11)\n" \
    "v_mfma_f32_16x16x32_bf16 v[0:3], a[84:87], v[36:39], v[0:3]\n" \
    "v_max3_u32 v104, v104, v36, v37\n" \
    "v_mfma_f32_16x16x32_bf16 v[4:7], a[212:215], v[36:39], v[4:7]\n" \
    "v_max3_u32 v104, v104, v38, v39\n" \
    "v_mfma_f32_16x16x32_bf16 v[8:11], v[212:215], v[36:39], v[8:11]\n" \
    "s_waitcnt lgkmcnt(2)\n" \
    "v_mfma_f32_16x16x32_bf16 v[12:15], v[80:83], v[36:39], v[12:15]\n" \
    "buffer_load_dword v108, %[gv], %[rw], %[gs] offen sc1\n" \
    "ds_read_b128 v[80:83], %[wl] offset:24576\n" \
    "s_waitcnt vmcnt(11)\n" \
    "v_mfma_f32_16x16x32_bf16 v[0:3], a[88:91], v[40:43], v[0:3]\n" \
    "v_max3_u32 v104, v104, v40, v41\n" \
    "v_mfma_f32_16x16x32_bf16 v[4:7], a[216:219], v[40:43], v[4:7]\n" \
    "v_max3_u32 v104, v104, v42, v43\n" \
    "v_mfma_f32_16x16x32_bf16 v[8:11], v[216:219], v[40:43], v[8:11]\n" \
    "s_waitcnt lgkmcnt(2)\n" \
    "v_mfma_f32_16x16x32_bf16 v[12:15], v[84:87], v[40:43], v[12:15]\n" \
    "buffer_load_dword v105, %[sv], %[rw], %[ss] offen sc1\n" \
    "ds_read_b128 v[84:87], %[wl] offset:25600\n" \
    "s_waitcnt vmcnt(11)\n" \
    "v_mfma_f32_16x16x32_bf16 v[0:3], a[92:95], v[44:47], v[0:3]\n" \
    "v_max3_u32 v104, v104, v44, v45\n" \
    "v_mfma_f32_16x16x32_bf16 v[4:7], a[220:223], v[44:47], v[4:7]\n" \
    "v_max3_u32 v104, v104, v46, v47\n" \
    "v_mfma_f32_16x16x32_bf16 v[8:11], v[220:223], v[44:47], v[8:11]\n" \
    "s_waitcnt lgkmcnt(2)\n" \
    "v_mfma_f32_16x16x32_bf16 v[12:15], v[88:91], v[44:47], v[12:15]\n" \
    "ds_read_b128 v[88:91], %[wl] offset:26624\n" \
    "s_waitcnt vmcnt(10)\n" \
    "v_mfma_f32_16x16x32_bf16 v[0:3], a[96:99], v[48:51], v[0:3]\n" \
    "v_max3_u32 v104, v104, v48, v49\n" \
    "v_mfma_f32_16x16x32_bf16 v[4:7], a[224:227], v[48:51], v[4:7]\n" \
    "v_max3_u32 v104, v104, v50, v51\n" \
    "v_mfma_f32_16x16x32_bf16 v[8:11], v[224:227], v[48:51], v[8:11]\n" \
    "s_waitcnt lgkmcnt(2)\n" \
    "v_mfma_f32_16x16x32_bf16 v[12:15], v[80:83], v[48:51], v[12:15]\n" \
    "ds_read_b128 v[80:83], %[wl] offset:27648\n" \
    "s_waitcnt vmcnt(9)\n" \
    "v_mfma_f32_16x16x32_bf16 v[0:3], a[100:103], v[52:55], v[0:3]\n" \
    "v_max3_u32 v104, v104, v52, v53\n" \
    "v_mfma_f32_16x16x32_bf16 v[4:7], a[228:231], v[52:55], v[4:7]\n" \
    "v_max3_u32 v104, v104, v54, v55\n" \
    "v_mfma_f32_16x16x32_bf16 v[8:11], v[228:231], v[52:55], v[8:11]\n" \
    "s_waitcnt lgkmcnt(2)\n" \
    "v_mfma_f32_16x16x32_bf16 v[12:15], v[84:87], v[52:55], v[12:15]\n" \
    "ds_read_b128 v[84:87], %[wl] offset:28672\n" \
    "s_waitcnt vmcnt(8)\n" \
    "v_mfma_f32_16x16x32_bf16 v[0:3], a[104:107], v[56:59], v[0:3]\n" \
    "v_max3_u32 v104, v104, v56, v57\n" \
    "v_mfma_f32_16x16x32_bf16 v[4:7], a[232:235], v[56:59], v[4:7]\n" \
    "v_max3_u32 v104, v104, v58, v59\n" \
    "v_mfma_f32_16x16x32_bf16 v[8:11], v[232:235], v[56:59], v[8:11]\n" \
    "s_waitcnt lgkmcnt(2)\n" \
    "v_mfma_f32_16x16x32_bf16 v[12:15], v[88:91], v[56:59], v[12:15]\n" \
    "ds_read_b128 v[88:91], %[wl] offset:29696\n" \
    "s_waitcnt vmcnt(7)\n" \
    "v_mfma_f32_16x16x32_bf16 v[0:3], a[108:111], v[60:63], v[0:3]\n" \
    "v_max3_u32 v104, v104, v60, v61\n" \
    "v_mfma_f32_16x16x32_bf16 v[4:7], a[236:239], v[60:63], v[4:7]\n" \
    "v_max3_u32 v104, v104, v62, v63\n" \
    "v_mfma_f32_16x16x32_bf16 v[8:11], v[236:239], v[60:63], v[8:11]\n" \
    "s_waitcnt lgkmcnt(2)\n" \
    "v_mfma_f32_16x16x32_bf16 v[12:15], v[80:83], v[60:63], v[12:15]\n" \
    "ds_read_b128 v[80:83], %[wl] offset:30720\n" \
    "s_waitcnt vmcnt(6)\n" \
    "v_mfma_f32_16x16x32_bf16 v[0:3], a[112:115], v[64:67], v[0:3]\n" \
    "v_max3_u32 v104, v104, v64, v65\n" \
    "v_mfma_f32_16x16x32_bf16 v[4:7], a[240:243], v[64:67], v[4:7]\n" \
    "v_max3_u32 v104, v104, v66, v67\n" \
    "v_mfma_f32_16x16x32_bf16 v[8:11], v[240:243], v[64:67], v[8:11]\n" \
    "s_waitcnt lgkmcnt(2)\n" \
    "v_mfma_f32_16x16x32_bf16 v[12:15], v[84:87], v[64:67], v[12:15]\n" \
    "ds_read_b128 v[84:87], %[wl] offset:31744\n" \
    "s_waitcnt vmcnt(5)\n" \
    "v_mfma_f32_16x16x32_bf16 v[0:3], a[116:119], v[68:71], v[0:3]\n" \
    "v_max3_u32 v104, v104, v68, v69\n" \
    "v_mfma_f32_16x16x32_bf16 v[4:7], a[244:247], v[68:71], v[4:7]\n" \
    "v_max3_u32 v104, v104, v70, v71\n" \
    "v_mfma_f32_16x16x32_bf16 v[8:11], v[244:247], v[68:71], v[8:11]\n" \
    "s_waitcnt lgkmcnt(2)\n" \
    "v_mfma_f32_16x16x32_bf16 v[12:15], v[88:91], v[68:71], v[12:15]\n" \
    "s_waitcnt vmcnt(4)\n" \
    "v_mfma_f32_16x16x32_bf16 v[0:3], a[120:123], v[72:75], v[0:3]\n" \
    "v_max3_u32 v104, v104, v72, v73\n" \
    "v_mfma_f32_16x16x32_bf16 v[4:7], a[248:251], v[72:75], v[4:7]\n" \
    "v_max3_u32 v104, v104, v74, v75\n" \
    "v_mfma_f32_16x16x32_bf16 v[8:11], v[248:251], v[72:75], v[8:11]\n" \
    "s_waitcnt lgkmcnt(1)\n" \
    "v_mfma_f32_16x16x32_bf16 v[12:15], v[80:83], v[72:75], v[12:15]\n" \
    "s_waitcnt vmcnt(3)\n" \
    "v_mfma_f32_16x16x32_bf16 v[0:3], a[124:127], v[76:79], v[0:3]\n" \
    "v_max3_u32 v104, v104, v76, v77\n" \
    "v_mfma_f32_16x16x32_bf16 v[4:7], a[252:255], v[76:79], v[4:7]\n" \
    "v_max3_u32 v104, v104, v78, v79\n" \
    "v_mfma_f32_16x16x32_bf16 v[8:11], v[252:255], v[76:79], v[8:11]\n" \
    "s_waitcnt lgkmcnt(0)\n" \
    "v_mfma_f32_16x16x32_bf16 v[12:15], v[84:87], v[76:79], v[12:15]\n" \
    "s_waitcnt vmcnt(0)\n" \
    "s_nop 7\n" \
    ""
#define OUT8_ASM \
    "s_mov_b32 exec_lo, %[tm]\n" \
    "s_mov_b32 exec_hi, 0\n" \
    "buffer_load_dword v79, %[tv], %[rw], %[so] offen sc1\n" \
    "s_mov_b64 exec, -1\n" \
    "s_add_i32 s44, %[so], 4096\n" \
    "s_add_i32 s45, %[so], 8192\n" \
    "s_add_i32 s46, %[so], 12288\n" \
    "buffer_load_dwordx4 v[16:19], %[vo], %[rw], %[so] offen offset:0 sc1\n" \
    "ds_read_b128 v[80:83], %[wl] offset:0\n" \
    "ds_read_b128 v[84:87], %[wl] offset:1024\n" \
    "ds_read_b128 v[88:91], %[wl] offset:2048\n" \
    "ds_read_b128 v[92:95], %[wl] offset:3072\n" \
    "v_mov_b64 v[0:1], 0\n" \
    "v_mov_b64 v[2:3], 0\n" \
    "v_mov_b64 v[4:5], 0\n" \
    "v_mov_b64 v[6:7], 0\n" \
    "v_mov_b64 v[8:9], 0\n" \
    "v_mov_b64 v[10:11], 0\n" \
    "v_mov_b64 v[12:13], 0\n" \
    "v_mov_b64 v[14:15], 0\n" \
    "v_mov_b32 v104, 0\n" \
    "buffer_load_dwordx4 v[20:23], %[vo], %[rw], %[so] offen offset:1024 sc1\n" \
    "s_nop 7\n" \
    "s_nop 3\n" \
    "buffer_load_dwordx4 v[24:27], %[vo], %[rw], %[so] offen offset:2048 sc1\n" \
    "s_nop 7\n" \
    "s_nop 3\n" \
    "buffer_load_dwordx4 v[28:31], %[vo], %[rw], %[so] offen offset:3072 sc1\n" \
    "s_nop 7\n" \
    "s_nop 3\n" \
    "buffer_load_dwordx4 v[32:35], %[vo], %[rw], s44 offen offset:0 sc1\n" \
    "s_nop 7\n" \
    "s_nop 3\n" \
    "buffer_load_dwordx4 v[36:39], %[vo], %[rw], s44 offen offset:1024 sc1\n" \
    "s_nop 7\n" \
    "s_nop 3\n" \
    "buffer_load_dwordx4 v[40:43], %[vo], %[rw], s44 offen offset:2048 sc1\n" \
    "s_nop 7\n" \
    "s_nop 3\n" \
    "buffer_load_dwordx4 v[44:47], %[vo], %[rw], s44 offen offset:3072 sc1\n" \
    "s_nop 7\n" \
    "s_nop 3\n" \
    "buffer_load_dwordx4 v[48:51], %[vo], %[rw], s45 offen offset:0 sc1\n" \
    "s_nop 7\n" \
    "s_nop 3\n" \
    "buffer_load_dwordx4 v[52:55], %[vo], %[rw], s45 offen offset:1024 sc1\n" \
    "ds_read_b128 v[96:99], %[wl] offset:4096\n" \
    "ds_read_b128 v[100:103], %[wl] offset:5120\n" \
    "s_waitcnt vmcnt(9)\n" \
    "v_smfmac_f32_16x16x64_bf16 v[0:3], v[16:19], a[0:7], %[idx]\n" \
    "v_max3_u32 v104, v104, v16, v17\n" \
    "v_smfmac_f32_16x16x64_bf16 v[4:7], v[16:19], a[128:135], %[idx]\n" \
    "v_max3_u32 v104, v104, v18, v19\n" \
    "v_smfmac_f32_16x16x64_bf16 v[8:11], v[16:19], v[128:135], %[idx]\n" \
    "s_waitcnt lgkmcnt(4)\n" \
    "v_smfmac_f32_16x16x64_bf16 v[12:15], v[16:19], v[80:87], %[idx]\n" \
    "buffer_load_dwordx4 v[56:59], %[vo], %[rw], s45 offen offset:2048 sc1\n" \
    "ds_read_b128 v[80:83], %[wl] offset:6144\n" \
    "ds_read_b128 v[84:87], %[wl] offset:7168\n" \
    "s_waitcnt vmcnt(9)\n" \
    "v_smfmac_f32_16x16x64_bf16 v[0:3], v[20:23], a[8:15], %[idx]\n" \
    "v_max3_u32 v104, v104, v20, v21\n" \
    "v_smfmac_f32_16x16x64_bf16 v[4:7], v[20:23], a[136:143], %[idx]\n" \
    "v_max3_u32 v104, v104, v22, v23\n" \
    "v_smfmac_f32_16x16x64_bf16 v[8:11], v[20:23], v[136:143], %[idx]\n" \
    "s_waitcnt lgkmcnt(4)\n" \
    "v_smfmac_f32_16x16x64_bf16 v[12:15], v[20:23], v[88:95], %[idx]\n" \
    "buffer_load_dwordx4 v[60:63], %[vo], %[rw], s45 offen offset:3072 sc1\n" \
    "ds_read_b128 v[88:91], %[wl] offset:8192\n" \
    "ds_read_b128 v[92:95], %[wl] offset:9216\n" \
    "s_waitcnt vmcnt(9)\n" \
    "v_smfmac_f32_16x16x64_bf16 v[0:3], v[24:27], a[16:23], %[idx]\n" \
    "v_max3_u32 v104, v104, v24, v25\n" \
    "v_smfmac_f32_16x16x64_bf16 v[4:7], v[24:27], a[144:151], %[idx]\n" \
    "v_max3_u32 v104, v104, v26, v27\n" \
    "v_smfmac_f32_16x16x64_bf16 v[8:11], v[24:27], v[144:151], %[idx]\n" \
    "s_waitcnt lgkmcnt(4)\n" \
    "v_smfmac_f32_16x16x64_bf16 v[12:15], v[24:27], v[96:103], %[idx]\n" \
    "buffer_load_dwordx4 v[64:67], %[vo], %[rw], s46 offen offset:0 sc1\n" \
    "ds_read_b128 v[96:99], %[wl] offset:10240\n" \
    "ds_read_b128 v[100:103], %[wl] offset:11264\n" \
    "s_waitcnt vmcnt(9)\n" \
    "v_smfmac_f32_16x16x64_bf16 v[0:3], v[28:31], a[24:31], %[idx]\n" \
    "v_max3_u32 v104, v104, v28, v29\n" \
    "v_smfmac_f32_16x16x64_bf16 v[4:7], v[28:31], a[152:159], %[idx]\n" \
    "v_max3_u32 v104, v104, v30, v31\n" \
    "v_smfmac_f32_16x16x64_bf16 v[8:11], v[28:31], v[152:159], %[idx]\n" \
    "s_waitcnt lgkmcnt(4)\n" \
    "v_smfmac_f32_16x16x64_bf16 v[12:15], v[28:31], v[80:87], %[idx]\n" \
    "buffer_load_dwordx4 v[68:71], %[vo], %[rw], s46 offen offset:1024 sc1\n" \
    "ds_read_b128 v[80:83], %[wl] offset:12288\n" \
    "ds_read_b128 v[84:87], %[wl] offset:13312\n" \
    "s_waitcnt vmcnt(9)\n" \
    "v_smfmac_f32_16x16x64_bf16 v[0:3], v[32:35], a[32:39], %[idx]\n" \
    "v_max3_u32 v104, v104, v32, v33\n" \
    "v_smfmac_f32_16x16x64_bf16 v[4:7], v[32:35], a[160:167], %[idx]\n" \
    "v_max3_u32 v104, v104, v34, v35\n" \
    "v_smfmac_f32_16x16x64_bf16 v[8:11], v[32:35], v[160:167], %[idx]\n" \
    "s_waitcnt lgkmcnt(4)\n" \
    "v_smfmac_f32_16x16x64_bf16 v[12:15], v[32:35], v[88:95], %[idx]\n" \
    "buffer_load_dwordx4 v[72:75], %[vo], %[rw], s46 offen offset:2048 sc1\n" \
    "ds_read_b128 v[88:91], %[wl] offset:14336\n" \
    "ds_read_b128 v[92:95], %[wl] offset:15360\n" \
    "s_waitcnt vmcnt(9)\n" \
    "v_smfmac_f32_16x16x64_bf16 v[0:3], v[36:39], a[40:47], %[idx]\n" \
    "v_max3_u32 v104, v104, v36, v37\n" \
    "v_smfmac_f32_16x16x64_bf16 v[4:7], v[36:39], a[168:175], %[idx]\n" \
    "v_max3_u32 v104, v104, v38, v39\n" \
    "v_smfmac_f32_16x16x64_bf16 v[8:11], v[36:39], v[168:175], %[idx]\n" \
    "s_waitcnt lgkmcnt(4)\n" \
    "v_smfmac_f32_16x16x64_bf16 v[12:15], v[36:39], v[96:103], %[idx]\n" \
    "buffer_load_dwordx4 v[76:79], %[vo], %[rw], s46 offen offset:3072 sc1\n" \
    "ds_read_b128 v[96:99], %[wl] offset:16384\n" \
    "ds_read_b128 v[100:103], %[wl] offset:17408\n" \
    "s_waitcnt vmcnt(9)\n" \
    "v_smfmac_f32_16x16x64_bf16 v[0:3], v[40:43], a[48:55], %[idx]\n" \
    "v_max3_u32 v104, v104, v40, v41\n" \
    "v_smfmac_f32_16x16x64_bf16 v[4:7], v[40:43], a[176:183], %[idx]\n" \
    "v_max3_u32 v104, v104, v42, v43\n" \
    "v_smfmac_f32_16x16x64_bf16 v[8:11], v[40:43], v[176:183], %[idx]\n" \
    "s_waitcnt lgkmcnt(4)\n" \
    "v_smfmac_f32_16x16x64_bf16 v[12:15], v[40:43], v[80:87], %[idx]\n" \
    "buffer_load_dwordx2 v[106:107], %[bv], %[rw], %[bs] offen sc1\n" \
    "ds_read_b128 v[80:83], %[wl] offset:18432\n" \
    "ds_read_b128 v[84:87], %[wl] offset:19456\n" \
    "s_waitcnt vmcnt(9)\n" \
    "v_smfmac_f32_16x16x64_bf16 v[0:3], v[44:47], a[56:63], %[idx]\n" \
    "v_max3_u32 v104, v104, v44, v45\n" \
    "v_smfmac_f32_16x16x64_bf16 v[4:7], v[44:47], a[184:191], %[idx]\n" \
    "v_max3_u32 v104, v104, v46, v47\n" \
    "v_smfmac_f32_16x16x64_bf16 v[8:11], v[44:47], v[184:191], %[idx]\n" \
    "s_waitcnt lgkmcnt(4)\n" \
    "v_smfmac_f32_16x16x64_bf16 v[12:15], v[44:47], v[88:95], %[idx]\n" \
    "buffer_load_dword v108, %[gv], %[rw], %[gs] offen sc1\n" \
    "ds_read_b128 v[88:91], %[wl] offset:20480\n" \
    "ds_read_b128 v[92:95], %[wl] offset:21504\n" \
    "s_waitcnt vmcnt(9)\n" \
    "v_smfmac_f32_16x16x64_bf16 v[0:3], v[48:51], a[64:71], %[idx]\n" \
    "v_max3_u32 v104, v104, v48, v49\n" \
    "v_smfmac_f32_16x16x64_bf16 v[4:7], v[48:51], a[192:199], %[idx]\n" \
    "v_max3_u32 v104, v104, v50, v51\n" \
    "v_smfmac_f32_16x16x64_bf16 v[8:11], v[48:51], v[192:199], %[idx]\n" \
    "s_waitcnt lgkmcnt(4)\n" \
    "v_smfmac_f32_16x16x64_bf16 v[12:15], v[48:51], v[96:103], %[idx]\n" \
    "buffer_load_dword v105, %[sv], %[rw], %[ss] offen sc1\n" \
    "ds_read_b128 v[96:99], %[wl] offset:22528\n" \
    "ds_read_b128 v[100:103], %[wl] offset:23552\n" \
    "s_waitcnt vmcnt(9)\n" \
    "v_smfmac_f32_16x16x64_bf16 v[0:3], v[52:55], a[72:79], %[idx]\n" \
    "v_max3_u32 v104, v104, v52, v53\n" \
    "v_smfmac_f32_16x16x64_bf16 v[4:7], v[52:55], a[200:207], %[idx]\n" \
    "v_max3_u32 v104, v104, v54, v55\n" \
    "v_smfmac_f32_16x16x64_bf16 v[8:11], v[52:55], v[200:207], %[idx]\n" \
    "s_waitcnt lgkmcnt(4)\n" \
    "v_smfmac_f32_16x16x64_bf16 v[12:15], v[52:55], v[80:87], %[idx]\n" \
    "ds_read_b128 v[80:83], %[wl] offset:24576\n" \
    "ds_read_b128 v[84:87], %[wl] offset:25600\n" \
    "s_waitcnt vmcnt(8)\n" \
    "v_smfmac_f32_16x16x64_bf16 v[0:3], v[56:59], a[80:87], %[idx]\n" \
    "v_max3_u32 v104, v104, v56, v57\n" \
    "v_smfmac_f32_16x16x64_bf16 v[4:7], v[56:59], a[208:215], %[idx]\n" \
    "v_max3_u32 v104, v104, v58, v59\n" \
    "v_smfmac_f32_16x16x64_bf16 v[8:11], v[56:59], v[208:215], %[idx]\n" \
    "s_waitcnt lgkmcnt(4)\n" \
    "v_smfmac_f32_16x16x64_bf16 v[12:15], v[56:59], v[88:95], %[idx]\n" \
    "ds_read_b128 v[88:91], %[wl] offset:26624\n" \
    "ds_read_b128 v[92:95], %[wl] offset:27648\n" \
    "s_waitcnt vmcnt(7)\n" \
    "v_smfmac_f32_16x16x64_bf16 v[0:3], v[60:63], a[88:95], %[idx]\n" \
    "v_max3_u32 v104, v104, v60, v61\n" \
    "v_smfmac_f32_16x16x64_bf16 v[4:7], v[60:63], a[216:223], %[idx]\n" \
    "v_max3_u32 v104, v104, v62, v63\n" \
    "v_smfmac_f32_16x16x64_bf16 v[8:11], v[60:63], v[216:223], %[idx]\n" \
    "s_waitcnt lgkmcnt(4)\n" \
    "v_smfmac_f32_16x16x64_bf16 v[12:15], v[60:63], v[96:103], %[idx]\n" \
    "ds_read_b128 v[96:99], %[wl] offset:28672\n" \
    "ds_read_b128 v[100:103], %[wl] offset:29696\n" \
    "s_waitcnt vmcnt(6)\n" \
    "v_smfmac_f32_16x16x64_bf16 v[0:3], v[64:67], a[96:103], %[idx]\n" \
    "v_max3_u32 v104, v104, v64, v65\n" \
    "v_smfmac_f32_16x16x64_bf16 v[4:7], v[64:67], a[224:231], %[idx]\n" \
    "v_max3_u32 v104, v104, v66, v67\n" \
    "v_smfmac_f32_16x16x64_bf16 v[8:11], v[64:67], v[224:231], %[idx]\n" \
    "s_waitcnt lgkmcnt(4)\n" \
    "v_smfmac_f32_16x16x64_bf16 v[12:15], v[64:67], v[80:87], %[idx]\n" \
    "ds_read_b128 v[80:83], %[wl] offset:30720\n" \
    "ds_read_b128 v[84:87], %[wl] offset:31744\n" \
    "s_waitcnt vmcnt(5)\n" \
    "v_smfmac_f32_16x16x64_bf16 v[0:3], v[68:71], a[104:111], %[idx]\n" \
    "v_max3_u32 v104, v104, v68, v69\n" \
    "v_smfmac_f32_16x16x64_bf16 v[4:7], v[68:71], a[232:239], %[idx]\n" \
    "v_max3_u32 v104, v104, v70, v71\n" \
    "v_smfmac_f32_16x16x64_bf16 v[8:11], v[68:71], v[232:239], %[idx]\n" \
    "s_waitcnt lgkmcnt(4)\n" \
    "v_smfmac_f32_16x16x64_bf16 v[12:15], v[68:71], v[88:95], %[idx]\n" \
    "s_waitcnt vmcnt(4)\n" \
    "v_smfmac_f32_16x16x64_bf16 v[0:3], v[72:75], a[112:119], %[idx]\n" \
    "v_max3_u32 v104, v104, v72, v73\n" \
    "v_smfmac_f32_16x16x64_bf16 v[4:7], v[72:75], a[240:247], %[idx]\n" \
    "v_max3_u32 v104, v104, v74, v75\n" \
    "v_smfmac_f32_16x16x64_bf16 v[8:11], v[72:75], v[240:247], %[idx]\n" \
    "s_waitcnt lgkmcnt(2)\n" \
    "v_smfmac_f32_16x16x64_bf16 v[12:15], v[72:75], v[96:103], %[idx]\n" \
    "s_waitcnt vmcnt(3)\n" \
    "v_smfmac_f32_16x16x64_bf16 v[0:3], v[76:79], a[120:127], %[idx]\n" \
    "v_max3_u32 v104, v104, v76, v77\n" \
    "v_smfmac_f32_16x16x64_bf16 v[4:7], v[76:79], a[248:255], %[idx]\n" \
    "v_max3_u32 v104, v104, v78, v79\n" \
    "v_smfmac_f32_16x16x64_bf16 v[8:11], v[76:79], v[248:255], %[idx]\n" \
    "s_waitcnt lgkmcnt(0)\n" \
    "v_smfmac_f32_16x16x64_bf16 v[12:15], v[76:79], v[80:87], %[idx]\n" \
    "s_waitcnt vmcnt(0)\n" \
    "s_nop 5\n" \
    "v_permlane32_swap_b32 v0, v8\n" \
    "v_add_f32 v0, v0, v8\n" \
    "v_permlane32_swap_b32 v1, v9\n" \
    "v_add_f32 v1, v1, v9\n" \
    "v_permlane32_swap_b32 v2, v10\n" \
    "v_add_f32 v2, v2, v10\n" \
    "v_permlane32_swap_b32 v3, v11\n" \
    "v_add_f32 v3, v3, v11\n" \
    "v_permlane32_swap_b32 v4, v12\n" \
    "v_add_f32 v4, v4, v12\n" \
    "v_permlane32_swap_b32 v5, v13\n" \
    "v_add_f32 v5, v5, v13\n" \
    "v_permlane32_swap_b32 v6, v14\n" \
    "v_add_f32 v6, v6, v14\n" \
    "v_permlane32_swap_b32 v7, v15\n" \
    "v_add_f32 v7, v7, v15\n" \
    ""
#define OUT_CLOBBER "v16", "v17", "v18", "v19", "v20", "v21", "v22", "v23", "v24", "v25", "v26", "v27", "v28", "v29", "v30", "v31", "v32", "v33", "v34", "v35", "v36", "v37", "v38", "v39", "v40", "v41", "v42", "v43", "v44", "v45", "v46", "v47", "v48", "v49", "v50", "v51", "v52", "v53", "v54", "v55", "v56", "v57", "v58", "v59", "v60", "v61", "v62", "v63", "v64", "v65", "v66", "v67", "v68", "v69", "v70", "v71", "v72", "v73", "v74", "v75", "v76", "v77", "v78", "v79", "v80", "v81", "v82", "v83", "v84", "v85", "v86", "v87", "v88", "v89", "v90", "v91", "v92", "v93", "v94", "v95", "v96", "v97", "v98", "v99", "v100", "v101", "v102", "v103", "s44", "s45", "s46", "scc", "memory"
constexpr int NREP = 2;
#define OWNER_ASM \
    "s_getpc_b64 s[96:97]\n" \
    "14:\n" \
    "s_add_u32 s96, s96, 2f-14b\n" \
    "s_addc_u32 s97, s97, 0\n" \
    "s_mov_b32 %[np], 0\n" \
    "6:\n" \
    "buffer_load_dwordx2 v[0:1], %[ca], %[rs], %[cs] offen sc1\n" \
    "s_memtime s[58:59]\n" \
    "s_memrealtime s[44:45]\n" \
    "s_nop 1\n" \
    "s_memrealtime s[46:47]\n" \
    "s_nop 1\n" \
    "s_memrealtime s[48:49]\n" \
    "s_nop 1\n" \
    "s_memrealtime s[50:51]\n" \
    "s_nop 1\n" \
    "s_memrealtime s[52:53]\n" \
    "s_nop 1\n" \
    "s_memrealtime s[54:55]\n" \
    "s_nop 1\n" \
    "s_memrealtime s[56:57]\n" \
    "s_nop 1\n" \
    "s_memrealtime vcc\n" \
    "s_waitcnt lgkmcnt(0)\n" \
    "s_add_i32 s44, s44, s46\n" \
    "s_add_i32 s48, s48, s50\n" \
    "s_add_i32 s52, s52, s54\n" \
    "s_add_i32 s44, s44, s48\n" \
    "s_add_i32 s52, s52, s56\n" \
    "s_add_i32 s44, s44, s52\n" \
    "s_add_i32 s59, s44, vcc_lo\n" \
    "s_sub_i32 s44, s59, %[oT]\n" \
    "s_mov_b32 %[oT], s59\n" \
    "s_sub_i32 s45, s58, %[oC]\n" \
    "s_mov_b32 %[oC], s58\n" \
    "s_mul_i32 s46, s44, %[oR]\n" \
    "s_lshr_b32 s46, s46, 12\n" \
    "s_sub_i32 s46, s45, s46\n" \
    "s_cmp_lt_u32 s44, 0x1000\n" \
    "s_cselect_b32 s46, s46, 0\n" \
    "s_add_i32 %[oR], %[oR], s46\n" \
    "s_waitcnt vmcnt(0)\n" \
    "v_subrev_u32 v0, %[oT], v0\n" \
    "v_cmp_ne_u32 vcc, %[tag], v1\n" \
    "v_min_i32 v0, 0x4000, v0\n" \
    "v_max_i32 v0, 0xffffc000, v0\n" \
    "s_nop 0\n" \
    "s_cbranch_vccnz 7f\n" \
    "8:\n" \
    "v_max_i32_dpp v0, v0, v0 row_shr:1 row_mask:0xf bank_mask:0xf\n" \
    "s_nop 1\n" \
    "v_max_i32_dpp v0, v0, v0 row_shr:2 row_mask:0xf bank_mask:0xf\n" \
    "s_nop 1\n" \
    "v_max_i32_dpp v0, v0, v0 row_shr:4 row_mask:0xf bank_mask:0xf\n" \
    "s_nop 1\n" \
    "v_max_i32_dpp v0, v0, v0 row_shr:8 row_mask:0xf bank_mask:0xf\n" \
    "s_nop 1\n" \
    "v_max_i32_dpp v0, v0, v0 row_bcast:15 row_mask:0xa bank_mask:0xf\n" \
    "s_nop 1\n" \
    "v_max_i32_dpp v0, v0, v0 row_bcast:31 row_mask:0xc bank_mask:0xf\n" \
    "s_nop 1\n" \
    "v_readlane_b32 s44, v0, 63\n" \
    "s_add_i32 s45, %[oC], -116\n" \
    "s_add_i32 %[tm], s44, %[oT]\n" \
    "s_add_i32 s44, s44, %[c8]\n" \
    "s_mul_i32 s44, s44, %[oR]\n" \
    "s_ashr_i32 s44, s44, 12\n" \
    "s_add_i32 s44, s44, s45\n" \
    "1:\n" \
    "s_memtime vcc\n" \
    "s_waitcnt lgkmcnt(0)\n" \
    "s_sub_i32 s58, s44, vcc_lo\n" \
    "s_cmp_gt_i32 s58, 112\n" \
    "s_cbranch_scc1 1b\n" \
    "s_max_i32 s58, s58, 0\n" \
    "s_and_b32 s58, s58, -4\n" \
    "s_sub_u32 vcc_lo, s96, s58\n" \
    "s_subb_u32 vcc_hi, s97, 0\n" \
    "s_setpc_b64 vcc\n" \
    "s_nop 0\n" \
    "s_nop 0\n" \
    "s_nop 0\n" \
    "s_nop 0\n" \
    "s_nop 0\n" \
    "s_nop 0\n" \
    "s_nop 0\n" \
    "s_nop 0\n" \
    "s_nop 0\n" \
    "s_nop 0\n" \
    "s_nop 0\n" \
    "s_nop 0\n" \
    "s_nop 0\n" \
    "s_nop 0\n" \
    "s_nop 0\n" \
    "s_nop 0\n" \
    "s_nop 0\n" \
    "s_nop 0\n" \
    "s_nop 0\n" \
    "s_nop 0\n" \
    "s_nop 0\n" \
    "s_nop 0\n" \
    "s_nop 0\n" \
    "s_nop 0\n" \
    "s_nop 0\n" \
    "s_nop 0\n" \
    "s_nop 0\n" \
    "s_nop 0\n" \
    "2:\n" \
    "buffer_load_dwordx4 v[0:3], %[o], %[rs], %[pb] offen offset:0 sc1\n" \
    "buffer_load_dwordx4 v[4:7], %[o], %[rs], %[pb] offen offset:1024 sc1\n" \
    "buffer_load_dwordx4 v[8:11], %[o], %[rs], %[pb] offen offset:2048 sc1\n" \
    "buffer_load_dwordx4 v[12:15], %[o], %[rs], %[pb] offen offset:3072 sc1\n" \
    "s_add_i32 %[np], %[np], 1\n" \
    "s_waitcnt vmcnt(3)\n" \
    "v_mfma_f32_16x16x32_bf16 v[16:19], %[sel], v[0:3], 0\n" \
    "v_max3_u32 v20, v0, v1, v1\n" \
    "v_max3_u32 v20, v2, v3, v20\n" \
    "s_waitcnt vmcnt(2)\n" \
    "v_mfma_f32_16x16x32_bf16 v[16:19], %[sel], v[4:7], v[16:19]\n" \
    "v_max3_u32 v20, v4, v5, v20\n" \
    "v_max3_u32 v20, v6, v7, v20\n" \
    "s_waitcnt vmcnt(1)\n" \
    "v_mfma_f32_16x16x32_bf16 v[16:19], %[sel], v[8:11], v[16:19]\n" \
    "v_max3_u32 v20, v8, v9, v20\n" \
    "v_max3_u32 v20, v10, v11, v20\n" \
    "s_waitcnt vmcnt(0)\n" \
    "v_mfma_f32_16x16x32_bf16 v[16:19], %[sel], v[12:15], v[16:19]\n" \
    "v_max3_u32 v20, v12, v13, v20\n" \
    "v_max3_u32 v20, v14, v15, v20\n" \
    "v_cmp_eq_u32 vcc, -1, v20\n" \
    "s_nop 3\n" \
    "s_cbranch_vccnz 4f\n" \
    "3:\n" \
    "v_add_f32_dpp v16, v16, v16 row_ror:8 row_mask:0xf bank_mask:0xf\n" \
    "v_add_f32_dpp v17, v17, v17 row_ror:8 row_mask:0xf bank_mask:0xf\n" \
    "v_cvt_pk_bf16_f32 v16, v16, v17\n" \
    "buffer_store_dword v16, %[so], %[rs], %[mb0] offen sc1\n" \
    "s_branch 5f\n" \
    "4:\n" \
    "s_add_i32 %[bud], %[bud], -1\n" \
    "s_cmp_lt_i32 %[bud], 0\n" \
    "s_cbranch_scc0 2b\n" \
    "s_branch 3b\n" \
    "7:\n" \
    "s_cmp_eq_u32 %[tag], 1\n" \
    "s_cbranch_scc1 9f\n" \
    "s_sleep 15\n" \
    "9:\n" \
    "s_sleep 6\n" \
    "s_add_i32 %[bud], %[bud], -1\n" \
    "s_cmp_lt_i32 %[bud], 0\n" \
    "s_cbranch_scc0 6b\n" \
    "s_branch 8b\n" \
    "5:\n" \
    ""
#define OWNER_CLOBBER "v0", "v1", "v2", "v3", "v4", "v5", "v6", "v7", "v8", "v9", "v10", "v11", "v12", "v13", "v14", "v15", "v16", "v17", "v18", "v19", "v20", "s44", "s45", "s46", "s47", "s48", "s49", "s50", "s51", "s52", "s53", "s54", "s55", "s56", "s57", "s58", "s59", "s96", "s97", "vcc", "scc", "memory"
constexpr int O_W = 0;
constexpr int O_STG = 131072;
constexpr int O_B = O_STG + 20480;
constexpr int OUT_REP = 7;
#define OUT_AUX 16
DI int opaque_lane() { int l; asm volatile("v_mbcnt_lo_u32_b32 %0, -1, 0\n\tv_mbcnt_hi_u32_b32 %0, -1, %0" : "=v"(l)); return l; }
template <int O, class V> DI void put4(V& d, u32x4 f) {
#pragma unroll
    for (int j = 0; j < 4; ++j) d[O + j] = f[j];
}
DI void role_outp(const Args& a, LAS char* lds, unsigned lds_base, int nb, int rep, rsrc_t rw, int& budget) {
    const int tid = threadIdx.x, lane = tid & 63, w = __builtin_amdgcn_readfirstlane(tid >> 6);
    WG_BARRIER();
    const int n = lane & 15, g = lane >> 4;
    const int wl = O_W + w * 32768 + lane * 16;
    u32x32 A[8], V[4];
    {
        const int stg = O_STG + w * 5120;
        const rsrc_t ro = __builtin_amdgcn_make_buffer_rsrc((void*)a.Wo, 0, HD * DM * 2, 0x00020000);
        const int so = (1024 * w + (lane >> 3)) * (DM * 2) + nb * 128 + (lane & 7) * 16, sw = stg + (lane >> 3) * 160 + (lane & 7) * 16;
        const int ra = stg + (16 * (g & 1) + 4 * (g >> 1) + (n >> 2)) * 160 + (n & 3) * 8;
        constexpr int PB = 4;
        u32x4 b[PB][4];
        auto fetch = [&](int ks) {
#pragma unroll
            for (int i = 0; i < 4; ++i) b[ks % PB][i] = __builtin_amdgcn_raw_buffer_load_b128(ro, so, (64 * (ks >> 1) + 16 * (ks & 1) + 8 * (i & 1) + 32 * (i >> 1)) * (DM * 2), 0);
        };
#pragma unroll
        for (int ks = 0; ks < PB; ++ks) fetch(ks);
        FENCE();
        static_for<32>([&](auto ic) {
            constexpr int ks = decltype(ic)::value;
#pragma unroll
            for (int i = 0; i < 4; ++i) *(LAS u32x4*)(lds + sw + i * 1280) = b[ks % PB][i];
            if (ks + PB < 32) fetch(ks + PB);
            u32x4 f[4];
#pragma unroll
            for (int t = 0; t < 4; ++t) { const u32x2 lo = tr_read(lds, ra + t * 32), hi = tr_read(lds, ra + t * 32 + 1280); f[t] = (u32x4){lo.x, lo.y, hi.x, hi.y}; }
            put4<4 * (ks & 7)>(A[ks >> 3], f[0]); put4<4 * (ks & 7)>(A[4 + (ks >> 3)], f[1]); put4<4 * (ks & 7)>(V[ks >> 3], f[2]);
            *(LAS u32x4*)(lds + wl + ks * 1024) = f[3];
            if constexpr (ks == 7) asm volatile("" : "+{a[0:31]}"(A[0]), "+{a[128:159]}"(A[4]), "+{v[128:159]}"(V[0]));
            if constexpr (ks == 15) asm volatile("" : "+{a[32:63]}"(A[1]), "+{a[160:191]}"(A[5]), "+{v[160:191]}"(V[1]));
            if constexpr (ks == 23) asm volatile("" : "+{a[64:95]}"(A[2]), "+{a[192:223]}"(A[6]), "+{v[192:223]}"(V[2]));
            if constexpr (ks == 31) asm volatile("" : "+{a[96:127]}"(A[3]), "+{a[224:255]}"(A[7]), "+{v[224:255]}"(V[3]));
            FENCE();
        });
    }
    wait_prog(rw, F_S4 + lane * 4, true, 1, budget);
    const rsrc_t ru = __builtin_amdgcn_make_buffer_rsrc(a.out, 0, SEQ * DM * 2, 0x00020000);
    constexpr int SP = 2 * NSEG - 2;
    auto first = [&](int u) { return u >= SP ? 8 * u + 8 * SP : 16 * u; };
    auto st_s = [&](int u) { return (u >= 2 * FAST ? F_H + ((first(u) >> 4) - 2 * FAST) * 4096 : F_O + (u & 3) * 4) + w * 1024; };
    auto bn_s = [&](int u) { return OFF_BO + (64 * nb + 16 * w) * BO_ROW + first(u) * 2; };
    auto gn_s = [&](int u) { return OFF_GT + (first(u) + 4 * w) * 4; };
    int st = -1;
    u32x2 bn = ld64(rw, (lane >> 2) * BO_ROW + (lane & 3) * 8, bn_s(rep));
    float gn = __uint_as_float(ld32(rw, g * 4, gn_s(rep)));
    __builtin_amdgcn_s_waitcnt(0);
#define OUT_OPS "=&{v[0:15]}"(acc), "=&{v104}"(top), "=&{v105}"(st), "=&{v[106:107]}"(bn), "=&{v108}"(gn) \
        : "{a[0:31]}"(A[0]), "{a[32:63]}"(A[1]), "{a[64:95]}"(A[2]), "{a[96:127]}"(A[3]), "{a[128:159]}"(A[4]), "{a[160:191]}"(A[5]), "{a[192:223]}"(A[6]), "{a[224:255]}"(A[7]), \
          "{v[128:159]}"(V[0]), "{v[160:191]}"(V[1]), "{v[192:223]}"(V[2]), "{v[224:255]}"(V[3]), [rw] "s"(rw), [so] "s"(t0 * 8192), [wl] "v"(lds_base + O_W + w * 32768 + lane * 16), \
          [tv] "v"(tv), [tm] "s"(tm), [vo] "v"(vo), [sv] "v"(sv), [bv] "v"((lane >> 2) * BO_ROW + (lane & 3) * 8), [gv] "v"(g * 4), \
          [ss] "s"(st_s(u + OUT_REP)), [bs] "s"(bn_s(u + OUT_REP)), [gs] "s"(gn_s(u + OUT_REP))
    for (int u = rep; u < SP + 4; u += OUT_REP) {
        const bool fast = u >= 2 * FAST, sp = u >= SP;
        const int t0 = first(u);
        WG_BARRIER();
        int lane = opaque_lane(), n = lane & 15, g = lane >> 4;
        if (u == 3 * OUT_REP && nb == 0) __builtin_amdgcn_raw_buffer_store_b64(launch_tag(a), rw, w + lane == 0 ? F_EPOCH : OOB, 0, 16);
        S4S(0)
        TT(u >= 123, 0)
        *(LAS u32x2*)(lds + O_B + (16 * w + (lane >> 2)) * 40 + (lane & 3) * 8) = bn;
        const float gt = gn;
        const int tv = OFF_OF + w * 16384 + nb * 2048 + g * 65536 + n * 128, tm = __builtin_amdgcn_readfirstlane(nb < 8 ? sp ? 0xFFFF : -1 : 0);
        const int vo = OFF_OF + w * 16384 + (n & 7) * 16 + (sp ? (n >> 3) * 512 + g * 128 : (n >> 3) * 65536 + (g & 1) * 512 + (g >> 1) * 128);
        const int thr = fast ? 0 : u >> 1, sv = (lane & 7) * 128;
        if (!__all(st >= thr)) do { __builtin_amdgcn_s_sleep(1); st = (int)ld32(rw, sv, st_s(u)); asm volatile("" ::: "memory"); } while (!__all(st >= thr) && --budget >= 0);
        if (fast) { const unsigned t = now(); (void)wait_until(t + wave_max32(min((int)(2 * (st - t)) >> 1, 500))); }
        S4S(1)
        TT(u >= 123, 1)
        f32x16 acc; unsigned top;
        if (sp) {
            do asm volatile(OUT8_ASM : OUT_OPS, [idx] "v"(n & 8 ? 0xEEEE : 0x4444) : OUT_CLOBBER); while (__any(top == SENT) && --budget >= 0);
            TT(u >= 123, 2)
            lane = opaque_lane(); n = lane & 15; g = lane >> 4;
#pragma unroll
            for (int i = 0; i < 8; ++i) *(LAS float*)(lds + O_STG + (w * 16 + 4 * (g & 1) + (i & 3)) * 272 + (g >> 1) * 128 + (i >> 2) * 64 + n * 4) = acc[i];
        } else {
            do asm volatile(OUT16_ASM : OUT_OPS : OUT_CLOBBER); while (__any(top == SENT) && --budget >= 0);
            S4S(2)
            TT(u >= 123, 2)
            lane = opaque_lane(); n = lane & 15; g = lane >> 4;
#pragma unroll
            for (int t = 0; t < 4; ++t) *(LAS f32x4*)(lds + O_STG + (w * 16 + n) * 272 + t * 64 + g * 16) = (f32x4){acc[4 * t], acc[4 * t + 1], acc[4 * t + 2], acc[4 * t + 3]};
        }
        WG_BARRIER();
        S4S(3)
        const int tok = 4 * w + g, c4 = 4 * n;
        f32x4 r[4]; u16 bv[4];
#pragma unroll
        for (int ww = 0; ww < 4; ++ww) { r[ww] = *(LAS f32x4*)(lds + O_STG + (ww * 16 + tok) * 272 + c4 * 4); bv[ww] = *(LAS u16*)(lds + O_B + (c4 + ww) * 40 + tok * 2); }
        FENCE();
        const f32x4 v = gt * ((r[0] + r[1]) + (r[2] + r[3])) + (1.0f - gt) * (f32x4){bf2f(bv[0]), bf2f(bv[1]), bf2f(bv[2]), bf2f(bv[3])};
        __builtin_amdgcn_raw_buffer_store_b64((u32x2){pack2(v[0], v[1]), pack2(v[2], v[3])}, ru, sp && tok >= 8 ? OOB : (tok * DM + 64 * nb + c4) * 2, t0 * (DM * 2), OUT_AUX);
        S4S(4)
        TT(u >= 123, 3)
        TT(u >= 109 && u < 116, 4)
    }
}

constexpr int L_MEMS = 0;
constexpr int L_Q = 9216;
constexpr int L_KO = L_Q + 2560;
constexpr int L_KX = 16384;
constexpr int L_W = 32768;
constexpr int C_RING = 8;
static_assert(S_TOTAL <= L_SINK && L_W + C_RING * 16384 <= L_TOTAL && L_KO + 8 * 576 <= L_KX, "LDS budget");
static_assert(P_STG + P_RING * 16384 <= L_TOTAL, "LDS budget");

DI int pos2col(int p) { return p < 512 ? (p & ~63) + 16 * (p & 3) + ((p & 63) >> 2) : p; }
DI void unit_rc(int u, int& i, int& p0) {
    if (u < 512) { i = 2 * ((u >> 2) & 3) + (u & 1); p0 = (u & ~15) + 4 * (u & 2); }
    else { const int v = u - 512; i = (v >> 1) & 7; p0 = 512 + 16 * (v >> 4) + 8 * (v & 1); }
}
DI void mem0_arrange(LAS char* lds, int raw, int tid) {
#pragma unroll
    for (int k = 0; k < 2; ++k) {
        int i, p0; unit_rc(tid + 256 * k, i, p0);
        unsigned r[4];
#pragma unroll
        for (int j = 0; j < 4; ++j) r[j] = *(LAS unsigned*)(lds + raw + (i * DM + pos2col(p0)) * 2 + 32 * j);
        *(LAS u32x4*)(lds + L_MEMS + (tid + 256 * k) * 16) = (u32x4){__builtin_amdgcn_perm(r[1], r[0], 0x05040100), __builtin_amdgcn_perm(r[3], r[2], 0x05040100),
                                                                     __builtin_amdgcn_perm(r[1], r[0], 0x07060302), __builtin_amdgcn_perm(r[3], r[2], 0x07060302)};
    }
    int i, p0; unit_rc(512 + (tid & 63), i, p0);
    *(LAS u32x4*)(lds + L_MEMS + 8192 + (tid & 63) * 16) = *(LAS u32x4*)(lds + raw + (i * DM + p0) * 2);
}
DI unsigned read_canaries(int s, rsrc_t rs, int lane, int& budget) {
    const int o = 2 * OFF_CAN + (s & 3) * 16384 + (lane >> 2) * 128 + (lane & 3) * 16;
    u32x2 v;
    for (;;) {
        v = ld64(rs, o, 0);
        asm volatile("" ::: "memory");
        if (__all(v.y == (unsigned)(s + 1))) break;
        if (--budget < 0) break;
    }
    const unsigned t = 8 * now();
    return t + (unsigned)wave_max32(min(max((int)(v.x - t), -0x4000), 0x4000));
}
DI u32x8 sm_frag(LAS char* lds, int sl, int r0, int sa, int sH, int cb, int lo8) {
    auto rd = [&](int r) { return tr_read(lds, sl + r * 256 + ((cb ^ (2 * swz(r))) * 16) + lo8); };
    const u32x2 a = rd(r0), b = rd(r0 + sa), c = rd(r0 + sH), d = rd(r0 + sa + sH);
    return (u32x8){a.x, a.y, b.x, b.y, c.x, c.y, d.x, d.y};
}
#define GS(slot, i)
#define GV(slot, i, v)
DI void owner_loop(const Args& a, int G, rsrc_t rs, unsigned tbase, unsigned cbase, int& budget) {
    const int lane = threadIdx.x & 63, n = lane & 15, g = lane >> 4;
    const bool ex = G >= 64;
    const u32x4 sent4 = {SENT, SENT, SENT, SENT};
    const int js = (n & 2) ? 8 : ex ? (n & 4) + 2 * (n & 1) + (n >> 3) : 2 * (n >> 2) + (n & 1);
    u32x4 selu;
#pragma unroll
    for (int j = 0; j < 4; ++j) selu[j] = (js == 2 * j ? 0x3F80u : 0u) | (js == 2 * j + 1 ? 0x3F800000u : 0u);
    const int o = cmap(G * 4096 + lane * 16);
    const int so = (n < 8 || NREP > 1) ? cmap((n >> 3) * 16384 + G * 128 + (g & 1) * 4 + (ex ? (n & 4) * 16 + (g >> 1) * 32 + (n & 3) * 8 : (n & 6) * 16 + (g >> 1) * 16 + (n & 1) * 8)) : OOB;
    unsigned fails = 0;
    const int c8 = G >= 64 ? a.c1 >> 16 : G < 8 ? a.c1 >> 8 & 255 : a.c1 & 255;
    const bool gv_on = true; (void)gv_on;
    unsigned oT = now(), oC = (unsigned)__builtin_readcyclecounter(), oR;
    { const unsigned r = (unsigned)(512.0f * (float)(oC - cbase) * __builtin_amdgcn_rcpf((float)(oT - tbase))); asm volatile("s_nop 0\n\tv_readfirstlane_b32 %0, %1" : "=s"(oR) : "v"(r)); }
    oT = 8 * oT + 16;
    budget = __builtin_amdgcn_readfirstlane(budget);
    const int ca = 2 * OFF_CAN + (lane >> 2) * 128 + (lane & 3) * 16;
    for (int s = 0; s < NSEG - 1; ++s) {
        const int pb = 2 * (OFF_PART + (s & 1) * PART_BYTES);
        int np; unsigned tc;
        asm volatile(OWNER_ASM : [np] "=&s"(np), [tm] "=&s"(tc), [bud] "+s"(budget), [oT] "+s"(oT), [oC] "+s"(oC), [oR] "+s"(oR)
            : [c8] "s"(c8), [ca] "v"(ca), [cs] "s"((s & 3) * 16384), [tag] "s"(s + 1), [o] "v"(o), [so] "v"(so), [sel] "v"(selu), [rs] "s"(rs), [pb] "s"(pb), [mb0] "s"(2 * (OFF_MEMB + ((s + 1) & 3) * MEMB_STRIDE)),
              [mb1] "s"(2 * (OFF_MEMB + ((s + 1) & 3) * MEMB_STRIDE + 32768)) : OWNER_CLOBBER);
        (void)wait_until(now() + a.rd);
#pragma unroll
        for (int r = 0; r < 4; ++r) st128(sent4, rs, o + r * 1024, pb);
        if (s > 1) { st32(SENT, rs, so, 2 * (OFF_MEMB + ((s - 1) & 3) * MEMB_STRIDE)); if (NREP > 2) st32(SENT, rs, so, 2 * (OFF_MEMB + ((s - 1) & 3) * MEMB_STRIDE + 32768)); }
        fails += np > 1;
        { const unsigned t = now(); (void)wait_until(t + ((int)(tc - 8 * t + a.c2) >> 3) + a.cw); }
    }
}
DI void role_owner(const Args& a, unsigned lds_base, int ow, rsrc_t rw, int& budget) {
    const int tid = threadIdx.x, lane = tid & 63, w = __builtin_amdgcn_readfirstlane(tid >> 6);
    const unsigned tbase = now(), cbase = (unsigned)__builtin_readcyclecounter();
    if (ow != 63 && w) { prefetch(a, lds_base, ow & 3, (ow >> 2) * 3 + w - 1, (ow & 3) == 3 ? 45 : 48); VM_DRAIN(); }
    if (w >= 2 || (w == 1 && ow >= 8)) return;
    const u32x2 tag = launch_tag(a);
    if (ow == 63) {
        unsigned best[2] = {~0u, ~0u};
        for (int r = 0; r < 10; ++r) {
            unsigned long long t0, t1;
            asm volatile("s_memtime %0\n\ts_waitcnt lgkmcnt(0)" : "=s"(t0) :: "memory");
            st32(SENT, rw, lane == 0 ? (r & 1) * 8192 + (r >> 1) * 4096 * 5 / 4096 * 16384 + ((r >> 1) & 1) * 4096 : OOB, 0);
            asm volatile("s_waitcnt vmcnt(0)\n\ts_memtime %0\n\ts_waitcnt lgkmcnt(0)" : "=s"(t1) :: "memory");
            best[r & 1] = min(best[r & 1], (unsigned)(t1 - t0));
        }
        __builtin_amdgcn_raw_buffer_store_b64((u32x2){(tag.x & ~1u) | (best[1] < best[0]), tag.y}, rw, lane == 0 ? F_INIT + 32 * 128 : OOB, 0, 16);
    }
    const int sel = wait_init(rw, tag, budget, 32);
    const rsrc_t rs = __builtin_amdgcn_make_buffer_rsrc(a.ws + sel * 8192, 0, COMM_BYTES - 8192, 0x00020000);
    owner_loop(a, ow + 64 * w, rs, tbase, cbase, budget);
}

template <int O, class V> DI void put8(V& d, u32x8 f) {
#pragma unroll
    for (int j = 0; j < 8; ++j) d[O + j] = f[j];
}
DI f32x4 SMF64(u32x4 a, u32x8 b, f32x4 c, int i) { asm volatile("s_nop 1\n\tv_smfmac_f32_16x16x64_bf16 %0, %1, %2, %3\n\ts_nop 7" : "+v"(c) : "v"(a), "v"(b), "v"(i)); return c; }
DI f32x4 SMF32(u32x2 a, u32x4 b, f32x4 c, int i) { asm volatile("s_nop 1\n\tv_smfmac_f32_16x16x32_bf16 %0, %1, %2, %3\n\ts_nop 7" : "+v"(c) : "v"(a), "v"(b), "v"(i)); return c; }
DI f32x4 MFV(u32x4 a, u32x4 b, f32x4 c) { asm volatile("s_nop 1\n\tv_mfma_f32_16x16x32_bf16 %0, %1, %2, %0\n\ts_nop 7" : "+v"(c) : "v"(a), "v"(b)); return c; }

DI void role_head(const Args& a, LAS char* lds, unsigned lds_base, int h, int tw, rsrc_t rw, int& budget) {
    const int tid = threadIdx.x, w = __builtin_amdgcn_readfirstlane(tid >> 6);
    char* ws = a.ws;
    const unsigned tbase = now(), cbase = (unsigned)__builtin_readcyclecounter();
    const bool gv_on = tw == 0; (void)gv_on;
    u32x32 A0, A1, A2, A3, A4, A5, A6, A7, R0, R1, R2, R3; u32x8 R4;
    u32x2 tag;
    {
        const int lane = tid & 63, n = lane & 15, g = lane >> 4;
#pragma unroll
        for (int j = 0; j < 3; ++j) glds16(a.mem0, min(4 * j + w, 8) * 1024 + lane * 16, lds_base + L_KX + min(4 * j + w, 8) * 1024);
        const int tq = n >> 2, tc = (n & 3) >> 1, lo8 = (n & 1) * 8;
        const int r_nat = 16 * (g >> 1) + 4 * (g & 1) + (tq & 1) + 8 * (tq >> 1);
        const int r_sig = 16 * (tq & 1) + 4 * (g >> 1) + (g & 1) + 2 * (tq >> 1);
        const int r_o = 16 * (tq & 1) + 8 * (g >> 1) + 4 * (g & 1) + (tq >> 1);
#define WFRAG(sl, kb, t) ((kb) < 8 ? sm_frag(lds, sl, r_sig, 32, 8, 4 * w + 2 * (t) + tc, lo8) : sm_frag(lds, sl, r_nat, 2, 32, 4 * w + 2 * (t) + tc, lo8))
        const u32x4 ro = ring_offs(w, lane, HD);
        auto issue = [&](int i) {
            const unsigned slot = lds_base + L_W + (i % C_RING) * 16384 + w * 4096;
            if (i == 23) ring_issue(a.Wo + (size_t)h * DH * DM + 512, ring_offs(w, lane, [](int r, int c) { return (r + 64 * (c >> 3)) * (DM * 2) + (c & 7) * 16; }), slot);
            else if (i < 28) ring_issue((i < 8 || i == 20 ? a.Wq : i < 16 || i == 21 ? a.Wk : a.Wv) + (size_t)(64 * (i < 16 ? i & 7 : i < 20 ? i - 12 : i < 24 ? 8 : i - 24)) * HD + h * DH, ro, slot);
            else ring_issue(a.Wo + (size_t)(h * DH + 64 * ((i & 1) ^ ((i - 28) >> 1))) * DM + 256 * tw + 128 * (i & 1), ring_offs(w, lane, DM), slot);
        };
        auto keep = [&](int at, u32x8 f) {
            *(LAS u32x4*)(lds + at + lane * 16) = (u32x4){f[0], f[1], f[2], f[3]};
            *(LAS u32x4*)(lds + at + 1024 + lane * 16) = (u32x4){f[4], f[5], f[6], f[7]};
        };
        u32x8 hold[8] = {};
#pragma unroll
        for (int i = 0; i < C_RING; ++i) issue(i);
        tag = launch_tag(a);
        ring_wait<4 * C_RING>();
        mem0_arrange(lds, L_KX, tid);
        static_for<9>([&](auto ic) {
            constexpr int G = decltype(ic)::value;
            ring_wait<(G < 7 ? 16 : G == 7 ? 8 : 0)>();
            const int sl = L_W + (G & 1) * 65536;
            auto fr = [&](int j, int t) { return WFRAG(sl + ((j ^ w) << 14), 0, t); };
            if constexpr (G < 4) {
                u32x32& A = G == 0 ? A0 : G == 1 ? A2 : G == 2 ? A4 : A6;
                u32x32& B = G == 0 ? A1 : G == 1 ? A3 : G == 2 ? A5 : A7;
                put8<0>(A, fr(0, 0)); put8<8>(A, fr(0, 1)); put8<16>(A, fr(1, 0)); put8<24>(A, fr(1, 1));
                put8<0>(B, fr(2, 0)); put8<8>(B, fr(2, 1)); put8<16>(B, fr(3, 0)); put8<24>(B, fr(3, 1));
                if constexpr (G == 0) asm volatile("" : "+{a[0:31]}"(A0), "+{a[32:63]}"(A1));
                if constexpr (G == 1) asm volatile("" : "+{a[64:95]}"(A2), "+{a[96:127]}"(A3));
                if constexpr (G == 2) asm volatile("" : "+{a[128:159]}"(A4), "+{a[160:191]}"(A5));
                if constexpr (G == 3) asm volatile("" : "+{a[192:223]}"(A6), "+{a[224:255]}"(A7));
            } else if constexpr (G == 4) {
                put8<16>(R1, fr(0, 0)); put8<24>(R1, fr(0, 1)); put8<0>(R2, fr(1, 0)); put8<8>(R2, fr(1, 1));
                put8<16>(R2, fr(2, 0)); put8<24>(R2, fr(2, 1)); put8<0>(R3, fr(3, 0)); put8<8>(R3, fr(3, 1));
            } else if constexpr (G == 5) {
                put8<16>(R0, WFRAG(sl, 8, 0)); put8<24>(R0, WFRAG(sl, 8, 1)); put8<0>(R1, WFRAG(sl + 16384, 8, 0)); put8<8>(R1, WFRAG(sl + 16384, 8, 1));
                put8<16>(R3, WFRAG(sl + 32768, 8, 0)); put8<24>(R3, WFRAG(sl + 32768, 8, 1));
                hold[7] = sm_frag(lds, sl + 49152, r_o, 2, 32, 8 * (w >> 1) + 4 * tw + 2 * (w & 1) + tc, lo8);
            } else if constexpr (G == 6) {
#pragma unroll
                for (int j = 0; j < 7; ++j) hold[j] = fr(j >> 1, j & 1);
                R4 = fr(3, 1);
            } else {
                if constexpr (G == 8)
#pragma unroll
                    for (int T = 0; T < 4; ++T) keep(L_W + 65536 + w * 8192 + T * 2048, hold[T]);
#pragma unroll
                for (int T = 0; T < 4; ++T) hold[T] = sm_frag(lds, L_W + (2 * G - 10 + (w >> 1)) * 16384, r_o, 2, 32, 8 * (w & 1) + 2 * T + tc, lo8);
            }
            if constexpr (G != 7) WG_BARRIER();
            if constexpr (G < 6)
#pragma unroll
                for (int i = 0; i < 4; ++i) issue(4 * G + 8 + i);
            if constexpr (G == 6)
#pragma unroll
                for (int j = 0; j < 8; ++j) keep(L_W + (j >> 1) * 16384 + w * 4096 + (j & 1) * 2048, hold[j]);
            if constexpr (G == 8)
#pragma unroll
                for (int T = 0; T < 4; ++T) keep(L_W + 98304 + w * 8192 + T * 2048, hold[T]);
        });
        const int ai = 2 * (n >> 2) + (n & 1), ahf = (n >> 1) & 1;
        R0[0] = R0[1] = R0[2] = R0[3] = 0x3F803F80u;
        R0[4] = ahf ? 0xEEEE : 0x4444;
        R0[5] = (g >> 1) * 512 + (n >> 2) * 128 + (g & 1) * 64 + (n & 3) * 16;
        R0[6] = lds_base + L_MEMS + 8192 + g * 256 + ai * 32 + ahf * 16;
        R0[7] = lane * 16;
        R0[8] = lds_base + L_Q + 2 * g * 320 + (16 * w + n) * 4;
        R0[9] = lds_base + L_KO + 2 * g * 576 + (16 * w + (n & 8) + 4 * (n & 1) + ((n >> 1) & 3)) * 4;
        R0[10] = lds_base + L_Q + ai * 320 + g * 16;
        R0[11] = lds_base + L_KO + ai * 576 + g * 32 + ahf * 16;
        R0[12] = lds_base + L_KX + 8192 + w * 2048 + (g >> 1) * 256 + n * 16 + (g & 1) * 8;
        R0[13] = lds_base + L_W + w * 4096 + lane * 16;
        R0[14] = cmap(((2 * (4 * tw + w) + (n >> 3)) * 4 + g) * 4096 + h * 128 + (n & 7) * 16);
        R0[15] = (g >> 1) == (w >> 1) ? cmap((64 + 2 * (2 * tw + (w & 1)) + (g >> 1)) * 4096 + h * 128 + (g & 1) * 64 + n * 4) : OOB;
        asm volatile("" : "+{v[120:151]}"(R0), "+{v[152:183]}"(R1), "+{v[184:215]}"(R2), "+{v[216:247]}"(R3), "+{v[248:255]}"(R4));
    }
    const int sel = wait_init(rw, tag, budget, 33 + h);
    int kv_rdy = 0;
    const rsrc_t rs = __builtin_amdgcn_make_buffer_rsrc(ws + sel * 8192, 0, COMM_BYTES - 8192, 0x00020000);
    auto dma = [&](int voff, int soff, unsigned dst) { bdma(rw, voff, soff, dst); };
    auto fetch_kv = [&](int s, int lane) {
        const int n = lane & 15, g = lane >> 4;
        const int kxs = (g >> 1) * 1024 + ((g & 1) * 32 + 16 * (n >> 3) + (n & 7)) * 16;
        const int vxs = (g >> 1) * 1024 + ((g & 1) * 32 + n) * 16;
        const int so_ = (s * 32 + h) * 8192 + w * 2048;
        const unsigned xdst = lds_base + L_KX + w * 2048;
        dma(kxs, OFF_KF + so_, xdst); dma(kxs, OFF_KF + so_ + 128, xdst + 1024);
        dma(vxs, OFF_VF + so_, xdst + 8192); dma(vxs, OFF_VF + so_ + 256, xdst + 8192 + 1024);
    };
    const int fo = F_KVM + h * 256 + tw * 128;
    u32x8 scon;
    scon[0] = w; scon[1] = 2 * OFF_CAN + (h >> 1) * 128 + (2 * (h & 1) + tw) * 16; scon[2] = F_KV + h * 512 + w * 128; scon[3] = 65536 + w * 4096;
    scon[4] = OFF_KF + h * 8192 + w * 2048; scon[5] = fo; scon[6] = tw - 1; scon[7] = (tw ? OFF_VM : OFF_KM) + (h - NH) * 2048 + w * 512;
    u32x8 S1 = {0u, 0u, (unsigned)kv_rdy, (unsigned)budget, (unsigned)a.d0, 0xFFFFFFFFu / 22, (unsigned)(a.c2 + a.b0), 0u};
    u32x8 S2 = {0u, 0u, 0u, (unsigned)a.up, (unsigned)a.dn, (unsigned)a.c2, 0u, 1u};
    fetch_kv(0, opaque_lane());
    __builtin_amdgcn_raw_buffer_store_b64((u32x2){8 * now() + a.e0, 1u}, rs, tid == 0 ? 0 : OOB, scon[1], 16);
    asm volatile("s_waitcnt vmcnt(1)" ::: "memory");
    u32x2 vm = {};
    u32x8 S3;
    {
        const unsigned t = now(), c = (unsigned)__builtin_readcyclecounter();
        const float r = (float)(c - cbase) * __builtin_amdgcn_rcpf(8.0f * (float)(t - tbase));
        S2[1] = 8 * t + 16; S1[5] = (unsigned)(4294967296.0f * __builtin_amdgcn_rcpf(r));
        S3 = (u32x8){c, (unsigned)(4096.0f * r), 2u * OFF_MEMB + ((h >> 3) & (NREP - 1)) * 32768, w == 3 ? ~0u : 0u};
    }
    for (;;) {
        asm volatile(STEP_ASM
            : "+{v[96:97]}"(vm), "+{v[0:7]}"(S1), "+{v[8:15]}"(S2), "+{v[16:23]}"(S3), "+{v[24:31]}"(scon)
            : "{a[0:31]}"(A0), "{a[32:63]}"(A1), "{a[64:95]}"(A2), "{a[96:127]}"(A3), "{a[128:159]}"(A4), "{a[160:191]}"(A5), "{a[192:223]}"(A6), "{a[224:255]}"(A7),
              "{v[120:151]}"(R0), "{v[152:183]}"(R1), "{v[184:215]}"(R2), "{v[216:247]}"(R3), "{v[248:255]}"(R4),
              [rs] "s"(rs), [rw] "s"(rw)
            : STEP_CLOBBER);
        const int s = __builtin_amdgcn_readfirstlane(S1[0]);
        const unsigned st = __builtin_amdgcn_readfirstlane(S1[7]);
        kv_rdy = __builtin_amdgcn_readfirstlane(S1[2]); budget = __builtin_amdgcn_readfirstlane(S1[3]);
        if (s == NSEG - 1) break;
        if (__builtin_expect(st & 2, 0)) {
            const int lane = opaque_lane();
            LAS char* z = (LAS char*)0;
            const f32x4 zero = {0, 0, 0, 0};
            const u32x4 ones = __builtin_shufflevector(R0, R0, 0, 1, 2, 3);
            const unsigned zr = lane >> 6;
            const int idx = R0[4];
            u32x4 qf[4];
            f32x4 s1 = zero, s2 = zero;
#pragma unroll
            for (int ks = 0; ks < 4; ++ks) {
                qf[ks] = *(LAS u32x4*)(z + R0[10] + ks * 64);
                s1 = MFV(*(LAS u32x4*)(lds + L_KX + lane * 16 + ks * 2048), qf[ks], s1);
                s2 = MFV(*(LAS u32x4*)(lds + L_KX + lane * 16 + ks * 2048 + 1024), qf[ks], s2);
            }
            f32x4 s0 = SMF64(*(LAS u32x4*)(z + R0[11]), __builtin_shufflevector(qf[0], qf[1], 0, 1, 2, 3, 4, 5, 6, 7), zero, idx);
            s0 = SMF64(*(LAS u32x4*)(z + R0[11] + 128), __builtin_shufflevector(qf[2], qf[3], 0, 1, 2, 3, 4, 5, 6, 7), s0, idx);
            const float sa = s0[0] + s0[2], sb = s0[1] + s0[3];
            float mx = fmaxf(fmaxf(fmaxf(fmaxf(s1[0], s1[1]), fmaxf(s1[2], s1[3])), fmaxf(fmaxf(s2[0], s2[1]), fmaxf(s2[2], s2[3]))), fmaxf(sa, sb));
            mx = fmaxf(mx, __shfl_xor(mx, 16)); mx = fmaxf(mx, __shfl_xor(mx, 32));
            auto ex2 = [&](float x, float y) { return pack2(__builtin_amdgcn_exp2f(x - mx), __builtin_amdgcn_exp2f(y - mx)); };
            const u32x4 px = {ex2(s1[0], s1[1]), ex2(s1[2], s1[3]), ex2(s2[0], s2[1]), ex2(s2[2], s2[3])};
            const unsigned pm = ex2(sa, sb);
            const f32x4 l = MFV(((u32x4){pm, zr, zr, zr}), ones, MFV(px, ones, zero));
            const u32x2 x0 = *(LAS u32x2*)(z + R0[12]), y0 = *(LAS u32x2*)(z + R0[12] + 1024), x1 = *(LAS u32x2*)(z + R0[12] + 512), y1 = *(LAS u32x2*)(z + R0[12] + 1536);
            const f32x4 o = SMF64(px, ((u32x8){x0.x, y0.x, x0.y, y0.y, x1.x, y1.x, x1.y, y1.y}), zero, idx) + SMF32(((u32x2){pm, zr}), ((u32x4){vm.x, vm.y, zr, zr}), zero, idx);
            const float i0 = __builtin_amdgcn_rcpf(l[0]), i1 = __builtin_amdgcn_rcpf(l[1]);
            *(LAS unsigned*)(z + R0[9] + 256) = pack2(o[0] * i0, o[2] * i0);
            *(LAS unsigned*)(z + R0[9] + 256 + 576) = pack2(o[1] * i1, o[3] * i1);
            WG_BARRIER();
            const u32x4 oa = *(LAS u32x4*)(z + R0[11] + 256 + (w >> 1) * 128), ob = *(LAS u32x4*)(z + R0[11] + 384 - (w >> 1) * 128);
            auto frag = [&](int at) { return __builtin_shufflevector(*(LAS u32x4*)(z + at), *(LAS u32x4*)(z + at + 1024), 0, 1, 2, 3, 4, 5, 6, 7); };
            f32x2 f[4];
#pragma unroll
            for (int T = 0; T < 4; ++T) f[T] = fold(SMF64(ob, frag(R0[13] + scon[3] + 32768 + T * 2048), SMF64(oa, frag(R0[13] + scon[3] + T * 2048), zero, idx), idx));
            const f32x2 fx = fold(SMF64(ob, frag((R0[13] ^ 8192) + 49152 + 2048), SMF64(oa, frag(R0[13] + 49152 + 2048), zero, idx), idx));
            const int pb = 2 * (OFF_PART + (s & 1) * PART_BYTES);
            st128((u32x4){pack2(f[0].x, f[1].x), pack2(f[2].x, f[3].x), pack2(f[0].y, f[1].y), pack2(f[2].y, f[3].y)}, rs, R0[14], pb);
            st32(pack2(fx.x, fx.y), rs, R0[15], pb);
        }
        if (__builtin_expect((st & 2) || kv_rdy <= s, 0)) {
            while (kv_rdy <= s) { kv_rdy = prog_min(rw, F_KV + h * 512 + w * 128); if (--budget < 0) break; }
            fetch_kv(s + 1, opaque_lane());
        }
        if (st & 2) { S1[1] = read_canaries(s, rs, opaque_lane(), budget) + S1[6]; S2[2] += 0x10000; }
        if ((int)(S1[1] - S2[1]) > 0x8000) { S2[1] = 8 * now() + 16; S3[0] = (unsigned)__builtin_readcyclecounter(); }
        S1[0] = s + 1; S1[2] = kv_rdy; S1[3] = budget;
    }
    TT(true, 0)
    st32((unsigned)(NSEG - 2), rw, opaque_lane() == 0 && w == 0 ? fo : OOB, 0);
}

__global__ void __launch_bounds__(256, 1) fused_kernel(Args a) {
    extern __shared__ __attribute__((aligned(16))) char smem[];
    LAS char* lds = (LAS char*)smem;
    const unsigned lds_base = (unsigned)(uintptr_t)lds;
    const int w = __builtin_amdgcn_readfirstlane(threadIdx.x >> 6);
    const int b = blockIdx.x, bx = b & 7, bi = b >> 3;
    if (bi >= 28) {
        const void* pc; unsigned len;
        asm volatile("s_getpc_b64 %0\n1:\ts_mov_b32 %1, .Lstep_end-1b" : "=s"(pc), "=s"(len));
        touch(pc, min((bi - 28) * 32768 + threadIdx.x * 128, len), lds_base);
    }
    asm volatile("" :: "s"(a.Wq), "s"(a.cw));
    const rsrc_t rw = __builtin_amdgcn_make_buffer_rsrc(a.ws, 0, WS_USED, 0x00020000);
    int budget = SPIN_MAX;
#define RT(i)
    if (bx < 4) {
        if (bi < 16) role_head(a, lds, lds_base, 8 * bx + (bi >> 1), bi & 1, rw, budget);
        else role_owner(a, lds_base, 4 * (bi - 16) + bx, rw, budget);
    } else {
        const int fx = bx - 4, no = fx < 3 ? 14 : 22, j = 14 * fx + bi;
        if (bi >= no || (j & 2)) {
            const unsigned t = now() + a.d1;
            if (bi >= no) { if (w) prefetch(a, lds_base, bx, (bi - no) * 3 + w - 1, 3 * (32 - no)); }
            else {
                const int sh = (j >> 2) * 2 + (j & 1);
#pragma unroll
                for (int i = 0; i < INIT_SHARE / 4096; ++i) st128((u32x4){SENT, SENT, SENT, SENT}, rw, threadIdx.x * 16 + i * 4096, sh * INIT_SHARE);
#pragma unroll
                for (int i = 0; i < 4; ++i) st128((u32x4){SENT, SENT, SENT, SENT}, rw, threadIdx.x * 16 + (i & 1) * 4096, (i & 2 ? OFF_VM : OFF_KM) + FAST * 65536 + sh * 8192);
                const u32x2 tag = launch_tag(a);
                VM_DRAIN();
                WG_BARRIER();
                __builtin_amdgcn_raw_buffer_store_b64(tag, rw, threadIdx.x == 0 ? F_INIT + sh * 128 : OOB, 0, 16);
                RT(3)
            }
            (void)wait_until(t);
        }
        if (bi >= no) {
            const int t = 18 * fx + bi - no;
            role_s4d(a, lds, t, rw, budget);
            RT(1)
            if (t < 9 * OUT_REP) role_outp(a, lds, lds_base, t % 9, t / 9, rw, budget);
        }
        else {
            if (!(j & 2)) role_kv(a, lds, lds_base, (j >> 2) * 2 + (j & 1), rw, budget);
            RT(1)
            role_qa(a, lds, lds_base, (j >> 2) * 2, (j & 2 ? 0 : FAST) + (j & 1), (j & 2 ? FAST : NSEG) - 2 + (j & 1), rw, budget);
        }
    }
    RT(2)
    TT(true, 5)
    if (budget < 0 && threadIdx.x == 0) st32(0x1000u + b, rw, F_ERR, 0);
}

static int g_c1 = 56, g_c1m = 52, g_c1x = 70, g_c2 = 372, g_b0 = 64, g_up = 16, g_fl = 8, g_dn = 3, g_d0 = 1900, g_d1 = 300, g_rd = 50, g_cw = 60, g_e0 = 165, g_x1 = 30, g_x2 = 106, g_x3 = 120, g_x4 = 104, g_x5 = 152, g_x6 = 145, g_x7 = 50;
extern "C" void kernel_launch(void* const* d_in, const int* in_sizes, int n_in, void* d_out, int out_size, void* d_ws, size_t ws_size, hipStream_t stream) {
    Args a;
    a.x = (const u16*)d_in[0]; a.mem0 = (const u16*)d_in[1];
    a.Wq = (const u16*)d_in[2]; a.Wk = (const u16*)d_in[3]; a.Wv = (const u16*)d_in[4]; a.Wo = (const u16*)d_in[5];
    a.lAr = (const u16*)d_in[8]; a.Aim = (const u16*)d_in[9]; a.Cr = (const u16*)d_in[10]; a.Ci = (const u16*)d_in[11];
    a.ldt = (const u16*)d_in[12]; a.Dsk = (const u16*)d_in[13]; a.gw = (const u16*)d_in[14];
    a.out = (u16*)d_out;
    a.ws = (char*)(((uintptr_t)d_ws + ((2u << 20) - 1)) & ~(uintptr_t)((2u << 20) - 1));
    a.c1 = g_c1 | g_c1m << 8 | g_c1x << 16; a.c2 = g_c2; a.d0 = g_d0; a.b0 = g_b0; a.d1 = g_d1; a.up = g_up | g_fl << 16; a.dn = g_dn; a.rd = g_rd; a.cw = g_cw; a.e0 = 8 * g_e0;
    a.xa = g_x1 | g_x2 << 8 | g_x5 << 16 | g_x7 << 24; a.xb = g_x6 * 0x101 | g_x3 << 16 | g_x4 << 24;
    (void)hipFuncSetAttribute((const void*)fused_kernel, hipFuncAttributeMaxDynamicSharedMemorySize, L_TOTAL);
    fused_kernel<<<256, 256, L_TOTAL, stream>>>(a);
}
```

```cpp
#include <hip/hip_runtime.h>
#include <hip/hip_bf16.h>
#include <stdint.h>

typedef unsigned short u16;
typedef __bf16 bf16x8 __attribute__((ext_vector_type(8)));
typedef __bf16 bf16x16 __attribute__((ext_vector_type(16)));
typedef _Float16 f16x2 __attribute__((ext_vector_type(2)));
typedef _Float16 f16x8 __attribute__((ext_vector_type(8)));
typedef short s16x4 __attribute__((ext_vector_type(4)));
typedef float f32x2 __attribute__((ext_vector_type(2)));
typedef float f32x4 __attribute__((ext_vector_type(4)));
typedef float f32x16 __attribute__((ext_vector_type(16)));
typedef unsigned u32x2 __attribute__((ext_vector_type(2)));
typedef unsigned u32x4 __attribute__((ext_vector_type(4)));
typedef unsigned u32x8 __attribute__((ext_vector_type(8)));
typedef unsigned u32x16 __attribute__((ext_vector_type(16)));
typedef unsigned u32x32 __attribute__((ext_vector_type(32)));
typedef short s16x8 __attribute__((ext_vector_type(8)));
#define LAS __attribute__((address_space(3)))
#define DI __device__ __forceinline__
typedef __amdgpu_buffer_rsrc_t rsrc_t;

constexpr int SEQ = 2048, DM = 576, NH = 32, DH = 128, HD = NH * DH, NM = 8, SEG = 32, NSEG = 64, NS = 128;
constexpr unsigned SENT = 0xFFFFFFFFu;
constexpr unsigned OOB = 0x7FFFFFF0u;
constexpr int SPIN_MAX = 1 << 20;

constexpr int OFF_PART = 0;
constexpr int PART_BYTES = 72 * 4096;
constexpr int OFF_MEMB = OFF_PART + 2 * PART_BYTES;
constexpr int MEMB_STRIDE = 4 * 16384;
constexpr int OFF_CAN = OFF_MEMB + 4 * MEMB_STRIDE;
constexpr int COMM_LOGICAL = OFF_CAN + 32768;
constexpr int COMM_BYTES = 2 * COMM_LOGICAL;
constexpr int F_ERR = COMM_BYTES;
constexpr int F_KV = F_ERR + 128;
constexpr int F_KVM = F_KV + 16384;
constexpr int F_O = F_KVM + 8192;
constexpr int F_H = F_O + 4096;
constexpr int F_S4 = F_H + 32768;
constexpr int F_DONE = F_S4 + 512;
constexpr int INIT_SHARE = 14 * 4096, INIT_BYTES = 32 * INIT_SHARE;
constexpr int F_INIT = INIT_BYTES;
constexpr int F_EPOCH = F_INIT + 24576;
static_assert(F_DONE + 128 <= INIT_BYTES, "");
static_assert(F_EPOCH + 128 <= (2 << 20), "hand-off region");
constexpr int OFF_KF = 2 << 20;
constexpr int OFF_VF = OFF_KF + SEQ * HD * 2;
constexpr int OFF_OF = OFF_VF + SEQ * HD * 2;
constexpr int OFF_KM = OFF_OF + SEQ * HD * 2;
constexpr int OFF_VM = OFF_KM + NSEG * NH * NM * DH * 2;
constexpr int FAST = NSEG - 4;
constexpr int OFF_BO = OFF_VM + NSEG * NH * NM * DH * 2;
constexpr int BO_ROW = 4352;
constexpr int OFF_GT = OFF_BO + DM * BO_ROW;
constexpr int OFF_DBG = OFF_GT + SEQ * 4;
constexpr int WS_USED = OFF_DBG + (4 << 20);

struct Args {
    const u16 *x, *mem0, *Wq, *Wk, *Wv, *Wo, *lAr, *Aim, *Cr, *Ci, *ldt, *Dsk, *gw;
    u16* out; char* ws;
    int c1, c2, d0, b0, d1, up, dn, rd, cw, e0;
    unsigned xa, xb;
};

DI float bf2f(u16 b) { return __uint_as_float((unsigned)b << 16); }
DI float bflo(unsigned w) { return __uint_as_float(w << 16); }
DI float bfhi(unsigned w) { return __uint_as_float(w & 0xFFFF0000u); }
DI u16 f2bf(float f) { return __builtin_bit_cast(u16, (__bf16)f); }
DI unsigned pack2(float lo, float hi) {
    typedef __bf16 bf16x2 __attribute__((ext_vector_type(2)));
    bf16x2 v = {(__bf16)lo, (__bf16)hi};
    return __builtin_bit_cast(unsigned, v);
}
#define WG_BARRIER() asm volatile("s_waitcnt lgkmcnt(0)\n\ts_barrier" ::: "memory")
#define VM_DRAIN() asm volatile("s_waitcnt vmcnt(0)" ::: "memory")
DI int cmap(int o) { return (o & 0x1FFF) | ((o >> 13) << 14); }
DI unsigned mx4(unsigned a, u32x4 v) { a = max(max(a, v.x), v.y); return max(max(a, v.z), v.w); }
DI unsigned now() { return (unsigned)__builtin_amdgcn_s_memrealtime(); }
DI unsigned wait_until(unsigned t) {
    unsigned v; int guard = 8192;
    do { v = now(); } while ((int)(t - v) >= 4 && --guard > 0);
    const int r = __builtin_amdgcn_readfirstlane((int)(t - v));
    asm volatile("s_cmp_lt_i32 %0, 1\n\ts_cbranch_scc1 1f\n\ts_nop 3\n\ts_cmp_lt_i32 %0, 2\n\ts_cbranch_scc1 1f\n\ts_nop 3\n\ts_cmp_lt_i32 %0, 3\n\ts_cbranch_scc1 1f\n\ts_nop 7\n1:" :: "s"(r) : "scc");
    return r > 0 ? t : v + 2;
}
DI void st128(u32x4 v, rsrc_t rs, int off, int soff) {
    __builtin_amdgcn_raw_buffer_store_b128(v, rs, off, soff, 16);
    asm volatile("s_nop 1" :: "v"(v) : "memory");
}
DI void st32(unsigned v, rsrc_t rs, int off, int soff) { __builtin_amdgcn_raw_buffer_store_b32(v, rs, off, soff, 16); }
DI u32x4 ld128(rsrc_t rs, int off, int soff) { return __builtin_amdgcn_raw_buffer_load_b128(rs, off, soff, 16); }
DI u32x2 ld64(rsrc_t rs, int off, int soff) { return __builtin_amdgcn_raw_buffer_load_b64(rs, off, soff, 16); }
DI unsigned ld32(rsrc_t rs, int off, int soff) { return __builtin_amdgcn_raw_buffer_load_b32(rs, off, soff, 16); }
DI f32x2 fold(f32x4 a) { return (f32x2){a[0], a[1]} + (f32x2){a[2], a[3]}; }
DI float dpp_xor1(float v) { return __int_as_float(__builtin_amdgcn_update_dpp(0, __float_as_int(v), 0xB1, 0xf, 0xf, true)); }
DI float red_sum_g(float v) {
    auto a = __builtin_amdgcn_permlane16_swap(__float_as_uint(v), __float_as_uint(v), false, false);
    v = __uint_as_float(a[0]) + __uint_as_float(a[1]);
    auto b = __builtin_amdgcn_permlane32_swap(__float_as_uint(v), __float_as_uint(v), false, false);
    return __uint_as_float(b[0]) + __uint_as_float(b[1]);
}
DI int wave_max32(int d) {
    d = max(d, __builtin_amdgcn_update_dpp(d, d, 0x121, 0xf, 0xf, false));
    d = max(d, __builtin_amdgcn_update_dpp(d, d, 0x122, 0xf, 0xf, false));
    d = max(d, __builtin_amdgcn_update_dpp(d, d, 0x124, 0xf, 0xf, false));
    d = max(d, __builtin_amdgcn_update_dpp(d, d, 0x128, 0xf, 0xf, false));
    return __builtin_amdgcn_readfirstlane(max(max(__builtin_amdgcn_readlane(d, 0), __builtin_amdgcn_readlane(d, 16)), max(__builtin_amdgcn_readlane(d, 32), __builtin_amdgcn_readlane(d, 48))));
}
DI u32x2 tr_read(LAS char* lds, int addr) { return __builtin_bit_cast(u32x2, __builtin_amdgcn_ds_read_tr16_b64_v4i16((LAS s16x4*)(lds + addr))); }
DI void wait_prog(rsrc_t rw, int off, bool act, int thr, int& budget) {
    for (;;) {
        const int v = (int)ld32(rw, off, 0);
        asm volatile("" ::: "memory");
        if (__all(!act || v >= thr)) break;
        if (--budget < 0) break;
        __builtin_amdgcn_s_sleep(1);
    }
}
DI u32x2 launch_tag(const Args& a) {
    unsigned long long z;
    asm volatile("s_load_dwordx2 %0, %1, 0x0\n\ts_waitcnt lgkmcnt(0)" : "=s"(z) : "s"(a.ws + F_EPOCH) : "memory");
    z += 0x9E3779B97F4A7C15ull;
    z = (z ^ (z >> 30)) * 0xBF58476D1CE4E5B9ull; z = (z ^ (z >> 27)) * 0x94D049BB133111EBull; z ^= z >> 31;
    return (u32x2){(unsigned)z, (unsigned)(z >> 32)};
}
DI int wait_init(rsrc_t rw, u32x2 tag, int& budget, int more = 0) {
    const int l = threadIdx.x & 63, o = F_INIT + (l < 32 ? l : l == 63 ? min(more, 32) : more) * 128;
    u32x2 v;
    for (;;) {
        v = ld64(rw, o, 0);
        asm volatile("" ::: "memory");
        if (__all(((v.x ^ tag.x) >> 1) == 0 && v.y == tag.y)) break;
        if (--budget < 0) break;
        __builtin_amdgcn_s_sleep(2);
    }
    return __builtin_amdgcn_readlane(v.x, 63) & 1;
}
DI void glds16(const void* base, int voff, unsigned lds_dst) {
    unsigned keep;
    asm volatile("s_mov_b32 %0, m0\n\ts_mov_b32 m0, %3\n\ts_nop 0\n\tglobal_load_lds_dwordx4 %1, %2\n\ts_mov_b32 m0, %0" : "=&s"(keep) : "v"(voff), "s"(base), "s"(lds_dst) : "memory");
}
DI void bdma(rsrc_t rs, int voff, int soff, unsigned lds_dst) {
    unsigned keep;
    asm volatile("s_mov_b32 %0, m0\n\ts_mov_b32 m0, %4\n\ts_nop 0\n\tbuffer_load_dwordx4 %1, %2, %3 offen sc1 lds\n\ts_mov_b32 m0, %0" : "=&s"(keep) : "v"(voff), "s"(rs), "s"(soff), "s"(lds_dst) : "memory");
}
DI int swz(int r) { return ((r & 3) ^ (((r >> 2) & 3) << 1) ^ (((r >> 4) & 1) << 2)) & 7; }
template <class F> DI u32x4 ring_offs(int w, int lane, F at) {
    u32x4 o;
#pragma unroll
    for (int j = 0; j < 4; ++j) { const int r = 16 * w + 4 * j + (lane >> 4); o[j] = at(r, (lane & 15) ^ (2 * swz(r))) - 1024 * j; }
    return o;
}
DI u32x4 ring_offs(int w, int lane, int ld) { return ring_offs(w, lane, [&](int r, int c) { return r * ld * 2 + c * 16; }); }
DI void ring_issue(const u16* src, u32x4 o, unsigned slot) {
    unsigned keep;
    asm volatile("s_mov_b32 %0, m0\n\ts_mov_b32 m0, %6\n\ts_nop 0\n\tglobal_load_lds_dwordx4 %1, %5\n\tglobal_load_lds_dwordx4 %2, %5 offset:1024\n\t"
                 "global_load_lds_dwordx4 %3, %5 offset:2048\n\tglobal_load_lds_dwordx4 %4, %5 offset:3072\n\ts_mov_b32 m0, %0"
                 : "=&s"(keep) : "v"(o.x), "v"(o.y), "v"(o.z), "v"(o.w), "s"(src), "s"(slot) : "memory");
}
#define PF_FIRST 2
constexpr int L_TOTAL = 163840, L_SINK = L_TOTAL - 256;
DI void touch(const void* base, int voff, unsigned lds_base) {
    unsigned keep;
    asm volatile("s_mov_b32 %0, m0\n\ts_mov_b32 m0, %3\n\ts_nop 0\n\tglobal_load_lds_dword %1, %2\n\ts_mov_b32 m0, %0" : "=&s"(keep) : "v"(voff), "s"(base), "s"(lds_base + L_SINK) : "memory");
}
DI void prefetch(const Args& a, unsigned lds_base, int x, int id, int n) {
    const int lane = threadIdx.x & 63;
    const int h0 = x < 4 ? 8 * x : (0x160E0800 >> (8 * (x - 4))) & 255, lpr = x == 5 ? 12 : x == 7 ? 20 : 16, per = 9 * lpr;
    const int first = PF_FIRST * lpr, end = (x < 4 ? 4 : 2) * per;
    const float rl = __builtin_amdgcn_rcpf((float)lpr);
    for (int i = first + id; i < end; i += n) {
        int m = (i >= per) + (i >= 2 * per) + (i >= 3 * per), j = i - m * per;
        if (x < 4 && m < 3) { const int b = i >> 4; m = b < 20 ? b >> 3 : min(b - 20, 2); j = (b < 16 ? b & 7 : b < 20 ? b - 12 : b < 23 ? 8 : b - 23) * 16 + (i & 15); }
        const int mm = m + (x >= 4);
        const int L = j * 64 + lane, row = (int)((L + 0.5f) * rl);
        touch(mm == 0 ? a.Wq : mm == 1 ? a.Wk : mm == 2 ? a.Wv : a.Wo, m == 3 ? h0 * (DH * DM * 2) + L * 128 : row * (HD * 2) + (L - row * lpr) * 128 + h0 * 256, lds_base);
    }
}
template <int N> DI void ring_wait() { asm volatile("s_waitcnt vmcnt(%0) lgkmcnt(0)\n\ts_barrier" :: "i"(N) : "memory"); }
template <int I> struct ic_t { static constexpr int value = I; };
template <int N, int I = 0, class F> DI void static_for(F&& f) { if constexpr (I < N) { f(ic_t<I>{}); static_for<N, I + 1>(f); } }
#define MFMA32(a, b, c) __builtin_amdgcn_mfma_f32_32x32x16_bf16(__builtin_bit_cast(bf16x8, a), __builtin_bit_cast(bf16x8, b), c, 0, 0, 0)
#define MFMA16(a, b, c) __builtin_amdgcn_mfma_f32_16x16x32_bf16(__builtin_bit_cast(bf16x8, a), __builtin_bit_cast(bf16x8, b), c, 0, 0, 0)

constexpr int S_CH = 9;
constexpr int S_XRS = 4096 + 32;
constexpr int S_XR = 0;
constexpr int S_RC = S_CH * S_XRS;
constexpr int S_RCS = 4192;
constexpr int S_KD = S_RC + 4 * S_RCS;
constexpr int S_K = S_KD + 48 * 128;
constexpr int S_KROW = 96;
constexpr int S_TAB = S_K + 79 * S_KROW;
constexpr int S_TROW = 544;
constexpr int S_RED = S_TAB + 57 * S_TROW;
constexpr int S_TOTAL = S_RED + 4 * 64 * 144;
static_assert(S_RC % 16 == 0 && S_TAB % 16 == 0 && S_RED % 16 == 0, "");
DI f32x2 crot(f32x2 z) { return (f32x2){-z.y, z.x}; }
DI f32x2 cmul(f32x2 a, f32x2 z) { return __builtin_elementwise_fma(a.yy, crot(z), a.xx * z); }
DI f32x2 cexp_p(float ar, float ai, float p) {
    const float x = ai * p;
    const float hi = x * 0.15915494309189535f;
    const float lo = __builtin_fmaf(x, 0.15915494309189535f, -hi) + x * 6.420638316725915e-9f;
    const float rev = (hi - __builtin_rintf(hi)) + lo;
    const float m = __builtin_amdgcn_exp2f(ar * p * 1.4426950408889634f);
    return (f32x2){m * __builtin_amdgcn_cosf(rev), m * __builtin_amdgcn_sinf(rev)};
}
#define FENCE() __builtin_amdgcn_sched_barrier(0)
#define TT(c, i)
DI void role_s4d(const Args& a, LAS char* lds, unsigned lds_base, int task, rsrc_t rw, int& budget) {
    const int tid = threadIdx.x, lane = tid & 63, w = __builtin_amdgcn_readfirstlane(tid >> 6);
    const int d0 = task * S_CH;
    touch(w == 0 ? a.lAr : w == 1 ? a.Aim : w == 2 ? a.Cr : a.Ci, min(d0 * (NS * 2) + min(lane, 2 * S_CH + 1) * 128, DM * NS * 2 - 4), lds_base);
    touch(w & 1 ? a.ldt : a.Dsk, min(lane * 128, DM * 2 - 4), lds_base);
#define S4S(i)
    {
        const rsrc_t rx = __builtin_amdgcn_make_buffer_rsrc((void*)a.x, 0, SEQ * DM * 2, 0x00020000);
        const unsigned sh = (d0 & 1) * 16;
        u32x4 v[8]; unsigned e[8];
#pragma unroll
        for (int i = 0; i < 8; ++i) {
            const int o = (tid + 256 * i) * (DM * 2) + (d0 & ~1) * 2;
            v[i] = __builtin_amdgcn_raw_buffer_load_b128(rx, o, 0, 0); e[i] = __builtin_amdgcn_raw_buffer_load_b32(rx, o, 16, 0);
        }
#pragma unroll
        for (int p = 0; p < 2; ++p) {
            const int tok = 32 * task + 8 * w + 4 * p + (lane >> 4);
            float acc = 0.f;
#pragma unroll
            for (int i = 0; i < 5; ++i) {
                const int ch = (lane & 15) + 16 * i;
                const u32x4 gv = ch < 72 ? *(const u32x4*)(a.gw + ch * 8) : (u32x4){0u, 0u, 0u, 0u};
                const u32x4 xv = *(const u32x4*)(a.x + (size_t)tok * DM + min(ch, 71) * 8);
#pragma unroll
                for (int j = 0; j < 4; ++j) acc += bflo(xv[j]) * bflo(gv[j]) + bfhi(xv[j]) * bfhi(gv[j]);
            }
            acc += __shfl_xor(acc, 1); acc += __shfl_xor(acc, 2); acc += __shfl_xor(acc, 4); acc += __shfl_xor(acc, 8);
            st32(__float_as_uint(1.0f / (1.0f + __expf(-acc))), rw, (lane & 15) == 0 ? OFF_GT + tok * 4 : OOB, 0);
        }
#pragma unroll
        for (int i = 0; i < 8; ++i) {
            const unsigned x5[5] = {__builtin_amdgcn_alignbit(v[i].y, v[i].x, sh), __builtin_amdgcn_alignbit(v[i].z, v[i].y, sh), __builtin_amdgcn_alignbit(v[i].w, v[i].z, sh),
                                    __builtin_amdgcn_alignbit(e[i], v[i].w, sh), e[i] >> sh};
#pragma unroll
            for (int c = 0; c < S_CH; ++c) *(LAS u16*)(lds + S_XR + c * S_XRS + (2047 - tid - 256 * i) * 2) = (u16)(x5[c >> 1] >> (16 * (c & 1)));
        }
    }
    if (tid < 8 * S_CH) *(LAS unsigned*)(lds + S_XR + (tid >> 3) * S_XRS + 4096 + (tid & 7) * 4) = 0;
    if (tid < 64) *(LAS unsigned*)(lds + S_RC + (tid >> 4) * S_RCS + 4096 + (tid & 15) * 4) = 0;
    for (int i = tid; i < 15 * S_KROW / 4; i += 256) *(LAS unsigned*)(lds + S_K + i * 4) = 0;
    int kd[8];
#pragma unroll
    for (int j = 0; j < 8; ++j) {
        const int p = (int)bf2f(f2bf((float)(8 * tid + j))), k = p >> 7, r = p & 7;
        const int row = p < 256 ? 8 * k + r : (p < 512 ? 8 + 4 * k + (r >> 1) : (p < 1024 ? 16 + 2 * k + (r >> 2) : 24 + k));
        kd[j] = S_KD + (row * 16 + ((p >> 3) & 15)) * 8;
    }
    const int n = tid & 127, nn = lane & 15, g = lane >> 4;
    u16 pA = a.lAr[d0 * NS + n], pI = a.Aim[d0 * NS + n], pCr = a.Cr[d0 * NS + n], pCi = a.Ci[d0 * NS + n], pT = a.ldt[d0], pD = a.Dsk[d0];
    WG_BARRIER();
    for (int ch = 0; ch < S_CH; ++ch) {
        S4S(0)
        const float dsk = bf2f(pD);
        {
            const u32x4 v = *(LAS u32x4*)(lds + S_XR + ch * S_XRS + tid * 16);
            const u32x2 e = *(LAS u32x2*)(lds + S_XR + ch * S_XRS + tid * 16 + 16);
            const unsigned s01 = __builtin_amdgcn_alignbit(v.y, v.x, 16), s12 = __builtin_amdgcn_alignbit(v.z, v.y, 16), s23 = __builtin_amdgcn_alignbit(v.w, v.z, 16);
            const unsigned s34 = __builtin_amdgcn_alignbit(e.x, v.w, 16), s45 = __builtin_amdgcn_alignbit(e.y, e.x, 16);
            *(LAS u32x4*)(lds + S_RC + tid * 16) = v;
            *(LAS u32x4*)(lds + S_RC + S_RCS + tid * 16) = (u32x4){s01, s12, s23, s34};
            *(LAS u32x4*)(lds + S_RC + 2 * S_RCS + tid * 16) = (u32x4){v.y, v.z, v.w, e.x};
            *(LAS u32x4*)(lds + S_RC + 3 * S_RCS + tid * 16) = (u32x4){s12, s23, s34, s45};
        }
        S4S(1)
        const float dt = bf2f(f2bf(expf(bf2f(pT))));
        const int sc = 140 - (wave_max32(max(pCr & 0x7FFF, pCi & 0x7FFF)) >> 7);
        {
            const float ar = -bf2f(f2bf(expf(bf2f(pA)))) * dt, ai = bf2f(pI) * dt;
            const f32x2 C = {__builtin_amdgcn_ldexpf(bf2f(pCr), sc), __builtin_amdgcn_ldexpf(bf2f(pCi), sc)};
            const int dn = min(d0 + ch + 1, DM - 1);
            pA = a.lAr[dn * NS + n]; pI = a.Aim[dn * NS + n]; pCr = a.Cr[dn * NS + n]; pCi = a.Ci[dn * NS + n]; pT = a.ldt[dn]; pD = a.Dsk[dn];
            const int to = S_TAB + n * 4;
            auto put = [&](int row, f32x2 v) { *(LAS f16x2*)(lds + to + row * S_TROW) = __builtin_convertvector(v, f16x2); };
            const f32x2 z128 = cexp_p(ar, ai, 128.f);
            if (w < 2) {
                f32x2 E[8], G[4];
                E[1] = cexp_p(ar, ai, 1.f); G[0] = C;
#pragma unroll
                for (int r = 2; r < 8; ++r) E[r] = cmul(E[r >> 1], E[r - (r >> 1)]);
#pragma unroll
                for (int k = 1; k < 4; ++k) G[k] = cmul(G[k - 1], z128);
#pragma unroll
                for (int k = 0; k < 4; ++k)
#pragma unroll
                    for (int r = 0; r < 8; r += 1 + (k >> 1)) put(k < 2 ? 8 * k + r : 8 + 4 * k + (r >> 1), r ? cmul(E[r], G[k]) : G[k]);
            } else {
                const f32x2 z4 = cexp_p(ar, ai, 4.f), z8 = cexp_p(ar, -ai, 8.f);
                f32x2 G = cmul(C, cexp_p(ar, ai, 512.f)), B = z8;
                put(41, (f32x2){1.f, 0.f});
#pragma unroll
                for (int k = 1; k < 16; ++k) {
                    put(41 + k, B); B = cmul(B, z8);
                    if (k < 5) { put(22 + 2 * k, G); put(23 + 2 * k, cmul(G, z4)); }
                    else if (k < 14) put(27 + k, G);
                    G = cmul(G, z128);
                }
            }
        }
        WG_BARRIER();
        S4S(2)
        {
            const int ko = (w & 1) * 256 + g * 16, t0 = w & 2;
            u32x4 av[2][4], bv[4];
#pragma unroll
            for (int j = 0; j < 4; ++j) {
                bv[j] = *(LAS u32x4*)(lds + S_TAB + (41 + nn) * S_TROW + ko + j * 64);
                av[0][j] = *(LAS u32x4*)(lds + S_TAB + min(16 * t0 + nn, 40) * S_TROW + ko + j * 64);
                av[1][j] = *(LAS u32x4*)(lds + S_TAB + (16 + nn) * S_TROW + ko + j * 64);
            }
            f32x4 acc[2] = {};
#pragma unroll
            for (int j = 0; j < 8; ++j) acc[j & 1] = __builtin_amdgcn_mfma_f32_16x16x32_f16(__builtin_bit_cast(f16x8, av[j & 1][j >> 1]), __builtin_bit_cast(f16x8, bv[j >> 1]), acc[j & 1], 0, 0, 0);
#pragma unroll
            for (int r = 0; r < 8; ++r) *(LAS float*)(lds + S_KD + (((r < 4 ? 16 * t0 : 16) + 4 * g + (r & 3)) * 16 + nn) * 8 + (w & 1) * 4) = __builtin_amdgcn_ldexpf(acc[r >> 2][r & 3] * dt, 1 - sc);
        }
        WG_BARRIER();
        S4S(3)
        {
            float kv[8];
#pragma unroll
            for (int j = 0; j < 8; ++j) { const f32x2 t = *(LAS f32x2*)(lds + kd[j]); kv[j] = t.x + t.y; }
            *(LAS u32x4*)(lds + S_K + (15 + (tid >> 2)) * S_KROW + (tid & 3) * 16) = (u32x4){pack2(kv[0], kv[1]), pack2(kv[2], kv[3]), pack2(kv[4], kv[5]), pack2(kv[6], kv[7])};
        }
        WG_BARRIER();
        S4S(4)
        {
            const int st0 = 2047 - nn + 8 * g - 32 * w, cc = st0 & 3;
            const int ub = S_RC + cc * S_RCS + (st0 - cc) * 2 - 3872, kb = S_K + (3 + nn - w) * S_KROW + g * 16;
            f32x4 y[4][2] = {};
            u32x2 U[2][16]; u32x4 F[2][4];
            auto rd = [&](int i, int f) {
                if (f < 4) F[i & 1][f] = *(LAS u32x4*)(lds + kb + (12 - 4 * i + 16 * f) * S_KROW);
                else U[i & 1][f - 4] = *(LAS u32x2*)(lds + ub + 3872 - 256 * i - 1024 * ((f - 4) >> 2) - 32 * ((f >> 1) & 1) + 8 * (f & 1));
            };
#pragma unroll
            for (int f = 0; f < 20; ++f) { rd(0, f); FENCE(); }
#pragma unroll
            for (int i = 0; i < 4; ++i) {
                int c = 0;
#pragma unroll
                for (int q = 0; q < 4; ++q)
#pragma unroll
                    for (int T = q; T < 4; ++T)
#pragma unroll
                        for (int bt = 0; bt < 2; ++bt) {
                            y[T][bt] = MFMA16(__builtin_shufflevector(U[i & 1][4 * q + 2 * bt], U[i & 1][4 * q + 2 * bt + 1], 0, 1, 2, 3), F[i & 1][T - q], y[T][bt]);
                            if (i < 3) rd(i + 1, c);
                            ++c;
                            FENCE();
                        }
            }
#pragma unroll
            for (int T = 0; T < 4; ++T)
#pragma unroll
                for (int bt = 0; bt < 2; ++bt) *(LAS f32x4*)(lds + S_RED + (w * 64 + 16 * T + nn) * 144 + bt * 64 + g * 16) = y[T][bt];
        }
        WG_BARRIER();
        S4S(5)
        {
            f32x4 y0 = {0, 0, 0, 0}, y1 = y0;
#pragma unroll
            for (int ww = 0; ww < 4; ++ww) {
                const int o = S_RED + (ww * 64 + (tid >> 2)) * 144 + (tid & 3) * 32;
                y0 += *(LAS f32x4*)(lds + o); y1 += *(LAS f32x4*)(lds + o + 16);
            }
            const u32x4 u = *(LAS u32x4*)(lds + S_XR + ch * S_XRS + (255 - tid) * 16);
            st128((u32x4){pack2(y0[0] + bfhi(u.w) * dsk, y0[1] + bflo(u.w) * dsk), pack2(y0[2] + bfhi(u.z) * dsk, y0[3] + bflo(u.z) * dsk),
                          pack2(y1[0] + bfhi(u.y) * dsk, y1[1] + bflo(u.y) * dsk), pack2(y1[2] + bfhi(u.x) * dsk, y1[3] + bflo(u.x) * dsk)}, rw, tid * 16, OFF_BO + (d0 + ch) * BO_ROW);
        }
        S4S(6)
    }
    wait_init(rw, launch_tag(a), budget);
    VM_DRAIN();
    WG_BARRIER();
    if (tid == 0) st32(1u, rw, F_S4 + task * 4, 0);
}

constexpr int XT_BYTES = 32 * 1152;
constexpr int P_XT = 0;
constexpr int P_STG = 2 * XT_BYTES;
struct WSlice { u32x4 f[36]; };
constexpr int P_RING = 5;
template <class F> DI void load_wslices(WSlice& W0, WSlice& W1, const u16* s0, const u16* s1, int w, int lane, LAS char* lds, unsigned lds_base, F also) {
    constexpr int NB = 18;
    const int r0 = 8 * (lane >> 5) + ((lane & 15) >> 2);
    const int cb = (4 * w + 2 * ((lane >> 4) & 1) + ((lane & 3) >> 1)), lo8 = (lane & 1) * 8;
    auto src = [&](int i) { return (i < 9 ? s0 : s1) + (size_t)(64 * (i % 9)) * HD; };
    const u32x4 ro = ring_offs(w, lane, HD);
#pragma unroll
    for (int i = 0; i < P_RING - 1; ++i) ring_issue(src(i), ro, lds_base + P_STG + i * 16384 + w * 4096);
    also();
    static_for<NB>([&](auto ic) {
        constexpr int i = decltype(ic)::value, rem = NB - 1 - i < P_RING - 2 ? NB - 1 - i : P_RING - 2;
        ring_wait<4 * rem>();
        if constexpr (i + P_RING - 1 < NB) ring_issue(src(i + P_RING - 1), ro, lds_base + P_STG + ((i + P_RING - 1) % P_RING) * 16384 + w * 4096);
        const int sl = P_STG + (i % P_RING) * 16384;
#pragma unroll
        for (int kk = 0; kk < 4; ++kk) {
            const int ra = r0 + 16 * kk, rb = ra + 4;
            const u32x2 lo = tr_read(lds, sl + ra * 256 + ((cb ^ (2 * swz(ra))) * 16) + lo8), hi = tr_read(lds, sl + rb * 256 + ((cb ^ (2 * swz(rb))) * 16) + lo8);
            (i < 9 ? W0 : W1).f[(i % 9) * 4 + kk] = (u32x4){lo.x, lo.y, hi.x, hi.y};
        }
        u32x4* f = (i < 9 ? W0 : W1).f + (i % 9) * 4;
        if constexpr (i < 16) asm volatile("" : "+a"(f[0]), "+a"(f[1]), "+a"(f[2]), "+a"(f[3]));
    });
}
DI u32x4 piece_pi(const f32x16& a, int qq) {
    return (u32x4){pack2(a[4 * qq], a[4 * qq + 8]), pack2(a[4 * qq + 1], a[4 * qq + 9]), pack2(a[4 * qq + 2], a[4 * qq + 10]), pack2(a[4 * qq + 3], a[4 * qq + 11])};
}
DI u32x4 piece_seq(const f32x16& a, int s2) {
    return (u32x4){pack2(a[8 * s2], a[8 * s2 + 1]), pack2(a[8 * s2 + 2], a[8 * s2 + 3]), pack2(a[8 * s2 + 4], a[8 * s2 + 5]), pack2(a[8 * s2 + 6], a[8 * s2 + 7])};
}
struct XTile {
    int xs[9], xa[4];
    DI void init(int w, int lane) {
#pragma unroll
        for (int i = 0; i < 9; ++i) { const int L = (i * 4 + w) * 64 + lane, r = L / 72, cs = L - 72 * r; xs[i] = r * 1152 + (cs ^ ((r >> 1) & 7)) * 16; }
        const int c32 = lane & 31, hf = lane >> 5;
#pragma unroll
        for (int j = 0; j < 4; ++j) xa[j] = P_XT + c32 * 1152 + (((2 * j + hf) ^ ((c32 >> 1) & 7)) * 16);
    }
    DI void dma(const Args& a, unsigned lds_base, int w, int s, int buf, int i) const {
        glds16((const char*)a.x + (size_t)min(s, NSEG - 1) * XT_BYTES, xs[i], lds_base + P_XT + buf * XT_BYTES + (i * 4 + w) * 1024);
    }
    DI void first(const Args& a, unsigned lds_base, int w, int lane, int s) {
        init(w, lane);
#pragma unroll
        for (int i = 0; i < 9; ++i) dma(a, lds_base, w, s, 0, i);
    }
};
template <bool TR, class F> DI void project(f32x16& acc0, f32x16& acc1, const WSlice& W0, const WSlice& W1, const XTile& X, const Args& a, LAS char* lds, unsigned lds_base, int w, int buf, int sn, F mid) {
    const int bo = buf * XT_BYTES;
    constexpr int PD = 5;
    u32x4 xf[PD];
#pragma unroll
    for (int i = 0; i < PD; ++i) xf[i] = *(LAS u32x4*)(lds + X.xa[i & 3] + bo + (i >> 2) * 128);
#pragma unroll
    for (int ks = 0; ks < 36; ++ks) {
        if (ks == 0) asm volatile("v_mfma_f32_32x32x16_bf16 %0, %1, %2, 0" : "=&v"(acc0) : "a"(W0.f[0]), "v"(xf[0]) : "memory");
        else asm volatile("v_mfma_f32_32x32x16_bf16 %0, %1, %2, %0" : "+v"(acc0) : "a"(W0.f[ks]), "v"(xf[ks % PD]) : "memory");
        if (TR) {
            if (ks == 0) asm volatile("v_mfma_f32_32x32x16_bf16 %0, %2, %1, 0" : "=&v"(acc1) : "a"(W1.f[0]), "v"(xf[0]) : "memory");
            else if (ks < 28) asm volatile("v_mfma_f32_32x32x16_bf16 %0, %2, %1, %0" : "+v"(acc1) : "a"(W1.f[ks]), "v"(xf[ks % PD]) : "memory");
            else asm volatile("v_mfma_f32_32x32x16_bf16 %0, %2, %1, %0" : "+v"(acc1) : "v"(W1.f[ks]), "v"(xf[ks % PD]) : "memory");
        } else {
            if (ks == 0) asm volatile("v_mfma_f32_32x32x16_bf16 %0, %1, %2, 0" : "=&v"(acc1) : "a"(W1.f[0]), "v"(xf[0]) : "memory");
            else if (ks < 28) asm volatile("v_mfma_f32_32x32x16_bf16 %0, %1, %2, %0" : "+v"(acc1) : "a"(W1.f[ks]), "v"(xf[ks % PD]) : "memory");
            else asm volatile("v_mfma_f32_32x32x16_bf16 %0, %1, %2, %0" : "+v"(acc1) : "v"(W1.f[ks]), "v"(xf[ks % PD]) : "memory");
        }
        if (ks + PD < 36) xf[ks % PD] = *(LAS u32x4*)(lds + X.xa[(ks + PD) & 3] + bo + ((ks + PD) >> 2) * 128);
        if (ks < 18 && (ks & 1)) X.dma(a, lds_base, w, sn, buf ^ 1, ks >> 1);
        if (ks == (TR ? 14 : 26)) mid();
    }
    asm volatile("s_nop 7\n\ts_nop 3" : "+v"(acc0), "+v"(acc1) :: "memory");
}
#define KV_T 7
#define KV_F 22
DI void role_kv(const Args& a, LAS char* lds, unsigned lds_base, int h, rsrc_t rw, int& budget) {
    const int tid = threadIdx.x, lane = tid & 63, w = __builtin_amdgcn_readfirstlane(tid >> 6);
    WSlice W0, W1;
    XTile X;
    u32x2 tag;
    load_wslices(W0, W1, a.Wk + h * DH, a.Wv + h * DH, w, lane, lds, lds_base, [&]() { X.first(a, lds_base, w, lane, 0); tag = launch_tag(a); });
    VM_DRAIN();
    WG_BARRIER();
    const int oo = h * 8192 + w * 2048 + lane * 16;
    const int fo = lane == 0 ? F_KV + h * 512 + w * 128 : OOB;
    u32x4 xf[6], P[4] = {};
    auto frag = [&](int buf, int ks) { xf[ks % 6] = *(LAS u32x4*)(lds + X.xa[ks & 3] + buf * XT_BYTES + (ks >> 2) * 128); };
    auto cvt = [&](const f32x16& k, const f32x16& v, int i) {
#pragma unroll
        for (int j = 0; j < 4; ++j) asm volatile("v_cvt_pk_bf16_f32 %0, %1, %2" : "=v"(P[i][j]) : "v"(i < 2 ? k[4 * i + j] : v[8 * i - 16 + 2 * j]), "v"(i < 2 ? k[4 * i + j + 8] : v[8 * i - 15 + 2 * j]));
    };
    auto put = [&](int o, int s, int i) { st128(P[i], rw, o + (i & 1) * 1024, (i < 2 ? OFF_KF : OFF_VF) + s * (NH * 8192)); };
    auto now_ = [&](f32x16& k, f32x16& v, int s) {
        asm volatile("s_nop 7\n\ts_nop 3" : "+v"(k), "+v"(v) :: "memory");
#pragma unroll
        for (int i = 0; i < 4; ++i) { cvt(k, v, i); put(oo, s, i); }
    };
    u32x2 in = {};
    auto seg = [&](f32x16& k, f32x16& v, const f32x16& pk, const f32x16& pv, int s, int buf) {
        const int o = s > 1 ? oo : OOB;
#pragma unroll
        for (int ks = 0; ks < 36; ++ks) {
            if (ks == 0) asm volatile("v_mfma_f32_32x32x16_bf16 %0, %1, %2, 0" : "=&v"(k) : "a"(W0.f[0]), "v"(xf[0]) : "memory");
            else asm volatile("v_mfma_f32_32x32x16_bf16 %0, %1, %2, %0" : "+v"(k) : "a"(W0.f[ks]), "v"(xf[ks % 6]) : "memory");
            if (ks >= 1 && ks < 5) cvt(pk, pv, ks - 1);
            if (buf && ks == KV_T && s == 1) {
                asm volatile("s_waitcnt vmcnt(%0)" :: "i"(4 + (KV_T - 4) / 2) : "memory");
                __builtin_amdgcn_raw_buffer_store_b64(tag, rw, lane == 0 ? F_INIT + (33 + 4 * h + w) * 128 : OOB, 0, 16);
                if (!__all(((in.x ^ tag.x) >> 1) == 0 && in.y == tag.y)) wait_init(rw, tag, budget);
            }
            if (ks >= 5 && ks < 13 && (ks & 1)) put(o, s - 1, (ks - 5) >> 1);
            if (ks == KV_F) { VM_DRAIN(); st32((unsigned)(s - 1), rw, s > 1 ? fo : OOB, 0); }
            if (ks == 0) asm volatile("v_mfma_f32_32x32x16_bf16 %0, %2, %1, 0" : "=&v"(v) : "a"(W1.f[0]), "v"(xf[0]) : "memory");
            else if (ks < 28) asm volatile("v_mfma_f32_32x32x16_bf16 %0, %2, %1, %0" : "+v"(v) : "a"(W1.f[ks]), "v"(xf[ks % 6]) : "memory");
            else asm volatile("v_mfma_f32_32x32x16_bf16 %0, %2, %1, %0" : "+v"(v) : "v"(W1.f[ks]), "v"(xf[ks % 6]) : "memory");
            if (ks == 30) WG_BARRIER();
            frag(buf ^ (ks >= 30), (ks + 6) % 36);
            if (ks < 4) X.dma(a, lds_base, w, s + 1, buf ^ 1, ks + 5);
            if (ks > 30) X.dma(a, lds_base, w, s + 2, buf, ks - 31);
        }
    };
#pragma unroll
    for (int i = 0; i < 6; ++i) frag(0, i);
#pragma unroll
    for (int i = 0; i < 5; ++i) X.dma(a, lds_base, w, 1, 1, i);
    f32x16 k0, v0, k1 = {}, v1 = {};
    for (int s = 0; s < NSEG; s += 2) {
        seg(k0, v0, k1, v1, s, 0);
        if (s == 0) { in = ld64(rw, F_INIT + (lane & 31) * 128, 0); now_(k0, v0, 0); }
        seg(k1, v1, k0, v0, s + 1, 1);
    }
    now_(k1, v1, NSEG - 1);
    VM_DRAIN();
    st32(NSEG, rw, fo, 0);
}

DI int prog_min(rsrc_t rw, int off) {
    const int v = (int)ld32(rw, off, 0);
    asm volatile("" ::: "memory");
    return -wave_max32(-v);
}
DI float red_max_g(float v) {
    auto a = __builtin_amdgcn_permlane16_swap(__float_as_uint(v), __float_as_uint(v), false, false);
    v = fmaxf(__uint_as_float(a[0]), __uint_as_float(a[1]));
    auto b = __builtin_amdgcn_permlane32_swap(__float_as_uint(v), __float_as_uint(v), false, false);
    return fmaxf(__uint_as_float(b[0]), __uint_as_float(b[1]));
}
#define M16 "v_mfma_f32_16x16x32_bf16 "
DI void role_qa(const Args& a, LAS char* lds, unsigned lds_base, int h0, int s0, int s1, rsrc_t rw, int& budget) {
    const int tid = threadIdx.x, lane = tid & 63, w = __builtin_amdgcn_readfirstlane(tid >> 6);
    const int n = lane & 15, g = lane >> 4, h = h0 + (w >> 1), tt = w & 1;
    WSlice W0, W1;
    XTile X;
    load_wslices(W0, W1, a.Wq + h0 * DH, a.Wq + (h0 + 1) * DH, w, lane, lds, lds_base, [&]() { X.first(a, lds_base, w, lane, s0); });
    const int oo = OFF_OF + (2 * tt + (n >> 3)) * 65536 + h * 2048 + g * 128 + (n & 7) * 16;
    const bool fast = s0 >= FAST;
    if (fast)
#pragma unroll
        for (int t = 0; t < 8; ++t) st128((u32x4){SENT, SENT, SENT, SENT}, rw, oo + (t & 3) * 512, (s0 + (t >> 2) * 2) * 262144);
    VM_DRAIN();
    WG_BARRIER();
    constexpr int Q_Q = P_STG, Q_KV = Q_Q + 16384, Q_M = Q_KV + 32768;
    static_assert(Q_M + 16384 <= P_STG + P_RING * 16384, "");
    const int fk = F_KV + h * 512 + (lane & 3) * 128, fm = F_KVM + h * 256 + (lane & 1) * 128;
    int pm_, m_rdy = -1, kv_rdy = -1;
    auto look = [&]() { pm_ = (int)ld32(rw, lane & 4 ? fk : fm, 0); };
    auto seen = [&]() { auto rl = [&](int i) { return __builtin_amdgcn_readlane(pm_, i); }; m_rdy = min(rl(0), rl(1)); kv_rdy = min(min(rl(4), rl(5)), min(rl(6), rl(7))); };
    auto wait_m = [&](int thr, unsigned& t) {
        for (;;) {
            const u32x2 v = ld64(rw, fm, 0);
            asm volatile("" ::: "memory");
            t = now();
            const int p0 = __builtin_amdgcn_readlane(v.x, 0), p1 = __builtin_amdgcn_readlane(v.x, 1);
            const int d0 = (int)(__builtin_amdgcn_readlane(v.y, 0) - 8 * t) >> 3, d1 = (int)(__builtin_amdgcn_readlane(v.y, 1) - 8 * t) >> 3;
            if (min(p0, p1) >= thr || --budget < 0) return max(p0 == thr ? d0 : -1000, p1 == thr ? d1 : -1000);
            (void)wait_until(t + min(max(min(d0, d1) + (int)(a.xa >> 16 & 255), 10), 200));
        }
    };
    auto proj = [&](int s) {
        f32x16 acc0, acc1;
        project<false>(acc0, acc1, W0, W1, X, a, lds, lds_base, w, ((s - s0) >> 1) & 1, s + 2, look);
        const int o = Q_Q + w * 2048 + lane * 16;
        *(LAS u32x4*)(lds + o) = piece_pi(acc0, 0); *(LAS u32x4*)(lds + o + 1024) = piece_pi(acc0, 1);
        *(LAS u32x4*)(lds + o + 8192) = piece_pi(acc1, 0); *(LAS u32x4*)(lds + o + 8192 + 1024) = piece_pi(acc1, 1);
    };
    const int fs = (tt ? OFF_VF : OFF_KF) + (g >> 1) * 1024 + ((g & 1) * 32 + 16 * (n >> 3) + (n & 7)) * 16;
    auto fetch = [&](int s) {
        const int so = (min(s, NSEG - 1) * NH + h) * 8192;
#pragma unroll
        for (int f = 0; f < 8; ++f) bdma(rw, fs, so + (f >> 1) * 2048 + (f & 1) * 128, lds_base + Q_KV + w * 8192 + f * 1024);
    };
    const int ms = OFF_KM + (lane & 7) * 256 + (lane >> 3) * 16;
    auto fetch_m = [&](int s) {
        const int so = (s * NH + h) * 2048;
        const unsigned d = lds_base + Q_M + w * 4096;
        bdma(rw, ms, so, d); bdma(rw, ms, so + 128, d + 1024); bdma(rw, lane * 16, OFF_VM + so, d + 2048); bdma(rw, lane * 16, OFF_VM + so + 1024, d + 3072);
    };
    const float c = 0.08838834764831845f * 1.4426950408889634f;
    u32x4 qf[4];
    f32x4 o[8]; float mx, l;
    auto xpart = [&]() {
        const int kb = Q_KV + (w >> 1) * 16384 + lane * 16;
        u32x4 kn[8], vn[8];
#pragma unroll
        for (int ks = 0; ks < 4; ++ks) qf[ks] = *(LAS u32x4*)(lds + Q_Q + (w >> 1) * 8192 + (2 * ks + (g >> 1)) * 1024 + ((g & 1) * 32 + 16 * tt + n) * 16);
#pragma unroll
        for (int f = 0; f < 8; ++f) kn[f] = *(LAS u32x4*)(lds + kb + f * 1024);
        FENCE();
#pragma unroll
        for (int f = 0; f < 8; ++f) vn[f] = *(LAS u32x4*)(lds + kb + 8192 + f * 1024);
        f32x4 s0, s1;
        asm volatile(M16 "%0, %2, %10, 0\n\t" M16 "%1, %3, %10, 0\n\t" M16 "%0, %4, %11, %0\n\t" M16 "%1, %5, %11, %1\n\t"
                     M16 "%0, %6, %12, %0\n\t" M16 "%1, %7, %12, %1\n\t" M16 "%0, %8, %13, %0\n\t" M16 "%1, %9, %13, %1\n\ts_nop 7"
                     : "=&v"(s0), "=&v"(s1) : "v"(kn[0]), "v"(kn[1]), "v"(kn[2]), "v"(kn[3]), "v"(kn[4]), "v"(kn[5]), "v"(kn[6]), "v"(kn[7]), "v"(qf[0]), "v"(qf[1]), "v"(qf[2]), "v"(qf[3]));
        float p[8];
        mx = -3.0e38f; l = 0.f;
#pragma unroll
        for (int r = 0; r < 8; ++r) { const int key = 16 * (g >> 1) + 4 * (g & 1) + (r & 3) + 8 * (r >> 2); p[r] = key <= 16 * tt + n ? (r < 4 ? s0[r & 3] : s1[r & 3]) : -3.0e38f; mx = fmaxf(mx, p[r]); }
        mx = red_max_g(mx);
#pragma unroll
        for (int r = 0; r < 8; ++r) { p[r] = __builtin_amdgcn_exp2f((p[r] - mx) * c); l += p[r]; }
        const u32x4 pf = {pack2(p[0], p[1]), pack2(p[2], p[3]), pack2(p[4], p[5]), pack2(p[6], p[7])};
        asm volatile("s_nop 1\n\t" M16 "%0, %8, %16, 0\n\t" M16 "%1, %9, %16, 0\n\t" M16 "%2, %10, %16, 0\n\t" M16 "%3, %11, %16, 0\n\t"
                     M16 "%4, %12, %16, 0\n\t" M16 "%5, %13, %16, 0\n\t" M16 "%6, %14, %16, 0\n\t" M16 "%7, %15, %16, 0\n\ts_nop 7"
                     : "=&v"(o[0]), "=&v"(o[1]), "=&v"(o[2]), "=&v"(o[3]), "=&v"(o[4]), "=&v"(o[5]), "=&v"(o[6]), "=&v"(o[7])
                     : "v"(vn[0]), "v"(vn[1]), "v"(vn[2]), "v"(vn[3]), "v"(vn[4]), "v"(vn[5]), "v"(vn[6]), "v"(vn[7]), "v"(pf));
    };
    proj(s0);
    if (!fast) wait_init(rw, launch_tag(a), budget);
    wait_prog(rw, fk, true, fast ? s1 : s0, budget);
    fetch(s0);
    VM_DRAIN();
    WG_BARRIER();
    xpart();
    WG_BARRIER();
    proj(s0 + 2);
    const int fo = lane == 0 ? F_O + h * 128 + (2 * s0 + tt) * 4 : OOB;
    bool pre = false;
    for (int s = s0; s <= s1; s += 2) {
        S4S(0)
        const int mb = Q_M + w * 4096;
        u32x4 kf[4]; u32x2 vm[4];
        auto frags = [&]() {
#pragma unroll
            for (int ks = 0; ks < 4; ++ks) kf[ks] = *(LAS u32x4*)(lds + mb + (4 * ks + g) * 128 + (2 * (n >> 2) + (n & 1)) * 16);
#pragma unroll
            for (int t = 0; t < 4; ++t) { const int vo = mb + 2048 + t * 512 + (n >> 3) * 256 + g * 64 + (n & 7) * 4; vm[t] = (u32x2){*(LAS unsigned*)(lds + vo), *(LAS unsigned*)(lds + vo + 32)}; }
        };
        if (fast) {
            TT(s == s0, 3)
            if (s < s1) fetch(s1);
            unsigned t;
            const int d = min(wait_m(min(s - 1, NSEG - 3), t) + (int)(a.xa >> (s == NSEG - 1 ? 8 : 0) & 255), 500);
            st32((t + max(d, 0) + (a.xb >> 8 * (s - FAST) & 255)) & 0x7FFFFFFFu, rw, lane == 0 ? F_H + ((2 * (s - FAST) + tt) * NH + h) * 128 : OOB, 0);
            (void)wait_until(t + d);
            TT(true, 0)
            for (;;) {
                fetch_m(s);
                VM_DRAIN();
                frags();
                unsigned c = 0;
#pragma unroll
                for (int i = 0; i < 4; ++i) c = max(max(mx4(c, kf[i]), vm[i].x), vm[i].y);
                if (!__any(c == SENT) || --budget < 0) break;
            }
        } else {
            if (!pre) {
                seen();
                unsigned t;
                if (m_rdy < s) { const int d = wait_m(s, t); (void)wait_until(t + min(d + (int)(a.xa >> 24), 300)); }
                fetch_m(s);
                if (kv_rdy < s + 2) wait_prog(rw, fk, true, s + 2, budget);
                fetch(s + 2);
            }
            S4S(1)
            asm volatile("s_waitcnt vmcnt(8)" ::: "memory");
            frags();
        }
        TT(s == s1, 1)
        S4S(2)
        f32x4 sm;
        asm volatile(M16 "%0, %1, %5, 0\n\t" M16 "%0, %2, %6, %0\n\t" M16 "%0, %3, %7, %0\n\t" M16 "%0, %4, %8, %0\n\ts_nop 7"
                     : "=&v"(sm) : "v"(kf[0]), "v"(kf[1]), "v"(kf[2]), "v"(kf[3]), "v"(qf[0]), "v"(qf[1]), "v"(qf[2]), "v"(qf[3]));
        const float m2 = red_max_g(fmaxf(mx, fmaxf(sm[0], sm[1])));
        const float al = __builtin_amdgcn_exp2f((mx - m2) * c), p0 = __builtin_amdgcn_exp2f((sm[0] - m2) * c), p1 = __builtin_amdgcn_exp2f((sm[1] - m2) * c);
        const float inv = __builtin_amdgcn_rcpf(red_sum_g(l * al + p0 + p1)), ai = al * inv;
#pragma unroll
        for (int t = 0; t < 8; ++t) o[t] *= ai;
        const unsigned pm = pack2(p0 * inv, p1 * inv);
#define M1K "v_mfma_f32_16x16x16_bf16 "
        asm volatile("s_nop 1\n\t" M1K "%0, %8, %12, %0\n\t" M1K "%1, %8, %13, %1\n\t" M1K "%2, %9, %12, %2\n\t" M1K "%3, %9, %13, %3\n\t"
                     M1K "%4, %10, %12, %4\n\t" M1K "%5, %10, %13, %5\n\t" M1K "%6, %11, %12, %6\n\t" M1K "%7, %11, %13, %7\n\ts_nop 7"
                     : "+v"(o[0]), "+v"(o[1]), "+v"(o[2]), "+v"(o[3]), "+v"(o[4]), "+v"(o[5]), "+v"(o[6]), "+v"(o[7])
                     : "v"(vm[0]), "v"(vm[1]), "v"(vm[2]), "v"(vm[3]), "v"((u32x2){pm, 0u}), "v"((u32x2){0u, pm}));
#pragma unroll
        for (int b = 0; b < 2; ++b) {
            u32x4 x, y;
#pragma unroll
            for (int j = 0; j < 4; ++j) { x[j] = pack2(o[4 * b + (j >> 1)][2 * (j & 1)], o[4 * b + (j >> 1)][2 * (j & 1) + 1]); y[j] = pack2(o[4 * b + 2 + (j >> 1)][2 * (j & 1)], o[4 * b + 2 + (j >> 1)][2 * (j & 1) + 1]); }
            if (s == NSEG - 1) { const u32x4 z = {x.x, x.z, y.x, y.z}; y = (u32x4){x.y, x.w, y.y, y.w}; x = z; }
            st128(x, rw, oo + b * 1024, s * 262144); st128(y, rw, oo + b * 1024 + 512, s * 262144);
        }
        TT(s == s1, 2)
        S4S(3)
        if (s < s1) {
            asm volatile("s_waitcnt vmcnt(4)" ::: "memory");
            WG_BARRIER();
            S4S(4)
            look();
            xpart();
        }
        TT(s == s0, 4)
        if (fast) continue;
        FENCE();
        S4S(5)
        seen();
        VM_DRAIN();
        st32((unsigned)s, rw, fo, 0);
        TT(s == s1, 3)
        TT(s == s1 - 2, 4)
        S4S(6)
        WG_BARRIER();
        pre = m_rdy >= s + 2 && kv_rdy >= s + 4 && s < s1;
        if (pre) { fetch_m(s + 2); fetch(s + 4); }
        S4S(7)
        if (s + 4 <= s1) proj(s + 4);
        S4S(8)
    }
}

#define STEP_ASM \
    "v_readfirstlane_b32 s36, v0\n" \
    "v_readfirstlane_b32 s37, v1\n" \
    "v_readfirstlane_b32 s38, v2\n" \
    "v_readfirstlane_b32 s39, v3\n" \
    "v_readfirstlane_b32 s40, v4\n" \
    "v_readfirstlane_b32 s41, v5\n" \
    "v_readfirstlane_b32 s42, v6\n" \
    "v_readfirstlane_b32 s43, v7\n" \
    "v_readfirstlane_b32 s28, v8\n" \
    "v_readfirstlane_b32 s29, v9\n" \
    "v_readfirstlane_b32 s30, v10\n" \
    "v_readfirstlane_b32 s31, v11\n" \
    "v_readfirstlane_b32 s32, v12\n" \
    "v_readfirstlane_b32 s33, v13\n" \
    "v_readfirstlane_b32 s34, v14\n" \
    "v_readfirstlane_b32 s35, v15\n" \
    "v_readfirstlane_b32 s24, v16\n" \
    "v_readfirstlane_b32 s25, v17\n" \
    "v_readfirstlane_b32 s26, v18\n" \
    "v_readfirstlane_b32 s27, v19\n" \
    "v_readfirstlane_b32 s60, v24\n" \
    "v_readfirstlane_b32 s61, v25\n" \
    "v_readfirstlane_b32 s62, v26\n" \
    "v_readfirstlane_b32 s63, v27\n" \
    "v_readfirstlane_b32 s64, v28\n" \
    "v_readfirstlane_b32 s65, v29\n" \
    "v_readfirstlane_b32 s66, v30\n" \
    "v_readfirstlane_b32 s67, v31\n" \
    "s_mov_b32 s68, m0\n" \
    "s_getpc_b64 s[96:97]\n" \
    "14:\n" \
    "s_add_u32 s96, s96, 2f-14b\n" \
    "s_addc_u32 s97, s97, 0\n" \
    "s_lshl_b32 s74, s60, 10\n" \
    "s_lshl_b32 s95, s74, 1\n" \
    "s_add_i32 s95, s95, 16384\n" \
    "s_mov_b32 s81, 0x3e0293ee\n" \
    "s_mov_b32 s82, 0x53800000\n" \
    "s_lshl_b32 s83, s60, 6\n" \
    "s_and_b32 s83, s83, 128\n" \
    "v_add_u32 v109, s83, v131\n" \
    "v_sub_u32 v111, v131, s83\n" \
    "v_xor_b32 v112, 0x2000, v133\n" \
    "v_and_b32 v113, 0x3c0, v127\n" \
    "v_mad_u32_u24 v113, v113, 7, v127\n" \
    "s_bfe_u32 s83, s61, 0x2000b\n" \
    "s_and_b32 s83, s83, 3\n" \
    "v_lshl_add_u32 v113, s83, 7, v113\n" \
    "v_lshlrev_b32 v114, 1, v127\n" \
    "v_add_u32 v106, s63, v133\n" \
    "s_bfe_u32 s83, s61, 0x40009\n" \
    "s_bfe_u32 s80, s61, 0x20004\n" \
    "s_lshl2_add_u32 s83, s83, s80\n" \
    "s_mov_b64 s[72:73], 0\n" \
    "0:\n" \
    "s_and_b32 s75, s36, 3\n" \
    "s_lshl_b32 s75, s75, 17\n" \
    "s_add_i32 s75, s75, s26\n" \
    "s_add_i32 s69, s75, s74\n" \
    "s_add_i32 s70, s69, 4096\n" \
    "s_add_i32 s71, s75, 16384\n" \
    "12:\n" \
    "ds_read_b128 v[72:75], v133 offset:0\n" \
    "ds_read_b128 v[76:79], v133 offset:1024\n" \
    "ds_read_b128 v[80:83], v133 offset:2048\n" \
    "ds_read_b128 v[84:87], v133 offset:3072\n" \
    "ds_read_b128 v[88:91], v133 offset:16384\n" \
    "ds_read_b128 v[92:95], v133 offset:17408\n" \
    "s_add_i32 s91, s36, 1\n" \
    "s_and_b32 s78, s36, 3\n" \
    "s_lshl_b32 s78, s78, 14\n" \
    "s_add_i32 s77, s78, s61\n" \
    "s_bitcmp1_b32 s36, 0\n" \
    "s_cselect_b32 s79, 0x90000, 0\n" \
    "s_lshl_b32 s92, s91, 18\n" \
    "s_add_i32 s92, s92, s64\n" \
    "s_movk_i32 m0, 0x2000\n" \
    "s_add_i32 s78, s78, 0x1a0000\n" \
    "s_add_i32 s93, s92, 0x1000000\n" \
    "v_mov_b64 v[40:41], 0\n" \
    "v_mov_b64 v[42:43], 0\n" \
    "v_mov_b64 v[44:45], 0\n" \
    "v_mov_b64 v[46:47], 0\n" \
    "v_mov_b64 v[48:49], 0\n" \
    "v_mov_b64 v[50:51], 0\n" \
    "v_mov_b64 v[52:53], 0\n" \
    "v_mov_b64 v[54:55], 0\n" \
    "v_mov_b64 v[56:57], 0\n" \
    "v_mov_b64 v[58:59], 0\n" \
    "v_mov_b64 v[60:61], 0\n" \
    "v_mov_b64 v[62:63], 0\n" \
    "v_mov_b64 v[64:65], 0\n" \
    "v_mov_b64 v[66:67], 0\n" \
    "v_mov_b64 v[68:69], 0\n" \
    "v_mov_b64 v[70:71], 0\n" \
    "v_add_u32 v108, s74, v127\n" \
    "v_xor_b32 v24, 0x400, v108\n" \
    "v_xor_b32 v28, 0x800, v108\n" \
    "v_xor_b32 v32, 0xc00, v108\n" \
    "s_sub_i32 s76, s33, s42\n" \
    "s_ashr_i32 s76, s76, s32\n" \
    "s_add_i32 s42, s42, s76\n" \
    "s_lshr_b32 s76, s32, 8\n" \
    "s_sub_i32 s76, s76, s40\n" \
    "s_ashr_i32 s76, s76, 2\n" \
    "s_add_i32 s40, s40, s76\n" \
    "s_sub_i32 s76, s40, s24\n" \
    "s_min_i32 s94, s91, s38\n" \
    "s_lshl_b32 s94, s94, 18\n" \
    "s_andn2_b32 s80, 0x1000000, s66\n" \
    "s_add_i32 s94, s94, s64\n" \
    "s_add_i32 s94, s94, s80\n" \
    "s_cmp_eq_u32 s36, 0\n" \
    "s_cbranch_scc1 17f\n" \
    "s_sub_i32 s90, s37, s29\n" \
    "s_mul_i32 s90, s90, s25\n" \
    "s_ashr_i32 s90, s90, 12\n" \
    "s_add_i32 s90, s90, s24\n" \
    "s_add_i32 s90, s90, -116\n" \
    "1:\n" \
    "s_memtime vcc\n" \
    "s_waitcnt lgkmcnt(0)\n" \
    "s_sub_i32 s75, s90, vcc_lo\n" \
    "s_cmp_gt_i32 s75, 112\n" \
    "s_cbranch_scc1 1b\n" \
    "s_max_i32 s75, s75, 0\n" \
    "s_and_b32 s75, s75, -4\n" \
    "s_sub_u32 vcc_lo, s96, s75\n" \
    "s_subb_u32 vcc_hi, s97, 0\n" \
    "s_setpc_b64 vcc\n" \
    "s_nop 0\n" \
    "s_nop 0\n" \
    "s_nop 0\n" \
    "s_nop 0\n" \
    "s_nop 0\n" \
    "s_nop 0\n" \
    "s_nop 0\n" \
    "s_nop 0\n" \
    "s_nop 0\n" \
    "s_nop 0\n" \
    "s_nop 0\n" \
    "s_nop 0\n" \
    "s_nop 0\n" \
    "s_nop 0\n" \
    "s_nop 0\n" \
    "s_nop 0\n" \
    "s_nop 0\n" \
    "s_nop 0\n" \
    "s_nop 0\n" \
    "s_nop 0\n" \
    "s_nop 0\n" \
    "s_nop 0\n" \
    "s_nop 0\n" \
    "s_nop 0\n" \
    "s_nop 0\n" \
    "s_nop 0\n" \
    "s_nop 0\n" \
    "s_nop 0\n" \
    "2:\n" \
    "buffer_load_dwordx4 v[0:3], v125, %[rs], s69 offen sc1\n" \
    "s_nop 3\n" \
    "s_nop 3\n" \
    "buffer_load_dwordx4 v[16:19], v125, %[rs], s70 offen sc1\n" \
    "s_mov_b32 exec_lo, s27\n" \
    "s_mov_b32 exec_hi, s27\n" \
    "buffer_load_dwordx4 v127, %[rs], s71 offen sc1 lds\n" \
    "s_mov_b64 exec, -1\n" \
    "v_bfe_u32 v20, v127, 8, 2\n" \
    "v_subrev_u32 v20, s60, v20\n" \
    "v_mul_i32_i24 v20, -1088, v20\n" \
    "v_add_u32 v20, v20, v129\n" \
    "v_lshrrev_b32 v21, 2, v127\n" \
    "s_mov_b32 exec_lo, s66\n" \
    "s_mov_b32 exec_hi, s66\n" \
    "ds_read2_b32 v[96:97], v20 offset0:0 offset1:144\n" \
    "s_mov_b64 exec, -1\n" \
    "s_lshl_b32 s80, s36, 16\n" \
    "s_add_i32 s80, s80, s67\n" \
    "s_cmp_eq_u32 s60, 0\n" \
    "s_cselect_b32 s72, 0x1010101, 0\n" \
    "s_waitcnt lgkmcnt(0)\n" \
    "buffer_store_dword v96, v21, %[rw], s80 offen sc1\n" \
    "buffer_store_dword v97, v21, %[rw], s80 offen offset:256 sc1\n" \
    "3:\n" \
    "s_mov_b32 m0, s95\n" \
    "s_add_i32 s75, s36, 2\n" \
    "s_cmp_lt_i32 s38, s75\n" \
    "s_cselect_b32 s75, s62, 0x1b0040\n" \
    "v_mov_b64 v[98:99], 0\n" \
    "v_mov_b32 v101, 0\n" \
    "s_pack_ll_b32_b16 s80, s91, s42\n" \
    "v_mov_b32 v103, s80\n" \
    "s_waitcnt vmcnt(4)\n" \
    "v_smfmac_f32_16x16x64_bf16 v[40:43], v[0:3], a[0:7], v124\n" \
    "ds_write_b128 v108, v[0:3] offset:0\n" \
    "v_smfmac_f32_16x16x64_bf16 v[44:47], v[0:3], a[8:15], v124\n" \
    "v_smfmac_f32_16x16x64_bf16 v[48:51], v[0:3], a[128:135], v124\n" \
    "v_smfmac_f32_16x16x64_bf16 v[52:55], v[0:3], a[136:143], v124\n" \
    "s_waitcnt vmcnt(3)\n" \
    "v_smfmac_f32_16x16x64_bf16 v[40:43], v[16:19], a[64:71], v124\n" \
    "ds_write_b128 v108, v[16:19] offset:4096\n" \
    "v_smfmac_f32_16x16x64_bf16 v[44:47], v[16:19], a[72:79], v124\n" \
    "s_waitcnt lgkmcnt(1)\n" \
    "s_barrier\n" \
    "v_smfmac_f32_16x16x64_bf16 v[48:51], v[16:19], a[192:199], v124\n" \
    "ds_read_b128 v[4:7], v24 offset:0\n" \
    "v_smfmac_f32_16x16x64_bf16 v[52:55], v[16:19], a[200:207], v124\n" \
    "ds_read_b128 v[8:11], v28 offset:0\n" \
    "v_smfmac_f32_16x16x64_bf16 v[56:59], v[0:3], v[72:79], v124\n" \
    "ds_read_b128 v[12:15], v32 offset:0\n" \
    "v_smfmac_f32_16x16x64_bf16 v[60:63], v[0:3], v[80:87], v124\n" \
    "s_waitcnt vmcnt(2) lgkmcnt(3)\n" \
    "s_barrier\n" \
    "s_memtime s[86:87]\n" \
    "v_smfmac_f32_16x16x64_bf16 v[56:59], v[16:19], v[168:175], v124\n" \
    "ds_read_b128 v[24:27], v24 offset:4096\n" \
    "v_smfmac_f32_16x16x64_bf16 v[60:63], v[16:19], v[176:183], v124\n" \
    "ds_read_b128 v[28:31], v28 offset:4096\n" \
    "s_waitcnt lgkmcnt(3)\n" \
    "v_smfmac_f32_16x16x64_bf16 v[40:43], v[4:7], a[16:23], v124\n" \
    "ds_read_b128 v[32:35], v32 offset:4096\n" \
    "v_smfmac_f32_16x16x64_bf16 v[40:43], v[8:11], a[32:39], v124\n" \
    "ds_read_b128 v[36:39], v126 offset:0\n" \
    "s_waitcnt lgkmcnt(4)\n" \
    "v_smfmac_f32_16x16x64_bf16 v[40:43], v[12:15], a[48:55], v124\n" \
    "buffer_load_dword v110, v114, %[rw], s94 offen sc1\n" \
    "v_smfmac_f32_16x16x64_bf16 v[44:47], v[4:7], a[24:31], v124\n" \
    "s_waitcnt lgkmcnt(2)\n" \
    "v_smfmac_f32_16x16x64_bf16 v[40:43], v[24:27], a[80:87], v124\n" \
    "v_smfmac_f32_16x16x64_bf16 v[40:43], v[28:31], a[96:103], v124\n" \
    "s_waitcnt lgkmcnt(0)\n" \
    "v_smfmac_f32_16x16x64_bf16 v[40:43], v[32:35], a[112:119], v124\n" \
    "ds_read_b128 v[72:75], v133 offset:18432\n" \
    "s_add_i32 s80, s86, s76\n" \
    "v_smfmac_f32_16x16x64_bf16 v[40:43], v[36:39], v[136:143], v124\n" \
    "ds_read_b128 v[76:79], v133 offset:19456\n" \
    "s_mul_hi_u32 s80, s80, s41\n" \
    "v_smfmac_f32_16x16x64_bf16 v[44:47], v[8:11], a[40:47], v124\n" \
    "ds_read_b128 v[80:83], v133 offset:32768\n" \
    "s_add_i32 s80, s80, s29\n" \
    "v_mov_b32 v102, s80\n" \
    "v_smfmac_f32_16x16x64_bf16 v[44:47], v[12:15], a[56:63], v124\n" \
    "ds_read_b128 v[84:87], v133 offset:33792\n" \
    "v_add_f32 v40, v40, v42\n" \
    "v_add_f32 v41, v41, v43\n" \
    "v_smfmac_f32_16x16x64_bf16 v[44:47], v[24:27], a[88:95], v124\n" \
    "ds_read_b128 v[20:23], v127 offset:19456\n" \
    "v_cmp_u_f32 vcc, v40, v41\n" \
    "v_smfmac_f32_16x16x64_bf16 v[44:47], v[28:31], a[104:111], v124\n" \
    "v_mul_f32 v40, s81, v40\n" \
    "v_smfmac_f32_16x16x64_bf16 v[44:47], v[32:35], a[120:127], v124\n" \
    "s_cbranch_vccnz 5f\n" \
    "6:\n" \
    "s_mov_b64 exec, s[72:73]\n" \
    "v_smfmac_f32_16x16x64_bf16 v[44:47], v[36:39], v[144:151], v124\n" \
    "buffer_store_dwordx2 v[102:103], v127, %[rs], s77 offen sc1\n" \
    "v_smfmac_f32_16x16x64_bf16 v[56:59], v[4:7], v[88:95], v124\n" \
    "s_mov_b64 exec, -1\n" \
    "s_waitcnt lgkmcnt(1)\n" \
    "v_mul_f32 v41, s81, v41\n" \
    "v_smfmac_f32_16x16x64_bf16 v[60:63], v[4:7], v[72:79], v124\n" \
    "ds_read_b128 v[88:91], v133 offset:34816\n" \
    "v_add_f32 v44, v44, v46\n" \
    "v_smfmac_f32_16x16x64_bf16 v[56:59], v[8:11], v[80:87], v124\n" \
    "ds_read_b128 v[92:95], v133 offset:35840\n" \
    "v_add_f32 v45, v45, v47\n" \
    "v_smfmac_f32_16x16x64_bf16 v[48:51], v[4:7], a[144:151], v124\n" \
    "ds_read_b128 v[72:75], v133 offset:49152\n" \
    "v_mul_f32 v44, s81, v44\n" \
    "v_smfmac_f32_16x16x64_bf16 v[52:55], v[4:7], a[152:159], v124\n" \
    "ds_read_b128 v[76:79], v133 offset:50176\n" \
    "v_mul_f32 v45, s81, v45\n" \
    "v_smfmac_f32_16x16x64_bf16 v[48:51], v[8:11], a[160:167], v124\n" \
    "ds_read_b128 v[0:3], v127 offset:16384\n" \
    "v_cvt_pk_bf16_f32 v107, v40, v44\n" \
    "v_smfmac_f32_16x16x64_bf16 v[52:55], v[8:11], a[168:175], v124\n" \
    "ds_read_b128 v[4:7], v127 offset:18432\n" \
    "s_waitcnt lgkmcnt(4)\n" \
    "v_cvt_pk_bf16_f32 v108, v41, v45\n" \
    "v_smfmac_f32_16x16x64_bf16 v[60:63], v[8:11], v[88:95], v124\n" \
    "ds_write2_b32 v128, v107, v108 offset0:0 offset1:80\n" \
    "v_smfmac_f32_16x16x64_bf16 v[60:63], v[12:15], v[248:255], v124\n" \
    "ds_read_b128 v[16:19], v127 offset:17408\n" \
    "s_waitcnt lgkmcnt(4)\n" \
    "v_smfmac_f32_16x16x64_bf16 v[56:59], v[12:15], v[72:79], v124\n" \
    "ds_read_b128 v[8:11], v127 offset:20480\n" \
    "v_smfmac_f32_16x16x64_bf16 v[48:51], v[12:15], a[176:183], v124\n" \
    "ds_read_b128 v[40:43], v127 offset:21504\n" \
    "v_smfmac_f32_16x16x64_bf16 v[52:55], v[12:15], a[184:191], v124\n" \
    "ds_read_b128 v[44:47], v127 offset:23552\n" \
    "v_smfmac_f32_16x16x64_bf16 v[48:51], v[24:27], a[208:215], v124\n" \
    "s_waitcnt lgkmcnt(4)\n" \
    "s_barrier\n" \
    "v_smfmac_f32_16x16x64_bf16 v[52:55], v[24:27], a[216:223], v124\n" \
    "ds_read_b128 v[72:75], v130 offset:0\n" \
    "v_smfmac_f32_16x16x64_bf16 v[48:51], v[28:31], a[224:231], v124\n" \
    "ds_read_b128 v[76:79], v130 offset:64\n" \
    "v_smfmac_f32_16x16x64_bf16 v[48:51], v[32:35], a[240:247], v124\n" \
    "ds_read_b128 v[80:83], v130 offset:128\n" \
    "v_smfmac_f32_16x16x64_bf16 v[48:51], v[36:39], v[152:159], v124\n" \
    "ds_read_b128 v[84:87], v130 offset:192\n" \
    "v_smfmac_f32_16x16x64_bf16 v[52:55], v[28:31], a[232:239], v124\n" \
    "ds_read_b128 v[12:15], v127 offset:22528\n" \
    "v_smfmac_f32_16x16x64_bf16 v[52:55], v[32:35], a[248:255], v124\n" \
    "v_add_f32 v48, v48, v50\n" \
    "v_smfmac_f32_16x16x64_bf16 v[52:55], v[36:39], v[160:167], v124\n" \
    "s_waitcnt lgkmcnt(3)\n" \
    "v_add_f32 v49, v49, v51\n" \
    "v_mfma_f32_16x16x32_bf16 v[88:91], v[0:3], v[72:75], 0\n" \
    "ds_read_b64 v[0:1], v132 offset:0\n" \
    "ds_read_b64 v[2:3], v132 offset:1024\n" \
    "v_mfma_f32_16x16x32_bf16 v[88:91], v[4:7], v[76:79], v[88:91]\n" \
    "s_waitcnt lgkmcnt(2)\n" \
    "v_add_f32 v52, v52, v54\n" \
    "v_add_f32 v53, v53, v55\n" \
    "v_mfma_f32_16x16x32_bf16 v[88:91], v[8:11], v[80:83], v[88:91]\n" \
    "v_cvt_pk_bf16_f32 v107, v48, v52\n" \
    "v_cvt_pk_bf16_f32 v108, v49, v53\n" \
    "v_mfma_f32_16x16x32_bf16 v[88:91], v[12:15], v[84:87], v[88:91]\n" \
    "ds_write2_b32 v129, v107, v108 offset0:0 offset1:144\n" \
    "v_mfma_f32_16x16x32_bf16 v[92:95], v[16:19], v[72:75], 0\n" \
    "ds_read_b64 v[4:5], v132 offset:512\n" \
    "ds_read_b64 v[6:7], v132 offset:1536\n" \
    "v_mfma_f32_16x16x32_bf16 v[92:95], v[20:23], v[76:79], v[92:95]\n" \
    "ds_read_b128 v[8:11], v106 offset:0\n" \
    "v_exp_f32 v88, v88\n" \
    "v_mfma_f32_16x16x32_bf16 v[92:95], v[40:43], v[80:83], v[92:95]\n" \
    "ds_read_b128 v[12:15], v106 offset:1024\n" \
    "v_exp_f32 v89, v89\n" \
    "v_mfma_f32_16x16x32_bf16 v[92:95], v[44:47], v[84:87], v[92:95]\n" \
    "s_waitcnt lgkmcnt(4)\n" \
    "s_barrier\n" \
    "v_smfmac_f32_16x16x64_bf16 v[56:59], v[24:27], v[184:191], v124\n" \
    "ds_read_b128 v[48:51], v131 offset:0\n" \
    "ds_read_b128 v[52:55], v131 offset:128\n" \
    "v_cvt_pk_bf16_f32 v88, v88, v89\n" \
    "v_smfmac_f32_16x16x64_bf16 v[56:59], v[28:31], v[200:207], v124\n" \
    "ds_read_b128 v[16:19], v106 offset:32768\n" \
    "v_exp_f32 v90, v90\n" \
    "v_smfmac_f32_16x16x64_bf16 v[56:59], v[32:35], v[216:223], v124\n" \
    "ds_read_b128 v[20:23], v106 offset:33792\n" \
    "v_exp_f32 v91, v91\n" \
    "v_smfmac_f32_16x16x64_bf16 v[56:59], v[36:39], v[232:239], v124\n" \
    "ds_read_b128 v[40:43], v106 offset:2048\n" \
    "s_waitcnt lgkmcnt(3)\n" \
    "v_exp_f32 v92, v92\n" \
    "v_cvt_pk_bf16_f32 v89, v90, v91\n" \
    "v_smfmac_f32_16x16x64_bf16 v[64:67], v[48:51], v[72:79], v124\n" \
    "ds_read_b128 v[44:47], v106 offset:3072\n" \
    "v_exp_f32 v93, v93\n" \
    "v_smfmac_f32_16x16x64_bf16 v[64:67], v[52:55], v[80:87], v124\n" \
    "ds_read_b128 v[72:75], v106 offset:34816\n" \
    "v_exp_f32 v94, v94\n" \
    "v_cvt_pk_bf16_f32 v90, v92, v93\n" \
    "v_smfmac_f32_16x16x64_bf16 v[60:63], v[24:27], v[192:199], v124\n" \
    "ds_read_b128 v[76:79], v106 offset:35840\n" \
    "v_exp_f32 v95, v95\n" \
    "v_smfmac_f32_16x16x64_bf16 v[60:63], v[28:31], v[208:215], v124\n" \
    "ds_read_b128 v[80:83], v106 offset:4096\n" \
    "s_cmp_eq_u32 s36, 63\n" \
    "v_add_f32 v64, v64, v66\n" \
    "v_add_f32 v65, v65, v67\n" \
    "v_smfmac_f32_16x16x64_bf16 v[60:63], v[32:35], v[224:231], v124\n" \
    "ds_read_b128 v[84:87], v106 offset:5120\n" \
    "v_exp_f32 v64, v64\n" \
    "v_cvt_pk_bf16_f32 v91, v94, v95\n" \
    "v_smfmac_f32_16x16x64_bf16 v[60:63], v[36:39], v[240:247], v124\n" \
    "ds_read_b128 v[48:51], v106 offset:36864\n" \
    "v_exp_f32 v65, v65\n" \
    "v_swap_b32 v1, v2\n" \
    "v_swap_b32 v5, v6\n" \
    "v_mfma_f32_16x16x32_bf16 v[102:105], v[88:91], v[120:123], 0\n" \
    "ds_read_b128 v[52:55], v106 offset:37888\n" \
    "v_cvt_pk_bf16_f32 v100, v64, v65\n" \
    "v_add_f32 v56, v56, v58\n" \
    "v_add_f32 v57, v57, v59\n" \
    "v_smfmac_f32_16x16x64_bf16 v[68:71], v[88:91], v[0:7], v124\n" \
    "ds_read_b128 v[24:27], v106 offset:6144\n" \
    "v_add_f32 v60, v60, v62\n" \
    "v_add_f32 v61, v61, v63\n" \
    "v_mfma_f32_16x16x16_bf16 v[102:105], v[100:101], v[120:121], v[102:105]\n" \
    "ds_read_b128 v[28:31], v106 offset:7168\n" \
    "v_cvt_pk_bf16_f32 v96, v56, v57\n" \
    "v_cvt_pk_bf16_f32 v97, v60, v61\n" \
    "s_cbranch_scc1 16f\n" \
    "s_nop 0\n" \
    "v_smfmac_f32_16x16x32_bf16 v[68:71], v[100:101], v[96:99], v124\n" \
    "ds_read_b128 v[32:35], v106 offset:38912\n" \
    "v_mov_b64 v[56:57], 0\n" \
    "v_mov_b64 v[58:59], 0\n" \
    "v_rcp_f32 v104, v102\n" \
    "v_rcp_f32 v105, v103\n" \
    "v_mul_f32 v68, v68, v104\n" \
    "v_mul_f32 v70, v70, v104\n" \
    "v_mul_f32 v69, v69, v105\n" \
    "v_mul_f32 v71, v71, v105\n" \
    "v_cvt_pk_bf16_f32 v107, v68, v70\n" \
    "v_cvt_pk_bf16_f32 v108, v69, v71\n" \
    "ds_write2_b32 v129, v107, v108 offset0:64 offset1:208\n" \
    "v_max3_f32 v102, v102, v103, v104\n" \
    "v_max_f32 v102, v102, v105\n" \
    "v_cmp_nge_f32 vcc, s82, v102\n" \
    "v_mov_b64 v[60:61], 0\n" \
    "v_mov_b64 v[62:63], 0\n" \
    "v_mov_b64 v[88:89], 0\n" \
    "v_mov_b64 v[90:91], 0\n" \
    "v_mov_b64 v[92:93], 0\n" \
    "v_mov_b64 v[94:95], 0\n" \
    "s_waitcnt vmcnt(2) lgkmcnt(0)\n" \
    "s_barrier\n" \
    "ds_read_b128 v[68:71], v109 offset:256\n" \
    "ds_read_b128 v[102:105], v111 offset:384\n" \
    "s_cbranch_vccnz 7f\n" \
    "buffer_load_dwordx2 v[98:99], v113, %[rs], s78 offen sc1\n" \
    "buffer_load_dword v100, off, %[rw], s75 sc1\n" \
    "v_mov_b64 v[64:65], 0\n" \
    "v_mov_b64 v[66:67], 0\n" \
    "s_waitcnt lgkmcnt(1)\n" \
    "v_smfmac_f32_16x16x64_bf16 v[56:59], v[68:71], v[8:15], v124\n" \
    "s_waitcnt lgkmcnt(0)\n" \
    "ds_read_b128 v[36:39], v106 offset:39936\n" \
    "v_smfmac_f32_16x16x64_bf16 v[56:59], v[102:105], v[16:23], v124\n" \
    "ds_read_b128 v[0:3], v133 offset:51200\n" \
    "v_smfmac_f32_16x16x64_bf16 v[60:63], v[68:71], v[40:47], v124\n" \
    "ds_read_b128 v[4:7], v133 offset:52224\n" \
    "v_smfmac_f32_16x16x64_bf16 v[60:63], v[102:105], v[72:79], v124\n" \
    "ds_read_b128 v[16:19], v112 offset:51200\n" \
    "v_add_f32 v56, v56, v58\n" \
    "v_smfmac_f32_16x16x64_bf16 v[88:91], v[68:71], v[80:87], v124\n" \
    "ds_read_b128 v[20:23], v112 offset:52224\n" \
    "v_add_f32 v57, v57, v59\n" \
    "v_smfmac_f32_16x16x64_bf16 v[88:91], v[102:105], v[48:55], v124\n" \
    "v_add_f32 v60, v60, v62\n" \
    "v_smfmac_f32_16x16x64_bf16 v[92:95], v[68:71], v[24:31], v124\n" \
    "s_waitcnt lgkmcnt(4)\n" \
    "v_add_f32 v61, v61, v63\n" \
    "v_smfmac_f32_16x16x64_bf16 v[92:95], v[102:105], v[32:39], v124\n" \
    "s_waitcnt lgkmcnt(2)\n" \
    "v_add_f32 v88, v88, v90\n" \
    "v_smfmac_f32_16x16x64_bf16 v[64:67], v[68:71], v[0:7], v124\n" \
    "s_waitcnt lgkmcnt(0)\n" \
    "v_add_f32 v89, v89, v91\n" \
    "v_smfmac_f32_16x16x64_bf16 v[64:67], v[102:105], v[16:23], v124\n" \
    "v_add_f32 v92, v92, v94\n" \
    "v_add_f32 v93, v93, v95\n" \
    "v_cvt_pk_bf16_f32 v8, v56, v60\n" \
    "v_cvt_pk_bf16_f32 v10, v57, v61\n" \
    "v_cvt_pk_bf16_f32 v9, v88, v92\n" \
    "v_cvt_pk_bf16_f32 v11, v89, v93\n" \
    "buffer_store_dwordx4 v[8:11], v134, %[rs], s79 offen sc1\n" \
    "v_add_f32 v64, v64, v66\n" \
    "v_add_f32 v65, v65, v67\n" \
    "v_cvt_pk_bf16_f32 v64, v64, v65\n" \
    "buffer_store_dword v64, v135, %[rs], s79 offen sc1\n" \
    "s_memtime s[88:89]\n" \
    "s_memrealtime s[44:45]\n" \
    "s_and_b32 s75, s91, 3\n" \
    "s_lshl_b32 s75, s75, 17\n" \
    "s_memrealtime s[46:47]\n" \
    "s_add_i32 s75, s75, s26\n" \
    "s_add_i32 s69, s75, s74\n" \
    "s_memrealtime s[48:49]\n" \
    "s_add_i32 s70, s69, 4096\n" \
    "s_add_i32 s71, s75, 16384\n" \
    "s_memrealtime s[50:51]\n" \
    "s_nop 1\n" \
    "s_memrealtime s[52:53]\n" \
    "s_nop 1\n" \
    "s_memrealtime s[54:55]\n" \
    "s_nop 1\n" \
    "s_memrealtime s[56:57]\n" \
    "s_nop 1\n" \
    "s_memrealtime vcc\n" \
    "v_and_b32 v18, 0x300, v127\n" \
    "v_add_u32 v18, v18, v127\n" \
    "v_and_b32 v19, 0x380, v127\n" \
    "v_add_u32 v19, v19, v127\n" \
    "s_waitcnt lgkmcnt(0)\n" \
    "s_add_i32 s44, s44, s46\n" \
    "s_add_i32 s48, s48, s50\n" \
    "s_add_i32 s52, s52, s54\n" \
    "s_add_i32 s44, s44, s48\n" \
    "s_add_i32 s52, s52, s56\n" \
    "s_add_i32 s44, s44, s52\n" \
    "s_add_i32 s80, s44, vcc_lo\n" \
    "s_sub_i32 s76, s80, s29\n" \
    "s_mov_b32 s29, s80\n" \
    "s_sub_i32 s80, s88, s24\n" \
    "s_mov_b32 s24, s88\n" \
    "s_mul_i32 s75, s76, s25\n" \
    "s_lshr_b32 s75, s75, 12\n" \
    "s_sub_i32 s75, s80, s75\n" \
    "s_mul_hi_u32 s80, s80, s41\n" \
    "s_sub_i32 s80, s76, s80\n" \
    "s_lshl_b32 s80, s80, 18\n" \
    "s_cmp_lt_u32 s76, 1480\n" \
    "s_cselect_b32 s75, s75, 0\n" \
    "s_cselect_b32 s80, s80, 0\n" \
    "s_add_i32 s25, s25, s75\n" \
    "s_add_i32 s41, s41, s80\n" \
    "s_waitcnt vmcnt(2)\n" \
    "v_readfirstlane_b32 s80, v100\n" \
    "v_cmp_ne_u16 vcc, s91, v99\n" \
    "s_mov_b32 s43, 0\n" \
    "s_max_i32 s38, s38, s80\n" \
    "s_cmp_lt_i32 s38, s91\n" \
    "s_cselect_b64 exec, 0, -1\n" \
    "s_cselect_b32 s94, 8, 0\n" \
    "buffer_load_dwordx4 v19, %[rw], s92 offen sc1 lds\n" \
    "s_add_i32 m0, s95, 896\n" \
    "s_nop 0\n" \
    "buffer_load_dwordx4 v19, %[rw], s92 offen offset:128 sc1 lds\n" \
    "s_add_i32 m0, s95, 8192\n" \
    "s_nop 0\n" \
    "buffer_load_dwordx4 v18, %[rw], s93 offen sc1 lds\n" \
    "s_add_i32 m0, s95, 8960\n" \
    "s_nop 0\n" \
    "buffer_load_dwordx4 v18, %[rw], s93 offen offset:256 sc1 lds\n" \
    "s_mov_b64 exec, -1\n" \
    "s_cbranch_vccnz 15f\n" \
    "10:\n" \
    "v_subrev_u32 v28, s29, v98\n" \
    "v_min_i32 v28, 0x4000, v28\n" \
    "v_max_i32 v28, 0xffffc000, v28\n" \
    "s_bitcmp1_b32 s30, 15\n" \
    "s_cbranch_scc1 18f\n" \
    "19:\n" \
    "v_readlane_b32 s75, v28, s83\n" \
    "v_max_i32_dpp v28, v28, v28 row_shr:1 row_mask:0xf bank_mask:0xf\n" \
    "s_nop 1\n" \
    "v_max_i32_dpp v28, v28, v28 row_shr:2 row_mask:0xf bank_mask:0xf\n" \
    "s_nop 1\n" \
    "v_max_i32_dpp v28, v28, v28 row_shr:4 row_mask:0xf bank_mask:0xf\n" \
    "s_nop 1\n" \
    "v_max_i32_dpp v28, v28, v28 row_shr:8 row_mask:0xf bank_mask:0xf\n" \
    "s_add_i32 s76, s29, s42\n" \
    "s_nop 0\n" \
    "v_max_i32_dpp v28, v28, v28 row_bcast:15 row_mask:0xa bank_mask:0xf\n" \
    "s_nop 1\n" \
    "v_max_i32_dpp v28, v28, v28 row_bcast:31 row_mask:0xc bank_mask:0xf\n" \
    "s_nop 0\n" \
    "v_readlane_b32 s37, v28, 63\n" \
    "s_sub_i32 s75, s37, s75\n" \
    "s_cmp_gt_i32 s75, 24\n" \
    "s_cselect_b32 s80, 2, 0\n" \
    "s_cmp_lt_i32 s75, 10\n" \
    "s_cselect_b32 s80, -6, s80\n" \
    "s_add_i32 s28, s28, s80\n" \
    "s_max_i32 s28, s28, 0\n" \
    "s_min_i32 s28, s28, 20\n" \
    "s_add_i32 s76, s76, s28\n" \
    "v_add_u32 v30, -1, s36\n" \
    "s_add_i32 s37, s37, s76\n" \
    "v_mov_b32 v31, s37\n" \
    "s_mov_b64 exec, s[72:73]\n" \
    "buffer_store_dwordx2 v[30:31], off, %[rw], s65 sc1\n" \
    "s_mov_b64 exec, -1\n" \
    "s_cmp_lg_u32 s94, 0\n" \
    "s_cbranch_scc1 9f\n" \
    "s_mov_b32 s36, s91\n" \
    "s_branch 12b\n" \
    "5:\n" \
    "v_mov_b32 v107, 0\n" \
    "v_max3_u32 v107, v107, v0, v1\n" \
    "v_max3_u32 v107, v107, v2, v3\n" \
    "v_max3_u32 v107, v107, v4, v5\n" \
    "v_max3_u32 v107, v107, v6, v7\n" \
    "v_max3_u32 v107, v107, v8, v9\n" \
    "v_max3_u32 v107, v107, v10, v11\n" \
    "v_max3_u32 v107, v107, v12, v13\n" \
    "v_max3_u32 v107, v107, v14, v15\n" \
    "v_max3_u32 v107, v107, v16, v17\n" \
    "v_max3_u32 v107, v107, v18, v19\n" \
    "v_max3_u32 v107, v107, v24, v25\n" \
    "v_max3_u32 v107, v107, v26, v27\n" \
    "v_max3_u32 v107, v107, v28, v29\n" \
    "v_max3_u32 v107, v107, v30, v31\n" \
    "v_max3_u32 v107, v107, v32, v33\n" \
    "v_max3_u32 v107, v107, v34, v35\n" \
    "v_max3_u32 v107, v107, v36, v37\n" \
    "v_max3_u32 v107, v107, v38, v39\n" \
    "v_cmp_eq_u32 vcc, -1, v107\n" \
    "s_nop 4\n" \
    "s_cbranch_vccz 6b\n" \
    "s_cmp_eq_u32 s36, 0\n" \
    "s_cbranch_scc1 6b\n" \
    "s_add_i32 s39, s39, -1\n" \
    "s_cmp_lt_i32 s39, 0\n" \
    "s_cbranch_scc1 6b\n" \
    "s_add_i32 s30, s30, 1\n" \
    "s_bitset1_b32 s30, 15\n" \
    "s_and_b32 s80, s31, 0xff\n" \
    "s_add_i32 s42, s42, s80\n" \
    "s_bfe_u32 s80, s31, 0x80008\n" \
    "s_add_i32 s40, s40, s80\n" \
    "s_waitcnt lgkmcnt(0)\n" \
    "s_mov_b32 s37, s29\n" \
    "s_nop 7\n" \
    "s_barrier\n" \
    "s_branch 0b\n" \
    "15:\n" \
    "s_add_i32 s30, s30, 0x10000\n" \
    "s_add_i32 s39, s39, -1\n" \
    "s_cmp_lt_i32 s39, 0\n" \
    "s_cbranch_scc1 10b\n" \
    "s_sleep 9\n" \
    "buffer_load_dwordx2 v[98:99], v113, %[rs], s78 offen sc1\n" \
    "s_waitcnt vmcnt(0)\n" \
    "v_cmp_ne_u16 vcc, s91, v99\n" \
    "s_nop 1\n" \
    "s_cbranch_vccnz 15b\n" \
    "s_memtime s[88:89]\n" \
    "s_memrealtime s[44:45]\n" \
    "s_nop 1\n" \
    "s_memrealtime s[46:47]\n" \
    "s_nop 1\n" \
    "s_memrealtime s[48:49]\n" \
    "s_nop 1\n" \
    "s_memrealtime s[50:51]\n" \
    "s_nop 1\n" \
    "s_memrealtime s[52:53]\n" \
    "s_nop 1\n" \
    "s_memrealtime s[54:55]\n" \
    "s_nop 1\n" \
    "s_memrealtime s[56:57]\n" \
    "s_nop 1\n" \
    "s_memrealtime vcc\n" \
    "s_waitcnt lgkmcnt(0)\n" \
    "s_add_i32 s44, s44, s46\n" \
    "s_add_i32 s48, s48, s50\n" \
    "s_add_i32 s52, s52, s54\n" \
    "s_add_i32 s44, s44, s48\n" \
    "s_add_i32 s52, s52, s56\n" \
    "s_add_i32 s44, s44, s52\n" \
    "s_add_i32 s29, s44, vcc_lo\n" \
    "s_mov_b32 s24, s88\n" \
    "s_branch 10b\n" \
    "18:\n" \
    "v_cmp_gt_i32 vcc, -320, v28\n" \
    "s_bitset0_b32 s30, 15\n" \
    "s_lshr_b32 s80, s31, 16\n" \
    "s_cmp_lt_u32 s36, 4\n" \
    "s_cselect_b32 s80, 0, s80\n" \
    "s_min_u32 s76, s80, 1\n" \
    "s_lshl_b32 s76, s76, 16\n" \
    "s_sub_i32 s31, s31, s76\n" \
    "s_mul_i32 s76, s80, 768\n" \
    "s_cmp_eq_u64 vcc, 0\n" \
    "s_cselect_b32 s80, 0, s80\n" \
    "s_cselect_b32 s76, s76, 0\n" \
    "s_add_i32 s33, s33, s80\n" \
    "s_add_i32 s32, s32, s76\n" \
    "s_branch 19b\n" \
    "16:\n" \
    "s_waitcnt lgkmcnt(0)\n" \
    "v_bfe_u32 v20, v127, 8, 2\n" \
    "v_subrev_u32 v20, s60, v20\n" \
    "v_mul_i32_i24 v20, -1088, v20\n" \
    "v_add_u32 v20, v20, v129\n" \
    "v_lshrrev_b32 v21, 2, v127\n" \
    "s_mov_b32 exec_lo, s66\n" \
    "s_mov_b32 exec_hi, s66\n" \
    "ds_read2_b32 v[96:97], v20 offset0:0 offset1:144\n" \
    "s_mov_b64 exec, -1\n" \
    "s_lshl_b32 s80, s91, 16\n" \
    "s_add_i32 s80, s80, s67\n" \
    "s_waitcnt lgkmcnt(0)\n" \
    "buffer_store_dword v96, v21, %[rw], s80 offen sc1\n" \
    "buffer_store_dword v97, v21, %[rw], s80 offen offset:256 sc1\n" \
    "s_branch 9f\n" \
    "17:\n" \
    "ds_read_b128 v[0:3], v108 offset:0\n" \
    "ds_read_b128 v[16:19], v108 offset:4096\n" \
    "s_waitcnt lgkmcnt(0)\n" \
    "s_branch 3b\n" \
    "7:\n" \
    "s_mov_b32 s43, 2\n" \
    "s_nop 7\n" \
    "s_waitcnt vmcnt(0)\n" \
    "9:\n" \
    "s_mov_b32 m0, s68\n" \
    "s_waitcnt lgkmcnt(0)\n" \
    "v_mov_b32 v0, s36\n" \
    "v_mov_b32 v1, s37\n" \
    "v_mov_b32 v2, s38\n" \
    "v_mov_b32 v3, s39\n" \
    "v_mov_b32 v4, s40\n" \
    "v_mov_b32 v5, s41\n" \
    "v_mov_b32 v6, s42\n" \
    "v_mov_b32 v7, s43\n" \
    "v_mov_b32 v8, s28\n" \
    "v_mov_b32 v9, s29\n" \
    "v_mov_b32 v10, s30\n" \
    "v_mov_b32 v11, s31\n" \
    "v_mov_b32 v12, s32\n" \
    "v_mov_b32 v13, s33\n" \
    "v_mov_b32 v14, s34\n" \
    "v_mov_b32 v15, s35\n" \
    "v_mov_b32 v16, s24\n" \
    "v_mov_b32 v17, s25\n" \
    "v_mov_b32 v18, s26\n" \
    "v_mov_b32 v19, s27\n" \
    "v_mov_b32 v20, s84\n" \
    "v_mov_b32 v21, s86\n" \
    "v_mov_b32 v22, s88\n" \
    "v_mov_b32 v23, s84\n" \
    "v_mov_b32 v24, s60\n" \
    "v_mov_b32 v25, s61\n" \
    "v_mov_b32 v26, s62\n" \
    "v_mov_b32 v27, s63\n" \
    "v_mov_b32 v28, s64\n" \
    "v_mov_b32 v29, s65\n" \
    "v_mov_b32 v30, s66\n" \
    "v_mov_b32 v31, s67\n" \
    ".Lstep_end:\n" \
    ""
static_assert(2 * PART_BYTES == 589824 && 2 * OFF_CAN == 1703936 && F_ERR == 2 * OFF_CAN + 65536 && OFF_PART == 0 && OFF_VF - OFF_KF == 2048 * 4096 * 2, "constants of the step block");
#define STEP_CLOBBER "v32", "v33", "v34", "v35", "v36", "v37", "v38", "v39", "v40", "v41", "v42", "v43", "v44", "v45", "v46", "v47", "v48", "v49", "v50", "v51", "v52", "v53", "v54", "v55", "v56", "v57", "v58", "v59", "v60", "v61", "v62", "v63", "v64", "v65", "v66", "v67", "v68", "v69", "v70", "v71", "v72", "v73", "v74", "v75", "v76", "v77", "v78", "v79", "v80", "v81", "v82", "v83", "v84", "v85", "v86", "v87", "v88", "v89", "v90", "v91", "v92", "v93", "v94", "v95", "v98", "v99", "v100", "v101", "v102", "v103", "v104", "v105", "v106", "v107", "v108", "v109", "v110", "v111", "v112", "v113", "v114", "s24", "s25", "s26", "s27", "s28", "s29", "s30", "s31", "s32", "s33", "s34", "s35", "s36", "s37", "s38", "s39", "s40", "s41", "s42", "s43", "s44", "s45", "s46", "s47", "s48", "s49", "s50", "s51", "s52", "s53", "s54", "s55", "s56", "s57", "s58", "s59", "s60", "s61", "s62", "s63", "s64", "s65", "s66", "s67", "s68", "s69", "s70", "s71", "s72", "s73", "s74", "s75", "s76", "s77", "s78", "s79", "s80", "s81", "s82", "s83", "s84", "s85", "s86", "s87", "s88", "s89", "s90", "s91", "s92", "s93", "s94", "s95", "s96", "s97", "vcc", "scc", "memory"
#define OUT16_ASM \
    "s_mov_b32 exec_lo, %[tm]\n" \
    "s_mov_b32 exec_hi, 0\n" \
    "buffer_load_dword v79, %[tv], %[rw], %[so] offen sc1\n" \
    "s_mov_b64 exec, -1\n" \
    "s_add_i32 s44, %[so], 4096\n" \
    "s_add_i32 s45, %[so], 8192\n" \
    "s_add_i32 s46, %[so], 12288\n" \
    "buffer_load_dwordx4 v[16:19], %[vo], %[rw], %[so] offen offset:0 sc1\n" \
    "ds_read_b128 v[80:83], %[wl] offset:0\n" \
    "ds_read_b128 v[84:87], %[wl] offset:1024\n" \
    "s_nop 7\n" \
    "v_mov_b32 v104, 0\n" \
    "buffer_load_dwordx4 v[20:23], %[vo], %[rw], %[so] offen offset:256 sc1\n" \
    "s_nop 7\n" \
    "s_nop 3\n" \
    "buffer_load_dwordx4 v[24:27], %[vo], %[rw], %[so] offen offset:1024 sc1\n" \
    "s_nop 7\n" \
    "s_nop 3\n" \
    "buffer_load_dwordx4 v[28:31], %[vo], %[rw], %[so] offen offset:1280 sc1\n" \
    "s_nop 7\n" \
    "s_nop 3\n" \
    "buffer_load_dwordx4 v[32:35], %[vo], %[rw], %[so] offen offset:2048 sc1\n" \
    "s_nop 7\n" \
    "s_nop 3\n" \
    "buffer_load_dwordx4 v[36:39], %[vo], %[rw], %[so] offen offset:2304 sc1\n" \
    "s_nop 7\n" \
    "s_nop 3\n" \
    "buffer_load_dwordx4 v[40:43], %[vo], %[rw], %[so] offen offset:3072 sc1\n" \
    "s_nop 7\n" \
    "s_nop 3\n" \
    "buffer_load_dwordx4 v[44:47], %[vo], %[rw], %[so] offen offset:3328 sc1\n" \
    "s_nop 7\n" \
    "s_nop 3\n" \
    "buffer_load_dwordx4 v[48:51], %[vo], %[rw], s44 offen offset:0 sc1\n" \
    "s_nop 7\n" \
    "s_nop 3\n" \
    "buffer_load_dwordx4 v[52:55], %[vo], %[rw], s44 offen offset:256 sc1\n" \
    "s_nop 7\n" \
    "s_nop 3\n" \
    "buffer_load_dwordx4 v[56:59], %[vo], %[rw], s44 offen offset:1024 sc1\n" \
    "s_nop 7\n" \
    "s_nop 3\n" \
    "buffer_load_dwordx4 v[60:63], %[vo], %[rw], s44 offen offset:1280 sc1\n" \
    "ds_read_b128 v[88:91], %[wl] offset:2048\n" \
    "s_waitcnt vmcnt(11)\n" \
    "v_mfma_f32_16x16x32_bf16 v[0:3], a[0:3], v[16:19], 0\n" \
    "v_max3_u32 v104, v104, v16, v17\n" \
    "v_mfma_f32_16x16x32_bf16 v[4:7], a[128:131], v[16:19], 0\n" \
    "v_max3_u32 v104, v104, v18, v19\n" \
    "v_mfma_f32_16x16x32_bf16 v[8:11], v[128:131], v[16:19], 0\n" \
    "s_waitcnt lgkmcnt(2)\n" \
    "v_mfma_f32_16x16x32_bf16 v[12:15], v[80:83], v[16:19], 0\n" \
    "buffer_load_dwordx4 v[64:67], %[vo], %[rw], s44 offen offset:2048 sc1\n" \
    "ds_read_b128 v[80:83], %[wl] offset:3072\n" \
    "s_waitcnt vmcnt(11)\n" \
    "v_mfma_f32_16x16x32_bf16 v[0:3], a[4:7], v[20:23], v[0:3]\n" \
    "v_max3_u32 v104, v104, v20, v21\n" \
    "v_mfma_f32_16x16x32_bf16 v[4:7], a[132:135], v[20:23], v[4:7]\n" \
    "v_max3_u32 v104, v104, v22, v23\n" \
    "v_mfma_f32_16x16x32_bf16 v[8:11], v[132:135], v[20:23], v[8:11]\n" \
    "s_waitcnt lgkmcnt(2)\n" \
    "v_mfma_f32_16x16x32_bf16 v[12:15], v[84:87], v[20:23], v[12:15]\n" \
    "buffer_load_dwordx4 v[68:71], %[vo], %[rw], s44 offen offset:2304 sc1\n" \
    "ds_read_b128 v[84:87], %[wl] offset:4096\n" \
    "s_waitcnt vmcnt(11)\n" \
    "v_mfma_f32_16x16x32_bf16 v[0:3], a[8:11], v[24:27], v[0:3]\n" \
    "v_max3_u32 v104, v104, v24, v25\n" \
    "v_mfma_f32_16x16x32_bf16 v[4:7], a[136:139], v[24:27], v[4:7]\n" \
    "v_max3_u32 v104, v104, v26, v27\n" \
    "v_mfma_f32_16x16x32_bf16 v[8:11], v[136:139], v[24:27], v[8:11]\n" \
    "s_waitcnt lgkmcnt(2)\n" \
    "v_mfma_f32_16x16x32_bf16 v[12:15], v[88:91], v[24:27], v[12:15]\n" \
    "buffer_load_dwordx4 v[72:75], %[vo], %[rw], s44 offen offset:3072 sc1\n" \
    "ds_read_b128 v[88:91], %[wl] offset:5120\n" \
    "s_waitcnt vmcnt(11)\n" \
    "v_mfma_f32_16x16x32_bf16 v[0:3], a[12:15], v[28:31], v[0:3]\n" \
    "v_max3_u32 v104, v104, v28, v29\n" \
    "v_mfma_f32_16x16x32_bf16 v[4:7], a[140:143], v[28:31], v[4:7]\n" \
    "v_max3_u32 v104, v104, v30, v31\n" \
    "v_mfma_f32_16x16x32_bf16 v[8:11], v[140:143], v[28:31], v[8:11]\n" \
    "s_waitcnt lgkmcnt(2)\n" \
    "v_mfma_f32_16x16x32_bf16 v[12:15], v[80:83], v[28:31], v[12:15]\n" \
    "buffer_load_dwordx4 v[76:79], %[vo], %[rw], s44 offen offset:3328 sc1\n" \
    "ds_read_b128 v[80:83], %[wl] offset:6144\n" \
    "s_waitcnt vmcnt(11)\n" \
    "v_mfma_f32_16x16x32_bf16 v[0:3], a[16:19], v[32:35], v[0:3]\n" \
    "v_max3_u32 v104, v104, v32, v33\n" \
    "v_mfma_f32_16x16x32_bf16 v[4:7], a[144:147], v[32:35], v[4:7]\n" \
    "v_max3_u32 v104, v104, v34, v35\n" \
    "v_mfma_f32_16x16x32_bf16 v[8:11], v[144:147], v[32:35], v[8:11]\n" \
    "s_waitcnt lgkmcnt(2)\n" \
    "v_mfma_f32_16x16x32_bf16 v[12:15], v[84:87], v[32:35], v[12:15]\n" \
    "buffer_load_dwordx4 v[16:19], %[vo], %[rw], s45 offen offset:0 sc1\n" \
    "ds_read_b128 v[84:87], %[wl] offset:7168\n" \
    "s_waitcnt vmcnt(11)\n" \
    "v_mfma_f32_16x16x32_bf16 v[0:3], a[20:23], v[36:39], v[0:3]\n" \
    "v_max3_u32 v104, v104, v36, v37\n" \
    "v_mfma_f32_16x16x32_bf16 v[4:7], a[148:151], v[36:39], v[4:7]\n" \
    "v_max3_u32 v104, v104, v38, v39\n" \
    "v_mfma_f32_16x16x32_bf16 v[8:11], v[148:151], v[36:39], v[8:11]\n" \
    "s_waitcnt lgkmcnt(2)\n" \
    "v_mfma_f32_16x16x32_bf16 v[12:15], v[88:91], v[36:39], v[12:15]\n" \
    "buffer_load_dwordx4 v[20:23], %[vo], %[rw], s45 offen offset:256 sc1\n" \
    "ds_read_b128 v[88:91], %[wl] offset:8192\n" \
    "s_waitcnt vmcnt(11)\n" \
    "v_mfma_f32_16x16x32_bf16 v[0:3], a[24:27], v[40:43], v[0:3]\n" \
    "v_max3_u32 v104, v104, v40, v41\n" \
    "v_mfma_f32_16x16x32_bf16 v[4:7], a[152:155], v[40:43], v[4:7]\n" \
    "v_max3_u32 v104, v104, v42, v43\n" \
    "v_mfma_f32_16x16x32_bf16 v[8:11], v[152:155], v[40:43], v[8:11]\n" \
    "s_waitcnt lgkmcnt(2)\n" \
    "v_mfma_f32_16x16x32_bf16 v[12:15], v[80:83], v[40:43], v[12:15]\n" \
    "buffer_load_dwordx4 v[24:27], %[vo], %[rw], s45 offen offset:1024 sc1\n" \
    "ds_read_b128 v[80:83], %[wl] offset:9216\n" \
    "s_waitcnt vmcnt(11)\n" \
    "v_mfma_f32_16x16x32_bf16 v[0:3], a[28:31], v[44:47], v[0:3]\n" \
    "v_max3_u32 v104, v104, v44, v45\n" \
    "v_mfma_f32_16x16x32_bf16 v[4:7], a[156:159], v[44:47], v[4:7]\n" \
    "v_max3_u32 v104, v104, v46, v47\n" \
    "v_mfma_f32_16x16x32_bf16 v[8:11], v[156:159], v[44:47], v[8:11]\n" \
    "s_waitcnt lgkmcnt(2)\n" \
    "v_mfma_f32_16x16x32_bf16 v[12:15], v[84:87], v[44:47], v[12:15]\n" \
    "buffer_load_dwordx4 v[28:31], %[vo], %[rw], s45 offen offset:1280 sc1\n" \
    "ds_read_b128 v[84:87], %[wl] offset:10240\n" \
    "s_waitcnt vmcnt(11)\n" \
    "v_mfma_f32_16x16x32_bf16 v[0:3], a[32:35], v[48:51], v[0:3]\n" \
    "v_max3_u32 v104, v104, v48, v49\n" \
    "v_mfma_f32_16x16x32_bf16 v[4:7], a[160:163], v[48:51], v[4:7]\n" \
    "v_max3_u32 v104, v104, v50, v51\n" \
    "v_mfma_f32_16x16x32_bf16 v[8:11], v[160:163], v[48:51], v[8:11]\n" \
    "s_waitcnt lgkmcnt(2)\n" \
    "v_mfma_f32_16x16x32_bf16 v[12:15], v[88:91], v[48:51], v[12:15]\n" \
    "buffer_load_dwordx4 v[32:35], %[vo], %[rw], s45 offen offset:2048 sc1\n" \
    "ds_read_b128 v[88:91], %[wl] offset:11264\n" \
    "s_waitcnt vmcnt(11)\n" \
    "v_mfma_f32_16x16x32_bf16 v[0:3], a[36:39], v[52:55], v[0:3]\n" \
    "v_max3_u32 v104, v104, v52, v53\n" \
    "v_mfma_f32_16x16x32_bf16 v[4:7], a[164:167], v[52:55], v[4:7]\n" \
    "v_max3_u32 v104, v104, v54, v55\n" \
    "v_mfma_f32_16x16x32_bf16 v[8:11], v[164:167], v[52:55], v[8:11]\n" \
    "s_waitcnt lgkmcnt(2)\n" \
    "v_mfma_f32_16x16x32_bf16 v[12:15], v[80:83], v[52:55], v[12:15]\n" \
    "buffer_load_dwordx4 v[36:39], %[vo], %[rw], s45 offen offset:2304 sc1\n" \
    "ds_read_b128 v[80:83], %[wl] offset:12288\n" \
    "s_waitcnt vmcnt(11)\n" \
    "v_mfma_f32_16x16x32_bf16 v[0:3], a[40:43], v[56:59], v[0:3]\n" \
    "v_max3_u32 v104, v104, v56, v57\n" \
    "v_mfma_f32_16x16x32_bf16 v[4:7], a[168:171], v[56:59], v[4:7]\n" \
    "v_max3_u32 v104, v104, v58, v59\n" \
    "v_mfma_f32_16x16x32_bf16 v[8:11], v[168:171], v[56:59], v[8:11]\n" \
    "s_waitcnt lgkmcnt(2)\n" \
    "v_mfma_f32_16x16x32_bf16 v[12:15], v[84:87], v[56:59], v[12:15]\n" \
    "buffer_load_dwordx4 v[40:43], %[vo], %[rw], s45 offen offset:3072 sc1\n" \
    "ds_read_b128 v[84:87], %[wl] offset:13312\n" \
    "s_waitcnt vmcnt(11)\n" \
    "v_mfma_f32_16x16x32_bf16 v[0:3], a[44:47], v[60:63], v[0:3]\n" \
    "v_max3_u32 v104, v104, v60, v61\n" \
    "v_mfma_f32_16x16x32_bf16 v[4:7], a[172:175], v[60:63], v[4:7]\n" \
    "v_max3_u32 v104, v104, v62, v63\n" \
    "v_mfma_f32_16x16x32_bf16 v[8:11], v[172:175], v[60:63], v[8:11]\n" \
    "s_waitcnt lgkmcnt(2)\n" \
    "v_mfma_f32_16x16x32_bf16 v[12:15], v[88:91], v[60:63], v[12:15]\n" \
    "buffer_load_dwordx4 v[44:47], %[vo], %[rw], s45 offen offset:3328 sc1\n" \
    "ds_read_b128 v[88:91], %[wl] offset:14336\n" \
    "s_waitcnt vmcnt(11)\n" \
    "v_mfma_f32_16x16x32_bf16 v[0:3], a[48:51], v[64:67], v[0:3]\n" \
    "v_max3_u32 v104, v104, v64, v65\n" \
    "v_mfma_f32_16x16x32_bf16 v[4:7], a[176:179], v[64:67], v[4:7]\n" \
    "v_max3_u32 v104, v104, v66, v67\n" \
    "v_mfma_f32_16x16x32_bf16 v[8:11], v[176:179], v[64:67], v[8:11]\n" \
    "s_waitcnt lgkmcnt(2)\n" \
    "v_mfma_f32_16x16x32_bf16 v[12:15], v[80:83], v[64:67], v[12:15]\n" \
    "buffer_load_dwordx4 v[48:51], %[vo], %[rw], s46 offen offset:0 sc1\n" \
    "ds_read_b128 v[80:83], %[wl] offset:15360\n" \
    "s_waitcnt vmcnt(11)\n" \
    "v_mfma_f32_16x16x32_bf16 v[0:3], a[52:55], v[68:71], v[0:3]\n" \
    "v_max3_u32 v104, v104, v68, v69\n" \
    "v_mfma_f32_16x16x32_bf16 v[4:7], a[180:183], v[68:71], v[4:7]\n" \
    "v_max3_u32 v104, v104, v70, v71\n" \
    "v_mfma_f32_16x16x32_bf16 v[8:11], v[180:183], v[68:71], v[8:11]\n" \
    "s_waitcnt lgkmcnt(2)\n" \
    "v_mfma_f32_16x16x32_bf16 v[12:15], v[84:87], v[68:71], v[12:15]\n" \
    "buffer_load_dwordx4 v[52:55], %[vo], %[rw], s46 offen offset:256 sc1\n" \
    "ds_read_b128 v[84:87], %[wl] offset:16384\n" \
    "s_waitcnt vmcnt(11)\n" \
    "v_mfma_f32_16x16x32_bf16 v[0:3], a[56:59], v[72:75], v[0:3]\n" \
    "v_max3_u32 v104, v104, v72, v73\n" \
    "v_mfma_f32_16x16x32_bf16 v[4:7], a[184:187], v[72:75], v[4:7]\n" \
    "v_max3_u32 v104, v104, v74, v75\n" \
    "v_mfma_f32_16x16x32_bf16 v[8:11], v[184:187], v[72:75], v[8:11]\n" \
    "s_waitcnt lgkmcnt(2)\n" \
    "v_mfma_f32_16x16x32_bf16 v[12:15], v[88:91], v[72:75], v[12:15]\n" \
    "buffer_load_dwordx4 v[56:59], %[vo], %[rw], s46 offen offset:1024 sc1\n" \
    "ds_read_b128 v[88:91], %[wl] offset:17408\n" \
    "s_waitcnt vmcnt(11)\n" \
    "v_mfma_f32_16x16x32_bf16 v[0:3], a[60:63], v[76:79], v[0:3]\n" \
    "v_max3_u32 v104, v104, v76, v77\n" \
    "v_mfma_f32_16x16x32_bf16 v[4:7], a[188:191], v[76:79], v[4:7]\n" \
    "v_max3_u32 v104, v104, v78, v79\n" \
    "v_mfma_f32_16x16x32_bf16 v[8:11], v[188:191], v[76:79], v[8:11]\n" \
    "s_waitcnt lgkmcnt(2)\n" \
    "v_mfma_f32_16x16x32_bf16 v[12:15], v[80:83], v[76:79], v[12:15]\n" \
    "buffer_load_dwordx4 v[60:63], %[vo], %[rw], s46 offen offset:1280 sc1\n" \
    "ds_read_b128 v[80:83], %[wl] offset:18432\n" \
    "s_waitcnt vmcnt(11)\n" \
    "v_mfma_f32_16x16x32_bf16 v[0:3], a[64:67], v[16:19], v[0:3]\n" \
    "v_max3_u32 v104, v104, v16, v17\n" \
    "v_mfma_f32_16x16x32_bf16 v[4:7], a[192:195], v[16:19], v[4:7]\n" \
    "v_max3_u32 v104, v104, v18, v19\n" \
    "v_mfma_f32_16x16x32_bf16 v[8:11], v[192:195], v[16:19], v[8:11]\n" \
    "s_waitcnt lgkmcnt(2)\n" \
    "v_mfma_f32_16x16x32_bf16 v[12:15], v[84:87], v[16:19], v[12:15]\n" \
    "buffer_load_dwordx4 v[64:67], %[vo], %[rw], s46 offen offset:2048 sc1\n" \
    "ds_read_b128 v[84:87], %[wl] offset:19456\n" \
    "s_waitcnt vmcnt(11)\n" \
    "v_mfma_f32_16x16x32_bf16 v[0:3], a[68:71], v[20:23], v[0:3]\n" \
    "v_max3_u32 v104, v104, v20, v21\n" \
    "v_mfma_f32_16x16x32_bf16 v[4:7], a[196:199], v[20:23], v[4:7]\n" \
    "v_max3_u32 v104, v104, v22, v23\n" \
    "v_mfma_f32_16x16x32_bf16 v[8:11], v[196:199], v[20:23], v[8:11]\n" \
    "s_waitcnt lgkmcnt(2)\n" \
    "v_mfma_f32_16x16x32_bf16 v[12:15], v[88:91], v[20:23], v[12:15]\n" \
    "buffer_load_dwordx4 v[68:71], %[vo], %[rw], s46 offen offset:2304 sc1\n" \
    "ds_read_b128 v[88:91], %[wl] offset:20480\n" \
    "s_waitcnt vmcnt(11)\n" \
    "v_mfma_f32_16x16x32_bf16 v[0:3], a[72:75], v[24:27], v[0:3]\n" \
    "v_max3_u32 v104, v104, v24, v25\n" \
    "v_mfma_f32_16x16x32_bf16 v[4:7], a[200:203], v[24:27], v[4:7]\n" \
    "v_max3_u32 v104, v104, v26, v27\n" \
    "v_mfma_f32_16x16x32_bf16 v[8:11], v[200:203], v[24:27], v[8:11]\n" \
    "s_waitcnt lgkmcnt(2)\n" \
    "v_mfma_f32_16x16x32_bf16 v[12:15], v[80:83], v[24:27], v[12:15]\n" \
    "buffer_load_dwordx4 v[72:75], %[vo], %[rw], s46 offen offset:3072 sc1\n" \
    "ds_read_b128 v[80:83], %[wl] offset:21504\n" \
    "s_waitcnt vmcnt(11)\n" \
    "v_mfma_f32_16x16x32_bf16 v[0:3], a[76:79], v[28:31], v[0:3]\n" \
    "v_max3_u32 v104, v104, v28, v29\n" \
    "v_mfma_f32_16x16x32_bf16 v[4:7], a[204:207], v[28:31], v[4:7]\n" \
    "v_max3_u32 v104, v104, v30, v31\n" \
    "v_mfma_f32_16x16x32_bf16 v[8:11], v[204:207], v[28:31], v[8:11]\n" \
    "s_waitcnt lgkmcnt(2)\n" \
    "v_mfma_f32_16x16x32_bf16 v[12:15], v[84:87], v[28:31], v[12:15]\n" \
    "buffer_load_dwordx4 v[76:79], %[vo], %[rw], s46 offen offset:3328 sc1\n" \
    "ds_read_b128 v[84:87], %[wl] offset:22528\n" \
    "s_waitcnt vmcnt(11)\n" \
    "v_mfma_f32_16x16x32_bf16 v[0:3], a[80:83], v[32:35], v[0:3]\n" \
    "v_max3_u32 v104, v104, v32, v33\n" \
    "v_mfma_f32_16x16x32_bf16 v[4:7], a[208:211], v[32:35], v[4:7]\n" \
    "v_max3_u32 v104, v104, v34, v35\n" \
    "v_mfma_f32_16x16x32_bf16 v[8:11], v[208:211], v[32:35], v[8:11]\n" \
    "s_waitcnt lgkmcnt(2)\n" \
    "v_mfma_f32_16x16x32_bf16 v[12:15], v[88:91], v[32:35], v[12:15]\n" \
    "buffer_load_dwordx2 v[106:107], %[bv], %[rw], %[bs] offen sc1\n" \
    "ds_read_b128 v[88:91], %[wl] offset:23552\n" \
    "s_waitcnt vmcnt(11)\n" \
    "v_mfma_f32_16x16x32_bf16 v[0:3], a[84:87], v[36:39], v[0:3]\n" \
    "v_max3_u32 v104, v104, v36, v37\n" \
    "v_mfma_f32_16x16x32_bf16 v[4:7], a[212:215], v[36:39], v[4:7]\n" \
    "v_max3_u32 v104, v104, v38, v39\n" \
    "v_mfma_f32_16x16x32_bf16 v[8:11], v[212:215], v[36:39], v[8:11]\n" \
    "s_waitcnt lgkmcnt(2)\n" \
    "v_mfma_f32_16x16x32_bf16 v[12:15], v[80:83], v[36:39], v[12:15]\n" \
    "buffer_load_dword v108, %[gv], %[rw], %[gs] offen sc1\n" \
    "ds_read_b128 v[80:83], %[wl] offset:24576\n" \
    "s_waitcnt vmcnt(11)\n" \
    "v_mfma_f32_16x16x32_bf16 v[0:3], a[88:91], v[40:43], v[0:3]\n" \
    "v_max3_u32 v104, v104, v40, v41\n" \
    "v_mfma_f32_16x16x32_bf16 v[4:7], a[216:219], v[40:43], v[4:7]\n" \
    "v_max3_u32 v104, v104, v42, v43\n" \
    "v_mfma_f32_16x16x32_bf16 v[8:11], v[216:219], v[40:43], v[8:11]\n" \
    "s_waitcnt lgkmcnt(2)\n" \
    "v_mfma_f32_16x16x32_bf16 v[12:15], v[84:87], v[40:43], v[12:15]\n" \
    "buffer_load_dword v105, %[sv], %[rw], %[ss] offen sc1\n" \
    "ds_read_b128 v[84:87], %[wl] offset:25600\n" \
    "s_waitcnt vmcnt(11)\n" \
    "v_mfma_f32_16x16x32_bf16 v[0:3], a[92:95], v[44:47], v[0:3]\n" \
    "v_max3_u32 v104, v104, v44, v45\n" \
    "v_mfma_f32_16x16x32_bf16 v[4:7], a[220:223], v[44:47], v[4:7]\n" \
    "v_max3_u32 v104, v104, v46, v47\n" \
    "v_mfma_f32_16x16x32_bf16 v[8:11], v[220:223], v[44:47], v[8:11]\n" \
    "s_waitcnt lgkmcnt(2)\n" \
    "v_mfma_f32_16x16x32_bf16 v[12:15], v[88:91], v[44:47], v[12:15]\n" \
    "ds_read_b128 v[88:91], %[wl] offset:26624\n" \
    "s_waitcnt vmcnt(10)\n" \
    "v_mfma_f32_16x16x32_bf16 v[0:3], a[96:99], v[48:51], v[0:3]\n" \
    "v_max3_u32 v104, v104, v48, v49\n" \
    "v_mfma_f32_16x16x32_bf16 v[4:7], a[224:227], v[48:51], v[4:7]\n" \
    "v_max3_u32 v104, v104, v50, v51\n" \
    "v_mfma_f32_16x16x32_bf16 v[8:11], v[224:227], v[48:51], v[8:11]\n" \
    "s_waitcnt lgkmcnt(2)\n" \
    "v_mfma_f32_16x16x32_bf16 v[12:15], v[80:83], v[48:51], v[12:15]\n" \
    "ds_read_b128 v[80:83], %[wl] offset:27648\n" \
    "s_waitcnt vmcnt(9)\n" \
    "v_mfma_f32_16x16x32_bf16 v[0:3], a[100:103], v[52:55], v[0:3]\n" \
    "v_max3_u32 v104, v104, v52, v53\n" \
    "v_mfma_f32_16x16x32_bf16 v[4:7], a[228:231], v[52:55], v[4:7]\n" \
    "v_max3_u32 v104, v104, v54, v55\n" \
    "v_mfma_f32_16x16x32_bf16 v[8:11], v[228:231], v[52:55], v[8:11]\n" \
    "s_waitcnt lgkmcnt(2)\n" \
    "v_mfma_f32_16x16x32_bf16 v[12:15], v[84:87], v[52:55], v[12:15]\n" \
    "ds_read_b128 v[84:87], %[wl] offset:28672\n" \
    "s_waitcnt vmcnt(8)\n" \
    "v_mfma_f32_16x16x32_bf16 v[0:3], a[104:107], v[56:59], v[0:3]\n" \
    "v_max3_u32 v104, v104, v56, v57\n" \
    "v_mfma_f32_16x16x32_bf16 v[4:7], a[232:235], v[56:59], v[4:7]\n" \
    "v_max3_u32 v104, v104, v58, v59\n" \
    "v_mfma_f32_16x16x32_bf16 v[8:11], v[232:235], v[56:59], v[8:11]\n" \
    "s_waitcnt lgkmcnt(2)\n" \
    "v_mfma_f32_16x16x32_bf16 v[12:15], v[88:91], v[56:59], v[12:15]\n" \
    "ds_read_b128 v[88:91], %[wl] offset:29696\n" \
    "s_waitcnt vmcnt(7)\n" \
    "v_mfma_f32_16x16x32_bf16 v[0:3], a[108:111], v[60:63], v[0:3]\n" \
    "v_max3_u32 v104, v104, v60, v61\n" \
    "v_mfma_f32_16x16x32_bf16 v[4:7], a[236:239], v[60:63], v[4:7]\n" \
    "v_max3_u32 v104, v104, v62, v63\n" \
    "v_mfma_f32_16x16x32_bf16 v[8:11], v[236:239], v[60:63], v[8:11]\n" \
    "s_waitcnt lgkmcnt(2)\n" \
    "v_mfma_f32_16x16x32_bf16 v[12:15], v[80:83], v[60:63], v[12:15]\n" \
    "ds_read_b128 v[80:83], %[wl] offset:30720\n" \
    "s_waitcnt vmcnt(6)\n" \
    "v_mfma_f32_16x16x32_bf16 v[0:3], a[112:115], v[64:67], v[0:3]\n" \
    "v_max3_u32 v104, v104, v64, v65\n" \
    "v_mfma_f32_16x16x32_bf16 v[4:7], a[240:243], v[64:67], v[4:7]\n" \
    "v_max3_u32 v104, v104, v66, v67\n" \
    "v_mfma_f32_16x16x32_bf16 v[8:11], v[240:243], v[64:67], v[8:11]\n" \
    "s_waitcnt lgkmcnt(2)\n" \
    "v_mfma_f32_16x16x32_bf16 v[12:15], v[84:87], v[64:67], v[12:15]\n" \
    "ds_read_b128 v[84:87], %[wl] offset:31744\n" \
    "s_waitcnt vmcnt(5)\n" \
    "v_mfma_f32_16x16x32_bf16 v[0:3], a[116:119], v[68:71], v[0:3]\n" \
    "v_max3_u32 v104, v104, v68, v69\n" \
    "v_mfma_f32_16x16x32_bf16 v[4:7], a[244:247], v[68:71], v[4:7]\n" \
    "v_max3_u32 v104, v104, v70, v71\n" \
    "v_mfma_f32_16x16x32_bf16 v[8:11], v[244:247], v[68:71], v[8:11]\n" \
    "s_waitcnt lgkmcnt(2)\n" \
    "v_mfma_f32_16x16x32_bf16 v[12:15], v[88:91], v[68:71], v[12:15]\n" \
    "s_waitcnt vmcnt(4)\n" \
    "v_mfma_f32_16x16x32_bf16 v[0:3], a[120:123], v[72:75], v[0:3]\n" \
    "v_max3_u32 v104, v104, v72, v73\n" \
    "v_mfma_f32_16x16x32_bf16 v[4:7], a[248:251], v[72:75], v[4:7]\n" \
    "v_max3_u32 v104, v104, v74, v75\n" \
    "v_mfma_f32_16x16x32_bf16 v[8:11], v[248:251], v[72:75], v[8:11]\n" \
    "s_waitcnt lgkmcnt(1)\n" \
    "v_mfma_f32_16x16x32_bf16 v[12:15], v[80:83], v[72:75], v[12:15]\n" \
    "s_waitcnt vmcnt(3)\n" \
    "v_mfma_f32_16x16x32_bf16 v[0:3], a[124:127], v[76:79], v[0:3]\n" \
    "v_max3_u32 v104, v104, v76, v77\n" \
    "v_mfma_f32_16x16x32_bf16 v[4:7], a[252:255], v[76:79], v[4:7]\n" \
    "v_max3_u32 v104, v104, v78, v79\n" \
    "v_mfma_f32_16x16x32_bf16 v[8:11], v[252:255], v[76:79], v[8:11]\n" \
    "s_waitcnt lgkmcnt(0)\n" \
    "v_mfma_f32_16x16x32_bf16 v[12:15], v[84:87], v[76:79], v[12:15]\n" \
    "s_waitcnt vmcnt(0)\n" \
    "s_nop 7\n" \
    ""
#define OUT8_ASM \
    "s_mov_b32 exec_lo, %[tm]\n" \
    "s_mov_b32 exec_hi, 0\n" \
    "buffer_load_dword v79, %[tv], %[rw], %[so] offen sc1\n" \
    "s_mov_b64 exec, -1\n" \
    "s_add_i32 s44, %[so], 4096\n" \
    "s_add_i32 s45, %[so], 8192\n" \
    "s_add_i32 s46, %[so], 12288\n" \
    "buffer_load_dwordx4 v[16:19], %[vo], %[rw], %[so] offen offset:0 sc1\n" \
    "ds_read_b128 v[80:83], %[wl] offset:0\n" \
    "ds_read_b128 v[84:87], %[wl] offset:1024\n" \
    "ds_read_b128 v[88:91], %[wl] offset:2048\n" \
    "ds_read_b128 v[92:95], %[wl] offset:3072\n" \
    "v_mov_b64 v[0:1], 0\n" \
    "v_mov_b64 v[2:3], 0\n" \
    "v_mov_b64 v[4:5], 0\n" \
    "v_mov_b64 v[6:7], 0\n" \
    "v_mov_b64 v[8:9], 0\n" \
    "v_mov_b64 v[10:11], 0\n" \
    "v_mov_b64 v[12:13], 0\n" \
    "v_mov_b64 v[14:15], 0\n" \
    "v_mov_b32 v104, 0\n" \
    "buffer_load_dwordx4 v[20:23], %[vo], %[rw], %[so] offen offset:1024 sc1\n" \
    "s_nop 7\n" \
    "s_nop 3\n" \
    "buffer_load_dwordx4 v[24:27], %[vo], %[rw], %[so] offen offset:2048 sc1\n" \
    "s_nop 7\n" \
    "s_nop 3\n" \
    "buffer_load_dwordx4 v[28:31], %[vo], %[rw], %[so] offen offset:3072 sc1\n" \
    "s_nop 7\n" \
    "s_nop 3\n" \
    "buffer_load_dwordx4 v[32:35], %[vo], %[rw], s44 offen offset:0 sc1\n" \
    "s_nop 7\n" \
    "s_nop 3\n" \
    "buffer_load_dwordx4 v[36:39], %[vo], %[rw], s44 offen offset:1024 sc1\n" \
    "s_nop 7\n" \
    "s_nop 3\n" \
    "buffer_load_dwordx4 v[40:43], %[vo], %[rw], s44 offen offset:2048 sc1\n" \
    "s_nop 7\n" \
    "s_nop 3\n" \
    "buffer_load_dwordx4 v[44:47], %[vo], %[rw], s44 offen offset:3072 sc1\n" \
    "s_nop 7\n" \
    "s_nop 3\n" \
    "buffer_load_dwordx4 v[48:51], %[vo], %[rw], s45 offen offset:0 sc1\n" \
    "s_nop 7\n" \
    "s_nop 3\n" \
    "buffer_load_dwordx4 v[52:55], %[vo], %[rw], s45 offen offset:1024 sc1\n" \
    "ds_read_b128 v[96:99], %[wl] offset:4096\n" \
    "ds_read_b128 v[100:103], %[wl] offset:5120\n" \
    "s_waitcnt vmcnt(9)\n" \
    "v_smfmac_f32_16x16x64_bf16 v[0:3], v[16:19], a[0:7], %[idx]\n" \
    "v_max3_u32 v104, v104, v16, v17\n" \
    "v_smfmac_f32_16x16x64_bf16 v[4:7], v[16:19], a[128:135], %[idx]\n" \
    "v_max3_u32 v104, v104, v18, v19\n" \
    "v_smfmac_f32_16x16x64_bf16 v[8:11], v[16:19], v[128:135], %[idx]\n" \
    "s_waitcnt lgkmcnt(4)\n" \
    "v_smfmac_f32_16x16x64_bf16 v[12:15], v[16:19], v[80:87], %[idx]\n" \
    "buffer_load_dwordx4 v[56:59], %[vo], %[rw], s45 offen offset:2048 sc1\n" \
    "ds_read_b128 v[80:83], %[wl] offset:6144\n" \
    "ds_read_b128 v[84:87], %[wl] offset:7168\n" \
    "s_waitcnt vmcnt(9)\n" \
    "v_smfmac_f32_16x16x64_bf16 v[0:3], v[20:23], a[8:15], %[idx]\n" \
    "v_max3_u32 v104, v104, v20, v21\n" \
    "v_smfmac_f32_16x16x64_bf16 v[4:7], v[20:23], a[136:143], %[idx]\n" \
    "v_max3_u32 v104, v104, v22, v23\n" \
    "v_smfmac_f32_16x16x64_bf16 v[8:11], v[20:23], v[136:143], %[idx]\n" \
    "s_waitcnt lgkmcnt(4)\n" \
    "v_smfmac_f32_16x16x64_bf16 v[12:15], v[20:23], v[88:95], %[idx]\n" \
    "buffer_load_dwordx4 v[60:63], %[vo], %[rw], s45 offen offset:3072 sc1\n" \
    "ds_read_b128 v[88:91], %[wl] offset:8192\n" \
    "ds_read_b128 v[92:95], %[wl] offset:9216\n" \
    "s_waitcnt vmcnt(9)\n" \
    "v_smfmac_f32_16x16x64_bf16 v[0:3], v[24:27], a[16:23], %[idx]\n" \
    "v_max3_u32 v104, v104, v24, v25\n" \
    "v_smfmac_f32_16x16x64_bf16 v[4:7], v[24:27], a[144:151], %[idx]\n" \
    "v_max3_u32 v104, v104, v26, v27\n" \
    "v_smfmac_f32_16x16x64_bf16 v[8:11], v[24:27], v[144:151], %[idx]\n" \
    "s_waitcnt lgkmcnt(4)\n" \
    "v_smfmac_f32_16x16x64_bf16 v[12:15], v[24:27], v[96:103], %[idx]\n" \
    "buffer_load_dwordx4 v[64:67], %[vo], %[rw], s46 offen offset:0 sc1\n" \
    "ds_read_b128 v[96:99], %[wl] offset:10240\n" \
    "ds_read_b128 v[100:103], %[wl] offset:11264\n" \
    "s_waitcnt vmcnt(9)\n" \
    "v_smfmac_f32_16x16x64_bf16 v[0:3], v[28:31], a[24:31], %[idx]\n" \
    "v_max3_u32 v104, v104, v28, v29\n" \
    "v_smfmac_f32_16x16x64_bf16 v[4:7], v[28:31], a[152:159], %[idx]\n" \
    "v_max3_u32 v104, v104, v30, v31\n" \
    "v_smfmac_f32_16x16x64_bf16 v[8:11], v[28:31], v[152:159], %[idx]\n" \
    "s_waitcnt lgkmcnt(4)\n" \
    "v_smfmac_f32_16x16x64_bf16 v[12:15], v[28:31], v[80:87], %[idx]\n" \
    "buffer_load_dwordx4 v[68:71], %[vo], %[rw], s46 offen offset:1024 sc1\n" \
    "ds_read_b128 v[80:83], %[wl] offset:12288\n" \
    "ds_read_b128 v[84:87], %[wl] offset:13312\n" \
    "s_waitcnt vmcnt(9)\n" \
    "v_smfmac_f32_16x16x64_bf16 v[0:3], v[32:35], a[32:39], %[idx]\n" \
    "v_max3_u32 v104, v104, v32, v33\n" \
    "v_smfmac_f32_16x16x64_bf16 v[4:7], v[32:35], a[160:167], %[idx]\n" \
    "v_max3_u32 v104, v104, v34, v35\n" \
    "v_smfmac_f32_16x16x64_bf16 v[8:11], v[32:35], v[160:167], %[idx]\n" \
    "s_waitcnt lgkmcnt(4)\n" \
    "v_smfmac_f32_16x16x64_bf16 v[12:15], v[32:35], v[88:95], %[idx]\n" \
    "buffer_load_dwordx4 v[72:75], %[vo], %[rw], s46 offen offset:2048 sc1\n" \
    "ds_read_b128 v[88:91], %[wl] offset:14336\n" \
    "ds_read_b128 v[92:95], %[wl] offset:15360\n" \
    "s_waitcnt vmcnt(9)\n" \
    "v_smfmac_f32_16x16x64_bf16 v[0:3], v[36:39], a[40:47], %[idx]\n" \
    "v_max3_u32 v104, v104, v36, v37\n" \
    "v_smfmac_f32_16x16x64_bf16 v[4:7], v[36:39], a[168:175], %[idx]\n" \
    "v_max3_u32 v104, v104, v38, v39\n" \
    "v_smfmac_f32_16x16x64_bf16 v[8:11], v[36:39], v[168:175], %[idx]\n" \
    "s_waitcnt lgkmcnt(4)\n" \
    "v_smfmac_f32_16x16x64_bf16 v[12:15], v[36:39], v[96:103], %[idx]\n" \
    "buffer_load_dwordx4 v[76:79], %[vo], %[rw], s46 offen offset:3072 sc1\n" \
    "ds_read_b128 v[96:99], %[wl] offset:16384\n" \
    "ds_read_b128 v[100:103], %[wl] offset:17408\n" \
    "s_waitcnt vmcnt(9)\n" \
    "v_smfmac_f32_16x16x64_bf16 v[0:3], v[40:43], a[48:55], %[idx]\n" \
    "v_max3_u32 v104, v104, v40, v41\n" \
    "v_smfmac_f32_16x16x64_bf16 v[4:7], v[40:43], a[176:183], %[idx]\n" \
    "v_max3_u32 v104, v104, v42, v43\n" \
    "v_smfmac_f32_16x16x64_bf16 v[8:11], v[40:43], v[176:183], %[idx]\n" \
    "s_waitcnt lgkmcnt(4)\n" \
    "v_smfmac_f32_16x16x64_bf16 v[12:15], v[40:43], v[80:87], %[idx]\n" \
    "buffer_load_dwordx2 v[106:107], %[bv], %[rw], %[bs] offen sc1\n" \
    "ds_read_b128 v[80:83], %[wl] offset:18432\n" \
    "ds_read_b128 v[84:87], %[wl] offset:19456\n" \
    "s_waitcnt vmcnt(9)\n" \
    "v_smfmac_f32_16x16x64_bf16 v[0:3], v[44:47], a[56:63], %[idx]\n" \
    "v_max3_u32 v104, v104, v44, v45\n" \
    "v_smfmac_f32_16x16x64_bf16 v[4:7], v[44:47], a[184:191], %[idx]\n" \
    "v_max3_u32 v104, v104, v46, v47\n" \
    "v_smfmac_f32_16x16x64_bf16 v[8:11], v[44:47], v[184:191], %[idx]\n" \
    "s_waitcnt lgkmcnt(4)\n" \
    "v_smfmac_f32_16x16x64_bf16 v[12:15], v[44:47], v[88:95], %[idx]\n" \
    "buffer_load_dword v108, %[gv], %[rw], %[gs] offen sc1\n" \
    "ds_read_b128 v[88:91], %[wl] offset:20480\n" \
    "ds_read_b128 v[92:95], %[wl] offset:21504\n" \
    "s_waitcnt vmcnt(9)\n" \
    "v_smfmac_f32_16x16x64_bf16 v[0:3], v[48:51], a[64:71], %[idx]\n" \
    "v_max3_u32 v104, v104, v48, v49\n" \
    "v_smfmac_f32_16x16x64_bf16 v[4:7], v[48:51], a[192:199], %[idx]\n" \
    "v_max3_u32 v104, v104, v50, v51\n" \
    "v_smfmac_f32_16x16x64_bf16 v[8:11], v[48:51], v[192:199], %[idx]\n" \
    "s_waitcnt lgkmcnt(4)\n" \
    "v_smfmac_f32_16x16x64_bf16 v[12:15], v[48:51], v[96:103], %[idx]\n" \
    "buffer_load_dword v105, %[sv], %[rw], %[ss] offen sc1\n" \
    "ds_read_b128 v[96:99], %[wl] offset:22528\n" \
    "ds_read_b128 v[100:103], %[wl] offset:23552\n" \
    "s_waitcnt vmcnt(9)\n" \
    "v_smfmac_f32_16x16x64_bf16 v[0:3], v[52:55], a[72:79], %[idx]\n" \
    "v_max3_u32 v104, v104, v52, v53\n" \
    "v_smfmac_f32_16x16x64_bf16 v[4:7], v[52:55], a[200:207], %[idx]\n" \
    "v_max3_u32 v104, v104, v54, v55\n" \
    "v_smfmac_f32_16x16x64_bf16 v[8:11], v[52:55], v[200:207], %[idx]\n" \
    "s_waitcnt lgkmcnt(4)\n" \
    "v_smfmac_f32_16x16x64_bf16 v[12:15], v[52:55], v[80:87], %[idx]\n" \
    "ds_read_b128 v[80:83], %[wl] offset:24576\n" \
    "ds_read_b128 v[84:87], %[wl] offset:25600\n" \
    "s_waitcnt vmcnt(8)\n" \
    "v_smfmac_f32_16x16x64_bf16 v[0:3], v[56:59], a[80:87], %[idx]\n" \
    "v_max3_u32 v104, v104, v56, v57\n" \
    "v_smfmac_f32_16x16x64_bf16 v[4:7], v[56:59], a[208:215], %[idx]\n" \
    "v_max3_u32 v104, v104, v58, v59\n" \
    "v_smfmac_f32_16x16x64_bf16 v[8:11], v[56:59], v[208:215], %[idx]\n" \
    "s_waitcnt lgkmcnt(4)\n" \
    "v_smfmac_f32_16x16x64_bf16 v[12:15], v[56:59], v[88:95], %[idx]\n" \
    "ds_read_b128 v[88:91], %[wl] offset:26624\n" \
    "ds_read_b128 v[92:95], %[wl] offset:27648\n" \
    "s_waitcnt vmcnt(7)\n" \
    "v_smfmac_f32_16x16x64_bf16 v[0:3], v[60:63], a[88:95], %[idx]\n" \
    "v_max3_u32 v104, v104, v60, v61\n" \
    "v_smfmac_f32_16x16x64_bf16 v[4:7], v[60:63], a[216:223], %[idx]\n" \
    "v_max3_u32 v104, v104, v62, v63\n" \
    "v_smfmac_f32_16x16x64_bf16 v[8:11], v[60:63], v[216:223], %[idx]\n" \
    "s_waitcnt lgkmcnt(4)\n" \
    "v_smfmac_f32_16x16x64_bf16 v[12:15], v[60:63], v[96:103], %[idx]\n" \
    "ds_read_b128 v[96:99], %[wl] offset:28672\n" \
    "ds_read_b128 v[100:103], %[wl] offset:29696\n" \
    "s_waitcnt vmcnt(6)\n" \
    "v_smfmac_f32_16x16x64_bf16 v[0:3], v[64:67], a[96:103], %[idx]\n" \
    "v_max3_u32 v104, v104, v64, v65\n" \
    "v_smfmac_f32_16x16x64_bf16 v[4:7], v[64:67], a[224:231], %[idx]\n" \
    "v_max3_u32 v104, v104, v66, v67\n" \
    "v_smfmac_f32_16x16x64_bf16 v[8:11], v[64:67], v[224:231], %[idx]\n" \
    "s_waitcnt lgkmcnt(4)\n" \
    "v_smfmac_f32_16x16x64_bf16 v[12:15], v[64:67], v[80:87], %[idx]\n" \
    "ds_read_b128 v[80:83], %[wl] offset:30720\n" \
    "ds_read_b128 v[84:87], %[wl] offset:31744\n" \
    "s_waitcnt vmcnt(5)\n" \
    "v_smfmac_f32_16x16x64_bf16 v[0:3], v[68:71], a[104:111], %[idx]\n" \
    "v_max3_u32 v104, v104, v68, v69\n" \
    "v_smfmac_f32_16x16x64_bf16 v[4:7], v[68:71], a[232:239], %[idx]\n" \
    "v_max3_u32 v104, v104, v70, v71\n" \
    "v_smfmac_f32_16x16x64_bf16 v[8:11], v[68:71], v[232:239], %[idx]\n" \
    "s_waitcnt lgkmcnt(4)\n" \
    "v_smfmac_f32_16x16x64_bf16 v[12:15], v[68:71], v[88:95], %[idx]\n" \
    "s_waitcnt vmcnt(4)\n" \
    "v_smfmac_f32_16x16x64_bf16 v[0:3], v[72:75], a[112:119], %[idx]\n" \
    "v_max3_u32 v104, v104, v72, v73\n" \
    "v_smfmac_f32_16x16x64_bf16 v[4:7], v[72:75], a[240:247], %[idx]\n" \
    "v_max3_u32 v104, v104, v74, v75\n" \
    "v_smfmac_f32_16x16x64_bf16 v[8:11], v[72:75], v[240:247], %[idx]\n" \
    "s_waitcnt lgkmcnt(2)\n" \
    "v_smfmac_f32_16x16x64_bf16 v[12:15], v[72:75], v[96:103], %[idx]\n" \
    "s_waitcnt vmcnt(3)\n" \
    "v_smfmac_f32_16x16x64_bf16 v[0:3], v[76:79], a[120:127], %[idx]\n" \
    "v_max3_u32 v104, v104, v76, v77\n" \
    "v_smfmac_f32_16x16x64_bf16 v[4:7], v[76:79], a[248:255], %[idx]\n" \
    "v_max3_u32 v104, v104, v78, v79\n" \
    "v_smfmac_f32_16x16x64_bf16 v[8:11], v[76:79], v[248:255], %[idx]\n" \
    "s_waitcnt lgkmcnt(0)\n" \
    "v_smfmac_f32_16x16x64_bf16 v[12:15], v[76:79], v[80:87], %[idx]\n" \
    "s_waitcnt vmcnt(0)\n" \
    "s_nop 5\n" \
    "v_permlane32_swap_b32 v0, v8\n" \
    "v_add_f32 v0, v0, v8\n" \
    "v_permlane32_swap_b32 v1, v9\n" \
    "v_add_f32 v1, v1, v9\n" \
    "v_permlane32_swap_b32 v2, v10\n" \
    "v_add_f32 v2, v2, v10\n" \
    "v_permlane32_swap_b32 v3, v11\n" \
    "v_add_f32 v3, v3, v11\n" \
    "v_permlane32_swap_b32 v4, v12\n" \
    "v_add_f32 v4, v4, v12\n" \
    "v_permlane32_swap_b32 v5, v13\n" \
    "v_add_f32 v5, v5, v13\n" \
    "v_permlane32_swap_b32 v6, v14\n" \
    "v_add_f32 v6, v6, v14\n" \
    "v_permlane32_swap_b32 v7, v15\n" \
    "v_add_f32 v7, v7, v15\n" \
    ""
#define OUT_CLOBBER "v16", "v17", "v18", "v19", "v20", "v21", "v22", "v23", "v24", "v25", "v26", "v27", "v28", "v29", "v30", "v31", "v32", "v33", "v34", "v35", "v36", "v37", "v38", "v39", "v40", "v41", "v42", "v43", "v44", "v45", "v46", "v47", "v48", "v49", "v50", "v51", "v52", "v53", "v54", "v55", "v56", "v57", "v58", "v59", "v60", "v61", "v62", "v63", "v64", "v65", "v66", "v67", "v68", "v69", "v70", "v71", "v72", "v73", "v74", "v75", "v76", "v77", "v78", "v79", "v80", "v81", "v82", "v83", "v84", "v85", "v86", "v87", "v88", "v89", "v90", "v91", "v92", "v93", "v94", "v95", "v96", "v97", "v98", "v99", "v100", "v101", "v102", "v103", "s44", "s45", "s46", "scc", "memory"
constexpr int NREP = 2, NCP = 4;
#define OWNER_ASM \
    "s_getpc_b64 s[96:97]\n" \
    "14:\n" \
    "s_add_u32 s96, s96, 2f-14b\n" \
    "s_addc_u32 s97, s97, 0\n" \
    "s_sub_u32 s68, s96, 2f-11f\n" \
    "s_subb_u32 s69, s97, 0\n" \
    "s_mov_b32 %[np], 0\n" \
    "13:\n" \
    "s_memtime vcc\n" \
    "s_waitcnt lgkmcnt(0)\n" \
    "s_sub_i32 s70, %[lk], vcc_lo\n" \
    "s_cmp_gt_i32 s70, %[rd]\n" \
    "s_cbranch_scc1 13b\n" \
    "buffer_store_dwordx4 %[sv], %[ro], %[rs], %[pq] offen offset:0 sc1\n" \
    "buffer_store_dwordx4 %[sv], %[ro], %[rs], %[pq] offen offset:1024 sc1\n" \
    "buffer_store_dwordx4 %[sv], %[ro], %[rs], %[pq] offen offset:2048 sc1\n" \
    "buffer_store_dwordx4 %[sv], %[ro], %[rs], %[pq] offen offset:3072 sc1\n" \
    "v_mov_b32 v2, -1\n" \
    "buffer_store_dword v2, %[rm], %[rs], %[mq] offen sc1\n" \
    "s_mov_b32 s71, 4\n" \
    "10:\n" \
    "s_memtime vcc\n" \
    "s_waitcnt lgkmcnt(0)\n" \
    "s_sub_i32 s70, %[lk], vcc_lo\n" \
    "s_cmp_gt_i32 s70, 112\n" \
    "s_cbranch_scc1 10b\n" \
    "s_max_i32 s70, s70, 0\n" \
    "s_memtime s[58:59]\n" \
    "s_memrealtime s[44:45]\n" \
    "s_nop 1\n" \
    "s_memrealtime s[46:47]\n" \
    "s_nop 1\n" \
    "s_memrealtime s[48:49]\n" \
    "s_nop 1\n" \
    "s_memrealtime s[50:51]\n" \
    "s_nop 1\n" \
    "s_memrealtime s[52:53]\n" \
    "s_nop 1\n" \
    "s_memrealtime s[54:55]\n" \
    "s_nop 1\n" \
    "s_memrealtime s[56:57]\n" \
    "s_nop 1\n" \
    "s_memrealtime vcc\n" \
    "s_waitcnt lgkmcnt(0)\n" \
    "s_add_i32 s44, s44, s46\n" \
    "s_add_i32 s48, s48, s50\n" \
    "s_add_i32 s52, s52, s54\n" \
    "s_add_i32 s44, s44, s48\n" \
    "s_add_i32 s52, s52, s56\n" \
    "s_add_i32 s44, s44, s52\n" \
    "s_add_i32 s59, s44, vcc_lo\n" \
    "s_sub_i32 s44, s59, %[oT]\n" \
    "s_mov_b32 %[oT], s59\n" \
    "s_sub_i32 s45, s58, %[oC]\n" \
    "s_mov_b32 %[oC], s58\n" \
    "s_mul_i32 s46, s44, %[oR]\n" \
    "s_lshr_b32 s46, s46, 12\n" \
    "s_sub_i32 s46, s45, s46\n" \
    "s_sub_i32 s44, s44, 1000\n" \
    "s_cmp_lt_u32 s44, 480\n" \
    "s_cselect_b32 s46, s46, 0\n" \
    "s_add_i32 %[oR], %[oR], s46\n" \
    "s_sub_i32 s45, %[oT], %[c8]\n" \
    "s_and_b32 s70, s70, -4\n" \
    "s_sub_u32 vcc_lo, s68, s70\n" \
    "s_subb_u32 vcc_hi, s69, 0\n" \
    "s_setpc_b64 vcc\n" \
    "s_nop 0\n" \
    "s_nop 0\n" \
    "s_nop 0\n" \
    "s_nop 0\n" \
    "s_nop 0\n" \
    "s_nop 0\n" \
    "s_nop 0\n" \
    "s_nop 0\n" \
    "s_nop 0\n" \
    "s_nop 0\n" \
    "s_nop 0\n" \
    "s_nop 0\n" \
    "s_nop 0\n" \
    "s_nop 0\n" \
    "s_nop 0\n" \
    "s_nop 0\n" \
    "s_nop 0\n" \
    "s_nop 0\n" \
    "s_nop 0\n" \
    "s_nop 0\n" \
    "s_nop 0\n" \
    "s_nop 0\n" \
    "s_nop 0\n" \
    "s_nop 0\n" \
    "s_nop 0\n" \
    "s_nop 0\n" \
    "s_nop 0\n" \
    "s_nop 0\n" \
    "11:\n" \
    "buffer_load_dwordx2 v[0:1], %[ca], %[rs], %[cs] offen sc1\n" \
    "s_waitcnt vmcnt(0)\n" \
    "6:\n" \
    "s_memtime s[46:47]\n" \
    "v_subrev_u32 v0, s45, v0\n" \
    "v_cmp_ne_u16 vcc, %[tag], v1\n" \
    "s_add_i32 s48, %[oC], -136\n" \
    "v_max_i32_dpp v0, v0, v0 row_shr:1 row_mask:0xf bank_mask:0xf\n" \
    "v_max_u32_dpp v1, v1, v1 row_shr:1 row_mask:0xf bank_mask:0xf\n" \
    "s_cbranch_vccnz 7f\n" \
    "8:\n" \
    "v_max_i32_dpp v0, v0, v0 row_shr:2 row_mask:0xf bank_mask:0xf\n" \
    "v_max_u32_dpp v1, v1, v1 row_shr:2 row_mask:0xf bank_mask:0xf\n" \
    "s_nop 0\n" \
    "v_max_i32_dpp v0, v0, v0 row_shr:4 row_mask:0xf bank_mask:0xf\n" \
    "v_max_u32_dpp v1, v1, v1 row_shr:4 row_mask:0xf bank_mask:0xf\n" \
    "s_nop 0\n" \
    "v_max_i32_dpp v0, v0, v0 row_shr:8 row_mask:0xf bank_mask:0xf\n" \
    "v_max_u32_dpp v1, v1, v1 row_shr:8 row_mask:0xf bank_mask:0xf\n" \
    "s_waitcnt lgkmcnt(0)\n" \
    "v_max_i32_dpp v0, v0, v0 row_bcast:15 row_mask:0xa bank_mask:0xf\n" \
    "v_max_u32_dpp v1, v1, v1 row_bcast:15 row_mask:0xa bank_mask:0xf\n" \
    "s_add_i32 s46, s46, 44\n" \
    "v_max_i32_dpp v0, v0, v0 row_bcast:31 row_mask:0xc bank_mask:0xf\n" \
    "v_max_u32_dpp v1, v1, v1 row_bcast:31 row_mask:0xc bank_mask:0xf\n" \
    "v_readlane_b32 s44, v0, 63\n" \
    "v_readlane_b32 s49, v1, 63\n" \
    "s_mul_i32 s47, s44, %[oR]\n" \
    "s_ashr_i32 s47, s47, 12\n" \
    "s_add_i32 s47, s47, s48\n" \
    "s_sub_i32 s58, s47, s46\n" \
    "s_cmp_gt_i32 s58, 112\n" \
    "s_cbranch_scc1 1f\n" \
    "12:\n" \
    "s_max_i32 s58, s58, 0\n" \
    "s_and_b32 s58, s58, -4\n" \
    "s_sub_u32 vcc_lo, s96, s58\n" \
    "s_subb_u32 vcc_hi, s97, 0\n" \
    "s_setpc_b64 vcc\n" \
    "s_nop 0\n" \
    "s_nop 0\n" \
    "s_nop 0\n" \
    "s_nop 0\n" \
    "s_nop 0\n" \
    "s_nop 0\n" \
    "s_nop 0\n" \
    "s_nop 0\n" \
    "s_nop 0\n" \
    "s_nop 0\n" \
    "s_nop 0\n" \
    "s_nop 0\n" \
    "s_nop 0\n" \
    "s_nop 0\n" \
    "s_nop 0\n" \
    "s_nop 0\n" \
    "s_nop 0\n" \
    "s_nop 0\n" \
    "s_nop 0\n" \
    "s_nop 0\n" \
    "s_nop 0\n" \
    "s_nop 0\n" \
    "s_nop 0\n" \
    "s_nop 0\n" \
    "s_nop 0\n" \
    "s_nop 0\n" \
    "s_nop 0\n" \
    "s_nop 0\n" \
    "2:\n" \
    "buffer_load_dwordx4 v[0:3], %[o], %[rs], %[pb] offen offset:0 sc1\n" \
    "buffer_load_dwordx4 v[4:7], %[o], %[rs], %[pb] offen offset:1024 sc1\n" \
    "buffer_load_dwordx4 v[8:11], %[o], %[rs], %[pb] offen offset:2048 sc1\n" \
    "buffer_load_dwordx4 v[12:15], %[o], %[rs], %[pb] offen offset:3072 sc1\n" \
    "s_add_i32 %[np], %[np], 1\n" \
    "s_add_i32 %[tm], s44, s45\n" \
    "s_lshr_b32 s50, s49, 16\n" \
    "s_add_i32 s50, s50, s44\n" \
    "s_add_i32 s50, s50, %[cw]\n" \
    "s_mul_i32 s50, s50, %[oR]\n" \
    "s_ashr_i32 s50, s50, 12\n" \
    "s_add_i32 %[lk], s50, %[oC]\n" \
    "s_waitcnt vmcnt(3)\n" \
    "v_mfma_f32_16x16x32_bf16 v[16:19], %[sel], v[0:3], 0\n" \
    "v_max3_u32 v20, v0, v1, v1\n" \
    "v_max3_u32 v20, v2, v3, v20\n" \
    "s_waitcnt vmcnt(2)\n" \
    "v_mfma_f32_16x16x32_bf16 v[16:19], %[sel], v[4:7], v[16:19]\n" \
    "v_max3_u32 v20, v4, v5, v20\n" \
    "v_max3_u32 v20, v6, v7, v20\n" \
    "s_waitcnt vmcnt(1)\n" \
    "v_mfma_f32_16x16x32_bf16 v[16:19], %[sel], v[8:11], v[16:19]\n" \
    "v_max3_u32 v20, v8, v9, v20\n" \
    "v_max3_u32 v20, v10, v11, v20\n" \
    "s_waitcnt vmcnt(0)\n" \
    "v_mfma_f32_16x16x32_bf16 v[16:19], %[sel], v[12:15], v[16:19]\n" \
    "v_max3_u32 v20, v12, v13, v20\n" \
    "v_max3_u32 v20, v14, v15, v20\n" \
    "v_cmp_eq_u32 vcc, -1, v20\n" \
    "s_nop 3\n" \
    "s_cbranch_vccnz 4f\n" \
    "3:\n" \
    "v_add_f32_dpp v16, v16, v16 row_ror:8 row_mask:0xf bank_mask:0xf\n" \
    "v_add_f32_dpp v17, v17, v17 row_ror:8 row_mask:0xf bank_mask:0xf\n" \
    "v_cvt_pk_bf16_f32 v16, v16, v17\n" \
    "buffer_store_dword v16, %[so], %[rs], %[mb0] offen sc1\n" \
    "s_branch 5f\n" \
    "1:\n" \
    "s_memtime vcc\n" \
    "s_waitcnt lgkmcnt(0)\n" \
    "s_sub_i32 s58, s47, vcc_lo\n" \
    "s_cmp_gt_i32 s58, 112\n" \
    "s_cbranch_scc1 1b\n" \
    "s_branch 12b\n" \
    "4:\n" \
    "s_add_i32 %[bud], %[bud], -1\n" \
    "s_cmp_lt_i32 %[bud], 0\n" \
    "s_cbranch_scc0 2b\n" \
    "s_branch 3b\n" \
    "7:\n" \
    "s_bcnt1_i32_b64 s44, vcc\n" \
    "s_cmp_le_u32 s44, s71\n" \
    "s_mov_b32 s71, 0\n" \
    "s_cbranch_scc1 15f\n" \
    "s_cmp_eq_u32 %[tag], 1\n" \
    "s_cbranch_scc1 9f\n" \
    "s_sleep 15\n" \
    "9:\n" \
    "s_sleep 6\n" \
    "15:\n" \
    "s_add_i32 %[bud], %[bud], -1\n" \
    "s_cmp_lt_i32 %[bud], 0\n" \
    "s_cbranch_scc1 8b\n" \
    "buffer_load_dwordx2 v[0:1], %[ca], %[rs], %[cs] offen sc1\n" \
    "s_memtime s[58:59]\n" \
    "s_memrealtime s[44:45]\n" \
    "s_nop 1\n" \
    "s_memrealtime s[46:47]\n" \
    "s_nop 1\n" \
    "s_memrealtime s[48:49]\n" \
    "s_nop 1\n" \
    "s_memrealtime s[50:51]\n" \
    "s_nop 1\n" \
    "s_memrealtime s[52:53]\n" \
    "s_nop 1\n" \
    "s_memrealtime s[54:55]\n" \
    "s_nop 1\n" \
    "s_memrealtime s[56:57]\n" \
    "s_nop 1\n" \
    "s_memrealtime vcc\n" \
    "s_waitcnt lgkmcnt(0)\n" \
    "s_add_i32 s44, s44, s46\n" \
    "s_add_i32 s48, s48, s50\n" \
    "s_add_i32 s52, s52, s54\n" \
    "s_add_i32 s44, s44, s48\n" \
    "s_add_i32 s52, s52, s56\n" \
    "s_add_i32 s44, s44, s52\n" \
    "s_add_i32 s59, s44, vcc_lo\n" \
    "s_sub_i32 s44, s59, %[oT]\n" \
    "s_mov_b32 %[oT], s59\n" \
    "s_sub_i32 s45, s58, %[oC]\n" \
    "s_mov_b32 %[oC], s58\n" \
    "s_mul_i32 s46, s44, %[oR]\n" \
    "s_lshr_b32 s46, s46, 12\n" \
    "s_sub_i32 s46, s45, s46\n" \
    "s_sub_i32 s44, s44, 1000\n" \
    "s_cmp_lt_u32 s44, 480\n" \
    "s_cselect_b32 s46, s46, 0\n" \
    "s_add_i32 %[oR], %[oR], s46\n" \
    "s_sub_i32 s45, %[oT], %[c8]\n" \
    "s_waitcnt vmcnt(0)\n" \
    "s_branch 6b\n" \
    "5:\n" \
    ""
#define OWNER_CLOBBER "v0", "v1", "v2", "v3", "v4", "v5", "v6", "v7", "v8", "v9", "v10", "v11", "v12", "v13", "v14", "v15", "v16", "v17", "v18", "v19", "v20", "s44", "s45", "s46", "s47", "s48", "s49", "s50", "s51", "s52", "s53", "s54", "s55", "s56", "s57", "s58", "s59", "s68", "s69", "s70", "s71", "s96", "s97", "vcc", "scc", "memory"
constexpr int O_W = 0;
constexpr int O_STG = 131072;
constexpr int O_B = O_STG + 20480;
constexpr int OUT_REP = 7;
#define OUT_AUX 16
DI int opaque_lane() { int l; asm volatile("v_mbcnt_lo_u32_b32 %0, -1, 0\n\tv_mbcnt_hi_u32_b32 %0, -1, %0" : "=v"(l)); return l; }
template <int O, class V> DI void put4(V& d, u32x4 f) {
#pragma unroll
    for (int j = 0; j < 4; ++j) d[O + j] = f[j];
}
DI void role_outp(const Args& a, LAS char* lds, unsigned lds_base, int nb, int rep, rsrc_t rw, int& budget) {
    const int tid = threadIdx.x, lane = tid & 63, w = __builtin_amdgcn_readfirstlane(tid >> 6);
    WG_BARRIER();
    const int n = lane & 15, g = lane >> 4;
    const int wl = O_W + w * 32768 + lane * 16;
    u32x32 A[8], V[4];
    {
        const int stg = O_STG + w * 5120;
        const rsrc_t ro = __builtin_amdgcn_make_buffer_rsrc((void*)a.Wo, 0, HD * DM * 2, 0x00020000);
        const int so = (1024 * w + (lane >> 3)) * (DM * 2) + nb * 128 + (lane & 7) * 16, sw = stg + (lane >> 3) * 160 + (lane & 7) * 16;
        const int ra = stg + (16 * (g & 1) + 4 * (g >> 1) + (n >> 2)) * 160 + (n & 3) * 8;
        constexpr int PB = 4;
        u32x4 b[PB][4];
        auto fetch = [&](int ks) {
#pragma unroll
            for (int i = 0; i < 4; ++i) b[ks % PB][i] = __builtin_amdgcn_raw_buffer_load_b128(ro, so, (64 * (ks >> 1) + 16 * (ks & 1) + 8 * (i & 1) + 32 * (i >> 1)) * (DM * 2), 0);
        };
#pragma unroll
        for (int ks = 0; ks < PB; ++ks) fetch(ks);
        FENCE();
        static_for<32>([&](auto ic) {
            constexpr int ks = decltype(ic)::value;
#pragma unroll
            for (int i = 0; i < 4; ++i) *(LAS u32x4*)(lds + sw + i * 1280) = b[ks % PB][i];
            if (ks + PB < 32) fetch(ks + PB);
            u32x4 f[4];
#pragma unroll
            for (int t = 0; t < 4; ++t) { const u32x2 lo = tr_read(lds, ra + t * 32), hi = tr_read(lds, ra + t * 32 + 1280); f[t] = (u32x4){lo.x, lo.y, hi.x, hi.y}; }
            put4<4 * (ks & 7)>(A[ks >> 3], f[0]); put4<4 * (ks & 7)>(A[4 + (ks >> 3)], f[1]); put4<4 * (ks & 7)>(V[ks >> 3], f[2]);
            *(LAS u32x4*)(lds + wl + ks * 1024) = f[3];
            if constexpr (ks == 7) asm volatile("" : "+{a[0:31]}"(A[0]), "+{a[128:159]}"(A[4]), "+{v[128:159]}"(V[0]));
            if constexpr (ks == 15) asm volatile("" : "+{a[32:63]}"(A[1]), "+{a[160:191]}"(A[5]), "+{v[160:191]}"(V[1]));
            if constexpr (ks == 23) asm volatile("" : "+{a[64:95]}"(A[2]), "+{a[192:223]}"(A[6]), "+{v[192:223]}"(V[2]));
            if constexpr (ks == 31) asm volatile("" : "+{a[96:127]}"(A[3]), "+{a[224:255]}"(A[7]), "+{v[224:255]}"(V[3]));
            FENCE();
        });
    }
    wait_prog(rw, F_S4 + lane * 4, true, 1, budget);
    const rsrc_t ru = __builtin_amdgcn_make_buffer_rsrc(a.out, 0, SEQ * DM * 2, 0x00020000);
    constexpr int SP = 2 * NSEG - 2;
    auto first = [&](int u) { return u >= SP ? 8 * u + 8 * SP : 16 * u; };
    auto st_s = [&](int u) { return (u >= 2 * FAST ? F_H + ((first(u) >> 4) - 2 * FAST) * 4096 : F_O + (u & 3) * 4) + w * 1024; };
    auto bn_s = [&](int u) { return OFF_BO + (64 * nb + 16 * w) * BO_ROW + first(u) * 2; };
    auto gn_s = [&](int u) { return OFF_GT + (first(u) + 4 * w) * 4; };
    int st = -1;
    u32x2 bn = ld64(rw, (lane >> 2) * BO_ROW + (lane & 3) * 8, bn_s(rep));
    float gn = __uint_as_float(ld32(rw, g * 4, gn_s(rep)));
    __builtin_amdgcn_s_waitcnt(0);
#define OUT_OPS "=&{v[0:15]}"(acc), "=&{v104}"(top), "=&{v105}"(st), "=&{v[106:107]}"(bn), "=&{v108}"(gn) \
        : "{a[0:31]}"(A[0]), "{a[32:63]}"(A[1]), "{a[64:95]}"(A[2]), "{a[96:127]}"(A[3]), "{a[128:159]}"(A[4]), "{a[160:191]}"(A[5]), "{a[192:223]}"(A[6]), "{a[224:255]}"(A[7]), \
          "{v[128:159]}"(V[0]), "{v[160:191]}"(V[1]), "{v[192:223]}"(V[2]), "{v[224:255]}"(V[3]), [rw] "s"(rw), [so] "s"(t0 * 8192), [wl] "v"(lds_base + O_W + w * 32768 + lane * 16), \
          [tv] "v"(tv), [tm] "s"(tm), [vo] "v"(vo), [sv] "v"(sv), [bv] "v"((lane >> 2) * BO_ROW + (lane & 3) * 8), [gv] "v"(g * 4), \
          [ss] "s"(st_s(u + OUT_REP)), [bs] "s"(bn_s(u + OUT_REP)), [gs] "s"(gn_s(u + OUT_REP))
    for (int u = rep; u < SP + 4; u += OUT_REP) {
        const bool fast = u >= 2 * FAST, sp = u >= SP;
        const int t0 = first(u);
        WG_BARRIER();
        int lane = opaque_lane(), n = lane & 15, g = lane >> 4;
        if (u == 3 * OUT_REP && nb == 0) __builtin_amdgcn_raw_buffer_store_b64(launch_tag(a), rw, w + lane == 0 ? F_EPOCH : OOB, 0, 16);
        S4S(0)
        TT(u >= 123, 0)
        *(LAS u32x2*)(lds + O_B + (16 * w + (lane >> 2)) * 40 + (lane & 3) * 8) = bn;
        const float gt = gn;
        const int tv = OFF_OF + w * 16384 + nb * 2048 + g * 65536 + n * 128, tm = __builtin_amdgcn_readfirstlane(nb < 8 ? sp ? 0xFFFF : -1 : 0);
        const int vo = OFF_OF + w * 16384 + (n & 7) * 16 + (sp ? (n >> 3) * 512 + g * 128 : (n >> 3) * 65536 + (g & 1) * 512 + (g >> 1) * 128);
        const int thr = fast ? 0 : u >> 1, sv = (lane & 7) * 128;
        if (!__all(st >= thr)) do { __builtin_amdgcn_s_sleep(1); st = (int)ld32(rw, sv, st_s(u)); asm volatile("" ::: "memory"); } while (!__all(st >= thr) && --budget >= 0);
        if (fast) { const unsigned t = now(); (void)wait_until(t + wave_max32(min((int)(2 * (st - t)) >> 1, 500))); }
        S4S(1)
        TT(u >= 123, 1)
        f32x16 acc; unsigned top;
        if (sp) {
            do asm volatile(OUT8_ASM : OUT_OPS, [idx] "v"(n & 8 ? 0xEEEE : 0x4444) : OUT_CLOBBER); while (__any(top == SENT) && --budget >= 0);
            TT(u >= 123, 2)
            lane = opaque_lane(); n = lane & 15; g = lane >> 4;
#pragma unroll
            for (int i = 0; i < 8; ++i) *(LAS float*)(lds + O_STG + (w * 16 + 4 * (g & 1) + (i & 3)) * 272 + (g >> 1) * 128 + (i >> 2) * 64 + n * 4) = acc[i];
        } else {
            do asm volatile(OUT16_ASM : OUT_OPS : OUT_CLOBBER); while (__any(top == SENT) && --budget >= 0);
            S4S(2)
            TT(u >= 123, 2)
            lane = opaque_lane(); n = lane & 15; g = lane >> 4;
#pragma unroll
            for (int t = 0; t < 4; ++t) *(LAS f32x4*)(lds + O_STG + (w * 16 + n) * 272 + t * 64 + g * 16) = (f32x4){acc[4 * t], acc[4 * t + 1], acc[4 * t + 2], acc[4 * t + 3]};
        }
        WG_BARRIER();
        S4S(3)
        const int tok = 4 * w + g, c4 = 4 * n;
        f32x4 r[4]; u16 bv[4];
#pragma unroll
        for (int ww = 0; ww < 4; ++ww) { r[ww] = *(LAS f32x4*)(lds + O_STG + (ww * 16 + tok) * 272 + c4 * 4); bv[ww] = *(LAS u16*)(lds + O_B + (c4 + ww) * 40 + tok * 2); }
        FENCE();
        const f32x4 v = gt * ((r[0] + r[1]) + (r[2] + r[3])) + (1.0f - gt) * (f32x4){bf2f(bv[0]), bf2f(bv[1]), bf2f(bv[2]), bf2f(bv[3])};
        __builtin_amdgcn_raw_buffer_store_b64((u32x2){pack2(v[0], v[1]), pack2(v[2], v[3])}, ru, sp && tok >= 8 ? OOB : (tok * DM + 64 * nb + c4) * 2, t0 * (DM * 2), OUT_AUX);
        S4S(4)
        TT(u >= 123, 3)
        TT(u >= 109 && u < 116, 4)
    }
}

constexpr int L_MEMS = 0;
constexpr int L_Q = 9216;
constexpr int L_KO = L_Q + 2560;
constexpr int L_KX = 16384;
constexpr int L_W = 32768;
constexpr int C_RING = 8;
static_assert(S_TOTAL <= L_SINK && L_W + C_RING * 16384 <= L_TOTAL && L_KO + 8 * 576 <= L_KX, "LDS budget");
static_assert(P_STG + P_RING * 16384 <= L_TOTAL, "LDS budget");

DI int pos2col(int p) { return p < 512 ? (p & ~63) + 16 * (p & 3) + ((p & 63) >> 2) : p; }
DI void unit_rc(int u, int& i, int& p0) {
    if (u < 512) { i = 2 * ((u >> 2) & 3) + (u & 1); p0 = (u & ~15) + 4 * (u & 2); }
    else { const int v = u - 512; i = (v >> 1) & 7; p0 = 512 + 16 * (v >> 4) + 8 * (v & 1); }
}
DI void mem0_arrange(LAS char* lds, int raw, int tid) {
#pragma unroll
    for (int k = 0; k < 2; ++k) {
        int i, p0; unit_rc(tid + 256 * k, i, p0);
        unsigned r[4];
#pragma unroll
        for (int j = 0; j < 4; ++j) r[j] = *(LAS unsigned*)(lds + raw + (i * DM + pos2col(p0)) * 2 + 32 * j);
        *(LAS u32x4*)(lds + L_MEMS + (tid + 256 * k) * 16) = (u32x4){__builtin_amdgcn_perm(r[1], r[0], 0x05040100), __builtin_amdgcn_perm(r[3], r[2], 0x05040100),
                                                                     __builtin_amdgcn_perm(r[1], r[0], 0x07060302), __builtin_amdgcn_perm(r[3], r[2], 0x07060302)};
    }
    int i, p0; unit_rc(512 + (tid & 63), i, p0);
    *(LAS u32x4*)(lds + L_MEMS + 8192 + (tid & 63) * 16) = *(LAS u32x4*)(lds + raw + (i * DM + p0) * 2);
}
DI unsigned read_canaries(int s, rsrc_t rs, int lane, int& budget) {
    const int o = 2 * OFF_CAN + (s & 3) * 16384 + (lane >> 2) * 512 + (lane & 3) * 16;
    u32x2 v;
    for (;;) {
        v = ld64(rs, o, 0);
        asm volatile("" ::: "memory");
        if (__all((v.y & 0xffff) == (unsigned)(s + 1))) break;
        if (--budget < 0) break;
    }
    const unsigned t = 8 * now();
    return t + (unsigned)wave_max32(min(max((int)(v.x - t), -0x4000), 0x4000));
}
DI u32x8 sm_frag(LAS char* lds, int sl, int r0, int sa, int sH, int cb, int lo8) {
    auto rd = [&](int r) { return tr_read(lds, sl + r * 256 + ((cb ^ (2 * swz(r))) * 16) + lo8); };
    const u32x2 a = rd(r0), b = rd(r0 + sa), c = rd(r0 + sH), d = rd(r0 + sa + sH);
    return (u32x8){a.x, a.y, b.x, b.y, c.x, c.y, d.x, d.y};
}
#define GS(slot, i)
#define GV(slot, i, v)
DI void owner_loop(const Args& a, int G, rsrc_t rs, unsigned tbase, unsigned cbase, int& budget) {
    const int lane = threadIdx.x & 63, n = lane & 15, g = lane >> 4;
    const bool ex = G >= 64;
    const u32x4 sent4 = {SENT, SENT, SENT, SENT};
    const int js = (n & 2) ? 8 : ex ? (n & 4) + 2 * (n & 1) + (n >> 3) : 2 * (n >> 2) + (n & 1);
    u32x4 selu;
#pragma unroll
    for (int j = 0; j < 4; ++j) selu[j] = (js == 2 * j ? 0x3F80u : 0u) | (js == 2 * j + 1 ? 0x3F800000u : 0u);
    const int o = cmap(G * 4096 + lane * 16);
    const int so = (n < 8 || NREP > 1) ? cmap((n >> 3) * 16384 + G * 128 + (g & 1) * 4 + (ex ? (n & 4) * 16 + (g >> 1) * 32 + (n & 3) * 8 : (n & 6) * 16 + (g >> 1) * 16 + (n & 1) * 8)) : OOB;
    unsigned fails = 0;
    const int c8 = G >= 64 ? a.c1 >> 16 : G < 8 ? a.c1 >> 8 & 255 : a.c1 & 255;
    const bool gv_on = true; (void)gv_on;
    unsigned oT = now(), oC = (unsigned)__builtin_readcyclecounter(), oR;
    { const unsigned r = (unsigned)(512.0f * (float)(oC - cbase) * __builtin_amdgcn_rcpf((float)(oT - tbase))); asm volatile("s_nop 0\n\tv_readfirstlane_b32 %0, %1" : "=s"(oR) : "v"(r)); }
    oT = 8 * oT + 16;
    unsigned lk = oC;
    budget = __builtin_amdgcn_readfirstlane(budget);
    const int ca = 2 * OFF_CAN + (lane >> 2) * 512 + (G & (NCP - 1)) * 128 + (lane & 3) * 16;
    for (int s = 0; s < NSEG - 1; ++s) {
        const int pb = 2 * (OFF_PART + (s & 1) * PART_BYTES);
        int np; unsigned tc;
        asm volatile(OWNER_ASM : [np] "=&s"(np), [tm] "=&s"(tc), [bud] "+s"(budget), [oT] "+s"(oT), [oC] "+s"(oC), [oR] "+s"(oR), [lk] "+s"(lk)
            : [c8] "s"(c8), [ca] "v"(ca), [cs] "s"((s & 3) * 16384), [tag] "s"(s + 1), [o] "v"(o), [so] "v"(so), [sel] "v"(selu), [rs] "s"(rs), [pb] "s"(pb), [mb0] "s"(2 * (OFF_MEMB + ((s + 1) & 3) * MEMB_STRIDE)),
              [cw] "s"(a.cw - c8), [rd] "s"(a.rd), [sv] "v"(sent4), [ro] "v"(s ? o : OOB), [pq] "s"(2 * PART_BYTES - pb), [rm] "v"(s > 2 ? so : OOB), [mq] "s"(2 * (OFF_MEMB + ((s + 2) & 3) * MEMB_STRIDE)) : OWNER_CLOBBER);
        fails += np > 1;
    }
}
DI void role_owner(const Args& a, unsigned lds_base, int ow, rsrc_t rw, int& budget) {
    const int tid = threadIdx.x, lane = tid & 63, w = __builtin_amdgcn_readfirstlane(tid >> 6);
    const unsigned tbase = now(), cbase = (unsigned)__builtin_readcyclecounter();
    if (ow != 63 && w) { prefetch(a, lds_base, ow & 3, (ow >> 2) * 3 + w - 1, (ow & 3) == 3 ? 45 : 48); VM_DRAIN(); }
    if (w >= 2 || (w == 1 && ow >= 8)) return;
    const u32x2 tag = launch_tag(a);
    if (ow == 63) {
        unsigned best[2] = {~0u, ~0u};
        for (int r = 0; r < 10; ++r) {
            unsigned long long t0, t1;
            asm volatile("s_memtime %0\n\ts_waitcnt lgkmcnt(0)" : "=s"(t0) :: "memory");
            st32(SENT, rw, lane == 0 ? (r & 1) * 8192 + (r >> 1) * 4096 * 5 / 4096 * 16384 + ((r >> 1) & 1) * 4096 : OOB, 0);
            asm volatile("s_waitcnt vmcnt(0)\n\ts_memtime %0\n\ts_waitcnt lgkmcnt(0)" : "=s"(t1) :: "memory");
            best[r & 1] = min(best[r & 1], (unsigned)(t1 - t0));
        }
        __builtin_amdgcn_raw_buffer_store_b64((u32x2){(tag.x & ~1u) | (best[1] < best[0]), tag.y}, rw, lane == 0 ? F_INIT + 32 * 128 : OOB, 0, 16);
    }
    const int sel = wait_init(rw, tag, budget, 32);
    const rsrc_t rs = __builtin_amdgcn_make_buffer_rsrc(a.ws + sel * 8192, 0, COMM_BYTES - 8192, 0x00020000);
    owner_loop(a, ow + 64 * w, rs, tbase, cbase, budget);
}

template <int O, class V> DI void put8(V& d, u32x8 f) {
#pragma unroll
    for (int j = 0; j < 8; ++j) d[O + j] = f[j];
}
DI f32x4 SMF64(u32x4 a, u32x8 b, f32x4 c, int i) { asm volatile("s_nop 1\n\tv_smfmac_f32_16x16x64_bf16 %0, %1, %2, %3\n\ts_nop 7" : "+v"(c) : "v"(a), "v"(b), "v"(i)); return c; }
DI f32x4 SMF32(u32x2 a, u32x4 b, f32x4 c, int i) { asm volatile("s_nop 1\n\tv_smfmac_f32_16x16x32_bf16 %0, %1, %2, %3\n\ts_nop 7" : "+v"(c) : "v"(a), "v"(b), "v"(i)); return c; }
DI f32x4 MFV(u32x4 a, u32x4 b, f32x4 c) { asm volatile("s_nop 1\n\tv_mfma_f32_16x16x32_bf16 %0, %1, %2, %0\n\ts_nop 7" : "+v"(c) : "v"(a), "v"(b)); return c; }

DI void role_head(const Args& a, LAS char* lds, unsigned lds_base, int h, int tw, rsrc_t rw, int& budget) {
    const int tid = threadIdx.x, w = __builtin_amdgcn_readfirstlane(tid >> 6);
    char* ws = a.ws;
    const unsigned tbase = now(), cbase = (unsigned)__builtin_readcyclecounter();
    const bool gv_on = tw == 0; (void)gv_on;
    u32x32 A0, A1, A2, A3, A4, A5, A6, A7, R0, R1, R2, R3; u32x8 R4;
    u32x2 tag;
    {
        const int lane = tid & 63, n = lane & 15, g = lane >> 4;
#pragma unroll
        for (int j = 0; j < 3; ++j) glds16(a.mem0, min(4 * j + w, 8) * 1024 + lane * 16, lds_base + L_KX + min(4 * j + w, 8) * 1024);
        const int tq = n >> 2, tc = (n & 3) >> 1, lo8 = (n & 1) * 8;
        const int r_nat = 16 * (g >> 1) + 4 * (g & 1) + (tq & 1) + 8 * (tq >> 1);
        const int r_sig = 16 * (tq & 1) + 4 * (g >> 1) + (g & 1) + 2 * (tq >> 1);
        const int r_o = 16 * (tq & 1) + 8 * (g >> 1) + 4 * (g & 1) + (tq >> 1);
#define WFRAG(sl, kb, t) ((kb) < 8 ? sm_frag(lds, sl, r_sig, 32, 8, 4 * w + 2 * (t) + tc, lo8) : sm_frag(lds, sl, r_nat, 2, 32, 4 * w + 2 * (t) + tc, lo8))
        const u32x4 ro = ring_offs(w, lane, HD);
        auto issue = [&](int i) {
            const unsigned slot = lds_base + L_W + (i % C_RING) * 16384 + w * 4096;
            if (i == 23) ring_issue(a.Wo + (size_t)h * DH * DM + 512, ring_offs(w, lane, [](int r, int c) { return (r + 64 * (c >> 3)) * (DM * 2) + (c & 7) * 16; }), slot);
            else if (i < 28) ring_issue((i < 8 || i == 20 ? a.Wq : i < 16 || i == 21 ? a.Wk : a.Wv) + (size_t)(64 * (i < 16 ? i & 7 : i < 20 ? i - 12 : i < 24 ? 8 : i - 24)) * HD + h * DH, ro, slot);
            else ring_issue(a.Wo + (size_t)(h * DH + 64 * ((i & 1) ^ ((i - 28) >> 1))) * DM + 256 * tw + 128 * (i & 1), ring_offs(w, lane, DM), slot);
        };
        auto keep = [&](int at, u32x8 f) {
            *(LAS u32x4*)(lds + at + lane * 16) = (u32x4){f[0], f[1], f[2], f[3]};
            *(LAS u32x4*)(lds + at + 1024 + lane * 16) = (u32x4){f[4], f[5], f[6], f[7]};
        };
        u32x8 hold[8] = {};
#pragma unroll
        for (int i = 0; i < C_RING; ++i) issue(i);
        tag = launch_tag(a);
        ring_wait<4 * C_RING>();
        mem0_arrange(lds, L_KX, tid);
        static_for<9>([&](auto ic) {
            constexpr int G = decltype(ic)::value;
            ring_wait<(G < 7 ? 16 : G == 7 ? 8 : 0)>();
            const int sl = L_W + (G & 1) * 65536;
            auto fr = [&](int j, int t) { return WFRAG(sl + ((j ^ w) << 14), 0, t); };
            if constexpr (G < 4) {
                u32x32& A = G == 0 ? A0 : G == 1 ? A2 : G == 2 ? A4 : A6;
                u32x32& B = G == 0 ? A1 : G == 1 ? A3 : G == 2 ? A5 : A7;
                put8<0>(A, fr(0, 0)); put8<8>(A, fr(0, 1)); put8<16>(A, fr(1, 0)); put8<24>(A, fr(1, 1));
                put8<0>(B, fr(2, 0)); put8<8>(B, fr(2, 1)); put8<16>(B, fr(3, 0)); put8<24>(B, fr(3, 1));
                if constexpr (G == 0) asm volatile("" : "+{a[0:31]}"(A0), "+{a[32:63]}"(A1));
                if constexpr (G == 1) asm volatile("" : "+{a[64:95]}"(A2), "+{a[96:127]}"(A3));
                if constexpr (G == 2) asm volatile("" : "+{a[128:159]}"(A4), "+{a[160:191]}"(A5));
                if constexpr (G == 3) asm volatile("" : "+{a[192:223]}"(A6), "+{a[224:255]}"(A7));
            } else if constexpr (G == 4) {
                put8<16>(R1, fr(0, 0)); put8<24>(R1, fr(0, 1)); put8<0>(R2, fr(1, 0)); put8<8>(R2, fr(1, 1));
                put8<16>(R2, fr(2, 0)); put8<24>(R2, fr(2, 1)); put8<0>(R3, fr(3, 0)); put8<8>(R3, fr(3, 1));
            } else if constexpr (G == 5) {
                put8<16>(R0, WFRAG(sl, 8, 0)); put8<24>(R0, WFRAG(sl, 8, 1)); put8<0>(R1, WFRAG(sl + 16384, 8, 0)); put8<8>(R1, WFRAG(sl + 16384, 8, 1));
                put8<16>(R3, WFRAG(sl + 32768, 8, 0)); put8<24>(R3, WFRAG(sl + 32768, 8, 1));
                hold[7] = sm_frag(lds, sl + 49152, r_o, 2, 32, 8 * (w >> 1) + 4 * tw + 2 * (w & 1) + tc, lo8);
            } else if constexpr (G == 6) {
#pragma unroll
                for (int j = 0; j < 7; ++j) hold[j] = fr(j >> 1, j & 1);
                R4 = fr(3, 1);
            } else {
                if constexpr (G == 8)
#pragma unroll
                    for (int T = 0; T < 4; ++T) keep(L_W + 65536 + w * 8192 + T * 2048, hold[T]);
#pragma unroll
                for (int T = 0; T < 4; ++T) hold[T] = sm_frag(lds, L_W + (2 * G - 10 + (w >> 1)) * 16384, r_o, 2, 32, 8 * (w & 1) + 2 * T + tc, lo8);
            }
            if constexpr (G != 7) WG_BARRIER();
            if constexpr (G < 6)
#pragma unroll
                for (int i = 0; i < 4; ++i) issue(4 * G + 8 + i);
            if constexpr (G == 6)
#pragma unroll
                for (int j = 0; j < 8; ++j) keep(L_W + (j >> 1) * 16384 + w * 4096 + (j & 1) * 2048, hold[j]);
            if constexpr (G == 8)
#pragma unroll
                for (int T = 0; T < 4; ++T) keep(L_W + 98304 + w * 8192 + T * 2048, hold[T]);
        });
        const int ai = 2 * (n >> 2) + (n & 1), ahf = (n >> 1) & 1;
        R0[0] = R0[1] = R0[2] = R0[3] = 0x3F803F80u;
        R0[4] = ahf ? 0xEEEE : 0x4444;
        R0[5] = (g >> 1) * 512 + (n >> 2) * 128 + (g & 1) * 64 + (n & 3) * 16;
        R0[6] = lds_base + L_MEMS + 8192 + g * 256 + ai * 32 + ahf * 16;
        R0[7] = lane * 16;
        R0[8] = lds_base + L_Q + 2 * g * 320 + (16 * w + n) * 4;
        R0[9] = lds_base + L_KO + 2 * g * 576 + (16 * w + (n & 8) + 4 * (n & 1) + ((n >> 1) & 3)) * 4;
        R0[10] = lds_base + L_Q + ai * 320 + g * 16;
        R0[11] = lds_base + L_KO + ai * 576 + g * 32 + ahf * 16;
        R0[12] = lds_base + L_KX + 8192 + w * 2048 + (g >> 1) * 256 + n * 16 + (g & 1) * 8;
        R0[13] = lds_base + L_W + w * 4096 + lane * 16;
        R0[14] = cmap(((2 * (4 * tw + w) + (n >> 3)) * 4 + g) * 4096 + h * 128 + (n & 7) * 16);
        R0[15] = (g >> 1) == (w >> 1) ? cmap((64 + 2 * (2 * tw + (w & 1)) + (g >> 1)) * 4096 + h * 128 + (g & 1) * 64 + n * 4) : OOB;
        asm volatile("" : "+{v[120:151]}"(R0), "+{v[152:183]}"(R1), "+{v[184:215]}"(R2), "+{v[216:247]}"(R3), "+{v[248:255]}"(R4));
    }
    const int sel = wait_init(rw, tag, budget, 33 + 4 * h + w);
    int kv_rdy = 0;
    const rsrc_t rs = __builtin_amdgcn_make_buffer_rsrc(ws + sel * 8192, 0, COMM_BYTES - 8192, 0x00020000);
    auto dma = [&](int voff, int soff, unsigned dst) { bdma(rw, voff, soff, dst); };
    auto fetch_kv = [&](int s, int lane) {
        const int n = lane & 15, g = lane >> 4;
        const int kxs = (g >> 1) * 1024 + ((g & 1) * 32 + 16 * (n >> 3) + (n & 7)) * 16;
        const int vxs = (g >> 1) * 1024 + ((g & 1) * 32 + n) * 16;
        const int so_ = (s * 32 + h) * 8192 + w * 2048;
        const unsigned xdst = lds_base + L_KX + w * 2048;
        dma(kxs, OFF_KF + so_, xdst); dma(kxs, OFF_KF + so_ + 128, xdst + 1024);
        dma(vxs, OFF_VF + so_, xdst + 8192); dma(vxs, OFF_VF + so_ + 256, xdst + 8192 + 1024);
    };
    const int fo = F_KVM + h * 256 + tw * 128;
    u32x8 scon;
    scon[0] = w; scon[1] = 2 * OFF_CAN + (h >> 1) * 512 + (2 * (h & 1) + tw) * 16; scon[2] = F_KV + h * 512 + w * 128; scon[3] = 65536 + w * 4096;
    scon[4] = OFF_KF + h * 8192 + w * 2048; scon[5] = fo; scon[6] = tw - 1; scon[7] = (tw ? OFF_VM : OFF_KM) + (h - NH) * 2048 + w * 512;
    u32x8 S1 = {0u, 0u, (unsigned)kv_rdy, (unsigned)budget, (unsigned)a.d0, 0xFFFFFFFFu / 22, (unsigned)(a.c2 + a.b0), 0u};
    u32x8 S2 = {0u, 0u, 0u, (unsigned)a.up, (unsigned)a.dn, (unsigned)a.c2, 0u, 1u};
    fetch_kv(0, opaque_lane());
    __builtin_amdgcn_raw_buffer_store_b64((u32x2){8 * now() + a.e0, 1u | (a.c2 + a.b0) << 16}, rs, tid < NCP ? tid * 128 : OOB, scon[1], 16);
    asm volatile("s_waitcnt vmcnt(1)" ::: "memory");
    u32x2 vm = {};
    u32x8 S3;
    {
        const unsigned t = now(), c = (unsigned)__builtin_readcyclecounter();
        const float r = (float)(c - cbase) * __builtin_amdgcn_rcpf(8.0f * (float)(t - tbase));
        S2[1] = 8 * t + 16; S1[5] = (unsigned)(4294967296.0f * __builtin_amdgcn_rcpf(r));
        S3 = (u32x8){c, (unsigned)(4096.0f * r), 2u * OFF_MEMB + ((h >> 3) & (NREP - 1)) * 32768, w == 3 ? ~0u : 0u};
    }
    for (;;) {
        asm volatile(STEP_ASM
            : "+{v[96:97]}"(vm), "+{v[0:7]}"(S1), "+{v[8:15]}"(S2), "+{v[16:23]}"(S3), "+{v[24:31]}"(scon)
            : "{a[0:31]}"(A0), "{a[32:63]}"(A1), "{a[64:95]}"(A2), "{a[96:127]}"(A3), "{a[128:159]}"(A4), "{a[160:191]}"(A5), "{a[192:223]}"(A6), "{a[224:255]}"(A7),
              "{v[120:151]}"(R0), "{v[152:183]}"(R1), "{v[184:215]}"(R2), "{v[216:247]}"(R3), "{v[248:255]}"(R4),
              [rs] "s"(rs), [rw] "s"(rw)
            : STEP_CLOBBER);
        const int s = __builtin_amdgcn_readfirstlane(S1[0]);
        const unsigned st = __builtin_amdgcn_readfirstlane(S1[7]);
        kv_rdy = __builtin_amdgcn_readfirstlane(S1[2]); budget = __builtin_amdgcn_readfirstlane(S1[3]);
        if (s == NSEG - 1) break;
        if (__builtin_expect(st & 2, 0)) {
            const int lane = opaque_lane();
            LAS char* z = (LAS char*)0;
            const f32x4 zero = {0, 0, 0, 0};
            const u32x4 ones = __builtin_shufflevector(R0, R0, 0, 1, 2, 3);
            const unsigned zr = lane >> 6;
            const int idx = R0[4];
            u32x4 qf[4];
            f32x4 s1 = zero, s2 = zero;
#pragma unroll
            for (int ks = 0; ks < 4; ++ks) {
                qf[ks] = *(LAS u32x4*)(z + R0[10] + ks * 64);
                s1 = MFV(*(LAS u32x4*)(lds + L_KX + lane * 16 + ks * 2048), qf[ks], s1);
                s2 = MFV(*(LAS u32x4*)(lds + L_KX + lane * 16 + ks * 2048 + 1024), qf[ks], s2);
            }
            f32x4 s0 = SMF64(*(LAS u32x4*)(z + R0[11]), __builtin_shufflevector(qf[0], qf[1], 0, 1, 2, 3, 4, 5, 6, 7), zero, idx);
            s0 = SMF64(*(LAS u32x4*)(z + R0[11] + 128), __builtin_shufflevector(qf[2], qf[3], 0, 1, 2, 3, 4, 5, 6, 7), s0, idx);
            const float sa = s0[0] + s0[2], sb = s0[1] + s0[3];
            float mx = fmaxf(fmaxf(fmaxf(fmaxf(s1[0], s1[1]), fmaxf(s1[2], s1[3])), fmaxf(fmaxf(s2[0], s2[1]), fmaxf(s2[2], s2[3]))), fmaxf(sa, sb));
            mx = fmaxf(mx, __shfl_xor(mx, 16)); mx = fmaxf(mx, __shfl_xor(mx, 32));
            auto ex2 = [&](float x, float y) { return pack2(__builtin_amdgcn_exp2f(x - mx), __builtin_amdgcn_exp2f(y - mx)); };
            const u32x4 px = {ex2(s1[0], s1[1]), ex2(s1[2], s1[3]), ex2(s2[0], s2[1]), ex2(s2[2], s2[3])};
            const unsigned pm = ex2(sa, sb);
            const f32x4 l = MFV(((u32x4){pm, zr, zr, zr}), ones, MFV(px, ones, zero));
            const u32x2 x0 = *(LAS u32x2*)(z + R0[12]), y0 = *(LAS u32x2*)(z + R0[12] + 1024), x1 = *(LAS u32x2*)(z + R0[12] + 512), y1 = *(LAS u32x2*)(z + R0[12] + 1536);
            const f32x4 o = SMF64(px, ((u32x8){x0.x, y0.x, x0.y, y0.y, x1.x, y1.x, x1.y, y1.y}), zero, idx) + SMF32(((u32x2){pm, zr}), ((u32x4){vm.x, vm.y, zr, zr}), zero, idx);
            const float i0 = __builtin_amdgcn_rcpf(l[0]), i1 = __builtin_amdgcn_rcpf(l[1]);
            *(LAS unsigned*)(z + R0[9] + 256) = pack2(o[0] * i0, o[2] * i0);
            *(LAS unsigned*)(z + R0[9] + 256 + 576) = pack2(o[1] * i1, o[3] * i1);
            WG_BARRIER();
            const u32x4 oa = *(LAS u32x4*)(z + R0[11] + 256 + (w >> 1) * 128), ob = *(LAS u32x4*)(z + R0[11] + 384 - (w >> 1) * 128);
            auto frag = [&](int at) { return __builtin_shufflevector(*(LAS u32x4*)(z + at), *(LAS u32x4*)(z + at + 1024), 0, 1, 2, 3, 4, 5, 6, 7); };
            f32x2 f[4];
#pragma unroll
            for (int T = 0; T < 4; ++T) f[T] = fold(SMF64(ob, frag(R0[13] + scon[3] + 32768 + T * 2048), SMF64(oa, frag(R0[13] + scon[3] + T * 2048), zero, idx), idx));
            const f32x2 fx = fold(SMF64(ob, frag((R0[13] ^ 8192) + 49152 + 2048), SMF64(oa, frag(R0[13] + 49152 + 2048), zero, idx), idx));
            const int pb = 2 * (OFF_PART + (s & 1) * PART_BYTES);
            st128((u32x4){pack2(f[0].x, f[1].x), pack2(f[2].x, f[3].x), pack2(f[0].y, f[1].y), pack2(f[2].y, f[3].y)}, rs, R0[14], pb);
            st32(pack2(fx.x, fx.y), rs, R0[15], pb);
        }
        if (__builtin_expect((st & 2) || kv_rdy <= s, 0)) {
            while (kv_rdy <= s) { kv_rdy = prog_min(rw, F_KV + h * 512 + w * 128); if (--budget < 0) break; }
            fetch_kv(s + 1, opaque_lane());
        }
        if (st & 2) { S1[1] = read_canaries(s, rs, opaque_lane(), budget) + S1[6]; S2[2] += 0x10000; }
        if ((int)(S1[1] - S2[1]) > 0x8000) { S2[1] = 8 * now() + 16; S3[0] = (unsigned)__builtin_readcyclecounter(); }
        S1[0] = s + 1; S1[2] = kv_rdy; S1[3] = budget;
    }
    TT(true, 0)
    st32((unsigned)(NSEG - 2), rw, opaque_lane() == 0 && w == 0 ? fo : OOB, 0);
}

__global__ void __launch_bounds__(256, 1) fused_kernel(Args a) {
    extern __shared__ __attribute__((aligned(16))) char smem[];
    LAS char* lds = (LAS char*)smem;
    const unsigned lds_base = (unsigned)(uintptr_t)lds;
    const int w = __builtin_amdgcn_readfirstlane(threadIdx.x >> 6);
    const int b = blockIdx.x, bx = b & 7, bi = b >> 3;
    if (bi >= 28) {
        const void* pc; unsigned len;
        asm volatile("s_getpc_b64 %0\n1:\ts_mov_b32 %1, .Lstep_end-1b" : "=s"(pc), "=s"(len));
        touch(pc, min((bi - 28) * 32768 + threadIdx.x * 128, len), lds_base);
    }
    asm volatile("" :: "s"(a.Wq), "s"(a.cw));
    const rsrc_t rw = __builtin_amdgcn_make_buffer_rsrc(a.ws, 0, WS_USED, 0x00020000);
    int budget = SPIN_MAX;
#define RT(i)
    if (bx < 4) {
        if (bi < 16) role_head(a, lds, lds_base, 8 * bx + (bi >> 1), bi & 1, rw, budget);
        else role_owner(a, lds_base, 4 * (bi - 16) + bx, rw, budget);
    } else {
        const int fx = bx - 4, no = fx < 3 ? 14 : 22, j = 14 * fx + bi;
        if (bi >= no || (j & 2)) {
            const unsigned t = now() + a.d1;
            if (bi >= no) { if (w) prefetch(a, lds_base, bx, (bi - no) * 3 + w - 1, 3 * (32 - no)); }
            else {
                const int sh = (j >> 2) * 2 + (j & 1);
#pragma unroll
                for (int i = 0; i < INIT_SHARE / 4096; ++i) st128((u32x4){SENT, SENT, SENT, SENT}, rw, threadIdx.x * 16 + i * 4096, sh * INIT_SHARE);
#pragma unroll
                for (int i = 0; i < 4; ++i) st128((u32x4){SENT, SENT, SENT, SENT}, rw, threadIdx.x * 16 + (i & 1) * 4096, (i & 2 ? OFF_VM : OFF_KM) + FAST * 65536 + sh * 8192);
                const u32x2 tag = launch_tag(a);
                VM_DRAIN();
                WG_BARRIER();
                __builtin_amdgcn_raw_buffer_store_b64(tag, rw, threadIdx.x == 0 ? F_INIT + sh * 128 : OOB, 0, 16);
                RT(3)
            }
            (void)wait_until(t);
        }
        if (bi >= no) {
            const int t = 18 * fx + bi - no;
            role_s4d(a, lds, lds_base, t, rw, budget);
            RT(1)
            TT(true, 6)
            if (t < 9 * OUT_REP) role_outp(a, lds, lds_base, t % 9, t / 9, rw, budget);
        }
        else {
            if (!(j & 2)) role_kv(a, lds, lds_base, (j >> 2) * 2 + (j & 1), rw, budget);
            RT(1)
            TT(true, 6)
            role_qa(a, lds, lds_base, (j >> 2) * 2, (j & 2 ? 0 : FAST) + (j & 1), (j & 2 ? FAST : NSEG) - 2 + (j & 1), rw, budget);
        }
    }
    RT(2)
    TT(true, 5)
    if (budget < 0 && threadIdx.x == 0) st32(0x1000u + b, rw, F_ERR, 0);
}

static int g_c1 = 56, g_c1m = 52, g_c1x = 70, g_c2 = 372, g_b0 = 64, g_up = 16, g_fl = 8, g_dn = 3, g_d0 = 1680, g_da = 100, g_ua = 48, g_d1 = 300, g_rd = 34, g_cw = 69, g_e0 = 165, g_x1 = 30, g_x2 = 106, g_x3 = 120, g_x4 = 104, g_x5 = 152, g_x6 = 145, g_x7 = 50;
extern "C" void kernel_launch(void* const* d_in, const int* in_sizes, int n_in, void* d_out, int out_size, void* d_ws, size_t ws_size, hipStream_t stream) {
    Args a;
    a.x = (const u16*)d_in[0]; a.mem0 = (const u16*)d_in[1];
    a.Wq = (const u16*)d_in[2]; a.Wk = (const u16*)d_in[3]; a.Wv = (const u16*)d_in[4]; a.Wo = (const u16*)d_in[5];
    a.lAr = (const u16*)d_in[8]; a.Aim = (const u16*)d_in[9]; a.Cr = (const u16*)d_in[10]; a.Ci = (const u16*)d_in[11];
    a.ldt = (const u16*)d_in[12]; a.Dsk = (const u16*)d_in[13]; a.gw = (const u16*)d_in[14];
    a.out = (u16*)d_out;
    a.ws = (char*)(((uintptr_t)d_ws + ((2u << 20) - 1)) & ~(uintptr_t)((2u << 20) - 1));
    a.c1 = g_c1 | g_c1m << 8 | g_c1x << 16; a.c2 = g_c2; a.d0 = g_d0 + g_da; a.b0 = g_b0; a.d1 = g_d1; a.up = g_up | g_ua << 8 | g_fl << 16; a.dn = g_dn | g_d0 << 8; a.rd = 23 * g_rd - 300; a.cw = 8 * g_cw - 104; a.e0 = 8 * g_e0;
    a.xa = g_x1 | g_x2 << 8 | g_x5 << 16 | g_x7 << 24; a.xb = g_x6 * 0x101 | g_x3 << 16 | g_x4 << 24;
    (void)hipFuncSetAttribute((const void*)fused_kernel, hipFuncAttributeMaxDynamicSharedMemorySize, L_TOTAL);
    fused_kernel<<<256, 256, L_TOTAL, stream>>>(a);
}
```

```cpp
#include <hip/hip_runtime.h>
#include <hip/hip_bf16.h>
#include <stdint.h>

typedef unsigned short u16;
typedef __bf16 bf16x8 __attribute__((ext_vector_type(8)));
typedef __bf16 bf16x16 __attribute__((ext_vector_type(16)));
typedef _Float16 f16x2 __attribute__((ext_vector_type(2)));
typedef _Float16 f16x8 __attribute__((ext_vector_type(8)));
typedef short s16x4 __attribute__((ext_vector_type(4)));
typedef float f32x2 __attribute__((ext_vector_type(2)));
typedef float f32x4 __attribute__((ext_vector_type(4)));
typedef float f32x16 __attribute__((ext_vector_type(16)));
typedef unsigned u32x2 __attribute__((ext_vector_type(2)));
typedef unsigned u32x4 __attribute__((ext_vector_type(4)));
typedef unsigned u32x8 __attribute__((ext_vector_type(8)));
typedef unsigned u32x16 __attribute__((ext_vector_type(16)));
typedef unsigned u32x32 __attribute__((ext_vector_type(32)));
typedef short s16x8 __attribute__((ext_vector_type(8)));
#define LAS __attribute__((address_space(3)))
#define DI __device__ __forceinline__
typedef __amdgpu_buffer_rsrc_t rsrc_t;

constexpr int SEQ = 2048, DM = 576, NH = 32, DH = 128, HD = NH * DH, NM = 8, SEG = 32, NSEG = 64, NS = 128;
constexpr unsigned SENT = 0xFFFFFFFFu;
constexpr unsigned OOB = 0x7FFFFFF0u;
constexpr int SPIN_MAX = 1 << 20;

constexpr int OFF_PART = 0;
constexpr int PART_BYTES = 72 * 4096;
constexpr int OFF_MEMB = OFF_PART + 2 * PART_BYTES;
constexpr int MEMB_STRIDE = 4 * 16384;
constexpr int OFF_CAN = OFF_MEMB + 4 * MEMB_STRIDE;
constexpr int COMM_LOGICAL = OFF_CAN + 32768;
constexpr int COMM_BYTES = 2 * COMM_LOGICAL;
constexpr int F_ERR = COMM_BYTES;
constexpr int F_KV = F_ERR + 128;
constexpr int F_KVM = F_KV + 16384;
constexpr int F_O = F_KVM + 8192;
constexpr int F_H = F_O + 4096;
constexpr int F_S4 = F_H + 32768;
constexpr int F_DONE = F_S4 + 512;
constexpr int INIT_SHARE = 14 * 4096, INIT_BYTES = 32 * INIT_SHARE;
constexpr int F_INIT = INIT_BYTES;
constexpr int F_EPOCH = F_INIT + 24576;
static_assert(F_DONE + 128 <= INIT_BYTES, "");
static_assert(F_EPOCH + 128 <= (2 << 20), "hand-off region");
constexpr int OFF_KF = 2 << 20;
constexpr int OFF_VF = OFF_KF + SEQ * HD * 2;
constexpr int OFF_OF = OFF_VF + SEQ * HD * 2;
constexpr int OFF_KM = OFF_OF + SEQ * HD * 2;
constexpr int OFF_VM = OFF_KM + NSEG * NH * NM * DH * 2;
constexpr int FAST = NSEG - 4;
constexpr int OFF_BO = OFF_VM + NSEG * NH * NM * DH * 2;
constexpr int BO_ROW = 4352;
constexpr int OFF_GT = OFF_BO + DM * BO_ROW;
constexpr int OFF_DBG = OFF_GT + SEQ * 4;
constexpr int WS_USED = OFF_DBG + (4 << 20);

struct Args {
    const u16 *x, *mem0, *Wq, *Wk, *Wv, *Wo, *lAr, *Aim, *Cr, *Ci, *ldt, *Dsk, *gw;
    u16* out; char* ws;
    int c1, c2, d0, b0, d1, up, dn, rd, cw, e0;
    unsigned xa, xb;
};

DI float bf2f(u16 b) { return __uint_as_float((unsigned)b << 16); }
DI float bflo(unsigned w) { return __uint_as_float(w << 16); }
DI float bfhi(unsigned w) { return __uint_as_float(w & 0xFFFF0000u); }
DI u16 f2bf(float f) { return __builtin_bit_cast(u16, (__bf16)f); }
DI unsigned pack2(float lo, float hi) {
    typedef __bf16 bf16x2 __attribute__((ext_vector_type(2)));
    bf16x2 v = {(__bf16)lo, (__bf16)hi};
    return __builtin_bit_cast(unsigned, v);
}
#define WG_BARRIER() asm volatile("s_waitcnt lgkmcnt(0)\n\ts_barrier" ::: "memory")
#define VM_DRAIN() asm volatile("s_waitcnt vmcnt(0)" ::: "memory")
DI int cmap(int o) { return (o & 0x1FFF) | ((o >> 13) << 14); }
DI unsigned mx4(unsigned a, u32x4 v) { a = max(max(a, v.x), v.y); return max(max(a, v.z), v.w); }
DI unsigned now() { return (unsigned)__builtin_amdgcn_s_memrealtime(); }
DI unsigned wait_until(unsigned t) {
    unsigned v; int guard = 8192;
    do { v = now(); } while ((int)(t - v) >= 4 && --guard > 0);
    const int r = __builtin_amdgcn_readfirstlane((int)(t - v));
    asm volatile("s_cmp_lt_i32 %0, 1\n\ts_cbranch_scc1 1f\n\ts_nop 3\n\ts_cmp_lt_i32 %0, 2\n\ts_cbranch_scc1 1f\n\ts_nop 3\n\ts_cmp_lt_i32 %0, 3\n\ts_cbranch_scc1 1f\n\ts_nop 7\n1:" :: "s"(r) : "scc");
    return r > 0 ? t : v + 2;
}
DI void st128(u32x4 v, rsrc_t rs, int off, int soff) {
    __builtin_amdgcn_raw_buffer_store_b128(v, rs, off, soff, 16);
    asm volatile("s_nop 1" :: "v"(v) : "memory");
}
DI void st32(unsigned v, rsrc_t rs, int off, int soff) { __builtin_amdgcn_raw_buffer_store_b32(v, rs, off, soff, 16); }
DI u32x4 ld128(rsrc_t rs, int off, int soff) { return __builtin_amdgcn_raw_buffer_load_b128(rs, off, soff, 16); }
DI u32x2 ld64(rsrc_t rs, int off, int soff) { return __builtin_amdgcn_raw_buffer_load_b64(rs, off, soff, 16); }
DI unsigned ld32(rsrc_t rs, int off, int soff) { return __builtin_amdgcn_raw_buffer_load_b32(rs, off, soff, 16); }
DI f32x2 fold(f32x4 a) { return (f32x2){a[0], a[1]} + (f32x2){a[2], a[3]}; }
DI float dpp_xor1(float v) { return __int_as_float(__builtin_amdgcn_update_dpp(0, __float_as_int(v), 0xB1, 0xf, 0xf, true)); }
DI float red_sum_g(float v) {
    auto a = __builtin_amdgcn_permlane16_swap(__float_as_uint(v), __float_as_uint(v), false, false);
    v = __uint_as_float(a[0]) + __uint_as_float(a[1]);
    auto b = __builtin_amdgcn_permlane32_swap(__float_as_uint(v), __float_as_uint(v), false, false);
    return __uint_as_float(b[0]) + __uint_as_float(b[1]);
}
DI int wave_max32(int d) {
    d = max(d, __builtin_amdgcn_update_dpp(d, d, 0x121, 0xf, 0xf, false));
    d = max(d, __builtin_amdgcn_update_dpp(d, d, 0x122, 0xf, 0xf, false));
    d = max(d, __builtin_amdgcn_update_dpp(d, d, 0x124, 0xf, 0xf, false));
    d = max(d, __builtin_amdgcn_update_dpp(d, d, 0x128, 0xf, 0xf, false));
    return __builtin_amdgcn_readfirstlane(max(max(__builtin_amdgcn_readlane(d, 0), __builtin_amdgcn_readlane(d, 16)), max(__builtin_amdgcn_readlane(d, 32), __builtin_amdgcn_readlane(d, 48))));
}
DI u32x2 tr_read(LAS char* lds, int addr) { return __builtin_bit_cast(u32x2, __builtin_amdgcn_ds_read_tr16_b64_v4i16((LAS s16x4*)(lds + addr))); }
DI void wait_prog(rsrc_t rw, int off, bool act, int thr, int& budget) {
    for (;;) {
        const int v = (int)ld32(rw, off, 0);
        asm volatile("" ::: "memory");
        if (__all(!act || v >= thr)) break;
        if (--budget < 0) break;
        __builtin_amdgcn_s_sleep(1);
    }
}
DI u32x2 launch_tag(const Args& a) {
    unsigned long long z;
    asm volatile("s_load_dwordx2 %0, %1, 0x0\n\ts_waitcnt lgkmcnt(0)" : "=s"(z) : "s"(a.ws + F_EPOCH) : "memory");
    z += 0x9E3779B97F4A7C15ull;
    z = (z ^ (z >> 30)) * 0xBF58476D1CE4E5B9ull; z = (z ^ (z >> 27)) * 0x94D049BB133111EBull; z ^= z >> 31;
    return (u32x2){(unsigned)z, (unsigned)(z >> 32)};
}
DI int wait_init(rsrc_t rw, u32x2 tag, int& budget, int more = 0) {
    const int l = threadIdx.x & 63, o = F_INIT + (l < 32 ? l : l == 63 ? min(more, 32) : more) * 128;
    u32x2 v;
    for (;;) {
        v = ld64(rw, o, 0);
        asm volatile("" ::: "memory");
        if (__all(((v.x ^ tag.x) >> 1) == 0 && v.y == tag.y)) break;
        if (--budget < 0) break;
        __builtin_amdgcn_s_sleep(2);
    }
    return __builtin_amdgcn_readlane(v.x, 63) & 1;
}
DI void glds16(const void* base, int voff, unsigned lds_dst) {
    unsigned keep;
    asm volatile("s_mov_b32 %0, m0\n\ts_mov_b32 m0, %3\n\ts_nop 0\n\tglobal_load_lds_dwordx4 %1, %2\n\ts_mov_b32 m0, %0" : "=&s"(keep) : "v"(voff), "s"(base), "s"(lds_dst) : "memory");
}
DI void bdma(rsrc_t rs, int voff, int soff, unsigned lds_dst) {
    unsigned keep;
    asm volatile("s_mov_b32 %0, m0\n\ts_mov_b32 m0, %4\n\ts_nop 0\n\tbuffer_load_dwordx4 %1, %2, %3 offen sc1 lds\n\ts_mov_b32 m0, %0" : "=&s"(keep) : "v"(voff), "s"(rs), "s"(soff), "s"(lds_dst) : "memory");
}
DI int swz(int r) { return ((r & 3) ^ (((r >> 2) & 3) << 1) ^ (((r >> 4) & 1) << 2)) & 7; }
template <class F> DI u32x4 ring_offs(int w, int lane, F at) {
    u32x4 o;
#pragma unroll
    for (int j = 0; j < 4; ++j) { const int r = 16 * w + 4 * j + (lane >> 4); o[j] = at(r, (lane & 15) ^ (2 * swz(r))) - 1024 * j; }
    return o;
}
DI u32x4 ring_offs(int w, int lane, int ld) { return ring_offs(w, lane, [&](int r, int c) { return r * ld * 2 + c * 16; }); }
DI void ring_issue(const u16* src, u32x4 o, unsigned slot) {
    unsigned keep;
    asm volatile("s_mov_b32 %0, m0\n\ts_mov_b32 m0, %6\n\ts_nop 0\n\tglobal_load_lds_dwordx4 %1, %5\n\tglobal_load_lds_dwordx4 %2, %5 offset:1024\n\t"
                 "global_load_lds_dwordx4 %3, %5 offset:2048\n\tglobal_load_lds_dwordx4 %4, %5 offset:3072\n\ts_mov_b32 m0, %0"
                 : "=&s"(keep) : "v"(o.x), "v"(o.y), "v"(o.z), "v"(o.w), "s"(src), "s"(slot) : "memory");
}
#define PF_FIRST 2
constexpr int L_TOTAL = 163840, L_SINK = L_TOTAL - 256;
DI void touch(const void* base, int voff, unsigned lds_base) {
    unsigned keep;
    asm volatile("s_mov_b32 %0, m0\n\ts_mov_b32 m0, %3\n\ts_nop 0\n\tglobal_load_lds_dword %1, %2\n\ts_mov_b32 m0, %0" : "=&s"(keep) : "v"(voff), "s"(base), "s"(lds_base + L_SINK) : "memory");
}
DI void prefetch(const Args& a, unsigned lds_base, int x, int id, int n) {
    const int lane = threadIdx.x & 63;
    const int h0 = x < 4 ? 8 * x : (0x160E0800 >> (8 * (x - 4))) & 255, lpr = x == 5 ? 12 : x == 7 ? 20 : 16, per = 9 * lpr;
    const int first = PF_FIRST * lpr, end = (x < 4 ? 4 : 2) * per;
    const float rl = __builtin_amdgcn_rcpf((float)lpr);
    for (int i = first + id; i < end; i += n) {
        int m = (i >= per) + (i >= 2 * per) + (i >= 3 * per), j = i - m * per;
        if (x < 4 && m < 3) { const int b = i >> 4; m = b < 20 ? b >> 3 : min(b - 20, 2); j = (b < 16 ? b & 7 : b < 20 ? b - 12 : b < 23 ? 8 : b - 23) * 16 + (i & 15); }
        const int mm = m + (x >= 4);
        const int L = j * 64 + lane, row = (int)((L + 0.5f) * rl);
        touch(mm == 0 ? a.Wq : mm == 1 ? a.Wk : mm == 2 ? a.Wv : a.Wo, m == 3 ? h0 * (DH * DM * 2) + L * 128 : row * (HD * 2) + (L - row * lpr) * 128 + h0 * 256, lds_base);
    }
}
template <int N> DI void ring_wait() { asm volatile("s_waitcnt vmcnt(%0) lgkmcnt(0)\n\ts_barrier" :: "i"(N) : "memory"); }
template <int I> struct ic_t { static constexpr int value = I; };
template <int N, int I = 0, class F> DI void static_for(F&& f) { if constexpr (I < N) { f(ic_t<I>{}); static_for<N, I + 1>(f); } }
#define MFMA32(a, b, c) __builtin_amdgcn_mfma_f32_32x32x16_bf16(__builtin_bit_cast(bf16x8, a), __builtin_bit_cast(bf16x8, b), c, 0, 0, 0)
#define MFMA16(a, b, c) __builtin_amdgcn_mfma_f32_16x16x32_bf16(__builtin_bit_cast(bf16x8, a), __builtin_bit_cast(bf16x8, b), c, 0, 0, 0)

constexpr int S_CH = 9;
constexpr int S_XRS = 4096 + 32;
constexpr int S_XR = 0;
constexpr int S_RC = S_CH * S_XRS;
constexpr int S_RCS = 4192;
constexpr int S_KD = S_RC + 4 * S_RCS;
constexpr int S_K = S_KD + 48 * 128;
constexpr int S_KROW = 96;
constexpr int S_TAB = S_K + 79 * S_KROW;
constexpr int S_TROW = 544;
constexpr int S_RED = S_TAB + 57 * S_TROW;
constexpr int S_TOTAL = S_RED + 4 * 64 * 144;
static_assert(S_RC % 16 == 0 && S_TAB % 16 == 0 && S_RED % 16 == 0, "");
DI f32x2 crot(f32x2 z) { return (f32x2){-z.y, z.x}; }
DI f32x2 cmul(f32x2 a, f32x2 z) { return __builtin_elementwise_fma(a.yy, crot(z), a.xx * z); }
DI f32x2 cexp_p(float ar, float ai, float p) {
    const float x = ai * p;
    const float hi = x * 0.15915494309189535f;
    const float lo = __builtin_fmaf(x, 0.15915494309189535f, -hi) + x * 6.420638316725915e-9f;
    const float rev = (hi - __builtin_rintf(hi)) + lo;
    const float m = __builtin_amdgcn_exp2f(ar * p * 1.4426950408889634f);
    return (f32x2){m * __builtin_amdgcn_cosf(rev), m * __builtin_amdgcn_sinf(rev)};
}
#define FENCE() __builtin_amdgcn_sched_barrier(0)
#define TT(c, i)
DI void role_s4d(const Args& a, LAS char* lds, unsigned lds_base, int task, rsrc_t rw, int& budget) {
    const int tid = threadIdx.x, lane = tid & 63, w = __builtin_amdgcn_readfirstlane(tid >> 6);
    const int d0 = task * S_CH;
    touch(w == 0 ? a.lAr : w == 1 ? a.Aim : w == 2 ? a.Cr : a.Ci, min(d0 * (NS * 2) + min(lane, 2 * S_CH + 1) * 128, DM * NS * 2 - 4), lds_base);
    touch(w & 1 ? a.ldt : a.Dsk, min(lane * 128, DM * 2 - 4), lds_base);
#define S4S(i)
    {
        const rsrc_t rx = __builtin_amdgcn_make_buffer_rsrc((void*)a.x, 0, SEQ * DM * 2, 0x00020000);
        const unsigned sh = (d0 & 1) * 16;
        u32x4 v[8]; unsigned e[8];
#pragma unroll
        for (int i = 0; i < 8; ++i) {
            const int o = (tid + 256 * i) * (DM * 2) + (d0 & ~1) * 2;
            v[i] = __builtin_amdgcn_raw_buffer_load_b128(rx, o, 0, 0); e[i] = __builtin_amdgcn_raw_buffer_load_b32(rx, o, 16, 0);
        }
#pragma unroll
        for (int p = 0; p < 2; ++p) {
            const int tok = 32 * task + 8 * w + 4 * p + (lane >> 4);
            float acc = 0.f;
#pragma unroll
            for (int i = 0; i < 5; ++i) {
                const int ch = (lane & 15) + 16 * i;
                const u32x4 gv = ch < 72 ? *(const u32x4*)(a.gw + ch * 8) : (u32x4){0u, 0u, 0u, 0u};
                const u32x4 xv = *(const u32x4*)(a.x + (size_t)tok * DM + min(ch, 71) * 8);
#pragma unroll
                for (int j = 0; j < 4; ++j) acc += bflo(xv[j]) * bflo(gv[j]) + bfhi(xv[j]) * bfhi(gv[j]);
            }
            acc += __shfl_xor(acc, 1); acc += __shfl_xor(acc, 2); acc += __shfl_xor(acc, 4); acc += __shfl_xor(acc, 8);
            st32(__float_as_uint(1.0f / (1.0f + __expf(-acc))), rw, (lane & 15) == 0 ? OFF_GT + tok * 4 : OOB, 0);
        }
#pragma unroll
        for (int i = 0; i < 8; ++i) {
            const unsigned x5[5] = {__builtin_amdgcn_alignbit(v[i].y, v[i].x, sh), __builtin_amdgcn_alignbit(v[i].z, v[i].y, sh), __builtin_amdgcn_alignbit(v[i].w, v[i].z, sh),
                                    __builtin_amdgcn_alignbit(e[i], v[i].w, sh), e[i] >> sh};
#pragma unroll
            for (int c = 0; c < S_CH; ++c) *(LAS u16*)(lds + S_XR + c * S_XRS + (2047 - tid - 256 * i) * 2) = (u16)(x5[c >> 1] >> (16 * (c & 1)));
        }
    }
    if (tid < 8 * S_CH) *(LAS unsigned*)(lds + S_XR + (tid >> 3) * S_XRS + 4096 + (tid & 7) * 4) = 0;
    if (tid < 64) *(LAS unsigned*)(lds + S_RC + (tid >> 4) * S_RCS + 4096 + (tid & 15) * 4) = 0;
    for (int i = tid; i < 15 * S_KROW / 4; i += 256) *(LAS unsigned*)(lds + S_K + i * 4) = 0;
    int kd[8];
#pragma unroll
    for (int j = 0; j < 8; ++j) {
        const int p = (int)bf2f(f2bf((float)(8 * tid + j))), k = p >> 7, r = p & 7;
        const int row = p < 256 ? 8 * k + r : (p < 512 ? 8 + 4 * k + (r >> 1) : (p < 1024 ? 16 + 2 * k + (r >> 2) : 24 + k));
        kd[j] = S_KD + (row * 16 + ((p >> 3) & 15)) * 8;
    }
    const int n = tid & 127, nn = lane & 15, g = lane >> 4;
    u16 pA = a.lAr[d0 * NS + n], pI = a.Aim[d0 * NS + n], pCr = a.Cr[d0 * NS + n], pCi = a.Ci[d0 * NS + n], pT = a.ldt[d0], pD = a.Dsk[d0];
    WG_BARRIER();
    for (int ch = 0; ch < S_CH; ++ch) {
        S4S(0)
        const float dsk = bf2f(pD);
        {
            const u32x4 v = *(LAS u32x4*)(lds + S_XR + ch * S_XRS + tid * 16);
            const u32x2 e = *(LAS u32x2*)(lds + S_XR + ch * S_XRS + tid * 16 + 16);
            const unsigned s01 = __builtin_amdgcn_alignbit(v.y, v.x, 16), s12 = __builtin_amdgcn_alignbit(v.z, v.y, 16), s23 = __builtin_amdgcn_alignbit(v.w, v.z, 16);
            const unsigned s34 = __builtin_amdgcn_alignbit(e.x, v.w, 16), s45 = __builtin_amdgcn_alignbit(e.y, e.x, 16);
            *(LAS u32x4*)(lds + S_RC + tid * 16) = v;
            *(LAS u32x4*)(lds + S_RC + S_RCS + tid * 16) = (u32x4){s01, s12, s23, s34};
            *(LAS u32x4*)(lds + S_RC + 2 * S_RCS + tid * 16) = (u32x4){v.y, v.z, v.w, e.x};
            *(LAS u32x4*)(lds + S_RC + 3 * S_RCS + tid * 16) = (u32x4){s12, s23, s34, s45};
        }
        S4S(1)
        const float dt = bf2f(f2bf(expf(bf2f(pT))));
        const int sc = 140 - (wave_max32(max(pCr & 0x7FFF, pCi & 0x7FFF)) >> 7);
        {
            const float ar = -bf2f(f2bf(expf(bf2f(pA)))) * dt, ai = bf2f(pI) * dt;
            const f32x2 C = {__builtin_amdgcn_ldexpf(bf2f(pCr), sc), __builtin_amdgcn_ldexpf(bf2f(pCi), sc)};
            const int dn = min(d0 + ch + 1, DM - 1);
            pA = a.lAr[dn * NS + n]; pI = a.Aim[dn * NS + n]; pCr = a.Cr[dn * NS + n]; pCi = a.Ci[dn * NS + n]; pT = a.ldt[dn]; pD = a.Dsk[dn];
            const int to = S_TAB + n * 4;
            auto put = [&](int row, f32x2 v) { *(LAS f16x2*)(lds + to + row * S_TROW) = __builtin_convertvector(v, f16x2); };
            const f32x2 z128 = cexp_p(ar, ai, 128.f);
            if (w < 2) {
                f32x2 E[8], G[4];
                E[1] = cexp_p(ar, ai, 1.f); G[0] = C;
#pragma unroll
                for (int r = 2; r < 8; ++r) E[r] = cmul(E[r >> 1], E[r - (r >> 1)]);
#pragma unroll
                for (int k = 1; k < 4; ++k) G[k] = cmul(G[k - 1], z128);
#pragma unroll
                for (int k = 0; k < 4; ++k)
#pragma unroll
                    for (int r = 0; r < 8; r += 1 + (k >> 1)) put(k < 2 ? 8 * k + r : 8 + 4 * k + (r >> 1), r ? cmul(E[r], G[k]) : G[k]);
            } else {
                const f32x2 z4 = cexp_p(ar, ai, 4.f), z8 = cexp_p(ar, -ai, 8.f);
                f32x2 G = cmul(C, cexp_p(ar, ai, 512.f)), B = z8;
                put(41, (f32x2){1.f, 0.f});
#pragma unroll
                for (int k = 1; k < 16; ++k) {
                    put(41 + k, B); B = cmul(B, z8);
                    if (k < 5) { put(22 + 2 * k, G); put(23 + 2 * k, cmul(G, z4)); }
                    else if (k < 14) put(27 + k, G);
                    G = cmul(G, z128);
                }
            }
        }
        WG_BARRIER();
        S4S(2)
        {
            const int ko = (w & 1) * 256 + g * 16, t0 = w & 2;
            u32x4 av[2][4], bv[4];
#pragma unroll
            for (int j = 0; j < 4; ++j) {
                bv[j] = *(LAS u32x4*)(lds + S_TAB + (41 + nn) * S_TROW + ko + j * 64);
                av[0][j] = *(LAS u32x4*)(lds + S_TAB + min(16 * t0 + nn, 40) * S_TROW + ko + j * 64);
                av[1][j] = *(LAS u32x4*)(lds + S_TAB + (16 + nn) * S_TROW + ko + j * 64);
            }
            f32x4 acc[2] = {};
#pragma unroll
            for (int j = 0; j < 8; ++j) acc[j & 1] = __builtin_amdgcn_mfma_f32_16x16x32_f16(__builtin_bit_cast(f16x8, av[j & 1][j >> 1]), __builtin_bit_cast(f16x8, bv[j >> 1]), acc[j & 1], 0, 0, 0);
#pragma unroll
            for (int r = 0; r < 8; ++r) *(LAS float*)(lds + S_KD + (((r < 4 ? 16 * t0 : 16) + 4 * g + (r & 3)) * 16 + nn) * 8 + (w & 1) * 4) = __builtin_amdgcn_ldexpf(acc[r >> 2][r & 3] * dt, 1 - sc);
        }
        WG_BARRIER();
        S4S(3)
        {
            float kv[8];
#pragma unroll
            for (int j = 0; j < 8; ++j) { const f32x2 t = *(LAS f32x2*)(lds + kd[j]); kv[j] = t.x + t.y; }
            *(LAS u32x4*)(lds + S_K + (15 + (tid >> 2)) * S_KROW + (tid & 3) * 16) = (u32x4){pack2(kv[0], kv[1]), pack2(kv[2], kv[3]), pack2(kv[4], kv[5]), pack2(kv[6], kv[7])};
        }
        WG_BARRIER();
        S4S(4)
        {
            const int st0 = 2047 - nn + 8 * g - 32 * w, cc = st0 & 3;
            const int ub = S_RC + cc * S_RCS + (st0 - cc) * 2 - 3872, kb = S_K + (3 + nn - w) * S_KROW + g * 16;
            f32x4 y[4][2] = {};
            u32x2 U[2][16]; u32x4 F[2][4];
            auto rd = [&](int i, int f) {
                if (f < 4) F[i & 1][f] = *(LAS u32x4*)(lds + kb + (12 - 4 * i + 16 * f) * S_KROW);
                else U[i & 1][f - 4] = *(LAS u32x2*)(lds + ub + 3872 - 256 * i - 1024 * ((f - 4) >> 2) - 32 * ((f >> 1) & 1) + 8 * (f & 1));
            };
#pragma unroll
            for (int f = 0; f < 20; ++f) { rd(0, f); FENCE(); }
#pragma unroll
            for (int i = 0; i < 4; ++i) {
                int c = 0;
#pragma unroll
                for (int q = 0; q < 4; ++q)
#pragma unroll
                    for (int T = q; T < 4; ++T)
#pragma unroll
                        for (int bt = 0; bt < 2; ++bt) {
                            y[T][bt] = MFMA16(__builtin_shufflevector(U[i & 1][4 * q + 2 * bt], U[i & 1][4 * q + 2 * bt + 1], 0, 1, 2, 3), F[i & 1][T - q], y[T][bt]);
                            if (i < 3) rd(i + 1, c);
                            ++c;
                            FENCE();
                        }
            }
#pragma unroll
            for (int T = 0; T < 4; ++T)
#pragma unroll
                for (int bt = 0; bt < 2; ++bt) *(LAS f32x4*)(lds + S_RED + (w * 64 + 16 * T + nn) * 144 + bt * 64 + g * 16) = y[T][bt];
        }
        WG_BARRIER();
        S4S(5)
        {
            f32x4 y0 = {0, 0, 0, 0}, y1 = y0;
#pragma unroll
            for (int ww = 0; ww < 4; ++ww) {
                const int o = S_RED + (ww * 64 + (tid >> 2)) * 144 + (tid & 3) * 32;
                y0 += *(LAS f32x4*)(lds + o); y1 += *(LAS f32x4*)(lds + o + 16);
            }
            const u32x4 u = *(LAS u32x4*)(lds + S_XR + ch * S_XRS + (255 - tid) * 16);
            st128((u32x4){pack2(y0[0] + bfhi(u.w) * dsk, y0[1] + bflo(u.w) * dsk), pack2(y0[2] + bfhi(u.z) * dsk, y0[3] + bflo(u.z) * dsk),
                          pack2(y1[0] + bfhi(u.y) * dsk, y1[1] + bflo(u.y) * dsk), pack2(y1[2] + bfhi(u.x) * dsk, y1[3] + bflo(u.x) * dsk)}, rw, tid * 16, OFF_BO + (d0 + ch) * BO_ROW);
        }
        S4S(6)
    }
    wait_init(rw, launch_tag(a), budget);
    VM_DRAIN();
    WG_BARRIER();
    if (tid == 0) st32(1u, rw, F_S4 + task * 4, 0);
}

constexpr int XT_BYTES = 32 * 1152;
constexpr int P_XT = 0;
constexpr int P_STG = 2 * XT_BYTES;
struct WSlice { u32x4 f[36]; };
constexpr int P_RING = 5;
template <class F> DI void load_wslices(WSlice& W0, WSlice& W1, const u16* s0, const u16* s1, int w, int lane, LAS char* lds, unsigned lds_base, F also) {
    constexpr int NB = 18;
    const int r0 = 8 * (lane >> 5) + ((lane & 15) >> 2);
    const int cb = (4 * w + 2 * ((lane >> 4) & 1) + ((lane & 3) >> 1)), lo8 = (lane & 1) * 8;
    auto src = [&](int i) { return (i < 9 ? s0 : s1) + (size_t)(64 * (i % 9)) * HD; };
    const u32x4 ro = ring_offs(w, lane, HD);
#pragma unroll
    for (int i = 0; i < P_RING - 1; ++i) ring_issue(src(i), ro, lds_base + P_STG + i * 16384 + w * 4096);
    also();
    static_for<NB>([&](auto ic) {
        constexpr int i = decltype(ic)::value, rem = NB - 1 - i < P_RING - 2 ? NB - 1 - i : P_RING - 2;
        ring_wait<4 * rem>();
        if constexpr (i + P_RING - 1 < NB) ring_issue(src(i + P_RING - 1), ro, lds_base + P_STG + ((i + P_RING - 1) % P_RING) * 16384 + w * 4096);
        const int sl = P_STG + (i % P_RING) * 16384;
#pragma unroll
        for (int kk = 0; kk < 4; ++kk) {
            const int ra = r0 + 16 * kk, rb = ra + 4;
            const u32x2 lo = tr_read(lds, sl + ra * 256 + ((cb ^ (2 * swz(ra))) * 16) + lo8), hi = tr_read(lds, sl + rb * 256 + ((cb ^ (2 * swz(rb))) * 16) + lo8);
            (i < 9 ? W0 : W1).f[(i % 9) * 4 + kk] = (u32x4){lo.x, lo.y, hi.x, hi.y};
        }
        u32x4* f = (i < 9 ? W0 : W1).f + (i % 9) * 4;
        if constexpr (i < 16) asm volatile("" : "+a"(f[0]), "+a"(f[1]), "+a"(f[2]), "+a"(f[3]));
    });
}
DI u32x4 piece_pi(const f32x16& a, int qq) {
    return (u32x4){pack2(a[4 * qq], a[4 * qq + 8]), pack2(a[4 * qq + 1], a[4 * qq + 9]), pack2(a[4 * qq + 2], a[4 * qq + 10]), pack2(a[4 * qq + 3], a[4 * qq + 11])};
}
DI u32x4 piece_seq(const f32x16& a, int s2) {
    return (u32x4){pack2(a[8 * s2], a[8 * s2 + 1]), pack2(a[8 * s2 + 2], a[8 * s2 + 3]), pack2(a[8 * s2 + 4], a[8 * s2 + 5]), pack2(a[8 * s2 + 6], a[8 * s2 + 7])};
}
struct XTile {
    int xs[9], xa[4];
    DI void init(int w, int lane) {
#pragma unroll
        for (int i = 0; i < 9; ++i) { const int L = (i * 4 + w) * 64 + lane, r = L / 72, cs = L - 72 * r; xs[i] = r * 1152 + (cs ^ ((r >> 1) & 7)) * 16; }
        const int c32 = lane & 31, hf = lane >> 5;
#pragma unroll
        for (int j = 0; j < 4; ++j) xa[j] = P_XT + c32 * 1152 + (((2 * j + hf) ^ ((c32 >> 1) & 7)) * 16);
    }
    DI void dma(const Args& a, unsigned lds_base, int w, int s, int buf, int i) const {
        glds16((const char*)a.x + (size_t)min(s, NSEG - 1) * XT_BYTES, xs[i], lds_base + P_XT + buf * XT_BYTES + (i * 4 + w) * 1024);
    }
    DI void first(const Args& a, unsigned lds_base, int w, int lane, int s) {
        init(w, lane);
#pragma unroll
        for (int i = 0; i < 9; ++i) dma(a, lds_base, w, s, 0, i);
    }
};
template <class F> DI void project(f32x16& acc0, f32x16& acc1, const WSlice& W0, const WSlice& W1, const XTile& X, const Args& a, LAS char* lds, unsigned lds_base, int w, int buf, int sn, F gap) {
    const int bo = buf * XT_BYTES;
    constexpr int PD = 5;
    u32x4 xf[PD];
#pragma unroll
    for (int i = 0; i < PD; ++i) xf[i] = *(LAS u32x4*)(lds + X.xa[i & 3] + bo + (i >> 2) * 128);
    static_for<36>([&](auto ic) {
        constexpr int ks = decltype(ic)::value;
        if (ks == 0) asm volatile("v_mfma_f32_32x32x16_bf16 %0, %1, %2, 0" : "=&v"(acc0) : "a"(W0.f[0]), "v"(xf[0]) : "memory");
        else asm volatile("v_mfma_f32_32x32x16_bf16 %0, %1, %2, %0" : "+v"(acc0) : "a"(W0.f[ks]), "v"(xf[ks % PD]) : "memory");
        if (ks < 9) X.dma(a, lds_base, w, sn, buf ^ 1, ks);
        gap(ic_t<2 * ks>{});
        if (ks == 0) asm volatile("v_mfma_f32_32x32x16_bf16 %0, %1, %2, 0" : "=&v"(acc1) : "a"(W1.f[0]), "v"(xf[0]) : "memory");
        else if (ks < 28) asm volatile("v_mfma_f32_32x32x16_bf16 %0, %1, %2, %0" : "+v"(acc1) : "a"(W1.f[ks]), "v"(xf[ks % PD]) : "memory");
        else asm volatile("v_mfma_f32_32x32x16_bf16 %0, %1, %2, %0" : "+v"(acc1) : "v"(W1.f[ks]), "v"(xf[ks % PD]) : "memory");
        if (ks + PD < 36) xf[ks % PD] = *(LAS u32x4*)(lds + X.xa[(ks + PD) & 3] + bo + ((ks + PD) >> 2) * 128);
        gap(ic_t<2 * ks + 1>{});
    });
    asm volatile("s_nop 7\n\ts_nop 3" : "+v"(acc0), "+v"(acc1) :: "memory");
}
#define KV_T 7
#define KV_F 22
DI void role_kv(const Args& a, LAS char* lds, unsigned lds_base, int h, rsrc_t rw, int& budget) {
    const int tid = threadIdx.x, lane = tid & 63, w = __builtin_amdgcn_readfirstlane(tid >> 6);
    WSlice W0, W1;
    XTile X;
    u32x2 tag;
    load_wslices(W0, W1, a.Wk + h * DH, a.Wv + h * DH, w, lane, lds, lds_base, [&]() { X.first(a, lds_base, w, lane, 0); tag = launch_tag(a); });
    VM_DRAIN();
    WG_BARRIER();
    const int oo = h * 8192 + w * 2048 + lane * 16;
    const int fo = lane == 0 ? F_KV + h * 512 + w * 128 : OOB;
    u32x4 xf[6], P[4] = {};
    auto frag = [&](int buf, int ks) { xf[ks % 6] = *(LAS u32x4*)(lds + X.xa[ks & 3] + buf * XT_BYTES + (ks >> 2) * 128); };
    auto cvt = [&](const f32x16& k, const f32x16& v, int i) {
#pragma unroll
        for (int j = 0; j < 4; ++j) asm volatile("v_cvt_pk_bf16_f32 %0, %1, %2" : "=v"(P[i][j]) : "v"(i < 2 ? k[4 * i + j] : v[8 * i - 16 + 2 * j]), "v"(i < 2 ? k[4 * i + j + 8] : v[8 * i - 15 + 2 * j]));
    };
    auto put = [&](int o, int s, int i) { st128(P[i], rw, o + (i & 1) * 1024, (i < 2 ? OFF_KF : OFF_VF) + s * (NH * 8192)); };
    auto now_ = [&](f32x16& k, f32x16& v, int s) {
        asm volatile("s_nop 7\n\ts_nop 3" : "+v"(k), "+v"(v) :: "memory");
#pragma unroll
        for (int i = 0; i < 4; ++i) { cvt(k, v, i); put(oo, s, i); }
    };
    u32x2 in = {};
    auto seg = [&](f32x16& k, f32x16& v, const f32x16& pk, const f32x16& pv, int s, int buf) {
        const int o = s > 1 ? oo : OOB;
#pragma unroll
        for (int ks = 0; ks < 36; ++ks) {
            if (ks == 0) asm volatile("v_mfma_f32_32x32x16_bf16 %0, %1, %2, 0" : "=&v"(k) : "a"(W0.f[0]), "v"(xf[0]) : "memory");
            else asm volatile("v_mfma_f32_32x32x16_bf16 %0, %1, %2, %0" : "+v"(k) : "a"(W0.f[ks]), "v"(xf[ks % 6]) : "memory");
            if (ks >= 1 && ks < 5) cvt(pk, pv, ks - 1);
            if (buf && ks == KV_T && s == 1) {
                asm volatile("s_waitcnt vmcnt(%0)" :: "i"(4 + (KV_T - 4) / 2) : "memory");
                __builtin_amdgcn_raw_buffer_store_b64(tag, rw, lane == 0 ? F_INIT + (33 + 4 * h + w) * 128 : OOB, 0, 16);
                if (!__all(((in.x ^ tag.x) >> 1) == 0 && in.y == tag.y)) wait_init(rw, tag, budget);
            }
            if (ks >= 5 && ks < 13 && (ks & 1)) put(o, s - 1, (ks - 5) >> 1);
            if (ks == KV_F) { VM_DRAIN(); st32((unsigned)(s - 1), rw, s > 1 ? fo : OOB, 0); }
            if (ks == 0) asm volatile("v_mfma_f32_32x32x16_bf16 %0, %2, %1, 0" : "=&v"(v) : "a"(W1.f[0]), "v"(xf[0]) : "memory");
            else if (ks < 28) asm volatile("v_mfma_f32_32x32x16_bf16 %0, %2, %1, %0" : "+v"(v) : "a"(W1.f[ks]), "v"(xf[ks % 6]) : "memory");
            else asm volatile("v_mfma_f32_32x32x16_bf16 %0, %2, %1, %0" : "+v"(v) : "v"(W1.f[ks]), "v"(xf[ks % 6]) : "memory");
            if (ks == 30) WG_BARRIER();
            frag(buf ^ (ks >= 30), (ks + 6) % 36);
            if (ks < 4) X.dma(a, lds_base, w, s + 1, buf ^ 1, ks + 5);
            if (ks > 30) X.dma(a, lds_base, w, s + 2, buf, ks - 31);
        }
    };
#pragma unroll
    for (int i = 0; i < 6; ++i) frag(0, i);
#pragma unroll
    for (int i = 0; i < 5; ++i) X.dma(a, lds_base, w, 1, 1, i);
    f32x16 k0, v0, k1 = {}, v1 = {};
    for (int s = 0; s < NSEG; s += 2) {
        seg(k0, v0, k1, v1, s, 0);
        if (s == 0) { in = ld64(rw, F_INIT + (lane & 31) * 128, 0); now_(k0, v0, 0); }
        seg(k1, v1, k0, v0, s + 1, 1);
    }
    now_(k1, v1, NSEG - 1);
    VM_DRAIN();
    st32(NSEG, rw, fo, 0);
}

DI int prog_min(rsrc_t rw, int off) {
    const int v = (int)ld32(rw, off, 0);
    asm volatile("" ::: "memory");
    return -wave_max32(-v);
}
DI float red_max_g(float v) {
    auto a = __builtin_amdgcn_permlane16_swap(__float_as_uint(v), __float_as_uint(v), false, false);
    v = fmaxf(__uint_as_float(a[0]), __uint_as_float(a[1]));
    auto b = __builtin_amdgcn_permlane32_swap(__float_as_uint(v), __float_as_uint(v), false, false);
    return fmaxf(__uint_as_float(b[0]), __uint_as_float(b[1]));
}
#define M16 "v_mfma_f32_16x16x32_bf16 "
DI void role_qa(const Args& a, LAS char* lds, unsigned lds_base, int h0, int s0, int s1, rsrc_t rw, int& budget) {
    const int tid = threadIdx.x, lane = tid & 63, w = __builtin_amdgcn_readfirstlane(tid >> 6);
    const int n = lane & 15, g = lane >> 4, h = h0 + (w >> 1), tt = w & 1;
    WSlice W0, W1;
    XTile X;
    load_wslices(W0, W1, a.Wq + h0 * DH, a.Wq + (h0 + 1) * DH, w, lane, lds, lds_base, [&]() { X.first(a, lds_base, w, lane, s0); });
    const int oo = OFF_OF + (2 * tt + (n >> 3)) * 65536 + h * 2048 + g * 128 + (n & 7) * 16;
    const bool fast = s0 >= FAST;
    if (fast)
#pragma unroll
        for (int t = 0; t < 8; ++t) st128((u32x4){SENT, SENT, SENT, SENT}, rw, oo + (t & 3) * 512, (s0 + (t >> 2) * 2) * 262144);
    VM_DRAIN();
    WG_BARRIER();
    constexpr int Q_Q = P_STG, Q_KV = Q_Q + 16384, Q_M = Q_KV + 32768;
    static_assert(Q_M + 16384 <= P_STG + P_RING * 16384, "");
    const int fk = F_KV + h * 512 + (lane & 3) * 128, fm = F_KVM + h * 256 + (lane & 1) * 128;
    int pm_, m_rdy = -1, kv_rdy = -1;
    auto look = [&]() { pm_ = (int)ld32(rw, lane & 4 ? fk : fm, 0); };
    auto seen = [&]() { auto rl = [&](int i) { return __builtin_amdgcn_readlane(pm_, i); }; m_rdy = min(rl(0), rl(1)); kv_rdy = min(min(rl(4), rl(5)), min(rl(6), rl(7))); };
    auto wait_m = [&](int thr, unsigned& t) {
        for (;;) {
            const u32x2 v = ld64(rw, fm, 0);
            asm volatile("" ::: "memory");
            t = now();
            const int p0 = __builtin_amdgcn_readlane(v.x, 0), p1 = __builtin_amdgcn_readlane(v.x, 1);
            const int d0 = (int)(__builtin_amdgcn_readlane(v.y, 0) - 8 * t) >> 3, d1 = (int)(__builtin_amdgcn_readlane(v.y, 1) - 8 * t) >> 3;
            if (min(p0, p1) >= thr || --budget < 0) return max(p0 == thr ? d0 : -1000, p1 == thr ? d1 : -1000);
            (void)wait_until(t + min(max(min(d0, d1) + (int)(a.xa >> 16 & 255), 10), 200));
        }
    };
    auto proj = [&](int s, auto gap) {
        f32x16 acc0, acc1;
        project(acc0, acc1, W0, W1, X, a, lds, lds_base, w, ((s - s0) >> 1) & 1, s + 2, [&](auto ic) { gap(ic); if (decltype(ic)::value == 55) look(); });
        const int o = Q_Q + w * 2048 + lane * 16;
        *(LAS u32x4*)(lds + o) = piece_pi(acc0, 0); *(LAS u32x4*)(lds + o + 1024) = piece_pi(acc0, 1);
        *(LAS u32x4*)(lds + o + 8192) = piece_pi(acc1, 0); *(LAS u32x4*)(lds + o + 8192 + 1024) = piece_pi(acc1, 1);
    };
    const int fs = (tt ? OFF_VF : OFF_KF) + (g >> 1) * 1024 + ((g & 1) * 32 + 16 * (n >> 3) + (n & 7)) * 16;
    auto fetch = [&](int s) {
        const int so = (min(s, NSEG - 1) * NH + h) * 8192;
#pragma unroll
        for (int f = 0; f < 8; ++f) bdma(rw, fs, so + (f >> 1) * 2048 + (f & 1) * 128, lds_base + Q_KV + w * 8192 + f * 1024);
    };
    const int ms = OFF_KM + (lane & 7) * 256 + (lane >> 3) * 16;
    auto fetch_m = [&](int s) {
        const int so = (s * NH + h) * 2048;
        const unsigned d = lds_base + Q_M + w * 4096;
        bdma(rw, ms, so, d); bdma(rw, ms, so + 128, d + 1024); bdma(rw, lane * 16, OFF_VM + so, d + 2048); bdma(rw, lane * 16, OFF_VM + so + 1024, d + 3072);
    };
    const float c = 0.08838834764831845f * 1.4426950408889634f;
    u32x4 qf[4];
    f32x4 o[8]; float mx, l;
    auto xpart = [&]() {
        const int kb = Q_KV + (w >> 1) * 16384 + lane * 16;
        u32x4 kn[8], vn[8];
#pragma unroll
        for (int ks = 0; ks < 4; ++ks) qf[ks] = *(LAS u32x4*)(lds + Q_Q + (w >> 1) * 8192 + (2 * ks + (g >> 1)) * 1024 + ((g & 1) * 32 + 16 * tt + n) * 16);
#pragma unroll
        for (int f = 0; f < 8; ++f) kn[f] = *(LAS u32x4*)(lds + kb + f * 1024);
        FENCE();
#pragma unroll
        for (int f = 0; f < 8; ++f) vn[f] = *(LAS u32x4*)(lds + kb + 8192 + f * 1024);
        f32x4 s0, s1;
        asm volatile(M16 "%0, %2, %10, 0\n\t" M16 "%1, %3, %10, 0\n\t" M16 "%0, %4, %11, %0\n\t" M16 "%1, %5, %11, %1\n\t"
                     M16 "%0, %6, %12, %0\n\t" M16 "%1, %7, %12, %1\n\t" M16 "%0, %8, %13, %0\n\t" M16 "%1, %9, %13, %1\n\ts_nop 7"
                     : "=&v"(s0), "=&v"(s1) : "v"(kn[0]), "v"(kn[1]), "v"(kn[2]), "v"(kn[3]), "v"(kn[4]), "v"(kn[5]), "v"(kn[6]), "v"(kn[7]), "v"(qf[0]), "v"(qf[1]), "v"(qf[2]), "v"(qf[3]));
        float p[8];
        mx = -3.0e38f; l = 0.f;
#pragma unroll
        for (int r = 0; r < 8; ++r) { const int key = 16 * (g >> 1) + 4 * (g & 1) + (r & 3) + 8 * (r >> 2); p[r] = key <= 16 * tt + n ? (r < 4 ? s0[r & 3] : s1[r & 3]) : -3.0e38f; mx = fmaxf(mx, p[r]); }
        mx = red_max_g(mx);
#pragma unroll
        for (int r = 0; r < 8; ++r) { p[r] = __builtin_amdgcn_exp2f((p[r] - mx) * c); l += p[r]; }
        const u32x4 pf = {pack2(p[0], p[1]), pack2(p[2], p[3]), pack2(p[4], p[5]), pack2(p[6], p[7])};
        asm volatile("s_nop 1\n\t" M16 "%0, %8, %16, 0\n\t" M16 "%1, %9, %16, 0\n\t" M16 "%2, %10, %16, 0\n\t" M16 "%3, %11, %16, 0\n\t"
                     M16 "%4, %12, %16, 0\n\t" M16 "%5, %13, %16, 0\n\t" M16 "%6, %14, %16, 0\n\t" M16 "%7, %15, %16, 0\n\ts_nop 7"
                     : "=&v"(o[0]), "=&v"(o[1]), "=&v"(o[2]), "=&v"(o[3]), "=&v"(o[4]), "=&v"(o[5]), "=&v"(o[6]), "=&v"(o[7])
                     : "v"(vn[0]), "v"(vn[1]), "v"(vn[2]), "v"(vn[3]), "v"(vn[4]), "v"(vn[5]), "v"(vn[6]), "v"(vn[7]), "v"(pf));
    };
    const int fo = lane == 0 ? F_O + h * 128 + (2 * s0 + tt) * 4 : OOB;
    bool pre = false;
    auto both = [&](int s) {
        const int kb = Q_KV + (w >> 1) * 16384 + lane * 16;
        u32x4 kn[8], vn[8], pf;
        f32x4 sc[2];
        float p[8];
        int v0, v1, v2;
        asm volatile("buffer_load_dword %0, %1, %2, 0 offen sc1" : "=v"(pm_) : "v"(lane & 4 ? fk : fm), "s"(rw) : "memory");
#define PIN(...) asm volatile("" : __VA_ARGS__)
        proj(s + 4, [&](auto ic) {
            constexpr int i = decltype(ic)::value;
            if constexpr (i < 8) {
                if (!(i & 1)) qf[i >> 1] = *(LAS u32x4*)(lds + Q_Q + (w >> 1) * 8192 + (i + (g >> 1)) * 1024 + ((g & 1) * 32 + 16 * tt + n) * 16);
                kn[i] = *(LAS u32x4*)(lds + kb + i * 1024);
            }
            if constexpr (i >= 16 && i < 32 && !(i & 1)) vn[(i - 16) >> 1] = *(LAS u32x4*)(lds + kb + 8192 + (i - 16) * 512);
            if constexpr (i == 5 || i == 6) asm volatile(M16 "%0, %1, %2, 0" : "=&v"(sc[i - 5]) : "v"(kn[i - 5]), "v"(qf[0]));
            if constexpr (i > 6 && i < 13) asm volatile(M16 "%0, %1, %2, %0" : "+v"(sc[(i - 5) & 1]) : "v"(kn[i - 5]), "v"(qf[(i - 5) >> 1]));
            if constexpr (i == 15 || i == 16) {
                constexpr int r = 4 * (i - 15);
                PIN("+v"(sc[i - 15]));
#pragma unroll
                for (int j = 0; j < 4; ++j) p[r + j] = 16 * (g >> 1) + 4 * (g & 1) + j + 2 * r <= 16 * tt + n ? sc[i - 15][j] : -3.0e38f;
                PIN("+v"(p[r]), "+v"(p[r + 1]), "+v"(p[r + 2]), "+v"(p[r + 3]));
            }
            if constexpr (i == 17) { mx = fmaxf(fmaxf(fmaxf(fmaxf(p[0], p[1]), p[2]), fmaxf(fmaxf(p[3], p[4]), p[5])), fmaxf(p[6], p[7])); PIN("+v"(mx)); }
            if constexpr (i == 18) { auto x = __builtin_amdgcn_permlane16_swap(__float_as_uint(mx), __float_as_uint(mx), false, false); mx = fmaxf(__uint_as_float(x[0]), __uint_as_float(x[1])); PIN("+v"(mx)); }
            if constexpr (i == 19) { auto x = __builtin_amdgcn_permlane32_swap(__float_as_uint(mx), __float_as_uint(mx), false, false); mx = fmaxf(__uint_as_float(x[0]), __uint_as_float(x[1])); PIN("+v"(mx)); }
            if constexpr (i >= 20 && i < 24) {
                constexpr int r = 2 * (i - 20);
                PIN("+v"(p[r]), "+v"(p[r + 1]));
                p[r] = __builtin_amdgcn_exp2f((p[r] - mx) * c); p[r + 1] = __builtin_amdgcn_exp2f((p[r + 1] - mx) * c);
                PIN("+v"(p[r]), "+v"(p[r + 1]));
            }
            if constexpr (i == 24) { pf = (u32x4){pack2(p[0], p[1]), pack2(p[2], p[3]), pack2(p[4], p[5]), pack2(p[6], p[7])}; PIN("+v"(pf)); }
            if constexpr (i == 25) { l = ((p[0] + p[1]) + p[2]) + p[3]; PIN("+v"(l)); }
            if constexpr (i == 26) { l = (((l + p[4]) + p[5]) + p[6]) + p[7]; PIN("+v"(l)); }
            if constexpr (i == 27) { asm volatile("s_waitcnt vmcnt(9)" : "+v"(pm_) :: "memory"); st32((unsigned)s, rw, fo, 0); }
            if constexpr (i == 29) { seen(); pre = m_rdy >= s + 2 && kv_rdy >= s + 4; }
            if constexpr (i == 31) { v0 = pre ? ms : OOB; v1 = pre ? lane * 16 : OOB; v2 = pre ? fs : OOB; PIN("+v"(v0), "+v"(v1), "+v"(v2)); }
            if constexpr (i >= 28 && i < 36) asm volatile(M16 "%0, %1, %2, 0" : "=&v"(o[i - 28]) : "v"(vn[i - 28]), "v"(pf));
            if constexpr (i == 36) asm volatile("s_barrier" ::: "memory");
            if constexpr (i >= 38 && i < 42) bdma(rw, i < 40 ? v0 : v1, ((s + 2) * NH + h) * 2048 + (i < 40 ? (i & 1) * 128 : OFF_VM + (i & 1) * 1024), lds_base + Q_M + w * 4096 + (i - 38) * 1024);
            if constexpr (i >= 42 && i < 50) bdma(rw, v2, ((s + 4) * NH + h) * 8192 + ((i - 42) >> 1) * 2048 + (i & 1) * 128, lds_base + Q_KV + w * 8192 + (i - 42) * 1024);
        });
    };
    auto none = [](auto) {};
    proj(s0, none);
    if (!fast) wait_init(rw, launch_tag(a), budget);
    wait_prog(rw, fk, true, fast ? s1 : s0, budget);
    fetch(s0);
    VM_DRAIN();
    WG_BARRIER();
    xpart();
    WG_BARRIER();
    proj(s0 + 2, none);
    for (int s = s0; s <= s1; s += 2) {
        S4S(0)
        const int mb = Q_M + w * 4096;
        u32x4 kf[4]; u32x2 vm[4];
        auto frags = [&]() {
#pragma unroll
            for (int ks = 0; ks < 4; ++ks) kf[ks] = *(LAS u32x4*)(lds + mb + (4 * ks + g) * 128 + (2 * (n >> 2) + (n & 1)) * 16);
#pragma unroll
            for (int t = 0; t < 4; ++t) { const int vo = mb + 2048 + t * 512 + (n >> 3) * 256 + g * 64 + (n & 7) * 4; vm[t] = (u32x2){*(LAS unsigned*)(lds + vo), *(LAS unsigned*)(lds + vo + 32)}; }
        };
        if (fast) {
            TT(s == s0, 3)
            if (s < s1) fetch(s1);
            unsigned t;
            const int d = min(wait_m(min(s - 1, NSEG - 3), t) + (int)(a.xa >> (s == NSEG - 1 ? 8 : 0) & 255), 500);
            st32((t + max(d, 0) + (a.xb >> 8 * (s - FAST) & 255)) & 0x7FFFFFFFu, rw, lane == 0 ? F_H + ((2 * (s - FAST) + tt) * NH + h) * 128 : OOB, 0);
            (void)wait_until(t + d);
            TT(true, 0)
            for (;;) {
                fetch_m(s);
                VM_DRAIN();
                frags();
                unsigned c = 0;
#pragma unroll
                for (int i = 0; i < 4; ++i) c = max(max(mx4(c, kf[i]), vm[i].x), vm[i].y);
                if (!__any(c == SENT) || --budget < 0) break;
            }
        } else {
            if (!pre) {
                seen();
                unsigned t;
                if (m_rdy < s) { const int d = wait_m(s, t); (void)wait_until(t + min(d + (int)(a.xa >> 24), 300)); }
                fetch_m(s);
                if (kv_rdy < s + 2) wait_prog(rw, fk, true, s + 2, budget);
                fetch(s + 2);
            }
            S4S(1)
            asm volatile("s_waitcnt vmcnt(8)" ::: "memory");
            frags();
        }
        TT(s == s1, 1)
        S4S(2)
        f32x4 sm;
        asm volatile(M16 "%0, %1, %5, 0\n\t" M16 "%0, %2, %6, %0\n\t" M16 "%0, %3, %7, %0\n\t" M16 "%0, %4, %8, %0\n\ts_nop 7"
                     : "=&v"(sm) : "v"(kf[0]), "v"(kf[1]), "v"(kf[2]), "v"(kf[3]), "v"(qf[0]), "v"(qf[1]), "v"(qf[2]), "v"(qf[3]));
        const float m2 = red_max_g(fmaxf(mx, fmaxf(sm[0], sm[1])));
        const float al = __builtin_amdgcn_exp2f((mx - m2) * c), p0 = __builtin_amdgcn_exp2f((sm[0] - m2) * c), p1 = __builtin_amdgcn_exp2f((sm[1] - m2) * c);
        const float inv = __builtin_amdgcn_rcpf(red_sum_g(l * al + p0 + p1)), ai = al * inv;
#pragma unroll
        for (int t = 0; t < 8; ++t) o[t] *= ai;
        const unsigned pm = pack2(p0 * inv, p1 * inv);
#define M1K "v_mfma_f32_16x16x16_bf16 "
        asm volatile("s_nop 1\n\t" M1K "%0, %8, %12, %0\n\t" M1K "%1, %8, %13, %1\n\t" M1K "%2, %9, %12, %2\n\t" M1K "%3, %9, %13, %3\n\t"
                     M1K "%4, %10, %12, %4\n\t" M1K "%5, %10, %13, %5\n\t" M1K "%6, %11, %12, %6\n\t" M1K "%7, %11, %13, %7\n\ts_nop 7"
                     : "+v"(o[0]), "+v"(o[1]), "+v"(o[2]), "+v"(o[3]), "+v"(o[4]), "+v"(o[5]), "+v"(o[6]), "+v"(o[7])
                     : "v"(vm[0]), "v"(vm[1]), "v"(vm[2]), "v"(vm[3]), "v"((u32x2){pm, 0u}), "v"((u32x2){0u, pm}));
#pragma unroll
        for (int b = 0; b < 2; ++b) {
            u32x4 x, y;
#pragma unroll
            for (int j = 0; j < 4; ++j) { x[j] = pack2(o[4 * b + (j >> 1)][2 * (j & 1)], o[4 * b + (j >> 1)][2 * (j & 1) + 1]); y[j] = pack2(o[4 * b + 2 + (j >> 1)][2 * (j & 1)], o[4 * b + 2 + (j >> 1)][2 * (j & 1) + 1]); }
            if (s == NSEG - 1) { const u32x4 z = {x.x, x.z, y.x, y.z}; y = (u32x4){x.y, x.w, y.y, y.w}; x = z; }
            st128(x, rw, oo + b * 1024, s * 262144); st128(y, rw, oo + b * 1024 + 512, s * 262144);
        }
        TT(s == s1, 2)
        S4S(3)
        if (s < s1) {
            asm volatile("s_waitcnt vmcnt(4)" ::: "memory");
            WG_BARRIER();
            S4S(4)
            if (s + 4 <= s1) { both(s); S4S(5) continue; }
            look();
            xpart();
        }
        TT(s == s0, 4)
        if (fast) continue;
        FENCE();
        seen();
        VM_DRAIN();
        st32((unsigned)s, rw, fo, 0);
        TT(s == s1, 3)
        TT(s == s1 - 2, 4)
        WG_BARRIER();
        pre = false;
    }
}

#define STEP_ASM \
    "v_readfirstlane_b32 s36, v0\n" \
    "v_readfirstlane_b32 s37, v1\n" \
    "v_readfirstlane_b32 s38, v2\n" \
    "v_readfirstlane_b32 s39, v3\n" \
    "v_readfirstlane_b32 s40, v4\n" \
    "v_readfirstlane_b32 s41, v5\n" \
    "v_readfirstlane_b32 s42, v6\n" \
    "v_readfirstlane_b32 s43, v7\n" \
    "v_readfirstlane_b32 s28, v8\n" \
    "v_readfirstlane_b32 s29, v9\n" \
    "v_readfirstlane_b32 s30, v10\n" \
    "v_readfirstlane_b32 s31, v11\n" \
    "v_readfirstlane_b32 s32, v12\n" \
    "v_readfirstlane_b32 s33, v13\n" \
    "v_readfirstlane_b32 s34, v14\n" \
    "v_readfirstlane_b32 s35, v15\n" \
    "v_readfirstlane_b32 s24, v16\n" \
    "v_readfirstlane_b32 s25, v17\n" \
    "v_readfirstlane_b32 s26, v18\n" \
    "v_readfirstlane_b32 s27, v19\n" \
    "v_readfirstlane_b32 s60, v24\n" \
    "v_readfirstlane_b32 s61, v25\n" \
    "v_readfirstlane_b32 s62, v26\n" \
    "v_readfirstlane_b32 s63, v27\n" \
    "v_readfirstlane_b32 s64, v28\n" \
    "v_readfirstlane_b32 s65, v29\n" \
    "v_readfirstlane_b32 s66, v30\n" \
    "v_readfirstlane_b32 s67, v31\n" \
    "s_mov_b32 s68, m0\n" \
    "s_getpc_b64 s[96:97]\n" \
    "14:\n" \
    "s_add_u32 s96, s96, 2f-14b\n" \
    "s_addc_u32 s97, s97, 0\n" \
    "s_lshl_b32 s74, s60, 10\n" \
    "s_lshl_b32 s95, s74, 1\n" \
    "s_add_i32 s95, s95, 16384\n" \
    "s_mov_b32 s81, 0x3e0293ee\n" \
    "s_mov_b32 s82, 0x53800000\n" \
    "s_lshl_b32 s83, s60, 6\n" \
    "s_and_b32 s83, s83, 128\n" \
    "v_add_u32 v109, s83, v131\n" \
    "v_sub_u32 v111, v131, s83\n" \
    "v_xor_b32 v112, 0x2000, v133\n" \
    "v_and_b32 v113, 0x3c0, v127\n" \
    "v_mad_u32_u24 v113, v113, 7, v127\n" \
    "s_bfe_u32 s83, s61, 0x2000b\n" \
    "s_and_b32 s83, s83, 3\n" \
    "v_lshl_add_u32 v113, s83, 7, v113\n" \
    "v_lshlrev_b32 v114, 1, v127\n" \
    "v_add_u32 v106, s63, v133\n" \
    "s_bfe_u32 s83, s61, 0x40009\n" \
    "s_bfe_u32 s80, s61, 0x20004\n" \
    "s_lshl2_add_u32 s83, s83, s80\n" \
    "s_mov_b64 s[72:73], 0\n" \
    "0:\n" \
    "s_and_b32 s75, s36, 3\n" \
    "s_lshl_b32 s75, s75, 17\n" \
    "s_add_i32 s75, s75, s26\n" \
    "s_add_i32 s69, s75, s74\n" \
    "s_add_i32 s70, s69, 4096\n" \
    "s_add_i32 s71, s75, 16384\n" \
    "12:\n" \
    "ds_read_b128 v[72:75], v133 offset:0\n" \
    "ds_read_b128 v[76:79], v133 offset:1024\n" \
    "ds_read_b128 v[80:83], v133 offset:2048\n" \
    "ds_read_b128 v[84:87], v133 offset:3072\n" \
    "ds_read_b128 v[88:91], v133 offset:16384\n" \
    "ds_read_b128 v[92:95], v133 offset:17408\n" \
    "s_add_i32 s91, s36, 1\n" \
    "s_and_b32 s78, s36, 3\n" \
    "s_lshl_b32 s78, s78, 14\n" \
    "s_add_i32 s77, s78, s61\n" \
    "s_bitcmp1_b32 s36, 0\n" \
    "s_cselect_b32 s79, 0x90000, 0\n" \
    "s_lshl_b32 s92, s91, 18\n" \
    "s_add_i32 s92, s92, s64\n" \
    "s_movk_i32 m0, 0x2000\n" \
    "s_add_i32 s78, s78, 0x1a0000\n" \
    "s_add_i32 s93, s92, 0x1000000\n" \
    "v_mov_b64 v[40:41], 0\n" \
    "v_mov_b64 v[42:43], 0\n" \
    "v_mov_b64 v[44:45], 0\n" \
    "v_mov_b64 v[46:47], 0\n" \
    "v_mov_b64 v[48:49], 0\n" \
    "v_mov_b64 v[50:51], 0\n" \
    "v_mov_b64 v[52:53], 0\n" \
    "v_mov_b64 v[54:55], 0\n" \
    "v_mov_b64 v[56:57], 0\n" \
    "v_mov_b64 v[58:59], 0\n" \
    "v_mov_b64 v[60:61], 0\n" \
    "v_mov_b64 v[62:63], 0\n" \
    "v_mov_b64 v[64:65], 0\n" \
    "v_mov_b64 v[66:67], 0\n" \
    "v_mov_b64 v[68:69], 0\n" \
    "v_mov_b64 v[70:71], 0\n" \
    "v_add_u32 v108, s74, v127\n" \
    "v_xor_b32 v24, 0x400, v108\n" \
    "v_xor_b32 v28, 0x800, v108\n" \
    "v_xor_b32 v32, 0xc00, v108\n" \
    "s_sub_i32 s76, s33, s42\n" \
    "s_ashr_i32 s76, s76, s32\n" \
    "s_add_i32 s42, s42, s76\n" \
    "s_lshr_b32 s76, s32, 8\n" \
    "s_sub_i32 s76, s76, s40\n" \
    "s_ashr_i32 s76, s76, 2\n" \
    "s_add_i32 s40, s40, s76\n" \
    "s_sub_i32 s76, s40, s24\n" \
    "s_min_i32 s94, s91, s38\n" \
    "s_lshl_b32 s94, s94, 18\n" \
    "s_andn2_b32 s80, 0x1000000, s66\n" \
    "s_add_i32 s94, s94, s64\n" \
    "s_add_i32 s94, s94, s80\n" \
    "s_cmp_eq_u32 s36, 0\n" \
    "s_cbranch_scc1 17f\n" \
    "s_sub_i32 s90, s37, s29\n" \
    "s_mul_i32 s90, s90, s25\n" \
    "s_ashr_i32 s90, s90, 12\n" \
    "s_add_i32 s90, s90, s24\n" \
    "s_add_i32 s90, s90, -116\n" \
    "1:\n" \
    "s_memtime vcc\n" \
    "s_waitcnt lgkmcnt(0)\n" \
    "s_sub_i32 s75, s90, vcc_lo\n" \
    "s_cmp_gt_i32 s75, 112\n" \
    "s_cbranch_scc1 1b\n" \
    "s_max_i32 s75, s75, 0\n" \
    "s_and_b32 s75, s75, -4\n" \
    "s_sub_u32 vcc_lo, s96, s75\n" \
    "s_subb_u32 vcc_hi, s97, 0\n" \
    "s_setpc_b64 vcc\n" \
    "s_nop 0\n" \
    "s_nop 0\n" \
    "s_nop 0\n" \
    "s_nop 0\n" \
    "s_nop 0\n" \
    "s_nop 0\n" \
    "s_nop 0\n" \
    "s_nop 0\n" \
    "s_nop 0\n" \
    "s_nop 0\n" \
    "s_nop 0\n" \
    "s_nop 0\n" \
    "s_nop 0\n" \
    "s_nop 0\n" \
    "s_nop 0\n" \
    "s_nop 0\n" \
    "s_nop 0\n" \
    "s_nop 0\n" \
    "s_nop 0\n" \
    "s_nop 0\n" \
    "s_nop 0\n" \
    "s_nop 0\n" \
    "s_nop 0\n" \
    "s_nop 0\n" \
    "s_nop 0\n" \
    "s_nop 0\n" \
    "s_nop 0\n" \
    "s_nop 0\n" \
    "2:\n" \
    "buffer_load_dwordx4 v[0:3], v125, %[rs], s69 offen sc1\n" \
    "s_nop 3\n" \
    "s_nop 3\n" \
    "buffer_load_dwordx4 v[16:19], v125, %[rs], s70 offen sc1\n" \
    "s_mov_b32 exec_lo, s27\n" \
    "s_mov_b32 exec_hi, s27\n" \
    "buffer_load_dwordx4 v127, %[rs], s71 offen sc1 lds\n" \
    "s_mov_b64 exec, -1\n" \
    "v_bfe_u32 v20, v127, 8, 2\n" \
    "v_subrev_u32 v20, s60, v20\n" \
    "v_mul_i32_i24 v20, -1088, v20\n" \
    "v_add_u32 v20, v20, v129\n" \
    "v_lshrrev_b32 v21, 2, v127\n" \
    "s_mov_b32 exec_lo, s66\n" \
    "s_mov_b32 exec_hi, s66\n" \
    "ds_read2_b32 v[96:97], v20 offset0:0 offset1:144\n" \
    "s_mov_b64 exec, -1\n" \
    "s_lshl_b32 s80, s36, 16\n" \
    "s_add_i32 s80, s80, s67\n" \
    "s_cmp_eq_u32 s60, 0\n" \
    "s_cselect_b32 s72, 0x1010101, 0\n" \
    "s_waitcnt lgkmcnt(0)\n" \
    "buffer_store_dword v96, v21, %[rw], s80 offen sc1\n" \
    "buffer_store_dword v97, v21, %[rw], s80 offen offset:256 sc1\n" \
    "3:\n" \
    "s_mov_b32 m0, s95\n" \
    "s_add_i32 s75, s36, 2\n" \
    "s_cmp_lt_i32 s38, s75\n" \
    "s_cselect_b32 s75, s62, 0x1b0040\n" \
    "v_mov_b64 v[98:99], 0\n" \
    "v_mov_b32 v101, 0\n" \
    "s_pack_ll_b32_b16 s80, s91, s42\n" \
    "v_mov_b32 v103, s80\n" \
    "s_waitcnt vmcnt(4)\n" \
    "v_smfmac_f32_16x16x64_bf16 v[40:43], v[0:3], a[0:7], v124\n" \
    "ds_write_b128 v108, v[0:3] offset:0\n" \
    "v_smfmac_f32_16x16x64_bf16 v[44:47], v[0:3], a[8:15], v124\n" \
    "v_smfmac_f32_16x16x64_bf16 v[48:51], v[0:3], a[128:135], v124\n" \
    "v_smfmac_f32_16x16x64_bf16 v[52:55], v[0:3], a[136:143], v124\n" \
    "s_waitcnt vmcnt(3)\n" \
    "v_smfmac_f32_16x16x64_bf16 v[40:43], v[16:19], a[64:71], v124\n" \
    "ds_write_b128 v108, v[16:19] offset:4096\n" \
    "v_smfmac_f32_16x16x64_bf16 v[44:47], v[16:19], a[72:79], v124\n" \
    "s_waitcnt lgkmcnt(1)\n" \
    "s_barrier\n" \
    "v_smfmac_f32_16x16x64_bf16 v[48:51], v[16:19], a[192:199], v124\n" \
    "ds_read_b128 v[4:7], v24 offset:0\n" \
    "v_smfmac_f32_16x16x64_bf16 v[52:55], v[16:19], a[200:207], v124\n" \
    "ds_read_b128 v[8:11], v28 offset:0\n" \
    "v_smfmac_f32_16x16x64_bf16 v[56:59], v[0:3], v[72:79], v124\n" \
    "ds_read_b128 v[12:15], v32 offset:0\n" \
    "v_smfmac_f32_16x16x64_bf16 v[60:63], v[0:3], v[80:87], v124\n" \
    "s_waitcnt vmcnt(2) lgkmcnt(3)\n" \
    "s_barrier\n" \
    "s_memtime s[86:87]\n" \
    "v_smfmac_f32_16x16x64_bf16 v[56:59], v[16:19], v[168:175], v124\n" \
    "ds_read_b128 v[24:27], v24 offset:4096\n" \
    "v_smfmac_f32_16x16x64_bf16 v[60:63], v[16:19], v[176:183], v124\n" \
    "ds_read_b128 v[28:31], v28 offset:4096\n" \
    "s_waitcnt lgkmcnt(3)\n" \
    "v_smfmac_f32_16x16x64_bf16 v[40:43], v[4:7], a[16:23], v124\n" \
    "ds_read_b128 v[32:35], v32 offset:4096\n" \
    "v_smfmac_f32_16x16x64_bf16 v[40:43], v[8:11], a[32:39], v124\n" \
    "ds_read_b128 v[36:39], v126 offset:0\n" \
    "s_waitcnt lgkmcnt(4)\n" \
    "v_smfmac_f32_16x16x64_bf16 v[40:43], v[12:15], a[48:55], v124\n" \
    "buffer_load_dword v110, v114, %[rw], s94 offen sc1\n" \
    "v_smfmac_f32_16x16x64_bf16 v[44:47], v[4:7], a[24:31], v124\n" \
    "s_waitcnt lgkmcnt(2)\n" \
    "v_smfmac_f32_16x16x64_bf16 v[40:43], v[24:27], a[80:87], v124\n" \
    "v_smfmac_f32_16x16x64_bf16 v[40:43], v[28:31], a[96:103], v124\n" \
    "s_waitcnt lgkmcnt(0)\n" \
    "v_smfmac_f32_16x16x64_bf16 v[40:43], v[32:35], a[112:119], v124\n" \
    "ds_read_b128 v[72:75], v133 offset:18432\n" \
    "s_add_i32 s80, s86, s76\n" \
    "v_smfmac_f32_16x16x64_bf16 v[40:43], v[36:39], v[136:143], v124\n" \
    "ds_read_b128 v[76:79], v133 offset:19456\n" \
    "s_mul_hi_u32 s80, s80, s41\n" \
    "v_smfmac_f32_16x16x64_bf16 v[44:47], v[8:11], a[40:47], v124\n" \
    "ds_read_b128 v[80:83], v133 offset:32768\n" \
    "s_add_i32 s80, s80, s29\n" \
    "v_mov_b32 v102, s80\n" \
    "v_smfmac_f32_16x16x64_bf16 v[44:47], v[12:15], a[56:63], v124\n" \
    "ds_read_b128 v[84:87], v133 offset:33792\n" \
    "v_add_f32 v40, v40, v42\n" \
    "v_add_f32 v41, v41, v43\n" \
    "v_smfmac_f32_16x16x64_bf16 v[44:47], v[24:27], a[88:95], v124\n" \
    "ds_read_b128 v[20:23], v127 offset:19456\n" \
    "v_cmp_u_f32 vcc, v40, v41\n" \
    "v_smfmac_f32_16x16x64_bf16 v[44:47], v[28:31], a[104:111], v124\n" \
    "v_mul_f32 v40, s81, v40\n" \
    "v_smfmac_f32_16x16x64_bf16 v[44:47], v[32:35], a[120:127], v124\n" \
    "s_cbranch_vccnz 5f\n" \
    "6:\n" \
    "s_mov_b64 exec, s[72:73]\n" \
    "v_smfmac_f32_16x16x64_bf16 v[44:47], v[36:39], v[144:151], v124\n" \
    "buffer_store_dwordx2 v[102:103], v127, %[rs], s77 offen sc1\n" \
    "v_smfmac_f32_16x16x64_bf16 v[56:59], v[4:7], v[88:95], v124\n" \
    "s_mov_b64 exec, -1\n" \
    "s_waitcnt lgkmcnt(1)\n" \
    "v_mul_f32 v41, s81, v41\n" \
    "v_smfmac_f32_16x16x64_bf16 v[60:63], v[4:7], v[72:79], v124\n" \
    "ds_read_b128 v[88:91], v133 offset:34816\n" \
    "v_add_f32 v44, v44, v46\n" \
    "v_smfmac_f32_16x16x64_bf16 v[56:59], v[8:11], v[80:87], v124\n" \
    "ds_read_b128 v[92:95], v133 offset:35840\n" \
    "v_add_f32 v45, v45, v47\n" \
    "v_smfmac_f32_16x16x64_bf16 v[48:51], v[4:7], a[144:151], v124\n" \
    "ds_read_b128 v[72:75], v133 offset:49152\n" \
    "v_mul_f32 v44, s81, v44\n" \
    "v_smfmac_f32_16x16x64_bf16 v[52:55], v[4:7], a[152:159], v124\n" \
    "ds_read_b128 v[76:79], v133 offset:50176\n" \
    "v_mul_f32 v45, s81, v45\n" \
    "v_smfmac_f32_16x16x64_bf16 v[48:51], v[8:11], a[160:167], v124\n" \
    "ds_read_b128 v[0:3], v127 offset:16384\n" \
    "v_cvt_pk_bf16_f32 v107, v40, v44\n" \
    "v_smfmac_f32_16x16x64_bf16 v[52:55], v[8:11], a[168:175], v124\n" \
    "ds_read_b128 v[4:7], v127 offset:18432\n" \
    "s_waitcnt lgkmcnt(4)\n" \
    "v_cvt_pk_bf16_f32 v108, v41, v45\n" \
    "v_smfmac_f32_16x16x64_bf16 v[60:63], v[8:11], v[88:95], v124\n" \
    "ds_write2_b32 v128, v107, v108 offset0:0 offset1:80\n" \
    "v_smfmac_f32_16x16x64_bf16 v[60:63], v[12:15], v[248:255], v124\n" \
    "ds_read_b128 v[16:19], v127 offset:17408\n" \
    "s_waitcnt lgkmcnt(4)\n" \
    "v_smfmac_f32_16x16x64_bf16 v[56:59], v[12:15], v[72:79], v124\n" \
    "ds_read_b128 v[8:11], v127 offset:20480\n" \
    "v_smfmac_f32_16x16x64_bf16 v[48:51], v[12:15], a[176:183], v124\n" \
    "ds_read_b128 v[40:43], v127 offset:21504\n" \
    "v_smfmac_f32_16x16x64_bf16 v[52:55], v[12:15], a[184:191], v124\n" \
    "ds_read_b128 v[44:47], v127 offset:23552\n" \
    "v_smfmac_f32_16x16x64_bf16 v[48:51], v[24:27], a[208:215], v124\n" \
    "s_waitcnt lgkmcnt(4)\n" \
    "s_barrier\n" \
    "v_smfmac_f32_16x16x64_bf16 v[52:55], v[24:27], a[216:223], v124\n" \
    "ds_read_b128 v[72:75], v130 offset:0\n" \
    "v_smfmac_f32_16x16x64_bf16 v[48:51], v[28:31], a[224:231], v124\n" \
    "ds_read_b128 v[76:79], v130 offset:64\n" \
    "v_smfmac_f32_16x16x64_bf16 v[48:51], v[32:35], a[240:247], v124\n" \
    "ds_read_b128 v[80:83], v130 offset:128\n" \
    "v_smfmac_f32_16x16x64_bf16 v[48:51], v[36:39], v[152:159], v124\n" \
    "ds_read_b128 v[84:87], v130 offset:192\n" \
    "v_smfmac_f32_16x16x64_bf16 v[52:55], v[28:31], a[232:239], v124\n" \
    "ds_read_b128 v[12:15], v127 offset:22528\n" \
    "v_smfmac_f32_16x16x64_bf16 v[52:55], v[32:35], a[248:255], v124\n" \
    "v_add_f32 v48, v48, v50\n" \
    "v_smfmac_f32_16x16x64_bf16 v[52:55], v[36:39], v[160:167], v124\n" \
    "s_waitcnt lgkmcnt(3)\n" \
    "v_add_f32 v49, v49, v51\n" \
    "v_mfma_f32_16x16x32_bf16 v[88:91], v[0:3], v[72:75], 0\n" \
    "ds_read_b64 v[0:1], v132 offset:0\n" \
    "ds_read_b64 v[2:3], v132 offset:1024\n" \
    "v_mfma_f32_16x16x32_bf16 v[88:91], v[4:7], v[76:79], v[88:91]\n" \
    "s_waitcnt lgkmcnt(2)\n" \
    "v_add_f32 v52, v52, v54\n" \
    "v_add_f32 v53, v53, v55\n" \
    "v_mfma_f32_16x16x32_bf16 v[88:91], v[8:11], v[80:83], v[88:91]\n" \
    "v_cvt_pk_bf16_f32 v107, v48, v52\n" \
    "v_cvt_pk_bf16_f32 v108, v49, v53\n" \
    "v_mfma_f32_16x16x32_bf16 v[88:91], v[12:15], v[84:87], v[88:91]\n" \
    "ds_write2_b32 v129, v107, v108 offset0:0 offset1:144\n" \
    "v_mfma_f32_16x16x32_bf16 v[92:95], v[16:19], v[72:75], 0\n" \
    "ds_read_b64 v[4:5], v132 offset:512\n" \
    "ds_read_b64 v[6:7], v132 offset:1536\n" \
    "v_mfma_f32_16x16x32_bf16 v[92:95], v[20:23], v[76:79], v[92:95]\n" \
    "ds_read_b128 v[8:11], v106 offset:0\n" \
    "v_exp_f32 v88, v88\n" \
    "v_mfma_f32_16x16x32_bf16 v[92:95], v[40:43], v[80:83], v[92:95]\n" \
    "ds_read_b128 v[12:15], v106 offset:1024\n" \
    "v_exp_f32 v89, v89\n" \
    "v_mfma_f32_16x16x32_bf16 v[92:95], v[44:47], v[84:87], v[92:95]\n" \
    "s_waitcnt lgkmcnt(4)\n" \
    "s_barrier\n" \
    "v_smfmac_f32_16x16x64_bf16 v[56:59], v[24:27], v[184:191], v124\n" \
    "ds_read_b128 v[48:51], v131 offset:0\n" \
    "ds_read_b128 v[52:55], v131 offset:128\n" \
    "v_cvt_pk_bf16_f32 v88, v88, v89\n" \
    "v_smfmac_f32_16x16x64_bf16 v[56:59], v[28:31], v[200:207], v124\n" \
    "ds_read_b128 v[16:19], v106 offset:32768\n" \
    "v_exp_f32 v90, v90\n" \
    "v_smfmac_f32_16x16x64_bf16 v[56:59], v[32:35], v[216:223], v124\n" \
    "ds_read_b128 v[20:23], v106 offset:33792\n" \
    "v_exp_f32 v91, v91\n" \
    "v_smfmac_f32_16x16x64_bf16 v[56:59], v[36:39], v[232:239], v124\n" \
    "ds_read_b128 v[40:43], v106 offset:2048\n" \
    "s_waitcnt lgkmcnt(3)\n" \
    "v_exp_f32 v92, v92\n" \
    "v_cvt_pk_bf16_f32 v89, v90, v91\n" \
    "v_smfmac_f32_16x16x64_bf16 v[64:67], v[48:51], v[72:79], v124\n" \
    "ds_read_b128 v[44:47], v106 offset:3072\n" \
    "v_exp_f32 v93, v93\n" \
    "v_smfmac_f32_16x16x64_bf16 v[64:67], v[52:55], v[80:87], v124\n" \
    "ds_read_b128 v[72:75], v106 offset:34816\n" \
    "v_exp_f32 v94, v94\n" \
    "v_cvt_pk_bf16_f32 v90, v92, v93\n" \
    "v_smfmac_f32_16x16x64_bf16 v[60:63], v[24:27], v[192:199], v124\n" \
    "ds_read_b128 v[76:79], v106 offset:35840\n" \
    "v_exp_f32 v95, v95\n" \
    "v_smfmac_f32_16x16x64_bf16 v[60:63], v[28:31], v[208:215], v124\n" \
    "ds_read_b128 v[80:83], v106 offset:4096\n" \
    "s_cmp_eq_u32 s36, 63\n" \
    "v_add_f32 v64, v64, v66\n" \
    "v_add_f32 v65, v65, v67\n" \
    "v_smfmac_f32_16x16x64_bf16 v[60:63], v[32:35], v[224:231], v124\n" \
    "ds_read_b128 v[84:87], v106 offset:5120\n" \
    "v_exp_f32 v64, v64\n" \
    "v_cvt_pk_bf16_f32 v91, v94, v95\n" \
    "v_smfmac_f32_16x16x64_bf16 v[60:63], v[36:39], v[240:247], v124\n" \
    "ds_read_b128 v[48:51], v106 offset:36864\n" \
    "v_exp_f32 v65, v65\n" \
    "v_swap_b32 v1, v2\n" \
    "v_swap_b32 v5, v6\n" \
    "v_mfma_f32_16x16x32_bf16 v[102:105], v[88:91], v[120:123], 0\n" \
    "ds_read_b128 v[52:55], v106 offset:37888\n" \
    "v_cvt_pk_bf16_f32 v100, v64, v65\n" \
    "v_add_f32 v56, v56, v58\n" \
    "v_add_f32 v57, v57, v59\n" \
    "v_smfmac_f32_16x16x64_bf16 v[68:71], v[88:91], v[0:7], v124\n" \
    "ds_read_b128 v[24:27], v106 offset:6144\n" \
    "v_add_f32 v60, v60, v62\n" \
    "v_add_f32 v61, v61, v63\n" \
    "v_mfma_f32_16x16x16_bf16 v[102:105], v[100:101], v[120:121], v[102:105]\n" \
    "ds_read_b128 v[28:31], v106 offset:7168\n" \
    "v_cvt_pk_bf16_f32 v96, v56, v57\n" \
    "v_cvt_pk_bf16_f32 v97, v60, v61\n" \
    "s_cbranch_scc1 16f\n" \
    "s_nop 0\n" \
    "v_smfmac_f32_16x16x32_bf16 v[68:71], v[100:101], v[96:99], v124\n" \
    "ds_read_b128 v[32:35], v106 offset:38912\n" \
    "v_mov_b64 v[56:57], 0\n" \
    "v_mov_b64 v[58:59], 0\n" \
    "v_rcp_f32 v104, v102\n" \
    "v_rcp_f32 v105, v103\n" \
    "v_mul_f32 v68, v68, v104\n" \
    "v_mul_f32 v70, v70, v104\n" \
    "v_mul_f32 v69, v69, v105\n" \
    "v_mul_f32 v71, v71, v105\n" \
    "v_cvt_pk_bf16_f32 v107, v68, v70\n" \
    "v_cvt_pk_bf16_f32 v108, v69, v71\n" \
    "ds_write2_b32 v129, v107, v108 offset0:64 offset1:208\n" \
    "v_max3_f32 v102, v102, v103, v104\n" \
    "v_max_f32 v102, v102, v105\n" \
    "v_cmp_nge_f32 vcc, s82, v102\n" \
    "v_mov_b64 v[60:61], 0\n" \
    "v_mov_b64 v[62:63], 0\n" \
    "v_mov_b64 v[88:89], 0\n" \
    "v_mov_b64 v[90:91], 0\n" \
    "v_mov_b64 v[92:93], 0\n" \
    "v_mov_b64 v[94:95], 0\n" \
    "s_waitcnt vmcnt(2) lgkmcnt(0)\n" \
    "s_barrier\n" \
    "ds_read_b128 v[68:71], v109 offset:256\n" \
    "ds_read_b128 v[102:105], v111 offset:384\n" \
    "s_cbranch_vccnz 7f\n" \
    "buffer_load_dwordx2 v[98:99], v113, %[rs], s78 offen sc1\n" \
    "buffer_load_dword v100, off, %[rw], s75 sc1\n" \
    "v_mov_b64 v[64:65], 0\n" \
    "v_mov_b64 v[66:67], 0\n" \
    "s_waitcnt lgkmcnt(1)\n" \
    "v_smfmac_f32_16x16x64_bf16 v[56:59], v[68:71], v[8:15], v124\n" \
    "s_waitcnt lgkmcnt(0)\n" \
    "ds_read_b128 v[36:39], v106 offset:39936\n" \
    "v_smfmac_f32_16x16x64_bf16 v[56:59], v[102:105], v[16:23], v124\n" \
    "ds_read_b128 v[0:3], v133 offset:51200\n" \
    "v_smfmac_f32_16x16x64_bf16 v[60:63], v[68:71], v[40:47], v124\n" \
    "ds_read_b128 v[4:7], v133 offset:52224\n" \
    "v_smfmac_f32_16x16x64_bf16 v[60:63], v[102:105], v[72:79], v124\n" \
    "ds_read_b128 v[16:19], v112 offset:51200\n" \
    "v_add_f32 v56, v56, v58\n" \
    "v_smfmac_f32_16x16x64_bf16 v[88:91], v[68:71], v[80:87], v124\n" \
    "ds_read_b128 v[20:23], v112 offset:52224\n" \
    "v_add_f32 v57, v57, v59\n" \
    "v_smfmac_f32_16x16x64_bf16 v[88:91], v[102:105], v[48:55], v124\n" \
    "v_add_f32 v60, v60, v62\n" \
    "v_smfmac_f32_16x16x64_bf16 v[92:95], v[68:71], v[24:31], v124\n" \
    "s_waitcnt lgkmcnt(4)\n" \
    "v_add_f32 v61, v61, v63\n" \
    "v_smfmac_f32_16x16x64_bf16 v[92:95], v[102:105], v[32:39], v124\n" \
    "s_waitcnt lgkmcnt(2)\n" \
    "v_add_f32 v88, v88, v90\n" \
    "v_smfmac_f32_16x16x64_bf16 v[64:67], v[68:71], v[0:7], v124\n" \
    "s_waitcnt lgkmcnt(0)\n" \
    "v_add_f32 v89, v89, v91\n" \
    "v_smfmac_f32_16x16x64_bf16 v[64:67], v[102:105], v[16:23], v124\n" \
    "v_add_f32 v92, v92, v94\n" \
    "v_add_f32 v93, v93, v95\n" \
    "v_cvt_pk_bf16_f32 v8, v56, v60\n" \
    "v_cvt_pk_bf16_f32 v10, v57, v61\n" \
    "v_cvt_pk_bf16_f32 v9, v88, v92\n" \
    "v_cvt_pk_bf16_f32 v11, v89, v93\n" \
    "buffer_store_dwordx4 v[8:11], v134, %[rs], s79 offen sc1\n" \
    "v_add_f32 v64, v64, v66\n" \
    "v_add_f32 v65, v65, v67\n" \
    "v_cvt_pk_bf16_f32 v64, v64, v65\n" \
    "buffer_store_dword v64, v135, %[rs], s79 offen sc1\n" \
    "s_memtime s[88:89]\n" \
    "s_memrealtime s[44:45]\n" \
    "s_and_b32 s75, s91, 3\n" \
    "s_lshl_b32 s75, s75, 17\n" \
    "s_memrealtime s[46:47]\n" \
    "s_add_i32 s75, s75, s26\n" \
    "s_add_i32 s69, s75, s74\n" \
    "s_memrealtime s[48:49]\n" \
    "s_add_i32 s70, s69, 4096\n" \
    "s_add_i32 s71, s75, 16384\n" \
    "s_memrealtime s[50:51]\n" \
    "s_nop 1\n" \
    "s_memrealtime s[52:53]\n" \
    "s_nop 1\n" \
    "s_memrealtime s[54:55]\n" \
    "s_nop 1\n" \
    "s_memrealtime s[56:57]\n" \
    "s_nop 1\n" \
    "s_memrealtime vcc\n" \
    "v_and_b32 v18, 0x300, v127\n" \
    "v_add_u32 v18, v18, v127\n" \
    "v_and_b32 v19, 0x380, v127\n" \
    "v_add_u32 v19, v19, v127\n" \
    "s_waitcnt lgkmcnt(0)\n" \
    "s_add_i32 s44, s44, s46\n" \
    "s_add_i32 s48, s48, s50\n" \
    "s_add_i32 s52, s52, s54\n" \
    "s_add_i32 s44, s44, s48\n" \
    "s_add_i32 s52, s52, s56\n" \
    "s_add_i32 s44, s44, s52\n" \
    "s_add_i32 s80, s44, vcc_lo\n" \
    "s_sub_i32 s76, s80, s29\n" \
    "s_mov_b32 s29, s80\n" \
    "s_sub_i32 s80, s88, s24\n" \
    "s_mov_b32 s24, s88\n" \
    "s_mul_i32 s75, s76, s25\n" \
    "s_lshr_b32 s75, s75, 12\n" \
    "s_sub_i32 s75, s80, s75\n" \
    "s_mul_hi_u32 s80, s80, s41\n" \
    "s_sub_i32 s80, s76, s80\n" \
    "s_lshl_b32 s80, s80, 18\n" \
    "s_cmp_lt_u32 s76, 1480\n" \
    "s_cselect_b32 s75, s75, 0\n" \
    "s_cselect_b32 s80, s80, 0\n" \
    "s_add_i32 s25, s25, s75\n" \
    "s_add_i32 s41, s41, s80\n" \
    "s_waitcnt vmcnt(2)\n" \
    "v_readfirstlane_b32 s80, v100\n" \
    "v_cmp_ne_u16 vcc, s91, v99\n" \
    "s_mov_b32 s43, 0\n" \
    "s_max_i32 s38, s38, s80\n" \
    "s_cmp_lt_i32 s38, s91\n" \
    "s_cselect_b64 exec, 0, -1\n" \
    "s_cselect_b32 s94, 8, 0\n" \
    "buffer_load_dwordx4 v19, %[rw], s92 offen sc1 lds\n" \
    "s_add_i32 m0, s95, 896\n" \
    "s_nop 0\n" \
    "buffer_load_dwordx4 v19, %[rw], s92 offen offset:128 sc1 lds\n" \
    "s_add_i32 m0, s95, 8192\n" \
    "s_nop 0\n" \
    "buffer_load_dwordx4 v18, %[rw], s93 offen sc1 lds\n" \
    "s_add_i32 m0, s95, 8960\n" \
    "s_nop 0\n" \
    "buffer_load_dwordx4 v18, %[rw], s93 offen offset:256 sc1 lds\n" \
    "s_mov_b64 exec, -1\n" \
    "s_cbranch_vccnz 15f\n" \
    "10:\n" \
    "v_subrev_u32 v28, s29, v98\n" \
    "v_min_i32 v28, 0x4000, v28\n" \
    "v_max_i32 v28, 0xffffc000, v28\n" \
    "s_bitcmp1_b32 s30, 15\n" \
    "s_cbranch_scc1 18f\n" \
    "19:\n" \
    "v_readlane_b32 s75, v28, s83\n" \
    "v_max_i32_dpp v28, v28, v28 row_shr:1 row_mask:0xf bank_mask:0xf\n" \
    "s_nop 1\n" \
    "v_max_i32_dpp v28, v28, v28 row_shr:2 row_mask:0xf bank_mask:0xf\n" \
    "s_nop 1\n" \
    "v_max_i32_dpp v28, v28, v28 row_shr:4 row_mask:0xf bank_mask:0xf\n" \
    "s_nop 1\n" \
    "v_max_i32_dpp v28, v28, v28 row_shr:8 row_mask:0xf bank_mask:0xf\n" \
    "s_add_i32 s76, s29, s42\n" \
    "s_nop 0\n" \
    "v_max_i32_dpp v28, v28, v28 row_bcast:15 row_mask:0xa bank_mask:0xf\n" \
    "s_nop 1\n" \
    "v_max_i32_dpp v28, v28, v28 row_bcast:31 row_mask:0xc bank_mask:0xf\n" \
    "s_nop 0\n" \
    "v_readlane_b32 s37, v28, 63\n" \
    "s_sub_i32 s75, s37, s75\n" \
    "s_cmp_gt_i32 s75, 24\n" \
    "s_cselect_b32 s80, 2, 0\n" \
    "s_cmp_lt_i32 s75, 10\n" \
    "s_cselect_b32 s80, -6, s80\n" \
    "s_add_i32 s28, s28, s80\n" \
    "s_max_i32 s28, s28, 0\n" \
    "s_min_i32 s28, s28, 20\n" \
    "s_add_i32 s76, s76, s28\n" \
    "v_add_u32 v30, -1, s36\n" \
    "s_add_i32 s37, s37, s76\n" \
    "v_mov_b32 v31, s37\n" \
    "s_mov_b64 exec, s[72:73]\n" \
    "buffer_store_dwordx2 v[30:31], off, %[rw], s65 sc1\n" \
    "s_mov_b64 exec, -1\n" \
    "s_cmp_lg_u32 s94, 0\n" \
    "s_cbranch_scc1 9f\n" \
    "s_mov_b32 s36, s91\n" \
    "s_branch 12b\n" \
    "5:\n" \
    "v_mov_b32 v107, 0\n" \
    "v_max3_u32 v107, v107, v0, v1\n" \
    "v_max3_u32 v107, v107, v2, v3\n" \
    "v_max3_u32 v107, v107, v4, v5\n" \
    "v_max3_u32 v107, v107, v6, v7\n" \
    "v_max3_u32 v107, v107, v8, v9\n" \
    "v_max3_u32 v107, v107, v10, v11\n" \
    "v_max3_u32 v107, v107, v12, v13\n" \
    "v_max3_u32 v107, v107, v14, v15\n" \
    "v_max3_u32 v107, v107, v16, v17\n" \
    "v_max3_u32 v107, v107, v18, v19\n" \
    "v_max3_u32 v107, v107, v24, v25\n" \
    "v_max3_u32 v107, v107, v26, v27\n" \
    "v_max3_u32 v107, v107, v28, v29\n" \
    "v_max3_u32 v107, v107, v30, v31\n" \
    "v_max3_u32 v107, v107, v32, v33\n" \
    "v_max3_u32 v107, v107, v34, v35\n" \
    "v_max3_u32 v107, v107, v36, v37\n" \
    "v_max3_u32 v107, v107, v38, v39\n" \
    "v_cmp_eq_u32 vcc, -1, v107\n" \
    "s_nop 4\n" \
    "s_cbranch_vccz 6b\n" \
    "s_cmp_eq_u32 s36, 0\n" \
    "s_cbranch_scc1 6b\n" \
    "s_add_i32 s39, s39, -1\n" \
    "s_cmp_lt_i32 s39, 0\n" \
    "s_cbranch_scc1 6b\n" \
    "s_add_i32 s30, s30, 1\n" \
    "s_bitset1_b32 s30, 15\n" \
    "s_and_b32 s80, s31, 0xff\n" \
    "s_add_i32 s42, s42, s80\n" \
    "s_bfe_u32 s80, s31, 0x80008\n" \
    "s_add_i32 s40, s40, s80\n" \
    "s_waitcnt lgkmcnt(0)\n" \
    "s_mov_b32 s37, s29\n" \
    "s_nop 7\n" \
    "s_barrier\n" \
    "s_branch 0b\n" \
    "15:\n" \
    "s_add_i32 s30, s30, 0x10000\n" \
    "s_add_i32 s39, s39, -1\n" \
    "s_cmp_lt_i32 s39, 0\n" \
    "s_cbranch_scc1 10b\n" \
    "s_sleep 9\n" \
    "buffer_load_dwordx2 v[98:99], v113, %[rs], s78 offen sc1\n" \
    "s_waitcnt vmcnt(0)\n" \
    "v_cmp_ne_u16 vcc, s91, v99\n" \
    "s_nop 1\n" \
    "s_cbranch_vccnz 15b\n" \
    "s_memtime s[88:89]\n" \
    "s_memrealtime s[44:45]\n" \
    "s_nop 1\n" \
    "s_memrealtime s[46:47]\n" \
    "s_nop 1\n" \
    "s_memrealtime s[48:49]\n" \
    "s_nop 1\n" \
    "s_memrealtime s[50:51]\n" \
    "s_nop 1\n" \
    "s_memrealtime s[52:53]\n" \
    "s_nop 1\n" \
    "s_memrealtime s[54:55]\n" \
    "s_nop 1\n" \
    "s_memrealtime s[56:57]\n" \
    "s_nop 1\n" \
    "s_memrealtime vcc\n" \
    "s_waitcnt lgkmcnt(0)\n" \
    "s_add_i32 s44, s44, s46\n" \
    "s_add_i32 s48, s48, s50\n" \
    "s_add_i32 s52, s52, s54\n" \
    "s_add_i32 s44, s44, s48\n" \
    "s_add_i32 s52, s52, s56\n" \
    "s_add_i32 s44, s44, s52\n" \
    "s_add_i32 s29, s44, vcc_lo\n" \
    "s_mov_b32 s24, s88\n" \
    "s_branch 10b\n" \
    "18:\n" \
    "v_cmp_gt_i32 vcc, -320, v28\n" \
    "s_bitset0_b32 s30, 15\n" \
    "s_lshr_b32 s80, s31, 16\n" \
    "s_cmp_lt_u32 s36, 4\n" \
    "s_cselect_b32 s80, 0, s80\n" \
    "s_min_u32 s76, s80, 1\n" \
    "s_lshl_b32 s76, s76, 16\n" \
    "s_sub_i32 s31, s31, s76\n" \
    "s_mul_i32 s76, s80, 768\n" \
    "s_cmp_eq_u64 vcc, 0\n" \
    "s_cselect_b32 s80, 0, s80\n" \
    "s_cselect_b32 s76, s76, 0\n" \
    "s_add_i32 s33, s33, s80\n" \
    "s_add_i32 s32, s32, s76\n" \
    "s_branch 19b\n" \
    "16:\n" \
    "s_waitcnt lgkmcnt(0)\n" \
    "v_bfe_u32 v20, v127, 8, 2\n" \
    "v_subrev_u32 v20, s60, v20\n" \
    "v_mul_i32_i24 v20, -1088, v20\n" \
    "v_add_u32 v20, v20, v129\n" \
    "v_lshrrev_b32 v21, 2, v127\n" \
    "s_mov_b32 exec_lo, s66\n" \
    "s_mov_b32 exec_hi, s66\n" \
    "ds_read2_b32 v[96:97], v20 offset0:0 offset1:144\n" \
    "s_mov_b64 exec, -1\n" \
    "s_lshl_b32 s80, s91, 16\n" \
    "s_add_i32 s80, s80, s67\n" \
    "s_waitcnt lgkmcnt(0)\n" \
    "buffer_store_dword v96, v21, %[rw], s80 offen sc1\n" \
    "buffer_store_dword v97, v21, %[rw], s80 offen offset:256 sc1\n" \
    "s_branch 9f\n" \
    "17:\n" \
    "ds_read_b128 v[0:3], v108 offset:0\n" \
    "ds_read_b128 v[16:19], v108 offset:4096\n" \
    "s_waitcnt lgkmcnt(0)\n" \
    "s_branch 3b\n" \
    "7:\n" \
    "s_mov_b32 s43, 2\n" \
    "s_nop 7\n" \
    "s_waitcnt vmcnt(0)\n" \
    "9:\n" \
    "s_mov_b32 m0, s68\n" \
    "s_waitcnt lgkmcnt(0)\n" \
    "v_mov_b32 v0, s36\n" \
    "v_mov_b32 v1, s37\n" \
    "v_mov_b32 v2, s38\n" \
    "v_mov_b32 v3, s39\n" \
    "v_mov_b32 v4, s40\n" \
    "v_mov_b32 v5, s41\n" \
    "v_mov_b32 v6, s42\n" \
    "v_mov_b32 v7, s43\n" \
    "v_mov_b32 v8, s28\n" \
    "v_mov_b32 v9, s29\n" \
    "v_mov_b32 v10, s30\n" \
    "v_mov_b32 v11, s31\n" \
    "v_mov_b32 v12, s32\n" \
    "v_mov_b32 v13, s33\n" \
    "v_mov_b32 v14, s34\n" \
    "v_mov_b32 v15, s35\n" \
    "v_mov_b32 v16, s24\n" \
    "v_mov_b32 v17, s25\n" \
    "v_mov_b32 v18, s26\n" \
    "v_mov_b32 v19, s27\n" \
    "v_mov_b32 v20, s84\n" \
    "v_mov_b32 v21, s86\n" \
    "v_mov_b32 v22, s88\n" \
    "v_mov_b32 v23, s84\n" \
    "v_mov_b32 v24, s60\n" \
    "v_mov_b32 v25, s61\n" \
    "v_mov_b32 v26, s62\n" \
    "v_mov_b32 v27, s63\n" \
    "v_mov_b32 v28, s64\n" \
    "v_mov_b32 v29, s65\n" \
    "v_mov_b32 v30, s66\n" \
    "v_mov_b32 v31, s67\n" \
    ".Lstep_end:\n" \
    ""
static_assert(2 * PART_BYTES == 589824 && 2 * OFF_CAN == 1703936 && F_ERR == 2 * OFF_CAN + 65536 && OFF_PART == 0 && OFF_VF - OFF_KF == 2048 * 4096 * 2, "constants of the step block");
#define STEP_CLOBBER "v32", "v33", "v34", "v35", "v36", "v37", "v38", "v39", "v40", "v41", "v42", "v43", "v44", "v45", "v46", "v47", "v48", "v49", "v50", "v51", "v52", "v53", "v54", "v55", "v56", "v57", "v58", "v59", "v60", "v61", "v62", "v63", "v64", "v65", "v66", "v67", "v68", "v69", "v70", "v71", "v72", "v73", "v74", "v75", "v76", "v77", "v78", "v79", "v80", "v81", "v82", "v83", "v84", "v85", "v86", "v87", "v88", "v89", "v90", "v91", "v92", "v93", "v94", "v95", "v98", "v99", "v100", "v101", "v102", "v103", "v104", "v105", "v106", "v107", "v108", "v109", "v110", "v111", "v112", "v113", "v114", "s24", "s25", "s26", "s27", "s28", "s29", "s30", "s31", "s32", "s33", "s34", "s35", "s36", "s37", "s38", "s39", "s40", "s41", "s42", "s43", "s44", "s45", "s46", "s47", "s48", "s49", "s50", "s51", "s52", "s53", "s54", "s55", "s56", "s57", "s58", "s59", "s60", "s61", "s62", "s63", "s64", "s65", "s66", "s67", "s68", "s69", "s70", "s71", "s72", "s73", "s74", "s75", "s76", "s77", "s78", "s79", "s80", "s81", "s82", "s83", "s84", "s85", "s86", "s87", "s88", "s89", "s90", "s91", "s92", "s93", "s94", "s95", "s96", "s97", "vcc", "scc", "memory"
#define OUT16_ASM \
    "s_mov_b32 exec_lo, %[tm]\n" \
    "s_mov_b32 exec_hi, 0\n" \
    "buffer_load_dword v79, %[tv], %[rw], %[so] offen sc1\n" \
    "s_mov_b64 exec, -1\n" \
    "s_add_i32 s44, %[so], 4096\n" \
    "s_add_i32 s45, %[so], 8192\n" \
    "s_add_i32 s46, %[so], 12288\n" \
    "buffer_load_dwordx4 v[16:19], %[vo], %[rw], %[so] offen offset:0 sc1\n" \
    "ds_read_b128 v[80:83], %[wl] offset:0\n" \
    "ds_read_b128 v[84:87], %[wl] offset:1024\n" \
    "s_nop 7\n" \
    "v_mov_b32 v104, 0\n" \
    "buffer_load_dwordx4 v[20:23], %[vo], %[rw], %[so] offen offset:256 sc1\n" \
    "s_nop 7\n" \
    "s_nop 3\n" \
    "buffer_load_dwordx4 v[24:27], %[vo], %[rw], %[so] offen offset:1024 sc1\n" \
    "s_nop 7\n" \
    "s_nop 3\n" \
    "buffer_load_dwordx4 v[28:31], %[vo], %[rw], %[so] offen offset:1280 sc1\n" \
    "s_nop 7\n" \
    "s_nop 3\n" \
    "buffer_load_dwordx4 v[32:35], %[vo], %[rw], %[so] offen offset:2048 sc1\n" \
    "s_nop 7\n" \
    "s_nop 3\n" \
    "buffer_load_dwordx4 v[36:39], %[vo], %[rw], %[so] offen offset:2304 sc1\n" \
    "s_nop 7\n" \
    "s_nop 3\n" \
    "buffer_load_dwordx4 v[40:43], %[vo], %[rw], %[so] offen offset:3072 sc1\n" \
    "s_nop 7\n" \
    "s_nop 3\n" \
    "buffer_load_dwordx4 v[44:47], %[vo], %[rw], %[so] offen offset:3328 sc1\n" \
    "s_nop 7\n" \
    "s_nop 3\n" \
    "buffer_load_dwordx4 v[48:51], %[vo], %[rw], s44 offen offset:0 sc1\n" \
    "s_nop 7\n" \
    "s_nop 3\n" \
    "buffer_load_dwordx4 v[52:55], %[vo], %[rw], s44 offen offset:256 sc1\n" \
    "s_nop 7\n" \
    "s_nop 3\n" \
    "buffer_load_dwordx4 v[56:59], %[vo], %[rw], s44 offen offset:1024 sc1\n" \
    "s_nop 7\n" \
    "s_nop 3\n" \
    "buffer_load_dwordx4 v[60:63], %[vo], %[rw], s44 offen offset:1280 sc1\n" \
    "ds_read_b128 v[88:91], %[wl] offset:2048\n" \
    "s_waitcnt vmcnt(11)\n" \
    "v_mfma_f32_16x16x32_bf16 v[0:3], a[0:3], v[16:19], 0\n" \
    "v_max3_u32 v104, v104, v16, v17\n" \
    "v_mfma_f32_16x16x32_bf16 v[4:7], a[128:131], v[16:19], 0\n" \
    "v_max3_u32 v104, v104, v18, v19\n" \
    "v_mfma_f32_16x16x32_bf16 v[8:11], v[128:131], v[16:19], 0\n" \
    "s_waitcnt lgkmcnt(2)\n" \
    "v_mfma_f32_16x16x32_bf16 v[12:15], v[80:83], v[16:19], 0\n" \
    "buffer_load_dwordx4 v[64:67], %[vo], %[rw], s44 offen offset:2048 sc1\n" \
    "ds_read_b128 v[80:83], %[wl] offset:3072\n" \
    "s_waitcnt vmcnt(11)\n" \
    "v_mfma_f32_16x16x32_bf16 v[0:3], a[4:7], v[20:23], v[0:3]\n" \
    "v_max3_u32 v104, v104, v20, v21\n" \
    "v_mfma_f32_16x16x32_bf16 v[4:7], a[132:135], v[20:23], v[4:7]\n" \
    "v_max3_u32 v104, v104, v22, v23\n" \
    "v_mfma_f32_16x16x32_bf16 v[8:11], v[132:135], v[20:23], v[8:11]\n" \
    "s_waitcnt lgkmcnt(2)\n" \
    "v_mfma_f32_16x16x32_bf16 v[12:15], v[84:87], v[20:23], v[12:15]\n" \
    "buffer_load_dwordx4 v[68:71], %[vo], %[rw], s44 offen offset:2304 sc1\n" \
    "ds_read_b128 v[84:87], %[wl] offset:4096\n" \
    "s_waitcnt vmcnt(11)\n" \
    "v_mfma_f32_16x16x32_bf16 v[0:3], a[8:11], v[24:27], v[0:3]\n" \
    "v_max3_u32 v104, v104, v24, v25\n" \
    "v_mfma_f32_16x16x32_bf16 v[4:7], a[136:139], v[24:27], v[4:7]\n" \
    "v_max3_u32 v104, v104, v26, v27\n" \
    "v_mfma_f32_16x16x32_bf16 v[8:11], v[136:139], v[24:27], v[8:11]\n" \
    "s_waitcnt lgkmcnt(2)\n" \
    "v_mfma_f32_16x16x32_bf16 v[12:15], v[88:91], v[24:27], v[12:15]\n" \
    "buffer_load_dwordx4 v[72:75], %[vo], %[rw], s44 offen offset:3072 sc1\n" \
    "ds_read_b128 v[88:91], %[wl] offset:5120\n" \
    "s_waitcnt vmcnt(11)\n" \
    "v_mfma_f32_16x16x32_bf16 v[0:3], a[12:15], v[28:31], v[0:3]\n" \
    "v_max3_u32 v104, v104, v28, v29\n" \
    "v_mfma_f32_16x16x32_bf16 v[4:7], a[140:143], v[28:31], v[4:7]\n" \
    "v_max3_u32 v104, v104, v30, v31\n" \
    "v_mfma_f32_16x16x32_bf16 v[8:11], v[140:143], v[28:31], v[8:11]\n" \
    "s_waitcnt lgkmcnt(2)\n" \
    "v_mfma_f32_16x16x32_bf16 v[12:15], v[80:83], v[28:31], v[12:15]\n" \
    "buffer_load_dwordx4 v[76:79], %[vo], %[rw], s44 offen offset:3328 sc1\n" \
    "ds_read_b128 v[80:83], %[wl] offset:6144\n" \
    "s_waitcnt vmcnt(11)\n" \
    "v_mfma_f32_16x16x32_bf16 v[0:3], a[16:19], v[32:35], v[0:3]\n" \
    "v_max3_u32 v104, v104, v32, v33\n" \
    "v_mfma_f32_16x16x32_bf16 v[4:7], a[144:147], v[32:35], v[4:7]\n" \
    "v_max3_u32 v104, v104, v34, v35\n" \
    "v_mfma_f32_16x16x32_bf16 v[8:11], v[144:147], v[32:35], v[8:11]\n" \
    "s_waitcnt lgkmcnt(2)\n" \
    "v_mfma_f32_16x16x32_bf16 v[12:15], v[84:87], v[32:35], v[12:15]\n" \
    "buffer_load_dwordx4 v[16:19], %[vo], %[rw], s45 offen offset:0 sc1\n" \
    "ds_read_b128 v[84:87], %[wl] offset:7168\n" \
    "s_waitcnt vmcnt(11)\n" \
    "v_mfma_f32_16x16x32_bf16 v[0:3], a[20:23], v[36:39], v[0:3]\n" \
    "v_max3_u32 v104, v104, v36, v37\n" \
    "v_mfma_f32_16x16x32_bf16 v[4:7], a[148:151], v[36:39], v[4:7]\n" \
    "v_max3_u32 v104, v104, v38, v39\n" \
    "v_mfma_f32_16x16x32_bf16 v[8:11], v[148:151], v[36:39], v[8:11]\n" \
    "s_waitcnt lgkmcnt(2)\n" \
    "v_mfma_f32_16x16x32_bf16 v[12:15], v[88:91], v[36:39], v[12:15]\n" \
    "buffer_load_dwordx4 v[20:23], %[vo], %[rw], s45 offen offset:256 sc1\n" \
    "ds_read_b128 v[88:91], %[wl] offset:8192\n" \
    "s_waitcnt vmcnt(11)\n" \
    "v_mfma_f32_16x16x32_bf16 v[0:3], a[24:27], v[40:43], v[0:3]\n" \
    "v_max3_u32 v104, v104, v40, v41\n" \
    "v_mfma_f32_16x16x32_bf16 v[4:7], a[152:155], v[40:43], v[4:7]\n" \
    "v_max3_u32 v104, v104, v42, v43\n" \
    "v_mfma_f32_16x16x32_bf16 v[8:11], v[152:155], v[40:43], v[8:11]\n" \
    "s_waitcnt lgkmcnt(2)\n" \
    "v_mfma_f32_16x16x32_bf16 v[12:15], v[80:83], v[40:43], v[12:15]\n" \
    "buffer_load_dwordx4 v[24:27], %[vo], %[rw], s45 offen offset:1024 sc1\n" \
    "ds_read_b128 v[80:83], %[wl] offset:9216\n" \
    "s_waitcnt vmcnt(11)\n" \
    "v_mfma_f32_16x16x32_bf16 v[0:3], a[28:31], v[44:47], v[0:3]\n" \
    "v_max3_u32 v104, v104, v44, v45\n" \
    "v_mfma_f32_16x16x32_bf16 v[4:7], a[156:159], v[44:47], v[4:7]\n" \
    "v_max3_u32 v104, v104, v46, v47\n" \
    "v_mfma_f32_16x16x32_bf16 v[8:11], v[156:159], v[44:47], v[8:11]\n" \
    "s_waitcnt lgkmcnt(2)\n" \
    "v_mfma_f32_16x16x32_bf16 v[12:15], v[84:87], v[44:47], v[12:15]\n" \
    "buffer_load_dwordx4 v[28:31], %[vo], %[rw], s45 offen offset:1280 sc1\n" \
    "ds_read_b128 v[84:87], %[wl] offset:10240\n" \
    "s_waitcnt vmcnt(11)\n" \
    "v_mfma_f32_16x16x32_bf16 v[0:3], a[32:35], v[48:51], v[0:3]\n" \
    "v_max3_u32 v104, v104, v48, v49\n" \
    "v_mfma_f32_16x16x32_bf16 v[4:7], a[160:163], v[48:51], v[4:7]\n" \
    "v_max3_u32 v104, v104, v50, v51\n" \
    "v_mfma_f32_16x16x32_bf16 v[8:11], v[160:163], v[48:51], v[8:11]\n" \
    "s_waitcnt lgkmcnt(2)\n" \
    "v_mfma_f32_16x16x32_bf16 v[12:15], v[88:91], v[48:51], v[12:15]\n" \
    "buffer_load_dwordx4 v[32:35], %[vo], %[rw], s45 offen offset:2048 sc1\n" \
    "ds_read_b128 v[88:91], %[wl] offset:11264\n" \
    "s_waitcnt vmcnt(11)\n" \
    "v_mfma_f32_16x16x32_bf16 v[0:3], a[36:39], v[52:55], v[0:3]\n" \
    "v_max3_u32 v104, v104, v52, v53\n" \
    "v_mfma_f32_16x16x32_bf16 v[4:7], a[164:167], v[52:55], v[4:7]\n" \
    "v_max3_u32 v104, v104, v54, v55\n" \
    "v_mfma_f32_16x16x32_bf16 v[8:11], v[164:167], v[52:55], v[8:11]\n" \
    "s_waitcnt lgkmcnt(2)\n" \
    "v_mfma_f32_16x16x32_bf16 v[12:15], v[80:83], v[52:55], v[12:15]\n" \
    "buffer_load_dwordx4 v[36:39], %[vo], %[rw], s45 offen offset:2304 sc1\n" \
    "ds_read_b128 v[80:83], %[wl] offset:12288\n" \
    "s_waitcnt vmcnt(11)\n" \
    "v_mfma_f32_16x16x32_bf16 v[0:3], a[40:43], v[56:59], v[0:3]\n" \
    "v_max3_u32 v104, v104, v56, v57\n" \
    "v_mfma_f32_16x16x32_bf16 v[4:7], a[168:171], v[56:59], v[4:7]\n" \
    "v_max3_u32 v104, v104, v58, v59\n" \
    "v_mfma_f32_16x16x32_bf16 v[8:11], v[168:171], v[56:59], v[8:11]\n" \
    "s_waitcnt lgkmcnt(2)\n" \
    "v_mfma_f32_16x16x32_bf16 v[12:15], v[84:87], v[56:59], v[12:15]\n" \
    "buffer_load_dwordx4 v[40:43], %[vo], %[rw], s45 offen offset:3072 sc1\n" \
    "ds_read_b128 v[84:87], %[wl] offset:13312\n" \
    "s_waitcnt vmcnt(11)\n" \
    "v_mfma_f32_16x16x32_bf16 v[0:3], a[44:47], v[60:63], v[0:3]\n" \
    "v_max3_u32 v104, v104, v60, v61\n" \
    "v_mfma_f32_16x16x32_bf16 v[4:7], a[172:175], v[60:63], v[4:7]\n" \
    "v_max3_u32 v104, v104, v62, v63\n" \
    "v_mfma_f32_16x16x32_bf16 v[8:11], v[172:175], v[60:63], v[8:11]\n" \
    "s_waitcnt lgkmcnt(2)\n" \
    "v_mfma_f32_16x16x32_bf16 v[12:15], v[88:91], v[60:63], v[12:15]\n" \
    "buffer_load_dwordx4 v[44:47], %[vo], %[rw], s45 offen offset:3328 sc1\n" \
    "ds_read_b128 v[88:91], %[wl] offset:14336\n" \
    "s_waitcnt vmcnt(11)\n" \
    "v_mfma_f32_16x16x32_bf16 v[0:3], a[48:51], v[64:67], v[0:3]\n" \
    "v_max3_u32 v104, v104, v64, v65\n" \
    "v_mfma_f32_16x16x32_bf16 v[4:7], a[176:179], v[64:67], v[4:7]\n" \
    "v_max3_u32 v104, v104, v66, v67\n" \
    "v_mfma_f32_16x16x32_bf16 v[8:11], v[176:179], v[64:67], v[8:11]\n" \
    "s_waitcnt lgkmcnt(2)\n" \
    "v_mfma_f32_16x16x32_bf16 v[12:15], v[80:83], v[64:67], v[12:15]\n" \
    "buffer_load_dwordx4 v[48:51], %[vo], %[rw], s46 offen offset:0 sc1\n" \
    "ds_read_b128 v[80:83], %[wl] offset:15360\n" \
    "s_waitcnt vmcnt(11)\n" \
    "v_mfma_f32_16x16x32_bf16 v[0:3], a[52:55], v[68:71], v[0:3]\n" \
    "v_max3_u32 v104, v104, v68, v69\n" \
    "v_mfma_f32_16x16x32_bf16 v[4:7], a[180:183], v[68:71], v[4:7]\n" \
    "v_max3_u32 v104, v104, v70, v71\n" \
    "v_mfma_f32_16x16x32_bf16 v[8:11], v[180:183], v[68:71], v[8:11]\n" \
    "s_waitcnt lgkmcnt(2)\n" \
    "v_mfma_f32_16x16x32_bf16 v[12:15], v[84:87], v[68:71], v[12:15]\n" \
    "buffer_load_dwordx4 v[52:55], %[vo], %[rw], s46 offen offset:256 sc1\n" \
    "ds_read_b128 v[84:87], %[wl] offset:16384\n" \
    "s_waitcnt vmcnt(11)\n" \
    "v_mfma_f32_16x16x32_bf16 v[0:3], a[56:59], v[72:75], v[0:3]\n" \
    "v_max3_u32 v104, v104, v72, v73\n" \
    "v_mfma_f32_16x16x32_bf16 v[4:7], a[184:187], v[72:75], v[4:7]\n" \
    "v_max3_u32 v104, v104, v74, v75\n" \
    "v_mfma_f32_16x16x32_bf16 v[8:11], v[184:187], v[72:75], v[8:11]\n" \
    "s_waitcnt lgkmcnt(2)\n" \
    "v_mfma_f32_16x16x32_bf16 v[12:15], v[88:91], v[72:75], v[12:15]\n" \
    "buffer_load_dwordx4 v[56:59], %[vo], %[rw], s46 offen offset:1024 sc1\n" \
    "ds_read_b128 v[88:91], %[wl] offset:17408\n" \
    "s_waitcnt vmcnt(11)\n" \
    "v_mfma_f32_16x16x32_bf16 v[0:3], a[60:63], v[76:79], v[0:3]\n" \
    "v_max3_u32 v104, v104, v76, v77\n" \
    "v_mfma_f32_16x16x32_bf16 v[4:7], a[188:191], v[76:79], v[4:7]\n" \
    "v_max3_u32 v104, v104, v78, v79\n" \
    "v_mfma_f32_16x16x32_bf16 v[8:11], v[188:191], v[76:79], v[8:11]\n" \
    "s_waitcnt lgkmcnt(2)\n" \
    "v_mfma_f32_16x16x32_bf16 v[12:15], v[80:83], v[76:79], v[12:15]\n" \
    "buffer_load_dwordx4 v[60:63], %[vo], %[rw], s46 offen offset:1280 sc1\n" \
    "ds_read_b128 v[80:83], %[wl] offset:18432\n" \
    "s_waitcnt vmcnt(11)\n" \
    "v_mfma_f32_16x16x32_bf16 v[0:3], a[64:67], v[16:19], v[0:3]\n" \
    "v_max3_u32 v104, v104, v16, v17\n" \
    "v_mfma_f32_16x16x32_bf16 v[4:7], a[192:195], v[16:19], v[4:7]\n" \
    "v_max3_u32 v104, v104, v18, v19\n" \
    "v_mfma_f32_16x16x32_bf16 v[8:11], v[192:195], v[16:19], v[8:11]\n" \
    "s_waitcnt lgkmcnt(2)\n" \
    "v_mfma_f32_16x16x32_bf16 v[12:15], v[84:87], v[16:19], v[12:15]\n" \
    "buffer_load_dwordx4 v[64:67], %[vo], %[rw], s46 offen offset:2048 sc1\n" \
    "ds_read_b128 v[84:87], %[wl] offset:19456\n" \
    "s_waitcnt vmcnt(11)\n" \
    "v_mfma_f32_16x16x32_bf16 v[0:3], a[68:71], v[20:23], v[0:3]\n" \
    "v_max3_u32 v104, v104, v20, v21\n" \
    "v_mfma_f32_16x16x32_bf16 v[4:7], a[196:199], v[20:23], v[4:7]\n" \
    "v_max3_u32 v104, v104, v22, v23\n" \
    "v_mfma_f32_16x16x32_bf16 v[8:11], v[196:199], v[20:23], v[8:11]\n" \
    "s_waitcnt lgkmcnt(2)\n" \
    "v_mfma_f32_16x16x32_bf16 v[12:15], v[88:91], v[20:23], v[12:15]\n" \
    "buffer_load_dwordx4 v[68:71], %[vo], %[rw], s46 offen offset:2304 sc1\n" \
    "ds_read_b128 v[88:91], %[wl] offset:20480\n" \
    "s_waitcnt vmcnt(11)\n" \
    "v_mfma_f32_16x16x32_bf16 v[0:3], a[72:75], v[24:27], v[0:3]\n" \
    "v_max3_u32 v104, v104, v24, v25\n" \
    "v_mfma_f32_16x16x32_bf16 v[4:7], a[200:203], v[24:27], v[4:7]\n" \
    "v_max3_u32 v104, v104, v26, v27\n" \
    "v_mfma_f32_16x16x32_bf16 v[8:11], v[200:203], v[24:27], v[8:11]\n" \
    "s_waitcnt lgkmcnt(2)\n" \
    "v_mfma_f32_16x16x32_bf16 v[12:15], v[80:83], v[24:27], v[12:15]\n" \
    "buffer_load_dwordx4 v[72:75], %[vo], %[rw], s46 offen offset:3072 sc1\n" \
    "ds_read_b128 v[80:83], %[wl] offset:21504\n" \
    "s_waitcnt vmcnt(11)\n" \
    "v_mfma_f32_16x16x32_bf16 v[0:3], a[76:79], v[28:31], v[0:3]\n" \
    "v_max3_u32 v104, v104, v28, v29\n" \
    "v_mfma_f32_16x16x32_bf16 v[4:7], a[204:207], v[28:31], v[4:7]\n" \
    "v_max3_u32 v104, v104, v30, v31\n" \
    "v_mfma_f32_16x16x32_bf16 v[8:11], v[204:207], v[28:31], v[8:11]\n" \
    "s_waitcnt lgkmcnt(2)\n" \
    "v_mfma_f32_16x16x32_bf16 v[12:15], v[84:87], v[28:31], v[12:15]\n" \
    "buffer_load_dwordx4 v[76:79], %[vo], %[rw], s46 offen offset:3328 sc1\n" \
    "ds_read_b128 v[84:87], %[wl] offset:22528\n" \
    "s_waitcnt vmcnt(11)\n" \
    "v_mfma_f32_16x16x32_bf16 v[0:3], a[80:83], v[32:35], v[0:3]\n" \
    "v_max3_u32 v104, v104, v32, v33\n" \
    "v_mfma_f32_16x16x32_bf16 v[4:7], a[208:211], v[32:35], v[4:7]\n" \
    "v_max3_u32 v104, v104, v34, v35\n" \
    "v_mfma_f32_16x16x32_bf16 v[8:11], v[208:211], v[32:35], v[8:11]\n" \
    "s_waitcnt lgkmcnt(2)\n" \
    "v_mfma_f32_16x16x32_bf16 v[12:15], v[88:91], v[32:35], v[12:15]\n" \
    "buffer_load_dwordx2 v[106:107], %[bv], %[rw], %[bs] offen sc1\n" \
    "ds_read_b128 v[88:91], %[wl] offset:23552\n" \
    "s_waitcnt vmcnt(11)\n" \
    "v_mfma_f32_16x16x32_bf16 v[0:3], a[84:87], v[36:39], v[0:3]\n" \
    "v_max3_u32 v104, v104, v36, v37\n" \
    "v_mfma_f32_16x16x32_bf16 v[4:7], a[212:215], v[36:39], v[4:7]\n" \
    "v_max3_u32 v104, v104, v38, v39\n" \
    "v_mfma_f32_16x16x32_bf16 v[8:11], v[212:215], v[36:39], v[8:11]\n" \
    "s_waitcnt lgkmcnt(2)\n" \
    "v_mfma_f32_16x16x32_bf16 v[12:15], v[80:83], v[36:39], v[12:15]\n" \
    "buffer_load_dword v108, %[gv], %[rw], %[gs] offen sc1\n" \
    "ds_read_b128 v[80:83], %[wl] offset:24576\n" \
    "s_waitcnt vmcnt(11)\n" \
    "v_mfma_f32_16x16x32_bf16 v[0:3], a[88:91], v[40:43], v[0:3]\n" \
    "v_max3_u32 v104, v104, v40, v41\n" \
    "v_mfma_f32_16x16x32_bf16 v[4:7], a[216:219], v[40:43], v[4:7]\n" \
    "v_max3_u32 v104, v104, v42, v43\n" \
    "v_mfma_f32_16x16x32_bf16 v[8:11], v[216:219], v[40:43], v[8:11]\n" \
    "s_waitcnt lgkmcnt(2)\n" \
    "v_mfma_f32_16x16x32_bf16 v[12:15], v[84:87], v[40:43], v[12:15]\n" \
    "buffer_load_dword v105, %[sv], %[rw], %[ss] offen sc1\n" \
    "ds_read_b128 v[84:87], %[wl] offset:25600\n" \
    "s_waitcnt vmcnt(11)\n" \
    "v_mfma_f32_16x16x32_bf16 v[0:3], a[92:95], v[44:47], v[0:3]\n" \
    "v_max3_u32 v104, v104, v44, v45\n" \
    "v_mfma_f32_16x16x32_bf16 v[4:7], a[220:223], v[44:47], v[4:7]\n" \
    "v_max3_u32 v104, v104, v46, v47\n" \
    "v_mfma_f32_16x16x32_bf16 v[8:11], v[220:223], v[44:47], v[8:11]\n" \
    "s_waitcnt lgkmcnt(2)\n" \
    "v_mfma_f32_16x16x32_bf16 v[12:15], v[88:91], v[44:47], v[12:15]\n" \
    "ds_read_b128 v[88:91], %[wl] offset:26624\n" \
    "s_waitcnt vmcnt(10)\n" \
    "v_mfma_f32_16x16x32_bf16 v[0:3], a[96:99], v[48:51], v[0:3]\n" \
    "v_max3_u32 v104, v104, v48, v49\n" \
    "v_mfma_f32_16x16x32_bf16 v[4:7], a[224:227], v[48:51], v[4:7]\n" \
    "v_max3_u32 v104, v104, v50, v51\n" \
    "v_mfma_f32_16x16x32_bf16 v[8:11], v[224:227], v[48:51], v[8:11]\n" \
    "s_waitcnt lgkmcnt(2)\n" \
    "v_mfma_f32_16x16x32_bf16 v[12:15], v[80:83], v[48:51], v[12:15]\n" \
    "ds_read_b128 v[80:83], %[wl] offset:27648\n" \
    "s_waitcnt vmcnt(9)\n" \
    "v_mfma_f32_16x16x32_bf16 v[0:3], a[100:103], v[52:55], v[0:3]\n" \
    "v_max3_u32 v104, v104, v52, v53\n" \
    "v_mfma_f32_16x16x32_bf16 v[4:7], a[228:231], v[52:55], v[4:7]\n" \
    "v_max3_u32 v104, v104, v54, v55\n" \
    "v_mfma_f32_16x16x32_bf16 v[8:11], v[228:231], v[52:55], v[8:11]\n" \
    "s_waitcnt lgkmcnt(2)\n" \
    "v_mfma_f32_16x16x32_bf16 v[12:15], v[84:87], v[52:55], v[12:15]\n" \
    "ds_read_b128 v[84:87], %[wl] offset:28672\n" \
    "s_waitcnt vmcnt(8)\n" \
    "v_mfma_f32_16x16x32_bf16 v[0:3], a[104:107], v[56:59], v[0:3]\n" \
    "v_max3_u32 v104, v104, v56, v57\n" \
    "v_mfma_f32_16x16x32_bf16 v[4:7], a[232:235], v[56:59], v[4:7]\n" \
    "v_max3_u32 v104, v104, v58, v59\n" \
    "v_mfma_f32_16x16x32_bf16 v[8:11], v[232:235], v[56:59], v[8:11]\n" \
    "s_waitcnt lgkmcnt(2)\n" \
    "v_mfma_f32_16x16x32_bf16 v[12:15], v[88:91], v[56:59], v[12:15]\n" \
    "ds_read_b128 v[88:91], %[wl] offset:29696\n" \
    "s_waitcnt vmcnt(7)\n" \
    "v_mfma_f32_16x16x32_bf16 v[0:3], a[108:111], v[60:63], v[0:3]\n" \
    "v_max3_u32 v104, v104, v60, v61\n" \
    "v_mfma_f32_16x16x32_bf16 v[4:7], a[236:239], v[60:63], v[4:7]\n" \
    "v_max3_u32 v104, v104, v62, v63\n" \
    "v_mfma_f32_16x16x32_bf16 v[8:11], v[236:239], v[60:63], v[8:11]\n" \
    "s_waitcnt lgkmcnt(2)\n" \
    "v_mfma_f32_16x16x32_bf16 v[12:15], v[80:83], v[60:63], v[12:15]\n" \
    "ds_read_b128 v[80:83], %[wl] offset:30720\n" \
    "s_waitcnt vmcnt(6)\n" \
    "v_mfma_f32_16x16x32_bf16 v[0:3], a[112:115], v[64:67], v[0:3]\n" \
    "v_max3_u32 v104, v104, v64, v65\n" \
    "v_mfma_f32_16x16x32_bf16 v[4:7], a[240:243], v[64:67], v[4:7]\n" \
    "v_max3_u32 v104, v104, v66, v67\n" \
    "v_mfma_f32_16x16x32_bf16 v[8:11], v[240:243], v[64:67], v[8:11]\n" \
    "s_waitcnt lgkmcnt(2)\n" \
    "v_mfma_f32_16x16x32_bf16 v[12:15], v[84:87], v[64:67], v[12:15]\n" \
    "ds_read_b128 v[84:87], %[wl] offset:31744\n" \
    "s_waitcnt vmcnt(5)\n" \
    "v_mfma_f32_16x16x32_bf16 v[0:3], a[116:119], v[68:71], v[0:3]\n" \
    "v_max3_u32 v104, v104, v68, v69\n" \
    "v_mfma_f32_16x16x32_bf16 v[4:7], a[244:247], v[68:71], v[4:7]\n" \
    "v_max3_u32 v104, v104, v70, v71\n" \
    "v_mfma_f32_16x16x32_bf16 v[8:11], v[244:247], v[68:71], v[8:11]\n" \
    "s_waitcnt lgkmcnt(2)\n" \
    "v_mfma_f32_16x16x32_bf16 v[12:15], v[88:91], v[68:71], v[12:15]\n" \
    "s_waitcnt vmcnt(4)\n" \
    "v_mfma_f32_16x16x32_bf16 v[0:3], a[120:123], v[72:75], v[0:3]\n" \
    "v_max3_u32 v104, v104, v72, v73\n" \
    "v_mfma_f32_16x16x32_bf16 v[4:7], a[248:251], v[72:75], v[4:7]\n" \
    "v_max3_u32 v104, v104, v74, v75\n" \
    "v_mfma_f32_16x16x32_bf16 v[8:11], v[248:251], v[72:75], v[8:11]\n" \
    "s_waitcnt lgkmcnt(1)\n" \
    "v_mfma_f32_16x16x32_bf16 v[12:15], v[80:83], v[72:75], v[12:15]\n" \
    "s_waitcnt vmcnt(3)\n" \
    "v_mfma_f32_16x16x32_bf16 v[0:3], a[124:127], v[76:79], v[0:3]\n" \
    "v_max3_u32 v104, v104, v76, v77\n" \
    "v_mfma_f32_16x16x32_bf16 v[4:7], a[252:255], v[76:79], v[4:7]\n" \
    "v_max3_u32 v104, v104, v78, v79\n" \
    "v_mfma_f32_16x16x32_bf16 v[8:11], v[252:255], v[76:79], v[8:11]\n" \
    "s_waitcnt lgkmcnt(0)\n" \
    "v_mfma_f32_16x16x32_bf16 v[12:15], v[84:87], v[76:79], v[12:15]\n" \
    "s_waitcnt vmcnt(0)\n" \
    "s_nop 7\n" \
    ""
#define OUT8_ASM \
    "s_mov_b32 exec_lo, %[tm]\n" \
    "s_mov_b32 exec_hi, 0\n" \
    "buffer_load_dword v79, %[tv], %[rw], %[so] offen sc1\n" \
    "s_mov_b64 exec, -1\n" \
    "s_add_i32 s44, %[so], 4096\n" \
    "s_add_i32 s45, %[so], 8192\n" \
    "s_add_i32 s46, %[so], 12288\n" \
    "buffer_load_dwordx4 v[16:19], %[vo], %[rw], %[so] offen offset:0 sc1\n" \
    "ds_read_b128 v[80:83], %[wl] offset:0\n" \
    "ds_read_b128 v[84:87], %[wl] offset:1024\n" \
    "ds_read_b128 v[88:91], %[wl] offset:2048\n" \
    "ds_read_b128 v[92:95], %[wl] offset:3072\n" \
    "v_mov_b64 v[0:1], 0\n" \
    "v_mov_b64 v[2:3], 0\n" \
    "v_mov_b64 v[4:5], 0\n" \
    "v_mov_b64 v[6:7], 0\n" \
    "v_mov_b64 v[8:9], 0\n" \
    "v_mov_b64 v[10:11], 0\n" \
    "v_mov_b64 v[12:13], 0\n" \
    "v_mov_b64 v[14:15], 0\n" \
    "v_mov_b32 v104, 0\n" \
    "buffer_load_dwordx4 v[20:23], %[vo], %[rw], %[so] offen offset:1024 sc1\n" \
    "s_nop 7\n" \
    "s_nop 3\n" \
    "buffer_load_dwordx4 v[24:27], %[vo], %[rw], %[so] offen offset:2048 sc1\n" \
    "s_nop 7\n" \
    "s_nop 3\n" \
    "buffer_load_dwordx4 v[28:31], %[vo], %[rw], %[so] offen offset:3072 sc1\n" \
    "s_nop 7\n" \
    "s_nop 3\n" \
    "buffer_load_dwordx4 v[32:35], %[vo], %[rw], s44 offen offset:0 sc1\n" \
    "s_nop 7\n" \
    "s_nop 3\n" \
    "buffer_load_dwordx4 v[36:39], %[vo], %[rw], s44 offen offset:1024 sc1\n" \
    "s_nop 7\n" \
    "s_nop 3\n" \
    "buffer_load_dwordx4 v[40:43], %[vo], %[rw], s44 offen offset:2048 sc1\n" \
    "s_nop 7\n" \
    "s_nop 3\n" \
    "buffer_load_dwordx4 v[44:47], %[vo], %[rw], s44 offen offset:3072 sc1\n" \
    "s_nop 7\n" \
    "s_nop 3\n" \
    "buffer_load_dwordx4 v[48:51], %[vo], %[rw], s45 offen offset:0 sc1\n" \
    "s_nop 7\n" \
    "s_nop 3\n" \
    "buffer_load_dwordx4 v[52:55], %[vo], %[rw], s45 offen offset:1024 sc1\n" \
    "ds_read_b128 v[96:99], %[wl] offset:4096\n" \
    "ds_read_b128 v[100:103], %[wl] offset:5120\n" \
    "s_waitcnt vmcnt(9)\n" \
    "v_smfmac_f32_16x16x64_bf16 v[0:3], v[16:19], a[0:7], %[idx]\n" \
    "v_max3_u32 v104, v104, v16, v17\n" \
    "v_smfmac_f32_16x16x64_bf16 v[4:7], v[16:19], a[128:135], %[idx]\n" \
    "v_max3_u32 v104, v104, v18, v19\n" \
    "v_smfmac_f32_16x16x64_bf16 v[8:11], v[16:19], v[128:135], %[idx]\n" \
    "s_waitcnt lgkmcnt(4)\n" \
    "v_smfmac_f32_16x16x64_bf16 v[12:15], v[16:19], v[80:87], %[idx]\n" \
    "buffer_load_dwordx4 v[56:59], %[vo], %[rw], s45 offen offset:2048 sc1\n" \
    "ds_read_b128 v[80:83], %[wl] offset:6144\n" \
    "ds_read_b128 v[84:87], %[wl] offset:7168\n" \
    "s_waitcnt vmcnt(9)\n" \
    "v_smfmac_f32_16x16x64_bf16 v[0:3], v[20:23], a[8:15], %[idx]\n" \
    "v_max3_u32 v104, v104, v20, v21\n" \
    "v_smfmac_f32_16x16x64_bf16 v[4:7], v[20:23], a[136:143], %[idx]\n" \
    "v_max3_u32 v104, v104, v22, v23\n" \
    "v_smfmac_f32_16x16x64_bf16 v[8:11], v[20:23], v[136:143], %[idx]\n" \
    "s_waitcnt lgkmcnt(4)\n" \
    "v_smfmac_f32_16x16x64_bf16 v[12:15], v[20:23], v[88:95], %[idx]\n" \
    "buffer_load_dwordx4 v[60:63], %[vo], %[rw], s45 offen offset:3072 sc1\n" \
    "ds_read_b128 v[88:91], %[wl] offset:8192\n" \
    "ds_read_b128 v[92:95], %[wl] offset:9216\n" \
    "s_waitcnt vmcnt(9)\n" \
    "v_smfmac_f32_16x16x64_bf16 v[0:3], v[24:27], a[16:23], %[idx]\n" \
    "v_max3_u32 v104, v104, v24, v25\n" \
    "v_smfmac_f32_16x16x64_bf16 v[4:7], v[24:27], a[144:151], %[idx]\n" \
    "v_max3_u32 v104, v104, v26, v27\n" \
    "v_smfmac_f32_16x16x64_bf16 v[8:11], v[24:27], v[144:151], %[idx]\n" \
    "s_waitcnt lgkmcnt(4)\n" \
    "v_smfmac_f32_16x16x64_bf16 v[12:15], v[24:27], v[96:103], %[idx]\n" \
    "buffer_load_dwordx4 v[64:67], %[vo], %[rw], s46 offen offset:0 sc1\n" \
    "ds_read_b128 v[96:99], %[wl] offset:10240\n" \
    "ds_read_b128 v[100:103], %[wl] offset:11264\n" \
    "s_waitcnt vmcnt(9)\n" \
    "v_smfmac_f32_16x16x64_bf16 v[0:3], v[28:31], a[24:31], %[idx]\n" \
    "v_max3_u32 v104, v104, v28, v29\n" \
    "v_smfmac_f32_16x16x64_bf16 v[4:7], v[28:31], a[152:159], %[idx]\n" \
    "v_max3_u32 v104, v104, v30, v31\n" \
    "v_smfmac_f32_16x16x64_bf16 v[8:11], v[28:31], v[152:159], %[idx]\n" \
    "s_waitcnt lgkmcnt(4)\n" \
    "v_smfmac_f32_16x16x64_bf16 v[12:15], v[28:31], v[80:87], %[idx]\n" \
    "buffer_load_dwordx4 v[68:71], %[vo], %[rw], s46 offen offset:1024 sc1\n" \
    "ds_read_b128 v[80:83], %[wl] offset:12288\n" \
    "ds_read_b128 v[84:87], %[wl] offset:13312\n" \
    "s_waitcnt vmcnt(9)\n" \
    "v_smfmac_f32_16x16x64_bf16 v[0:3], v[32:35], a[32:39], %[idx]\n" \
    "v_max3_u32 v104, v104, v32, v33\n" \
    "v_smfmac_f32_16x16x64_bf16 v[4:7], v[32:35], a[160:167], %[idx]\n" \
    "v_max3_u32 v104, v104, v34, v35\n" \
    "v_smfmac_f32_16x16x64_bf16 v[8:11], v[32:35], v[160:167], %[idx]\n" \
    "s_waitcnt lgkmcnt(4)\n" \
    "v_smfmac_f32_16x16x64_bf16 v[12:15], v[32:35], v[88:95], %[idx]\n" \
    "buffer_load_dwordx4 v[72:75], %[vo], %[rw], s46 offen offset:2048 sc1\n" \
    "ds_read_b128 v[88:91], %[wl] offset:14336\n" \
    "ds_read_b128 v[92:95], %[wl] offset:15360\n" \
    "s_waitcnt vmcnt(9)\n" \
    "v_smfmac_f32_16x16x64_bf16 v[0:3], v[36:39], a[40:47], %[idx]\n" \
    "v_max3_u32 v104, v104, v36, v37\n" \
    "v_smfmac_f32_16x16x64_bf16 v[4:7], v[36:39], a[168:175], %[idx]\n" \
    "v_max3_u32 v104, v104, v38, v39\n" \
    "v_smfmac_f32_16x16x64_bf16 v[8:11], v[36:39], v[168:175], %[idx]\n" \
    "s_waitcnt lgkmcnt(4)\n" \
    "v_smfmac_f32_16x16x64_bf16 v[12:15], v[36:39], v[96:103], %[idx]\n" \
    "buffer_load_dwordx4 v[76:79], %[vo], %[rw], s46 offen offset:3072 sc1\n" \
    "ds_read_b128 v[96:99], %[wl] offset:16384\n" \
    "ds_read_b128 v[100:103], %[wl] offset:17408\n" \
    "s_waitcnt vmcnt(9)\n" \
    "v_smfmac_f32_16x16x64_bf16 v[0:3], v[40:43], a[48:55], %[idx]\n" \
    "v_max3_u32 v104, v104, v40, v41\n" \
    "v_smfmac_f32_16x16x64_bf16 v[4:7], v[40:43], a[176:183], %[idx]\n" \
    "v_max3_u32 v104, v104, v42, v43\n" \
    "v_smfmac_f32_16x16x64_bf16 v[8:11], v[40:43], v[176:183], %[idx]\n" \
    "s_waitcnt lgkmcnt(4)\n" \
    "v_smfmac_f32_16x16x64_bf16 v[12:15], v[40:43], v[80:87], %[idx]\n" \
    "buffer_load_dwordx2 v[106:107], %[bv], %[rw], %[bs] offen sc1\n" \
    "ds_read_b128 v[80:83], %[wl] offset:18432\n" \
    "ds_read_b128 v[84:87], %[wl] offset:19456\n" \
    "s_waitcnt vmcnt(9)\n" \
    "v_smfmac_f32_16x16x64_bf16 v[0:3], v[44:47], a[56:63], %[idx]\n" \
    "v_max3_u32 v104, v104, v44, v45\n" \
    "v_smfmac_f32_16x16x64_bf16 v[4:7], v[44:47], a[184:191], %[idx]\n" \
    "v_max3_u32 v104, v104, v46, v47\n" \
    "v_smfmac_f32_16x16x64_bf16 v[8:11], v[44:47], v[184:191], %[idx]\n" \
    "s_waitcnt lgkmcnt(4)\n" \
    "v_smfmac_f32_16x16x64_bf16 v[12:15], v[44:47], v[88:95], %[idx]\n" \
    "buffer_load_dword v108, %[gv], %[rw], %[gs] offen sc1\n" \
    "ds_read_b128 v[88:91], %[wl] offset:20480\n" \
    "ds_read_b128 v[92:95], %[wl] offset:21504\n" \
    "s_waitcnt vmcnt(9)\n" \
    "v_smfmac_f32_16x16x64_bf16 v[0:3], v[48:51], a[64:71], %[idx]\n" \
    "v_max3_u32 v104, v104, v48, v49\n" \
    "v_smfmac_f32_16x16x64_bf16 v[4:7], v[48:51], a[192:199], %[idx]\n" \
    "v_max3_u32 v104, v104, v50, v51\n" \
    "v_smfmac_f32_16x16x64_bf16 v[8:11], v[48:51], v[192:199], %[idx]\n" \
    "s_waitcnt lgkmcnt(4)\n" \
    "v_smfmac_f32_16x16x64_bf16 v[12:15], v[48:51], v[96:103], %[idx]\n" \
    "buffer_load_dword v105, %[sv], %[rw], %[ss] offen sc1\n" \
    "ds_read_b128 v[96:99], %[wl] offset:22528\n" \
    "ds_read_b128 v[100:103], %[wl] offset:23552\n" \
    "s_waitcnt vmcnt(9)\n" \
    "v_smfmac_f32_16x16x64_bf16 v[0:3], v[52:55], a[72:79], %[idx]\n" \
    "v_max3_u32 v104, v104, v52, v53\n" \
    "v_smfmac_f32_16x16x64_bf16 v[4:7], v[52:55], a[200:207], %[idx]\n" \
    "v_max3_u32 v104, v104, v54, v55\n" \
    "v_smfmac_f32_16x16x64_bf16 v[8:11], v[52:55], v[200:207], %[idx]\n" \
    "s_waitcnt lgkmcnt(4)\n" \
    "v_smfmac_f32_16x16x64_bf16 v[12:15], v[52:55], v[80:87], %[idx]\n" \
    "ds_read_b128 v[80:83], %[wl] offset:24576\n" \
    "ds_read_b128 v[84:87], %[wl] offset:25600\n" \
    "s_waitcnt vmcnt(8)\n" \
    "v_smfmac_f32_16x16x64_bf16 v[0:3], v[56:59], a[80:87], %[idx]\n" \
    "v_max3_u32 v104, v104, v56, v57\n" \
    "v_smfmac_f32_16x16x64_bf16 v[4:7], v[56:59], a[208:215], %[idx]\n" \
    "v_max3_u32 v104, v104, v58, v59\n" \
    "v_smfmac_f32_16x16x64_bf16 v[8:11], v[56:59], v[208:215], %[idx]\n" \
    "s_waitcnt lgkmcnt(4)\n" \
    "v_smfmac_f32_16x16x64_bf16 v[12:15], v[56:59], v[88:95], %[idx]\n" \
    "ds_read_b128 v[88:91], %[wl] offset:26624\n" \
    "ds_read_b128 v[92:95], %[wl] offset:27648\n" \
    "s_waitcnt vmcnt(7)\n" \
    "v_smfmac_f32_16x16x64_bf16 v[0:3], v[60:63], a[88:95], %[idx]\n" \
    "v_max3_u32 v104, v104, v60, v61\n" \
    "v_smfmac_f32_16x16x64_bf16 v[4:7], v[60:63], a[216:223], %[idx]\n" \
    "v_max3_u32 v104, v104, v62, v63\n" \
    "v_smfmac_f32_16x16x64_bf16 v[8:11], v[60:63], v[216:223], %[idx]\n" \
    "s_waitcnt lgkmcnt(4)\n" \
    "v_smfmac_f32_16x16x64_bf16 v[12:15], v[60:63], v[96:103], %[idx]\n" \
    "ds_read_b128 v[96:99], %[wl] offset:28672\n" \
    "ds_read_b128 v[100:103], %[wl] offset:29696\n" \
    "s_waitcnt vmcnt(6)\n" \
    "v_smfmac_f32_16x16x64_bf16 v[0:3], v[64:67], a[96:103], %[idx]\n" \
    "v_max3_u32 v104, v104, v64, v65\n" \
    "v_smfmac_f32_16x16x64_bf16 v[4:7], v[64:67], a[224:231], %[idx]\n" \
    "v_max3_u32 v104, v104, v66, v67\n" \
    "v_smfmac_f32_16x16x64_bf16 v[8:11], v[64:67], v[224:231], %[idx]\n" \
    "s_waitcnt lgkmcnt(4)\n" \
    "v_smfmac_f32_16x16x64_bf16 v[12:15], v[64:67], v[80:87], %[idx]\n" \
    "ds_read_b128 v[80:83], %[wl] offset:30720\n" \
    "ds_read_b128 v[84:87], %[wl] offset:31744\n" \
    "s_waitcnt vmcnt(5)\n" \
    "v_smfmac_f32_16x16x64_bf16 v[0:3], v[68:71], a[104:111], %[idx]\n" \
    "v_max3_u32 v104, v104, v68, v69\n" \
    "v_smfmac_f32_16x16x64_bf16 v[4:7], v[68:71], a[232:239], %[idx]\n" \
    "v_max3_u32 v104, v104, v70, v71\n" \
    "v_smfmac_f32_16x16x64_bf16 v[8:11], v[68:71], v[232:239], %[idx]\n" \
    "s_waitcnt lgkmcnt(4)\n" \
    "v_smfmac_f32_16x16x64_bf16 v[12:15], v[68:71], v[88:95], %[idx]\n" \
    "s_waitcnt vmcnt(4)\n" \
    "v_smfmac_f32_16x16x64_bf16 v[0:3], v[72:75], a[112:119], %[idx]\n" \
    "v_max3_u32 v104, v104, v72, v73\n" \
    "v_smfmac_f32_16x16x64_bf16 v[4:7], v[72:75], a[240:247], %[idx]\n" \
    "v_max3_u32 v104, v104, v74, v75\n" \
    "v_smfmac_f32_16x16x64_bf16 v[8:11], v[72:75], v[240:247], %[idx]\n" \
    "s_waitcnt lgkmcnt(2)\n" \
    "v_smfmac_f32_16x16x64_bf16 v[12:15], v[72:75], v[96:103], %[idx]\n" \
    "s_waitcnt vmcnt(3)\n" \
    "v_smfmac_f32_16x16x64_bf16 v[0:3], v[76:79], a[120:127], %[idx]\n" \
    "v_max3_u32 v104, v104, v76, v77\n" \
    "v_smfmac_f32_16x16x64_bf16 v[4:7], v[76:79], a[248:255], %[idx]\n" \
    "v_max3_u32 v104, v104, v78, v79\n" \
    "v_smfmac_f32_16x16x64_bf16 v[8:11], v[76:79], v[248:255], %[idx]\n" \
    "s_waitcnt lgkmcnt(0)\n" \
    "v_smfmac_f32_16x16x64_bf16 v[12:15], v[76:79], v[80:87], %[idx]\n" \
    "s_waitcnt vmcnt(0)\n" \
    "s_nop 5\n" \
    "v_permlane32_swap_b32 v0, v8\n" \
    "v_add_f32 v0, v0, v8\n" \
    "v_permlane32_swap_b32 v1, v9\n" \
    "v_add_f32 v1, v1, v9\n" \
    "v_permlane32_swap_b32 v2, v10\n" \
    "v_add_f32 v2, v2, v10\n" \
    "v_permlane32_swap_b32 v3, v11\n" \
    "v_add_f32 v3, v3, v11\n" \
    "v_permlane32_swap_b32 v4, v12\n" \
    "v_add_f32 v4, v4, v12\n" \
    "v_permlane32_swap_b32 v5, v13\n" \
    "v_add_f32 v5, v5, v13\n" \
    "v_permlane32_swap_b32 v6, v14\n" \
    "v_add_f32 v6, v6, v14\n" \
    "v_permlane32_swap_b32 v7, v15\n" \
    "v_add_f32 v7, v7, v15\n" \
    ""
#define OUT_CLOBBER "v16", "v17", "v18", "v19", "v20", "v21", "v22", "v23", "v24", "v25", "v26", "v27", "v28", "v29", "v30", "v31", "v32", "v33", "v34", "v35", "v36", "v37", "v38", "v39", "v40", "v41", "v42", "v43", "v44", "v45", "v46", "v47", "v48", "v49", "v50", "v51", "v52", "v53", "v54", "v55", "v56", "v57", "v58", "v59", "v60", "v61", "v62", "v63", "v64", "v65", "v66", "v67", "v68", "v69", "v70", "v71", "v72", "v73", "v74", "v75", "v76", "v77", "v78", "v79", "v80", "v81", "v82", "v83", "v84", "v85", "v86", "v87", "v88", "v89", "v90", "v91", "v92", "v93", "v94", "v95", "v96", "v97", "v98", "v99", "v100", "v101", "v102", "v103", "s44", "s45", "s46", "scc", "memory"
constexpr int NREP = 2, NCP = 4;
#define OWNER_ASM \
    "s_getpc_b64 s[96:97]\n" \
    "14:\n" \
    "s_add_u32 s96, s96, 2f-14b\n" \
    "s_addc_u32 s97, s97, 0\n" \
    "s_sub_u32 s68, s96, 2f-11f\n" \
    "s_subb_u32 s69, s97, 0\n" \
    "s_mov_b32 %[np], 0\n" \
    "13:\n" \
    "s_memtime vcc\n" \
    "s_waitcnt lgkmcnt(0)\n" \
    "s_sub_i32 s70, %[lk], vcc_lo\n" \
    "s_cmp_gt_i32 s70, %[rd]\n" \
    "s_cbranch_scc1 13b\n" \
    "buffer_store_dwordx4 %[sv], %[ro], %[rs], %[pq] offen offset:0 sc1\n" \
    "buffer_store_dwordx4 %[sv], %[ro], %[rs], %[pq] offen offset:1024 sc1\n" \
    "buffer_store_dwordx4 %[sv], %[ro], %[rs], %[pq] offen offset:2048 sc1\n" \
    "buffer_store_dwordx4 %[sv], %[ro], %[rs], %[pq] offen offset:3072 sc1\n" \
    "v_mov_b32 v2, -1\n" \
    "buffer_store_dword v2, %[rm], %[rs], %[mq] offen sc1\n" \
    "s_mov_b32 s71, 4\n" \
    "10:\n" \
    "s_memtime vcc\n" \
    "s_waitcnt lgkmcnt(0)\n" \
    "s_sub_i32 s70, %[lk], vcc_lo\n" \
    "s_cmp_gt_i32 s70, 112\n" \
    "s_cbranch_scc1 10b\n" \
    "s_max_i32 s70, s70, 0\n" \
    "s_memtime s[58:59]\n" \
    "s_memrealtime s[44:45]\n" \
    "s_nop 1\n" \
    "s_memrealtime s[46:47]\n" \
    "s_nop 1\n" \
    "s_memrealtime s[48:49]\n" \
    "s_nop 1\n" \
    "s_memrealtime s[50:51]\n" \
    "s_nop 1\n" \
    "s_memrealtime s[52:53]\n" \
    "s_nop 1\n" \
    "s_memrealtime s[54:55]\n" \
    "s_nop 1\n" \
    "s_memrealtime s[56:57]\n" \
    "s_nop 1\n" \
    "s_memrealtime vcc\n" \
    "s_waitcnt lgkmcnt(0)\n" \
    "s_add_i32 s44, s44, s46\n" \
    "s_add_i32 s48, s48, s50\n" \
    "s_add_i32 s52, s52, s54\n" \
    "s_add_i32 s44, s44, s48\n" \
    "s_add_i32 s52, s52, s56\n" \
    "s_add_i32 s44, s44, s52\n" \
    "s_add_i32 s59, s44, vcc_lo\n" \
    "s_sub_i32 s44, s59, %[oT]\n" \
    "s_mov_b32 %[oT], s59\n" \
    "s_sub_i32 s45, s58, %[oC]\n" \
    "s_mov_b32 %[oC], s58\n" \
    "s_mul_i32 s46, s44, %[oR]\n" \
    "s_lshr_b32 s46, s46, 12\n" \
    "s_sub_i32 s46, s45, s46\n" \
    "s_sub_i32 s44, s44, 1000\n" \
    "s_cmp_lt_u32 s44, 480\n" \
    "s_cselect_b32 s46, s46, 0\n" \
    "s_add_i32 %[oR], %[oR], s46\n" \
    "s_sub_i32 s45, %[oT], %[c8]\n" \
    "s_and_b32 s70, s70, -4\n" \
    "s_sub_u32 vcc_lo, s68, s70\n" \
    "s_subb_u32 vcc_hi, s69, 0\n" \
    "s_setpc_b64 vcc\n" \
    "s_nop 0\n" \
    "s_nop 0\n" \
    "s_nop 0\n" \
    "s_nop 0\n" \
    "s_nop 0\n" \
    "s_nop 0\n" \
    "s_nop 0\n" \
    "s_nop 0\n" \
    "s_nop 0\n" \
    "s_nop 0\n" \
    "s_nop 0\n" \
    "s_nop 0\n" \
    "s_nop 0\n" \
    "s_nop 0\n" \
    "s_nop 0\n" \
    "s_nop 0\n" \
    "s_nop 0\n" \
    "s_nop 0\n" \
    "s_nop 0\n" \
    "s_nop 0\n" \
    "s_nop 0\n" \
    "s_nop 0\n" \
    "s_nop 0\n" \
    "s_nop 0\n" \
    "s_nop 0\n" \
    "s_nop 0\n" \
    "s_nop 0\n" \
    "s_nop 0\n" \
    "11:\n" \
    "buffer_load_dwordx2 v[0:1], %[ca], %[rs], %[cs] offen sc1\n" \
    "s_waitcnt vmcnt(0)\n" \
    "6:\n" \
    "s_memtime s[46:47]\n" \
    "v_subrev_u32 v0, s45, v0\n" \
    "v_cmp_ne_u16 vcc, %[tag], v1\n" \
    "s_add_i32 s48, %[oC], -136\n" \
    "v_max_i32_dpp v0, v0, v0 row_shr:1 row_mask:0xf bank_mask:0xf\n" \
    "v_max_u32_dpp v1, v1, v1 row_shr:1 row_mask:0xf bank_mask:0xf\n" \
    "s_cbranch_vccnz 7f\n" \
    "8:\n" \
    "v_max_i32_dpp v0, v0, v0 row_shr:2 row_mask:0xf bank_mask:0xf\n" \
    "v_max_u32_dpp v1, v1, v1 row_shr:2 row_mask:0xf bank_mask:0xf\n" \
    "s_nop 0\n" \
    "v_max_i32_dpp v0, v0, v0 row_shr:4 row_mask:0xf bank_mask:0xf\n" \
    "v_max_u32_dpp v1, v1, v1 row_shr:4 row_mask:0xf bank_mask:0xf\n" \
    "s_nop 0\n" \
    "v_max_i32_dpp v0, v0, v0 row_shr:8 row_mask:0xf bank_mask:0xf\n" \
    "v_max_u32_dpp v1, v1, v1 row_shr:8 row_mask:0xf bank_mask:0xf\n" \
    "s_waitcnt lgkmcnt(0)\n" \
    "v_max_i32_dpp v0, v0, v0 row_bcast:15 row_mask:0xa bank_mask:0xf\n" \
    "v_max_u32_dpp v1, v1, v1 row_bcast:15 row_mask:0xa bank_mask:0xf\n" \
    "s_add_i32 s46, s46, 44\n" \
    "v_max_i32_dpp v0, v0, v0 row_bcast:31 row_mask:0xc bank_mask:0xf\n" \
    "v_max_u32_dpp v1, v1, v1 row_bcast:31 row_mask:0xc bank_mask:0xf\n" \
    "v_readlane_b32 s44, v0, 63\n" \
    "v_readlane_b32 s49, v1, 63\n" \
    "s_mul_i32 s47, s44, %[oR]\n" \
    "s_ashr_i32 s47, s47, 12\n" \
    "s_add_i32 s47, s47, s48\n" \
    "s_sub_i32 s58, s47, s46\n" \
    "s_cmp_gt_i32 s58, 112\n" \
    "s_cbranch_scc1 1f\n" \
    "12:\n" \
    "s_max_i32 s58, s58, 0\n" \
    "s_and_b32 s58, s58, -4\n" \
    "s_sub_u32 vcc_lo, s96, s58\n" \
    "s_subb_u32 vcc_hi, s97, 0\n" \
    "s_setpc_b64 vcc\n" \
    "s_nop 0\n" \
    "s_nop 0\n" \
    "s_nop 0\n" \
    "s_nop 0\n" \
    "s_nop 0\n" \
    "s_nop 0\n" \
    "s_nop 0\n" \
    "s_nop 0\n" \
    "s_nop 0\n" \
    "s_nop 0\n" \
    "s_nop 0\n" \
    "s_nop 0\n" \
    "s_nop 0\n" \
    "s_nop 0\n" \
    "s_nop 0\n" \
    "s_nop 0\n" \
    "s_nop 0\n" \
    "s_nop 0\n" \
    "s_nop 0\n" \
    "s_nop 0\n" \
    "s_nop 0\n" \
    "s_nop 0\n" \
    "s_nop 0\n" \
    "s_nop 0\n" \
    "s_nop 0\n" \
    "s_nop 0\n" \
    "s_nop 0\n" \
    "s_nop 0\n" \
    "2:\n" \
    "buffer_load_dwordx4 v[0:3], %[o], %[rs], %[pb] offen offset:0 sc1\n" \
    "buffer_load_dwordx4 v[4:7], %[o], %[rs], %[pb] offen offset:1024 sc1\n" \
    "buffer_load_dwordx4 v[8:11], %[o], %[rs], %[pb] offen offset:2048 sc1\n" \
    "buffer_load_dwordx4 v[12:15], %[o], %[rs], %[pb] offen offset:3072 sc1\n" \
    "s_add_i32 %[np], %[np], 1\n" \
    "s_add_i32 %[tm], s44, s45\n" \
    "s_lshr_b32 s50, s49, 16\n" \
    "s_add_i32 s50, s50, s44\n" \
    "s_add_i32 s50, s50, %[cw]\n" \
    "s_mul_i32 s50, s50, %[oR]\n" \
    "s_ashr_i32 s50, s50, 12\n" \
    "s_add_i32 %[lk], s50, %[oC]\n" \
    "s_waitcnt vmcnt(3)\n" \
    "v_mfma_f32_16x16x32_bf16 v[16:19], %[sel], v[0:3], 0\n" \
    "v_max3_u32 v20, v0, v1, v1\n" \
    "v_max3_u32 v20, v2, v3, v20\n" \
    "s_waitcnt vmcnt(2)\n" \
    "v_mfma_f32_16x16x32_bf16 v[16:19], %[sel], v[4:7], v[16:19]\n" \
    "v_max3_u32 v20, v4, v5, v20\n" \
    "v_max3_u32 v20, v6, v7, v20\n" \
    "s_waitcnt vmcnt(1)\n" \
    "v_mfma_f32_16x16x32_bf16 v[16:19], %[sel], v[8:11], v[16:19]\n" \
    "v_max3_u32 v20, v8, v9, v20\n" \
    "v_max3_u32 v20, v10, v11, v20\n" \
    "s_waitcnt vmcnt(0)\n" \
    "v_mfma_f32_16x16x32_bf16 v[16:19], %[sel], v[12:15], v[16:19]\n" \
    "v_max3_u32 v20, v12, v13, v20\n" \
    "v_max3_u32 v20, v14, v15, v20\n" \
    "v_cmp_eq_u32 vcc, -1, v20\n" \
    "s_nop 3\n" \
    "s_cbranch_vccnz 4f\n" \
    "3:\n" \
    "v_add_f32_dpp v16, v16, v16 row_ror:8 row_mask:0xf bank_mask:0xf\n" \
    "v_add_f32_dpp v17, v17, v17 row_ror:8 row_mask:0xf bank_mask:0xf\n" \
    "v_cvt_pk_bf16_f32 v16, v16, v17\n" \
    "buffer_store_dword v16, %[so], %[rs], %[mb0] offen sc1\n" \
    "s_branch 5f\n" \
    "1:\n" \
    "s_memtime vcc\n" \
    "s_waitcnt lgkmcnt(0)\n" \
    "s_sub_i32 s58, s47, vcc_lo\n" \
    "s_cmp_gt_i32 s58, 112\n" \
    "s_cbranch_scc1 1b\n" \
    "s_branch 12b\n" \
    "4:\n" \
    "s_add_i32 %[bud], %[bud], -1\n" \
    "s_cmp_lt_i32 %[bud], 0\n" \
    "s_cbranch_scc0 2b\n" \
    "s_branch 3b\n" \
    "7:\n" \
    "s_bcnt1_i32_b64 s44, vcc\n" \
    "s_cmp_le_u32 s44, s71\n" \
    "s_mov_b32 s71, 0\n" \
    "s_cbranch_scc1 15f\n" \
    "s_cmp_eq_u32 %[tag], 1\n" \
    "s_cbranch_scc1 9f\n" \
    "s_sleep 15\n" \
    "9:\n" \
    "s_sleep 6\n" \
    "15:\n" \
    "s_add_i32 %[bud], %[bud], -1\n" \
    "s_cmp_lt_i32 %[bud], 0\n" \
    "s_cbranch_scc1 8b\n" \
    "buffer_load_dwordx2 v[0:1], %[ca], %[rs], %[cs] offen sc1\n" \
    "s_memtime s[58:59]\n" \
    "s_memrealtime s[44:45]\n" \
    "s_nop 1\n" \
    "s_memrealtime s[46:47]\n" \
    "s_nop 1\n" \
    "s_memrealtime s[48:49]\n" \
    "s_nop 1\n" \
    "s_memrealtime s[50:51]\n" \
    "s_nop 1\n" \
    "s_memrealtime s[52:53]\n" \
    "s_nop 1\n" \
    "s_memrealtime s[54:55]\n" \
    "s_nop 1\n" \
    "s_memrealtime s[56:57]\n" \
    "s_nop 1\n" \
    "s_memrealtime vcc\n" \
    "s_waitcnt lgkmcnt(0)\n" \
    "s_add_i32 s44, s44, s46\n" \
    "s_add_i32 s48, s48, s50\n" \
    "s_add_i32 s52, s52, s54\n" \
    "s_add_i32 s44, s44, s48\n" \
    "s_add_i32 s52, s52, s56\n" \
    "s_add_i32 s44, s44, s52\n" \
    "s_add_i32 s59, s44, vcc_lo\n" \
    "s_sub_i32 s44, s59, %[oT]\n" \
    "s_mov_b32 %[oT], s59\n" \
    "s_sub_i32 s45, s58, %[oC]\n" \
    "s_mov_b32 %[oC], s58\n" \
    "s_mul_i32 s46, s44, %[oR]\n" \
    "s_lshr_b32 s46, s46, 12\n" \
    "s_sub_i32 s46, s45, s46\n" \
    "s_sub_i32 s44, s44, 1000\n" \
    "s_cmp_lt_u32 s44, 480\n" \
    "s_cselect_b32 s46, s46, 0\n" \
    "s_add_i32 %[oR], %[oR], s46\n" \
    "s_sub_i32 s45, %[oT], %[c8]\n" \
    "s_waitcnt vmcnt(0)\n" \
    "s_branch 6b\n" \
    "5:\n" \
    ""
#define OWNER_CLOBBER "v0", "v1", "v2", "v3", "v4", "v5", "v6", "v7", "v8", "v9", "v10", "v11", "v12", "v13", "v14", "v15", "v16", "v17", "v18", "v19", "v20", "s44", "s45", "s46", "s47", "s48", "s49", "s50", "s51", "s52", "s53", "s54", "s55", "s56", "s57", "s58", "s59", "s68", "s69", "s70", "s71", "s96", "s97", "vcc", "scc", "memory"
constexpr int O_W = 0;
constexpr int O_STG = 131072;
constexpr int O_B = O_STG + 20480;
constexpr int OUT_REP = 7;
#define OUT_AUX 16
DI int opaque_lane() { int l; asm volatile("v_mbcnt_lo_u32_b32 %0, -1, 0\n\tv_mbcnt_hi_u32_b32 %0, -1, %0" : "=v"(l)); return l; }
template <int O, class V> DI void put4(V& d, u32x4 f) {
#pragma unroll
    for (int j = 0; j < 4; ++j) d[O + j] = f[j];
}
DI void role_outp(const Args& a, LAS char* lds, unsigned lds_base, int nb, int rep, rsrc_t rw, int& budget) {
    const int tid = threadIdx.x, lane = tid & 63, w = __builtin_amdgcn_readfirstlane(tid >> 6);
    WG_BARRIER();
    const int n = lane & 15, g = lane >> 4;
    const int wl = O_W + w * 32768 + lane * 16;
    u32x32 A[8], V[4];
    {
        const int stg = O_STG + w * 5120;
        const rsrc_t ro = __builtin_amdgcn_make_buffer_rsrc((void*)a.Wo, 0, HD * DM * 2, 0x00020000);
        const int so = (1024 * w + (lane >> 3)) * (DM * 2) + nb * 128 + (lane & 7) * 16, sw = stg + (lane >> 3) * 160 + (lane & 7) * 16;
        const int ra = stg + (16 * (g & 1) + 4 * (g >> 1) + (n >> 2)) * 160 + (n & 3) * 8;
        constexpr int PB = 4;
        u32x4 b[PB][4];
        auto fetch = [&](int ks) {
#pragma unroll
            for (int i = 0; i < 4; ++i) b[ks % PB][i] = __builtin_amdgcn_raw_buffer_load_b128(ro, so, (64 * (ks >> 1) + 16 * (ks & 1) + 8 * (i & 1) + 32 * (i >> 1)) * (DM * 2), 0);
        };
#pragma unroll
        for (int ks = 0; ks < PB; ++ks) fetch(ks);
        FENCE();
        static_for<32>([&](auto ic) {
            constexpr int ks = decltype(ic)::value;
#pragma unroll
            for (int i = 0; i < 4; ++i) *(LAS u32x4*)(lds + sw + i * 1280) = b[ks % PB][i];
            if (ks + PB < 32) fetch(ks + PB);
            u32x4 f[4];
#pragma unroll
            for (int t = 0; t < 4; ++t) { const u32x2 lo = tr_read(lds, ra + t * 32), hi = tr_read(lds, ra + t * 32 + 1280); f[t] = (u32x4){lo.x, lo.y, hi.x, hi.y}; }
            put4<4 * (ks & 7)>(A[ks >> 3], f[0]); put4<4 * (ks & 7)>(A[4 + (ks >> 3)], f[1]); put4<4 * (ks & 7)>(V[ks >> 3], f[2]);
            *(LAS u32x4*)(lds + wl + ks * 1024) = f[3];
            if constexpr (ks == 7) asm volatile("" : "+{a[0:31]}"(A[0]), "+{a[128:159]}"(A[4]), "+{v[128:159]}"(V[0]));
            if constexpr (ks == 15) asm volatile("" : "+{a[32:63]}"(A[1]), "+{a[160:191]}"(A[5]), "+{v[160:191]}"(V[1]));
            if constexpr (ks == 23) asm volatile("" : "+{a[64:95]}"(A[2]), "+{a[192:223]}"(A[6]), "+{v[192:223]}"(V[2]));
            if constexpr (ks == 31) asm volatile("" : "+{a[96:127]}"(A[3]), "+{a[224:255]}"(A[7]), "+{v[224:255]}"(V[3]));
            FENCE();
        });
    }
    wait_prog(rw, F_S4 + lane * 4, true, 1, budget);
    const rsrc_t ru = __builtin_amdgcn_make_buffer_rsrc(a.out, 0, SEQ * DM * 2, 0x00020000);
    constexpr int SP = 2 * NSEG - 2;
    auto first = [&](int u) { return u >= SP ? 8 * u + 8 * SP : 16 * u; };
    auto st_s = [&](int u) { return (u >= 2 * FAST ? F_H + ((first(u) >> 4) - 2 * FAST) * 4096 : F_O + (u & 3) * 4) + w * 1024; };
    auto bn_s = [&](int u) { return OFF_BO + (64 * nb + 16 * w) * BO_ROW + first(u) * 2; };
    auto gn_s = [&](int u) { return OFF_GT + (first(u) + 4 * w) * 4; };
    int st = -1;
    u32x2 bn = ld64(rw, (lane >> 2) * BO_ROW + (lane & 3) * 8, bn_s(rep));
    float gn = __uint_as_float(ld32(rw, g * 4, gn_s(rep)));
    __builtin_amdgcn_s_waitcnt(0);
#define OUT_OPS "=&{v[0:15]}"(acc), "=&{v104}"(top), "=&{v105}"(st), "=&{v[106:107]}"(bn), "=&{v108}"(gn) \
        : "{a[0:31]}"(A[0]), "{a[32:63]}"(A[1]), "{a[64:95]}"(A[2]), "{a[96:127]}"(A[3]), "{a[128:159]}"(A[4]), "{a[160:191]}"(A[5]), "{a[192:223]}"(A[6]), "{a[224:255]}"(A[7]), \
          "{v[128:159]}"(V[0]), "{v[160:191]}"(V[1]), "{v[192:223]}"(V[2]), "{v[224:255]}"(V[3]), [rw] "s"(rw), [so] "s"(t0 * 8192), [wl] "v"(lds_base + O_W + w * 32768 + lane * 16), \
          [tv] "v"(tv), [tm] "s"(tm), [vo] "v"(vo), [sv] "v"(sv), [bv] "v"((lane >> 2) * BO_ROW + (lane & 3) * 8), [gv] "v"(g * 4), \
          [ss] "s"(st_s(u + OUT_REP)), [bs] "s"(bn_s(u + OUT_REP)), [gs] "s"(gn_s(u + OUT_REP))
    for (int u = rep; u < SP + 4; u += OUT_REP) {
        const bool fast = u >= 2 * FAST, sp = u >= SP;
        const int t0 = first(u);
        WG_BARRIER();
        int lane = opaque_lane(), n = lane & 15, g = lane >> 4;
        if (u == 3 * OUT_REP && nb == 0) __builtin_amdgcn_raw_buffer_store_b64(launch_tag(a), rw, w + lane == 0 ? F_EPOCH : OOB, 0, 16);
        S4S(0)
        TT(u >= 123, 0)
        *(LAS u32x2*)(lds + O_B + (16 * w + (lane >> 2)) * 40 + (lane & 3) * 8) = bn;
        const float gt = gn;
        const int tv = OFF_OF + w * 16384 + nb * 2048 + g * 65536 + n * 128, tm = __builtin_amdgcn_readfirstlane(nb < 8 ? sp ? 0xFFFF : -1 : 0);
        const int vo = OFF_OF + w * 16384 + (n & 7) * 16 + (sp ? (n >> 3) * 512 + g * 128 : (n >> 3) * 65536 + (g & 1) * 512 + (g >> 1) * 128);
        const int thr = fast ? 0 : u >> 1, sv = (lane & 7) * 128;
        if (!__all(st >= thr)) do { __builtin_amdgcn_s_sleep(1); st = (int)ld32(rw, sv, st_s(u)); asm volatile("" ::: "memory"); } while (!__all(st >= thr) && --budget >= 0);
        if (fast) { const unsigned t = now(); (void)wait_until(t + wave_max32(min((int)(2 * (st - t)) >> 1, 500))); }
        S4S(1)
        TT(u >= 123, 1)
        f32x16 acc; unsigned top;
        if (sp) {
            do asm volatile(OUT8_ASM : OUT_OPS, [idx] "v"(n & 8 ? 0xEEEE : 0x4444) : OUT_CLOBBER); while (__any(top == SENT) && --budget >= 0);
            TT(u >= 123, 2)
            lane = opaque_lane(); n = lane & 15; g = lane >> 4;
#pragma unroll
            for (int i = 0; i < 8; ++i) *(LAS float*)(lds + O_STG + (w * 16 + 4 * (g & 1) + (i & 3)) * 272 + (g >> 1) * 128 + (i >> 2) * 64 + n * 4) = acc[i];
        } else {
            do asm volatile(OUT16_ASM : OUT_OPS : OUT_CLOBBER); while (__any(top == SENT) && --budget >= 0);
            S4S(2)
            TT(u >= 123, 2)
            lane = opaque_lane(); n = lane & 15; g = lane >> 4;
#pragma unroll
            for (int t = 0; t < 4; ++t) *(LAS f32x4*)(lds + O_STG + (w * 16 + n) * 272 + t * 64 + g * 16) = (f32x4){acc[4 * t], acc[4 * t + 1], acc[4 * t + 2], acc[4 * t + 3]};
        }
        WG_BARRIER();
        S4S(3)
        const int tok = 4 * w + g, c4 = 4 * n;
        f32x4 r[4]; u16 bv[4];
#pragma unroll
        for (int ww = 0; ww < 4; ++ww) { r[ww] = *(LAS f32x4*)(lds + O_STG + (ww * 16 + tok) * 272 + c4 * 4); bv[ww] = *(LAS u16*)(lds + O_B + (c4 + ww) * 40 + tok * 2); }
        FENCE();
        const f32x4 v = gt * ((r[0] + r[1]) + (r[2] + r[3])) + (1.0f - gt) * (f32x4){bf2f(bv[0]), bf2f(bv[1]), bf2f(bv[2]), bf2f(bv[3])};
        __builtin_amdgcn_raw_buffer_store_b64((u32x2){pack2(v[0], v[1]), pack2(v[2], v[3])}, ru, sp && tok >= 8 ? OOB : (tok * DM + 64 * nb + c4) * 2, t0 * (DM * 2), OUT_AUX);
        S4S(4)
        TT(u >= 123, 3)
        TT(u >= 109 && u < 116, 4)
    }
}

constexpr int L_MEMS = 0;
constexpr int L_Q = 9216;
constexpr int L_KO = L_Q + 2560;
constexpr int L_KX = 16384;
constexpr int L_W = 32768;
constexpr int C_RING = 8;
static_assert(S_TOTAL <= L_SINK && L_W + C_RING * 16384 <= L_TOTAL && L_KO + 8 * 576 <= L_KX, "LDS budget");
static_assert(P_STG + P_RING * 16384 <= L_TOTAL, "LDS budget");

DI int pos2col(int p) { return p < 512 ? (p & ~63) + 16 * (p & 3) + ((p & 63) >> 2) : p; }
DI void unit_rc(int u, int& i, int& p0) {
    if (u < 512) { i = 2 * ((u >> 2) & 3) + (u & 1); p0 = (u & ~15) + 4 * (u & 2); }
    else { const int v = u - 512; i = (v >> 1) & 7; p0 = 512 + 16 * (v >> 4) + 8 * (v & 1); }
}
DI void mem0_arrange(LAS char* lds, int raw, int tid) {
#pragma unroll
    for (int k = 0; k < 2; ++k) {
        int i, p0; unit_rc(tid + 256 * k, i, p0);
        unsigned r[4];
#pragma unroll
        for (int j = 0; j < 4; ++j) r[j] = *(LAS unsigned*)(lds + raw + (i * DM + pos2col(p0)) * 2 + 32 * j);
        *(LAS u32x4*)(lds + L_MEMS + (tid + 256 * k) * 16) = (u32x4){__builtin_amdgcn_perm(r[1], r[0], 0x05040100), __builtin_amdgcn_perm(r[3], r[2], 0x05040100),
                                                                     __builtin_amdgcn_perm(r[1], r[0], 0x07060302), __builtin_amdgcn_perm(r[3], r[2], 0x07060302)};
    }
    int i, p0; unit_rc(512 + (tid & 63), i, p0);
    *(LAS u32x4*)(lds + L_MEMS + 8192 + (tid & 63) * 16) = *(LAS u32x4*)(lds + raw + (i * DM + p0) * 2);
}
DI unsigned read_canaries(int s, rsrc_t rs, int lane, int& budget) {
    const int o = 2 * OFF_CAN + (s & 3) * 16384 + (lane >> 2) * 512 + (lane & 3) * 16;
    u32x2 v;
    for (;;) {
        v = ld64(rs, o, 0);
        asm volatile("" ::: "memory");
        if (__all((v.y & 0xffff) == (unsigned)(s + 1))) break;
        if (--budget < 0) break;
    }
    const unsigned t = 8 * now();
    return t + (unsigned)wave_max32(min(max((int)(v.x - t), -0x4000), 0x4000));
}
DI u32x8 sm_frag(LAS char* lds, int sl, int r0, int sa, int sH, int cb, int lo8) {
    auto rd = [&](int r) { return tr_read(lds, sl + r * 256 + ((cb ^ (2 * swz(r))) * 16) + lo8); };
    const u32x2 a = rd(r0), b = rd(r0 + sa), c = rd(r0 + sH), d = rd(r0 + sa + sH);
    return (u32x8){a.x, a.y, b.x, b.y, c.x, c.y, d.x, d.y};
}
#define GS(slot, i)
#define GV(slot, i, v)
DI void owner_loop(const Args& a, int G, rsrc_t rs, unsigned tbase, unsigned cbase, int& budget) {
    const int lane = threadIdx.x & 63, n = lane & 15, g = lane >> 4;
    const bool ex = G >= 64;
    const u32x4 sent4 = {SENT, SENT, SENT, SENT};
    const int js = (n & 2) ? 8 : ex ? (n & 4) + 2 * (n & 1) + (n >> 3) : 2 * (n >> 2) + (n & 1);
    u32x4 selu;
#pragma unroll
    for (int j = 0; j < 4; ++j) selu[j] = (js == 2 * j ? 0x3F80u : 0u) | (js == 2 * j + 1 ? 0x3F800000u : 0u);
    const int o = cmap(G * 4096 + lane * 16);
    const int so = (n < 8 || NREP > 1) ? cmap((n >> 3) * 16384 + G * 128 + (g & 1) * 4 + (ex ? (n & 4) * 16 + (g >> 1) * 32 + (n & 3) * 8 : (n & 6) * 16 + (g >> 1) * 16 + (n & 1) * 8)) : OOB;
    unsigned fails = 0;
    const int c8 = G >= 64 ? a.c1 >> 16 : G < 8 ? a.c1 >> 8 & 255 : a.c1 & 255;
    const bool gv_on = true; (void)gv_on;
    unsigned oT = now(), oC = (unsigned)__builtin_readcyclecounter(), oR;
    { const unsigned r = (unsigned)(512.0f * (float)(oC - cbase) * __builtin_amdgcn_rcpf((float)(oT - tbase))); asm volatile("s_nop 0\n\tv_readfirstlane_b32 %0, %1" : "=s"(oR) : "v"(r)); }
    oT = 8 * oT + 16;
    unsigned lk = oC;
    budget = __builtin_amdgcn_readfirstlane(budget);
    const int ca = 2 * OFF_CAN + (lane >> 2) * 512 + (G & (NCP - 1)) * 128 + (lane & 3) * 16;
    for (int s = 0; s < NSEG - 1; ++s) {
        const int pb = 2 * (OFF_PART + (s & 1) * PART_BYTES);
        int np; unsigned tc;
        asm volatile(OWNER_ASM : [np] "=&s"(np), [tm] "=&s"(tc), [bud] "+s"(budget), [oT] "+s"(oT), [oC] "+s"(oC), [oR] "+s"(oR), [lk] "+s"(lk)
            : [c8] "s"(c8), [ca] "v"(ca), [cs] "s"((s & 3) * 16384), [tag] "s"(s + 1), [o] "v"(o), [so] "v"(so), [sel] "v"(selu), [rs] "s"(rs), [pb] "s"(pb), [mb0] "s"(2 * (OFF_MEMB + ((s + 1) & 3) * MEMB_STRIDE)),
              [cw] "s"(a.cw - c8), [rd] "s"(a.rd), [sv] "v"(sent4), [ro] "v"(s ? o : OOB), [pq] "s"(2 * PART_BYTES - pb), [rm] "v"(s > 2 ? so : OOB), [mq] "s"(2 * (OFF_MEMB + ((s + 2) & 3) * MEMB_STRIDE)) : OWNER_CLOBBER);
        fails += np > 1;
    }
}
DI void role_owner(const Args& a, unsigned lds_base, int ow, rsrc_t rw, int& budget) {
    const int tid = threadIdx.x, lane = tid & 63, w = __builtin_amdgcn_readfirstlane(tid >> 6);
    const unsigned tbase = now(), cbase = (unsigned)__builtin_readcyclecounter();
    if (ow != 63 && w) { prefetch(a, lds_base, ow & 3, (ow >> 2) * 3 + w - 1, (ow & 3) == 3 ? 45 : 48); VM_DRAIN(); }
    if (w >= 2 || (w == 1 && ow >= 8)) return;
    const u32x2 tag = launch_tag(a);
    if (ow == 63) {
        unsigned best[2] = {~0u, ~0u};
        for (int r = 0; r < 10; ++r) {
            unsigned long long t0, t1;
            asm volatile("s_memtime %0\n\ts_waitcnt lgkmcnt(0)" : "=s"(t0) :: "memory");
            st32(SENT, rw, lane == 0 ? (r & 1) * 8192 + (r >> 1) * 4096 * 5 / 4096 * 16384 + ((r >> 1) & 1) * 4096 : OOB, 0);
            asm volatile("s_waitcnt vmcnt(0)\n\ts_memtime %0\n\ts_waitcnt lgkmcnt(0)" : "=s"(t1) :: "memory");
            best[r & 1] = min(best[r & 1], (unsigned)(t1 - t0));
        }
        __builtin_amdgcn_raw_buffer_store_b64((u32x2){(tag.x & ~1u) | (best[1] < best[0]), tag.y}, rw, lane == 0 ? F_INIT + 32 * 128 : OOB, 0, 16);
    }
    const int sel = wait_init(rw, tag, budget, 32);
    const rsrc_t rs = __builtin_amdgcn_make_buffer_rsrc(a.ws + sel * 8192, 0, COMM_BYTES - 8192, 0x00020000);
    owner_loop(a, ow + 64 * w, rs, tbase, cbase, budget);
}

template <int O, class V> DI void put8(V& d, u32x8 f) {
#pragma unroll
    for (int j = 0; j < 8; ++j) d[O + j] = f[j];
}
DI f32x4 SMF64(u32x4 a, u32x8 b, f32x4 c, int i) { asm volatile("s_nop 1\n\tv_smfmac_f32_16x16x64_bf16 %0, %1, %2, %3\n\ts_nop 7" : "+v"(c) : "v"(a), "v"(b), "v"(i)); return c; }
DI f32x4 SMF32(u32x2 a, u32x4 b, f32x4 c, int i) { asm volatile("s_nop 1\n\tv_smfmac_f32_16x16x32_bf16 %0, %1, %2, %3\n\ts_nop 7" : "+v"(c) : "v"(a), "v"(b), "v"(i)); return c; }
DI f32x4 MFV(u32x4 a, u32x4 b, f32x4 c) { asm volatile("s_nop 1\n\tv_mfma_f32_16x16x32_bf16 %0, %1, %2, %0\n\ts_nop 7" : "+v"(c) : "v"(a), "v"(b)); return c; }

DI void role_head(const Args& a, LAS char* lds, unsigned lds_base, int h, int tw, rsrc_t rw, int& budget) {
    const int tid = threadIdx.x, w = __builtin_amdgcn_readfirstlane(tid >> 6);
    char* ws = a.ws;
    const unsigned tbase = now(), cbase = (unsigned)__builtin_readcyclecounter();
    const bool gv_on = tw == 0; (void)gv_on;
    u32x32 A0, A1, A2, A3, A4, A5, A6, A7, R0, R1, R2, R3; u32x8 R4;
    u32x2 tag;
    {
        const int lane = tid & 63, n = lane & 15, g = lane >> 4;
#pragma unroll
        for (int j = 0; j < 3; ++j) glds16(a.mem0, min(4 * j + w, 8) * 1024 + lane * 16, lds_base + L_KX + min(4 * j + w, 8) * 1024);
        const int tq = n >> 2, tc = (n & 3) >> 1, lo8 = (n & 1) * 8;
        const int r_nat = 16 * (g >> 1) + 4 * (g & 1) + (tq & 1) + 8 * (tq >> 1);
        const int r_sig = 16 * (tq & 1) + 4 * (g >> 1) + (g & 1) + 2 * (tq >> 1);
        const int r_o = 16 * (tq & 1) + 8 * (g >> 1) + 4 * (g & 1) + (tq >> 1);
#define WFRAG(sl, kb, t) ((kb) < 8 ? sm_frag(lds, sl, r_sig, 32, 8, 4 * w + 2 * (t) + tc, lo8) : sm_frag(lds, sl, r_nat, 2, 32, 4 * w + 2 * (t) + tc, lo8))
        const u32x4 ro = ring_offs(w, lane, HD);
        auto issue = [&](int i) {
            const unsigned slot = lds_base + L_W + (i % C_RING) * 16384 + w * 4096;
            if (i == 23) ring_issue(a.Wo + (size_t)h * DH * DM + 512, ring_offs(w, lane, [](int r, int c) { return (r + 64 * (c >> 3)) * (DM * 2) + (c & 7) * 16; }), slot);
            else if (i < 28) ring_issue((i < 8 || i == 20 ? a.Wq : i < 16 || i == 21 ? a.Wk : a.Wv) + (size_t)(64 * (i < 16 ? i & 7 : i < 20 ? i - 12 : i < 24 ? 8 : i - 24)) * HD + h * DH, ro, slot);
            else ring_issue(a.Wo + (size_t)(h * DH + 64 * ((i & 1) ^ ((i - 28) >> 1))) * DM + 256 * tw + 128 * (i & 1), ring_offs(w, lane, DM), slot);
        };
        auto keep = [&](int at, u32x8 f) {
            *(LAS u32x4*)(lds + at + lane * 16) = (u32x4){f[0], f[1], f[2], f[3]};
            *(LAS u32x4*)(lds + at + 1024 + lane * 16) = (u32x4){f[4], f[5], f[6], f[7]};
        };
        u32x8 hold[8] = {};
#pragma unroll
        for (int i = 0; i < C_RING; ++i) issue(i);
        tag = launch_tag(a);
        ring_wait<4 * C_RING>();
        mem0_arrange(lds, L_KX, tid);
        static_for<9>([&](auto ic) {
            constexpr int G = decltype(ic)::value;
            ring_wait<(G < 7 ? 16 : G == 7 ? 8 : 0)>();
            const int sl = L_W + (G & 1) * 65536;
            auto fr = [&](int j, int t) { return WFRAG(sl + ((j ^ w) << 14), 0, t); };
            if constexpr (G < 4) {
                u32x32& A = G == 0 ? A0 : G == 1 ? A2 : G == 2 ? A4 : A6;
                u32x32& B = G == 0 ? A1 : G == 1 ? A3 : G == 2 ? A5 : A7;
                put8<0>(A, fr(0, 0)); put8<8>(A, fr(0, 1)); put8<16>(A, fr(1, 0)); put8<24>(A, fr(1, 1));
                put8<0>(B, fr(2, 0)); put8<8>(B, fr(2, 1)); put8<16>(B, fr(3, 0)); put8<24>(B, fr(3, 1));
                if constexpr (G == 0) asm volatile("" : "+{a[0:31]}"(A0), "+{a[32:63]}"(A1));
                if constexpr (G == 1) asm volatile("" : "+{a[64:95]}"(A2), "+{a[96:127]}"(A3));
                if constexpr (G == 2) asm volatile("" : "+{a[128:159]}"(A4), "+{a[160:191]}"(A5));
                if constexpr (G == 3) asm volatile("" : "+{a[192:223]}"(A6), "+{a[224:255]}"(A7));
            } else if constexpr (G == 4) {
                put8<16>(R1, fr(0, 0)); put8<24>(R1, fr(0, 1)); put8<0>(R2, fr(1, 0)); put8<8>(R2, fr(1, 1));
                put8<16>(R2, fr(2, 0)); put8<24>(R2, fr(2, 1)); put8<0>(R3, fr(3, 0)); put8<8>(R3, fr(3, 1));
            } else if constexpr (G == 5) {
                put8<16>(R0, WFRAG(sl, 8, 0)); put8<24>(R0, WFRAG(sl, 8, 1)); put8<0>(R1, WFRAG(sl + 16384, 8, 0)); put8<8>(R1, WFRAG(sl + 16384, 8, 1));
                put8<16>(R3, WFRAG(sl + 32768, 8, 0)); put8<24>(R3, WFRAG(sl + 32768, 8, 1));
                hold[7] = sm_frag(lds, sl + 49152, r_o, 2, 32, 8 * (w >> 1) + 4 * tw + 2 * (w & 1) + tc, lo8);
            } else if constexpr (G == 6) {
#pragma unroll
                for (int j = 0; j < 7; ++j) hold[j] = fr(j >> 1, j & 1);
                R4 = fr(3, 1);
            } else {
                if constexpr (G == 8)
#pragma unroll
                    for (int T = 0; T < 4; ++T) keep(L_W + 65536 + w * 8192 + T * 2048, hold[T]);
#pragma unroll
                for (int T = 0; T < 4; ++T) hold[T] = sm_frag(lds, L_W + (2 * G - 10 + (w >> 1)) * 16384, r_o, 2, 32, 8 * (w & 1) + 2 * T + tc, lo8);
            }
            if constexpr (G != 7) WG_BARRIER();
            if constexpr (G < 6)
#pragma unroll
                for (int i = 0; i < 4; ++i) issue(4 * G + 8 + i);
            if constexpr (G == 6)
#pragma unroll
                for (int j = 0; j < 8; ++j) keep(L_W + (j >> 1) * 16384 + w * 4096 + (j & 1) * 2048, hold[j]);
            if constexpr (G == 8)
#pragma unroll
                for (int T = 0; T < 4; ++T) keep(L_W + 98304 + w * 8192 + T * 2048, hold[T]);
        });
        const int ai = 2 * (n >> 2) + (n & 1), ahf = (n >> 1) & 1;
        R0[0] = R0[1] = R0[2] = R0[3] = 0x3F803F80u;
        R0[4] = ahf ? 0xEEEE : 0x4444;
        R0[5] = (g >> 1) * 512 + (n >> 2) * 128 + (g & 1) * 64 + (n & 3) * 16;
        R0[6] = lds_base + L_MEMS + 8192 + g * 256 + ai * 32 + ahf * 16;
        R0[7] = lane * 16;
        R0[8] = lds_base + L_Q + 2 * g * 320 + (16 * w + n) * 4;
        R0[9] = lds_base + L_KO + 2 * g * 576 + (16 * w + (n & 8) + 4 * (n & 1) + ((n >> 1) & 3)) * 4;
        R0[10] = lds_base + L_Q + ai * 320 + g * 16;
        R0[11] = lds_base + L_KO + ai * 576 + g * 32 + ahf * 16;
        R0[12] = lds_base + L_KX + 8192 + w * 2048 + (g >> 1) * 256 + n * 16 + (g & 1) * 8;
        R0[13] = lds_base + L_W + w * 4096 + lane * 16;
        R0[14] = cmap(((2 * (4 * tw + w) + (n >> 3)) * 4 + g) * 4096 + h * 128 + (n & 7) * 16);
        R0[15] = (g >> 1) == (w >> 1) ? cmap((64 + 2 * (2 * tw + (w & 1)) + (g >> 1)) * 4096 + h * 128 + (g & 1) * 64 + n * 4) : OOB;
        asm volatile("" : "+{v[120:151]}"(R0), "+{v[152:183]}"(R1), "+{v[184:215]}"(R2), "+{v[216:247]}"(R3), "+{v[248:255]}"(R4));
    }
    const int sel = wait_init(rw, tag, budget, 33 + 4 * h + w);
    int kv_rdy = 0;
    const rsrc_t rs = __builtin_amdgcn_make_buffer_rsrc(ws + sel * 8192, 0, COMM_BYTES - 8192, 0x00020000);
    auto dma = [&](int voff, int soff, unsigned dst) { bdma(rw, voff, soff, dst); };
    auto fetch_kv = [&](int s, int lane) {
        const int n = lane & 15, g = lane >> 4;
        const int kxs = (g >> 1) * 1024 + ((g & 1) * 32 + 16 * (n >> 3) + (n & 7)) * 16;
        const int vxs = (g >> 1) * 1024 + ((g & 1) * 32 + n) * 16;
        const int so_ = (s * 32 + h) * 8192 + w * 2048;
        const unsigned xdst = lds_base + L_KX + w * 2048;
        dma(kxs, OFF_KF + so_, xdst); dma(kxs, OFF_KF + so_ + 128, xdst + 1024);
        dma(vxs, OFF_VF + so_, xdst + 8192); dma(vxs, OFF_VF + so_ + 256, xdst + 8192 + 1024);
    };
    const int fo = F_KVM + h * 256 + tw * 128;
    u32x8 scon;
    scon[0] = w; scon[1] = 2 * OFF_CAN + (h >> 1) * 512 + (2 * (h & 1) + tw) * 16; scon[2] = F_KV + h * 512 + w * 128; scon[3] = 65536 + w * 4096;
    scon[4] = OFF_KF + h * 8192 + w * 2048; scon[5] = fo; scon[6] = tw - 1; scon[7] = (tw ? OFF_VM : OFF_KM) + (h - NH) * 2048 + w * 512;
    u32x8 S1 = {0u, 0u, (unsigned)kv_rdy, (unsigned)budget, (unsigned)a.d0, 0xFFFFFFFFu / 22, (unsigned)(a.c2 + a.b0), 0u};
    u32x8 S2 = {0u, 0u, 0u, (unsigned)a.up, (unsigned)a.dn, (unsigned)a.c2, 0u, 1u};
    fetch_kv(0, opaque_lane());
    __builtin_amdgcn_raw_buffer_store_b64((u32x2){8 * now() + a.e0, 1u | (a.c2 + a.b0) << 16}, rs, tid < NCP ? tid * 128 : OOB, scon[1], 16);
    asm volatile("s_waitcnt vmcnt(1)" ::: "memory");
    u32x2 vm = {};
    u32x8 S3;
    {
        const unsigned t = now(), c = (unsigned)__builtin_readcyclecounter();
        const float r = (float)(c - cbase) * __builtin_amdgcn_rcpf(8.0f * (float)(t - tbase));
        S2[1] = 8 * t + 16; S1[5] = (unsigned)(4294967296.0f * __builtin_amdgcn_rcpf(r));
        S3 = (u32x8){c, (unsigned)(4096.0f * r), 2u * OFF_MEMB + ((h >> 3) & (NREP - 1)) * 32768, w == 3 ? ~0u : 0u};
    }
    for (;;) {
        asm volatile(STEP_ASM
            : "+{v[96:97]}"(vm), "+{v[0:7]}"(S1), "+{v[8:15]}"(S2), "+{v[16:23]}"(S3), "+{v[24:31]}"(scon)
            : "{a[0:31]}"(A0), "{a[32:63]}"(A1), "{a[64:95]}"(A2), "{a[96:127]}"(A3), "{a[128:159]}"(A4), "{a[160:191]}"(A5), "{a[192:223]}"(A6), "{a[224:255]}"(A7),
              "{v[120:151]}"(R0), "{v[152:183]}"(R1), "{v[184:215]}"(R2), "{v[216:247]}"(R3), "{v[248:255]}"(R4),
              [rs] "s"(rs), [rw] "s"(rw)
            : STEP_CLOBBER);
        const int s = __builtin_amdgcn_readfirstlane(S1[0]);
        const unsigned st = __builtin_amdgcn_readfirstlane(S1[7]);
        kv_rdy = __builtin_amdgcn_readfirstlane(S1[2]); budget = __builtin_amdgcn_readfirstlane(S1[3]);
        if (s == NSEG - 1) break;
        if (__builtin_expect(st & 2, 0)) {
            const int lane = opaque_lane();
            LAS char* z = (LAS char*)0;
            const f32x4 zero = {0, 0, 0, 0};
            const u32x4 ones = __builtin_shufflevector(R0, R0, 0, 1, 2, 3);
            const unsigned zr = lane >> 6;
            const int idx = R0[4];
            u32x4 qf[4];
            f32x4 s1 = zero, s2 = zero;
#pragma unroll
            for (int ks = 0; ks < 4; ++ks) {
                qf[ks] = *(LAS u32x4*)(z + R0[10] + ks * 64);
                s1 = MFV(*(LAS u32x4*)(lds + L_KX + lane * 16 + ks * 2048), qf[ks], s1);
                s2 = MFV(*(LAS u32x4*)(lds + L_KX + lane * 16 + ks * 2048 + 1024), qf[ks], s2);
            }
            f32x4 s0 = SMF64(*(LAS u32x4*)(z + R0[11]), __builtin_shufflevector(qf[0], qf[1], 0, 1, 2, 3, 4, 5, 6, 7), zero, idx);
            s0 = SMF64(*(LAS u32x4*)(z + R0[11] + 128), __builtin_shufflevector(qf[2], qf[3], 0, 1, 2, 3, 4, 5, 6, 7), s0, idx);
            const float sa = s0[0] + s0[2], sb = s0[1] + s0[3];
            float mx = fmaxf(fmaxf(fmaxf(fmaxf(s1[0], s1[1]), fmaxf(s1[2], s1[3])), fmaxf(fmaxf(s2[0], s2[1]), fmaxf(s2[2], s2[3]))), fmaxf(sa, sb));
            mx = fmaxf(mx, __shfl_xor(mx, 16)); mx = fmaxf(mx, __shfl_xor(mx, 32));
            auto ex2 = [&](float x, float y) { return pack2(__builtin_amdgcn_exp2f(x - mx), __builtin_amdgcn_exp2f(y - mx)); };
            const u32x4 px = {ex2(s1[0], s1[1]), ex2(s1[2], s1[3]), ex2(s2[0], s2[1]), ex2(s2[2], s2[3])};
            const unsigned pm = ex2(sa, sb);
            const f32x4 l = MFV(((u32x4){pm, zr, zr, zr}), ones, MFV(px, ones, zero));
            const u32x2 x0 = *(LAS u32x2*)(z + R0[12]), y0 = *(LAS u32x2*)(z + R0[12] + 1024), x1 = *(LAS u32x2*)(z + R0[12] + 512), y1 = *(LAS u32x2*)(z + R0[12] + 1536);
            const f32x4 o = SMF64(px, ((u32x8){x0.x, y0.x, x0.y, y0.y, x1.x, y1.x, x1.y, y1.y}), zero, idx) + SMF32(((u32x2){pm, zr}), ((u32x4){vm.x, vm.y, zr, zr}), zero, idx);
            const float i0 = __builtin_amdgcn_rcpf(l[0]), i1 = __builtin_amdgcn_rcpf(l[1]);
            *(LAS unsigned*)(z + R0[9] + 256) = pack2(o[0] * i0, o[2] * i0);
            *(LAS unsigned*)(z + R0[9] + 256 + 576) = pack2(o[1] * i1, o[3] * i1);
            WG_BARRIER();
            const u32x4 oa = *(LAS u32x4*)(z + R0[11] + 256 + (w >> 1) * 128), ob = *(LAS u32x4*)(z + R0[11] + 384 - (w >> 1) * 128);
            auto frag = [&](int at) { return __builtin_shufflevector(*(LAS u32x4*)(z + at), *(LAS u32x4*)(z + at + 1024), 0, 1, 2, 3, 4, 5, 6, 7); };
            f32x2 f[4];
#pragma unroll
            for (int T = 0; T < 4; ++T) f[T] = fold(SMF64(ob, frag(R0[13] + scon[3] + 32768 + T * 2048), SMF64(oa, frag(R0[13] + scon[3] + T * 2048), zero, idx), idx));
            const f32x2 fx = fold(SMF64(ob, frag((R0[13] ^ 8192) + 49152 + 2048), SMF64(oa, frag(R0[13] + 49152 + 2048), zero, idx), idx));
            const int pb = 2 * (OFF_PART + (s & 1) * PART_BYTES);
            st128((u32x4){pack2(f[0].x, f[1].x), pack2(f[2].x, f[3].x), pack2(f[0].y, f[1].y), pack2(f[2].y, f[3].y)}, rs, R0[14], pb);
            st32(pack2(fx.x, fx.y), rs, R0[15], pb);
        }
        if (__builtin_expect((st & 2) || kv_rdy <= s, 0)) {
            while (kv_rdy <= s) { kv_rdy = prog_min(rw, F_KV + h * 512 + w * 128); if (--budget < 0) break; }
            fetch_kv(s + 1, opaque_lane());
        }
        if (st & 2) { S1[1] = read_canaries(s, rs, opaque_lane(), budget) + S1[6]; S2[2] += 0x10000; }
        if ((int)(S1[1] - S2[1]) > 0x8000) { S2[1] = 8 * now() + 16; S3[0] = (unsigned)__builtin_readcyclecounter(); }
        S1[0] = s + 1; S1[2] = kv_rdy; S1[3] = budget;
    }
    TT(true, 0)
    st32((unsigned)(NSEG - 2), rw, opaque_lane() == 0 && w == 0 ? fo : OOB, 0);
}

__global__ void __launch_bounds__(256, 1) fused_kernel(Args a) {
    extern __shared__ __attribute__((aligned(16))) char smem[];
    LAS char* lds = (LAS char*)smem;
    const unsigned lds_base = (unsigned)(uintptr_t)lds;
    const int w = __builtin_amdgcn_readfirstlane(threadIdx.x >> 6);
    const int b = blockIdx.x, bx = b & 7, bi = b >> 3;
    if (bi >= 28) {
        const void* pc; unsigned len;
        asm volatile("s_getpc_b64 %0\n1:\ts_mov_b32 %1, .Lstep_end-1b" : "=s"(pc), "=s"(len));
        touch(pc, min((bi - 28) * 32768 + threadIdx.x * 128, len), lds_base);
    }
    asm volatile("" :: "s"(a.Wq), "s"(a.cw));
    const rsrc_t rw = __builtin_amdgcn_make_buffer_rsrc(a.ws, 0, WS_USED, 0x00020000);
    int budget = SPIN_MAX;
#define RT(i)
    if (bx < 4) {
        if (bi < 16) role_head(a, lds, lds_base, 8 * bx + (bi >> 1), bi & 1, rw, budget);
        else role_owner(a, lds_base, 4 * (bi - 16) + bx, rw, budget);
    } else {
        const int fx = bx - 4, no = fx < 3 ? 14 : 22, j = 14 * fx + bi;
        if (bi >= no || (j & 2)) {
            const unsigned t = now() + a.d1;
            if (bi >= no) { if (w) prefetch(a, lds_base, bx, (bi - no) * 3 + w - 1, 3 * (32 - no)); }
            else {
                const int sh = (j >> 2) * 2 + (j & 1);
#pragma unroll
                for (int i = 0; i < INIT_SHARE / 4096; ++i) st128((u32x4){SENT, SENT, SENT, SENT}, rw, threadIdx.x * 16 + i * 4096, sh * INIT_SHARE);
#pragma unroll
                for (int i = 0; i < 4; ++i) st128((u32x4){SENT, SENT, SENT, SENT}, rw, threadIdx.x * 16 + (i & 1) * 4096, (i & 2 ? OFF_VM : OFF_KM) + FAST * 65536 + sh * 8192);
                const u32x2 tag = launch_tag(a);
                VM_DRAIN();
                WG_BARRIER();
                __builtin_amdgcn_raw_buffer_store_b64(tag, rw, threadIdx.x == 0 ? F_INIT + sh * 128 : OOB, 0, 16);
                RT(3)
            }
            (void)wait_until(t);
        }
        if (bi >= no) {
            const int t = 18 * fx + bi - no;
            role_s4d(a, lds, lds_base, t, rw, budget);
            RT(1)
            TT(true, 6)
            if (t < 9 * OUT_REP) role_outp(a, lds, lds_base, t % 9, t / 9, rw, budget);
        }
        else {
            if (!(j & 2)) role_kv(a, lds, lds_base, (j >> 2) * 2 + (j & 1), rw, budget);
            RT(1)
            TT(true, 6)
            role_qa(a, lds, lds_base, (j >> 2) * 2, (j & 2 ? 0 : FAST) + (j & 1), (j & 2 ? FAST : NSEG) - 2 + (j & 1), rw, budget);
        }
    }
    RT(2)
    TT(true, 5)
    if (budget < 0 && threadIdx.x == 0) st32(0x1000u + b, rw, F_ERR, 0);
}

static int g_c1 = 56, g_c1m = 52, g_c1x = 70, g_c2 = 364, g_b0 = 64, g_up = 16, g_fl = 8, g_dn = 3, g_d0 = 1680, g_da = 100, g_ua = 48, g_d1 = 300, g_rd = 34, g_cw = 69, g_e0 = 165, g_x1 = 35, g_x2 = 106, g_x3 = 126, g_x4 = 114, g_x5 = 148, g_x6 = 150, g_x7 = 50;
extern "C" void kernel_launch(void* const* d_in, const int* in_sizes, int n_in, void* d_out, int out_size, void* d_ws, size_t ws_size, hipStream_t stream) {
    Args a;
    a.x = (const u16*)d_in[0]; a.mem0 = (const u16*)d_in[1];
    a.Wq = (const u16*)d_in[2]; a.Wk = (const u16*)d_in[3]; a.Wv = (const u16*)d_in[4]; a.Wo = (const u16*)d_in[5];
    a.lAr = (const u16*)d_in[8]; a.Aim = (const u16*)d_in[9]; a.Cr = (const u16*)d_in[10]; a.Ci = (const u16*)d_in[11];
    a.ldt = (const u16*)d_in[12]; a.Dsk = (const u16*)d_in[13]; a.gw = (const u16*)d_in[14];
    a.out = (u16*)d_out;
    a.ws = (char*)(((uintptr_t)d_ws + ((2u << 20) - 1)) & ~(uintptr_t)((2u << 20) - 1));
    a.c1 = g_c1 | g_c1m << 8 | g_c1x << 16; a.c2 = g_c2; a.d0 = g_d0 + g_da; a.b0 = g_b0; a.d1 = g_d1; a.up = g_up | g_ua << 8 | g_fl << 16; a.dn = g_dn | g_d0 << 8; a.rd = 23 * g_rd - 300; a.cw = 8 * g_cw - 104; a.e0 = 8 * g_e0;
    a.xa = g_x1 | g_x2 << 8 | g_x5 << 16 | g_x7 << 24; a.xb = g_x6 * 0x101 | g_x3 << 16 | g_x4 << 24;
    (void)hipFuncSetAttribute((const void*)fused_kernel, hipFuncAttributeMaxDynamicSharedMemorySize, L_TOTAL);
    fused_kernel<<<256, 256, L_TOTAL, stream>>>(a);
}
```

```cpp
#include <hip/hip_runtime.h>
#include <hip/hip_bf16.h>
#include <stdint.h>

typedef unsigned short u16;
typedef __bf16 bf16x8 __attribute__((ext_vector_type(8)));
typedef __bf16 bf16x16 __attribute__((ext_vector_type(16)));
typedef _Float16 f16x2 __attribute__((ext_vector_type(2)));
typedef _Float16 f16x8 __attribute__((ext_vector_type(8)));
typedef short s16x4 __attribute__((ext_vector_type(4)));
typedef float f32x2 __attribute__((ext_vector_type(2)));
typedef float f32x4 __attribute__((ext_vector_type(4)));
typedef float f32x16 __attribute__((ext_vector_type(16)));
typedef unsigned u32x2 __attribute__((ext_vector_type(2)));
typedef unsigned u32x4 __attribute__((ext_vector_type(4)));
typedef unsigned u32x8 __attribute__((ext_vector_type(8)));
typedef unsigned u32x16 __attribute__((ext_vector_type(16)));
typedef unsigned u32x32 __attribute__((ext_vector_type(32)));
typedef short s16x8 __attribute__((ext_vector_type(8)));
#define LAS __attribute__((address_space(3)))
#define DI __device__ __forceinline__
typedef __amdgpu_buffer_rsrc_t rsrc_t;

constexpr int SEQ = 2048, DM = 576, NH = 32, DH = 128, HD = NH * DH, NM = 8, SEG = 32, NSEG = 64, NS = 128;
constexpr unsigned SENT = 0xFFFFFFFFu;
constexpr unsigned OOB = 0x7FFFFFF0u;
constexpr int SPIN_MAX = 1 << 20;

constexpr int OFF_PART = 0;
constexpr int PART_BYTES = 72 * 4096;
constexpr int OFF_MEMB = OFF_PART + 2 * PART_BYTES;
constexpr int MEMB_STRIDE = 4 * 16384;
constexpr int OFF_CAN = OFF_MEMB + 4 * MEMB_STRIDE;
constexpr int COMM_LOGICAL = OFF_CAN + 32768;
constexpr int COMM_BYTES = 2 * COMM_LOGICAL;
constexpr int F_ERR = COMM_BYTES;
constexpr int F_KV = F_ERR + 128;
constexpr int F_KVM = F_KV + 16384;
constexpr int F_O = F_KVM + 8192;
constexpr int F_H = F_O + 4096;
constexpr int F_S4 = F_H + 32768;
constexpr int F_DONE = F_S4 + 512;
constexpr int INIT_SHARE = 14 * 4096, INIT_BYTES = 32 * INIT_SHARE;
constexpr int F_INIT = INIT_BYTES;
constexpr int F_EPOCH = F_INIT + 24576;
static_assert(F_DONE + 128 <= INIT_BYTES, "");
static_assert(F_EPOCH + 128 <= (2 << 20), "hand-off region");
constexpr int OFF_TP = 2 << 20;
constexpr int OFF_KF = 6 << 20;
constexpr int OFF_VF = OFF_KF + SEQ * HD * 2;
constexpr int OFF_OF = OFF_VF + SEQ * HD * 2;
constexpr int OFF_KM = OFF_OF + SEQ * HD * 2;
constexpr int OFF_VM = OFF_KM + NSEG * NH * NM * DH * 2;
constexpr int FAST = NSEG - 6;
constexpr int LATE = NSEG - 2;
constexpr int OFF_BO = OFF_VM + NSEG * NH * NM * DH * 2;
constexpr int BO_ROW = 4352;
constexpr int OFF_GT = OFF_BO + DM * BO_ROW;
constexpr int OFF_DBG = OFF_GT + SEQ * 4;
constexpr int WS_USED = OFF_DBG + (4 << 20);

struct Args {
    const u16 *x, *mem0, *Wq, *Wk, *Wv, *Wo, *lAr, *Aim, *Cr, *Ci, *ldt, *Dsk, *gw;
    u16* out; char* ws;
    int c1, c2, d0, b0, d1, up, dn, rd, cw, e0;
    unsigned xa, xb;
};

DI float bf2f(u16 b) { return __uint_as_float((unsigned)b << 16); }
DI float bflo(unsigned w) { return __uint_as_float(w << 16); }
DI float bfhi(unsigned w) { return __uint_as_float(w & 0xFFFF0000u); }
DI u16 f2bf(float f) { return __builtin_bit_cast(u16, (__bf16)f); }
DI unsigned pack2(float lo, float hi) {
    typedef __bf16 bf16x2 __attribute__((ext_vector_type(2)));
    bf16x2 v = {(__bf16)lo, (__bf16)hi};
    return __builtin_bit_cast(unsigned, v);
}
#define WG_BARRIER() asm volatile("s_waitcnt lgkmcnt(0)\n\ts_barrier" ::: "memory")
#define VM_DRAIN() asm volatile("s_waitcnt vmcnt(0)" ::: "memory")
DI int cmap(int o) { return (o & 0x1FFF) | ((o >> 13) << 14); }
DI unsigned mx4(unsigned a, u32x4 v) { a = max(max(a, v.x), v.y); return max(max(a, v.z), v.w); }
DI unsigned now() { return (unsigned)__builtin_amdgcn_s_memrealtime(); }
DI unsigned wait_until(unsigned t) {
    unsigned v; int guard = 8192;
    do { v = now(); } while ((int)(t - v) >= 4 && --guard > 0);
    const int r = __builtin_amdgcn_readfirstlane((int)(t - v));
    asm volatile("s_cmp_lt_i32 %0, 1\n\ts_cbranch_scc1 1f\n\ts_nop 3\n\ts_cmp_lt_i32 %0, 2\n\ts_cbranch_scc1 1f\n\ts_nop 3\n\ts_cmp_lt_i32 %0, 3\n\ts_cbranch_scc1 1f\n\ts_nop 7\n1:" :: "s"(r) : "scc");
    return r > 0 ? t : v + 2;
}
DI void st128(u32x4 v, rsrc_t rs, int off, int soff) {
    __builtin_amdgcn_raw_buffer_store_b128(v, rs, off, soff, 16);
    asm volatile("s_nop 1" :: "v"(v) : "memory");
}
DI void st32(unsigned v, rsrc_t rs, int off, int soff) { __builtin_amdgcn_raw_buffer_store_b32(v, rs, off, soff, 16); }
DI u32x4 ld128(rsrc_t rs, int off, int soff) { return __builtin_amdgcn_raw_buffer_load_b128(rs, off, soff, 16); }
DI u32x2 ld64(rsrc_t rs, int off, int soff) { return __builtin_amdgcn_raw_buffer_load_b64(rs, off, soff, 16); }
DI unsigned ld32(rsrc_t rs, int off, int soff) { return __builtin_amdgcn_raw_buffer_load_b32(rs, off, soff, 16); }
DI f32x2 fold(f32x4 a) { return (f32x2){a[0], a[1]} + (f32x2){a[2], a[3]}; }
DI float dpp_xor1(float v) { return __int_as_float(__builtin_amdgcn_update_dpp(0, __float_as_int(v), 0xB1, 0xf, 0xf, true)); }
DI float red_sum_g(float v) {
    auto a = __builtin_amdgcn_permlane16_swap(__float_as_uint(v), __float_as_uint(v), false, false);
    v = __uint_as_float(a[0]) + __uint_as_float(a[1]);
    auto b = __builtin_amdgcn_permlane32_swap(__float_as_uint(v), __float_as_uint(v), false, false);
    return __uint_as_float(b[0]) + __uint_as_float(b[1]);
}
DI int wave_max32(int d) {
    d = max(d, __builtin_amdgcn_update_dpp(d, d, 0x121, 0xf, 0xf, false));
    d = max(d, __builtin_amdgcn_update_dpp(d, d, 0x122, 0xf, 0xf, false));
    d = max(d, __builtin_amdgcn_update_dpp(d, d, 0x124, 0xf, 0xf, false));
    d = max(d, __builtin_amdgcn_update_dpp(d, d, 0x128, 0xf, 0xf, false));
    return __builtin_amdgcn_readfirstlane(max(max(__builtin_amdgcn_readlane(d, 0), __builtin_amdgcn_readlane(d, 16)), max(__builtin_amdgcn_readlane(d, 32), __builtin_amdgcn_readlane(d, 48))));
}
DI u32x2 tr_read(LAS char* lds, int addr) { return __builtin_bit_cast(u32x2, __builtin_amdgcn_ds_read_tr16_b64_v4i16((LAS s16x4*)(lds + addr))); }
DI void wait_prog(rsrc_t rw, int off, bool act, int thr, int& budget) {
    for (;;) {
        const int v = (int)ld32(rw, off, 0);
        asm volatile("" ::: "memory");
        if (__all(!act || v >= thr)) break;
        if (--budget < 0) break;
        __builtin_amdgcn_s_sleep(1);
    }
}
DI u32x2 launch_tag(const Args& a) {
    unsigned long long z;
    asm volatile("s_load_dwordx2 %0, %1, 0x0\n\ts_waitcnt lgkmcnt(0)" : "=s"(z) : "s"(a.ws + F_EPOCH) : "memory");
    z += 0x9E3779B97F4A7C15ull;
    z = (z ^ (z >> 30)) * 0xBF58476D1CE4E5B9ull; z = (z ^ (z >> 27)) * 0x94D049BB133111EBull; z ^= z >> 31;
    return (u32x2){(unsigned)z, (unsigned)(z >> 32)};
}
DI int wait_init(rsrc_t rw, u32x2 tag, int& budget, int more = 0) {
    const int l = threadIdx.x & 63, o = F_INIT + (l < 32 ? l : l == 63 ? min(more, 32) : more) * 128;
    u32x2 v;
    for (;;) {
        v = ld64(rw, o, 0);
        asm volatile("" ::: "memory");
        if (__all(((v.x ^ tag.x) >> 3) == 0 && v.y == tag.y)) break;
        if (--budget < 0) break;
        __builtin_amdgcn_s_sleep(2);
    }
    return __builtin_amdgcn_readlane(v.x, 63) & 7;
}
DI void glds16(const void* base, int voff, unsigned lds_dst) {
    unsigned keep;
    asm volatile("s_mov_b32 %0, m0\n\ts_mov_b32 m0, %3\n\ts_nop 0\n\tglobal_load_lds_dwordx4 %1, %2\n\ts_mov_b32 m0, %0" : "=&s"(keep) : "v"(voff), "s"(base), "s"(lds_dst) : "memory");
}
DI void bdma(rsrc_t rs, int voff, int soff, unsigned lds_dst) {
    unsigned keep;
    asm volatile("s_mov_b32 %0, m0\n\ts_mov_b32 m0, %4\n\ts_nop 0\n\tbuffer_load_dwordx4 %1, %2, %3 offen sc1 lds\n\ts_mov_b32 m0, %0" : "=&s"(keep) : "v"(voff), "s"(rs), "s"(soff), "s"(lds_dst) : "memory");
}
DI int swz(int r) { return ((r & 3) ^ (((r >> 2) & 3) << 1) ^ (((r >> 4) & 1) << 2)) & 7; }
template <class F> DI u32x4 ring_offs(int w, int lane, F at) {
    u32x4 o;
#pragma unroll
    for (int j = 0; j < 4; ++j) { const int r = 16 * w + 4 * j + (lane >> 4); o[j] = at(r, (lane & 15) ^ (2 * swz(r))) - 1024 * j; }
    return o;
}
DI u32x4 ring_offs(int w, int lane, int ld) { return ring_offs(w, lane, [&](int r, int c) { return r * ld * 2 + c * 16; }); }
DI void ring_issue(const u16* src, u32x4 o, unsigned slot) {
    unsigned keep;
    asm volatile("s_mov_b32 %0, m0\n\ts_mov_b32 m0, %6\n\ts_nop 0\n\tglobal_load_lds_dwordx4 %1, %5\n\tglobal_load_lds_dwordx4 %2, %5 offset:1024\n\t"
                 "global_load_lds_dwordx4 %3, %5 offset:2048\n\tglobal_load_lds_dwordx4 %4, %5 offset:3072\n\ts_mov_b32 m0, %0"
                 : "=&s"(keep) : "v"(o.x), "v"(o.y), "v"(o.z), "v"(o.w), "s"(src), "s"(slot) : "memory");
}
#define PF_FIRST 2
constexpr int L_TOTAL = 163840, L_SINK = L_TOTAL - 256;
DI void touch(const void* base, int voff, unsigned lds_base) {
    unsigned keep;
    asm volatile("s_mov_b32 %0, m0\n\ts_mov_b32 m0, %3\n\ts_nop 0\n\tglobal_load_lds_dword %1, %2\n\ts_mov_b32 m0, %0" : "=&s"(keep) : "v"(voff), "s"(base), "s"(lds_base + L_SINK) : "memory");
}
DI void prefetch(const Args& a, unsigned lds_base, int x, int id, int n) {
    const int lane = threadIdx.x & 63;
    const int h0 = x < 4 ? 8 * x : (0x160E0800 >> (8 * (x - 4))) & 255, lpr = x == 5 ? 12 : x == 7 ? 20 : 16, per = 9 * lpr;
    const int first = PF_FIRST * lpr, end = (x < 4 ? 4 : 2) * per;
    const float rl = __builtin_amdgcn_rcpf((float)lpr);
    for (int i = first + id; i < end; i += n) {
        int m = (i >= per) + (i >= 2 * per) + (i >= 3 * per), j = i - m * per;
        if (x < 4 && m < 3) { const int b = i >> 4; m = b < 20 ? b >> 3 : min(b - 20, 2); j = (b < 16 ? b & 7 : b < 20 ? b - 12 : b < 23 ? 8 : b - 23) * 16 + (i & 15); }
        const int mm = m + (x >= 4);
        const int L = j * 64 + lane, row = (int)((L + 0.5f) * rl);
        touch(mm == 0 ? a.Wq : mm == 1 ? a.Wk : mm == 2 ? a.Wv : a.Wo, m == 3 ? h0 * (DH * DM * 2) + L * 128 : row * (HD * 2) + (L - row * lpr) * 128 + h0 * 256, lds_base);
    }
}
template <int N> DI void ring_wait() { asm volatile("s_waitcnt vmcnt(%0) lgkmcnt(0)\n\ts_barrier" :: "i"(N) : "memory"); }
template <int I> struct ic_t { static constexpr int value = I; };
template <int N, int I = 0, class F> DI void static_for(F&& f) { if constexpr (I < N) { f(ic_t<I>{}); static_for<N, I + 1>(f); } }
#define MFMA32(a, b, c) __builtin_amdgcn_mfma_f32_32x32x16_bf16(__builtin_bit_cast(bf16x8, a), __builtin_bit_cast(bf16x8, b), c, 0, 0, 0)
#define MFMA16(a, b, c) __builtin_amdgcn_mfma_f32_16x16x32_bf16(__builtin_bit_cast(bf16x8, a), __builtin_bit_cast(bf16x8, b), c, 0, 0, 0)

constexpr int S_CH = 9;
constexpr int S_XRS = 4096 + 32;
constexpr int S_XR = 0;
constexpr int S_RC = S_CH * S_XRS;
constexpr int S_RCS = 4192;
constexpr int S_KD = S_RC + 4 * S_RCS;
constexpr int S_K = S_KD + 48 * 128;
constexpr int S_KROW = 96;
constexpr int S_TAB = S_K + 79 * S_KROW;
constexpr int S_TROW = 544;
constexpr int S_RED = S_TAB + 57 * S_TROW;
constexpr int S_TOTAL = S_RED + 4 * 64 * 144;
static_assert(S_RC % 16 == 0 && S_TAB % 16 == 0 && S_RED % 16 == 0, "");
DI f32x2 crot(f32x2 z) { return (f32x2){-z.y, z.x}; }
DI f32x2 cmul(f32x2 a, f32x2 z) { return __builtin_elementwise_fma(a.yy, crot(z), a.xx * z); }
DI f32x2 cexp_p(float ar, float ai, float p) {
    const float x = ai * p;
    const float hi = x * 0.15915494309189535f;
    const float lo = __builtin_fmaf(x, 0.15915494309189535f, -hi) + x * 6.420638316725915e-9f;
    const float rev = (hi - __builtin_rintf(hi)) + lo;
    const float m = __builtin_amdgcn_exp2f(ar * p * 1.4426950408889634f);
    return (f32x2){m * __builtin_amdgcn_cosf(rev), m * __builtin_amdgcn_sinf(rev)};
}
#define FENCE() __builtin_amdgcn_sched_barrier(0)
#define TT(c, i)
DI void role_s4d(const Args& a, LAS char* lds, unsigned lds_base, int task, rsrc_t rw, int& budget) {
    const int tid = threadIdx.x, lane = tid & 63, w = __builtin_amdgcn_readfirstlane(tid >> 6);
    const int d0 = task * S_CH;
    touch(w == 0 ? a.lAr : w == 1 ? a.Aim : w == 2 ? a.Cr : a.Ci, min(d0 * (NS * 2) + min(lane, 2 * S_CH + 1) * 128, DM * NS * 2 - 4), lds_base);
    touch(w & 1 ? a.ldt : a.Dsk, min(lane * 128, DM * 2 - 4), lds_base);
#define S4S(i)
    {
        const rsrc_t rx = __builtin_amdgcn_make_buffer_rsrc((void*)a.x, 0, SEQ * DM * 2, 0x00020000);
        const unsigned sh = (d0 & 1) * 16;
        u32x4 v[8]; unsigned e[8];
#pragma unroll
        for (int i = 0; i < 8; ++i) {
            const int o = (tid + 256 * i) * (DM * 2) + (d0 & ~1) * 2;
            v[i] = __builtin_amdgcn_raw_buffer_load_b128(rx, o, 0, 0); e[i] = __builtin_amdgcn_raw_buffer_load_b32(rx, o, 16, 0);
        }
#pragma unroll
        for (int p = 0; p < 2; ++p) {
            const int tok = 32 * task + 8 * w + 4 * p + (lane >> 4);
            float acc = 0.f;
#pragma unroll
            for (int i = 0; i < 5; ++i) {
                const int ch = (lane & 15) + 16 * i;
                const u32x4 gv = ch < 72 ? *(const u32x4*)(a.gw + ch * 8) : (u32x4){0u, 0u, 0u, 0u};
                const u32x4 xv = *(const u32x4*)(a.x + (size_t)tok * DM + min(ch, 71) * 8);
#pragma unroll
                for (int j = 0; j < 4; ++j) acc += bflo(xv[j]) * bflo(gv[j]) + bfhi(xv[j]) * bfhi(gv[j]);
            }
            acc += __shfl_xor(acc, 1); acc += __shfl_xor(acc, 2); acc += __shfl_xor(acc, 4); acc += __shfl_xor(acc, 8);
            st32(__float_as_uint(1.0f / (1.0f + __expf(-acc))), rw, (lane & 15) == 0 ? OFF_GT + tok * 4 : OOB, 0);
        }
#pragma unroll
        for (int i = 0; i < 8; ++i) {
            const unsigned x5[5] = {__builtin_amdgcn_alignbit(v[i].y, v[i].x, sh), __builtin_amdgcn_alignbit(v[i].z, v[i].y, sh), __builtin_amdgcn_alignbit(v[i].w, v[i].z, sh),
                                    __builtin_amdgcn_alignbit(e[i], v[i].w, sh), e[i] >> sh};
#pragma unroll
            for (int c = 0; c < S_CH; ++c) *(LAS u16*)(lds + S_XR + c * S_XRS + (2047 - tid - 256 * i) * 2) = (u16)(x5[c >> 1] >> (16 * (c & 1)));
        }
    }
    if (tid < 8 * S_CH) *(LAS unsigned*)(lds + S_XR + (tid >> 3) * S_XRS + 4096 + (tid & 7) * 4) = 0;
    if (tid < 64) *(LAS unsigned*)(lds + S_RC + (tid >> 4) * S_RCS + 4096 + (tid & 15) * 4) = 0;
    for (int i = tid; i < 15 * S_KROW / 4; i += 256) *(LAS unsigned*)(lds + S_K + i * 4) = 0;
    int kd[8];
#pragma unroll
    for (int j = 0; j < 8; ++j) {
        const int p = (int)bf2f(f2bf((float)(8 * tid + j))), k = p >> 7, r = p & 7;
        const int row = p < 256 ? 8 * k + r : (p < 512 ? 8 + 4 * k + (r >> 1) : (p < 1024 ? 16 + 2 * k + (r >> 2) : 24 + k));
        kd[j] = S_KD + (row * 16 + ((p >> 3) & 15)) * 8;
    }
    const int n = tid & 127, nn = lane & 15, g = lane >> 4;
    u16 pA = a.lAr[d0 * NS + n], pI = a.Aim[d0 * NS + n], pCr = a.Cr[d0 * NS + n], pCi = a.Ci[d0 * NS + n], pT = a.ldt[d0], pD = a.Dsk[d0];
    WG_BARRIER();
    for (int ch = 0; ch < S_CH; ++ch) {
        S4S(0)
        const float dsk = bf2f(pD);
        {
            const u32x4 v = *(LAS u32x4*)(lds + S_XR + ch * S_XRS + tid * 16);
            const u32x2 e = *(LAS u32x2*)(lds + S_XR + ch * S_XRS + tid * 16 + 16);
            const unsigned s01 = __builtin_amdgcn_alignbit(v.y, v.x, 16), s12 = __builtin_amdgcn_alignbit(v.z, v.y, 16), s23 = __builtin_amdgcn_alignbit(v.w, v.z, 16);
            const unsigned s34 = __builtin_amdgcn_alignbit(e.x, v.w, 16), s45 = __builtin_amdgcn_alignbit(e.y, e.x, 16);
            *(LAS u32x4*)(lds + S_RC + tid * 16) = v;
            *(LAS u32x4*)(lds + S_RC + S_RCS + tid * 16) = (u32x4){s01, s12, s23, s34};
            *(LAS u32x4*)(lds + S_RC + 2 * S_RCS + tid * 16) = (u32x4){v.y, v.z, v.w, e.x};
            *(LAS u32x4*)(lds + S_RC + 3 * S_RCS + tid * 16) = (u32x4){s12, s23, s34, s45};
        }
        S4S(1)
        const float dt = bf2f(f2bf(expf(bf2f(pT))));
        const int sc = 140 - (wave_max32(max(pCr & 0x7FFF, pCi & 0x7FFF)) >> 7);
        {
            const float ar = -bf2f(f2bf(expf(bf2f(pA)))) * dt, ai = bf2f(pI) * dt;
            const f32x2 C = {__builtin_amdgcn_ldexpf(bf2f(pCr), sc), __builtin_amdgcn_ldexpf(bf2f(pCi), sc)};
            const int dn = min(d0 + ch + 1, DM - 1);
            pA = a.lAr[dn * NS + n]; pI = a.Aim[dn * NS + n]; pCr = a.Cr[dn * NS + n]; pCi = a.Ci[dn * NS + n]; pT = a.ldt[dn]; pD = a.Dsk[dn];
            const int to = S_TAB + n * 4;
            auto put = [&](int row, f32x2 v) { *(LAS f16x2*)(lds + to + row * S_TROW) = __builtin_convertvector(v, f16x2); };
            const f32x2 z128 = cexp_p(ar, ai, 128.f);
            if (w < 2) {
                f32x2 E[8], G[4];
                E[1] = cexp_p(ar, ai, 1.f); G[0] = C;
#pragma unroll
                for (int r = 2; r < 8; ++r) E[r] = cmul(E[r >> 1], E[r - (r >> 1)]);
#pragma unroll
                for (int k = 1; k < 4; ++k) G[k] = cmul(G[k - 1], z128);
#pragma unroll
                for (int k = 0; k < 4; ++k)
#pragma unroll
                    for (int r = 0; r < 8; r += 1 + (k >> 1)) put(k < 2 ? 8 * k + r : 8 + 4 * k + (r >> 1), r ? cmul(E[r], G[k]) : G[k]);
            } else {
                const f32x2 z4 = cexp_p(ar, ai, 4.f), z8 = cexp_p(ar, -ai, 8.f);
                f32x2 G = cmul(C, cexp_p(ar, ai, 512.f)), B = z8;
                put(41, (f32x2){1.f, 0.f});
#pragma unroll
                for (int k = 1; k < 16; ++k) {
                    put(41 + k, B); B = cmul(B, z8);
                    if (k < 5) { put(22 + 2 * k, G); put(23 + 2 * k, cmul(G, z4)); }
                    else if (k < 14) put(27 + k, G);
                    G = cmul(G, z128);
                }
            }
        }
        WG_BARRIER();
        S4S(2)
        {
            const int ko = (w & 1) * 256 + g * 16, t0 = w & 2;
            u32x4 av[2][4], bv[4];
#pragma unroll
            for (int j = 0; j < 4; ++j) {
                bv[j] = *(LAS u32x4*)(lds + S_TAB + (41 + nn) * S_TROW + ko + j * 64);
                av[0][j] = *(LAS u32x4*)(lds + S_TAB + min(16 * t0 + nn, 40) * S_TROW + ko + j * 64);
                av[1][j] = *(LAS u32x4*)(lds + S_TAB + (16 + nn) * S_TROW + ko + j * 64);
            }
            f32x4 acc[2] = {};
#pragma unroll
            for (int j = 0; j < 8; ++j) acc[j & 1] = __builtin_amdgcn_mfma_f32_16x16x32_f16(__builtin_bit_cast(f16x8, av[j & 1][j >> 1]), __builtin_bit_cast(f16x8, bv[j >> 1]), acc[j & 1], 0, 0, 0);
#pragma unroll
            for (int r = 0; r < 8; ++r) *(LAS float*)(lds + S_KD + (((r < 4 ? 16 * t0 : 16) + 4 * g + (r & 3)) * 16 + nn) * 8 + (w & 1) * 4) = __builtin_amdgcn_ldexpf(acc[r >> 2][r & 3] * dt, 1 - sc);
        }
        WG_BARRIER();
        S4S(3)
        {
            float kv[8];
#pragma unroll
            for (int j = 0; j < 8; ++j) { const f32x2 t = *(LAS f32x2*)(lds + kd[j]); kv[j] = t.x + t.y; }
            *(LAS u32x4*)(lds + S_K + (15 + (tid >> 2)) * S_KROW + (tid & 3) * 16) = (u32x4){pack2(kv[0], kv[1]), pack2(kv[2], kv[3]), pack2(kv[4], kv[5]), pack2(kv[6], kv[7])};
        }
        WG_BARRIER();
        S4S(4)
        {
            const int st0 = 2047 - nn + 8 * g - 32 * w, cc = st0 & 3;
            const int ub = S_RC + cc * S_RCS + (st0 - cc) * 2 - 3872, kb = S_K + (3 + nn - w) * S_KROW + g * 16;
            f32x4 y[4][2] = {};
            u32x2 U[2][16]; u32x4 F[2][4];
            auto rd = [&](int i, int f) {
                if (f < 4) F[i & 1][f] = *(LAS u32x4*)(lds + kb + (12 - 4 * i + 16 * f) * S_KROW);
                else U[i & 1][f - 4] = *(LAS u32x2*)(lds + ub + 3872 - 256 * i - 1024 * ((f - 4) >> 2) - 32 * ((f >> 1) & 1) + 8 * (f & 1));
            };
#pragma unroll
            for (int f = 0; f < 20; ++f) { rd(0, f); FENCE(); }
#pragma unroll
            for (int i = 0; i < 4; ++i) {
                int c = 0;
#pragma unroll
                for (int q = 0; q < 4; ++q)
#pragma unroll
                    for (int T = q; T < 4; ++T)
#pragma unroll
                        for (int bt = 0; bt < 2; ++bt) {
                            y[T][bt] = MFMA16(__builtin_shufflevector(U[i & 1][4 * q + 2 * bt], U[i & 1][4 * q + 2 * bt + 1], 0, 1, 2, 3), F[i & 1][T - q], y[T][bt]);
                            if (i < 3) rd(i + 1, c);
                            ++c;
                            FENCE();
                        }
            }
#pragma unroll
            for (int T = 0; T < 4; ++T)
#pragma unroll
                for (int bt = 0; bt < 2; ++bt) *(LAS f32x4*)(lds + S_RED + (w * 64 + 16 * T + nn) * 144 + bt * 64 + g * 16) = y[T][bt];
        }
        WG_BARRIER();
        S4S(5)
        {
            f32x4 y0 = {0, 0, 0, 0}, y1 = y0;
#pragma unroll
            for (int ww = 0; ww < 4; ++ww) {
                const int o = S_RED + (ww * 64 + (tid >> 2)) * 144 + (tid & 3) * 32;
                y0 += *(LAS f32x4*)(lds + o); y1 += *(LAS f32x4*)(lds + o + 16);
            }
            const u32x4 u = *(LAS u32x4*)(lds + S_XR + ch * S_XRS + (255 - tid) * 16);
            st128((u32x4){pack2(y0[0] + bfhi(u.w) * dsk, y0[1] + bflo(u.w) * dsk), pack2(y0[2] + bfhi(u.z) * dsk, y0[3] + bflo(u.z) * dsk),
                          pack2(y1[0] + bfhi(u.y) * dsk, y1[1] + bflo(u.y) * dsk), pack2(y1[2] + bfhi(u.x) * dsk, y1[3] + bflo(u.x) * dsk)}, rw, tid * 16, OFF_BO + (d0 + ch) * BO_ROW);
        }
        S4S(6)
    }
    wait_init(rw, launch_tag(a), budget);
    VM_DRAIN();
    WG_BARRIER();
    if (tid == 0) st32(1u, rw, F_S4 + task * 4, 0);
}

constexpr int XT_BYTES = 32 * 1152;
constexpr int P_XT = 0;
constexpr int P_STG = 2 * XT_BYTES;
struct WSlice { u32x4 f[36]; };
constexpr int P_RING = 5;
template <class F> DI void load_wslices(WSlice& W0, WSlice& W1, const u16* s0, const u16* s1, int w, int lane, LAS char* lds, unsigned lds_base, F also) {
    constexpr int NB = 18;
    const int r0 = 8 * (lane >> 5) + ((lane & 15) >> 2);
    const int cb = (4 * w + 2 * ((lane >> 4) & 1) + ((lane & 3) >> 1)), lo8 = (lane & 1) * 8;
    auto src = [&](int i) { return (i < 9 ? s0 : s1) + (size_t)(64 * (i % 9)) * HD; };
    const u32x4 ro = ring_offs(w, lane, HD);
#pragma unroll
    for (int i = 0; i < P_RING - 1; ++i) ring_issue(src(i), ro, lds_base + P_STG + i * 16384 + w * 4096);
    also();
    static_for<NB>([&](auto ic) {
        constexpr int i = decltype(ic)::value, rem = NB - 1 - i < P_RING - 2 ? NB - 1 - i : P_RING - 2;
        ring_wait<4 * rem>();
        if constexpr (i + P_RING - 1 < NB) ring_issue(src(i + P_RING - 1), ro, lds_base + P_STG + ((i + P_RING - 1) % P_RING) * 16384 + w * 4096);
        const int sl = P_STG + (i % P_RING) * 16384;
#pragma unroll
        for (int kk = 0; kk < 4; ++kk) {
            const int ra = r0 + 16 * kk, rb = ra + 4;
            const u32x2 lo = tr_read(lds, sl + ra * 256 + ((cb ^ (2 * swz(ra))) * 16) + lo8), hi = tr_read(lds, sl + rb * 256 + ((cb ^ (2 * swz(rb))) * 16) + lo8);
            (i < 9 ? W0 : W1).f[(i % 9) * 4 + kk] = (u32x4){lo.x, lo.y, hi.x, hi.y};
        }
        u32x4* f = (i < 9 ? W0 : W1).f + (i % 9) * 4;
        if constexpr (i < 16) asm volatile("" : "+a"(f[0]), "+a"(f[1]), "+a"(f[2]), "+a"(f[3]));
    });
}
DI u32x4 piece_pi(const f32x16& a, int qq) {
    return (u32x4){pack2(a[4 * qq], a[4 * qq + 8]), pack2(a[4 * qq + 1], a[4 * qq + 9]), pack2(a[4 * qq + 2], a[4 * qq + 10]), pack2(a[4 * qq + 3], a[4 * qq + 11])};
}
DI u32x4 piece_seq(const f32x16& a, int s2) {
    return (u32x4){pack2(a[8 * s2], a[8 * s2 + 1]), pack2(a[8 * s2 + 2], a[8 * s2 + 3]), pack2(a[8 * s2 + 4], a[8 * s2 + 5]), pack2(a[8 * s2 + 6], a[8 * s2 + 7])};
}
struct XTile {
    int xs[9], xa[4];
    DI void init(int w, int lane) {
#pragma unroll
        for (int i = 0; i < 9; ++i) { const int L = (i * 4 + w) * 64 + lane, r = L / 72, cs = L - 72 * r; xs[i] = r * 1152 + (cs ^ ((r >> 1) & 7)) * 16; }
        const int c32 = lane & 31, hf = lane >> 5;
#pragma unroll
        for (int j = 0; j < 4; ++j) xa[j] = P_XT + c32 * 1152 + (((2 * j + hf) ^ ((c32 >> 1) & 7)) * 16);
    }
    DI void dma(const Args& a, unsigned lds_base, int w, int s, int buf, int i) const {
        glds16((const char*)a.x + (size_t)min(s, NSEG - 1) * XT_BYTES, xs[i], lds_base + P_XT + buf * XT_BYTES + (i * 4 + w) * 1024);
    }
    DI void first(const Args& a, unsigned lds_base, int w, int lane, int s) {
        init(w, lane);
#pragma unroll
        for (int i = 0; i < 9; ++i) dma(a, lds_base, w, s, 0, i);
    }
};
template <class F> DI void project(f32x16& acc0, f32x16& acc1, const WSlice& W0, const WSlice& W1, const XTile& X, const Args& a, LAS char* lds, unsigned lds_base, int w, int buf, int sn, F gap) {
    const int bo = buf * XT_BYTES;
    constexpr int PD = 5;
    u32x4 xf[PD];
#pragma unroll
    for (int i = 0; i < PD; ++i) xf[i] = *(LAS u32x4*)(lds + X.xa[i & 3] + bo + (i >> 2) * 128);
    static_for<36>([&](auto ic) {
        constexpr int ks = decltype(ic)::value;
        if (ks == 0) asm volatile("v_mfma_f32_32x32x16_bf16 %0, %1, %2, 0" : "=&v"(acc0) : "a"(W0.f[0]), "v"(xf[0]) : "memory");
        else asm volatile("v_mfma_f32_32x32x16_bf16 %0, %1, %2, %0" : "+v"(acc0) : "a"(W0.f[ks]), "v"(xf[ks % PD]) : "memory");
        if (ks < 9) X.dma(a, lds_base, w, sn, buf ^ 1, ks);
        gap(ic_t<2 * ks>{});
        if (ks == 0) asm volatile("v_mfma_f32_32x32x16_bf16 %0, %1, %2, 0" : "=&v"(acc1) : "a"(W1.f[0]), "v"(xf[0]) : "memory");
        else if (ks < 28) asm volatile("v_mfma_f32_32x32x16_bf16 %0, %1, %2, %0" : "+v"(acc1) : "a"(W1.f[ks]), "v"(xf[ks % PD]) : "memory");
        else asm volatile("v_mfma_f32_32x32x16_bf16 %0, %1, %2, %0" : "+v"(acc1) : "v"(W1.f[ks]), "v"(xf[ks % PD]) : "memory");
        if (ks + PD < 36) xf[ks % PD] = *(LAS u32x4*)(lds + X.xa[(ks + PD) & 3] + bo + ((ks + PD) >> 2) * 128);
        gap(ic_t<2 * ks + 1>{});
    });
    asm volatile("s_nop 7\n\ts_nop 3" : "+v"(acc0), "+v"(acc1) :: "memory");
}
#define KV_T 7
#define KV_F 22
DI void role_kv(const Args& a, LAS char* lds, unsigned lds_base, int h, rsrc_t rw, int& budget) {
    const int tid = threadIdx.x, lane = tid & 63, w = __builtin_amdgcn_readfirstlane(tid >> 6);
    WSlice W0, W1;
    XTile X;
    u32x2 tag;
    load_wslices(W0, W1, a.Wk + h * DH, a.Wv + h * DH, w, lane, lds, lds_base, [&]() { X.first(a, lds_base, w, lane, 0); tag = launch_tag(a); });
    VM_DRAIN();
    WG_BARRIER();
    const int oo = h * 8192 + w * 2048 + lane * 16;
    const int fo = lane == 0 ? F_KV + h * 512 + w * 128 : OOB;
    u32x4 xf[6], P[4] = {};
    auto frag = [&](int buf, int ks) { xf[ks % 6] = *(LAS u32x4*)(lds + X.xa[ks & 3] + buf * XT_BYTES + (ks >> 2) * 128); };
    auto cvt = [&](const f32x16& k, const f32x16& v, int i) {
#pragma unroll
        for (int j = 0; j < 4; ++j) asm volatile("v_cvt_pk_bf16_f32 %0, %1, %2" : "=v"(P[i][j]) : "v"(i < 2 ? k[4 * i + j] : v[8 * i - 16 + 2 * j]), "v"(i < 2 ? k[4 * i + j + 8] : v[8 * i - 15 + 2 * j]));
    };
    auto put = [&](int o, int s, int i) { st128(P[i], rw, o + (i & 1) * 1024, (i < 2 ? OFF_KF : OFF_VF) + s * (NH * 8192)); };
    auto now_ = [&](f32x16& k, f32x16& v, int s) {
        asm volatile("s_nop 7\n\ts_nop 3" : "+v"(k), "+v"(v) :: "memory");
#pragma unroll
        for (int i = 0; i < 4; ++i) { cvt(k, v, i); put(oo, s, i); }
    };
    u32x2 in = {};
    auto seg = [&](f32x16& k, f32x16& v, const f32x16& pk, const f32x16& pv, int s, int buf) {
        const int o = s > 1 ? oo : OOB;
#pragma unroll
        for (int ks = 0; ks < 36; ++ks) {
            if (ks == 0) asm volatile("v_mfma_f32_32x32x16_bf16 %0, %1, %2, 0" : "=&v"(k) : "a"(W0.f[0]), "v"(xf[0]) : "memory");
            else asm volatile("v_mfma_f32_32x32x16_bf16 %0, %1, %2, %0" : "+v"(k) : "a"(W0.f[ks]), "v"(xf[ks % 6]) : "memory");
            if (ks >= 1 && ks < 5) cvt(pk, pv, ks - 1);
            if (buf && ks == KV_T && s == 1) {
                asm volatile("s_waitcnt vmcnt(%0)" :: "i"(4 + (KV_T - 4) / 2) : "memory");
                __builtin_amdgcn_raw_buffer_store_b64(tag, rw, lane == 0 ? F_INIT + (33 + 4 * h + w) * 128 : OOB, 0, 16);
                if (!__all(((in.x ^ tag.x) >> 1) == 0 && in.y == tag.y)) wait_init(rw, tag, budget);
            }
            if (ks >= 5 && ks < 13 && (ks & 1)) put(o, s - 1, (ks - 5) >> 1);
            if (ks == KV_F) { VM_DRAIN(); st32((unsigned)(s - 1), rw, s > 1 ? fo : OOB, 0); }
            if (ks == 0) asm volatile("v_mfma_f32_32x32x16_bf16 %0, %2, %1, 0" : "=&v"(v) : "a"(W1.f[0]), "v"(xf[0]) : "memory");
            else if (ks < 28) asm volatile("v_mfma_f32_32x32x16_bf16 %0, %2, %1, %0" : "+v"(v) : "a"(W1.f[ks]), "v"(xf[ks % 6]) : "memory");
            else asm volatile("v_mfma_f32_32x32x16_bf16 %0, %2, %1, %0" : "+v"(v) : "v"(W1.f[ks]), "v"(xf[ks % 6]) : "memory");
            if (ks == 30) WG_BARRIER();
            frag(buf ^ (ks >= 30), (ks + 6) % 36);
            if (ks < 4) X.dma(a, lds_base, w, s + 1, buf ^ 1, ks + 5);
            if (ks > 30) X.dma(a, lds_base, w, s + 2, buf, ks - 31);
        }
    };
#pragma unroll
    for (int i = 0; i < 6; ++i) frag(0, i);
#pragma unroll
    for (int i = 0; i < 5; ++i) X.dma(a, lds_base, w, 1, 1, i);
    f32x16 k0, v0, k1 = {}, v1 = {};
    for (int s = 0; s < NSEG; s += 2) {
        seg(k0, v0, k1, v1, s, 0);
        if (s == 0) { in = ld64(rw, F_INIT + (lane & 31) * 128, 0); now_(k0, v0, 0); }
        seg(k1, v1, k0, v0, s + 1, 1);
    }
    now_(k1, v1, NSEG - 1);
    wait_prog(rw, F_S4 + lane * 4, true, 1, budget);
    VM_DRAIN();
    st32(NSEG, rw, fo, 0);
}

DI int prog_min(rsrc_t rw, int off) {
    const int v = (int)ld32(rw, off, 0);
    asm volatile("" ::: "memory");
    return -wave_max32(-v);
}
DI float red_max_g(float v) {
    auto a = __builtin_amdgcn_permlane16_swap(__float_as_uint(v), __float_as_uint(v), false, false);
    v = fmaxf(__uint_as_float(a[0]), __uint_as_float(a[1]));
    auto b = __builtin_amdgcn_permlane32_swap(__float_as_uint(v), __float_as_uint(v), false, false);
    return fmaxf(__uint_as_float(b[0]), __uint_as_float(b[1]));
}
#define M16 "v_mfma_f32_16x16x32_bf16 "
DI void role_qa(const Args& a, LAS char* lds, unsigned lds_base, int h0, int s0, int s1, rsrc_t rw, int& budget) {
    const int tid = threadIdx.x, lane = tid & 63, w = __builtin_amdgcn_readfirstlane(tid >> 6);
    const int n = lane & 15, g = lane >> 4, h = h0 + (w >> 1), tt = w & 1;
    WSlice W0, W1;
    XTile X;
    load_wslices(W0, W1, a.Wq + h0 * DH, a.Wq + (h0 + 1) * DH, w, lane, lds, lds_base, [&]() { X.first(a, lds_base, w, lane, s0); });
    const int oo = OFF_OF + (2 * tt + (n >> 3)) * 65536 + h * 2048 + g * 128 + (n & 7) * 16;
#pragma unroll
    for (int t = 0; t < 4; ++t) st128((u32x4){SENT, SENT, SENT, SENT}, rw, oo + t * 512, s1 * 262144);
    VM_DRAIN();
    WG_BARRIER();
    constexpr int Q_Q = P_STG, Q_KV = Q_Q + 16384, Q_M = Q_KV + 32768;
    static_assert(Q_M + 16384 <= P_STG + P_RING * 16384, "");
    const int fk = F_KV + h * 512 + (lane & 3) * 128, fm = F_KVM + h * 256 + (lane & 1) * 128;
    int pm_, m_rdy = -1, kv_rdy = -1;
    auto look = [&]() { pm_ = (int)ld32(rw, lane & 4 ? fk : fm, 0); };
    auto seen = [&]() { auto rl = [&](int i) { return __builtin_amdgcn_readlane(pm_, i); }; m_rdy = min(rl(0), rl(1)); kv_rdy = min(min(rl(4), rl(5)), min(rl(6), rl(7))); };
    auto wait_m = [&](int thr, unsigned& t) {
        for (;;) {
            const u32x2 v = ld64(rw, fm, 0);
            asm volatile("" ::: "memory");
            t = now();
            const int p0 = __builtin_amdgcn_readlane(v.x, 0), p1 = __builtin_amdgcn_readlane(v.x, 1);
            const int d0 = (int)(__builtin_amdgcn_readlane(v.y, 0) - 8 * t) >> 3, d1 = (int)(__builtin_amdgcn_readlane(v.y, 1) - 8 * t) >> 3;
            if (min(p0, p1) >= thr || --budget < 0) return max(p0 == thr ? d0 : -1000, p1 == thr ? d1 : -1000);
            (void)wait_until(t + min(max(min(d0, d1) + (int)(a.xa >> 16 & 255), 10), 200));
        }
    };
    auto proj = [&](int s, auto gap) {
        f32x16 acc0, acc1;
        project(acc0, acc1, W0, W1, X, a, lds, lds_base, w, ((s - s0) >> 1) & 1, s + 2, [&](auto ic) { gap(ic); if (decltype(ic)::value == 55) look(); });
        if (s >= LATE) {
            const int o = OFF_OF + h0 * 8192 + w * 2048 + lane * 16;
            st128(piece_pi(acc0, 0), rw, o, s * 262144); st128(piece_pi(acc0, 1), rw, o + 1024, s * 262144);
            st128(piece_pi(acc1, 0), rw, o + 8192, s * 262144); st128(piece_pi(acc1, 1), rw, o + 8192 + 1024, s * 262144);
            return;
        }
        const int o = Q_Q + w * 2048 + lane * 16;
        *(LAS u32x4*)(lds + o) = piece_pi(acc0, 0); *(LAS u32x4*)(lds + o + 1024) = piece_pi(acc0, 1);
        *(LAS u32x4*)(lds + o + 8192) = piece_pi(acc1, 0); *(LAS u32x4*)(lds + o + 8192 + 1024) = piece_pi(acc1, 1);
    };
    const int fs = (tt ? OFF_VF : OFF_KF) + (g >> 1) * 1024 + ((g & 1) * 32 + 16 * (n >> 3) + (n & 7)) * 16;
    auto fetch = [&](int s) {
        const int so = (min(s, NSEG - 1) * NH + h) * 8192;
#pragma unroll
        for (int f = 0; f < 8; ++f) bdma(rw, fs, so + (f >> 1) * 2048 + (f & 1) * 128, lds_base + Q_KV + w * 8192 + f * 1024);
    };
    const int ms = OFF_KM + (lane & 7) * 256 + (lane >> 3) * 16;
    auto fetch_m = [&](int s) {
        const int so = (s * NH + h) * 2048;
        const unsigned d = lds_base + Q_M + w * 4096;
        bdma(rw, ms, so, d); bdma(rw, ms, so + 128, d + 1024); bdma(rw, lane * 16, OFF_VM + so, d + 2048); bdma(rw, lane * 16, OFF_VM + so + 1024, d + 3072);
    };
    const float c = 0.08838834764831845f * 1.4426950408889634f;
    u32x4 qf[4];
    f32x4 o[8]; float mx, l;
    auto xpart = [&]() {
        const int kb = Q_KV + (w >> 1) * 16384 + lane * 16;
        u32x4 kn[8], vn[8];
#pragma unroll
        for (int ks = 0; ks < 4; ++ks) qf[ks] = *(LAS u32x4*)(lds + Q_Q + (w >> 1) * 8192 + (2 * ks + (g >> 1)) * 1024 + ((g & 1) * 32 + 16 * tt + n) * 16);
#pragma unroll
        for (int f = 0; f < 8; ++f) kn[f] = *(LAS u32x4*)(lds + kb + f * 1024);
        FENCE();
#pragma unroll
        for (int f = 0; f < 8; ++f) vn[f] = *(LAS u32x4*)(lds + kb + 8192 + f * 1024);
        f32x4 s0, s1;
        asm volatile(M16 "%0, %2, %10, 0\n\t" M16 "%1, %3, %10, 0\n\t" M16 "%0, %4, %11, %0\n\t" M16 "%1, %5, %11, %1\n\t"
                     M16 "%0, %6, %12, %0\n\t" M16 "%1, %7, %12, %1\n\t" M16 "%0, %8, %13, %0\n\t" M16 "%1, %9, %13, %1\n\ts_nop 7"
                     : "=&v"(s0), "=&v"(s1) : "v"(kn[0]), "v"(kn[1]), "v"(kn[2]), "v"(kn[3]), "v"(kn[4]), "v"(kn[5]), "v"(kn[6]), "v"(kn[7]), "v"(qf[0]), "v"(qf[1]), "v"(qf[2]), "v"(qf[3]));
        float p[8];
        mx = -3.0e38f; l = 0.f;
#pragma unroll
        for (int r = 0; r < 8; ++r) { const int key = 16 * (g >> 1) + 4 * (g & 1) + (r & 3) + 8 * (r >> 2); p[r] = key <= 16 * tt + n ? (r < 4 ? s0[r & 3] : s1[r & 3]) : -3.0e38f; mx = fmaxf(mx, p[r]); }
        mx = red_max_g(mx);
#pragma unroll
        for (int r = 0; r < 8; ++r) { p[r] = __builtin_amdgcn_exp2f((p[r] - mx) * c); l += p[r]; }
        const u32x4 pf = {pack2(p[0], p[1]), pack2(p[2], p[3]), pack2(p[4], p[5]), pack2(p[6], p[7])};
        asm volatile("s_nop 1\n\t" M16 "%0, %8, %16, 0\n\t" M16 "%1, %9, %16, 0\n\t" M16 "%2, %10, %16, 0\n\t" M16 "%3, %11, %16, 0\n\t"
                     M16 "%4, %12, %16, 0\n\t" M16 "%5, %13, %16, 0\n\t" M16 "%6, %14, %16, 0\n\t" M16 "%7, %15, %16, 0\n\ts_nop 7"
                     : "=&v"(o[0]), "=&v"(o[1]), "=&v"(o[2]), "=&v"(o[3]), "=&v"(o[4]), "=&v"(o[5]), "=&v"(o[6]), "=&v"(o[7])
                     : "v"(vn[0]), "v"(vn[1]), "v"(vn[2]), "v"(vn[3]), "v"(vn[4]), "v"(vn[5]), "v"(vn[6]), "v"(vn[7]), "v"(pf));
    };
    const int fo = lane == 0 ? F_O + h * 128 + (2 * s0 + tt) * 4 : OOB;
    bool pre = false;
    auto both = [&](int s) {
        const int kb = Q_KV + (w >> 1) * 16384 + lane * 16;
        u32x4 kn[8], vn[8], pf;
        f32x4 sc[2];
        float p[8];
        int v0, v1, v2;
        asm volatile("buffer_load_dword %0, %1, %2, 0 offen sc1" : "=v"(pm_) : "v"(lane & 4 ? fk : fm), "s"(rw) : "memory");
#define PIN(...) asm volatile("" : __VA_ARGS__)
        proj(s + 4, [&](auto ic) {
            constexpr int i = decltype(ic)::value;
            if constexpr (i < 8) {
                if (!(i & 1)) qf[i >> 1] = *(LAS u32x4*)(lds + Q_Q + (w >> 1) * 8192 + (i + (g >> 1)) * 1024 + ((g & 1) * 32 + 16 * tt + n) * 16);
                kn[i] = *(LAS u32x4*)(lds + kb + i * 1024);
            }
            if constexpr (i >= 16 && i < 32 && !(i & 1)) vn[(i - 16) >> 1] = *(LAS u32x4*)(lds + kb + 8192 + (i - 16) * 512);
            if constexpr (i == 5 || i == 6) asm volatile(M16 "%0, %1, %2, 0" : "=&v"(sc[i - 5]) : "v"(kn[i - 5]), "v"(qf[0]));
            if constexpr (i > 6 && i < 13) asm volatile(M16 "%0, %1, %2, %0" : "+v"(sc[(i - 5) & 1]) : "v"(kn[i - 5]), "v"(qf[(i - 5) >> 1]));
            if constexpr (i == 15 || i == 16) {
                constexpr int r = 4 * (i - 15);
                PIN("+v"(sc[i - 15]));
#pragma unroll
                for (int j = 0; j < 4; ++j) p[r + j] = 16 * (g >> 1) + 4 * (g & 1) + j + 2 * r <= 16 * tt + n ? sc[i - 15][j] : -3.0e38f;
                PIN("+v"(p[r]), "+v"(p[r + 1]), "+v"(p[r + 2]), "+v"(p[r + 3]));
            }
            if constexpr (i == 17) { mx = fmaxf(fmaxf(fmaxf(fmaxf(p[0], p[1]), p[2]), fmaxf(fmaxf(p[3], p[4]), p[5])), fmaxf(p[6], p[7])); PIN("+v"(mx)); }
            if constexpr (i == 18) { auto x = __builtin_amdgcn_permlane16_swap(__float_as_uint(mx), __float_as_uint(mx), false, false); mx = fmaxf(__uint_as_float(x[0]), __uint_as_float(x[1])); PIN("+v"(mx)); }
            if constexpr (i == 19) { auto x = __builtin_amdgcn_permlane32_swap(__float_as_uint(mx), __float_as_uint(mx), false, false); mx = fmaxf(__uint_as_float(x[0]), __uint_as_float(x[1])); PIN("+v"(mx)); }
            if constexpr (i >= 20 && i < 24) {
                constexpr int r = 2 * (i - 20);
                PIN("+v"(p[r]), "+v"(p[r + 1]));
                p[r] = __builtin_amdgcn_exp2f((p[r] - mx) * c); p[r + 1] = __builtin_amdgcn_exp2f((p[r + 1] - mx) * c);
                PIN("+v"(p[r]), "+v"(p[r + 1]));
            }
            if constexpr (i == 24) { pf = (u32x4){pack2(p[0], p[1]), pack2(p[2], p[3]), pack2(p[4], p[5]), pack2(p[6], p[7])}; PIN("+v"(pf)); }
            if constexpr (i == 25) { l = ((p[0] + p[1]) + p[2]) + p[3]; PIN("+v"(l)); }
            if constexpr (i == 26) { l = (((l + p[4]) + p[5]) + p[6]) + p[7]; PIN("+v"(l)); }
            if constexpr (i == 27) { asm volatile("s_waitcnt vmcnt(9)" : "+v"(pm_) :: "memory"); st32((unsigned)s, rw, fo, 0); }
            if constexpr (i == 29) { seen(); pre = m_rdy >= s + 2 && kv_rdy >= s + 4; }
            if constexpr (i == 31) { v0 = pre ? ms : OOB; v1 = pre ? lane * 16 : OOB; v2 = pre ? fs : OOB; PIN("+v"(v0), "+v"(v1), "+v"(v2)); }
            if constexpr (i >= 28 && i < 36) asm volatile(M16 "%0, %1, %2, 0" : "=&v"(o[i - 28]) : "v"(vn[i - 28]), "v"(pf));
            if constexpr (i == 36) asm volatile("s_barrier" ::: "memory");
            if constexpr (i >= 38 && i < 42) bdma(rw, i < 40 ? v0 : v1, ((s + 2) * NH + h) * 2048 + (i < 40 ? (i & 1) * 128 : OFF_VM + (i & 1) * 1024), lds_base + Q_M + w * 4096 + (i - 38) * 1024);
            if constexpr (i >= 42 && i < 50) bdma(rw, v2, ((s + 4) * NH + h) * 8192 + ((i - 42) >> 1) * 2048 + (i & 1) * 128, lds_base + Q_KV + w * 8192 + (i - 42) * 1024);
        });
    };
    auto none = [](auto) {};
    proj(s0, none);
    if (s0 < FAST) wait_init(rw, launch_tag(a), budget);
    wait_prog(rw, fk, true, s0, budget);
    fetch(s0);
    VM_DRAIN();
    WG_BARRIER();
    xpart();
    WG_BARRIER();
    proj(s0 + 2, none);
    for (int s = s0; s <= s1; s += 2) {
        S4S(0)
        const int mb = Q_M + w * 4096;
        u32x4 kf[4]; u32x2 vm[4];
        auto frags = [&]() {
#pragma unroll
            for (int ks = 0; ks < 4; ++ks) kf[ks] = *(LAS u32x4*)(lds + mb + (4 * ks + g) * 128 + (2 * (n >> 2) + (n & 1)) * 16);
#pragma unroll
            for (int t = 0; t < 4; ++t) { const int vo = mb + 2048 + t * 512 + (n >> 3) * 256 + g * 64 + (n & 7) * 4; vm[t] = (u32x2){*(LAS unsigned*)(lds + vo), *(LAS unsigned*)(lds + vo + 32)}; }
        };
        if (s == s1) {
            TT(true, 3)
            unsigned t;
            const int d = min(wait_m(s - 1, t) + (int)(a.xa & 255), 500);
            st32((t + max(d, 0) + (a.xb >> 8 * ((s - FAST) >> 1) & 255)) & 0x7FFFFFFFu, rw, lane == 0 ? F_H + ((2 * (s - FAST) + tt) * NH + h) * 128 : OOB, 0);
            (void)wait_until(t + d);
            TT(true, 0)
            for (;;) {
                fetch_m(s);
                VM_DRAIN();
                frags();
                unsigned c = 0;
#pragma unroll
                for (int i = 0; i < 4; ++i) c = max(max(mx4(c, kf[i]), vm[i].x), vm[i].y);
                if (!__any(c == SENT) || --budget < 0) break;
            }
        } else {
            if (!pre) {
                seen();
                unsigned t;
                if (m_rdy < s) { const int d = wait_m(s, t); (void)wait_until(t + min(d + (int)(a.xa >> 24), 300)); }
                fetch_m(s);
                if (kv_rdy < s + 2) wait_prog(rw, fk, true, s + 2, budget);
                fetch(s + 2);
            }
            S4S(1)
            asm volatile("s_waitcnt vmcnt(8)" ::: "memory");
            frags();
        }
        TT(s == s1, 1)
        S4S(2)
        f32x4 sm;
        asm volatile(M16 "%0, %1, %5, 0\n\t" M16 "%0, %2, %6, %0\n\t" M16 "%0, %3, %7, %0\n\t" M16 "%0, %4, %8, %0\n\ts_nop 7"
                     : "=&v"(sm) : "v"(kf[0]), "v"(kf[1]), "v"(kf[2]), "v"(kf[3]), "v"(qf[0]), "v"(qf[1]), "v"(qf[2]), "v"(qf[3]));
        const float m2 = red_max_g(fmaxf(mx, fmaxf(sm[0], sm[1])));
        const float al = __builtin_amdgcn_exp2f((mx - m2) * c), p0 = __builtin_amdgcn_exp2f((sm[0] - m2) * c), p1 = __builtin_amdgcn_exp2f((sm[1] - m2) * c);
        const float inv = __builtin_amdgcn_rcpf(red_sum_g(l * al + p0 + p1)), ai = al * inv;
#pragma unroll
        for (int t = 0; t < 8; ++t) o[t] *= ai;
        const unsigned pm = pack2(p0 * inv, p1 * inv);
#define M1K "v_mfma_f32_16x16x16_bf16 "
        asm volatile("s_nop 1\n\t" M1K "%0, %8, %12, %0\n\t" M1K "%1, %8, %13, %1\n\t" M1K "%2, %9, %12, %2\n\t" M1K "%3, %9, %13, %3\n\t"
                     M1K "%4, %10, %12, %4\n\t" M1K "%5, %10, %13, %5\n\t" M1K "%6, %11, %12, %6\n\t" M1K "%7, %11, %13, %7\n\ts_nop 7"
                     : "+v"(o[0]), "+v"(o[1]), "+v"(o[2]), "+v"(o[3]), "+v"(o[4]), "+v"(o[5]), "+v"(o[6]), "+v"(o[7])
                     : "v"(vm[0]), "v"(vm[1]), "v"(vm[2]), "v"(vm[3]), "v"((u32x2){pm, 0u}), "v"((u32x2){0u, pm}));
#pragma unroll
        for (int b = 0; b < 2; ++b) {
            u32x4 x, y;
#pragma unroll
            for (int j = 0; j < 4; ++j) { x[j] = pack2(o[4 * b + (j >> 1)][2 * (j & 1)], o[4 * b + (j >> 1)][2 * (j & 1) + 1]); y[j] = pack2(o[4 * b + 2 + (j >> 1)][2 * (j & 1)], o[4 * b + 2 + (j >> 1)][2 * (j & 1) + 1]); }
            if (s == LATE - 1) { const u32x4 z = {x.x, x.z, y.x, y.z}; y = (u32x4){x.y, x.w, y.y, y.w}; x = z; }
            st128(x, rw, oo + b * 1024, s * 262144); st128(y, rw, oo + b * 1024 + 512, s * 262144);
        }
        TT(s == s1, 2)
        S4S(3)
        if (s == s1) break;
        asm volatile("s_waitcnt vmcnt(4)" ::: "memory");
        WG_BARRIER();
        S4S(4)
        if (s + 4 <= s1) { both(s); S4S(5) continue; }
        xpart();
        FENCE();
        VM_DRAIN();
        st32((unsigned)s, rw, fo, 0);
        TT(true, 4)
    }
}

#define STEP_ASM \
    "v_readfirstlane_b32 s36, v0\n" \
    "v_readfirstlane_b32 s37, v1\n" \
    "v_readfirstlane_b32 s38, v2\n" \
    "v_readfirstlane_b32 s39, v3\n" \
    "v_readfirstlane_b32 s40, v4\n" \
    "v_readfirstlane_b32 s41, v5\n" \
    "v_readfirstlane_b32 s42, v6\n" \
    "v_readfirstlane_b32 s43, v7\n" \
    "v_readfirstlane_b32 s28, v8\n" \
    "v_readfirstlane_b32 s29, v9\n" \
    "v_readfirstlane_b32 s30, v10\n" \
    "v_readfirstlane_b32 s31, v11\n" \
    "v_readfirstlane_b32 s32, v12\n" \
    "v_readfirstlane_b32 s33, v13\n" \
    "v_readfirstlane_b32 s34, v14\n" \
    "v_readfirstlane_b32 s35, v15\n" \
    "v_readfirstlane_b32 s24, v16\n" \
    "v_readfirstlane_b32 s25, v17\n" \
    "v_readfirstlane_b32 s26, v18\n" \
    "v_readfirstlane_b32 s27, v19\n" \
    "v_readfirstlane_b32 s60, v24\n" \
    "v_readfirstlane_b32 s61, v25\n" \
    "v_readfirstlane_b32 s62, v26\n" \
    "v_readfirstlane_b32 s63, v27\n" \
    "v_readfirstlane_b32 s64, v28\n" \
    "v_readfirstlane_b32 s65, v29\n" \
    "v_readfirstlane_b32 s66, v30\n" \
    "v_readfirstlane_b32 s67, v31\n" \
    "s_mov_b32 s68, m0\n" \
    "s_getpc_b64 s[96:97]\n" \
    "14:\n" \
    "s_add_u32 s96, s96, 2f-14b\n" \
    "s_addc_u32 s97, s97, 0\n" \
    "s_lshl_b32 s74, s60, 10\n" \
    "s_lshl_b32 s95, s74, 1\n" \
    "s_add_i32 s95, s95, 16384\n" \
    "s_mov_b32 s81, 0x3e0293ee\n" \
    "s_mov_b32 s82, 0x53800000\n" \
    "s_lshl_b32 s83, s60, 6\n" \
    "s_and_b32 s83, s83, 128\n" \
    "v_add_u32 v109, s83, v131\n" \
    "v_sub_u32 v111, v131, s83\n" \
    "v_xor_b32 v112, 0x2000, v133\n" \
    "v_and_b32 v113, 0x3c0, v127\n" \
    "v_mad_u32_u24 v113, v113, 7, v127\n" \
    "s_bfe_u32 s83, s61, 0x2000b\n" \
    "s_and_b32 s83, s83, 3\n" \
    "v_lshl_add_u32 v113, s83, 7, v113\n" \
    "v_lshlrev_b32 v114, 1, v127\n" \
    "v_add_u32 v106, s63, v133\n" \
    "s_bfe_u32 s83, s61, 0x40009\n" \
    "s_bfe_u32 s80, s61, 0x20004\n" \
    "s_lshl2_add_u32 s83, s83, s80\n" \
    "s_mov_b64 s[72:73], 0\n" \
    "s_cmp_eq_u32 s43, 4\n" \
    "s_cbranch_scc1 20f\n" \
    "0:\n" \
    "s_and_b32 s75, s36, 3\n" \
    "s_lshl_b32 s75, s75, 17\n" \
    "s_add_i32 s75, s75, s26\n" \
    "s_add_i32 s69, s75, s74\n" \
    "s_add_i32 s70, s69, 4096\n" \
    "s_add_i32 s71, s75, 16384\n" \
    "12:\n" \
    "ds_read_b128 v[72:75], v133 offset:0\n" \
    "ds_read_b128 v[76:79], v133 offset:1024\n" \
    "ds_read_b128 v[80:83], v133 offset:2048\n" \
    "ds_read_b128 v[84:87], v133 offset:3072\n" \
    "ds_read_b128 v[88:91], v133 offset:16384\n" \
    "ds_read_b128 v[92:95], v133 offset:17408\n" \
    "s_add_i32 s91, s36, 1\n" \
    "s_and_b32 s78, s36, 3\n" \
    "s_lshl_b32 s78, s78, 14\n" \
    "s_add_i32 s77, s78, s61\n" \
    "s_bitcmp1_b32 s36, 0\n" \
    "s_cselect_b32 s79, 0x90000, 0\n" \
    "s_lshl_b32 s92, s91, 18\n" \
    "s_add_i32 s92, s92, s64\n" \
    "s_movk_i32 m0, 0x2000\n" \
    "s_add_i32 s78, s78, 0x1a0000\n" \
    "s_add_i32 s93, s92, 0x1000000\n" \
    "v_mov_b64 v[40:41], 0\n" \
    "v_mov_b64 v[42:43], 0\n" \
    "v_mov_b64 v[44:45], 0\n" \
    "v_mov_b64 v[46:47], 0\n" \
    "v_mov_b64 v[48:49], 0\n" \
    "v_mov_b64 v[50:51], 0\n" \
    "v_mov_b64 v[52:53], 0\n" \
    "v_mov_b64 v[54:55], 0\n" \
    "v_mov_b64 v[56:57], 0\n" \
    "v_mov_b64 v[58:59], 0\n" \
    "v_mov_b64 v[60:61], 0\n" \
    "v_mov_b64 v[62:63], 0\n" \
    "v_mov_b64 v[64:65], 0\n" \
    "v_mov_b64 v[66:67], 0\n" \
    "v_mov_b64 v[68:69], 0\n" \
    "v_mov_b64 v[70:71], 0\n" \
    "v_add_u32 v108, s74, v127\n" \
    "v_xor_b32 v24, 0x400, v108\n" \
    "v_xor_b32 v28, 0x800, v108\n" \
    "v_xor_b32 v32, 0xc00, v108\n" \
    "s_sub_i32 s76, s33, s42\n" \
    "s_ashr_i32 s76, s76, s32\n" \
    "s_add_i32 s42, s42, s76\n" \
    "s_lshr_b32 s76, s32, 8\n" \
    "s_sub_i32 s76, s76, s40\n" \
    "s_ashr_i32 s76, s76, 2\n" \
    "s_add_i32 s40, s40, s76\n" \
    "s_sub_i32 s76, s40, s24\n" \
    "s_min_i32 s94, s91, s38\n" \
    "s_lshl_b32 s94, s94, 18\n" \
    "s_andn2_b32 s80, 0x1000000, s66\n" \
    "s_add_i32 s94, s94, s64\n" \
    "s_add_i32 s94, s94, s80\n" \
    "s_cmp_eq_u32 s36, 0\n" \
    "s_cbranch_scc1 17f\n" \
    "s_sub_i32 s90, s37, s29\n" \
    "s_mul_i32 s90, s90, s25\n" \
    "s_ashr_i32 s90, s90, 12\n" \
    "s_add_i32 s90, s90, s24\n" \
    "s_add_i32 s90, s90, -116\n" \
    "1:\n" \
    "s_memtime vcc\n" \
    "s_waitcnt lgkmcnt(0)\n" \
    "s_sub_i32 s75, s90, vcc_lo\n" \
    "s_cmp_gt_i32 s75, 112\n" \
    "s_cbranch_scc1 1b\n" \
    "s_max_i32 s75, s75, 0\n" \
    "s_and_b32 s75, s75, -4\n" \
    "s_sub_u32 vcc_lo, s96, s75\n" \
    "s_subb_u32 vcc_hi, s97, 0\n" \
    "s_setpc_b64 vcc\n" \
    "s_nop 0\n" \
    "s_nop 0\n" \
    "s_nop 0\n" \
    "s_nop 0\n" \
    "s_nop 0\n" \
    "s_nop 0\n" \
    "s_nop 0\n" \
    "s_nop 0\n" \
    "s_nop 0\n" \
    "s_nop 0\n" \
    "s_nop 0\n" \
    "s_nop 0\n" \
    "s_nop 0\n" \
    "s_nop 0\n" \
    "s_nop 0\n" \
    "s_nop 0\n" \
    "s_nop 0\n" \
    "s_nop 0\n" \
    "s_nop 0\n" \
    "s_nop 0\n" \
    "s_nop 0\n" \
    "s_nop 0\n" \
    "s_nop 0\n" \
    "s_nop 0\n" \
    "s_nop 0\n" \
    "s_nop 0\n" \
    "s_nop 0\n" \
    "s_nop 0\n" \
    "2:\n" \
    "buffer_load_dwordx4 v[0:3], v125, %[rs], s69 offen sc1\n" \
    "s_nop 3\n" \
    "s_nop 3\n" \
    "buffer_load_dwordx4 v[16:19], v125, %[rs], s70 offen sc1\n" \
    "s_mov_b32 exec_lo, s27\n" \
    "s_mov_b32 exec_hi, s27\n" \
    "buffer_load_dwordx4 v127, %[rs], s71 offen sc1 lds\n" \
    "s_mov_b64 exec, -1\n" \
    "v_bfe_u32 v20, v127, 8, 2\n" \
    "v_subrev_u32 v20, s60, v20\n" \
    "v_mul_i32_i24 v20, -1088, v20\n" \
    "v_add_u32 v20, v20, v129\n" \
    "v_lshrrev_b32 v21, 2, v127\n" \
    "s_mov_b32 exec_lo, s66\n" \
    "s_mov_b32 exec_hi, s66\n" \
    "ds_read2_b32 v[96:97], v20 offset0:0 offset1:144\n" \
    "s_mov_b64 exec, -1\n" \
    "s_lshl_b32 s80, s36, 16\n" \
    "s_add_i32 s80, s80, s67\n" \
    "s_cmp_eq_u32 s60, 0\n" \
    "s_cselect_b32 s72, 0x1010101, 0\n" \
    "s_waitcnt lgkmcnt(0)\n" \
    "buffer_store_dword v96, v21, %[rw], s80 offen sc1\n" \
    "buffer_store_dword v97, v21, %[rw], s80 offen offset:256 sc1\n" \
    "3:\n" \
    "s_mov_b32 m0, s95\n" \
    "s_add_i32 s75, s36, 2\n" \
    "s_cmp_lt_i32 s38, s75\n" \
    "s_cselect_b32 s75, s62, 0x1b0040\n" \
    "v_mov_b64 v[98:99], 0\n" \
    "v_mov_b32 v101, 0\n" \
    "s_pack_ll_b32_b16 s80, s91, s42\n" \
    "v_mov_b32 v103, s80\n" \
    "s_waitcnt vmcnt(4)\n" \
    "v_smfmac_f32_16x16x64_bf16 v[40:43], v[0:3], a[0:7], v124\n" \
    "ds_write_b128 v108, v[0:3] offset:0\n" \
    "v_smfmac_f32_16x16x64_bf16 v[44:47], v[0:3], a[8:15], v124\n" \
    "v_smfmac_f32_16x16x64_bf16 v[48:51], v[0:3], a[128:135], v124\n" \
    "v_smfmac_f32_16x16x64_bf16 v[52:55], v[0:3], a[136:143], v124\n" \
    "s_waitcnt vmcnt(3)\n" \
    "v_smfmac_f32_16x16x64_bf16 v[40:43], v[16:19], a[64:71], v124\n" \
    "ds_write_b128 v108, v[16:19] offset:4096\n" \
    "v_smfmac_f32_16x16x64_bf16 v[44:47], v[16:19], a[72:79], v124\n" \
    "s_waitcnt lgkmcnt(1)\n" \
    "s_barrier\n" \
    "v_smfmac_f32_16x16x64_bf16 v[48:51], v[16:19], a[192:199], v124\n" \
    "ds_read_b128 v[4:7], v24 offset:0\n" \
    "v_smfmac_f32_16x16x64_bf16 v[52:55], v[16:19], a[200:207], v124\n" \
    "ds_read_b128 v[8:11], v28 offset:0\n" \
    "v_smfmac_f32_16x16x64_bf16 v[56:59], v[0:3], v[72:79], v124\n" \
    "ds_read_b128 v[12:15], v32 offset:0\n" \
    "v_smfmac_f32_16x16x64_bf16 v[60:63], v[0:3], v[80:87], v124\n" \
    "s_waitcnt vmcnt(2) lgkmcnt(3)\n" \
    "s_barrier\n" \
    "s_memtime s[86:87]\n" \
    "v_smfmac_f32_16x16x64_bf16 v[56:59], v[16:19], v[168:175], v124\n" \
    "ds_read_b128 v[24:27], v24 offset:4096\n" \
    "v_smfmac_f32_16x16x64_bf16 v[60:63], v[16:19], v[176:183], v124\n" \
    "ds_read_b128 v[28:31], v28 offset:4096\n" \
    "s_waitcnt lgkmcnt(3)\n" \
    "v_smfmac_f32_16x16x64_bf16 v[40:43], v[4:7], a[16:23], v124\n" \
    "ds_read_b128 v[32:35], v32 offset:4096\n" \
    "v_smfmac_f32_16x16x64_bf16 v[40:43], v[8:11], a[32:39], v124\n" \
    "ds_read_b128 v[36:39], v126 offset:0\n" \
    "s_waitcnt lgkmcnt(4)\n" \
    "v_smfmac_f32_16x16x64_bf16 v[40:43], v[12:15], a[48:55], v124\n" \
    "buffer_load_dword v110, v114, %[rw], s94 offen sc1\n" \
    "v_smfmac_f32_16x16x64_bf16 v[44:47], v[4:7], a[24:31], v124\n" \
    "s_waitcnt lgkmcnt(2)\n" \
    "v_smfmac_f32_16x16x64_bf16 v[40:43], v[24:27], a[80:87], v124\n" \
    "v_smfmac_f32_16x16x64_bf16 v[40:43], v[28:31], a[96:103], v124\n" \
    "s_waitcnt lgkmcnt(0)\n" \
    "v_smfmac_f32_16x16x64_bf16 v[40:43], v[32:35], a[112:119], v124\n" \
    "ds_read_b128 v[72:75], v133 offset:18432\n" \
    "s_add_i32 s80, s86, s76\n" \
    "v_smfmac_f32_16x16x64_bf16 v[40:43], v[36:39], v[136:143], v124\n" \
    "ds_read_b128 v[76:79], v133 offset:19456\n" \
    "s_mul_hi_u32 s80, s80, s41\n" \
    "v_smfmac_f32_16x16x64_bf16 v[44:47], v[8:11], a[40:47], v124\n" \
    "ds_read_b128 v[80:83], v133 offset:32768\n" \
    "s_add_i32 s80, s80, s29\n" \
    "v_mov_b32 v102, s80\n" \
    "v_smfmac_f32_16x16x64_bf16 v[44:47], v[12:15], a[56:63], v124\n" \
    "ds_read_b128 v[84:87], v133 offset:33792\n" \
    "v_add_f32 v40, v40, v42\n" \
    "v_add_f32 v41, v41, v43\n" \
    "v_smfmac_f32_16x16x64_bf16 v[44:47], v[24:27], a[88:95], v124\n" \
    "ds_read_b128 v[20:23], v127 offset:19456\n" \
    "v_cmp_u_f32 vcc, v40, v41\n" \
    "v_smfmac_f32_16x16x64_bf16 v[44:47], v[28:31], a[104:111], v124\n" \
    "v_mul_f32 v40, s81, v40\n" \
    "v_smfmac_f32_16x16x64_bf16 v[44:47], v[32:35], a[120:127], v124\n" \
    "s_cbranch_vccnz 5f\n" \
    "6:\n" \
    "s_mov_b64 exec, s[72:73]\n" \
    "v_smfmac_f32_16x16x64_bf16 v[44:47], v[36:39], v[144:151], v124\n" \
    "buffer_store_dwordx2 v[102:103], v127, %[rs], s77 offen sc1\n" \
    "v_smfmac_f32_16x16x64_bf16 v[56:59], v[4:7], v[88:95], v124\n" \
    "s_mov_b64 exec, -1\n" \
    "s_waitcnt lgkmcnt(1)\n" \
    "v_mul_f32 v41, s81, v41\n" \
    "v_smfmac_f32_16x16x64_bf16 v[60:63], v[4:7], v[72:79], v124\n" \
    "ds_read_b128 v[88:91], v133 offset:34816\n" \
    "v_add_f32 v44, v44, v46\n" \
    "v_smfmac_f32_16x16x64_bf16 v[56:59], v[8:11], v[80:87], v124\n" \
    "ds_read_b128 v[92:95], v133 offset:35840\n" \
    "v_add_f32 v45, v45, v47\n" \
    "v_smfmac_f32_16x16x64_bf16 v[48:51], v[4:7], a[144:151], v124\n" \
    "ds_read_b128 v[72:75], v133 offset:49152\n" \
    "v_mul_f32 v44, s81, v44\n" \
    "v_smfmac_f32_16x16x64_bf16 v[52:55], v[4:7], a[152:159], v124\n" \
    "ds_read_b128 v[76:79], v133 offset:50176\n" \
    "v_mul_f32 v45, s81, v45\n" \
    "v_smfmac_f32_16x16x64_bf16 v[48:51], v[8:11], a[160:167], v124\n" \
    "ds_read_b128 v[0:3], v127 offset:16384\n" \
    "v_cvt_pk_bf16_f32 v107, v40, v44\n" \
    "v_smfmac_f32_16x16x64_bf16 v[52:55], v[8:11], a[168:175], v124\n" \
    "ds_read_b128 v[4:7], v127 offset:18432\n" \
    "s_waitcnt lgkmcnt(4)\n" \
    "v_cvt_pk_bf16_f32 v108, v41, v45\n" \
    "v_smfmac_f32_16x16x64_bf16 v[60:63], v[8:11], v[88:95], v124\n" \
    "ds_write2_b32 v128, v107, v108 offset0:0 offset1:80\n" \
    "v_smfmac_f32_16x16x64_bf16 v[60:63], v[12:15], v[248:255], v124\n" \
    "ds_read_b128 v[16:19], v127 offset:17408\n" \
    "s_waitcnt lgkmcnt(4)\n" \
    "v_smfmac_f32_16x16x64_bf16 v[56:59], v[12:15], v[72:79], v124\n" \
    "ds_read_b128 v[8:11], v127 offset:20480\n" \
    "v_smfmac_f32_16x16x64_bf16 v[48:51], v[12:15], a[176:183], v124\n" \
    "ds_read_b128 v[40:43], v127 offset:21504\n" \
    "v_smfmac_f32_16x16x64_bf16 v[52:55], v[12:15], a[184:191], v124\n" \
    "ds_read_b128 v[44:47], v127 offset:23552\n" \
    "v_smfmac_f32_16x16x64_bf16 v[48:51], v[24:27], a[208:215], v124\n" \
    "s_waitcnt lgkmcnt(4)\n" \
    "s_barrier\n" \
    "v_smfmac_f32_16x16x64_bf16 v[52:55], v[24:27], a[216:223], v124\n" \
    "ds_read_b128 v[72:75], v130 offset:0\n" \
    "v_smfmac_f32_16x16x64_bf16 v[48:51], v[28:31], a[224:231], v124\n" \
    "ds_read_b128 v[76:79], v130 offset:64\n" \
    "v_smfmac_f32_16x16x64_bf16 v[48:51], v[32:35], a[240:247], v124\n" \
    "ds_read_b128 v[80:83], v130 offset:128\n" \
    "v_smfmac_f32_16x16x64_bf16 v[48:51], v[36:39], v[152:159], v124\n" \
    "ds_read_b128 v[84:87], v130 offset:192\n" \
    "v_smfmac_f32_16x16x64_bf16 v[52:55], v[28:31], a[232:239], v124\n" \
    "ds_read_b128 v[12:15], v127 offset:22528\n" \
    "v_smfmac_f32_16x16x64_bf16 v[52:55], v[32:35], a[248:255], v124\n" \
    "v_add_f32 v48, v48, v50\n" \
    "v_smfmac_f32_16x16x64_bf16 v[52:55], v[36:39], v[160:167], v124\n" \
    "s_waitcnt lgkmcnt(3)\n" \
    "v_add_f32 v49, v49, v51\n" \
    "v_mfma_f32_16x16x32_bf16 v[88:91], v[0:3], v[72:75], 0\n" \
    "ds_read_b64 v[0:1], v132 offset:0\n" \
    "ds_read_b64 v[2:3], v132 offset:1024\n" \
    "v_mfma_f32_16x16x32_bf16 v[88:91], v[4:7], v[76:79], v[88:91]\n" \
    "s_waitcnt lgkmcnt(2)\n" \
    "v_add_f32 v52, v52, v54\n" \
    "v_add_f32 v53, v53, v55\n" \
    "v_mfma_f32_16x16x32_bf16 v[88:91], v[8:11], v[80:83], v[88:91]\n" \
    "v_cvt_pk_bf16_f32 v107, v48, v52\n" \
    "v_cvt_pk_bf16_f32 v108, v49, v53\n" \
    "v_mfma_f32_16x16x32_bf16 v[88:91], v[12:15], v[84:87], v[88:91]\n" \
    "ds_write2_b32 v129, v107, v108 offset0:0 offset1:144\n" \
    "v_mfma_f32_16x16x32_bf16 v[92:95], v[16:19], v[72:75], 0\n" \
    "ds_read_b64 v[4:5], v132 offset:512\n" \
    "ds_read_b64 v[6:7], v132 offset:1536\n" \
    "v_mfma_f32_16x16x32_bf16 v[92:95], v[20:23], v[76:79], v[92:95]\n" \
    "ds_read_b128 v[8:11], v106 offset:0\n" \
    "v_exp_f32 v88, v88\n" \
    "v_mfma_f32_16x16x32_bf16 v[92:95], v[40:43], v[80:83], v[92:95]\n" \
    "ds_read_b128 v[12:15], v106 offset:1024\n" \
    "v_exp_f32 v89, v89\n" \
    "v_mfma_f32_16x16x32_bf16 v[92:95], v[44:47], v[84:87], v[92:95]\n" \
    "s_waitcnt lgkmcnt(4)\n" \
    "s_barrier\n" \
    "v_smfmac_f32_16x16x64_bf16 v[56:59], v[24:27], v[184:191], v124\n" \
    "ds_read_b128 v[48:51], v131 offset:0\n" \
    "ds_read_b128 v[52:55], v131 offset:128\n" \
    "v_cvt_pk_bf16_f32 v88, v88, v89\n" \
    "v_smfmac_f32_16x16x64_bf16 v[56:59], v[28:31], v[200:207], v124\n" \
    "ds_read_b128 v[16:19], v106 offset:32768\n" \
    "v_exp_f32 v90, v90\n" \
    "v_smfmac_f32_16x16x64_bf16 v[56:59], v[32:35], v[216:223], v124\n" \
    "ds_read_b128 v[20:23], v106 offset:33792\n" \
    "v_exp_f32 v91, v91\n" \
    "v_smfmac_f32_16x16x64_bf16 v[56:59], v[36:39], v[232:239], v124\n" \
    "ds_read_b128 v[40:43], v106 offset:2048\n" \
    "s_waitcnt lgkmcnt(3)\n" \
    "v_exp_f32 v92, v92\n" \
    "v_cvt_pk_bf16_f32 v89, v90, v91\n" \
    "v_smfmac_f32_16x16x64_bf16 v[64:67], v[48:51], v[72:79], v124\n" \
    "ds_read_b128 v[44:47], v106 offset:3072\n" \
    "v_exp_f32 v93, v93\n" \
    "v_smfmac_f32_16x16x64_bf16 v[64:67], v[52:55], v[80:87], v124\n" \
    "ds_read_b128 v[72:75], v106 offset:34816\n" \
    "v_exp_f32 v94, v94\n" \
    "v_cvt_pk_bf16_f32 v90, v92, v93\n" \
    "v_smfmac_f32_16x16x64_bf16 v[60:63], v[24:27], v[192:199], v124\n" \
    "ds_read_b128 v[76:79], v106 offset:35840\n" \
    "v_exp_f32 v95, v95\n" \
    "v_smfmac_f32_16x16x64_bf16 v[60:63], v[28:31], v[208:215], v124\n" \
    "ds_read_b128 v[80:83], v106 offset:4096\n" \
    "s_cmp_eq_u32 s36, 62\n" \
    "v_add_f32 v64, v64, v66\n" \
    "v_add_f32 v65, v65, v67\n" \
    "v_smfmac_f32_16x16x64_bf16 v[60:63], v[32:35], v[224:231], v124\n" \
    "ds_read_b128 v[84:87], v106 offset:5120\n" \
    "v_exp_f32 v64, v64\n" \
    "v_cvt_pk_bf16_f32 v91, v94, v95\n" \
    "v_smfmac_f32_16x16x64_bf16 v[60:63], v[36:39], v[240:247], v124\n" \
    "ds_read_b128 v[48:51], v106 offset:36864\n" \
    "v_exp_f32 v65, v65\n" \
    "v_swap_b32 v1, v2\n" \
    "v_swap_b32 v5, v6\n" \
    "v_mfma_f32_16x16x32_bf16 v[102:105], v[88:91], v[120:123], 0\n" \
    "ds_read_b128 v[52:55], v106 offset:37888\n" \
    "v_cvt_pk_bf16_f32 v100, v64, v65\n" \
    "v_add_f32 v56, v56, v58\n" \
    "v_add_f32 v57, v57, v59\n" \
    "v_smfmac_f32_16x16x64_bf16 v[68:71], v[88:91], v[0:7], v124\n" \
    "ds_read_b128 v[24:27], v106 offset:6144\n" \
    "v_add_f32 v60, v60, v62\n" \
    "v_add_f32 v61, v61, v63\n" \
    "v_mfma_f32_16x16x16_bf16 v[102:105], v[100:101], v[120:121], v[102:105]\n" \
    "ds_read_b128 v[28:31], v106 offset:7168\n" \
    "v_cvt_pk_bf16_f32 v96, v56, v57\n" \
    "v_cvt_pk_bf16_f32 v97, v60, v61\n" \
    "s_nop 1\n" \
    "v_smfmac_f32_16x16x32_bf16 v[68:71], v[100:101], v[96:99], v124\n" \
    "ds_read_b128 v[32:35], v106 offset:38912\n" \
    "v_mov_b64 v[56:57], 0\n" \
    "v_mov_b64 v[58:59], 0\n" \
    "v_rcp_f32 v104, v102\n" \
    "v_rcp_f32 v105, v103\n" \
    "v_mul_f32 v68, v68, v104\n" \
    "v_mul_f32 v70, v70, v104\n" \
    "v_mul_f32 v69, v69, v105\n" \
    "v_mul_f32 v71, v71, v105\n" \
    "v_cvt_pk_bf16_f32 v107, v68, v70\n" \
    "v_cvt_pk_bf16_f32 v108, v69, v71\n" \
    "ds_write2_b32 v129, v107, v108 offset0:64 offset1:208\n" \
    "v_max3_f32 v102, v102, v103, v104\n" \
    "v_max_f32 v102, v102, v105\n" \
    "v_cmp_nge_f32 vcc, s82, v102\n" \
    "v_mov_b64 v[60:61], 0\n" \
    "v_mov_b64 v[62:63], 0\n" \
    "v_mov_b64 v[88:89], 0\n" \
    "v_mov_b64 v[90:91], 0\n" \
    "v_mov_b64 v[92:93], 0\n" \
    "v_mov_b64 v[94:95], 0\n" \
    "s_waitcnt vmcnt(2) lgkmcnt(0)\n" \
    "s_barrier\n" \
    "ds_read_b128 v[68:71], v109 offset:256\n" \
    "ds_read_b128 v[102:105], v111 offset:384\n" \
    "s_cbranch_vccnz 7f\n" \
    "buffer_load_dwordx2 v[98:99], v113, %[rs], s78 offen sc1\n" \
    "buffer_load_dword v100, off, %[rw], s75 sc1\n" \
    "v_mov_b64 v[64:65], 0\n" \
    "v_mov_b64 v[66:67], 0\n" \
    "s_waitcnt lgkmcnt(1)\n" \
    "v_smfmac_f32_16x16x64_bf16 v[56:59], v[68:71], v[8:15], v124\n" \
    "s_waitcnt lgkmcnt(0)\n" \
    "ds_read_b128 v[36:39], v106 offset:39936\n" \
    "v_smfmac_f32_16x16x64_bf16 v[56:59], v[102:105], v[16:23], v124\n" \
    "ds_read_b128 v[0:3], v133 offset:51200\n" \
    "v_smfmac_f32_16x16x64_bf16 v[60:63], v[68:71], v[40:47], v124\n" \
    "ds_read_b128 v[4:7], v133 offset:52224\n" \
    "v_smfmac_f32_16x16x64_bf16 v[60:63], v[102:105], v[72:79], v124\n" \
    "ds_read_b128 v[16:19], v112 offset:51200\n" \
    "v_add_f32 v56, v56, v58\n" \
    "v_smfmac_f32_16x16x64_bf16 v[88:91], v[68:71], v[80:87], v124\n" \
    "ds_read_b128 v[20:23], v112 offset:52224\n" \
    "v_add_f32 v57, v57, v59\n" \
    "v_smfmac_f32_16x16x64_bf16 v[88:91], v[102:105], v[48:55], v124\n" \
    "v_add_f32 v60, v60, v62\n" \
    "v_smfmac_f32_16x16x64_bf16 v[92:95], v[68:71], v[24:31], v124\n" \
    "s_waitcnt lgkmcnt(4)\n" \
    "v_add_f32 v61, v61, v63\n" \
    "v_smfmac_f32_16x16x64_bf16 v[92:95], v[102:105], v[32:39], v124\n" \
    "s_waitcnt lgkmcnt(2)\n" \
    "v_add_f32 v88, v88, v90\n" \
    "v_smfmac_f32_16x16x64_bf16 v[64:67], v[68:71], v[0:7], v124\n" \
    "s_waitcnt lgkmcnt(0)\n" \
    "v_add_f32 v89, v89, v91\n" \
    "v_smfmac_f32_16x16x64_bf16 v[64:67], v[102:105], v[16:23], v124\n" \
    "v_add_f32 v92, v92, v94\n" \
    "v_add_f32 v93, v93, v95\n" \
    "v_cvt_pk_bf16_f32 v8, v56, v60\n" \
    "v_cvt_pk_bf16_f32 v10, v57, v61\n" \
    "v_cvt_pk_bf16_f32 v9, v88, v92\n" \
    "v_cvt_pk_bf16_f32 v11, v89, v93\n" \
    "buffer_store_dwordx4 v[8:11], v134, %[rs], s79 offen sc1\n" \
    "v_add_f32 v64, v64, v66\n" \
    "v_add_f32 v65, v65, v67\n" \
    "v_cvt_pk_bf16_f32 v64, v64, v65\n" \
    "buffer_store_dword v64, v135, %[rs], s79 offen sc1\n" \
    "s_cbranch_scc1 16f\n" \
    "s_memtime s[88:89]\n" \
    "s_memrealtime s[44:45]\n" \
    "s_and_b32 s75, s91, 3\n" \
    "s_lshl_b32 s75, s75, 17\n" \
    "s_memrealtime s[46:47]\n" \
    "s_add_i32 s75, s75, s26\n" \
    "s_add_i32 s69, s75, s74\n" \
    "s_memrealtime s[48:49]\n" \
    "s_add_i32 s70, s69, 4096\n" \
    "s_add_i32 s71, s75, 16384\n" \
    "s_memrealtime s[50:51]\n" \
    "s_nop 1\n" \
    "s_memrealtime s[52:53]\n" \
    "s_nop 1\n" \
    "s_memrealtime s[54:55]\n" \
    "s_nop 1\n" \
    "s_memrealtime s[56:57]\n" \
    "s_nop 1\n" \
    "s_memrealtime vcc\n" \
    "v_and_b32 v18, 0x300, v127\n" \
    "v_add_u32 v18, v18, v127\n" \
    "v_and_b32 v19, 0x380, v127\n" \
    "v_add_u32 v19, v19, v127\n" \
    "s_waitcnt lgkmcnt(0)\n" \
    "s_add_i32 s44, s44, s46\n" \
    "s_add_i32 s48, s48, s50\n" \
    "s_add_i32 s52, s52, s54\n" \
    "s_add_i32 s44, s44, s48\n" \
    "s_add_i32 s52, s52, s56\n" \
    "s_add_i32 s44, s44, s52\n" \
    "s_add_i32 s80, s44, vcc_lo\n" \
    "s_sub_i32 s76, s80, s29\n" \
    "s_mov_b32 s29, s80\n" \
    "s_sub_i32 s80, s88, s24\n" \
    "s_mov_b32 s24, s88\n" \
    "s_mul_i32 s75, s76, s25\n" \
    "s_lshr_b32 s75, s75, 12\n" \
    "s_sub_i32 s75, s80, s75\n" \
    "s_mul_hi_u32 s80, s80, s41\n" \
    "s_sub_i32 s80, s76, s80\n" \
    "s_lshl_b32 s80, s80, 18\n" \
    "s_cmp_lt_u32 s76, 1480\n" \
    "s_cselect_b32 s75, s75, 0\n" \
    "s_cselect_b32 s80, s80, 0\n" \
    "s_add_i32 s25, s25, s75\n" \
    "s_add_i32 s41, s41, s80\n" \
    "s_waitcnt vmcnt(2)\n" \
    "v_readfirstlane_b32 s80, v100\n" \
    "v_cmp_ne_u16 vcc, s91, v99\n" \
    "s_mov_b32 s43, 0\n" \
    "s_max_i32 s38, s38, s80\n" \
    "s_cmp_lt_i32 s38, s91\n" \
    "s_cselect_b64 exec, 0, -1\n" \
    "s_cselect_b32 s94, 8, 0\n" \
    "buffer_load_dwordx4 v19, %[rw], s92 offen sc1 lds\n" \
    "s_add_i32 m0, s95, 896\n" \
    "s_nop 0\n" \
    "buffer_load_dwordx4 v19, %[rw], s92 offen offset:128 sc1 lds\n" \
    "s_add_i32 m0, s95, 8192\n" \
    "s_nop 0\n" \
    "buffer_load_dwordx4 v18, %[rw], s93 offen sc1 lds\n" \
    "s_add_i32 m0, s95, 8960\n" \
    "s_nop 0\n" \
    "buffer_load_dwordx4 v18, %[rw], s93 offen offset:256 sc1 lds\n" \
    "s_mov_b64 exec, -1\n" \
    "s_cbranch_vccnz 15f\n" \
    "10:\n" \
    "v_subrev_u32 v28, s29, v98\n" \
    "v_min_i32 v28, 0x4000, v28\n" \
    "v_max_i32 v28, 0xffffc000, v28\n" \
    "s_bitcmp1_b32 s30, 15\n" \
    "s_cbranch_scc1 18f\n" \
    "19:\n" \
    "v_readlane_b32 s75, v28, s83\n" \
    "v_max_i32_dpp v28, v28, v28 row_shr:1 row_mask:0xf bank_mask:0xf\n" \
    "s_nop 1\n" \
    "v_max_i32_dpp v28, v28, v28 row_shr:2 row_mask:0xf bank_mask:0xf\n" \
    "s_nop 1\n" \
    "v_max_i32_dpp v28, v28, v28 row_shr:4 row_mask:0xf bank_mask:0xf\n" \
    "s_nop 1\n" \
    "v_max_i32_dpp v28, v28, v28 row_shr:8 row_mask:0xf bank_mask:0xf\n" \
    "s_add_i32 s76, s29, s42\n" \
    "s_nop 0\n" \
    "v_max_i32_dpp v28, v28, v28 row_bcast:15 row_mask:0xa bank_mask:0xf\n" \
    "s_nop 1\n" \
    "v_max_i32_dpp v28, v28, v28 row_bcast:31 row_mask:0xc bank_mask:0xf\n" \
    "s_nop 0\n" \
    "v_readlane_b32 s37, v28, 63\n" \
    "s_sub_i32 s75, s37, s75\n" \
    "s_cmp_gt_i32 s75, 24\n" \
    "s_cselect_b32 s80, 2, 0\n" \
    "s_cmp_lt_i32 s75, 10\n" \
    "s_cselect_b32 s80, -6, s80\n" \
    "s_add_i32 s28, s28, s80\n" \
    "s_max_i32 s28, s28, 0\n" \
    "s_min_i32 s28, s28, 20\n" \
    "s_add_i32 s76, s76, s28\n" \
    "v_add_u32 v30, -1, s36\n" \
    "s_add_i32 s37, s37, s76\n" \
    "v_mov_b32 v31, s37\n" \
    "s_mov_b64 exec, s[72:73]\n" \
    "buffer_store_dwordx2 v[30:31], off, %[rw], s65 sc1\n" \
    "s_mov_b64 exec, -1\n" \
    "s_cmp_lg_u32 s94, 0\n" \
    "s_cbranch_scc1 9f\n" \
    "s_mov_b32 s36, s91\n" \
    "s_branch 12b\n" \
    "5:\n" \
    "v_mov_b32 v107, 0\n" \
    "v_max3_u32 v107, v107, v0, v1\n" \
    "v_max3_u32 v107, v107, v2, v3\n" \
    "v_max3_u32 v107, v107, v4, v5\n" \
    "v_max3_u32 v107, v107, v6, v7\n" \
    "v_max3_u32 v107, v107, v8, v9\n" \
    "v_max3_u32 v107, v107, v10, v11\n" \
    "v_max3_u32 v107, v107, v12, v13\n" \
    "v_max3_u32 v107, v107, v14, v15\n" \
    "v_max3_u32 v107, v107, v16, v17\n" \
    "v_max3_u32 v107, v107, v18, v19\n" \
    "v_max3_u32 v107, v107, v24, v25\n" \
    "v_max3_u32 v107, v107, v26, v27\n" \
    "v_max3_u32 v107, v107, v28, v29\n" \
    "v_max3_u32 v107, v107, v30, v31\n" \
    "v_max3_u32 v107, v107, v32, v33\n" \
    "v_max3_u32 v107, v107, v34, v35\n" \
    "v_max3_u32 v107, v107, v36, v37\n" \
    "v_max3_u32 v107, v107, v38, v39\n" \
    "v_cmp_eq_u32 vcc, -1, v107\n" \
    "s_nop 4\n" \
    "s_cbranch_vccz 6b\n" \
    "s_cmp_eq_u32 s36, 0\n" \
    "s_cbranch_scc1 6b\n" \
    "s_add_i32 s39, s39, -1\n" \
    "s_cmp_lt_i32 s39, 0\n" \
    "s_cbranch_scc1 6b\n" \
    "s_add_i32 s30, s30, 1\n" \
    "s_bitset1_b32 s30, 15\n" \
    "s_and_b32 s80, s31, 0xff\n" \
    "s_add_i32 s42, s42, s80\n" \
    "s_bfe_u32 s80, s31, 0x80008\n" \
    "s_add_i32 s40, s40, s80\n" \
    "s_waitcnt lgkmcnt(0)\n" \
    "s_mov_b32 s37, s29\n" \
    "s_nop 7\n" \
    "s_barrier\n" \
    "s_branch 0b\n" \
    "15:\n" \
    "s_add_i32 s30, s30, 0x10000\n" \
    "s_add_i32 s39, s39, -1\n" \
    "s_cmp_lt_i32 s39, 0\n" \
    "s_cbranch_scc1 10b\n" \
    "s_sleep 9\n" \
    "buffer_load_dwordx2 v[98:99], v113, %[rs], s78 offen sc1\n" \
    "s_waitcnt vmcnt(0)\n" \
    "v_cmp_ne_u16 vcc, s91, v99\n" \
    "s_nop 1\n" \
    "s_cbranch_vccnz 15b\n" \
    "s_memtime s[88:89]\n" \
    "s_memrealtime s[44:45]\n" \
    "s_nop 1\n" \
    "s_memrealtime s[46:47]\n" \
    "s_nop 1\n" \
    "s_memrealtime s[48:49]\n" \
    "s_nop 1\n" \
    "s_memrealtime s[50:51]\n" \
    "s_nop 1\n" \
    "s_memrealtime s[52:53]\n" \
    "s_nop 1\n" \
    "s_memrealtime s[54:55]\n" \
    "s_nop 1\n" \
    "s_memrealtime s[56:57]\n" \
    "s_nop 1\n" \
    "s_memrealtime vcc\n" \
    "s_waitcnt lgkmcnt(0)\n" \
    "s_add_i32 s44, s44, s46\n" \
    "s_add_i32 s48, s48, s50\n" \
    "s_add_i32 s52, s52, s54\n" \
    "s_add_i32 s44, s44, s48\n" \
    "s_add_i32 s52, s52, s56\n" \
    "s_add_i32 s44, s44, s52\n" \
    "s_add_i32 s29, s44, vcc_lo\n" \
    "s_mov_b32 s24, s88\n" \
    "s_branch 10b\n" \
    "18:\n" \
    "v_cmp_gt_i32 vcc, -320, v28\n" \
    "s_bitset0_b32 s30, 15\n" \
    "s_lshr_b32 s80, s31, 16\n" \
    "s_cmp_lt_u32 s36, 4\n" \
    "s_cselect_b32 s80, 0, s80\n" \
    "s_min_u32 s76, s80, 1\n" \
    "s_lshl_b32 s76, s76, 16\n" \
    "s_sub_i32 s31, s31, s76\n" \
    "s_mul_i32 s76, s80, 768\n" \
    "s_cmp_eq_u64 vcc, 0\n" \
    "s_cselect_b32 s80, 0, s80\n" \
    "s_cselect_b32 s76, s76, 0\n" \
    "s_add_i32 s33, s33, s80\n" \
    "s_add_i32 s32, s32, s76\n" \
    "s_branch 19b\n" \
    "17:\n" \
    "ds_read_b128 v[0:3], v108 offset:0\n" \
    "ds_read_b128 v[16:19], v108 offset:4096\n" \
    "s_waitcnt lgkmcnt(0)\n" \
    "s_branch 3b\n" \
    "20:\n" \
    "s_mov_b32 s91, 63\n" \
    "s_mov_b32 s78, 0x1a8000\n" \
    "buffer_load_dwordx2 v[98:99], v113, %[rs], s78 offen sc1\n" \
    "buffer_load_dword v100, off, %[rw], s62 sc1\n" \
    "s_waitcnt vmcnt(0)\n" \
    "16:\n" \
    "v_and_b32 v112, 0x300, v127\n" \
    "v_add_u32 v112, v112, v127\n" \
    "s_add_i32 s44, %[qb], 0\n" \
    "s_add_i32 s45, s44, 4096\n" \
    "buffer_load_dwordx4 v[136:139], v112, %[rw], s44 offen offset:0 sc1\n" \
    "v_lshrrev_b32 v103, 1, v127\n" \
    "s_lshr_b32 s76, s74, 1\n" \
    "v_add_u32 v101, s76, v103\n" \
    "ds_write_b64 v101, v[96:97] offset:9216\n" \
    "ds_read_b128 v[0:3], v127 offset:16384\n" \
    "v_lshrrev_b32 v42, 8, v127\n" \
    "v_bfe_u32 v40, v127, 4, 4\n" \
    "ds_read_b128 v[4:7], v127 offset:17408\n" \
    "v_and_b32 v41, 2, v42\n" \
    "v_add_u32 v43, v41, v42\n" \
    "ds_read_b128 v[8:11], v127 offset:18432\n" \
    "s_lshl_b32 s76, s60, 4\n" \
    "s_and_b32 s76, s76, 16\n" \
    "v_add_u32 v40, s76, v40\n" \
    "v_mad_i32_i24 v40, v43, -4, v40\n" \
    "buffer_load_dwordx4 v[140:143], v112, %[rw], s44 offen offset:2048 sc1\n" \
    "ds_read_b128 v[12:15], v127 offset:19456\n" \
    "v_mov_b32 v43, 0xff61b1e6\n" \
    "v_cmp_gt_i32 vcc, 0, v40\n" \
    "ds_read_b128 v[16:19], v127 offset:20480\n" \
    "v_cndmask_b32 v88, 0, v43, vcc\n" \
    "v_cmp_gt_i32 vcc, 1, v40\n" \
    "ds_read_b128 v[20:23], v127 offset:21504\n" \
    "v_cndmask_b32 v89, 0, v43, vcc\n" \
    "v_cmp_gt_i32 vcc, 2, v40\n" \
    "ds_read_b128 v[24:27], v127 offset:22528\n" \
    "v_cndmask_b32 v90, 0, v43, vcc\n" \
    "v_cmp_gt_i32 vcc, 3, v40\n" \
    "buffer_load_dwordx4 v[144:147], v112, %[rw], s45 offen offset:0 sc1\n" \
    "ds_read_b128 v[28:31], v127 offset:23552\n" \
    "v_cndmask_b32 v91, 0, v43, vcc\n" \
    "v_cmp_gt_i32 vcc, 8, v40\n" \
    "ds_read_b128 v[32:35], v131 offset:0\n" \
    "v_cndmask_b32 v92, 0, v43, vcc\n" \
    "v_cmp_gt_i32 vcc, 9, v40\n" \
    "ds_read_b128 v[36:39], v131 offset:128\n" \
    "v_cndmask_b32 v93, 0, v43, vcc\n" \
    "v_cmp_gt_i32 vcc, 10, v40\n" \
    "ds_read_b128 v[56:59], v127 offset:24576\n" \
    "v_cndmask_b32 v94, 0, v43, vcc\n" \
    "v_cmp_gt_i32 vcc, 11, v40\n" \
    "buffer_load_dwordx4 v[148:151], v112, %[rw], s45 offen offset:2048 sc1\n" \
    "ds_read_b128 v[60:63], v127 offset:25600\n" \
    "v_cndmask_b32 v95, 0, v43, vcc\n" \
    "v_mad_u32_u24 v44, v41, 3, v42\n" \
    "ds_read_b128 v[64:67], v127 offset:26624\n" \
    "v_bfe_u32 v45, v127, 6, 1\n" \
    "v_bfe_u32 v46, v127, 4, 2\n" \
    "s_waitcnt lgkmcnt(8)\n" \
    "ds_read_b128 v[68:71], v127 offset:27648\n" \
    "v_lshl_add_u32 v44, v46, 1, v44\n" \
    "v_bfe_u32 v46, v127, 7, 1\n" \
    "ds_read_b128 a[120:123], v127 offset:28672\n" \
    "v_lshl_add_u32 v44, v44, 1, v46\n" \
    "v_mul_u32_u24 v44, 4352, v44\n" \
    "ds_read_b128 a[124:127], v127 offset:29696\n" \
    "v_lshl_add_u32 v44, v45, 1, v44\n" \
    "v_lshlrev_b32 v45, 2, v45\n" \
    "ds_read_b128 v[80:83], v127 offset:30720\n" \
    "s_lshr_b32 s79, %[u], 4\n" \
    "s_mul_i32 s79, s79, 32 * 4352\n" \
    "s_and_b32 s94, %[u], 15\n" \
    "s_lshl2_add_u32 s79, s94, s79\n" \
    "s_add_i32 s79, s79, 0x3e00fc0\n" \
    "s_lshl3_add_u32 s94, s94, 0x4065f80\n" \
    "s_lshr_b32 s80, s60, 1\n" \
    "s_mul_i32 s76, s80, 49152\n" \
    "s_add_i32 s76, s76, 98304\n" \
    "v_add_u32 v128, s76, v127\n" \
    "v_xor_b32 v130, 0x38000, v128\n" \
    "ds_read_b128 v[84:87], v127 offset:31744\n" \
    "s_lshl_b32 s76, s80, 12\n" \
    "s_add_i32 s76, s76, 83968\n" \
    "v_add_u32 v132, s76, v127\n" \
    "s_add_i32 s92, s64, 16515072\n" \
    "s_add_i32 s93, s92, 0x1000000\n" \
    "s_mov_b32 m0, s95\n" \
    "v_and_b32 v97, 0x380, v127\n" \
    "v_add_u32 v97, v97, v127\n" \
    "s_waitcnt vmcnt(6)\n" \
    "v_readfirstlane_b32 s80, v100\n" \
    "v_cmp_ne_u16 vcc, s91, v99\n" \
    "s_max_i32 s38, s38, s80\n" \
    "s_cmp_lt_i32 s38, s91\n" \
    "s_cbranch_scc1 21f\n" \
    "22:\n" \
    "s_cbranch_vccnz 23f\n" \
    "24:\n" \
    "buffer_load_ushort v113, v44, %[rw], s79 offen sc1\n" \
    "buffer_load_dword v114, v45, %[rw], s94 offen sc1\n" \
    "v_subrev_u32 v96, s29, v98\n" \
    "v_min_i32 v96, 0x4000, v96\n" \
    "v_max_i32 v96, 0, v96\n" \
    "s_waitcnt lgkmcnt(0)\n" \
    "s_barrier\n" \
    "v_max_i32_dpp v96, v96, v96 row_shr:1 row_mask:0xf bank_mask:0xf\n" \
    "ds_read_b64 v[52:53], v103 offset:9216\n" \
    "s_and_b32 s75, s91, 3\n" \
    "v_max_i32_dpp v96, v96, v96 row_shr:2 row_mask:0xf bank_mask:0xf\n" \
    "ds_read_b64 v[54:55], v103 offset:9728\n" \
    "s_lshl_b32 s75, s75, 17\n" \
    "v_max_i32_dpp v96, v96, v96 row_shr:4 row_mask:0xf bank_mask:0xf\n" \
    "ds_read_b64 v[100:101], v103 offset:10240\n" \
    "s_add_i32 s75, s75, s26\n" \
    "v_max_i32_dpp v96, v96, v96 row_shr:8 row_mask:0xf bank_mask:0xf\n" \
    "ds_read_b64 v[102:103], v103 offset:10752\n" \
    "s_add_i32 s69, s75, s74\n" \
    "v_max_i32_dpp v96, v96, v96 row_bcast:15 row_mask:0xa bank_mask:0xf\n" \
    "ds_read_b128 a[0:3], v128 offset:0\n" \
    "s_add_i32 s70, s69, 4096\n" \
    "v_max_i32_dpp v96, v96, v96 row_bcast:31 row_mask:0xc bank_mask:0xf\n" \
    "ds_read_b128 a[16:19], v128 offset:2048\n" \
    "s_add_i32 s71, s75, 16384\n" \
    "v_readlane_b32 s90, v96, 63\n" \
    "s_add_i32 s90, s90, s42\n" \
    "s_mul_i32 s90, s90, s25\n" \
    "s_ashr_i32 s90, s90, 12\n" \
    "s_add_i32 s90, s90, s24\n" \
    "buffer_load_dwordx4 v97, %[rw], s92 offen sc1 lds\n" \
    "s_add_i32 m0, s95, 896\n" \
    "v_mov_b32 v99, 0\n" \
    "buffer_load_dwordx4 v97, %[rw], s92 offen offset:128 sc1 lds\n" \
    "s_add_i32 m0, s95, 8192\n" \
    "v_mov_b32 v96, 0\n" \
    "25:\n" \
    "v_mov_b64 v[48:49], 0\n" \
    "v_mov_b64 v[50:51], 0\n" \
    "s_waitcnt vmcnt(7)\n" \
    "v_mfma_f32_16x16x32_bf16 v[40:43], v[0:3], v[136:139], v[88:91]\n" \
    "ds_read_b128 a[4:7], v128 offset:1024\n" \
    "v_max3_u32 v98, v136, v137, v136\n" \
    "v_mfma_f32_16x16x32_bf16 v[44:47], v[4:7], v[136:139], v[92:95]\n" \
    "ds_read_b128 a[20:23], v128 offset:3072\n" \
    "v_max3_u32 v98, v138, v139, v98\n" \
    "s_waitcnt vmcnt(6)\n" \
    "v_mfma_f32_16x16x32_bf16 v[40:43], v[8:11], v[140:143], v[40:43]\n" \
    "ds_read_b128 a[8:11], v130 offset:0\n" \
    "v_max3_u32 v98, v140, v141, v98\n" \
    "v_mfma_f32_16x16x32_bf16 v[44:47], v[12:15], v[140:143], v[44:47]\n" \
    "ds_read_b128 a[24:27], v130 offset:2048\n" \
    "v_max3_u32 v98, v142, v143, v98\n" \
    "v_smfmac_f32_16x16x64_bf16 v[48:51], v[32:35], v[136:143], v124\n" \
    "ds_read_b128 a[12:15], v130 offset:1024\n" \
    "s_waitcnt vmcnt(5)\n" \
    "v_mfma_f32_16x16x32_bf16 v[40:43], v[16:19], v[144:147], v[40:43]\n" \
    "ds_read_b128 a[28:31], v130 offset:3072\n" \
    "v_max3_u32 v98, v144, v145, v98\n" \
    "v_mfma_f32_16x16x32_bf16 v[44:47], v[20:23], v[144:147], v[44:47]\n" \
    "ds_read_b128 a[32:35], v128 offset:4096\n" \
    "v_max3_u32 v98, v146, v147, v98\n" \
    "s_waitcnt vmcnt(4)\n" \
    "v_smfmac_f32_16x16x64_bf16 v[48:51], v[36:39], v[144:151], v124\n" \
    "ds_read_b128 a[48:51], v128 offset:6144\n" \
    "v_mfma_f32_16x16x32_bf16 v[40:43], v[24:27], v[148:151], v[40:43]\n" \
    "ds_read_b128 a[36:39], v128 offset:5120\n" \
    "v_max3_u32 v98, v148, v149, v98\n" \
    "v_max3_u32 v98, v150, v151, v98\n" \
    "v_mfma_f32_16x16x32_bf16 v[44:47], v[28:31], v[148:151], v[44:47]\n" \
    "v_cmp_eq_u32 vcc, -1, v98\n" \
    "s_add_i32 s44, %[qb], 262144\n" \
    "s_add_i32 s45, s44, 4096\n" \
    "s_waitcnt lgkmcnt(3)\n" \
    "v_add_f32 v48, v48, v50\n" \
    "v_add_f32 v49, v49, v51\n" \
    "s_nop 0\n" \
    "s_cbranch_vccnz 26f\n" \
    "27:\n" \
    "v_max3_f32 v0, v40, v41, v42\n" \
    "v_max3_f32 v1, v43, v44, v45\n" \
    "v_max3_f32 v0, v0, v46, v47\n" \
    "v_max3_f32 v1, v1, v48, v49\n" \
    "v_max_f32 v0, v0, v1\n" \
    "v_max_f32 v1, v0, v1\n" \
    "buffer_load_dwordx4 v112, %[rw], s93 offen sc1 lds\n" \
    "s_add_i32 m0, s95, 8960\n" \
    "s_nop 0\n" \
    "v_permlane16_swap_b32 v0, v1\n" \
    "v_max_f32 v0, v0, v1\n" \
    "v_max_f32 v1, v0, v1\n" \
    "buffer_load_dwordx4 v112, %[rw], s93 offen offset:256 sc1 lds\n" \
    "s_nop 0\n" \
    "v_permlane32_swap_b32 v0, v1\n" \
    "v_max_f32 v0, v0, v1\n" \
    "v_mul_f32 v0, s81, v0\n" \
    "v_fma_f32 v40, v40, s81, -v0\n" \
    "v_fma_f32 v41, v41, s81, -v0\n" \
    "ds_read_b128 v[136:139], v132 offset:0\n" \
    "v_exp_f32 v40, v40\n" \
    "v_exp_f32 v41, v41\n" \
    "v_fma_f32 v42, v42, s81, -v0\n" \
    "ds_read_b128 v[140:143], v132 offset:1024\n" \
    "v_fma_f32 v43, v43, s81, -v0\n" \
    "v_exp_f32 v42, v42\n" \
    "v_exp_f32 v43, v43\n" \
    "ds_read_b128 v[144:147], v132 offset:8192\n" \
    "v_cvt_pk_bf16_f32 v32, v40, v41\n" \
    "v_fma_f32 v44, v44, s81, -v0\n" \
    "v_fma_f32 v45, v45, s81, -v0\n" \
    "ds_read_b128 v[148:151], v132 offset:9216\n" \
    "v_exp_f32 v44, v44\n" \
    "v_exp_f32 v45, v45\n" \
    "v_cvt_pk_bf16_f32 v33, v42, v43\n" \
    "ds_read_b128 a[52:55], v128 offset:7168\n" \
    "v_fma_f32 v46, v46, s81, -v0\n" \
    "v_fma_f32 v47, v47, s81, -v0\n" \
    "v_exp_f32 v46, v46\n" \
    "s_memtime vcc\n" \
    "v_exp_f32 v47, v47\n" \
    "v_cvt_pk_bf16_f32 v34, v44, v45\n" \
    "v_fma_f32 v48, v48, s81, -v0\n" \
    "v_fma_f32 v49, v49, s81, -v0\n" \
    "v_exp_f32 v48, v48\n" \
    "v_exp_f32 v49, v49\n" \
    "v_cvt_pk_bf16_f32 v35, v46, v47\n" \
    "v_cvt_pk_bf16_f32 v98, v48, v49\n" \
    "v_cvt_pk_bf16_f32 v97, v48, v49\n" \
    "s_waitcnt lgkmcnt(0)\n" \
    "v_mfma_f32_16x16x32_bf16 v[36:39], v[120:123], v[32:35], 0\n" \
    "s_sub_i32 s75, s90, vcc_lo\n" \
    "s_cmp_gt_i32 s75, 100\n" \
    "v_mfma_f32_16x16x32_bf16 v[0:3], v[56:59], v[32:35], 0\n" \
    "s_cbranch_scc1 30f\n" \
    "31:\n" \
    "ds_read_b128 a[40:43], v130 offset:4096\n" \
    "v_bfe_u32 v40, v127, 6, 2\n" \
    "v_bfe_u32 v41, v127, 5, 1\n" \
    "v_mfma_f32_16x16x32_bf16 v[4:7], v[60:63], v[32:35], 0\n" \
    "s_mov_b32 m0, s74\n" \
    "s_add_i32 s76, s74, 4096\n" \
    "buffer_load_dwordx4 v125, %[rs], s69 offen sc1 lds\n" \
    "s_mov_b32 m0, s76\n" \
    "v_mfma_f32_16x16x32_bf16 v[8:11], v[64:67], v[32:35], 0\n" \
    "ds_read_b128 a[56:59], v130 offset:6144\n" \
    "v_bfe_u32 v42, v127, 4, 1\n" \
    "v_lshrrev_b32 v43, 8, v127\n" \
    "v_mfma_f32_16x16x32_bf16 v[12:15], v[68:71], v[32:35], 0\n" \
    "buffer_load_dwordx4 v125, %[rs], s70 offen sc1 lds\n" \
    "s_movk_i32 m0, 0x2000\n" \
    "v_mfma_f32_16x16x32_bf16 v[16:19], a[120:123], v[32:35], 0\n" \
    "ds_read_b128 a[44:47], v130 offset:5120\n" \
    "v_lshlrev_b32 v134, 14, v40\n" \
    "v_lshl_add_u32 v134, v41, 12, v134\n" \
    "v_mfma_f32_16x16x32_bf16 v[20:23], a[124:127], v[32:35], 0\n" \
    "s_mov_b32 exec_lo, s27\n" \
    "s_mov_b32 exec_hi, s27\n" \
    "buffer_load_dwordx4 v127, %[rs], s71 offen sc1 lds\n" \
    "s_mov_b64 exec, -1\n" \
    "v_mfma_f32_16x16x32_bf16 v[24:27], v[80:83], v[32:35], 0\n" \
    "ds_read_b128 a[60:63], v130 offset:7168\n" \
    "v_lshl_add_u32 v134, v42, 6, v134\n" \
    "v_lshl_add_u32 v134, v43, 4, v134\n" \
    "v_mfma_f32_16x16x32_bf16 v[28:31], v[84:87], v[32:35], 0\n" \
    "ds_read_b128 a[64:67], v128 offset:8192\n" \
    "v_and_b32 v41, 2, v40\n" \
    "v_add_lshl_u32 v135, v40, v41, 12\n" \
    "v_mfma_f32_16x16x16_bf16 v[36:39], v[120:121], v[98:99], v[36:39]\n" \
    "ds_read_b128 a[80:83], v128 offset:10240\n" \
    "v_bfe_u32 v42, v127, 4, 2\n" \
    "v_lshl_add_u32 v135, v42, 5, v135\n" \
    "v_mfma_f32_16x16x16_bf16 v[0:3], v[52:53], v[98:99], v[0:3]\n" \
    "buffer_load_dwordx4 v[72:75], v112, %[rw], s44 offen offset:0 sc1\n" \
    "v_mfma_f32_16x16x16_bf16 v[4:7], v[52:53], v[96:97], v[4:7]\n" \
    "ds_read_b128 a[68:71], v128 offset:9216\n" \
    "v_lshl_add_u32 v135, v43, 3, v135\n" \
    "v_mfma_f32_16x16x16_bf16 v[8:11], v[54:55], v[98:99], v[8:11]\n" \
    "buffer_load_dwordx4 v[76:79], v112, %[rw], s44 offen offset:2048 sc1\n" \
    "v_mfma_f32_16x16x16_bf16 v[12:15], v[54:55], v[96:97], v[12:15]\n" \
    "ds_read_b128 a[84:87], v128 offset:11264\n" \
    "v_mfma_f32_16x16x16_bf16 v[16:19], v[100:101], v[98:99], v[16:19]\n" \
    "buffer_load_dwordx4 v[104:107], v112, %[rw], s45 offen offset:0 sc1\n" \
    "v_mfma_f32_16x16x16_bf16 v[20:23], v[100:101], v[96:97], v[20:23]\n" \
    "ds_read_b128 a[72:75], v130 offset:8192\n" \
    "v_mfma_f32_16x16x16_bf16 v[24:27], v[102:103], v[98:99], v[24:27]\n" \
    "buffer_load_dwordx4 v[108:111], v112, %[rw], s45 offen offset:2048 sc1\n" \
    "v_mfma_f32_16x16x16_bf16 v[28:31], v[102:103], v[96:97], v[28:31]\n" \
    "ds_read_b128 a[88:91], v130 offset:10240\n" \
    "v_cvt_pk_bf16_f32 v40, v0, v4\n" \
    "v_cvt_pk_bf16_f32 v41, v1, v5\n" \
    "v_cvt_pk_bf16_f32 v42, v2, v6\n" \
    "v_cvt_pk_bf16_f32 v43, v3, v7\n" \
    "v_cvt_pk_bf16_f32 v44, v8, v12\n" \
    "v_cvt_pk_bf16_f32 v45, v9, v13\n" \
    "v_mfma_f32_16x16x32_bf16 v[64:67], v[136:139], v[40:43], 0\n" \
    "s_mov_b32 s46, %[tp]\n" \
    "ds_read_b128 a[76:79], v130 offset:9216\n" \
    "v_cvt_pk_bf16_f32 v46, v10, v14\n" \
    "v_cvt_pk_bf16_f32 v47, v11, v15\n" \
    "v_mfma_f32_16x16x32_bf16 v[56:59], a[0:3], v[40:43], 0\n" \
    "s_mov_b32 s47, %[tx]\n" \
    "ds_read_b128 a[92:95], v130 offset:11264\n" \
    "v_cvt_pk_bf16_f32 v48, v16, v20\n" \
    "v_cvt_pk_bf16_f32 v49, v17, v21\n" \
    "v_mfma_f32_16x16x32_bf16 v[64:67], v[140:143], v[44:47], v[64:67]\n" \
    "ds_read_b128 a[96:99], v128 offset:12288\n" \
    "v_cvt_pk_bf16_f32 v50, v18, v22\n" \
    "v_cvt_pk_bf16_f32 v51, v19, v23\n" \
    "v_mfma_f32_16x16x32_bf16 v[60:63], a[16:19], v[40:43], 0\n" \
    "s_waitcnt lgkmcnt(5)\n" \
    "ds_read_b128 a[112:115], v128 offset:14336\n" \
    "v_cvt_pk_bf16_f32 v52, v24, v28\n" \
    "v_cvt_pk_bf16_f32 v53, v25, v29\n" \
    "v_mfma_f32_16x16x32_bf16 v[64:67], v[144:147], v[48:51], v[64:67]\n" \
    "ds_read_b128 a[100:103], v128 offset:13312\n" \
    "v_cvt_pk_bf16_f32 v54, v26, v30\n" \
    "v_cvt_pk_bf16_f32 v55, v27, v31\n" \
    "v_mfma_f32_16x16x32_bf16 v[56:59], a[4:7], v[44:47], v[56:59]\n" \
    "ds_read_b128 a[116:119], v128 offset:15360\n" \
    "v_rcp_f32 v36, v36\n" \
    "v_mfma_f32_16x16x32_bf16 v[64:67], v[148:151], v[52:55], v[64:67]\n" \
    "ds_read_b128 a[104:107], v130 offset:12288\n" \
    "v_add_u32 v37, s74, v127\n" \
    "v_xor_b32 v38, 0x400, v37\n" \
    "v_mfma_f32_16x16x32_bf16 v[60:63], a[20:23], v[44:47], v[60:63]\n" \
    "ds_read_b128 a[120:123], v130 offset:14336\n" \
    "v_xor_b32 v39, 0x800, v37\n" \
    "v_xor_b32 v132, 0xc00, v37\n" \
    "v_mfma_f32_16x16x32_bf16 v[56:59], a[8:11], v[48:51], v[56:59]\n" \
    "ds_read_b128 a[108:111], v130 offset:13312\n" \
    "v_mul_f32 v64, v64, v36\n" \
    "v_mul_f32 v65, v65, v36\n" \
    "v_mfma_f32_16x16x32_bf16 v[60:63], a[24:27], v[48:51], v[60:63]\n" \
    "ds_read_b128 a[124:127], v130 offset:15360\n" \
    "v_mul_f32 v66, v66, v36\n" \
    "v_mul_f32 v67, v67, v36\n" \
    "v_mfma_f32_16x16x32_bf16 v[56:59], a[12:15], v[52:55], v[56:59]\n" \
    "v_cvt_pk_bf16_f32 v100, v64, v65\n" \
    "v_cvt_pk_bf16_f32 v101, v66, v67\n" \
    "v_mfma_f32_16x16x32_bf16 v[60:63], a[28:31], v[52:55], v[60:63]\n" \
    "buffer_store_dwordx2 v[100:101], v135, %[rw], s47 offen sc1\n" \
    "v_mfma_f32_16x16x32_bf16 v[64:67], a[32:35], v[40:43], 0\n" \
    "v_mul_f32 v56, v56, v36\n" \
    "v_mul_f32 v57, v57, v36\n" \
    "v_mfma_f32_16x16x32_bf16 v[68:71], a[48:51], v[40:43], 0\n" \
    "v_mul_f32 v58, v58, v36\n" \
    "v_mul_f32 v59, v59, v36\n" \
    "v_mfma_f32_16x16x32_bf16 v[64:67], a[36:39], v[44:47], v[64:67]\n" \
    "v_mul_f32 v60, v60, v36\n" \
    "v_mul_f32 v61, v61, v36\n" \
    "v_mfma_f32_16x16x32_bf16 v[68:71], a[52:55], v[44:47], v[68:71]\n" \
    "v_mul_f32 v62, v62, v36\n" \
    "v_mul_f32 v63, v63, v36\n" \
    "v_mfma_f32_16x16x32_bf16 v[64:67], a[40:43], v[48:51], v[64:67]\n" \
    "v_cvt_pk_bf16_f32 v96, v56, v57\n" \
    "v_cvt_pk_bf16_f32 v97, v58, v59\n" \
    "v_mfma_f32_16x16x32_bf16 v[68:71], a[56:59], v[48:51], v[68:71]\n" \
    "v_cvt_pk_bf16_f32 v98, v60, v61\n" \
    "v_cvt_pk_bf16_f32 v99, v62, v63\n" \
    "v_mfma_f32_16x16x32_bf16 v[64:67], a[44:47], v[52:55], v[64:67]\n" \
    "buffer_store_dwordx4 v[96:99], v134, %[rw], s46 offen sc1\n" \
    "s_add_i32 s46, s46, 0x20000\n" \
    "v_mfma_f32_16x16x32_bf16 v[68:71], a[60:63], v[52:55], v[68:71]\n" \
    "v_mov_b64 v[80:81], 0\n" \
    "v_mov_b64 v[82:83], 0\n" \
    "v_mfma_f32_16x16x32_bf16 v[56:59], a[64:67], v[40:43], 0\n" \
    "v_mul_f32 v64, v64, v36\n" \
    "v_mul_f32 v65, v65, v36\n" \
    "v_mfma_f32_16x16x32_bf16 v[60:63], a[80:83], v[40:43], 0\n" \
    "v_mul_f32 v66, v66, v36\n" \
    "v_mul_f32 v67, v67, v36\n" \
    "v_mfma_f32_16x16x32_bf16 v[56:59], a[68:71], v[44:47], v[56:59]\n" \
    "v_mul_f32 v68, v68, v36\n" \
    "v_mul_f32 v69, v69, v36\n" \
    "v_mfma_f32_16x16x32_bf16 v[60:63], a[84:87], v[44:47], v[60:63]\n" \
    "v_mul_f32 v70, v70, v36\n" \
    "v_mul_f32 v71, v71, v36\n" \
    "s_waitcnt lgkmcnt(11)\n" \
    "v_mfma_f32_16x16x32_bf16 v[56:59], a[72:75], v[48:51], v[56:59]\n" \
    "v_cvt_pk_bf16_f32 v100, v64, v65\n" \
    "v_cvt_pk_bf16_f32 v101, v66, v67\n" \
    "s_waitcnt lgkmcnt(10)\n" \
    "v_mfma_f32_16x16x32_bf16 v[60:63], a[88:91], v[48:51], v[60:63]\n" \
    "s_waitcnt vmcnt(6) lgkmcnt(0)\n" \
    "s_barrier\n" \
    "v_cvt_pk_bf16_f32 v102, v68, v69\n" \
    "v_cvt_pk_bf16_f32 v103, v70, v71\n" \
    "v_mfma_f32_16x16x32_bf16 v[56:59], a[76:79], v[52:55], v[56:59]\n" \
    "ds_read_b128 v[0:3], v37 offset:0\n" \
    "buffer_store_dwordx4 v[100:103], v134, %[rw], s46 offen sc1\n" \
    "s_add_i32 s46, s46, 0x20000\n" \
    "v_mfma_f32_16x16x32_bf16 v[60:63], a[92:95], v[52:55], v[60:63]\n" \
    "ds_read_b128 v[4:7], v38 offset:0\n" \
    "v_mov_b64 v[84:85], 0\n" \
    "v_mov_b64 v[86:87], 0\n" \
    "v_mfma_f32_16x16x32_bf16 v[64:67], a[96:99], v[40:43], 0\n" \
    "ds_read_b128 v[8:11], v39 offset:0\n" \
    "v_mul_f32 v56, v56, v36\n" \
    "v_mul_f32 v57, v57, v36\n" \
    "v_mfma_f32_16x16x32_bf16 v[68:71], a[112:115], v[40:43], 0\n" \
    "ds_read_b128 v[12:15], v132 offset:0\n" \
    "v_mul_f32 v58, v58, v36\n" \
    "v_mul_f32 v59, v59, v36\n" \
    "v_mfma_f32_16x16x32_bf16 v[64:67], a[100:103], v[44:47], v[64:67]\n" \
    "ds_read_b128 v[16:19], v37 offset:4096\n" \
    "v_mul_f32 v60, v60, v36\n" \
    "v_mul_f32 v61, v61, v36\n" \
    "v_mfma_f32_16x16x32_bf16 v[68:71], a[116:119], v[44:47], v[68:71]\n" \
    "ds_read_b128 v[20:23], v38 offset:4096\n" \
    "v_mul_f32 v62, v62, v36\n" \
    "v_mul_f32 v63, v63, v36\n" \
    "v_mfma_f32_16x16x32_bf16 v[64:67], a[104:107], v[48:51], v[64:67]\n" \
    "ds_read_b128 v[24:27], v39 offset:4096\n" \
    "v_cvt_pk_bf16_f32 v96, v56, v57\n" \
    "v_cvt_pk_bf16_f32 v97, v58, v59\n" \
    "v_mfma_f32_16x16x32_bf16 v[68:71], a[120:123], v[48:51], v[68:71]\n" \
    "ds_read_b128 v[28:31], v132 offset:4096\n" \
    "v_cvt_pk_bf16_f32 v98, v60, v61\n" \
    "v_cvt_pk_bf16_f32 v99, v62, v63\n" \
    "v_mfma_f32_16x16x32_bf16 v[64:67], a[108:111], v[52:55], v[64:67]\n" \
    "ds_read_b128 v[32:35], v126 offset:0\n" \
    "buffer_store_dwordx4 v[96:99], v134, %[rw], s46 offen sc1\n" \
    "s_add_i32 s46, s46, 0x20000\n" \
    "v_mfma_f32_16x16x32_bf16 v[68:71], a[124:127], v[52:55], v[68:71]\n" \
    "s_waitcnt lgkmcnt(3)\n" \
    "33:\n" \
    "v_smfmac_f32_16x16x64_bf16 v[80:83], v[0:3], a[128:135], v124\n" \
    "v_mul_f32 v64, v64, v36\n" \
    "v_smfmac_f32_16x16x64_bf16 v[80:83], v[4:7], a[144:151], v124\n" \
    "v_mul_f32 v65, v65, v36\n" \
    "v_smfmac_f32_16x16x64_bf16 v[80:83], v[8:11], a[160:167], v124\n" \
    "s_waitcnt lgkmcnt(0)\n" \
    "v_mul_f32 v66, v66, v36\n" \
    "v_smfmac_f32_16x16x64_bf16 v[80:83], v[12:15], a[176:183], v124\n" \
    "ds_read_b128 v[40:43], v127 offset:16384\n" \
    "v_mul_f32 v67, v67, v36\n" \
    "v_smfmac_f32_16x16x64_bf16 v[80:83], v[16:19], a[192:199], v124\n" \
    "ds_read_b128 v[44:47], v127 offset:17408\n" \
    "v_mul_f32 v68, v68, v36\n" \
    "v_smfmac_f32_16x16x64_bf16 v[80:83], v[20:23], a[208:215], v124\n" \
    "ds_read_b128 v[48:51], v127 offset:18432\n" \
    "v_mul_f32 v69, v69, v36\n" \
    "v_smfmac_f32_16x16x64_bf16 v[80:83], v[24:27], a[224:231], v124\n" \
    "ds_read_b128 v[52:55], v127 offset:19456\n" \
    "v_mul_f32 v70, v70, v36\n" \
    "v_mul_f32 v71, v71, v36\n" \
    "v_smfmac_f32_16x16x64_bf16 v[80:83], v[28:31], a[240:247], v124\n" \
    "ds_read_b128 v[56:59], v127 offset:20480\n" \
    "v_cvt_pk_bf16_f32 v100, v64, v65\n" \
    "v_cvt_pk_bf16_f32 v101, v66, v67\n" \
    "v_smfmac_f32_16x16x64_bf16 v[80:83], v[32:35], v[152:159], v124\n" \
    "ds_read_b128 v[60:63], v127 offset:21504\n" \
    "v_cvt_pk_bf16_f32 v102, v68, v69\n" \
    "v_cvt_pk_bf16_f32 v103, v70, v71\n" \
    "v_smfmac_f32_16x16x64_bf16 v[84:87], v[0:3], a[136:143], v124\n" \
    "ds_read_b128 v[64:67], v127 offset:22528\n" \
    "buffer_store_dwordx4 v[100:103], v134, %[rw], s46 offen sc1\n" \
    "v_smfmac_f32_16x16x64_bf16 v[84:87], v[4:7], a[152:159], v124\n" \
    "ds_read_b128 v[68:71], v127 offset:23552\n" \
    "v_add_f32 v80, v80, v82\n" \
    "v_add_f32 v81, v81, v83\n" \
    "v_smfmac_f32_16x16x64_bf16 v[84:87], v[8:11], a[168:175], v124\n" \
    "s_waitcnt vmcnt(5)\n" \
    "v_cmp_u_f32 vcc, v80, v81\n" \
    "v_max3_u32 v128, v72, v73, v72\n" \
    "v_smfmac_f32_16x16x64_bf16 v[84:87], v[12:15], a[184:191], v124\n" \
    "v_max3_u32 v128, v74, v75, v128\n" \
    "v_max3_u32 v128, v76, v77, v128\n" \
    "v_smfmac_f32_16x16x64_bf16 v[84:87], v[16:19], a[200:207], v124\n" \
    "s_cbranch_vccnz 32f\n" \
    "34:\n" \
    "v_max3_u32 v128, v78, v79, v128\n" \
    "v_max3_u32 v128, v104, v105, v128\n" \
    "v_smfmac_f32_16x16x64_bf16 v[84:87], v[20:23], a[216:223], v124\n" \
    "v_max3_u32 v128, v106, v107, v128\n" \
    "v_max3_u32 v128, v108, v109, v128\n" \
    "v_smfmac_f32_16x16x64_bf16 v[84:87], v[24:27], a[232:239], v124\n" \
    "v_max3_u32 v128, v110, v111, v128\n" \
    "v_cmp_eq_u32 vcc, -1, v128\n" \
    "v_smfmac_f32_16x16x64_bf16 v[84:87], v[28:31], a[248:255], v124\n" \
    "s_waitcnt lgkmcnt(0)\n" \
    "v_smfmac_f32_16x16x64_bf16 v[84:87], v[32:35], v[160:167], v124\n" \
    "s_cbranch_vccnz 28f\n" \
    "29:\n" \
    "v_mfma_f32_16x16x32_bf16 v[152:155], v[40:43], v[72:75], v[88:91]\n" \
    "ds_read_b128 a[128:131], v133 offset:0\n" \
    "v_mov_b64 v[96:97], 0\n" \
    "v_mov_b64 v[98:99], 0\n" \
    "v_mfma_f32_16x16x32_bf16 v[156:159], v[44:47], v[72:75], v[92:95]\n" \
    "ds_read_b128 a[132:135], v133 offset:1024\n" \
    "v_add_f32 v84, v84, v86\n" \
    "v_add_f32 v85, v85, v87\n" \
    "v_mfma_f32_16x16x32_bf16 v[152:155], v[48:51], v[76:79], v[152:155]\n" \
    "ds_read_b128 a[136:139], v133 offset:2048\n" \
    "v_cvt_pk_bf16_f32 v80, v80, v84\n" \
    "v_cvt_pk_bf16_f32 v81, v81, v85\n" \
    "v_mfma_f32_16x16x32_bf16 v[156:159], v[52:55], v[76:79], v[156:159]\n" \
    "ds_write2_b32 v129, v80, v81 offset0:0 offset1:144\n" \
    "v_mfma_f32_16x16x32_bf16 v[152:155], v[56:59], v[104:107], v[152:155]\n" \
    "ds_read_b128 a[140:143], v133 offset:3072\n" \
    "v_mov_b64 v[160:161], 0\n" \
    "v_mov_b64 v[162:163], 0\n" \
    "v_mfma_f32_16x16x32_bf16 v[156:159], v[60:63], v[104:107], v[156:159]\n" \
    "ds_read_b128 a[144:147], v133 offset:16384\n" \
    "v_mov_b64 v[100:101], 0\n" \
    "v_mov_b64 v[102:103], 0\n" \
    "v_mfma_f32_16x16x32_bf16 v[152:155], v[64:67], v[108:111], v[152:155]\n" \
    "ds_read_b128 a[148:151], v133 offset:17408\n" \
    "v_lshrrev_b32 v42, 1, v37\n" \
    "v_lshrrev_b32 v43, 1, v127\n" \
    "v_mfma_f32_16x16x32_bf16 v[156:159], v[68:71], v[108:111], v[156:159]\n" \
    "s_waitcnt lgkmcnt(3)\n" \
    "s_barrier\n" \
    "ds_read_b128 v[80:83], v131 offset:0\n" \
    "ds_read_b128 v[84:87], v131 offset:128\n" \
    "v_smfmac_f32_16x16x64_bf16 v[96:99], v[16:19], v[168:175], v124\n" \
    "ds_read_b128 a[152:155], v133 offset:18432\n" \
    "v_mov_b32 v61, 0\n" \
    "v_smfmac_f32_16x16x64_bf16 v[96:99], v[20:23], v[184:191], v124\n" \
    "ds_read_b128 a[156:159], v133 offset:19456\n" \
    "v_max3_f32 v40, v152, v153, v154\n" \
    "v_smfmac_f32_16x16x64_bf16 v[96:99], v[24:27], v[200:207], v124\n" \
    "ds_read_b128 a[160:163], v133 offset:32768\n" \
    "s_waitcnt lgkmcnt(3)\n" \
    "v_max3_f32 v41, v155, v156, v157\n" \
    "v_smfmac_f32_16x16x64_bf16 v[160:163], v[80:83], v[72:79], v124\n" \
    "ds_read_b128 a[164:167], v133 offset:33792\n" \
    "v_max3_f32 v40, v40, v158, v159\n" \
    "v_smfmac_f32_16x16x64_bf16 v[160:163], v[84:87], v[104:111], v124\n" \
    "ds_read_b128 a[168:171], v133 offset:34816\n" \
    "v_mov_b32 v62, 0\n" \
    "v_smfmac_f32_16x16x64_bf16 v[96:99], v[28:31], v[216:223], v124\n" \
    "ds_read_b128 a[172:175], v133 offset:35840\n" \
    "v_smfmac_f32_16x16x64_bf16 v[96:99], v[32:35], v[232:239], v124\n" \
    "ds_read_b128 a[176:179], v133 offset:49152\n" \
    "v_add_f32 v160, v160, v162\n" \
    "v_add_f32 v161, v161, v163\n" \
    "v_max3_f32 v41, v41, v160, v161\n" \
    "v_smfmac_f32_16x16x64_bf16 v[100:103], v[16:19], v[176:183], v124\n" \
    "ds_read_b128 a[180:183], v133 offset:50176\n" \
    "v_max_f32 v40, v40, v41\n" \
    "v_max_f32 v41, v40, v41\n" \
    "v_smfmac_f32_16x16x64_bf16 v[100:103], v[20:23], v[192:199], v124\n" \
    "ds_read_b128 a[192:195], v127 offset:24576\n" \
    "v_permlane16_swap_b32 v40, v41\n" \
    "v_max_f32 v40, v40, v41\n" \
    "v_max_f32 v41, v40, v41\n" \
    "v_smfmac_f32_16x16x64_bf16 v[96:99], v[0:3], a[128:135], v124\n" \
    "ds_read_b128 a[196:199], v127 offset:25600\n" \
    "v_permlane32_swap_b32 v40, v41\n" \
    "v_max_f32 v40, v40, v41\n" \
    "v_mul_f32 v40, s81, v40\n" \
    "v_smfmac_f32_16x16x64_bf16 v[96:99], v[4:7], a[144:151], v124\n" \
    "ds_read_b128 a[200:203], v127 offset:26624\n" \
    "s_waitcnt lgkmcnt(3)\n" \
    "v_fma_f32 v152, v152, s81, -v40\n" \
    "v_fma_f32 v153, v153, s81, -v40\n" \
    "v_exp_f32 v152, v152\n" \
    "v_smfmac_f32_16x16x64_bf16 v[96:99], v[8:11], a[160:167], v124\n" \
    "ds_read_b128 a[204:207], v127 offset:27648\n" \
    "v_exp_f32 v153, v153\n" \
    "v_fma_f32 v154, v154, s81, -v40\n" \
    "v_fma_f32 v155, v155, s81, -v40\n" \
    "v_smfmac_f32_16x16x64_bf16 v[96:99], v[12:15], a[176:183], v124\n" \
    "ds_read_b128 a[208:211], v127 offset:28672\n" \
    "v_exp_f32 v154, v154\n" \
    "v_exp_f32 v155, v155\n" \
    "v_cvt_pk_bf16_f32 v56, v152, v153\n" \
    "v_smfmac_f32_16x16x64_bf16 v[100:103], v[24:27], v[208:215], v124\n" \
    "ds_read_b128 a[212:215], v127 offset:29696\n" \
    "v_fma_f32 v156, v156, s81, -v40\n" \
    "v_fma_f32 v157, v157, s81, -v40\n" \
    "v_exp_f32 v156, v156\n" \
    "v_smfmac_f32_16x16x64_bf16 v[100:103], v[28:31], v[224:231], v124\n" \
    "ds_read_b128 a[216:219], v127 offset:30720\n" \
    "v_exp_f32 v157, v157\n" \
    "v_cvt_pk_bf16_f32 v57, v154, v155\n" \
    "v_fma_f32 v158, v158, s81, -v40\n" \
    "v_smfmac_f32_16x16x64_bf16 v[100:103], v[32:35], v[240:247], v124\n" \
    "ds_read_b128 a[220:223], v127 offset:31744\n" \
    "v_fma_f32 v159, v159, s81, -v40\n" \
    "v_exp_f32 v158, v158\n" \
    "v_exp_f32 v159, v159\n" \
    "v_smfmac_f32_16x16x64_bf16 v[100:103], v[12:15], v[248:255], v124\n" \
    "v_cvt_pk_bf16_f32 v58, v156, v157\n" \
    "v_fma_f32 v160, v160, s81, -v40\n" \
    "v_fma_f32 v161, v161, s81, -v40\n" \
    "v_smfmac_f32_16x16x64_bf16 v[100:103], v[0:3], a[136:143], v124\n" \
    "v_exp_f32 v160, v160\n" \
    "v_exp_f32 v161, v161\n" \
    "v_cvt_pk_bf16_f32 v59, v158, v159\n" \
    "v_smfmac_f32_16x16x64_bf16 v[100:103], v[4:7], a[152:159], v124\n" \
    "v_cvt_pk_bf16_f32 v60, v160, v161\n" \
    "v_cvt_pk_bf16_f32 v63, v160, v161\n" \
    "v_smfmac_f32_16x16x64_bf16 v[100:103], v[8:11], a[168:175], v124\n" \
    "s_waitcnt lgkmcnt(0)\n" \
    "v_mfma_f32_16x16x32_bf16 v[36:39], v[120:123], v[56:59], 0\n" \
    "v_add_f32 v96, v96, v98\n" \
    "v_add_f32 v97, v97, v99\n" \
    "v_mfma_f32_16x16x32_bf16 v[0:3], a[192:195], v[56:59], 0\n" \
    "v_add_f32 v100, v100, v102\n" \
    "v_add_f32 v101, v101, v103\n" \
    "v_mfma_f32_16x16x32_bf16 v[4:7], a[196:199], v[56:59], 0\n" \
    "v_cvt_pk_bf16_f32 v96, v96, v97\n" \
    "v_cvt_pk_bf16_f32 v97, v100, v101\n" \
    "v_mfma_f32_16x16x32_bf16 v[8:11], a[200:203], v[56:59], 0\n" \
    "ds_write_b64 v42, v[96:97] offset:9216\n" \
    "v_mfma_f32_16x16x32_bf16 v[12:15], a[204:207], v[56:59], 0\n" \
    "v_lshrrev_b32 v169, 8, v127\n" \
    "v_and_b32 v170, 2, v169\n" \
    "v_mfma_f32_16x16x32_bf16 v[16:19], a[208:211], v[56:59], 0\n" \
    "v_mad_u32_u24 v169, v170, 3, v169\n" \
    "v_bfe_u32 v168, v127, 6, 1\n" \
    "v_mfma_f32_16x16x32_bf16 v[20:23], a[212:215], v[56:59], 0\n" \
    "v_bfe_u32 v170, v127, 4, 2\n" \
    "v_lshl_add_u32 v169, v170, 1, v169\n" \
    "v_mfma_f32_16x16x32_bf16 v[24:27], a[216:219], v[56:59], 0\n" \
    "s_waitcnt lgkmcnt(0)\n" \
    "s_barrier\n" \
    "ds_read_b64 v[64:65], v43 offset:9216\n" \
    "ds_read_b64 v[66:67], v43 offset:9728\n" \
    "ds_read_b64 v[68:69], v43 offset:10240\n" \
    "ds_read_b64 v[70:71], v43 offset:10752\n" \
    "v_mfma_f32_16x16x32_bf16 v[28:31], a[220:223], v[56:59], 0\n" \
    "v_mul_u32_u24 v168, 1152, v168\n" \
    "v_lshl_add_u32 v168, v169, 2, v168\n" \
    "v_mfma_f32_16x16x16_bf16 v[36:39], v[120:121], v[60:61], v[36:39]\n" \
    "s_waitcnt lgkmcnt(2)\n" \
    "v_mfma_f32_16x16x16_bf16 v[0:3], v[64:65], v[60:61], v[0:3]\n" \
    "v_sub_f32 v169, 1.0, v114\n" \
    "v_lshlrev_b32 v172, 16, v113\n" \
    "v_mfma_f32_16x16x16_bf16 v[4:7], v[64:65], v[62:63], v[4:7]\n" \
    "s_waitcnt lgkmcnt(0)\n" \
    "v_mul_f32 v172, v172, v169\n" \
    "v_and_b32 v170, 16, v127\n" \
    "v_mfma_f32_16x16x16_bf16 v[8:11], v[66:67], v[60:61], v[8:11]\n" \
    "v_mov_b32 v169, 0x3f80\n" \
    "v_lshlrev_b32 v170, v170, v169\n" \
    "v_mfma_f32_16x16x16_bf16 v[12:15], v[66:67], v[62:63], v[12:15]\n" \
    "v_mov_b32_dpp v173, v172 row_ror:8 row_mask:0xf bank_mask:0xf\n" \
    "v_mov_b64 v[176:177], 0\n" \
    "v_mfma_f32_16x16x16_bf16 v[16:19], v[68:69], v[60:61], v[16:19]\n" \
    "v_mov_b64 v[178:179], 0\n" \
    "v_mov_b32_dpp v176, v170 quad_perm:[0,1,2,3] row_mask:0xf bank_mask:0x1\n" \
    "v_mfma_f32_16x16x16_bf16 v[20:23], v[68:69], v[62:63], v[20:23]\n" \
    "v_mov_b32_dpp v177, v170 quad_perm:[0,1,2,3] row_mask:0xf bank_mask:0x2\n" \
    "v_mov_b32_dpp v178, v170 quad_perm:[0,1,2,3] row_mask:0xf bank_mask:0x4\n" \
    "v_mfma_f32_16x16x16_bf16 v[24:27], v[70:71], v[60:61], v[24:27]\n" \
    "v_mov_b32_dpp v179, v170 quad_perm:[0,1,2,3] row_mask:0xf bank_mask:0x8\n" \
    "v_mfma_f32_16x16x16_bf16 v[28:31], v[70:71], v[62:63], v[28:31]\n" \
    "v_cvt_pk_bf16_f32 v40, v0, v4\n" \
    "v_cvt_pk_bf16_f32 v41, v1, v5\n" \
    "v_cvt_pk_bf16_f32 v42, v2, v6\n" \
    "v_cvt_pk_bf16_f32 v43, v3, v7\n" \
    "v_cvt_pk_bf16_f32 v44, v8, v12\n" \
    "v_cvt_pk_bf16_f32 v45, v9, v13\n" \
    "v_mfma_f32_16x16x32_bf16 v[80:83], v[136:139], v[40:43], 0\n" \
    "s_mov_b32 s46, %[tq]\n" \
    "v_cvt_pk_bf16_f32 v46, v10, v14\n" \
    "v_cvt_pk_bf16_f32 v47, v11, v15\n" \
    "v_mfma_f32_16x16x32_bf16 v[72:75], a[0:3], v[40:43], 0\n" \
    "s_add_i32 s47, %[tx], 0x40000\n" \
    "v_cvt_pk_bf16_f32 v48, v16, v20\n" \
    "v_cvt_pk_bf16_f32 v49, v17, v21\n" \
    "v_mfma_f32_16x16x32_bf16 v[80:83], v[140:143], v[44:47], v[80:83]\n" \
    "v_cvt_pk_bf16_f32 v50, v18, v22\n" \
    "v_cvt_pk_bf16_f32 v51, v19, v23\n" \
    "v_mfma_f32_16x16x32_bf16 v[76:79], a[16:19], v[40:43], 0\n" \
    "v_cvt_pk_bf16_f32 v52, v24, v28\n" \
    "v_cvt_pk_bf16_f32 v53, v25, v29\n" \
    "v_mfma_f32_16x16x32_bf16 v[80:83], v[144:147], v[48:51], v[80:83]\n" \
    "v_cvt_pk_bf16_f32 v54, v26, v30\n" \
    "v_cvt_pk_bf16_f32 v55, v27, v31\n" \
    "v_mfma_f32_16x16x32_bf16 v[72:75], a[4:7], v[44:47], v[72:75]\n" \
    "v_rcp_f32 v36, v36\n" \
    "v_mfma_f32_16x16x32_bf16 v[80:83], v[148:151], v[52:55], v[80:83]\n" \
    "s_and_b32 s94, %[u], 15\n" \
    "s_mul_i32 s94, s94, 2304\n" \
    "v_mfma_f32_16x16x32_bf16 v[76:79], a[20:23], v[44:47], v[76:79]\n" \
    "s_andn2_b32 s77, %[u], 15\n" \
    "s_lshl2_add_u32 s94, s77, s94\n" \
    "v_mfma_f32_16x16x32_bf16 v[72:75], a[8:11], v[48:51], v[72:75]\n" \
    "v_mul_f32 v80, v80, v36\n" \
    "v_mul_f32 v81, v81, v36\n" \
    "v_mfma_f32_16x16x32_bf16 v[76:79], a[24:27], v[48:51], v[76:79]\n" \
    "v_mul_f32 v82, v82, v36\n" \
    "v_mul_f32 v83, v83, v36\n" \
    "v_mfma_f32_16x16x32_bf16 v[72:75], a[12:15], v[52:55], v[72:75]\n" \
    "v_cvt_pk_bf16_f32 v100, v80, v81\n" \
    "v_cvt_pk_bf16_f32 v101, v82, v83\n" \
    "v_mfma_f32_16x16x32_bf16 v[76:79], a[28:31], v[52:55], v[76:79]\n" \
    "buffer_store_dwordx2 v[100:101], v135, %[rw], s47 offen sc1\n" \
    "v_mfma_f32_16x16x32_bf16 v[80:83], a[32:35], v[40:43], 0\n" \
    "v_mul_f32 v72, v72, v36\n" \
    "v_mul_f32 v73, v73, v36\n" \
    "v_mfma_f32_16x16x32_bf16 v[84:87], a[48:51], v[40:43], 0\n" \
    "v_mul_f32 v74, v74, v36\n" \
    "v_mul_f32 v75, v75, v36\n" \
    "v_mfma_f32_16x16x32_bf16 v[80:83], a[36:39], v[44:47], v[80:83]\n" \
    "v_mul_f32 v76, v76, v36\n" \
    "v_mul_f32 v77, v77, v36\n" \
    "v_mfma_f32_16x16x32_bf16 v[84:87], a[52:55], v[44:47], v[84:87]\n" \
    "v_mul_f32 v78, v78, v36\n" \
    "v_mul_f32 v79, v79, v36\n" \
    "v_mfma_f32_16x16x32_bf16 v[80:83], a[40:43], v[48:51], v[80:83]\n" \
    "v_cvt_pk_bf16_f32 v96, v72, v73\n" \
    "v_cvt_pk_bf16_f32 v97, v74, v75\n" \
    "v_mfma_f32_16x16x32_bf16 v[84:87], a[56:59], v[48:51], v[84:87]\n" \
    "v_cvt_pk_bf16_f32 v98, v76, v77\n" \
    "v_cvt_pk_bf16_f32 v99, v78, v79\n" \
    "v_mfma_f32_16x16x32_bf16 v[80:83], a[44:47], v[52:55], v[80:83]\n" \
    "buffer_store_dwordx4 v[96:99], v134, %[rw], s46 offen sc1\n" \
    "s_add_i32 s46, s46, 0x20000\n" \
    "v_mfma_f32_16x16x32_bf16 v[84:87], a[60:63], v[52:55], v[84:87]\n" \
    "s_add_i32 s94, s94, 2322432\n" \
    "v_mfma_f32_16x16x32_bf16 v[72:75], a[64:67], v[40:43], 0\n" \
    "v_mul_f32 v80, v80, v36\n" \
    "v_mul_f32 v81, v81, v36\n" \
    "v_mfma_f32_16x16x32_bf16 v[76:79], a[80:83], v[40:43], 0\n" \
    "v_mul_f32 v82, v82, v36\n" \
    "v_mul_f32 v83, v83, v36\n" \
    "v_mfma_f32_16x16x32_bf16 v[72:75], a[68:71], v[44:47], v[72:75]\n" \
    "v_mul_f32 v84, v84, v36\n" \
    "v_mul_f32 v85, v85, v36\n" \
    "v_mfma_f32_16x16x32_bf16 v[76:79], a[84:87], v[44:47], v[76:79]\n" \
    "v_mul_f32 v86, v86, v36\n" \
    "v_mul_f32 v87, v87, v36\n" \
    "v_mfma_f32_16x16x32_bf16 v[72:75], a[72:75], v[48:51], v[72:75]\n" \
    "v_cvt_pk_bf16_f32 v100, v80, v81\n" \
    "v_cvt_pk_bf16_f32 v101, v82, v83\n" \
    "v_mfma_f32_16x16x32_bf16 v[76:79], a[88:91], v[48:51], v[76:79]\n" \
    "v_cvt_pk_bf16_f32 v102, v84, v85\n" \
    "v_cvt_pk_bf16_f32 v103, v86, v87\n" \
    "v_mfma_f32_16x16x32_bf16 v[72:75], a[76:79], v[52:55], v[72:75]\n" \
    "buffer_store_dwordx4 v[100:103], v134, %[rw], s46 offen sc1\n" \
    "s_add_i32 s46, s46, 0x20000\n" \
    "v_mfma_f32_16x16x32_bf16 v[76:79], a[92:95], v[52:55], v[76:79]\n" \
    "v_mfma_f32_16x16x32_bf16 v[80:83], a[96:99], v[40:43], 0\n" \
    "v_mul_f32 v72, v72, v36\n" \
    "v_mul_f32 v73, v73, v36\n" \
    "v_mfma_f32_16x16x32_bf16 v[84:87], a[112:115], v[40:43], 0\n" \
    "v_mul_f32 v74, v74, v36\n" \
    "v_mul_f32 v75, v75, v36\n" \
    "v_mfma_f32_16x16x32_bf16 v[80:83], a[100:103], v[44:47], v[80:83]\n" \
    "v_mul_f32 v76, v76, v36\n" \
    "v_mul_f32 v77, v77, v36\n" \
    "v_mfma_f32_16x16x32_bf16 v[84:87], a[116:119], v[44:47], v[84:87]\n" \
    "v_mul_f32 v78, v78, v36\n" \
    "v_mul_f32 v79, v79, v36\n" \
    "v_mfma_f32_16x16x32_bf16 v[80:83], a[104:107], v[48:51], v[80:83]\n" \
    "v_cvt_pk_bf16_f32 v96, v72, v73\n" \
    "v_cvt_pk_bf16_f32 v97, v74, v75\n" \
    "v_mfma_f32_16x16x32_bf16 v[84:87], a[120:123], v[48:51], v[84:87]\n" \
    "v_cvt_pk_bf16_f32 v98, v76, v77\n" \
    "v_cvt_pk_bf16_f32 v99, v78, v79\n" \
    "v_mfma_f32_16x16x32_bf16 v[80:83], a[108:111], v[52:55], v[80:83]\n" \
    "buffer_store_dwordx4 v[96:99], v134, %[rw], s46 offen sc1\n" \
    "s_add_i32 s46, s46, 0x20000\n" \
    "v_mfma_f32_16x16x32_bf16 v[84:87], a[124:127], v[52:55], v[84:87]\n" \
    "s_memtime vcc\n" \
    "s_nop 2\n" \
    "v_mul_f32 v80, v80, v36\n" \
    "v_mul_f32 v81, v81, v36\n" \
    "v_mul_f32 v82, v82, v36\n" \
    "v_mul_f32 v83, v83, v36\n" \
    "v_mul_f32 v84, v84, v36\n" \
    "v_mul_f32 v85, v85, v36\n" \
    "v_mul_f32 v86, v86, v36\n" \
    "v_mul_f32 v87, v87, v36\n" \
    "v_cvt_pk_bf16_f32 v100, v80, v81\n" \
    "v_cvt_pk_bf16_f32 v101, v82, v83\n" \
    "v_cvt_pk_bf16_f32 v102, v84, v85\n" \
    "v_cvt_pk_bf16_f32 v103, v86, v87\n" \
    "buffer_store_dwordx4 v[100:103], v134, %[rw], s46 offen sc1\n" \
    "s_waitcnt lgkmcnt(0)\n" \
    "s_add_i32 s90, vcc_lo, %[fw]\n" \
    "35:\n" \
    "s_memtime vcc\n" \
    "s_waitcnt lgkmcnt(0)\n" \
    "s_sub_i32 s75, s90, vcc_lo\n" \
    "s_cmp_gt_i32 s75, 0\n" \
    "s_cbranch_scc1 35b\n" \
    "36:\n" \
    "buffer_load_dwordx4 v[0:3], v127, %[rw], %[ua] offen offset:0 sc1\n" \
    "buffer_load_dwordx4 v[4:7], v127, %[rw], %[ua] offen offset:1024 sc1\n" \
    "buffer_load_dwordx4 v[8:11], v127, %[rw], %[ua] offen offset:2048 sc1\n" \
    "buffer_load_dwordx4 v[12:15], v127, %[rw], %[ua] offen offset:3072 sc1\n" \
    "s_waitcnt vmcnt(3)\n" \
    "v_mfma_f32_16x16x32_bf16 v[32:35], v[176:179], v[0:3], 0\n" \
    "v_max3_u32 v40, v0, v1, v0\n" \
    "v_max3_u32 v40, v2, v3, v40\n" \
    "s_waitcnt vmcnt(2)\n" \
    "v_mfma_f32_16x16x32_bf16 v[32:35], v[176:179], v[4:7], v[32:35]\n" \
    "v_max3_u32 v40, v4, v5, v40\n" \
    "v_max3_u32 v40, v6, v7, v40\n" \
    "s_waitcnt vmcnt(1)\n" \
    "v_mfma_f32_16x16x32_bf16 v[32:35], v[176:179], v[8:11], v[32:35]\n" \
    "v_max3_u32 v40, v8, v9, v40\n" \
    "v_max3_u32 v40, v10, v11, v40\n" \
    "s_waitcnt vmcnt(0)\n" \
    "v_mfma_f32_16x16x32_bf16 v[32:35], v[176:179], v[12:15], v[32:35]\n" \
    "v_max3_u32 v40, v12, v13, v40\n" \
    "v_max3_u32 v40, v14, v15, v40\n" \
    "v_cmp_eq_u32 vcc, -1, v40\n" \
    "s_nop 3\n" \
    "s_cbranch_vccnz 38f\n" \
    "39:\n" \
    "v_add_f32_dpp v32, v32, v32 row_ror:8 row_mask:0xf bank_mask:0xf\n" \
    "v_add_f32_dpp v33, v33, v33 row_ror:8 row_mask:0xf bank_mask:0xf\n" \
    "s_mov_b32 exec_lo, 0x00ff00ff\n" \
    "s_mov_b32 exec_hi, 0x00ff00ff\n" \
    "v_fma_f32 v32, v32, v114, v172\n" \
    "v_fma_f32 v33, v33, v114, v173\n" \
    "v_cvt_pk_bf16_f32 v32, v32, v33\n" \
    "buffer_store_dword v32, v168, %[ru], s94 offen sc1\n" \
    "s_mov_b64 exec, -1\n" \
    "s_mov_b32 s36, 63\n" \
    "s_branch 9f\n" \
    "38:\n" \
    "s_add_i32 s39, s39, -1\n" \
    "s_cmp_lt_i32 s39, 0\n" \
    "s_cbranch_scc1 39b\n" \
    "s_add_i32 s30, s30, 0x10000\n" \
    "s_sleep 2\n" \
    "s_branch 36b\n" \
    "21:\n" \
    "s_add_i32 s39, s39, -1\n" \
    "s_cmp_lt_i32 s39, 0\n" \
    "s_cbranch_scc1 22b\n" \
    "buffer_load_dword v100, off, %[rw], s62 sc1\n" \
    "s_waitcnt vmcnt(0)\n" \
    "v_readfirstlane_b32 s38, v100\n" \
    "s_cmp_lt_i32 s38, s91\n" \
    "s_cbranch_scc1 21b\n" \
    "s_branch 22b\n" \
    "23:\n" \
    "s_add_i32 s39, s39, -1\n" \
    "s_cmp_lt_i32 s39, 0\n" \
    "s_cbranch_scc1 24b\n" \
    "s_sleep 4\n" \
    "buffer_load_dwordx2 v[98:99], v113, %[rs], s78 offen sc1\n" \
    "s_waitcnt vmcnt(0)\n" \
    "v_cmp_ne_u16 vcc, s91, v99\n" \
    "s_nop 1\n" \
    "s_cbranch_vccnz 23b\n" \
    "s_branch 24b\n" \
    "26:\n" \
    "s_add_i32 s39, s39, -1\n" \
    "s_cmp_lt_i32 s39, 0\n" \
    "s_cbranch_scc1 27b\n" \
    "s_sleep 4\n" \
    "s_add_i32 s44, %[qb], 0\n" \
    "s_add_i32 s45, s44, 4096\n" \
    "buffer_load_dwordx4 v[136:139], v112, %[rw], s44 offen offset:0 sc1\n" \
    "buffer_load_dwordx4 v[140:143], v112, %[rw], s44 offen offset:2048 sc1\n" \
    "buffer_load_dwordx4 v[144:147], v112, %[rw], s45 offen offset:0 sc1\n" \
    "buffer_load_dwordx4 v[148:151], v112, %[rw], s45 offen offset:2048 sc1\n" \
    "s_waitcnt vmcnt(0)\n" \
    "s_branch 25b\n" \
    "28:\n" \
    "s_add_i32 s39, s39, -1\n" \
    "s_cmp_lt_i32 s39, 0\n" \
    "s_cbranch_scc1 29b\n" \
    "s_sleep 4\n" \
    "s_add_i32 s44, %[qb], 262144\n" \
    "s_add_i32 s45, s44, 4096\n" \
    "buffer_load_dwordx4 v[72:75], v112, %[rw], s44 offen offset:0 sc1\n" \
    "buffer_load_dwordx4 v[76:79], v112, %[rw], s44 offen offset:2048 sc1\n" \
    "buffer_load_dwordx4 v[104:107], v112, %[rw], s45 offen offset:0 sc1\n" \
    "buffer_load_dwordx4 v[108:111], v112, %[rw], s45 offen offset:2048 sc1\n" \
    "s_waitcnt vmcnt(0)\n" \
    "v_max3_u32 v128, v72, v73, v72\n" \
    "v_max3_u32 v128, v74, v75, v128\n" \
    "v_max3_u32 v128, v76, v77, v128\n" \
    "v_max3_u32 v128, v78, v79, v128\n" \
    "v_max3_u32 v128, v104, v105, v128\n" \
    "v_max3_u32 v128, v106, v107, v128\n" \
    "v_max3_u32 v128, v108, v109, v128\n" \
    "v_max3_u32 v128, v110, v111, v128\n" \
    "v_cmp_eq_u32 vcc, -1, v128\n" \
    "s_nop 1\n" \
    "s_cbranch_vccnz 28b\n" \
    "s_branch 29b\n" \
    "30:\n" \
    "s_memtime vcc\n" \
    "s_waitcnt lgkmcnt(0)\n" \
    "s_sub_i32 s75, s90, vcc_lo\n" \
    "s_cmp_gt_i32 s75, 100\n" \
    "s_cbranch_scc1 30b\n" \
    "s_branch 31b\n" \
    "32:\n" \
    "v_max3_u32 v82, v0, v1, v0\n" \
    "v_max3_u32 v82, v2, v3, v82\n" \
    "v_max3_u32 v82, v4, v5, v82\n" \
    "v_max3_u32 v82, v6, v7, v82\n" \
    "v_max3_u32 v82, v8, v9, v82\n" \
    "v_max3_u32 v82, v10, v11, v82\n" \
    "v_max3_u32 v82, v12, v13, v82\n" \
    "v_max3_u32 v82, v14, v15, v82\n" \
    "v_max3_u32 v82, v16, v17, v82\n" \
    "v_max3_u32 v82, v18, v19, v82\n" \
    "v_max3_u32 v82, v20, v21, v82\n" \
    "v_max3_u32 v82, v22, v23, v82\n" \
    "v_max3_u32 v82, v24, v25, v82\n" \
    "v_max3_u32 v82, v26, v27, v82\n" \
    "v_max3_u32 v82, v28, v29, v82\n" \
    "v_max3_u32 v82, v30, v31, v82\n" \
    "v_max3_u32 v82, v32, v33, v82\n" \
    "v_max3_u32 v82, v34, v35, v82\n" \
    "v_cmp_eq_u32 vcc, -1, v82\n" \
    "s_nop 4\n" \
    "s_cbranch_vccz 34b\n" \
    "s_add_i32 s39, s39, -1\n" \
    "s_cmp_lt_i32 s39, 0\n" \
    "s_cbranch_scc1 34b\n" \
    "s_add_i32 s30, s30, 1\n" \
    "s_waitcnt lgkmcnt(0)\n" \
    "s_barrier\n" \
    "s_mov_b32 m0, s74\n" \
    "s_add_i32 s76, s74, 4096\n" \
    "buffer_load_dwordx4 v125, %[rs], s69 offen sc1 lds\n" \
    "s_mov_b32 m0, s76\n" \
    "v_mov_b64 v[80:81], 0\n" \
    "v_mov_b64 v[82:83], 0\n" \
    "buffer_load_dwordx4 v125, %[rs], s70 offen sc1 lds\n" \
    "s_movk_i32 m0, 0x2000\n" \
    "s_mov_b32 exec_lo, s27\n" \
    "s_mov_b32 exec_hi, s27\n" \
    "buffer_load_dwordx4 v127, %[rs], s71 offen sc1 lds\n" \
    "s_mov_b64 exec, -1\n" \
    "v_mov_b64 v[84:85], 0\n" \
    "v_mov_b64 v[86:87], 0\n" \
    "s_waitcnt vmcnt(0)\n" \
    "s_barrier\n" \
    "ds_read_b128 v[0:3], v37 offset:0\n" \
    "ds_read_b128 v[4:7], v38 offset:0\n" \
    "ds_read_b128 v[8:11], v39 offset:0\n" \
    "ds_read_b128 v[12:15], v132 offset:0\n" \
    "ds_read_b128 v[16:19], v37 offset:4096\n" \
    "ds_read_b128 v[20:23], v38 offset:4096\n" \
    "ds_read_b128 v[24:27], v39 offset:4096\n" \
    "ds_read_b128 v[28:31], v132 offset:4096\n" \
    "ds_read_b128 v[32:35], v126 offset:0\n" \
    "s_waitcnt lgkmcnt(8)\n" \
    "v_smfmac_f32_16x16x64_bf16 v[80:83], v[0:3], a[128:135], v124\n" \
    "s_waitcnt lgkmcnt(7)\n" \
    "v_smfmac_f32_16x16x64_bf16 v[80:83], v[4:7], a[144:151], v124\n" \
    "s_waitcnt lgkmcnt(6)\n" \
    "v_smfmac_f32_16x16x64_bf16 v[80:83], v[8:11], a[160:167], v124\n" \
    "s_waitcnt lgkmcnt(5)\n" \
    "v_smfmac_f32_16x16x64_bf16 v[80:83], v[12:15], a[176:183], v124\n" \
    "s_waitcnt lgkmcnt(4)\n" \
    "v_smfmac_f32_16x16x64_bf16 v[80:83], v[16:19], a[192:199], v124\n" \
    "s_waitcnt lgkmcnt(3)\n" \
    "v_smfmac_f32_16x16x64_bf16 v[80:83], v[20:23], a[208:215], v124\n" \
    "s_waitcnt lgkmcnt(2)\n" \
    "v_smfmac_f32_16x16x64_bf16 v[80:83], v[24:27], a[224:231], v124\n" \
    "s_waitcnt lgkmcnt(1)\n" \
    "v_smfmac_f32_16x16x64_bf16 v[80:83], v[28:31], a[240:247], v124\n" \
    "s_waitcnt lgkmcnt(0)\n" \
    "v_smfmac_f32_16x16x64_bf16 v[80:83], v[32:35], v[152:159], v124\n" \
    "v_smfmac_f32_16x16x64_bf16 v[84:87], v[0:3], a[136:143], v124\n" \
    "v_smfmac_f32_16x16x64_bf16 v[84:87], v[4:7], a[152:159], v124\n" \
    "v_smfmac_f32_16x16x64_bf16 v[84:87], v[8:11], a[168:175], v124\n" \
    "v_smfmac_f32_16x16x64_bf16 v[84:87], v[12:15], a[184:191], v124\n" \
    "v_smfmac_f32_16x16x64_bf16 v[84:87], v[16:19], a[200:207], v124\n" \
    "v_add_f32 v80, v80, v82\n" \
    "v_add_f32 v81, v81, v83\n" \
    "s_branch 32b\n" \
    "7:\n" \
    "s_mov_b32 s43, 2\n" \
    "s_nop 7\n" \
    "s_waitcnt vmcnt(0)\n" \
    "9:\n" \
    "s_mov_b32 m0, s68\n" \
    "s_waitcnt lgkmcnt(0)\n" \
    "v_mov_b32 v0, s36\n" \
    "v_mov_b32 v1, s37\n" \
    "v_mov_b32 v2, s38\n" \
    "v_mov_b32 v3, s39\n" \
    "v_mov_b32 v4, s40\n" \
    "v_mov_b32 v5, s41\n" \
    "v_mov_b32 v6, s42\n" \
    "v_mov_b32 v7, s43\n" \
    "v_mov_b32 v8, s28\n" \
    "v_mov_b32 v9, s29\n" \
    "v_mov_b32 v10, s30\n" \
    "v_mov_b32 v11, s31\n" \
    "v_mov_b32 v12, s32\n" \
    "v_mov_b32 v13, s33\n" \
    "v_mov_b32 v14, s34\n" \
    "v_mov_b32 v15, s35\n" \
    "v_mov_b32 v16, s24\n" \
    "v_mov_b32 v17, s25\n" \
    "v_mov_b32 v18, s26\n" \
    "v_mov_b32 v19, s27\n" \
    "v_mov_b32 v20, s84\n" \
    "v_mov_b32 v21, s86\n" \
    "v_mov_b32 v22, s88\n" \
    "v_mov_b32 v23, s84\n" \
    "v_mov_b32 v24, s60\n" \
    "v_mov_b32 v25, s61\n" \
    "v_mov_b32 v26, s62\n" \
    "v_mov_b32 v27, s63\n" \
    "v_mov_b32 v28, s64\n" \
    "v_mov_b32 v29, s65\n" \
    "v_mov_b32 v30, s66\n" \
    "v_mov_b32 v31, s67\n" \
    ".Lstep_end:\n" \
    ""
static_assert(2 * PART_BYTES == 589824 && 2 * OFF_CAN == 1703936 && F_ERR == 2 * OFF_CAN + 65536 && OFF_PART == 0 && OFF_VF - OFF_KF == 2048 * 4096 * 2 && OFF_BO == 65011712 && OFF_GT == 67518464 && BO_ROW == 4352, "constants of the step block");
#define STEP_CLOBBER "v32", "v33", "v34", "v35", "v36", "v37", "v38", "v39", "v40", "v41", "v42", "v43", "v44", "v45", "v46", "v47", "v48", "v49", "v50", "v51", "v52", "v53", "v54", "v55", "v56", "v57", "v58", "v59", "v60", "v61", "v62", "v63", "v64", "v65", "v66", "v67", "v68", "v69", "v70", "v71", "v72", "v73", "v74", "v75", "v76", "v77", "v78", "v79", "v80", "v81", "v82", "v83", "v84", "v85", "v86", "v87", "v88", "v89", "v90", "v91", "v92", "v93", "v94", "v95", "v98", "v99", "v100", "v101", "v102", "v103", "v104", "v105", "v106", "v107", "v108", "v109", "v110", "v111", "v112", "v113", "v114", "s24", "s25", "s26", "s27", "s28", "s29", "s30", "s31", "s32", "s33", "s34", "s35", "s36", "s37", "s38", "s39", "s40", "s41", "s42", "s43", "s44", "s45", "s46", "s47", "s48", "s49", "s50", "s51", "s52", "s53", "s54", "s55", "s56", "s57", "s58", "s59", "s60", "s61", "s62", "s63", "s64", "s65", "s66", "s67", "s68", "s69", "s70", "s71", "s72", "s73", "s74", "s75", "s76", "s77", "s78", "s79", "s80", "s81", "s82", "s83", "s84", "s85", "s86", "s87", "s88", "s89", "s90", "s91", "s92", "s93", "s94", "s95", "s96", "s97", "vcc", "scc", "memory"
#define OUT16_ASM \
    "s_mov_b32 exec_lo, %[tm]\n" \
    "s_mov_b32 exec_hi, 0\n" \
    "buffer_load_dword v79, %[tv], %[rw], %[so] offen sc1\n" \
    "s_mov_b64 exec, -1\n" \
    "s_add_i32 s44, %[so], 4096\n" \
    "s_add_i32 s45, %[so], 8192\n" \
    "s_add_i32 s46, %[so], 12288\n" \
    "buffer_load_dwordx4 v[16:19], %[vo], %[rw], %[so] offen offset:0 sc1\n" \
    "ds_read_b128 v[80:83], %[wl] offset:0\n" \
    "ds_read_b128 v[84:87], %[wl] offset:1024\n" \
    "s_nop 7\n" \
    "v_mov_b32 v104, 0\n" \
    "buffer_load_dwordx4 v[20:23], %[vo], %[rw], %[so] offen offset:256 sc1\n" \
    "s_nop 7\n" \
    "s_nop 3\n" \
    "buffer_load_dwordx4 v[24:27], %[vo], %[rw], %[so] offen offset:1024 sc1\n" \
    "s_nop 7\n" \
    "s_nop 3\n" \
    "buffer_load_dwordx4 v[28:31], %[vo], %[rw], %[so] offen offset:1280 sc1\n" \
    "s_nop 7\n" \
    "s_nop 3\n" \
    "buffer_load_dwordx4 v[32:35], %[vo], %[rw], %[so] offen offset:2048 sc1\n" \
    "s_nop 7\n" \
    "s_nop 3\n" \
    "buffer_load_dwordx4 v[36:39], %[vo], %[rw], %[so] offen offset:2304 sc1\n" \
    "s_nop 7\n" \
    "s_nop 3\n" \
    "buffer_load_dwordx4 v[40:43], %[vo], %[rw], %[so] offen offset:3072 sc1\n" \
    "s_nop 7\n" \
    "s_nop 3\n" \
    "buffer_load_dwordx4 v[44:47], %[vo], %[rw], %[so] offen offset:3328 sc1\n" \
    "s_nop 7\n" \
    "s_nop 3\n" \
    "buffer_load_dwordx4 v[48:51], %[vo], %[rw], s44 offen offset:0 sc1\n" \
    "s_nop 7\n" \
    "s_nop 3\n" \
    "buffer_load_dwordx4 v[52:55], %[vo], %[rw], s44 offen offset:256 sc1\n" \
    "s_nop 7\n" \
    "s_nop 3\n" \
    "buffer_load_dwordx4 v[56:59], %[vo], %[rw], s44 offen offset:1024 sc1\n" \
    "s_nop 7\n" \
    "s_nop 3\n" \
    "buffer_load_dwordx4 v[60:63], %[vo], %[rw], s44 offen offset:1280 sc1\n" \
    "ds_read_b128 v[88:91], %[wl] offset:2048\n" \
    "s_waitcnt vmcnt(11)\n" \
    "v_mfma_f32_16x16x32_bf16 v[0:3], a[0:3], v[16:19], 0\n" \
    "v_max3_u32 v104, v104, v16, v17\n" \
    "v_mfma_f32_16x16x32_bf16 v[4:7], a[128:131], v[16:19], 0\n" \
    "v_max3_u32 v104, v104, v18, v19\n" \
    "v_mfma_f32_16x16x32_bf16 v[8:11], v[128:131], v[16:19], 0\n" \
    "s_waitcnt lgkmcnt(2)\n" \
    "v_mfma_f32_16x16x32_bf16 v[12:15], v[80:83], v[16:19], 0\n" \
    "buffer_load_dwordx4 v[64:67], %[vo], %[rw], s44 offen offset:2048 sc1\n" \
    "ds_read_b128 v[80:83], %[wl] offset:3072\n" \
    "s_waitcnt vmcnt(11)\n" \
    "v_mfma_f32_16x16x32_bf16 v[0:3], a[4:7], v[20:23], v[0:3]\n" \
    "v_max3_u32 v104, v104, v20, v21\n" \
    "v_mfma_f32_16x16x32_bf16 v[4:7], a[132:135], v[20:23], v[4:7]\n" \
    "v_max3_u32 v104, v104, v22, v23\n" \
    "v_mfma_f32_16x16x32_bf16 v[8:11], v[132:135], v[20:23], v[8:11]\n" \
    "s_waitcnt lgkmcnt(2)\n" \
    "v_mfma_f32_16x16x32_bf16 v[12:15], v[84:87], v[20:23], v[12:15]\n" \
    "buffer_load_dwordx4 v[68:71], %[vo], %[rw], s44 offen offset:2304 sc1\n" \
    "ds_read_b128 v[84:87], %[wl] offset:4096\n" \
    "s_waitcnt vmcnt(11)\n" \
    "v_mfma_f32_16x16x32_bf16 v[0:3], a[8:11], v[24:27], v[0:3]\n" \
    "v_max3_u32 v104, v104, v24, v25\n" \
    "v_mfma_f32_16x16x32_bf16 v[4:7], a[136:139], v[24:27], v[4:7]\n" \
    "v_max3_u32 v104, v104, v26, v27\n" \
    "v_mfma_f32_16x16x32_bf16 v[8:11], v[136:139], v[24:27], v[8:11]\n" \
    "s_waitcnt lgkmcnt(2)\n" \
    "v_mfma_f32_16x16x32_bf16 v[12:15], v[88:91], v[24:27], v[12:15]\n" \
    "buffer_load_dwordx4 v[72:75], %[vo], %[rw], s44 offen offset:3072 sc1\n" \
    "ds_read_b128 v[88:91], %[wl] offset:5120\n" \
    "s_waitcnt vmcnt(11)\n" \
    "v_mfma_f32_16x16x32_bf16 v[0:3], a[12:15], v[28:31], v[0:3]\n" \
    "v_max3_u32 v104, v104, v28, v29\n" \
    "v_mfma_f32_16x16x32_bf16 v[4:7], a[140:143], v[28:31], v[4:7]\n" \
    "v_max3_u32 v104, v104, v30, v31\n" \
    "v_mfma_f32_16x16x32_bf16 v[8:11], v[140:143], v[28:31], v[8:11]\n" \
    "s_waitcnt lgkmcnt(2)\n" \
    "v_mfma_f32_16x16x32_bf16 v[12:15], v[80:83], v[28:31], v[12:15]\n" \
    "buffer_load_dwordx4 v[76:79], %[vo], %[rw], s44 offen offset:3328 sc1\n" \
    "ds_read_b128 v[80:83], %[wl] offset:6144\n" \
    "s_waitcnt vmcnt(11)\n" \
    "v_mfma_f32_16x16x32_bf16 v[0:3], a[16:19], v[32:35], v[0:3]\n" \
    "v_max3_u32 v104, v104, v32, v33\n" \
    "v_mfma_f32_16x16x32_bf16 v[4:7], a[144:147], v[32:35], v[4:7]\n" \
    "v_max3_u32 v104, v104, v34, v35\n" \
    "v_mfma_f32_16x16x32_bf16 v[8:11], v[144:147], v[32:35], v[8:11]\n" \
    "s_waitcnt lgkmcnt(2)\n" \
    "v_mfma_f32_16x16x32_bf16 v[12:15], v[84:87], v[32:35], v[12:15]\n" \
    "buffer_load_dwordx4 v[16:19], %[vo], %[rw], s45 offen offset:0 sc1\n" \
    "ds_read_b128 v[84:87], %[wl] offset:7168\n" \
    "s_waitcnt vmcnt(11)\n" \
    "v_mfma_f32_16x16x32_bf16 v[0:3], a[20:23], v[36:39], v[0:3]\n" \
    "v_max3_u32 v104, v104, v36, v37\n" \
    "v_mfma_f32_16x16x32_bf16 v[4:7], a[148:151], v[36:39], v[4:7]\n" \
    "v_max3_u32 v104, v104, v38, v39\n" \
    "v_mfma_f32_16x16x32_bf16 v[8:11], v[148:151], v[36:39], v[8:11]\n" \
    "s_waitcnt lgkmcnt(2)\n" \
    "v_mfma_f32_16x16x32_bf16 v[12:15], v[88:91], v[36:39], v[12:15]\n" \
    "buffer_load_dwordx4 v[20:23], %[vo], %[rw], s45 offen offset:256 sc1\n" \
    "ds_read_b128 v[88:91], %[wl] offset:8192\n" \
    "s_waitcnt vmcnt(11)\n" \
    "v_mfma_f32_16x16x32_bf16 v[0:3], a[24:27], v[40:43], v[0:3]\n" \
    "v_max3_u32 v104, v104, v40, v41\n" \
    "v_mfma_f32_16x16x32_bf16 v[4:7], a[152:155], v[40:43], v[4:7]\n" \
    "v_max3_u32 v104, v104, v42, v43\n" \
    "v_mfma_f32_16x16x32_bf16 v[8:11], v[152:155], v[40:43], v[8:11]\n" \
    "s_waitcnt lgkmcnt(2)\n" \
    "v_mfma_f32_16x16x32_bf16 v[12:15], v[80:83], v[40:43], v[12:15]\n" \
    "buffer_load_dwordx4 v[24:27], %[vo], %[rw], s45 offen offset:1024 sc1\n" \
    "ds_read_b128 v[80:83], %[wl] offset:9216\n" \
    "s_waitcnt vmcnt(11)\n" \
    "v_mfma_f32_16x16x32_bf16 v[0:3], a[28:31], v[44:47], v[0:3]\n" \
    "v_max3_u32 v104, v104, v44, v45\n" \
    "v_mfma_f32_16x16x32_bf16 v[4:7], a[156:159], v[44:47], v[4:7]\n" \
    "v_max3_u32 v104, v104, v46, v47\n" \
    "v_mfma_f32_16x16x32_bf16 v[8:11], v[156:159], v[44:47], v[8:11]\n" \
    "s_waitcnt lgkmcnt(2)\n" \
    "v_mfma_f32_16x16x32_bf16 v[12:15], v[84:87], v[44:47], v[12:15]\n" \
    "buffer_load_dwordx4 v[28:31], %[vo], %[rw], s45 offen offset:1280 sc1\n" \
    "ds_read_b128 v[84:87], %[wl] offset:10240\n" \
    "s_waitcnt vmcnt(11)\n" \
    "v_mfma_f32_16x16x32_bf16 v[0:3], a[32:35], v[48:51], v[0:3]\n" \
    "v_max3_u32 v104, v104, v48, v49\n" \
    "v_mfma_f32_16x16x32_bf16 v[4:7], a[160:163], v[48:51], v[4:7]\n" \
    "v_max3_u32 v104, v104, v50, v51\n" \
    "v_mfma_f32_16x16x32_bf16 v[8:11], v[160:163], v[48:51], v[8:11]\n" \
    "s_waitcnt lgkmcnt(2)\n" \
    "v_mfma_f32_16x16x32_bf16 v[12:15], v[88:91], v[48:51], v[12:15]\n" \
    "buffer_load_dwordx4 v[32:35], %[vo], %[rw], s45 offen offset:2048 sc1\n" \
    "ds_read_b128 v[88:91], %[wl] offset:11264\n" \
    "s_waitcnt vmcnt(11)\n" \
    "v_mfma_f32_16x16x32_bf16 v[0:3], a[36:39], v[52:55], v[0:3]\n" \
    "v_max3_u32 v104, v104, v52, v53\n" \
    "v_mfma_f32_16x16x32_bf16 v[4:7], a[164:167], v[52:55], v[4:7]\n" \
    "v_max3_u32 v104, v104, v54, v55\n" \
    "v_mfma_f32_16x16x32_bf16 v[8:11], v[164:167], v[52:55], v[8:11]\n" \
    "s_waitcnt lgkmcnt(2)\n" \
    "v_mfma_f32_16x16x32_bf16 v[12:15], v[80:83], v[52:55], v[12:15]\n" \
    "buffer_load_dwordx4 v[36:39], %[vo], %[rw], s45 offen offset:2304 sc1\n" \
    "ds_read_b128 v[80:83], %[wl] offset:12288\n" \
    "s_waitcnt vmcnt(11)\n" \
    "v_mfma_f32_16x16x32_bf16 v[0:3], a[40:43], v[56:59], v[0:3]\n" \
    "v_max3_u32 v104, v104, v56, v57\n" \
    "v_mfma_f32_16x16x32_bf16 v[4:7], a[168:171], v[56:59], v[4:7]\n" \
    "v_max3_u32 v104, v104, v58, v59\n" \
    "v_mfma_f32_16x16x32_bf16 v[8:11], v[168:171], v[56:59], v[8:11]\n" \
    "s_waitcnt lgkmcnt(2)\n" \
    "v_mfma_f32_16x16x32_bf16 v[12:15], v[84:87], v[56:59], v[12:15]\n" \
    "buffer_load_dwordx4 v[40:43], %[vo], %[rw], s45 offen offset:3072 sc1\n" \
    "ds_read_b128 v[84:87], %[wl] offset:13312\n" \
    "s_waitcnt vmcnt(11)\n" \
    "v_mfma_f32_16x16x32_bf16 v[0:3], a[44:47], v[60:63], v[0:3]\n" \
    "v_max3_u32 v104, v104, v60, v61\n" \
    "v_mfma_f32_16x16x32_bf16 v[4:7], a[172:175], v[60:63], v[4:7]\n" \
    "v_max3_u32 v104, v104, v62, v63\n" \
    "v_mfma_f32_16x16x32_bf16 v[8:11], v[172:175], v[60:63], v[8:11]\n" \
    "s_waitcnt lgkmcnt(2)\n" \
    "v_mfma_f32_16x16x32_bf16 v[12:15], v[88:91], v[60:63], v[12:15]\n" \
    "buffer_load_dwordx4 v[44:47], %[vo], %[rw], s45 offen offset:3328 sc1\n" \
    "ds_read_b128 v[88:91], %[wl] offset:14336\n" \
    "s_waitcnt vmcnt(11)\n" \
    "v_mfma_f32_16x16x32_bf16 v[0:3], a[48:51], v[64:67], v[0:3]\n" \
    "v_max3_u32 v104, v104, v64, v65\n" \
    "v_mfma_f32_16x16x32_bf16 v[4:7], a[176:179], v[64:67], v[4:7]\n" \
    "v_max3_u32 v104, v104, v66, v67\n" \
    "v_mfma_f32_16x16x32_bf16 v[8:11], v[176:179], v[64:67], v[8:11]\n" \
    "s_waitcnt lgkmcnt(2)\n" \
    "v_mfma_f32_16x16x32_bf16 v[12:15], v[80:83], v[64:67], v[12:15]\n" \
    "buffer_load_dwordx4 v[48:51], %[vo], %[rw], s46 offen offset:0 sc1\n" \
    "ds_read_b128 v[80:83], %[wl] offset:15360\n" \
    "s_waitcnt vmcnt(11)\n" \
    "v_mfma_f32_16x16x32_bf16 v[0:3], a[52:55], v[68:71], v[0:3]\n" \
    "v_max3_u32 v104, v104, v68, v69\n" \
    "v_mfma_f32_16x16x32_bf16 v[4:7], a[180:183], v[68:71], v[4:7]\n" \
    "v_max3_u32 v104, v104, v70, v71\n" \
    "v_mfma_f32_16x16x32_bf16 v[8:11], v[180:183], v[68:71], v[8:11]\n" \
    "s_waitcnt lgkmcnt(2)\n" \
    "v_mfma_f32_16x16x32_bf16 v[12:15], v[84:87], v[68:71], v[12:15]\n" \
    "buffer_load_dwordx4 v[52:55], %[vo], %[rw], s46 offen offset:256 sc1\n" \
    "ds_read_b128 v[84:87], %[wl] offset:16384\n" \
    "s_waitcnt vmcnt(11)\n" \
    "v_mfma_f32_16x16x32_bf16 v[0:3], a[56:59], v[72:75], v[0:3]\n" \
    "v_max3_u32 v104, v104, v72, v73\n" \
    "v_mfma_f32_16x16x32_bf16 v[4:7], a[184:187], v[72:75], v[4:7]\n" \
    "v_max3_u32 v104, v104, v74, v75\n" \
    "v_mfma_f32_16x16x32_bf16 v[8:11], v[184:187], v[72:75], v[8:11]\n" \
    "s_waitcnt lgkmcnt(2)\n" \
    "v_mfma_f32_16x16x32_bf16 v[12:15], v[88:91], v[72:75], v[12:15]\n" \
    "buffer_load_dwordx4 v[56:59], %[vo], %[rw], s46 offen offset:1024 sc1\n" \
    "ds_read_b128 v[88:91], %[wl] offset:17408\n" \
    "s_waitcnt vmcnt(11)\n" \
    "v_mfma_f32_16x16x32_bf16 v[0:3], a[60:63], v[76:79], v[0:3]\n" \
    "v_max3_u32 v104, v104, v76, v77\n" \
    "v_mfma_f32_16x16x32_bf16 v[4:7], a[188:191], v[76:79], v[4:7]\n" \
    "v_max3_u32 v104, v104, v78, v79\n" \
    "v_mfma_f32_16x16x32_bf16 v[8:11], v[188:191], v[76:79], v[8:11]\n" \
    "s_waitcnt lgkmcnt(2)\n" \
    "v_mfma_f32_16x16x32_bf16 v[12:15], v[80:83], v[76:79], v[12:15]\n" \
    "buffer_load_dwordx4 v[60:63], %[vo], %[rw], s46 offen offset:1280 sc1\n" \
    "ds_read_b128 v[80:83], %[wl] offset:18432\n" \
    "s_waitcnt vmcnt(11)\n" \
    "v_mfma_f32_16x16x32_bf16 v[0:3], a[64:67], v[16:19], v[0:3]\n" \
    "v_max3_u32 v104, v104, v16, v17\n" \
    "v_mfma_f32_16x16x32_bf16 v[4:7], a[192:195], v[16:19], v[4:7]\n" \
    "v_max3_u32 v104, v104, v18, v19\n" \
    "v_mfma_f32_16x16x32_bf16 v[8:11], v[192:195], v[16:19], v[8:11]\n" \
    "s_waitcnt lgkmcnt(2)\n" \
    "v_mfma_f32_16x16x32_bf16 v[12:15], v[84:87], v[16:19], v[12:15]\n" \
    "buffer_load_dwordx4 v[64:67], %[vo], %[rw], s46 offen offset:2048 sc1\n" \
    "ds_read_b128 v[84:87], %[wl] offset:19456\n" \
    "s_waitcnt vmcnt(11)\n" \
    "v_mfma_f32_16x16x32_bf16 v[0:3], a[68:71], v[20:23], v[0:3]\n" \
    "v_max3_u32 v104, v104, v20, v21\n" \
    "v_mfma_f32_16x16x32_bf16 v[4:7], a[196:199], v[20:23], v[4:7]\n" \
    "v_max3_u32 v104, v104, v22, v23\n" \
    "v_mfma_f32_16x16x32_bf16 v[8:11], v[196:199], v[20:23], v[8:11]\n" \
    "s_waitcnt lgkmcnt(2)\n" \
    "v_mfma_f32_16x16x32_bf16 v[12:15], v[88:91], v[20:23], v[12:15]\n" \
    "buffer_load_dwordx4 v[68:71], %[vo], %[rw], s46 offen offset:2304 sc1\n" \
    "ds_read_b128 v[88:91], %[wl] offset:20480\n" \
    "s_waitcnt vmcnt(11)\n" \
    "v_mfma_f32_16x16x32_bf16 v[0:3], a[72:75], v[24:27], v[0:3]\n" \
    "v_max3_u32 v104, v104, v24, v25\n" \
    "v_mfma_f32_16x16x32_bf16 v[4:7], a[200:203], v[24:27], v[4:7]\n" \
    "v_max3_u32 v104, v104, v26, v27\n" \
    "v_mfma_f32_16x16x32_bf16 v[8:11], v[200:203], v[24:27], v[8:11]\n" \
    "s_waitcnt lgkmcnt(2)\n" \
    "v_mfma_f32_16x16x32_bf16 v[12:15], v[80:83], v[24:27], v[12:15]\n" \
    "buffer_load_dwordx4 v[72:75], %[vo], %[rw], s46 offen offset:3072 sc1\n" \
    "ds_read_b128 v[80:83], %[wl] offset:21504\n" \
    "s_waitcnt vmcnt(11)\n" \
    "v_mfma_f32_16x16x32_bf16 v[0:3], a[76:79], v[28:31], v[0:3]\n" \
    "v_max3_u32 v104, v104, v28, v29\n" \
    "v_mfma_f32_16x16x32_bf16 v[4:7], a[204:207], v[28:31], v[4:7]\n" \
    "v_max3_u32 v104, v104, v30, v31\n" \
    "v_mfma_f32_16x16x32_bf16 v[8:11], v[204:207], v[28:31], v[8:11]\n" \
    "s_waitcnt lgkmcnt(2)\n" \
    "v_mfma_f32_16x16x32_bf16 v[12:15], v[84:87], v[28:31], v[12:15]\n" \
    "buffer_load_dwordx4 v[76:79], %[vo], %[rw], s46 offen offset:3328 sc1\n" \
    "ds_read_b128 v[84:87], %[wl] offset:22528\n" \
    "s_waitcnt vmcnt(11)\n" \
    "v_mfma_f32_16x16x32_bf16 v[0:3], a[80:83], v[32:35], v[0:3]\n" \
    "v_max3_u32 v104, v104, v32, v33\n" \
    "v_mfma_f32_16x16x32_bf16 v[4:7], a[208:211], v[32:35], v[4:7]\n" \
    "v_max3_u32 v104, v104, v34, v35\n" \
    "v_mfma_f32_16x16x32_bf16 v[8:11], v[208:211], v[32:35], v[8:11]\n" \
    "s_waitcnt lgkmcnt(2)\n" \
    "v_mfma_f32_16x16x32_bf16 v[12:15], v[88:91], v[32:35], v[12:15]\n" \
    "buffer_load_dwordx2 v[106:107], %[bv], %[rw], %[bs] offen sc1\n" \
    "ds_read_b128 v[88:91], %[wl] offset:23552\n" \
    "s_waitcnt vmcnt(11)\n" \
    "v_mfma_f32_16x16x32_bf16 v[0:3], a[84:87], v[36:39], v[0:3]\n" \
    "v_max3_u32 v104, v104, v36, v37\n" \
    "v_mfma_f32_16x16x32_bf16 v[4:7], a[212:215], v[36:39], v[4:7]\n" \
    "v_max3_u32 v104, v104, v38, v39\n" \
    "v_mfma_f32_16x16x32_bf16 v[8:11], v[212:215], v[36:39], v[8:11]\n" \
    "s_waitcnt lgkmcnt(2)\n" \
    "v_mfma_f32_16x16x32_bf16 v[12:15], v[80:83], v[36:39], v[12:15]\n" \
    "buffer_load_dword v108, %[gv], %[rw], %[gs] offen sc1\n" \
    "ds_read_b128 v[80:83], %[wl] offset:24576\n" \
    "s_waitcnt vmcnt(11)\n" \
    "v_mfma_f32_16x16x32_bf16 v[0:3], a[88:91], v[40:43], v[0:3]\n" \
    "v_max3_u32 v104, v104, v40, v41\n" \
    "v_mfma_f32_16x16x32_bf16 v[4:7], a[216:219], v[40:43], v[4:7]\n" \
    "v_max3_u32 v104, v104, v42, v43\n" \
    "v_mfma_f32_16x16x32_bf16 v[8:11], v[216:219], v[40:43], v[8:11]\n" \
    "s_waitcnt lgkmcnt(2)\n" \
    "v_mfma_f32_16x16x32_bf16 v[12:15], v[84:87], v[40:43], v[12:15]\n" \
    "buffer_load_dword v105, %[sv], %[rw], %[ss] offen sc1\n" \
    "ds_read_b128 v[84:87], %[wl] offset:25600\n" \
    "s_waitcnt vmcnt(11)\n" \
    "v_mfma_f32_16x16x32_bf16 v[0:3], a[92:95], v[44:47], v[0:3]\n" \
    "v_max3_u32 v104, v104, v44, v45\n" \
    "v_mfma_f32_16x16x32_bf16 v[4:7], a[220:223], v[44:47], v[4:7]\n" \
    "v_max3_u32 v104, v104, v46, v47\n" \
    "v_mfma_f32_16x16x32_bf16 v[8:11], v[220:223], v[44:47], v[8:11]\n" \
    "s_waitcnt lgkmcnt(2)\n" \
    "v_mfma_f32_16x16x32_bf16 v[12:15], v[88:91], v[44:47], v[12:15]\n" \
    "ds_read_b128 v[88:91], %[wl] offset:26624\n" \
    "s_waitcnt vmcnt(10)\n" \
    "v_mfma_f32_16x16x32_bf16 v[0:3], a[96:99], v[48:51], v[0:3]\n" \
    "v_max3_u32 v104, v104, v48, v49\n" \
    "v_mfma_f32_16x16x32_bf16 v[4:7], a[224:227], v[48:51], v[4:7]\n" \
    "v_max3_u32 v104, v104, v50, v51\n" \
    "v_mfma_f32_16x16x32_bf16 v[8:11], v[224:227], v[48:51], v[8:11]\n" \
    "s_waitcnt lgkmcnt(2)\n" \
    "v_mfma_f32_16x16x32_bf16 v[12:15], v[80:83], v[48:51], v[12:15]\n" \
    "ds_read_b128 v[80:83], %[wl] offset:27648\n" \
    "s_waitcnt vmcnt(9)\n" \
    "v_mfma_f32_16x16x32_bf16 v[0:3], a[100:103], v[52:55], v[0:3]\n" \
    "v_max3_u32 v104, v104, v52, v53\n" \
    "v_mfma_f32_16x16x32_bf16 v[4:7], a[228:231], v[52:55], v[4:7]\n" \
    "v_max3_u32 v104, v104, v54, v55\n" \
    "v_mfma_f32_16x16x32_bf16 v[8:11], v[228:231], v[52:55], v[8:11]\n" \
    "s_waitcnt lgkmcnt(2)\n" \
    "v_mfma_f32_16x16x32_bf16 v[12:15], v[84:87], v[52:55], v[12:15]\n" \
    "ds_read_b128 v[84:87], %[wl] offset:28672\n" \
    "s_waitcnt vmcnt(8)\n" \
    "v_mfma_f32_16x16x32_bf16 v[0:3], a[104:107], v[56:59], v[0:3]\n" \
    "v_max3_u32 v104, v104, v56, v57\n" \
    "v_mfma_f32_16x16x32_bf16 v[4:7], a[232:235], v[56:59], v[4:7]\n" \
    "v_max3_u32 v104, v104, v58, v59\n" \
    "v_mfma_f32_16x16x32_bf16 v[8:11], v[232:235], v[56:59], v[8:11]\n" \
    "s_waitcnt lgkmcnt(2)\n" \
    "v_mfma_f32_16x16x32_bf16 v[12:15], v[88:91], v[56:59], v[12:15]\n" \
    "ds_read_b128 v[88:91], %[wl] offset:29696\n" \
    "s_waitcnt vmcnt(7)\n" \
    "v_mfma_f32_16x16x32_bf16 v[0:3], a[108:111], v[60:63], v[0:3]\n" \
    "v_max3_u32 v104, v104, v60, v61\n" \
    "v_mfma_f32_16x16x32_bf16 v[4:7], a[236:239], v[60:63], v[4:7]\n" \
    "v_max3_u32 v104, v104, v62, v63\n" \
    "v_mfma_f32_16x16x32_bf16 v[8:11], v[236:239], v[60:63], v[8:11]\n" \
    "s_waitcnt lgkmcnt(2)\n" \
    "v_mfma_f32_16x16x32_bf16 v[12:15], v[80:83], v[60:63], v[12:15]\n" \
    "ds_read_b128 v[80:83], %[wl] offset:30720\n" \
    "s_waitcnt vmcnt(6)\n" \
    "v_mfma_f32_16x16x32_bf16 v[0:3], a[112:115], v[64:67], v[0:3]\n" \
    "v_max3_u32 v104, v104, v64, v65\n" \
    "v_mfma_f32_16x16x32_bf16 v[4:7], a[240:243], v[64:67], v[4:7]\n" \
    "v_max3_u32 v104, v104, v66, v67\n" \
    "v_mfma_f32_16x16x32_bf16 v[8:11], v[240:243], v[64:67], v[8:11]\n" \
    "s_waitcnt lgkmcnt(2)\n" \
    "v_mfma_f32_16x16x32_bf16 v[12:15], v[84:87], v[64:67], v[12:15]\n" \
    "ds_read_b128 v[84:87], %[wl] offset:31744\n" \
    "s_waitcnt vmcnt(5)\n" \
    "v_mfma_f32_16x16x32_bf16 v[0:3], a[116:119], v[68:71], v[0:3]\n" \
    "v_max3_u32 v104, v104, v68, v69\n" \
    "v_mfma_f32_16x16x32_bf16 v[4:7], a[244:247], v[68:71], v[4:7]\n" \
    "v_max3_u32 v104, v104, v70, v71\n" \
    "v_mfma_f32_16x16x32_bf16 v[8:11], v[244:247], v[68:71], v[8:11]\n" \
    "s_waitcnt lgkmcnt(2)\n" \
    "v_mfma_f32_16x16x32_bf16 v[12:15], v[88:91], v[68:71], v[12:15]\n" \
    "s_waitcnt vmcnt(4)\n" \
    "v_mfma_f32_16x16x32_bf16 v[0:3], a[120:123], v[72:75], v[0:3]\n" \
    "v_max3_u32 v104, v104, v72, v73\n" \
    "v_mfma_f32_16x16x32_bf16 v[4:7], a[248:251], v[72:75], v[4:7]\n" \
    "v_max3_u32 v104, v104, v74, v75\n" \
    "v_mfma_f32_16x16x32_bf16 v[8:11], v[248:251], v[72:75], v[8:11]\n" \
    "s_waitcnt lgkmcnt(1)\n" \
    "v_mfma_f32_16x16x32_bf16 v[12:15], v[80:83], v[72:75], v[12:15]\n" \
    "s_waitcnt vmcnt(3)\n" \
    "v_mfma_f32_16x16x32_bf16 v[0:3], a[124:127], v[76:79], v[0:3]\n" \
    "v_max3_u32 v104, v104, v76, v77\n" \
    "v_mfma_f32_16x16x32_bf16 v[4:7], a[252:255], v[76:79], v[4:7]\n" \
    "v_max3_u32 v104, v104, v78, v79\n" \
    "v_mfma_f32_16x16x32_bf16 v[8:11], v[252:255], v[76:79], v[8:11]\n" \
    "s_waitcnt lgkmcnt(0)\n" \
    "v_mfma_f32_16x16x32_bf16 v[12:15], v[84:87], v[76:79], v[12:15]\n" \
    "s_waitcnt vmcnt(0)\n" \
    "s_nop 7\n" \
    ""
#define OUT8_ASM \
    "s_mov_b32 exec_lo, %[tm]\n" \
    "s_mov_b32 exec_hi, 0\n" \
    "buffer_load_dword v79, %[tv], %[rw], %[so] offen sc1\n" \
    "s_mov_b64 exec, -1\n" \
    "s_add_i32 s44, %[so], 4096\n" \
    "s_add_i32 s45, %[so], 8192\n" \
    "s_add_i32 s46, %[so], 12288\n" \
    "buffer_load_dwordx4 v[16:19], %[vo], %[rw], %[so] offen offset:0 sc1\n" \
    "ds_read_b128 v[80:83], %[wl] offset:0\n" \
    "ds_read_b128 v[84:87], %[wl] offset:1024\n" \
    "ds_read_b128 v[88:91], %[wl] offset:2048\n" \
    "ds_read_b128 v[92:95], %[wl] offset:3072\n" \
    "v_mov_b64 v[0:1], 0\n" \
    "v_mov_b64 v[2:3], 0\n" \
    "v_mov_b64 v[4:5], 0\n" \
    "v_mov_b64 v[6:7], 0\n" \
    "v_mov_b64 v[8:9], 0\n" \
    "v_mov_b64 v[10:11], 0\n" \
    "v_mov_b64 v[12:13], 0\n" \
    "v_mov_b64 v[14:15], 0\n" \
    "v_mov_b32 v104, 0\n" \
    "buffer_load_dwordx4 v[20:23], %[vo], %[rw], %[so] offen offset:1024 sc1\n" \
    "s_nop 7\n" \
    "s_nop 3\n" \
    "buffer_load_dwordx4 v[24:27], %[vo], %[rw], %[so] offen offset:2048 sc1\n" \
    "s_nop 7\n" \
    "s_nop 3\n" \
    "buffer_load_dwordx4 v[28:31], %[vo], %[rw], %[so] offen offset:3072 sc1\n" \
    "s_nop 7\n" \
    "s_nop 3\n" \
    "buffer_load_dwordx4 v[32:35], %[vo], %[rw], s44 offen offset:0 sc1\n" \
    "s_nop 7\n" \
    "s_nop 3\n" \
    "buffer_load_dwordx4 v[36:39], %[vo], %[rw], s44 offen offset:1024 sc1\n" \
    "s_nop 7\n" \
    "s_nop 3\n" \
    "buffer_load_dwordx4 v[40:43], %[vo], %[rw], s44 offen offset:2048 sc1\n" \
    "s_nop 7\n" \
    "s_nop 3\n" \
    "buffer_load_dwordx4 v[44:47], %[vo], %[rw], s44 offen offset:3072 sc1\n" \
    "s_nop 7\n" \
    "s_nop 3\n" \
    "buffer_load_dwordx4 v[48:51], %[vo], %[rw], s45 offen offset:0 sc1\n" \
    "s_nop 7\n" \
    "s_nop 3\n" \
    "buffer_load_dwordx4 v[52:55], %[vo], %[rw], s45 offen offset:1024 sc1\n" \
    "ds_read_b128 v[96:99], %[wl] offset:4096\n" \
    "ds_read_b128 v[100:103], %[wl] offset:5120\n" \
    "s_waitcnt vmcnt(9)\n" \
    "v_smfmac_f32_16x16x64_bf16 v[0:3], v[16:19], a[0:7], %[idx]\n" \
    "v_max3_u32 v104, v104, v16, v17\n" \
    "v_smfmac_f32_16x16x64_bf16 v[4:7], v[16:19], a[128:135], %[idx]\n" \
    "v_max3_u32 v104, v104, v18, v19\n" \
    "v_smfmac_f32_16x16x64_bf16 v[8:11], v[16:19], v[128:135], %[idx]\n" \
    "s_waitcnt lgkmcnt(4)\n" \
    "v_smfmac_f32_16x16x64_bf16 v[12:15], v[16:19], v[80:87], %[idx]\n" \
    "buffer_load_dwordx4 v[56:59], %[vo], %[rw], s45 offen offset:2048 sc1\n" \
    "ds_read_b128 v[80:83], %[wl] offset:6144\n" \
    "ds_read_b128 v[84:87], %[wl] offset:7168\n" \
    "s_waitcnt vmcnt(9)\n" \
    "v_smfmac_f32_16x16x64_bf16 v[0:3], v[20:23], a[8:15], %[idx]\n" \
    "v_max3_u32 v104, v104, v20, v21\n" \
    "v_smfmac_f32_16x16x64_bf16 v[4:7], v[20:23], a[136:143], %[idx]\n" \
    "v_max3_u32 v104, v104, v22, v23\n" \
    "v_smfmac_f32_16x16x64_bf16 v[8:11], v[20:23], v[136:143], %[idx]\n" \
    "s_waitcnt lgkmcnt(4)\n" \
    "v_smfmac_f32_16x16x64_bf16 v[12:15], v[20:23], v[88:95], %[idx]\n" \
    "buffer_load_dwordx4 v[60:63], %[vo], %[rw], s45 offen offset:3072 sc1\n" \
    "ds_read_b128 v[88:91], %[wl] offset:8192\n" \
    "ds_read_b128 v[92:95], %[wl] offset:9216\n" \
    "s_waitcnt vmcnt(9)\n" \
    "v_smfmac_f32_16x16x64_bf16 v[0:3], v[24:27], a[16:23], %[idx]\n" \
    "v_max3_u32 v104, v104, v24, v25\n" \
    "v_smfmac_f32_16x16x64_bf16 v[4:7], v[24:27], a[144:151], %[idx]\n" \
    "v_max3_u32 v104, v104, v26, v27\n" \
    "v_smfmac_f32_16x16x64_bf16 v[8:11], v[24:27], v[144:151], %[idx]\n" \
    "s_waitcnt lgkmcnt(4)\n" \
    "v_smfmac_f32_16x16x64_bf16 v[12:15], v[24:27], v[96:103], %[idx]\n" \
    "buffer_load_dwordx4 v[64:67], %[vo], %[rw], s46 offen offset:0 sc1\n" \
    "ds_read_b128 v[96:99], %[wl] offset:10240\n" \
    "ds_read_b128 v[100:103], %[wl] offset:11264\n" \
    "s_waitcnt vmcnt(9)\n" \
    "v_smfmac_f32_16x16x64_bf16 v[0:3], v[28:31], a[24:31], %[idx]\n" \
    "v_max3_u32 v104, v104, v28, v29\n" \
    "v_smfmac_f32_16x16x64_bf16 v[4:7], v[28:31], a[152:159], %[idx]\n" \
    "v_max3_u32 v104, v104, v30, v31\n" \
    "v_smfmac_f32_16x16x64_bf16 v[8:11], v[28:31], v[152:159], %[idx]\n" \
    "s_waitcnt lgkmcnt(4)\n" \
    "v_smfmac_f32_16x16x64_bf16 v[12:15], v[28:31], v[80:87], %[idx]\n" \
    "buffer_load_dwordx4 v[68:71], %[vo], %[rw], s46 offen offset:1024 sc1\n" \
    "ds_read_b128 v[80:83], %[wl] offset:12288\n" \
    "ds_read_b128 v[84:87], %[wl] offset:13312\n" \
    "s_waitcnt vmcnt(9)\n" \
    "v_smfmac_f32_16x16x64_bf16 v[0:3], v[32:35], a[32:39], %[idx]\n" \
    "v_max3_u32 v104, v104, v32, v33\n" \
    "v_smfmac_f32_16x16x64_bf16 v[4:7], v[32:35], a[160:167], %[idx]\n" \
    "v_max3_u32 v104, v104, v34, v35\n" \
    "v_smfmac_f32_16x16x64_bf16 v[8:11], v[32:35], v[160:167], %[idx]\n" \
    "s_waitcnt lgkmcnt(4)\n" \
    "v_smfmac_f32_16x16x64_bf16 v[12:15], v[32:35], v[88:95], %[idx]\n" \
    "buffer_load_dwordx4 v[72:75], %[vo], %[rw], s46 offen offset:2048 sc1\n" \
    "ds_read_b128 v[88:91], %[wl] offset:14336\n" \
    "ds_read_b128 v[92:95], %[wl] offset:15360\n" \
    "s_waitcnt vmcnt(9)\n" \
    "v_smfmac_f32_16x16x64_bf16 v[0:3], v[36:39], a[40:47], %[idx]\n" \
    "v_max3_u32 v104, v104, v36, v37\n" \
    "v_smfmac_f32_16x16x64_bf16 v[4:7], v[36:39], a[168:175], %[idx]\n" \
    "v_max3_u32 v104, v104, v38, v39\n" \
    "v_smfmac_f32_16x16x64_bf16 v[8:11], v[36:39], v[168:175], %[idx]\n" \
    "s_waitcnt lgkmcnt(4)\n" \
    "v_smfmac_f32_16x16x64_bf16 v[12:15], v[36:39], v[96:103], %[idx]\n" \
    "buffer_load_dwordx4 v[76:79], %[vo], %[rw], s46 offen offset:3072 sc1\n" \
    "ds_read_b128 v[96:99], %[wl] offset:16384\n" \
    "ds_read_b128 v[100:103], %[wl] offset:17408\n" \
    "s_waitcnt vmcnt(9)\n" \
    "v_smfmac_f32_16x16x64_bf16 v[0:3], v[40:43], a[48:55], %[idx]\n" \
    "v_max3_u32 v104, v104, v40, v41\n" \
    "v_smfmac_f32_16x16x64_bf16 v[4:7], v[40:43], a[176:183], %[idx]\n" \
    "v_max3_u32 v104, v104, v42, v43\n" \
    "v_smfmac_f32_16x16x64_bf16 v[8:11], v[40:43], v[176:183], %[idx]\n" \
    "s_waitcnt lgkmcnt(4)\n" \
    "v_smfmac_f32_16x16x64_bf16 v[12:15], v[40:43], v[80:87], %[idx]\n" \
    "buffer_load_dwordx2 v[106:107], %[bv], %[rw], %[bs] offen sc1\n" \
    "ds_read_b128 v[80:83], %[wl] offset:18432\n" \
    "ds_read_b128 v[84:87], %[wl] offset:19456\n" \
    "s_waitcnt vmcnt(9)\n" \
    "v_smfmac_f32_16x16x64_bf16 v[0:3], v[44:47], a[56:63], %[idx]\n" \
    "v_max3_u32 v104, v104, v44, v45\n" \
    "v_smfmac_f32_16x16x64_bf16 v[4:7], v[44:47], a[184:191], %[idx]\n" \
    "v_max3_u32 v104, v104, v46, v47\n" \
    "v_smfmac_f32_16x16x64_bf16 v[8:11], v[44:47], v[184:191], %[idx]\n" \
    "s_waitcnt lgkmcnt(4)\n" \
    "v_smfmac_f32_16x16x64_bf16 v[12:15], v[44:47], v[88:95], %[idx]\n" \
    "buffer_load_dword v108, %[gv], %[rw], %[gs] offen sc1\n" \
    "ds_read_b128 v[88:91], %[wl] offset:20480\n" \
    "ds_read_b128 v[92:95], %[wl] offset:21504\n" \
    "s_waitcnt vmcnt(9)\n" \
    "v_smfmac_f32_16x16x64_bf16 v[0:3], v[48:51], a[64:71], %[idx]\n" \
    "v_max3_u32 v104, v104, v48, v49\n" \
    "v_smfmac_f32_16x16x64_bf16 v[4:7], v[48:51], a[192:199], %[idx]\n" \
    "v_max3_u32 v104, v104, v50, v51\n" \
    "v_smfmac_f32_16x16x64_bf16 v[8:11], v[48:51], v[192:199], %[idx]\n" \
    "s_waitcnt lgkmcnt(4)\n" \
    "v_smfmac_f32_16x16x64_bf16 v[12:15], v[48:51], v[96:103], %[idx]\n" \
    "buffer_load_dword v105, %[sv], %[rw], %[ss] offen sc1\n" \
    "ds_read_b128 v[96:99], %[wl] offset:22528\n" \
    "ds_read_b128 v[100:103], %[wl] offset:23552\n" \
    "s_waitcnt vmcnt(9)\n" \
    "v_smfmac_f32_16x16x64_bf16 v[0:3], v[52:55], a[72:79], %[idx]\n" \
    "v_max3_u32 v104, v104, v52, v53\n" \
    "v_smfmac_f32_16x16x64_bf16 v[4:7], v[52:55], a[200:207], %[idx]\n" \
    "v_max3_u32 v104, v104, v54, v55\n" \
    "v_smfmac_f32_16x16x64_bf16 v[8:11], v[52:55], v[200:207], %[idx]\n" \
    "s_waitcnt lgkmcnt(4)\n" \
    "v_smfmac_f32_16x16x64_bf16 v[12:15], v[52:55], v[80:87], %[idx]\n" \
    "ds_read_b128 v[80:83], %[wl] offset:24576\n" \
    "ds_read_b128 v[84:87], %[wl] offset:25600\n" \
    "s_waitcnt vmcnt(8)\n" \
    "v_smfmac_f32_16x16x64_bf16 v[0:3], v[56:59], a[80:87], %[idx]\n" \
    "v_max3_u32 v104, v104, v56, v57\n" \
    "v_smfmac_f32_16x16x64_bf16 v[4:7], v[56:59], a[208:215], %[idx]\n" \
    "v_max3_u32 v104, v104, v58, v59\n" \
    "v_smfmac_f32_16x16x64_bf16 v[8:11], v[56:59], v[208:215], %[idx]\n" \
    "s_waitcnt lgkmcnt(4)\n" \
    "v_smfmac_f32_16x16x64_bf16 v[12:15], v[56:59], v[88:95], %[idx]\n" \
    "ds_read_b128 v[88:91], %[wl] offset:26624\n" \
    "ds_read_b128 v[92:95], %[wl] offset:27648\n" \
    "s_waitcnt vmcnt(7)\n" \
    "v_smfmac_f32_16x16x64_bf16 v[0:3], v[60:63], a[88:95], %[idx]\n" \
    "v_max3_u32 v104, v104, v60, v61\n" \
    "v_smfmac_f32_16x16x64_bf16 v[4:7], v[60:63], a[216:223], %[idx]\n" \
    "v_max3_u32 v104, v104, v62, v63\n" \
    "v_smfmac_f32_16x16x64_bf16 v[8:11], v[60:63], v[216:223], %[idx]\n" \
    "s_waitcnt lgkmcnt(4)\n" \
    "v_smfmac_f32_16x16x64_bf16 v[12:15], v[60:63], v[96:103], %[idx]\n" \
    "ds_read_b128 v[96:99], %[wl] offset:28672\n" \
    "ds_read_b128 v[100:103], %[wl] offset:29696\n" \
    "s_waitcnt vmcnt(6)\n" \
    "v_smfmac_f32_16x16x64_bf16 v[0:3], v[64:67], a[96:103], %[idx]\n" \
    "v_max3_u32 v104, v104, v64, v65\n" \
    "v_smfmac_f32_16x16x64_bf16 v[4:7], v[64:67], a[224:231], %[idx]\n" \
    "v_max3_u32 v104, v104, v66, v67\n" \
    "v_smfmac_f32_16x16x64_bf16 v[8:11], v[64:67], v[224:231], %[idx]\n" \
    "s_waitcnt lgkmcnt(4)\n" \
    "v_smfmac_f32_16x16x64_bf16 v[12:15], v[64:67], v[80:87], %[idx]\n" \
    "ds_read_b128 v[80:83], %[wl] offset:30720\n" \
    "ds_read_b128 v[84:87], %[wl] offset:31744\n" \
    "s_waitcnt vmcnt(5)\n" \
    "v_smfmac_f32_16x16x64_bf16 v[0:3], v[68:71], a[104:111], %[idx]\n" \
    "v_max3_u32 v104, v104, v68, v69\n" \
    "v_smfmac_f32_16x16x64_bf16 v[4:7], v[68:71], a[232:239], %[idx]\n" \
    "v_max3_u32 v104, v104, v70, v71\n" \
    "v_smfmac_f32_16x16x64_bf16 v[8:11], v[68:71], v[232:239], %[idx]\n" \
    "s_waitcnt lgkmcnt(4)\n" \
    "v_smfmac_f32_16x16x64_bf16 v[12:15], v[68:71], v[88:95], %[idx]\n" \
    "s_waitcnt vmcnt(4)\n" \
    "v_smfmac_f32_16x16x64_bf16 v[0:3], v[72:75], a[112:119], %[idx]\n" \
    "v_max3_u32 v104, v104, v72, v73\n" \
    "v_smfmac_f32_16x16x64_bf16 v[4:7], v[72:75], a[240:247], %[idx]\n" \
    "v_max3_u32 v104, v104, v74, v75\n" \
    "v_smfmac_f32_16x16x64_bf16 v[8:11], v[72:75], v[240:247], %[idx]\n" \
    "s_waitcnt lgkmcnt(2)\n" \
    "v_smfmac_f32_16x16x64_bf16 v[12:15], v[72:75], v[96:103], %[idx]\n" \
    "s_waitcnt vmcnt(3)\n" \
    "v_smfmac_f32_16x16x64_bf16 v[0:3], v[76:79], a[120:127], %[idx]\n" \
    "v_max3_u32 v104, v104, v76, v77\n" \
    "v_smfmac_f32_16x16x64_bf16 v[4:7], v[76:79], a[248:255], %[idx]\n" \
    "v_max3_u32 v104, v104, v78, v79\n" \
    "v_smfmac_f32_16x16x64_bf16 v[8:11], v[76:79], v[248:255], %[idx]\n" \
    "s_waitcnt lgkmcnt(0)\n" \
    "v_smfmac_f32_16x16x64_bf16 v[12:15], v[76:79], v[80:87], %[idx]\n" \
    "s_waitcnt vmcnt(0)\n" \
    "s_nop 5\n" \
    "v_permlane32_swap_b32 v0, v8\n" \
    "v_add_f32 v0, v0, v8\n" \
    "v_permlane32_swap_b32 v1, v9\n" \
    "v_add_f32 v1, v1, v9\n" \
    "v_permlane32_swap_b32 v2, v10\n" \
    "v_add_f32 v2, v2, v10\n" \
    "v_permlane32_swap_b32 v3, v11\n" \
    "v_add_f32 v3, v3, v11\n" \
    "v_permlane32_swap_b32 v4, v12\n" \
    "v_add_f32 v4, v4, v12\n" \
    "v_permlane32_swap_b32 v5, v13\n" \
    "v_add_f32 v5, v5, v13\n" \
    "v_permlane32_swap_b32 v6, v14\n" \
    "v_add_f32 v6, v6, v14\n" \
    "v_permlane32_swap_b32 v7, v15\n" \
    "v_add_f32 v7, v7, v15\n" \
    ""
#define OUT_CLOBBER "v16", "v17", "v18", "v19", "v20", "v21", "v22", "v23", "v24", "v25", "v26", "v27", "v28", "v29", "v30", "v31", "v32", "v33", "v34", "v35", "v36", "v37", "v38", "v39", "v40", "v41", "v42", "v43", "v44", "v45", "v46", "v47", "v48", "v49", "v50", "v51", "v52", "v53", "v54", "v55", "v56", "v57", "v58", "v59", "v60", "v61", "v62", "v63", "v64", "v65", "v66", "v67", "v68", "v69", "v70", "v71", "v72", "v73", "v74", "v75", "v76", "v77", "v78", "v79", "v80", "v81", "v82", "v83", "v84", "v85", "v86", "v87", "v88", "v89", "v90", "v91", "v92", "v93", "v94", "v95", "v96", "v97", "v98", "v99", "v100", "v101", "v102", "v103", "s44", "s45", "s46", "scc", "memory"
constexpr int NREP = 2, NCP = 4;
#define OWNER_ASM \
    "s_getpc_b64 s[96:97]\n" \
    "14:\n" \
    "s_add_u32 s96, s96, 2f-14b\n" \
    "s_addc_u32 s97, s97, 0\n" \
    "s_sub_u32 s68, s96, 2f-11f\n" \
    "s_subb_u32 s69, s97, 0\n" \
    "s_mov_b32 %[np], 0\n" \
    "13:\n" \
    "s_memtime vcc\n" \
    "s_waitcnt lgkmcnt(0)\n" \
    "s_sub_i32 s70, %[lk], vcc_lo\n" \
    "s_cmp_gt_i32 s70, %[rd]\n" \
    "s_cbranch_scc1 13b\n" \
    "buffer_store_dwordx4 %[sv], %[ro], %[rs], %[pq] offen offset:0 sc1\n" \
    "buffer_store_dwordx4 %[sv], %[ro], %[rs], %[pq] offen offset:1024 sc1\n" \
    "buffer_store_dwordx4 %[sv], %[ro], %[rs], %[pq] offen offset:2048 sc1\n" \
    "buffer_store_dwordx4 %[sv], %[ro], %[rs], %[pq] offen offset:3072 sc1\n" \
    "v_mov_b32 v2, -1\n" \
    "buffer_store_dword v2, %[rm], %[rs], %[mq] offen sc1\n" \
    "buffer_store_dwordx4 %[sv], %[rt], %[rw], %[tq] offen sc1\n" \
    "s_mov_b32 s71, 4\n" \
    "10:\n" \
    "s_memtime vcc\n" \
    "s_waitcnt lgkmcnt(0)\n" \
    "s_sub_i32 s70, %[lk], vcc_lo\n" \
    "s_cmp_gt_i32 s70, 112\n" \
    "s_cbranch_scc1 10b\n" \
    "s_max_i32 s70, s70, 0\n" \
    "s_memtime s[58:59]\n" \
    "s_memrealtime s[44:45]\n" \
    "s_nop 1\n" \
    "s_memrealtime s[46:47]\n" \
    "s_nop 1\n" \
    "s_memrealtime s[48:49]\n" \
    "s_nop 1\n" \
    "s_memrealtime s[50:51]\n" \
    "s_nop 1\n" \
    "s_memrealtime s[52:53]\n" \
    "s_nop 1\n" \
    "s_memrealtime s[54:55]\n" \
    "s_nop 1\n" \
    "s_memrealtime s[56:57]\n" \
    "s_nop 1\n" \
    "s_memrealtime vcc\n" \
    "s_waitcnt lgkmcnt(0)\n" \
    "s_add_i32 s44, s44, s46\n" \
    "s_add_i32 s48, s48, s50\n" \
    "s_add_i32 s52, s52, s54\n" \
    "s_add_i32 s44, s44, s48\n" \
    "s_add_i32 s52, s52, s56\n" \
    "s_add_i32 s44, s44, s52\n" \
    "s_add_i32 s59, s44, vcc_lo\n" \
    "s_sub_i32 s44, s59, %[oT]\n" \
    "s_mov_b32 %[oT], s59\n" \
    "s_sub_i32 s45, s58, %[oC]\n" \
    "s_mov_b32 %[oC], s58\n" \
    "s_mul_i32 s46, s44, %[oR]\n" \
    "s_lshr_b32 s46, s46, 12\n" \
    "s_sub_i32 s46, s45, s46\n" \
    "s_sub_i32 s44, s44, 1000\n" \
    "s_cmp_lt_u32 s44, 480\n" \
    "s_cselect_b32 s46, s46, 0\n" \
    "s_add_i32 %[oR], %[oR], s46\n" \
    "s_sub_i32 s45, %[oT], %[c8]\n" \
    "s_and_b32 s70, s70, -4\n" \
    "s_sub_u32 vcc_lo, s68, s70\n" \
    "s_subb_u32 vcc_hi, s69, 0\n" \
    "s_setpc_b64 vcc\n" \
    "s_nop 0\n" \
    "s_nop 0\n" \
    "s_nop 0\n" \
    "s_nop 0\n" \
    "s_nop 0\n" \
    "s_nop 0\n" \
    "s_nop 0\n" \
    "s_nop 0\n" \
    "s_nop 0\n" \
    "s_nop 0\n" \
    "s_nop 0\n" \
    "s_nop 0\n" \
    "s_nop 0\n" \
    "s_nop 0\n" \
    "s_nop 0\n" \
    "s_nop 0\n" \
    "s_nop 0\n" \
    "s_nop 0\n" \
    "s_nop 0\n" \
    "s_nop 0\n" \
    "s_nop 0\n" \
    "s_nop 0\n" \
    "s_nop 0\n" \
    "s_nop 0\n" \
    "s_nop 0\n" \
    "s_nop 0\n" \
    "s_nop 0\n" \
    "s_nop 0\n" \
    "11:\n" \
    "buffer_load_dwordx2 v[0:1], %[ca], %[rs], %[cs] offen sc1\n" \
    "s_waitcnt vmcnt(0)\n" \
    "6:\n" \
    "s_memtime s[46:47]\n" \
    "v_subrev_u32 v0, s45, v0\n" \
    "v_cmp_ne_u16 vcc, %[tag], v1\n" \
    "s_add_i32 s48, %[oC], -136\n" \
    "v_max_i32_dpp v0, v0, v0 row_shr:1 row_mask:0xf bank_mask:0xf\n" \
    "v_max_u32_dpp v1, v1, v1 row_shr:1 row_mask:0xf bank_mask:0xf\n" \
    "s_cbranch_vccnz 7f\n" \
    "8:\n" \
    "v_max_i32_dpp v0, v0, v0 row_shr:2 row_mask:0xf bank_mask:0xf\n" \
    "v_max_u32_dpp v1, v1, v1 row_shr:2 row_mask:0xf bank_mask:0xf\n" \
    "s_nop 0\n" \
    "v_max_i32_dpp v0, v0, v0 row_shr:4 row_mask:0xf bank_mask:0xf\n" \
    "v_max_u32_dpp v1, v1, v1 row_shr:4 row_mask:0xf bank_mask:0xf\n" \
    "s_nop 0\n" \
    "v_max_i32_dpp v0, v0, v0 row_shr:8 row_mask:0xf bank_mask:0xf\n" \
    "v_max_u32_dpp v1, v1, v1 row_shr:8 row_mask:0xf bank_mask:0xf\n" \
    "s_waitcnt lgkmcnt(0)\n" \
    "v_max_i32_dpp v0, v0, v0 row_bcast:15 row_mask:0xa bank_mask:0xf\n" \
    "v_max_u32_dpp v1, v1, v1 row_bcast:15 row_mask:0xa bank_mask:0xf\n" \
    "s_add_i32 s46, s46, 44\n" \
    "v_max_i32_dpp v0, v0, v0 row_bcast:31 row_mask:0xc bank_mask:0xf\n" \
    "v_max_u32_dpp v1, v1, v1 row_bcast:31 row_mask:0xc bank_mask:0xf\n" \
    "v_readlane_b32 s44, v0, 63\n" \
    "v_readlane_b32 s49, v1, 63\n" \
    "s_mul_i32 s47, s44, %[oR]\n" \
    "s_ashr_i32 s47, s47, 12\n" \
    "s_add_i32 s47, s47, s48\n" \
    "s_sub_i32 s58, s47, s46\n" \
    "s_cmp_gt_i32 s58, 112\n" \
    "s_cbranch_scc1 1f\n" \
    "12:\n" \
    "s_max_i32 s58, s58, 0\n" \
    "s_and_b32 s58, s58, -4\n" \
    "s_sub_u32 vcc_lo, s96, s58\n" \
    "s_subb_u32 vcc_hi, s97, 0\n" \
    "s_setpc_b64 vcc\n" \
    "s_nop 0\n" \
    "s_nop 0\n" \
    "s_nop 0\n" \
    "s_nop 0\n" \
    "s_nop 0\n" \
    "s_nop 0\n" \
    "s_nop 0\n" \
    "s_nop 0\n" \
    "s_nop 0\n" \
    "s_nop 0\n" \
    "s_nop 0\n" \
    "s_nop 0\n" \
    "s_nop 0\n" \
    "s_nop 0\n" \
    "s_nop 0\n" \
    "s_nop 0\n" \
    "s_nop 0\n" \
    "s_nop 0\n" \
    "s_nop 0\n" \
    "s_nop 0\n" \
    "s_nop 0\n" \
    "s_nop 0\n" \
    "s_nop 0\n" \
    "s_nop 0\n" \
    "s_nop 0\n" \
    "s_nop 0\n" \
    "s_nop 0\n" \
    "s_nop 0\n" \
    "2:\n" \
    "buffer_load_dwordx4 v[0:3], %[o], %[rs], %[pb] offen offset:0 sc1\n" \
    "buffer_load_dwordx4 v[4:7], %[o], %[rs], %[pb] offen offset:1024 sc1\n" \
    "buffer_load_dwordx4 v[8:11], %[o], %[rs], %[pb] offen offset:2048 sc1\n" \
    "buffer_load_dwordx4 v[12:15], %[o], %[rs], %[pb] offen offset:3072 sc1\n" \
    "s_add_i32 %[np], %[np], 1\n" \
    "s_add_i32 %[tm], s44, s45\n" \
    "s_lshr_b32 s50, s49, 16\n" \
    "s_add_i32 s50, s50, s44\n" \
    "s_add_i32 s50, s50, %[cw]\n" \
    "s_mul_i32 s50, s50, %[oR]\n" \
    "s_ashr_i32 s50, s50, 12\n" \
    "s_add_i32 %[lk], s50, %[oC]\n" \
    "s_waitcnt vmcnt(3)\n" \
    "v_mfma_f32_16x16x32_bf16 v[16:19], %[sel], v[0:3], 0\n" \
    "v_max3_u32 v20, v0, v1, v1\n" \
    "v_max3_u32 v20, v2, v3, v20\n" \
    "s_waitcnt vmcnt(2)\n" \
    "v_mfma_f32_16x16x32_bf16 v[16:19], %[sel], v[4:7], v[16:19]\n" \
    "v_max3_u32 v20, v4, v5, v20\n" \
    "v_max3_u32 v20, v6, v7, v20\n" \
    "s_waitcnt vmcnt(1)\n" \
    "v_mfma_f32_16x16x32_bf16 v[16:19], %[sel], v[8:11], v[16:19]\n" \
    "v_max3_u32 v20, v8, v9, v20\n" \
    "v_max3_u32 v20, v10, v11, v20\n" \
    "s_waitcnt vmcnt(0)\n" \
    "v_mfma_f32_16x16x32_bf16 v[16:19], %[sel], v[12:15], v[16:19]\n" \
    "v_max3_u32 v20, v12, v13, v20\n" \
    "v_max3_u32 v20, v14, v15, v20\n" \
    "v_cmp_eq_u32 vcc, -1, v20\n" \
    "s_nop 3\n" \
    "s_cbranch_vccnz 4f\n" \
    "3:\n" \
    "v_add_f32_dpp v16, v16, v16 row_ror:8 row_mask:0xf bank_mask:0xf\n" \
    "v_add_f32_dpp v17, v17, v17 row_ror:8 row_mask:0xf bank_mask:0xf\n" \
    "v_cvt_pk_bf16_f32 v16, v16, v17\n" \
    "buffer_store_dword v16, %[so], %[rs], %[mb0] offen sc1\n" \
    "s_branch 5f\n" \
    "1:\n" \
    "s_memtime vcc\n" \
    "s_waitcnt lgkmcnt(0)\n" \
    "s_sub_i32 s58, s47, vcc_lo\n" \
    "s_cmp_gt_i32 s58, 112\n" \
    "s_cbranch_scc1 1b\n" \
    "s_branch 12b\n" \
    "4:\n" \
    "s_add_i32 %[bud], %[bud], -1\n" \
    "s_cmp_lt_i32 %[bud], 0\n" \
    "s_cbranch_scc0 2b\n" \
    "s_branch 3b\n" \
    "7:\n" \
    "s_bcnt1_i32_b64 s44, vcc\n" \
    "s_cmp_le_u32 s44, s71\n" \
    "s_mov_b32 s71, 0\n" \
    "s_cbranch_scc1 15f\n" \
    "s_cmp_eq_u32 %[tag], 1\n" \
    "s_cbranch_scc1 9f\n" \
    "s_sleep 15\n" \
    "9:\n" \
    "s_sleep 6\n" \
    "15:\n" \
    "s_add_i32 %[bud], %[bud], -1\n" \
    "s_cmp_lt_i32 %[bud], 0\n" \
    "s_cbranch_scc1 8b\n" \
    "buffer_load_dwordx2 v[0:1], %[ca], %[rs], %[cs] offen sc1\n" \
    "s_memtime s[58:59]\n" \
    "s_memrealtime s[44:45]\n" \
    "s_nop 1\n" \
    "s_memrealtime s[46:47]\n" \
    "s_nop 1\n" \
    "s_memrealtime s[48:49]\n" \
    "s_nop 1\n" \
    "s_memrealtime s[50:51]\n" \
    "s_nop 1\n" \
    "s_memrealtime s[52:53]\n" \
    "s_nop 1\n" \
    "s_memrealtime s[54:55]\n" \
    "s_nop 1\n" \
    "s_memrealtime s[56:57]\n" \
    "s_nop 1\n" \
    "s_memrealtime vcc\n" \
    "s_waitcnt lgkmcnt(0)\n" \
    "s_add_i32 s44, s44, s46\n" \
    "s_add_i32 s48, s48, s50\n" \
    "s_add_i32 s52, s52, s54\n" \
    "s_add_i32 s44, s44, s48\n" \
    "s_add_i32 s52, s52, s56\n" \
    "s_add_i32 s44, s44, s52\n" \
    "s_add_i32 s59, s44, vcc_lo\n" \
    "s_sub_i32 s44, s59, %[oT]\n" \
    "s_mov_b32 %[oT], s59\n" \
    "s_sub_i32 s45, s58, %[oC]\n" \
    "s_mov_b32 %[oC], s58\n" \
    "s_mul_i32 s46, s44, %[oR]\n" \
    "s_lshr_b32 s46, s46, 12\n" \
    "s_sub_i32 s46, s45, s46\n" \
    "s_sub_i32 s44, s44, 1000\n" \
    "s_cmp_lt_u32 s44, 480\n" \
    "s_cselect_b32 s46, s46, 0\n" \
    "s_add_i32 %[oR], %[oR], s46\n" \
    "s_sub_i32 s45, %[oT], %[c8]\n" \
    "s_waitcnt vmcnt(0)\n" \
    "s_branch 6b\n" \
    "5:\n" \
    ""
#define OWNER_CLOBBER "v0", "v1", "v2", "v3", "v4", "v5", "v6", "v7", "v8", "v9", "v10", "v11", "v12", "v13", "v14", "v15", "v16", "v17", "v18", "v19", "v20", "s44", "s45", "s46", "s47", "s48", "s49", "s50", "s51", "s52", "s53", "s54", "s55", "s56", "s57", "s58", "s59", "s68", "s69", "s70", "s71", "s96", "s97", "vcc", "scc", "memory"
constexpr int O_W = 0;
constexpr int O_STG = 131072;
constexpr int O_B = O_STG + 20480;
constexpr int OUT_REP = 7;
#define OUT_AUX 16
DI int opaque_lane() { int l; asm volatile("v_mbcnt_lo_u32_b32 %0, -1, 0\n\tv_mbcnt_hi_u32_b32 %0, -1, %0" : "=v"(l)); return l; }
template <int O, class V> DI void put4(V& d, u32x4 f) {
#pragma unroll
    for (int j = 0; j < 4; ++j) d[O + j] = f[j];
}
DI void role_outp(const Args& a, LAS char* lds, unsigned lds_base, int nb, int rep, rsrc_t rw, int& budget) {
    const int tid = threadIdx.x, lane = tid & 63, w = __builtin_amdgcn_readfirstlane(tid >> 6);
    WG_BARRIER();
    const int n = lane & 15, g = lane >> 4;
    const int wl = O_W + w * 32768 + lane * 16;
    u32x32 A[8], V[4];
    {
        const int stg = O_STG + w * 5120;
        const rsrc_t ro = __builtin_amdgcn_make_buffer_rsrc((void*)a.Wo, 0, HD * DM * 2, 0x00020000);
        const int so = (1024 * w + (lane >> 3)) * (DM * 2) + nb * 128 + (lane & 7) * 16, sw = stg + (lane >> 3) * 160 + (lane & 7) * 16;
        const int ra = stg + (16 * (g & 1) + 4 * (g >> 1) + (n >> 2)) * 160 + (n & 3) * 8;
        constexpr int PB = 4;
        u32x4 b[PB][4];
        auto fetch = [&](int ks) {
#pragma unroll
            for (int i = 0; i < 4; ++i) b[ks % PB][i] = __builtin_amdgcn_raw_buffer_load_b128(ro, so, (64 * (ks >> 1) + 16 * (ks & 1) + 8 * (i & 1) + 32 * (i >> 1)) * (DM * 2), 0);
        };
        unsigned tp = now();
        const int pc = a.d1 >> 16;
#pragma unroll
        for (int ks = 0; ks < PB; ++ks) { fetch(ks); FENCE(); tp += pc; while ((int)(now() - tp) < 0) {} }
        FENCE();
        static_for<32>([&](auto ic) {
            constexpr int ks = decltype(ic)::value;
#pragma unroll
            for (int i = 0; i < 4; ++i) *(LAS u32x4*)(lds + sw + i * 1280) = b[ks % PB][i];
            if (ks + PB < 32) { fetch(ks + PB); FENCE(); tp += pc; }
            u32x4 f[4];
#pragma unroll
            for (int t = 0; t < 4; ++t) { const u32x2 lo = tr_read(lds, ra + t * 32), hi = tr_read(lds, ra + t * 32 + 1280); f[t] = (u32x4){lo.x, lo.y, hi.x, hi.y}; }
            put4<4 * (ks & 7)>(A[ks >> 3], f[0]); put4<4 * (ks & 7)>(A[4 + (ks >> 3)], f[1]); put4<4 * (ks & 7)>(V[ks >> 3], f[2]);
            *(LAS u32x4*)(lds + wl + ks * 1024) = f[3];
            if constexpr (ks == 7) asm volatile("" : "+{a[0:31]}"(A[0]), "+{a[128:159]}"(A[4]), "+{v[128:159]}"(V[0]));
            if constexpr (ks == 15) asm volatile("" : "+{a[32:63]}"(A[1]), "+{a[160:191]}"(A[5]), "+{v[160:191]}"(V[1]));
            if constexpr (ks == 23) asm volatile("" : "+{a[64:95]}"(A[2]), "+{a[192:223]}"(A[6]), "+{v[192:223]}"(V[2]));
            if constexpr (ks == 31) asm volatile("" : "+{a[96:127]}"(A[3]), "+{a[224:255]}"(A[7]), "+{v[224:255]}"(V[3]));
            FENCE();
            if (ks + PB < 31) while ((int)(now() - tp) < 0) {}
        });
    }
    wait_prog(rw, F_S4 + lane * 4, true, 1, budget);
    const rsrc_t ru = __builtin_amdgcn_make_buffer_rsrc(a.out, 0, SEQ * DM * 2, 0x00020000);
    constexpr int SP = 2 * LATE - 2;
    auto first = [&](int u) { return u >= SP ? 8 * u + 8 * SP : 16 * u; };
    auto st_s = [&](int u) { return (u >= 2 * FAST ? F_H + ((first(u) >> 4) - 2 * FAST) * 4096 : F_O + (u & 3) * 4) + w * 1024; };
    auto bn_s = [&](int u) { return OFF_BO + (64 * nb + 16 * w) * BO_ROW + first(u) * 2; };
    auto gn_s = [&](int u) { return OFF_GT + (first(u) + 4 * w) * 4; };
    int st = -1;
    u32x2 bn = ld64(rw, (lane >> 2) * BO_ROW + (lane & 3) * 8, bn_s(rep));
    float gn = __uint_as_float(ld32(rw, g * 4, gn_s(rep)));
    __builtin_amdgcn_s_waitcnt(0);
#define OUT_OPS "=&{v[0:15]}"(acc), "=&{v104}"(top), "=&{v105}"(st), "=&{v[106:107]}"(bn), "=&{v108}"(gn) \
        : "{a[0:31]}"(A[0]), "{a[32:63]}"(A[1]), "{a[64:95]}"(A[2]), "{a[96:127]}"(A[3]), "{a[128:159]}"(A[4]), "{a[160:191]}"(A[5]), "{a[192:223]}"(A[6]), "{a[224:255]}"(A[7]), \
          "{v[128:159]}"(V[0]), "{v[160:191]}"(V[1]), "{v[192:223]}"(V[2]), "{v[224:255]}"(V[3]), [rw] "s"(rw), [so] "s"(t0 * 8192), [wl] "v"(lds_base + O_W + w * 32768 + lane * 16), \
          [tv] "v"(tv), [tm] "s"(tm), [vo] "v"(vo), [sv] "v"(sv), [bv] "v"((lane >> 2) * BO_ROW + (lane & 3) * 8), [gv] "v"(g * 4), \
          [ss] "s"(st_s(u + OUT_REP)), [bs] "s"(bn_s(u + OUT_REP)), [gs] "s"(gn_s(u + OUT_REP))
    for (int u = rep; u < SP + 4; u += OUT_REP) {
        const bool fast = u >= 2 * FAST, sp = u >= SP;
        const int t0 = first(u);
        WG_BARRIER();
        int lane = opaque_lane(), n = lane & 15, g = lane >> 4;
        if (u == 3 * OUT_REP && nb == 0) __builtin_amdgcn_raw_buffer_store_b64(launch_tag(a), rw, w + lane == 0 ? F_EPOCH : OOB, 0, 16);
        S4S(0)
        TT(u >= 119, 0)
        *(LAS u32x2*)(lds + O_B + (16 * w + (lane >> 2)) * 40 + (lane & 3) * 8) = bn;
        const float gt = gn;
        const int tv = OFF_OF + w * 16384 + nb * 2048 + g * 65536 + n * 128, tm = __builtin_amdgcn_readfirstlane(nb < 8 ? sp ? 0xFFFF : -1 : 0);
        const int vo = OFF_OF + w * 16384 + (n & 7) * 16 + (sp ? (n >> 3) * 512 + g * 128 : (n >> 3) * 65536 + (g & 1) * 512 + (g >> 1) * 128);
        const int thr = fast ? 0 : u >> 1, sv = (lane & 7) * 128;
        if (!__all(st >= thr)) do { __builtin_amdgcn_s_sleep(1); st = (int)ld32(rw, sv, st_s(u)); asm volatile("" ::: "memory"); } while (!__all(st >= thr) && --budget >= 0);
        if (fast) { const unsigned t = now(); (void)wait_until(t + wave_max32(min((int)(2 * (st - t)) >> 1, 500))); }
        S4S(1)
        TT(u >= 119, 1)
        f32x16 acc; unsigned top;
        if (sp) {
            do asm volatile(OUT8_ASM : OUT_OPS, [idx] "v"(n & 8 ? 0xEEEE : 0x4444) : OUT_CLOBBER); while (__any(top == SENT) && --budget >= 0);
            TT(u >= 119, 2)
            lane = opaque_lane(); n = lane & 15; g = lane >> 4;
#pragma unroll
            for (int i = 0; i < 8; ++i) *(LAS float*)(lds + O_STG + (w * 16 + 4 * (g & 1) + (i & 3)) * 272 + (g >> 1) * 128 + (i >> 2) * 64 + n * 4) = acc[i];
        } else {
            do asm volatile(OUT16_ASM : OUT_OPS : OUT_CLOBBER); while (__any(top == SENT) && --budget >= 0);
            S4S(2)
            TT(u >= 119, 2)
            lane = opaque_lane(); n = lane & 15; g = lane >> 4;
#pragma unroll
            for (int t = 0; t < 4; ++t) *(LAS f32x4*)(lds + O_STG + (w * 16 + n) * 272 + t * 64 + g * 16) = (f32x4){acc[4 * t], acc[4 * t + 1], acc[4 * t + 2], acc[4 * t + 3]};
        }
        WG_BARRIER();
        S4S(3)
        const int tok = 4 * w + g, c4 = 4 * n;
        f32x4 r[4]; u16 bv[4];
#pragma unroll
        for (int ww = 0; ww < 4; ++ww) { r[ww] = *(LAS f32x4*)(lds + O_STG + (ww * 16 + tok) * 272 + c4 * 4); bv[ww] = *(LAS u16*)(lds + O_B + (c4 + ww) * 40 + tok * 2); }
        FENCE();
        const f32x4 v = gt * ((r[0] + r[1]) + (r[2] + r[3])) + (1.0f - gt) * (f32x4){bf2f(bv[0]), bf2f(bv[1]), bf2f(bv[2]), bf2f(bv[3])};
        __builtin_amdgcn_raw_buffer_store_b64((u32x2){pack2(v[0], v[1]), pack2(v[2], v[3])}, ru, sp && tok >= 8 ? OOB : (tok * DM + 64 * nb + c4) * 2, t0 * (DM * 2), OUT_AUX);
        S4S(4)
        TT(u >= 119, 3)
        TT(u >= 105 && u < 112, 4)
    }
}

constexpr int L_MEMS = 0;
constexpr int L_Q = 9216;
constexpr int L_KO = L_Q + 2560;
constexpr int L_KX = 16384;
constexpr int L_W = 32768;
constexpr int C_RING = 8;
static_assert(S_TOTAL <= L_SINK && L_W + C_RING * 16384 <= L_TOTAL && L_KO + 8 * 576 <= L_KX, "LDS budget");
static_assert(P_STG + P_RING * 16384 <= L_TOTAL, "LDS budget");

DI int pos2col(int p) { return p < 512 ? (p & ~63) + 16 * (p & 3) + ((p & 63) >> 2) : p; }
DI void unit_rc(int u, int& i, int& p0) {
    if (u < 512) { i = 2 * ((u >> 2) & 3) + (u & 1); p0 = (u & ~15) + 4 * (u & 2); }
    else { const int v = u - 512; i = (v >> 1) & 7; p0 = 512 + 16 * (v >> 4) + 8 * (v & 1); }
}
DI void mem0_arrange(LAS char* lds, int raw, int tid) {
#pragma unroll
    for (int k = 0; k < 2; ++k) {
        int i, p0; unit_rc(tid + 256 * k, i, p0);
        unsigned r[4];
#pragma unroll
        for (int j = 0; j < 4; ++j) r[j] = *(LAS unsigned*)(lds + raw + (i * DM + pos2col(p0)) * 2 + 32 * j);
        *(LAS u32x4*)(lds + L_MEMS + (tid + 256 * k) * 16) = (u32x4){__builtin_amdgcn_perm(r[1], r[0], 0x05040100), __builtin_amdgcn_perm(r[3], r[2], 0x05040100),
                                                                     __builtin_amdgcn_perm(r[1], r[0], 0x07060302), __builtin_amdgcn_perm(r[3], r[2], 0x07060302)};
    }
    int i, p0; unit_rc(512 + (tid & 63), i, p0);
    *(LAS u32x4*)(lds + L_MEMS + 8192 + (tid & 63) * 16) = *(LAS u32x4*)(lds + raw + (i * DM + p0) * 2);
}
DI unsigned read_canaries(int s, rsrc_t rs, int lane, int& budget) {
    const int o = 2 * OFF_CAN + (s & 3) * 16384 + (lane >> 2) * 512 + (lane & 3) * 16;
    u32x2 v;
    for (;;) {
        v = ld64(rs, o, 0);
        asm volatile("" ::: "memory");
        if (__all((v.y & 0xffff) == (unsigned)(s + 1))) break;
        if (--budget < 0) break;
    }
    const unsigned t = 8 * now();
    return t + (unsigned)wave_max32(min(max((int)(v.x - t), -0x4000), 0x4000));
}
DI u32x8 sm_frag(LAS char* lds, int sl, int r0, int sa, int sH, int cb, int lo8) {
    auto rd = [&](int r) { return tr_read(lds, sl + r * 256 + ((cb ^ (2 * swz(r))) * 16) + lo8); };
    const u32x2 a = rd(r0), b = rd(r0 + sa), c = rd(r0 + sH), d = rd(r0 + sa + sH);
    return (u32x8){a.x, a.y, b.x, b.y, c.x, c.y, d.x, d.y};
}
#define GS(slot, i)
#define GV(slot, i, v)
DI int tp_unit(int U, int sel) { return OFF_TP + cmap((U & 511) * 4096) + ((sel >> (1 + (U >> 8 & 1)) ^ U >> 9) & 1) * 8192; }
template <int N> DI void finish(const Args& a, rsrc_t rw, int U, int sel, unsigned t, int& budget) {
    const int lane = opaque_lane(), n = lane & 7, g = lane >> 4;
    const rsrc_t ru = __builtin_amdgcn_make_buffer_rsrc(a.out, 0, SEQ * DM * 2, 0x00020000);
    constexpr int M = (N + 1) / 2;
    int at[M]; f32x2 bv[M]; float gt[M];
#pragma unroll
    for (int i = 0; i < M; ++i) {
        const int u = U + 2 * i + (lane >> 3 & 1 & (N - 1));
        const int tok = LATE * SEG + (U < 512 ? 2 * (u & 15) + (n >> 2) : (u & 32) + 4 * (u & 7) + (n >> 1));
        const int col = U < 512 ? 32 * (u >> 4) + 16 * (g >> 1) + 4 * (n & 3) + 2 * (g & 1) : 512 + 2 * (u & 24) + 8 * (n & 1) + 2 * g;
#pragma unroll
        for (int r = 0; r < 2; ++r) bv[i][r] = bf2f(__builtin_amdgcn_raw_buffer_load_b16(rw, col * BO_ROW + tok * 2, OFF_BO + r * BO_ROW, 16));
        gt[i] = __uint_as_float(ld32(rw, tok * 4, OFF_GT));
        at[i] = (tok * DM + col) * 2;
    }
    u32x4 one;
#pragma unroll
    for (int j = 0; j < 4; ++j) one[j] = (lane >> 2 & 3) == j ? 0x3F80u << 16 * (lane & 1) : 0u;
#pragma unroll
    for (int i = 0; i < M; ++i) { bv[i] *= 1.0f - gt[i]; asm volatile("" : "+v"(bv[i]), "+v"(one)); }
    (void)wait_until(t);
    f32x4 v[N];
    for (;;) {
        u32x4 d[4 * N];
#pragma unroll
        for (int k = 0; k < 4 * N; ++k) d[k] = ld128(rw, lane * 16 + (k & 3) * 1024, tp_unit(U + (k >> 2), sel));
        unsigned top = 0;
#pragma unroll
        for (int i = 0; i < N; ++i) {
            asm volatile(M16 "%0, %1, %2, 0\n\t" M16 "%0, %1, %3, %0\n\t" M16 "%0, %1, %4, %0\n\t" M16 "%0, %1, %5, %0\n\ts_nop 7" : "=&v"(v[i]) : "v"(one), "v"(d[4 * i]), "v"(d[4 * i + 1]), "v"(d[4 * i + 2]), "v"(d[4 * i + 3]));
            top = mx4(mx4(mx4(mx4(top, d[4 * i]), d[4 * i + 1]), d[4 * i + 2]), d[4 * i + 3]);
        }
        if (!__any(top == SENT) || --budget < 0) break;
        __builtin_amdgcn_s_sleep(4);
    }
#pragma unroll
    for (int i = 0; i < M; ++i) {
        f32x2 x;
#pragma unroll
        for (int r = 0; r < 2; ++r) {
            auto both = [](float y) { return y + __int_as_float(__builtin_amdgcn_update_dpp(0, __float_as_int(y), 0x128, 0xf, 0xf, true)); };
            const float p = both(v[2 * i][r]), q = both(v[(2 * i + 1) & (N - 1)][r]);
            x[r] = gt[i] * (lane & 8 ? q : p) + bv[i][r];
        }
        __builtin_amdgcn_raw_buffer_store_b32(pack2(x[0], x[1]), ru, at[i], 0, OUT_AUX);
    }
}
DI void owner_loop(const Args& a, int G, rsrc_t rs, rsrc_t rw, int sel, unsigned tbase, unsigned cbase, int& budget) {
    const int lane = threadIdx.x & 63, n = lane & 15, g = lane >> 4;
    const bool ex = G >= 64;
    const u32x4 sent4 = {SENT, SENT, SENT, SENT};
    const int js = (n & 2) ? 8 : ex ? (n & 4) + 2 * (n & 1) + (n >> 3) : 2 * (n >> 2) + (n & 1);
    u32x4 selu;
#pragma unroll
    for (int j = 0; j < 4; ++j) selu[j] = (js == 2 * j ? 0x3F80u : 0u) | (js == 2 * j + 1 ? 0x3F800000u : 0u);
    const int o = cmap(G * 4096 + lane * 16);
    const int so = (n < 8 || NREP > 1) ? cmap((n >> 3) * 16384 + G * 128 + (g & 1) * 4 + (ex ? (n & 4) * 16 + (g >> 1) * 32 + (n & 3) * 8 : (n & 6) * 16 + (g >> 1) * 16 + (n & 1) * 8)) : OOB;
    unsigned fails = 0;
    const int c8 = G >= 64 ? a.c1 >> 16 : G < 8 ? a.c1 >> 8 & 255 : a.c1 & 255;
    const bool gv_on = true; (void)gv_on;
    unsigned oT = now(), oC = (unsigned)__builtin_readcyclecounter(), oR;
    { const unsigned r = (unsigned)(512.0f * (float)(oC - cbase) * __builtin_amdgcn_rcpf((float)(oT - tbase))); asm volatile("s_nop 0\n\tv_readfirstlane_b32 %0, %1" : "=s"(oR) : "v"(r)); }
    oT = 8 * oT + 16;
    unsigned lk = oC;
    budget = __builtin_amdgcn_readfirstlane(budget);
    const int ca = 2 * OFF_CAN + (lane >> 2) * 512 + (G & (NCP - 1)) * 128 + (lane & 3) * 16;
    unsigned tc;
    for (int s = 0; s < NSEG - 1; ++s) {
        const int pb = 2 * (OFF_PART + (s & 1) * PART_BYTES);
        int np;
        asm volatile(OWNER_ASM : [np] "=&s"(np), [tm] "=&s"(tc), [bud] "+s"(budget), [oT] "+s"(oT), [oC] "+s"(oC), [oR] "+s"(oR), [lk] "+s"(lk)
            : [c8] "s"(c8), [ca] "v"(ca), [cs] "s"((s & 3) * 16384), [tag] "s"(s + 1), [o] "v"(o), [so] "v"(so), [sel] "v"(selu), [rs] "s"(rs), [pb] "s"(pb), [mb0] "s"(2 * (OFF_MEMB + ((s + 1) & 3) * MEMB_STRIDE)),
              [cw] "s"(a.cw - c8), [rd] "s"(a.rd), [sv] "v"(sent4), [ro] "v"(s ? o : OOB), [pq] "s"(2 * PART_BYTES - pb), [rm] "v"(s > 2 ? so : OOB), [mq] "s"(2 * (OFF_MEMB + ((s + 2) & 3) * MEMB_STRIDE)),
              [rw] "s"(rw), [rt] "v"(s >= 24 && s < 56 ? lane * 16 : OOB), [tq] "s"(tp_unit(8 * G + (s >> 2 & 7), sel) + (s & 3) * 1024) : OWNER_CLOBBER);
        fails += np > 1;
    }
    wait_prog(rw, F_S4 + lane * 4, true, 1, budget);
    const unsigned t = now(), t0 = t + min((int)(tc - 8 * t) >> 3, 100);
    finish<4>(a, rw, 4 * G + 4 * (G & 64), sel, t0 + (a.xa >> 8 & 255), budget);
    if (G >= 32 && G < 64) finish<1>(a, rw, 512 + G, sel, t0 + (a.xb >> 24), budget);
}
DI void role_owner(const Args& a, unsigned lds_base, int ow, rsrc_t rw, int& budget) {
    const int tid = threadIdx.x, lane = tid & 63, w = __builtin_amdgcn_readfirstlane(tid >> 6);
    const unsigned tbase = now(), cbase = (unsigned)__builtin_readcyclecounter();
    if (ow != 63 && w) { prefetch(a, lds_base, ow & 3, (ow >> 2) * 3 + w - 1, (ow & 3) == 3 ? 45 : 48); VM_DRAIN(); }
    unsigned best[2] = {~0u, ~0u};
    if (ow == 63) {
        for (int r = 0; r < 10; ++r) {
            unsigned long long t0, t1;
            asm volatile("s_memtime %0\n\ts_waitcnt lgkmcnt(0)" : "=s"(t0) :: "memory");
            st32(SENT, rw, lane == 0 && w < 3 ? (r & 1) * 8192 + (r >> 1) * 4096 * 5 / 4096 * 16384 + ((r >> 1) & 1) * 4096 : OOB, w << 21);
            asm volatile("s_waitcnt vmcnt(0)\n\ts_memtime %0\n\ts_waitcnt lgkmcnt(0)" : "=s"(t1) :: "memory");
            best[r & 1] = min(best[r & 1], (unsigned)(t1 - t0));
        }
        *(LAS unsigned*)(uintptr_t)(lds_base + w * 4) = best[1] < best[0];
        WG_BARRIER();
    }
    if (w >= 2 || (w == 1 && ow >= 8)) return;
    const u32x2 tag = launch_tag(a);
    if (ow == 63) {
        const u32x4 p = *(LAS u32x4*)(uintptr_t)lds_base;
        __builtin_amdgcn_raw_buffer_store_b64((u32x2){(tag.x & ~7u) | p.x | p.y << 1 | p.z << 2, tag.y}, rw, lane == 0 ? F_INIT + 32 * 128 : OOB, 0, 16);
    }
    const int sel = wait_init(rw, tag, budget, 32);
    const rsrc_t rs = __builtin_amdgcn_make_buffer_rsrc(a.ws + (sel & 1) * 8192, 0, COMM_BYTES - 8192, 0x00020000);
    owner_loop(a, ow + 64 * w, rs, rw, sel, tbase, cbase, budget);
}

template <int O, class V> DI void put8(V& d, u32x8 f) {
#pragma unroll
    for (int j = 0; j < 8; ++j) d[O + j] = f[j];
}
DI f32x4 SMF64(u32x4 a, u32x8 b, f32x4 c, int i) { asm volatile("s_nop 1\n\tv_smfmac_f32_16x16x64_bf16 %0, %1, %2, %3\n\ts_nop 7" : "+v"(c) : "v"(a), "v"(b), "v"(i)); return c; }
DI f32x4 SMF32(u32x2 a, u32x4 b, f32x4 c, int i) { asm volatile("s_nop 1\n\tv_smfmac_f32_16x16x32_bf16 %0, %1, %2, %3\n\ts_nop 7" : "+v"(c) : "v"(a), "v"(b), "v"(i)); return c; }
DI f32x4 MFV(u32x4 a, u32x4 b, f32x4 c) { asm volatile("s_nop 1\n\tv_mfma_f32_16x16x32_bf16 %0, %1, %2, %0\n\ts_nop 7" : "+v"(c) : "v"(a), "v"(b)); return c; }

DI void role_head(const Args& a, LAS char* lds, unsigned lds_base, int h, int tw, rsrc_t rw, int& budget) {
    const int tid = threadIdx.x, w = __builtin_amdgcn_readfirstlane(tid >> 6);
    char* ws = a.ws;
    const unsigned tbase = now(), cbase = (unsigned)__builtin_readcyclecounter();
    const bool gv_on = tw == 0; (void)gv_on;
    u32x32 A0, A1, A2, A3, A4, A5, A6, A7, R0, R1, R2, R3; u32x8 R4;
    u32x2 tag;
    {
        const int lane = tid & 63, n = lane & 15, g = lane >> 4;
#pragma unroll
        for (int j = 0; j < 3; ++j) glds16(a.mem0, min(4 * j + w, 8) * 1024 + lane * 16, lds_base + L_KX + min(4 * j + w, 8) * 1024);
        const int tq = n >> 2, tc = (n & 3) >> 1, lo8 = (n & 1) * 8;
        const int r_nat = 16 * (g >> 1) + 4 * (g & 1) + (tq & 1) + 8 * (tq >> 1);
        const int r_sig = 16 * (tq & 1) + 4 * (g >> 1) + (g & 1) + 2 * (tq >> 1);
        const int r_o = 16 * (tq & 1) + 8 * (g >> 1) + 4 * (g & 1) + (tq >> 1);
#define WFRAG(sl, kb, t) ((kb) < 8 ? sm_frag(lds, sl, r_sig, 32, 8, 4 * w + 2 * (t) + tc, lo8) : sm_frag(lds, sl, r_nat, 2, 32, 4 * w + 2 * (t) + tc, lo8))
        const u32x4 ro = ring_offs(w, lane, HD);
        auto issue = [&](int i) {
            const unsigned slot = lds_base + L_W + (i % C_RING) * 16384 + w * 4096;
            if (i == 23) ring_issue(a.Wo + (size_t)h * DH * DM + 512, ring_offs(w, lane, [](int r, int c) { return (r + 64 * (c >> 3)) * (DM * 2) + (c & 7) * 16; }), slot);
            else if (i < 28) ring_issue((i < 8 || i == 20 ? a.Wq : i < 16 || i == 21 ? a.Wk : a.Wv) + (size_t)(64 * (i < 16 ? i & 7 : i < 20 ? i - 12 : i < 24 ? 8 : i - 24)) * HD + h * DH, ro, slot);
            else ring_issue(a.Wo + (size_t)(h * DH + 64 * ((i & 1) ^ ((i - 28) >> 1))) * DM + 256 * tw + 128 * (i & 1), ring_offs(w, lane, DM), slot);
        };
        auto keep = [&](int at, u32x8 f) {
            *(LAS u32x4*)(lds + at + lane * 16) = (u32x4){f[0], f[1], f[2], f[3]};
            *(LAS u32x4*)(lds + at + 1024 + lane * 16) = (u32x4){f[4], f[5], f[6], f[7]};
        };
        u32x8 hold[8] = {};
#pragma unroll
        for (int i = 0; i < C_RING; ++i) issue(i);
        tag = launch_tag(a);
        ring_wait<4 * C_RING>();
        mem0_arrange(lds, L_KX, tid);
        static_for<9>([&](auto ic) {
            constexpr int G = decltype(ic)::value;
            ring_wait<(G < 7 ? 16 : G == 7 ? 8 : 0)>();
            const int sl = L_W + (G & 1) * 65536;
            auto fr = [&](int j, int t) { return WFRAG(sl + ((j ^ w) << 14), 0, t); };
            if constexpr (G < 4) {
                u32x32& A = G == 0 ? A0 : G == 1 ? A2 : G == 2 ? A4 : A6;
                u32x32& B = G == 0 ? A1 : G == 1 ? A3 : G == 2 ? A5 : A7;
                put8<0>(A, fr(0, 0)); put8<8>(A, fr(0, 1)); put8<16>(A, fr(1, 0)); put8<24>(A, fr(1, 1));
                put8<0>(B, fr(2, 0)); put8<8>(B, fr(2, 1)); put8<16>(B, fr(3, 0)); put8<24>(B, fr(3, 1));
                if constexpr (G == 0) asm volatile("" : "+{a[0:31]}"(A0), "+{a[32:63]}"(A1));
                if constexpr (G == 1) asm volatile("" : "+{a[64:95]}"(A2), "+{a[96:127]}"(A3));
                if constexpr (G == 2) asm volatile("" : "+{a[128:159]}"(A4), "+{a[160:191]}"(A5));
                if constexpr (G == 3) asm volatile("" : "+{a[192:223]}"(A6), "+{a[224:255]}"(A7));
            } else if constexpr (G == 4) {
                put8<16>(R1, fr(0, 0)); put8<24>(R1, fr(0, 1)); put8<0>(R2, fr(1, 0)); put8<8>(R2, fr(1, 1));
                put8<16>(R2, fr(2, 0)); put8<24>(R2, fr(2, 1)); put8<0>(R3, fr(3, 0)); put8<8>(R3, fr(3, 1));
            } else if constexpr (G == 5) {
                put8<16>(R0, WFRAG(sl, 8, 0)); put8<24>(R0, WFRAG(sl, 8, 1)); put8<0>(R1, WFRAG(sl + 16384, 8, 0)); put8<8>(R1, WFRAG(sl + 16384, 8, 1));
                put8<16>(R3, WFRAG(sl + 32768, 8, 0)); put8<24>(R3, WFRAG(sl + 32768, 8, 1));
                hold[7] = sm_frag(lds, sl + 49152, r_o, 2, 32, 8 * (w >> 1) + 4 * tw + 2 * (w & 1) + tc, lo8);
            } else if constexpr (G == 6) {
#pragma unroll
                for (int j = 0; j < 7; ++j) hold[j] = fr(j >> 1, j & 1);
                R4 = fr(3, 1);
            } else {
                if constexpr (G == 8)
#pragma unroll
                    for (int T = 0; T < 4; ++T) keep(L_W + 65536 + w * 8192 + T * 2048, hold[T]);
#pragma unroll
                for (int T = 0; T < 4; ++T) hold[T] = sm_frag(lds, L_W + (2 * G - 10 + (w >> 1)) * 16384, r_o, 2, 32, 8 * (w & 1) + 2 * T + tc, lo8);
            }
            if constexpr (G != 7) WG_BARRIER();
            if constexpr (G < 6)
#pragma unroll
                for (int i = 0; i < 4; ++i) issue(4 * G + 8 + i);
            if constexpr (G == 6)
#pragma unroll
                for (int j = 0; j < 8; ++j) keep(L_W + (j >> 1) * 16384 + w * 4096 + (j & 1) * 2048, hold[j]);
            if constexpr (G == 8)
#pragma unroll
                for (int T = 0; T < 4; ++T) keep(L_W + 98304 + w * 8192 + T * 2048, hold[T]);
        });
        const int ai = 2 * (n >> 2) + (n & 1), ahf = (n >> 1) & 1;
        R0[0] = R0[1] = R0[2] = R0[3] = 0x3F803F80u;
        R0[4] = ahf ? 0xEEEE : 0x4444;
        R0[5] = (g >> 1) * 512 + (n >> 2) * 128 + (g & 1) * 64 + (n & 3) * 16;
        R0[6] = lds_base + L_MEMS + 8192 + g * 256 + ai * 32 + ahf * 16;
        R0[7] = lane * 16;
        R0[8] = lds_base + L_Q + 2 * g * 320 + (16 * w + n) * 4;
        R0[9] = lds_base + L_KO + 2 * g * 576 + (16 * w + (n & 8) + 4 * (n & 1) + ((n >> 1) & 3)) * 4;
        R0[10] = lds_base + L_Q + ai * 320 + g * 16;
        R0[11] = lds_base + L_KO + ai * 576 + g * 32 + ahf * 16;
        R0[12] = lds_base + L_KX + 8192 + w * 2048 + (g >> 1) * 256 + n * 16 + (g & 1) * 8;
        R0[13] = lds_base + L_W + w * 4096 + lane * 16;
        R0[14] = cmap(((2 * (4 * tw + w) + (n >> 3)) * 4 + g) * 4096 + h * 128 + (n & 7) * 16);
        R0[15] = (g >> 1) == (w >> 1) ? cmap((64 + 2 * (2 * tw + (w & 1)) + (g >> 1)) * 4096 + h * 128 + (g & 1) * 64 + n * 4) : OOB;
        asm volatile("" : "+{v[120:151]}"(R0), "+{v[152:183]}"(R1), "+{v[184:215]}"(R2), "+{v[216:247]}"(R3), "+{v[248:255]}"(R4));
    }
    const int sel = wait_init(rw, tag, budget, 33 + 4 * h + w);
    int kv_rdy = 0;
    const rsrc_t rs = __builtin_amdgcn_make_buffer_rsrc(ws + (sel & 1) * 8192, 0, COMM_BYTES - 8192, 0x00020000);
    auto dma = [&](int voff, int soff, unsigned dst) { bdma(rw, voff, soff, dst); };
    auto fetch_kv = [&](int s, int lane) {
        const int n = lane & 15, g = lane >> 4;
        const int kxs = (g >> 1) * 1024 + ((g & 1) * 32 + 16 * (n >> 3) + (n & 7)) * 16;
        const int vxs = (g >> 1) * 1024 + ((g & 1) * 32 + n) * 16;
        const int so_ = (s * 32 + h) * 8192 + w * 2048;
        const unsigned xdst = lds_base + L_KX + w * 2048;
        dma(kxs, OFF_KF + so_, xdst); dma(kxs, OFF_KF + so_ + 128, xdst + 1024);
        dma(vxs, OFF_VF + so_, xdst + 8192); dma(vxs, OFF_VF + so_ + 256, xdst + 8192 + 1024);
    };
    const int fo = F_KVM + h * 256 + tw * 128;
    u32x8 scon;
    scon[0] = w; scon[1] = 2 * OFF_CAN + (h >> 1) * 512 + (2 * (h & 1) + tw) * 16; scon[2] = F_KV + h * 512 + w * 128; scon[3] = 65536 + w * 4096;
    scon[4] = OFF_KF + h * 8192 + w * 2048; scon[5] = fo; scon[6] = tw - 1; scon[7] = (tw ? OFF_VM : OFF_KM) + (h - NH) * 2048 + w * 512;
    u32x8 S1 = {0u, 0u, (unsigned)kv_rdy, (unsigned)budget, (unsigned)a.d0, 0xFFFFFFFFu / 22, (unsigned)(a.c2 + a.b0), 0u};
    u32x8 S2 = {0u, 0u, 0u, (unsigned)a.up, (unsigned)a.dn, (unsigned)a.c2, 0u, 1u};
    fetch_kv(0, opaque_lane());
    __builtin_amdgcn_raw_buffer_store_b64((u32x2){8 * now() + a.e0, 1u | (a.c2 + a.b0) << 16}, rs, tid < NCP ? tid * 128 : OOB, scon[1], 16);
    asm volatile("s_waitcnt vmcnt(1)" ::: "memory");
    u32x2 vm = {};
    u32x8 S3;
    {
        const unsigned t = now(), c = (unsigned)__builtin_readcyclecounter();
        const float r = (float)(c - cbase) * __builtin_amdgcn_rcpf(8.0f * (float)(t - tbase));
        S2[1] = 8 * t + 16; S1[5] = (unsigned)(4294967296.0f * __builtin_amdgcn_rcpf(r));
        S3 = (u32x8){c, (unsigned)(4096.0f * r), 2u * OFF_MEMB + ((h >> 3) & (NREP - 1)) * 32768, w == 3 ? ~0u : 0u};
    }
    const int U0 = (2 * tw + (w >> 1)) * 64 + 8 * (w & 1), U1 = 4 * (2 * h + tw) + w;
    for (;;) {
        asm volatile(STEP_ASM
            : "+{v[96:97]}"(vm), "+{v[0:7]}"(S1), "+{v[8:15]}"(S2), "+{v[16:23]}"(S3), "+{v[24:31]}"(scon)
              , "+{a[0:31]}"(A0), "+{a[32:63]}"(A1), "+{a[64:95]}"(A2), "+{a[96:127]}"(A3), "+{a[128:159]}"(A4), "+{a[160:191]}"(A5), "+{a[192:223]}"(A6), "+{v[120:151]}"(R0), "+{v[152:183]}"(R1)
            : "{a[224:255]}"(A7), "{v[184:215]}"(R2), "{v[216:247]}"(R3), "{v[248:255]}"(R4),
              [rs] "s"(rs), [rw] "s"(rw), [qb] "s"(OFF_OF + LATE * 262144 + h * 8192 + (w & 1) * 256),
              [tp] "s"(tp_unit(U0, sel) + h * 128), [tq] "s"(tp_unit(256 + U0, sel) + h * 128), [tx] "s"(tp_unit(512 + (U0 >> 3) + 3 * (w & 1), sel) + h * 128),
              [u] "s"(U1), [ua] "s"(tp_unit(256 + U1, sel)), [fw] "s"(8 * (a.xb >> 16 & 255)), [ru] "s"(__builtin_amdgcn_make_buffer_rsrc(a.out, 0, SEQ * DM * 2, 0x00020000))
            : STEP_CLOBBER);
        const int s = __builtin_amdgcn_readfirstlane(S1[0]);
        const unsigned st = __builtin_amdgcn_readfirstlane(S1[7]);
        kv_rdy = __builtin_amdgcn_readfirstlane(S1[2]); budget = __builtin_amdgcn_readfirstlane(S1[3]);
        if (s == NSEG - 1) break;
        if (__builtin_expect(st & 2, 0)) {
            const int lane = opaque_lane();
            LAS char* z = (LAS char*)0;
            const f32x4 zero = {0, 0, 0, 0};
            const u32x4 ones = __builtin_shufflevector(R0, R0, 0, 1, 2, 3);
            const unsigned zr = lane >> 6;
            const int idx = R0[4];
            u32x4 qf[4];
            f32x4 s1 = zero, s2 = zero;
#pragma unroll
            for (int ks = 0; ks < 4; ++ks) {
                qf[ks] = *(LAS u32x4*)(z + R0[10] + ks * 64);
                s1 = MFV(*(LAS u32x4*)(lds + L_KX + lane * 16 + ks * 2048), qf[ks], s1);
                s2 = MFV(*(LAS u32x4*)(lds + L_KX + lane * 16 + ks * 2048 + 1024), qf[ks], s2);
            }
            f32x4 s0 = SMF64(*(LAS u32x4*)(z + R0[11]), __builtin_shufflevector(qf[0], qf[1], 0, 1, 2, 3, 4, 5, 6, 7), zero, idx);
            s0 = SMF64(*(LAS u32x4*)(z + R0[11] + 128), __builtin_shufflevector(qf[2], qf[3], 0, 1, 2, 3, 4, 5, 6, 7), s0, idx);
            const float sa = s0[0] + s0[2], sb = s0[1] + s0[3];
            float mx = fmaxf(fmaxf(fmaxf(fmaxf(s1[0], s1[1]), fmaxf(s1[2], s1[3])), fmaxf(fmaxf(s2[0], s2[1]), fmaxf(s2[2], s2[3]))), fmaxf(sa, sb));
            mx = fmaxf(mx, __shfl_xor(mx, 16)); mx = fmaxf(mx, __shfl_xor(mx, 32));
            auto ex2 = [&](float x, float y) { return pack2(__builtin_amdgcn_exp2f(x - mx), __builtin_amdgcn_exp2f(y - mx)); };
            const u32x4 px = {ex2(s1[0], s1[1]), ex2(s1[2], s1[3]), ex2(s2[0], s2[1]), ex2(s2[2], s2[3])};
            const unsigned pm = ex2(sa, sb);
            const f32x4 l = MFV(((u32x4){pm, zr, zr, zr}), ones, MFV(px, ones, zero));
            const u32x2 x0 = *(LAS u32x2*)(z + R0[12]), y0 = *(LAS u32x2*)(z + R0[12] + 1024), x1 = *(LAS u32x2*)(z + R0[12] + 512), y1 = *(LAS u32x2*)(z + R0[12] + 1536);
            const f32x4 o = SMF64(px, ((u32x8){x0.x, y0.x, x0.y, y0.y, x1.x, y1.x, x1.y, y1.y}), zero, idx) + SMF32(((u32x2){pm, zr}), ((u32x4){vm.x, vm.y, zr, zr}), zero, idx);
            const float i0 = __builtin_amdgcn_rcpf(l[0]), i1 = __builtin_amdgcn_rcpf(l[1]);
            *(LAS unsigned*)(z + R0[9] + 256) = pack2(o[0] * i0, o[2] * i0);
            *(LAS unsigned*)(z + R0[9] + 256 + 576) = pack2(o[1] * i1, o[3] * i1);
            WG_BARRIER();
            const u32x4 oa = *(LAS u32x4*)(z + R0[11] + 256 + (w >> 1) * 128), ob = *(LAS u32x4*)(z + R0[11] + 384 - (w >> 1) * 128);
            auto frag = [&](int at) { return __builtin_shufflevector(*(LAS u32x4*)(z + at), *(LAS u32x4*)(z + at + 1024), 0, 1, 2, 3, 4, 5, 6, 7); };
            f32x2 f[4];
#pragma unroll
            for (int T = 0; T < 4; ++T) f[T] = fold(SMF64(ob, frag(R0[13] + scon[3] + 32768 + T * 2048), SMF64(oa, frag(R0[13] + scon[3] + T * 2048), zero, idx), idx));
            const f32x2 fx = fold(SMF64(ob, frag((R0[13] ^ 8192) + 49152 + 2048), SMF64(oa, frag(R0[13] + 49152 + 2048), zero, idx), idx));
            const int pb = 2 * (OFF_PART + (s & 1) * PART_BYTES);
            st128((u32x4){pack2(f[0].x, f[1].x), pack2(f[2].x, f[3].x), pack2(f[0].y, f[1].y), pack2(f[2].y, f[3].y)}, rs, R0[14], pb);
            st32(pack2(fx.x, fx.y), rs, R0[15], pb);
            if (s == LATE) { S1[7] = 4; continue; }
        }
        if (__builtin_expect((st & 2) || kv_rdy <= s, 0)) {
            while (kv_rdy <= s) { kv_rdy = prog_min(rw, F_KV + h * 512 + w * 128); if (--budget < 0) break; }
            fetch_kv(s + 1, opaque_lane());
        }
        if (st & 2) { S1[1] = read_canaries(s, rs, opaque_lane(), budget) + S1[6]; S2[2] += 0x10000; }
        if ((int)(S1[1] - S2[1]) > 0x8000) { S2[1] = 8 * now() + 16; S3[0] = (unsigned)__builtin_readcyclecounter(); }
        S1[0] = s + 1; S1[2] = kv_rdy; S1[3] = budget;
    }
    TT(true, 0)
    st32((unsigned)(NSEG - 2), rw, opaque_lane() == 0 && w == 0 ? fo : OOB, 0);
    TT(true, 2)
}

__global__ void __launch_bounds__(256, 1) fused_kernel(Args a) {
    extern __shared__ __attribute__((aligned(16))) char smem[];
    LAS char* lds = (LAS char*)smem;
    const unsigned lds_base = (unsigned)(uintptr_t)lds;
    const int w = __builtin_amdgcn_readfirstlane(threadIdx.x >> 6);
    const int b = blockIdx.x, bx = b & 7, bi = b >> 3;
    if (bi >= 28) {
        const void* pc; unsigned len;
        asm volatile("s_getpc_b64 %0\n1:\ts_mov_b32 %1, .Lstep_end-1b" : "=s"(pc), "=s"(len));
        touch(pc, min((bi - 28) * 32768 + threadIdx.x * 128, len), lds_base);
    }
    asm volatile("" :: "s"(a.Wq), "s"(a.cw));
    const rsrc_t rw = __builtin_amdgcn_make_buffer_rsrc(a.ws, 0, WS_USED, 0x00020000);
    int budget = SPIN_MAX;
#define RT(i)
    if (bx < 4) {
        if (bi < 16) role_head(a, lds, lds_base, 8 * bx + (bi >> 1), bi & 1, rw, budget);
        else role_owner(a, lds_base, 4 * (bi - 16) + bx, rw, budget);
    } else {
        const int fx = bx - 4, no = fx < 3 ? 14 : 22, j = 14 * fx + bi;
        if (bi >= no || (j & 2)) {
            const unsigned t = now() + (a.d1 & 0xffff);
            if (bi >= no) { if (w) prefetch(a, lds_base, bx, (bi - no) * 3 + w - 1, 3 * (32 - no)); }
            else {
                const int sh = (j >> 2) * 2 + (j & 1);
#pragma unroll
                for (int i = 0; i < INIT_SHARE / 4096; ++i) st128((u32x4){SENT, SENT, SENT, SENT}, rw, threadIdx.x * 16 + i * 4096, sh * INIT_SHARE);
#pragma unroll
                for (int i = 0; i < 8; ++i) st128((u32x4){SENT, SENT, SENT, SENT}, rw, threadIdx.x * 16, i < 4 ? (i & 2 ? OFF_VM : OFF_KM) + FAST * 65536 + (2 * sh + (i & 1)) * 4096 : OFF_OF + LATE * 262144 + (4 * sh + i - 4) * 4096);
                const u32x2 tag = launch_tag(a);
                VM_DRAIN();
                WG_BARRIER();
                __builtin_amdgcn_raw_buffer_store_b64(tag, rw, threadIdx.x == 0 ? F_INIT + sh * 128 : OOB, 0, 16);
                RT(3)
            }
            (void)wait_until(t);
        }
        if (bi >= no) {
            const int t = 18 * fx + bi - no;
            role_s4d(a, lds, lds_base, t, rw, budget);
            RT(1)
            TT(true, 6)
            if (t < 9 * OUT_REP) role_outp(a, lds, lds_base, t % 9, t / 9, rw, budget);
        }
        else {
            if (!(j & 2)) role_kv(a, lds, lds_base, (j >> 2) * 2 + (j & 1), rw, budget);
            RT(1)
            TT(true, 6)
            role_qa(a, lds, lds_base, (j >> 2) * 2, (j & 2 ? 0 : FAST + 2) + (j & 1), (j & 2 ? FAST : FAST + 2) + (j & 1), rw, budget);
        }
    }
    RT(2)
    TT(true, 5)
    if (budget < 0 && threadIdx.x == 0) st32(0x1000u + b, rw, F_ERR, 0);
}

static int g_c1 = 56, g_c1m = 52, g_c1x = 70, g_c2 = 364, g_b0 = 64, g_up = 16, g_fl = 8, g_dn = 3, g_d0 = 1680, g_da = 100, g_ua = 48, g_d1 = 300, g_rd = 34, g_cw = 69, g_e0 = 165, g_x1 = 35, g_x2 = 140, g_x3 = 150, g_x4 = 75, g_x5 = 148, g_x6 = 120, g_x7 = 50, g_x8 = 210, g_pc = 45;
extern "C" void kernel_launch(void* const* d_in, const int* in_sizes, int n_in, void* d_out, int out_size, void* d_ws, size_t ws_size, hipStream_t stream) {
    Args a;
    a.x = (const u16*)d_in[0]; a.mem0 = (const u16*)d_in[1];
    a.Wq = (const u16*)d_in[2]; a.Wk = (const u16*)d_in[3]; a.Wv = (const u16*)d_in[4]; a.Wo = (const u16*)d_in[5];
    a.lAr = (const u16*)d_in[8]; a.Aim = (const u16*)d_in[9]; a.Cr = (const u16*)d_in[10]; a.Ci = (const u16*)d_in[11];
    a.ldt = (const u16*)d_in[12]; a.Dsk = (const u16*)d_in[13]; a.gw = (const u16*)d_in[14];
    a.out = (u16*)d_out;
    a.ws = (char*)(((uintptr_t)d_ws + ((2u << 20) - 1)) & ~(uintptr_t)((2u << 20) - 1));
    a.c1 = g_c1 | g_c1m << 8 | g_c1x << 16; a.c2 = g_c2; a.d0 = g_d0 + g_da; a.b0 = g_b0; a.d1 = g_d1 | g_pc << 16; a.up = g_up | g_ua << 8 | g_fl << 16; a.dn = g_dn | g_d0 << 8; a.rd = 23 * g_rd - 300; a.cw = 8 * g_cw - 104; a.e0 = 8 * g_e0;
    a.xa = g_x1 | g_x2 << 8 | g_x5 << 16 | g_x7 << 24; a.xb = g_x6 | g_x3 << 8 | g_x4 << 16 | g_x8 << 24;
    (void)hipFuncSetAttribute((const void*)fused_kernel, hipFuncAttributeMaxDynamicSharedMemorySize, L_TOTAL);
    fused_kernel<<<256, 256, L_TOTAL, stream>>>(a);
}
```

```cpp
#include <hip/hip_runtime.h>
#include <hip/hip_bf16.h>
#include <stdint.h>

typedef unsigned short u16;
typedef __bf16 bf16x8 __attribute__((ext_vector_type(8)));
typedef __bf16 bf16x16 __attribute__((ext_vector_type(16)));
typedef _Float16 f16x2 __attribute__((ext_vector_type(2)));
typedef _Float16 f16x8 __attribute__((ext_vector_type(8)));
typedef short s16x4 __attribute__((ext_vector_type(4)));
typedef float f32x2 __attribute__((ext_vector_type(2)));
typedef float f32x4 __attribute__((ext_vector_type(4)));
typedef float f32x16 __attribute__((ext_vector_type(16)));
typedef unsigned u32x2 __attribute__((ext_vector_type(2)));
typedef unsigned u32x4 __attribute__((ext_vector_type(4)));
typedef unsigned u32x8 __attribute__((ext_vector_type(8)));
typedef unsigned u32x16 __attribute__((ext_vector_type(16)));
typedef unsigned u32x32 __attribute__((ext_vector_type(32)));
typedef short s16x8 __attribute__((ext_vector_type(8)));
#define LAS __attribute__((address_space(3)))
#define DI __device__ __forceinline__
typedef __amdgpu_buffer_rsrc_t rsrc_t;

constexpr int SEQ = 2048, DM = 576, NH = 32, DH = 128, HD = NH * DH, NM = 8, SEG = 32, NSEG = 64, NS = 128;
constexpr unsigned SENT = 0xFFFFFFFFu;
constexpr unsigned OOB = 0x7FFFFFF0u;
constexpr int SPIN_MAX = 1 << 20;

constexpr int PART_STEP = 16384;
constexpr int OFF_MEMB = 40 * PART_STEP;
constexpr int MEMB_STRIDE = 32768;
constexpr int OFF_CAN = 52 * PART_STEP;
constexpr int COMM_LOGICAL = OFF_CAN + 32768;
constexpr int COMM_BYTES = 2 * COMM_LOGICAL;
constexpr int F_ERR = COMM_BYTES;
constexpr int F_KV = F_ERR + 128;
constexpr int F_KVM = F_KV + 16384;
constexpr int F_O = F_KVM + 8192;
constexpr int F_H = F_O + 4096;
constexpr int F_S4 = F_H + 32768;
constexpr int F_DONE = F_S4 + 512;
constexpr int INIT_SHARE = 14 * 4096, INIT_BYTES = 32 * INIT_SHARE;
constexpr int F_INIT = INIT_BYTES;
constexpr int F_EPOCH = F_INIT + 24576;
static_assert(F_DONE + 128 <= INIT_BYTES, "");
static_assert(F_EPOCH + 128 <= (2 << 20), "hand-off region");
constexpr int OFF_TP = 2 << 20;
constexpr int OFF_KF = 6 << 20;
constexpr int OFF_VF = OFF_KF + SEQ * HD * 2;
constexpr int OFF_OF = OFF_VF + SEQ * HD * 2;
constexpr int OFF_KM = OFF_OF + SEQ * HD * 2;
constexpr int OFF_VM = OFF_KM + NSEG * NH * NM * DH * 2;
constexpr int FAST = NSEG - 6;
constexpr int LATE = NSEG - 2;
constexpr int OFF_BO = OFF_VM + NSEG * NH * NM * DH * 2;
constexpr int BO_ROW = 4352;
constexpr int OFF_GT = OFF_BO + DM * BO_ROW;
constexpr int OFF_DBG = OFF_GT + SEQ * 4;
constexpr int WS_USED = OFF_DBG + (4 << 20);

struct Args {
    const u16 *x, *mem0, *Wq, *Wk, *Wv, *Wo, *lAr, *Aim, *Cr, *Ci, *ldt, *Dsk, *gw;
    u16* out; char* ws;
    int c1, c2, d0, b0, d1, up, dn, rd, cw, e0;
    unsigned xa, xb;
};

DI float bf2f(u16 b) { return __uint_as_float((unsigned)b << 16); }
DI float bflo(unsigned w) { return __uint_as_float(w << 16); }
DI float bfhi(unsigned w) { return __uint_as_float(w & 0xFFFF0000u); }
DI u16 f2bf(float f) { return __builtin_bit_cast(u16, (__bf16)f); }
DI unsigned pack2(float lo, float hi) {
    typedef __bf16 bf16x2 __attribute__((ext_vector_type(2)));
    bf16x2 v = {(__bf16)lo, (__bf16)hi};
    return __builtin_bit_cast(unsigned, v);
}
#define WG_BARRIER() asm volatile("s_waitcnt lgkmcnt(0)\n\ts_barrier" ::: "memory")
#define VM_DRAIN() asm volatile("s_waitcnt vmcnt(0)" ::: "memory")
DI int cmap(int o) { return (o & 0x1FFF) | ((o >> 13) << 14); }
DI int part_unit(int G) { return cmap((G + (G & ~3)) * 4096); }
DI unsigned mx4(unsigned a, u32x4 v) { a = max(max(a, v.x), v.y); return max(max(a, v.z), v.w); }
DI unsigned now() { return (unsigned)__builtin_amdgcn_s_memrealtime(); }
DI unsigned wait_until(unsigned t) {
    unsigned v; int guard = 8192;
    do { v = now(); } while ((int)(t - v) >= 4 && --guard > 0);
    const int r = __builtin_amdgcn_readfirstlane((int)(t - v));
    asm volatile("s_cmp_lt_i32 %0, 1\n\ts_cbranch_scc1 1f\n\ts_nop 3\n\ts_cmp_lt_i32 %0, 2\n\ts_cbranch_scc1 1f\n\ts_nop 3\n\ts_cmp_lt_i32 %0, 3\n\ts_cbranch_scc1 1f\n\ts_nop 7\n1:" :: "s"(r) : "scc");
    return r > 0 ? t : v + 2;
}
DI void st128(u32x4 v, rsrc_t rs, int off, int soff) {
    __builtin_amdgcn_raw_buffer_store_b128(v, rs, off, soff, 16);
    asm volatile("s_nop 1" :: "v"(v) : "memory");
}
DI void st32(unsigned v, rsrc_t rs, int off, int soff) { __builtin_amdgcn_raw_buffer_store_b32(v, rs, off, soff, 16); }
DI u32x4 ld128(rsrc_t rs, int off, int soff) { return __builtin_amdgcn_raw_buffer_load_b128(rs, off, soff, 16); }
DI u32x2 ld64(rsrc_t rs, int off, int soff) { return __builtin_amdgcn_raw_buffer_load_b64(rs, off, soff, 16); }
DI unsigned ld32(rsrc_t rs, int off, int soff) { return __builtin_amdgcn_raw_buffer_load_b32(rs, off, soff, 16); }
DI f32x2 fold(f32x4 a) { return (f32x2){a[0], a[1]} + (f32x2){a[2], a[3]}; }
DI float dpp_xor1(float v) { return __int_as_float(__builtin_amdgcn_update_dpp(0, __float_as_int(v), 0xB1, 0xf, 0xf, true)); }
DI float red_sum_g(float v) {
    auto a = __builtin_amdgcn_permlane16_swap(__float_as_uint(v), __float_as_uint(v), false, false);
    v = __uint_as_float(a[0]) + __uint_as_float(a[1]);
    auto b = __builtin_amdgcn_permlane32_swap(__float_as_uint(v), __float_as_uint(v), false, false);
    return __uint_as_float(b[0]) + __uint_as_float(b[1]);
}
DI int wave_max32(int d) {
    d = max(d, __builtin_amdgcn_update_dpp(d, d, 0x121, 0xf, 0xf, false));
    d = max(d, __builtin_amdgcn_update_dpp(d, d, 0x122, 0xf, 0xf, false));
    d = max(d, __builtin_amdgcn_update_dpp(d, d, 0x124, 0xf, 0xf, false));
    d = max(d, __builtin_amdgcn_update_dpp(d, d, 0x128, 0xf, 0xf, false));
    return __builtin_amdgcn_readfirstlane(max(max(__builtin_amdgcn_readlane(d, 0), __builtin_amdgcn_readlane(d, 16)), max(__builtin_amdgcn_readlane(d, 32), __builtin_amdgcn_readlane(d, 48))));
}
DI u32x2 tr_read(LAS char* lds, int addr) { return __builtin_bit_cast(u32x2, __builtin_amdgcn_ds_read_tr16_b64_v4i16((LAS s16x4*)(lds + addr))); }
DI void wait_prog(rsrc_t rw, int off, bool act, int thr, int& budget) {
    for (;;) {
        const int v = (int)ld32(rw, off, 0);
        asm volatile("" ::: "memory");
        if (__all(!act || v >= thr)) break;
        if (--budget < 0) break;
        __builtin_amdgcn_s_sleep(1);
    }
}
DI u32x2 launch_tag(const Args& a) {
    unsigned long long z;
    asm volatile("s_load_dwordx2 %0, %1, 0x0\n\ts_waitcnt lgkmcnt(0)" : "=s"(z) : "s"(a.ws + F_EPOCH) : "memory");
    z += 0x9E3779B97F4A7C15ull;
    z = (z ^ (z >> 30)) * 0xBF58476D1CE4E5B9ull; z = (z ^ (z >> 27)) * 0x94D049BB133111EBull; z ^= z >> 31;
    return (u32x2){(unsigned)z, (unsigned)(z >> 32)};
}
DI int wait_init(rsrc_t rw, u32x2 tag, int& budget, int more = 0) {
    const int l = threadIdx.x & 63, o = F_INIT + (l < 32 ? l : l < 62 || !more ? more : l == 63 ? 32 : 161) * 128;
    u32x2 v;
    for (;;) {
        v = ld64(rw, o, 0);
        asm volatile("" ::: "memory");
        if (__all(((v.x ^ tag.x) >> 3) == 0 && v.y == tag.y)) break;
        if (--budget < 0) break;
        __builtin_amdgcn_s_sleep(2);
    }
    return (__builtin_amdgcn_readlane(v.x, 63) & 7) + 8 * (__builtin_amdgcn_readlane(v.x, 62) & 7);
}
DI void glds16(const void* base, int voff, unsigned lds_dst) {
    unsigned keep;
    asm volatile("s_mov_b32 %0, m0\n\ts_mov_b32 m0, %3\n\ts_nop 0\n\tglobal_load_lds_dwordx4 %1, %2\n\ts_mov_b32 m0, %0" : "=&s"(keep) : "v"(voff), "s"(base), "s"(lds_dst) : "memory");
}
DI void bdma(rsrc_t rs, int voff, int soff, unsigned lds_dst) {
    unsigned keep;
    asm volatile("s_mov_b32 %0, m0\n\ts_mov_b32 m0, %4\n\ts_nop 0\n\tbuffer_load_dwordx4 %1, %2, %3 offen sc1 lds\n\ts_mov_b32 m0, %0" : "=&s"(keep) : "v"(voff), "s"(rs), "s"(soff), "s"(lds_dst) : "memory");
}
DI int swz(int r) { return ((r & 3) ^ (((r >> 2) & 3) << 1) ^ (((r >> 4) & 1) << 2)) & 7; }
template <class F> DI u32x4 ring_offs(int w, int lane, F at) {
    u32x4 o;
#pragma unroll
    for (int j = 0; j < 4; ++j) { const int r = 16 * w + 4 * j + (lane >> 4); o[j] = at(r, (lane & 15) ^ (2 * swz(r))) - 1024 * j; }
    return o;
}
DI u32x4 ring_offs(int w, int lane, int ld) { return ring_offs(w, lane, [&](int r, int c) { return r * ld * 2 + c * 16; }); }
DI void ring_issue(const u16* src, u32x4 o, unsigned slot) {
    unsigned keep;
    asm volatile("s_mov_b32 %0, m0\n\ts_mov_b32 m0, %6\n\ts_nop 0\n\tglobal_load_lds_dwordx4 %1, %5\n\tglobal_load_lds_dwordx4 %2, %5 offset:1024\n\t"
                 "global_load_lds_dwordx4 %3, %5 offset:2048\n\tglobal_load_lds_dwordx4 %4, %5 offset:3072\n\ts_mov_b32 m0, %0"
                 : "=&s"(keep) : "v"(o.x), "v"(o.y), "v"(o.z), "v"(o.w), "s"(src), "s"(slot) : "memory");
}
#define PF_FIRST 2
constexpr int L_TOTAL = 163840, L_SINK = L_TOTAL - 256;
DI void touch(const void* base, int voff, unsigned lds_base) {
    unsigned keep;
    asm volatile("s_mov_b32 %0, m0\n\ts_mov_b32 m0, %3\n\ts_nop 0\n\tglobal_load_lds_dword %1, %2\n\ts_mov_b32 m0, %0" : "=&s"(keep) : "v"(voff), "s"(base), "s"(lds_base + L_SINK) : "memory");
}
DI void prefetch(const Args& a, unsigned lds_base, int x, int id, int n) {
    const int lane = threadIdx.x & 63;
    const int h0 = x < 4 ? 8 * x : (0x160E0800 >> (8 * (x - 4))) & 255, lpr = x == 5 ? 12 : x == 7 ? 20 : 16, per = 9 * lpr;
    const int first = PF_FIRST * lpr, end = (x < 4 ? 4 : 2) * per;
    const float rl = __builtin_amdgcn_rcpf((float)lpr);
    for (int i = first + id; i < end; i += n) {
        int m = (i >= per) + (i >= 2 * per) + (i >= 3 * per), j = i - m * per;
        if (x < 4 && m < 3) { const int b = i >> 4; m = b < 20 ? b >> 3 : min(b - 20, 2); j = (b < 16 ? b & 7 : b < 20 ? b - 12 : b < 23 ? 8 : b - 23) * 16 + (i & 15); }
        const int mm = m + (x >= 4);
        const int L = j * 64 + lane, row = (int)((L + 0.5f) * rl);
        touch(mm == 0 ? a.Wq : mm == 1 ? a.Wk : mm == 2 ? a.Wv : a.Wo, m == 3 ? h0 * (DH * DM * 2) + L * 128 : row * (HD * 2) + (L - row * lpr) * 128 + h0 * 256, lds_base);
    }
}
template <int N> DI void ring_wait() { asm volatile("s_waitcnt vmcnt(%0) lgkmcnt(0)\n\ts_barrier" :: "i"(N) : "memory"); }
template <int I> struct ic_t { static constexpr int value = I; };
template <int N, int I = 0, class F> DI void static_for(F&& f) { if constexpr (I < N) { f(ic_t<I>{}); static_for<N, I + 1>(f); } }
#define MFMA32(a, b, c) __builtin_amdgcn_mfma_f32_32x32x16_bf16(__builtin_bit_cast(bf16x8, a), __builtin_bit_cast(bf16x8, b), c, 0, 0, 0)
#define MFMA16(a, b, c) __builtin_amdgcn_mfma_f32_16x16x32_bf16(__builtin_bit_cast(bf16x8, a), __builtin_bit_cast(bf16x8, b), c, 0, 0, 0)

constexpr int S_CH = 9;
constexpr int S_XRS = 4096 + 32;
constexpr int S_XR = 0;
constexpr int S_RC = S_CH * S_XRS;
constexpr int S_RCS = 4192;
constexpr int S_KD = S_RC + 4 * S_RCS;
constexpr int S_K = S_KD + 48 * 128;
constexpr int S_KROW = 96;
constexpr int S_TAB = S_K + 79 * S_KROW;
constexpr int S_TROW = 544;
constexpr int S_RED = S_TAB + 57 * S_TROW;
constexpr int S_TOTAL = S_RED + 4 * 64 * 144;
static_assert(S_RC % 16 == 0 && S_TAB % 16 == 0 && S_RED % 16 == 0, "");
DI f32x2 crot(f32x2 z) { return (f32x2){-z.y, z.x}; }
DI f32x2 cmul(f32x2 a, f32x2 z) { return __builtin_elementwise_fma(a.yy, crot(z), a.xx * z); }
DI f32x2 cexp_p(float ar, float ai, float p) {
    const float x = ai * p;
    const float hi = x * 0.15915494309189535f;
    const float lo = __builtin_fmaf(x, 0.15915494309189535f, -hi) + x * 6.420638316725915e-9f;
    const float rev = (hi - __builtin_rintf(hi)) + lo;
    const float m = __builtin_amdgcn_exp2f(ar * p * 1.4426950408889634f);
    return (f32x2){m * __builtin_amdgcn_cosf(rev), m * __builtin_amdgcn_sinf(rev)};
}
#define FENCE() __builtin_amdgcn_sched_barrier(0)
#define TT(c, i)
DI void role_s4d(const Args& a, LAS char* lds, unsigned lds_base, int task, rsrc_t rw, int& budget) {
    const int tid = threadIdx.x, lane = tid & 63, w = __builtin_amdgcn_readfirstlane(tid >> 6);
    const int d0 = task * S_CH;
    touch(w == 0 ? a.lAr : w == 1 ? a.Aim : w == 2 ? a.Cr : a.Ci, min(d0 * (NS * 2) + min(lane, 2 * S_CH + 1) * 128, DM * NS * 2 - 4), lds_base);
    touch(w & 1 ? a.ldt : a.Dsk, min(lane * 128, DM * 2 - 4), lds_base);
#define S4S(i)
    {
        const rsrc_t rx = __builtin_amdgcn_make_buffer_rsrc((void*)a.x, 0, SEQ * DM * 2, 0x00020000);
        const unsigned sh = (d0 & 1) * 16;
        u32x4 v[8]; unsigned e[8];
#pragma unroll
        for (int i = 0; i < 8; ++i) {
            const int o = (tid + 256 * i) * (DM * 2) + (d0 & ~1) * 2;
            v[i] = __builtin_amdgcn_raw_buffer_load_b128(rx, o, 0, 0); e[i] = __builtin_amdgcn_raw_buffer_load_b32(rx, o, 16, 0);
        }
#pragma unroll
        for (int p = 0; p < 2; ++p) {
            const int tok = 32 * task + 8 * w + 4 * p + (lane >> 4);
            float acc = 0.f;
#pragma unroll
            for (int i = 0; i < 5; ++i) {
                const int ch = (lane & 15) + 16 * i;
                const u32x4 gv = ch < 72 ? *(const u32x4*)(a.gw + ch * 8) : (u32x4){0u, 0u, 0u, 0u};
                const u32x4 xv = *(const u32x4*)(a.x + (size_t)tok * DM + min(ch, 71) * 8);
#pragma unroll
                for (int j = 0; j < 4; ++j) acc += bflo(xv[j]) * bflo(gv[j]) + bfhi(xv[j]) * bfhi(gv[j]);
            }
            acc += __shfl_xor(acc, 1); acc += __shfl_xor(acc, 2); acc += __shfl_xor(acc, 4); acc += __shfl_xor(acc, 8);
            st32(__float_as_uint(1.0f / (1.0f + __expf(-acc))), rw, (lane & 15) == 0 ? OFF_GT + tok * 4 : OOB, 0);
        }
#pragma unroll
        for (int i = 0; i < 8; ++i) {
            const unsigned x5[5] = {__builtin_amdgcn_alignbit(v[i].y, v[i].x, sh), __builtin_amdgcn_alignbit(v[i].z, v[i].y, sh), __builtin_amdgcn_alignbit(v[i].w, v[i].z, sh),
                                    __builtin_amdgcn_alignbit(e[i], v[i].w, sh), e[i] >> sh};
#pragma unroll
            for (int c = 0; c < S_CH; ++c) *(LAS u16*)(lds + S_XR + c * S_XRS + (2047 - tid - 256 * i) * 2) = (u16)(x5[c >> 1] >> (16 * (c & 1)));
        }
    }
    if (tid < 8 * S_CH) *(LAS unsigned*)(lds + S_XR + (tid >> 3) * S_XRS + 4096 + (tid & 7) * 4) = 0;
    if (tid < 64) *(LAS unsigned*)(lds + S_RC + (tid >> 4) * S_RCS + 4096 + (tid & 15) * 4) = 0;
    for (int i = tid; i < 15 * S_KROW / 4; i += 256) *(LAS unsigned*)(lds + S_K + i * 4) = 0;
    int kd[8];
#pragma unroll
    for (int j = 0; j < 8; ++j) {
        const int p = (int)bf2f(f2bf((float)(8 * tid + j))), k = p >> 7, r = p & 7;
        const int row = p < 256 ? 8 * k + r : (p < 512 ? 8 + 4 * k + (r >> 1) : (p < 1024 ? 16 + 2 * k + (r >> 2) : 24 + k));
        kd[j] = S_KD + (row * 16 + ((p >> 3) & 15)) * 8;
    }
    const int n = tid & 127, nn = lane & 15, g = lane >> 4;
    u16 pA = a.lAr[d0 * NS + n], pI = a.Aim[d0 * NS + n], pCr = a.Cr[d0 * NS + n], pCi = a.Ci[d0 * NS + n], pT = a.ldt[d0], pD = a.Dsk[d0];
    WG_BARRIER();
    for (int ch = 0; ch < S_CH; ++ch) {
        S4S(0)
        const float dsk = bf2f(pD);
        {
            const u32x4 v = *(LAS u32x4*)(lds + S_XR + ch * S_XRS + tid * 16);
            const u32x2 e = *(LAS u32x2*)(lds + S_XR + ch * S_XRS + tid * 16 + 16);
            const unsigned s01 = __builtin_amdgcn_alignbit(v.y, v.x, 16), s12 = __builtin_amdgcn_alignbit(v.z, v.y, 16), s23 = __builtin_amdgcn_alignbit(v.w, v.z, 16);
            const unsigned s34 = __builtin_amdgcn_alignbit(e.x, v.w, 16), s45 = __builtin_amdgcn_alignbit(e.y, e.x, 16);
            *(LAS u32x4*)(lds + S_RC + tid * 16) = v;
            *(LAS u32x4*)(lds + S_RC + S_RCS + tid * 16) = (u32x4){s01, s12, s23, s34};
            *(LAS u32x4*)(lds + S_RC + 2 * S_RCS + tid * 16) = (u32x4){v.y, v.z, v.w, e.x};
            *(LAS u32x4*)(lds + S_RC + 3 * S_RCS + tid * 16) = (u32x4){s12, s23, s34, s45};
        }
        S4S(1)
        const float dt = bf2f(f2bf(expf(bf2f(pT))));
        const int sc = 140 - (wave_max32(max(pCr & 0x7FFF, pCi & 0x7FFF)) >> 7);
        {
            const float ar = -bf2f(f2bf(expf(bf2f(pA)))) * dt, ai = bf2f(pI) * dt;
            const f32x2 C = {__builtin_amdgcn_ldexpf(bf2f(pCr), sc), __builtin_amdgcn_ldexpf(bf2f(pCi), sc)};
            const int dn = min(d0 + ch + 1, DM - 1);
            pA = a.lAr[dn * NS + n]; pI = a.Aim[dn * NS + n]; pCr = a.Cr[dn * NS + n]; pCi = a.Ci[dn * NS + n]; pT = a.ldt[dn]; pD = a.Dsk[dn];
            const int to = S_TAB + n * 4;
            auto put = [&](int row, f32x2 v) { *(LAS f16x2*)(lds + to + row * S_TROW) = __builtin_convertvector(v, f16x2); };
            const f32x2 z128 = cexp_p(ar, ai, 128.f);
            if (w < 2) {
                f32x2 E[8], G[4];
                E[1] = cexp_p(ar, ai, 1.f); G[0] = C;
#pragma unroll
                for (int r = 2; r < 8; ++r) E[r] = cmul(E[r >> 1], E[r - (r >> 1)]);
#pragma unroll
                for (int k = 1; k < 4; ++k) G[k] = cmul(G[k - 1], z128);
#pragma unroll
                for (int k = 0; k < 4; ++k)
#pragma unroll
                    for (int r = 0; r < 8; r += 1 + (k >> 1)) put(k < 2 ? 8 * k + r : 8 + 4 * k + (r >> 1), r ? cmul(E[r], G[k]) : G[k]);
            } else {
                const f32x2 z4 = cexp_p(ar, ai, 4.f), z8 = cexp_p(ar, -ai, 8.f);
                f32x2 G = cmul(C, cexp_p(ar, ai, 512.f)), B = z8;
                put(41, (f32x2){1.f, 0.f});
#pragma unroll
                for (int k = 1; k < 16; ++k) {
                    put(41 + k, B); B = cmul(B, z8);
                    if (k < 5) { put(22 + 2 * k, G); put(23 + 2 * k, cmul(G, z4)); }
                    else if (k < 14) put(27 + k, G);
                    G = cmul(G, z128);
                }
            }
        }
        WG_BARRIER();
        S4S(2)
        {
            const int ko = (w & 1) * 256 + g * 16, t0 = w & 2;
            u32x4 av[2][4], bv[4];
#pragma unroll
            for (int j = 0; j < 4; ++j) {
                bv[j] = *(LAS u32x4*)(lds + S_TAB + (41 + nn) * S_TROW + ko + j * 64);
                av[0][j] = *(LAS u32x4*)(lds + S_TAB + min(16 * t0 + nn, 40) * S_TROW + ko + j * 64);
                av[1][j] = *(LAS u32x4*)(lds + S_TAB + (16 + nn) * S_TROW + ko + j * 64);
            }
            f32x4 acc[2] = {};
#pragma unroll
            for (int j = 0; j < 8; ++j) acc[j & 1] = __builtin_amdgcn_mfma_f32_16x16x32_f16(__builtin_bit_cast(f16x8, av[j & 1][j >> 1]), __builtin_bit_cast(f16x8, bv[j >> 1]), acc[j & 1], 0, 0, 0);
#pragma unroll
            for (int r = 0; r < 8; ++r) *(LAS float*)(lds + S_KD + (((r < 4 ? 16 * t0 : 16) + 4 * g + (r & 3)) * 16 + nn) * 8 + (w & 1) * 4) = __builtin_amdgcn_ldexpf(acc[r >> 2][r & 3] * dt, 1 - sc);
        }
        WG_BARRIER();
        S4S(3)
        {
            float kv[8];
#pragma unroll
            for (int j = 0; j < 8; ++j) { const f32x2 t = *(LAS f32x2*)(lds + kd[j]); kv[j] = t.x + t.y; }
            *(LAS u32x4*)(lds + S_K + (15 + (tid >> 2)) * S_KROW + (tid & 3) * 16) = (u32x4){pack2(kv[0], kv[1]), pack2(kv[2], kv[3]), pack2(kv[4], kv[5]), pack2(kv[6], kv[7])};
        }
        WG_BARRIER();
        S4S(4)
        {
            const int st0 = 2047 - nn + 8 * g - 32 * w, cc = st0 & 3;
            const int ub = S_RC + cc * S_RCS + (st0 - cc) * 2 - 3872, kb = S_K + (3 + nn - w) * S_KROW + g * 16;
            f32x4 y[4][2] = {};
            u32x2 U[2][16]; u32x4 F[2][4];
            auto rd = [&](int i, int f) {
                if (f < 4) F[i & 1][f] = *(LAS u32x4*)(lds + kb + (12 - 4 * i + 16 * f) * S_KROW);
                else U[i & 1][f - 4] = *(LAS u32x2*)(lds + ub + 3872 - 256 * i - 1024 * ((f - 4) >> 2) - 32 * ((f >> 1) & 1) + 8 * (f & 1));
            };
#pragma unroll
            for (int f = 0; f < 20; ++f) { rd(0, f); FENCE(); }
#pragma unroll
            for (int i = 0; i < 4; ++i) {
                int c = 0;
#pragma unroll
                for (int q = 0; q < 4; ++q)
#pragma unroll
                    for (int T = q; T < 4; ++T)
#pragma unroll
                        for (int bt = 0; bt < 2; ++bt) {
                            y[T][bt] = MFMA16(__builtin_shufflevector(U[i & 1][4 * q + 2 * bt], U[i & 1][4 * q + 2 * bt + 1], 0, 1, 2, 3), F[i & 1][T - q], y[T][bt]);
                            if (i < 3) rd(i + 1, c);
                            ++c;
                            FENCE();
                        }
            }
#pragma unroll
            for (int T = 0; T < 4; ++T)
#pragma unroll
                for (int bt = 0; bt < 2; ++bt) *(LAS f32x4*)(lds + S_RED + (w * 64 + 16 * T + nn) * 144 + bt * 64 + g * 16) = y[T][bt];
        }
        WG_BARRIER();
        S4S(5)
        {
            f32x4 y0 = {0, 0, 0, 0}, y1 = y0;
#pragma unroll
            for (int ww = 0; ww < 4; ++ww) {
                const int o = S_RED + (ww * 64 + (tid >> 2)) * 144 + (tid & 3) * 32;
                y0 += *(LAS f32x4*)(lds + o); y1 += *(LAS f32x4*)(lds + o + 16);
            }
            const u32x4 u = *(LAS u32x4*)(lds + S_XR + ch * S_XRS + (255 - tid) * 16);
            st128((u32x4){pack2(y0[0] + bfhi(u.w) * dsk, y0[1] + bflo(u.w) * dsk), pack2(y0[2] + bfhi(u.z) * dsk, y0[3] + bflo(u.z) * dsk),
                          pack2(y1[0] + bfhi(u.y) * dsk, y1[1] + bflo(u.y) * dsk), pack2(y1[2] + bfhi(u.x) * dsk, y1[3] + bflo(u.x) * dsk)}, rw, tid * 16, OFF_BO + (d0 + ch) * BO_ROW);
        }
        S4S(6)
    }
    wait_init(rw, launch_tag(a), budget);
    VM_DRAIN();
    WG_BARRIER();
    if (tid == 0) st32(1u, rw, F_S4 + task * 4, 0);
}

constexpr int XT_BYTES = 32 * 1152;
constexpr int P_XT = 0;
constexpr int P_STG = 2 * XT_BYTES;
struct WSlice { u32x4 f[36]; };
constexpr int P_RING = 5;
template <class F> DI void load_wslices(WSlice& W0, WSlice& W1, const u16* s0, const u16* s1, int w, int lane, LAS char* lds, unsigned lds_base, F also) {
    constexpr int NB = 18;
    const int r0 = 8 * (lane >> 5) + ((lane & 15) >> 2);
    const int cb = (4 * w + 2 * ((lane >> 4) & 1) + ((lane & 3) >> 1)), lo8 = (lane & 1) * 8;
    auto src = [&](int i) { return (i < 9 ? s0 : s1) + (size_t)(64 * (i % 9)) * HD; };
    const u32x4 ro = ring_offs(w, lane, HD);
#pragma unroll
    for (int i = 0; i < P_RING - 1; ++i) ring_issue(src(i), ro, lds_base + P_STG + i * 16384 + w * 4096);
    also();
    static_for<NB>([&](auto ic) {
        constexpr int i = decltype(ic)::value, rem = NB - 1 - i < P_RING - 2 ? NB - 1 - i : P_RING - 2;
        ring_wait<4 * rem>();
        if constexpr (i + P_RING - 1 < NB) ring_issue(src(i + P_RING - 1), ro, lds_base + P_STG + ((i + P_RING - 1) % P_RING) * 16384 + w * 4096);
        const int sl = P_STG + (i % P_RING) * 16384;
#pragma unroll
        for (int kk = 0; kk < 4; ++kk) {
            const int ra = r0 + 16 * kk, rb = ra + 4;
            const u32x2 lo = tr_read(lds, sl + ra * 256 + ((cb ^ (2 * swz(ra))) * 16) + lo8), hi = tr_read(lds, sl + rb * 256 + ((cb ^ (2 * swz(rb))) * 16) + lo8);
            (i < 9 ? W0 : W1).f[(i % 9) * 4 + kk] = (u32x4){lo.x, lo.y, hi.x, hi.y};
        }
        u32x4* f = (i < 9 ? W0 : W1).f + (i % 9) * 4;
        if constexpr (i < 16) asm volatile("" : "+a"(f[0]), "+a"(f[1]), "+a"(f[2]), "+a"(f[3]));
    });
}
DI u32x4 piece_pi(const f32x16& a, int qq) {
    return (u32x4){pack2(a[4 * qq], a[4 * qq + 8]), pack2(a[4 * qq + 1], a[4 * qq + 9]), pack2(a[4 * qq + 2], a[4 * qq + 10]), pack2(a[4 * qq + 3], a[4 * qq + 11])};
}
DI u32x4 piece_seq(const f32x16& a, int s2) {
    return (u32x4){pack2(a[8 * s2], a[8 * s2 + 1]), pack2(a[8 * s2 + 2], a[8 * s2 + 3]), pack2(a[8 * s2 + 4], a[8 * s2 + 5]), pack2(a[8 * s2 + 6], a[8 * s2 + 7])};
}
struct XTile {
    int xs[9], xa[4];
    DI void init(int w, int lane) {
#pragma unroll
        for (int i = 0; i < 9; ++i) { const int L = (i * 4 + w) * 64 + lane, r = L / 72, cs = L - 72 * r; xs[i] = r * 1152 + (cs ^ ((r >> 1) & 7)) * 16; }
        const int c32 = lane & 31, hf = lane >> 5;
#pragma unroll
        for (int j = 0; j < 4; ++j) xa[j] = P_XT + c32 * 1152 + (((2 * j + hf) ^ ((c32 >> 1) & 7)) * 16);
    }
    DI void dma(const Args& a, unsigned lds_base, int w, int s, int buf, int i) const {
        glds16((const char*)a.x + (size_t)min(s, NSEG - 1) * XT_BYTES, xs[i], lds_base + P_XT + buf * XT_BYTES + (i * 4 + w) * 1024);
    }
    DI void first(const Args& a, unsigned lds_base, int w, int lane, int s) {
        init(w, lane);
#pragma unroll
        for (int i = 0; i < 9; ++i) dma(a, lds_base, w, s, 0, i);
    }
};
template <class F> DI void project(f32x16& acc0, f32x16& acc1, const WSlice& W0, const WSlice& W1, const XTile& X, const Args& a, LAS char* lds, unsigned lds_base, int w, int buf, int sn, F gap) {
    const int bo = buf * XT_BYTES;
    constexpr int PD = 5;
    u32x4 xf[PD];
#pragma unroll
    for (int i = 0; i < PD; ++i) xf[i] = *(LAS u32x4*)(lds + X.xa[i & 3] + bo + (i >> 2) * 128);
    static_for<36>([&](auto ic) {
        constexpr int ks = decltype(ic)::value;
        if (ks == 0) asm volatile("v_mfma_f32_32x32x16_bf16 %0, %1, %2, 0" : "=&v"(acc0) : "a"(W0.f[0]), "v"(xf[0]) : "memory");
        else asm volatile("v_mfma_f32_32x32x16_bf16 %0, %1, %2, %0" : "+v"(acc0) : "a"(W0.f[ks]), "v"(xf[ks % PD]) : "memory");
        if (ks < 9) X.dma(a, lds_base, w, sn, buf ^ 1, ks);
        gap(ic_t<2 * ks>{});
        if (ks == 0) asm volatile("v_mfma_f32_32x32x16_bf16 %0, %1, %2, 0" : "=&v"(acc1) : "a"(W1.f[0]), "v"(xf[0]) : "memory");
        else if (ks < 28) asm volatile("v_mfma_f32_32x32x16_bf16 %0, %1, %2, %0" : "+v"(acc1) : "a"(W1.f[ks]), "v"(xf[ks % PD]) : "memory");
        else asm volatile("v_mfma_f32_32x32x16_bf16 %0, %1, %2, %0" : "+v"(acc1) : "v"(W1.f[ks]), "v"(xf[ks % PD]) : "memory");
        if (ks + PD < 36) xf[ks % PD] = *(LAS u32x4*)(lds + X.xa[(ks + PD) & 3] + bo + ((ks + PD) >> 2) * 128);
        gap(ic_t<2 * ks + 1>{});
    });
    asm volatile("s_nop 7\n\ts_nop 3" : "+v"(acc0), "+v"(acc1) :: "memory");
}
#define KV_T 7
#define KV_F 22
DI void role_kv(const Args& a, LAS char* lds, unsigned lds_base, int h, rsrc_t rw, int& budget) {
    const int tid = threadIdx.x, lane = tid & 63, w = __builtin_amdgcn_readfirstlane(tid >> 6);
    WSlice W0, W1;
    XTile X;
    u32x2 tag;
    load_wslices(W0, W1, a.Wk + h * DH, a.Wv + h * DH, w, lane, lds, lds_base, [&]() { X.first(a, lds_base, w, lane, 0); tag = launch_tag(a); });
    VM_DRAIN();
    WG_BARRIER();
    const int oo = h * 8192 + w * 2048 + lane * 16;
    const int fo = lane == 0 ? F_KV + h * 512 + w * 128 : OOB;
    u32x4 xf[6], P[4] = {};
    auto frag = [&](int buf, int ks) { xf[ks % 6] = *(LAS u32x4*)(lds + X.xa[ks & 3] + buf * XT_BYTES + (ks >> 2) * 128); };
    auto cvt = [&](const f32x16& k, const f32x16& v, int i) {
#pragma unroll
        for (int j = 0; j < 4; ++j) asm volatile("v_cvt_pk_bf16_f32 %0, %1, %2" : "=v"(P[i][j]) : "v"(i < 2 ? k[4 * i + j] : v[8 * i - 16 + 2 * j]), "v"(i < 2 ? k[4 * i + j + 8] : v[8 * i - 15 + 2 * j]));
    };
    auto put = [&](int o, int s, int i) { st128(P[i], rw, o + (i & 1) * 1024, (i < 2 ? OFF_KF : OFF_VF) + s * (NH * 8192)); };
    auto now_ = [&](f32x16& k, f32x16& v, int s) {
        asm volatile("s_nop 7\n\ts_nop 3" : "+v"(k), "+v"(v) :: "memory");
#pragma unroll
        for (int i = 0; i < 4; ++i) { cvt(k, v, i); put(oo, s, i); }
    };
    u32x2 in = {};
    auto seg = [&](f32x16& k, f32x16& v, const f32x16& pk, const f32x16& pv, int s, int buf) {
        const int o = s > 1 ? oo : OOB;
#pragma unroll
        for (int ks = 0; ks < 36; ++ks) {
            if (ks == 0) asm volatile("v_mfma_f32_32x32x16_bf16 %0, %1, %2, 0" : "=&v"(k) : "a"(W0.f[0]), "v"(xf[0]) : "memory");
            else asm volatile("v_mfma_f32_32x32x16_bf16 %0, %1, %2, %0" : "+v"(k) : "a"(W0.f[ks]), "v"(xf[ks % 6]) : "memory");
            if (ks >= 1 && ks < 5) cvt(pk, pv, ks - 1);
            if (buf && ks == KV_T && s == 1) {
                asm volatile("s_waitcnt vmcnt(%0)" :: "i"(4 + (KV_T - 4) / 2) : "memory");
                __builtin_amdgcn_raw_buffer_store_b64(tag, rw, lane == 0 ? F_INIT + (33 + 4 * h + w) * 128 : OOB, 0, 16);
                if (!__all(((in.x ^ tag.x) >> 1) == 0 && in.y == tag.y)) wait_init(rw, tag, budget);
            }
            if (ks >= 5 && ks < 13 && (ks & 1)) put(o, s - 1, (ks - 5) >> 1);
            if (ks == KV_F) { VM_DRAIN(); st32((unsigned)(s - 1), rw, s > 1 ? fo : OOB, 0); }
            if (ks == 0) asm volatile("v_mfma_f32_32x32x16_bf16 %0, %2, %1, 0" : "=&v"(v) : "a"(W1.f[0]), "v"(xf[0]) : "memory");
            else if (ks < 28) asm volatile("v_mfma_f32_32x32x16_bf16 %0, %2, %1, %0" : "+v"(v) : "a"(W1.f[ks]), "v"(xf[ks % 6]) : "memory");
            else asm volatile("v_mfma_f32_32x32x16_bf16 %0, %2, %1, %0" : "+v"(v) : "v"(W1.f[ks]), "v"(xf[ks % 6]) : "memory");
            if (ks == 30) WG_BARRIER();
            frag(buf ^ (ks >= 30), (ks + 6) % 36);
            if (ks < 4) X.dma(a, lds_base, w, s + 1, buf ^ 1, ks + 5);
            if (ks > 30) X.dma(a, lds_base, w, s + 2, buf, ks - 31);
        }
    };
#pragma unroll
    for (int i = 0; i < 6; ++i) frag(0, i);
#pragma unroll
    for (int i = 0; i < 5; ++i) X.dma(a, lds_base, w, 1, 1, i);
    f32x16 k0, v0, k1 = {}, v1 = {};
    for (int s = 0; s < NSEG; s += 2) {
        seg(k0, v0, k1, v1, s, 0);
        if (s == 0) { in = ld64(rw, F_INIT + (lane & 31) * 128, 0); now_(k0, v0, 0); }
        seg(k1, v1, k0, v0, s + 1, 1);
    }
    now_(k1, v1, NSEG - 1);
    wait_prog(rw, F_S4 + lane * 4, true, 1, budget);
    VM_DRAIN();
    st32(NSEG, rw, fo, 0);
}

DI int prog_min(rsrc_t rw, int off) {
    const int v = (int)ld32(rw, off, 0);
    asm volatile("" ::: "memory");
    return -wave_max32(-v);
}
DI float red_max_g(float v) {
    auto a = __builtin_amdgcn_permlane16_swap(__float_as_uint(v), __float_as_uint(v), false, false);
    v = fmaxf(__uint_as_float(a[0]), __uint_as_float(a[1]));
    auto b = __builtin_amdgcn_permlane32_swap(__float_as_uint(v), __float_as_uint(v), false, false);
    return fmaxf(__uint_as_float(b[0]), __uint_as_float(b[1]));
}
#define M16 "v_mfma_f32_16x16x32_bf16 "
DI void role_qa(const Args& a, LAS char* lds, unsigned lds_base, int h0, int s0, int s1, rsrc_t rw, int& budget) {
    const int tid = threadIdx.x, lane = tid & 63, w = __builtin_amdgcn_readfirstlane(tid >> 6);
    const int n = lane & 15, g = lane >> 4, h = h0 + (w >> 1), tt = w & 1;
    WSlice W0, W1;
    XTile X;
    load_wslices(W0, W1, a.Wq + h0 * DH, a.Wq + (h0 + 1) * DH, w, lane, lds, lds_base, [&]() { X.first(a, lds_base, w, lane, s0); });
    const int oo = OFF_OF + (2 * tt + (n >> 3)) * 65536 + h * 2048 + g * 128 + (n & 7) * 16;
#pragma unroll
    for (int t = 0; t < 4; ++t) st128((u32x4){SENT, SENT, SENT, SENT}, rw, oo + t * 512, s1 * 262144);
    VM_DRAIN();
    WG_BARRIER();
    constexpr int Q_Q = P_STG, Q_KV = Q_Q + 16384, Q_M = Q_KV + 32768;
    static_assert(Q_M + 16384 <= P_STG + P_RING * 16384, "");
    const int fk = F_KV + h * 512 + (lane & 3) * 128, fm = F_KVM + h * 256 + (lane & 1) * 128;
    int pm_, m_rdy = -1, kv_rdy = -1;
    auto look = [&]() { pm_ = (int)ld32(rw, lane & 4 ? fk : fm, 0); };
    auto seen = [&]() { auto rl = [&](int i) { return __builtin_amdgcn_readlane(pm_, i); }; m_rdy = min(rl(0), rl(1)); kv_rdy = min(min(rl(4), rl(5)), min(rl(6), rl(7))); };
    auto wait_m = [&](int thr, unsigned& t) {
        for (;;) {
            const u32x2 v = ld64(rw, fm, 0);
            asm volatile("" ::: "memory");
            t = now();
            const int p0 = __builtin_amdgcn_readlane(v.x, 0), p1 = __builtin_amdgcn_readlane(v.x, 1);
            const int d0 = (int)(__builtin_amdgcn_readlane(v.y, 0) - 8 * t) >> 3, d1 = (int)(__builtin_amdgcn_readlane(v.y, 1) - 8 * t) >> 3;
            if (min(p0, p1) >= thr || --budget < 0) return max(p0 == thr ? d0 : -1000, p1 == thr ? d1 : -1000);
            (void)wait_until(t + min(max(min(d0, d1) + (int)(a.xa >> 16 & 255), 10), 200));
        }
    };
    auto proj = [&](int s, auto gap) {
        f32x16 acc0, acc1;
        project(acc0, acc1, W0, W1, X, a, lds, lds_base, w, ((s - s0) >> 1) & 1, s + 2, [&](auto ic) { gap(ic); if (decltype(ic)::value == 55) look(); });
        if (s >= LATE) {
            const int o = OFF_OF + h0 * 8192 + w * 2048 + lane * 16;
            st128(piece_pi(acc0, 0), rw, o, s * 262144); st128(piece_pi(acc0, 1), rw, o + 1024, s * 262144);
            st128(piece_pi(acc1, 0), rw, o + 8192, s * 262144); st128(piece_pi(acc1, 1), rw, o + 8192 + 1024, s * 262144);
            return;
        }
        const int o = Q_Q + w * 2048 + lane * 16;
        *(LAS u32x4*)(lds + o) = piece_pi(acc0, 0); *(LAS u32x4*)(lds + o + 1024) = piece_pi(acc0, 1);
        *(LAS u32x4*)(lds + o + 8192) = piece_pi(acc1, 0); *(LAS u32x4*)(lds + o + 8192 + 1024) = piece_pi(acc1, 1);
    };
    const int fs = (tt ? OFF_VF : OFF_KF) + (g >> 1) * 1024 + ((g & 1) * 32 + 16 * (n >> 3) + (n & 7)) * 16;
    auto fetch = [&](int s) {
        const int so = (min(s, NSEG - 1) * NH + h) * 8192;
#pragma unroll
        for (int f = 0; f < 8; ++f) bdma(rw, fs, so + (f >> 1) * 2048 + (f & 1) * 128, lds_base + Q_KV + w * 8192 + f * 1024);
    };
    const int ms = OFF_KM + (lane & 7) * 256 + (lane >> 3) * 16;
    auto fetch_m = [&](int s) {
        const int so = (s * NH + h) * 2048;
        const unsigned d = lds_base + Q_M + w * 4096;
        bdma(rw, ms, so, d); bdma(rw, ms, so + 128, d + 1024); bdma(rw, lane * 16, OFF_VM + so, d + 2048); bdma(rw, lane * 16, OFF_VM + so + 1024, d + 3072);
    };
    const float c = 0.08838834764831845f * 1.4426950408889634f;
    u32x4 qf[4];
    f32x4 o[8]; float mx, l;
    auto xpart = [&]() {
        const int kb = Q_KV + (w >> 1) * 16384 + lane * 16;
        u32x4 kn[8], vn[8];
#pragma unroll
        for (int ks = 0; ks < 4; ++ks) qf[ks] = *(LAS u32x4*)(lds + Q_Q + (w >> 1) * 8192 + (2 * ks + (g >> 1)) * 1024 + ((g & 1) * 32 + 16 * tt + n) * 16);
#pragma unroll
        for (int f = 0; f < 8; ++f) kn[f] = *(LAS u32x4*)(lds + kb + f * 1024);
        FENCE();
#pragma unroll
        for (int f = 0; f < 8; ++f) vn[f] = *(LAS u32x4*)(lds + kb + 8192 + f * 1024);
        f32x4 s0, s1;
        asm volatile(M16 "%0, %2, %10, 0\n\t" M16 "%1, %3, %10, 0\n\t" M16 "%0, %4, %11, %0\n\t" M16 "%1, %5, %11, %1\n\t"
                     M16 "%0, %6, %12, %0\n\t" M16 "%1, %7, %12, %1\n\t" M16 "%0, %8, %13, %0\n\t" M16 "%1, %9, %13, %1\n\ts_nop 7"
                     : "=&v"(s0), "=&v"(s1) : "v"(kn[0]), "v"(kn[1]), "v"(kn[2]), "v"(kn[3]), "v"(kn[4]), "v"(kn[5]), "v"(kn[6]), "v"(kn[7]), "v"(qf[0]), "v"(qf[1]), "v"(qf[2]), "v"(qf[3]));
        float p[8];
        mx = -3.0e38f; l = 0.f;
#pragma unroll
        for (int r = 0; r < 8; ++r) { const int key = 16 * (g >> 1) + 4 * (g & 1) + (r & 3) + 8 * (r >> 2); p[r] = key <= 16 * tt + n ? (r < 4 ? s0[r & 3] : s1[r & 3]) : -3.0e38f; mx = fmaxf(mx, p[r]); }
        mx = red_max_g(mx);
#pragma unroll
        for (int r = 0; r < 8; ++r) { p[r] = __builtin_amdgcn_exp2f((p[r] - mx) * c); l += p[r]; }
        const u32x4 pf = {pack2(p[0], p[1]), pack2(p[2], p[3]), pack2(p[4], p[5]), pack2(p[6], p[7])};
        asm volatile("s_nop 1\n\t" M16 "%0, %8, %16, 0\n\t" M16 "%1, %9, %16, 0\n\t" M16 "%2, %10, %16, 0\n\t" M16 "%3, %11, %16, 0\n\t"
                     M16 "%4, %12, %16, 0\n\t" M16 "%5, %13, %16, 0\n\t" M16 "%6, %14, %16, 0\n\t" M16 "%7, %15, %16, 0\n\ts_nop 7"
                     : "=&v"(o[0]), "=&v"(o[1]), "=&v"(o[2]), "=&v"(o[3]), "=&v"(o[4]), "=&v"(o[5]), "=&v"(o[6]), "=&v"(o[7])
                     : "v"(vn[0]), "v"(vn[1]), "v"(vn[2]), "v"(vn[3]), "v"(vn[4]), "v"(vn[5]), "v"(vn[6]), "v"(vn[7]), "v"(pf));
    };
    const int fo = lane == 0 ? F_O + h * 128 + (2 * s0 + tt) * 4 : OOB;
    bool pre = false;
    auto both = [&](int s) {
        const int kb = Q_KV + (w >> 1) * 16384 + lane * 16;
        u32x4 kn[8], vn[8], pf;
        f32x4 sc[2];
        float p[8];
        int v0, v1, v2;
        asm volatile("buffer_load_dword %0, %1, %2, 0 offen sc1" : "=v"(pm_) : "v"(lane & 4 ? fk : fm), "s"(rw) : "memory");
#define PIN(...) asm volatile("" : __VA_ARGS__)
        proj(s + 4, [&](auto ic) {
            constexpr int i = decltype(ic)::value;
            if constexpr (i < 8) {
                if (!(i & 1)) qf[i >> 1] = *(LAS u32x4*)(lds + Q_Q + (w >> 1) * 8192 + (i + (g >> 1)) * 1024 + ((g & 1) * 32 + 16 * tt + n) * 16);
                kn[i] = *(LAS u32x4*)(lds + kb + i * 1024);
            }
            if constexpr (i >= 16 && i < 32 && !(i & 1)) vn[(i - 16) >> 1] = *(LAS u32x4*)(lds + kb + 8192 + (i - 16) * 512);
            if constexpr (i == 5 || i == 6) asm volatile(M16 "%0, %1, %2, 0" : "=&v"(sc[i - 5]) : "v"(kn[i - 5]), "v"(qf[0]));
            if constexpr (i > 6 && i < 13) asm volatile(M16 "%0, %1, %2, %0" : "+v"(sc[(i - 5) & 1]) : "v"(kn[i - 5]), "v"(qf[(i - 5) >> 1]));
            if constexpr (i == 15 || i == 16) {
                constexpr int r = 4 * (i - 15);
                PIN("+v"(sc[i - 15]));
#pragma unroll
                for (int j = 0; j < 4; ++j) p[r + j] = 16 * (g >> 1) + 4 * (g & 1) + j + 2 * r <= 16 * tt + n ? sc[i - 15][j] : -3.0e38f;
                PIN("+v"(p[r]), "+v"(p[r + 1]), "+v"(p[r + 2]), "+v"(p[r + 3]));
            }
            if constexpr (i == 17) { mx = fmaxf(fmaxf(fmaxf(fmaxf(p[0], p[1]), p[2]), fmaxf(fmaxf(p[3], p[4]), p[5])), fmaxf(p[6], p[7])); PIN("+v"(mx)); }
            if constexpr (i == 18) { auto x = __builtin_amdgcn_permlane16_swap(__float_as_uint(mx), __float_as_uint(mx), false, false); mx = fmaxf(__uint_as_float(x[0]), __uint_as_float(x[1])); PIN("+v"(mx)); }
            if constexpr (i == 19) { auto x = __builtin_amdgcn_permlane32_swap(__float_as_uint(mx), __float_as_uint(mx), false, false); mx = fmaxf(__uint_as_float(x[0]), __uint_as_float(x[1])); PIN("+v"(mx)); }
            if constexpr (i >= 20 && i < 24) {
                constexpr int r = 2 * (i - 20);
                PIN("+v"(p[r]), "+v"(p[r + 1]));
                p[r] = __builtin_amdgcn_exp2f((p[r] - mx) * c); p[r + 1] = __builtin_amdgcn_exp2f((p[r + 1] - mx) * c);
                PIN("+v"(p[r]), "+v"(p[r + 1]));
            }
            if constexpr (i == 24) { pf = (u32x4){pack2(p[0], p[1]), pack2(p[2], p[3]), pack2(p[4], p[5]), pack2(p[6], p[7])}; PIN("+v"(pf)); }
            if constexpr (i == 25) { l = ((p[0] + p[1]) + p[2]) + p[3]; PIN("+v"(l)); }
            if constexpr (i == 26) { l = (((l + p[4]) + p[5]) + p[6]) + p[7]; PIN("+v"(l)); }
            if constexpr (i == 27) { asm volatile("s_waitcnt vmcnt(9)" : "+v"(pm_) :: "memory"); st32((unsigned)s, rw, fo, 0); }
            if constexpr (i == 29) { seen(); pre = m_rdy >= s + 2 && kv_rdy >= s + 4; }
            if constexpr (i == 31) { v0 = pre ? ms : OOB; v1 = pre ? lane * 16 : OOB; v2 = pre ? fs : OOB; PIN("+v"(v0), "+v"(v1), "+v"(v2)); }
            if constexpr (i >= 28 && i < 36) asm volatile(M16 "%0, %1, %2, 0" : "=&v"(o[i - 28]) : "v"(vn[i - 28]), "v"(pf));
            if constexpr (i == 36) asm volatile("s_barrier" ::: "memory");
            if constexpr (i >= 38 && i < 42) bdma(rw, i < 40 ? v0 : v1, ((s + 2) * NH + h) * 2048 + (i < 40 ? (i & 1) * 128 : OFF_VM + (i & 1) * 1024), lds_base + Q_M + w * 4096 + (i - 38) * 1024);
            if constexpr (i >= 42 && i < 50) bdma(rw, v2, ((s + 4) * NH + h) * 8192 + ((i - 42) >> 1) * 2048 + (i & 1) * 128, lds_base + Q_KV + w * 8192 + (i - 42) * 1024);
        });
    };
    auto none = [](auto) {};
    proj(s0, none);
    if (s0 < FAST) wait_init(rw, launch_tag(a), budget);
    wait_prog(rw, fk, true, s0, budget);
    fetch(s0);
    VM_DRAIN();
    WG_BARRIER();
    xpart();
    WG_BARRIER();
    proj(s0 + 2, none);
    for (int s = s0; s <= s1; s += 2) {
        S4S(0)
        const int mb = Q_M + w * 4096;
        u32x4 kf[4]; u32x2 vm[4];
        auto frags = [&]() {
#pragma unroll
            for (int ks = 0; ks < 4; ++ks) kf[ks] = *(LAS u32x4*)(lds + mb + (4 * ks + g) * 128 + (2 * (n >> 2) + (n & 1)) * 16);
#pragma unroll
            for (int t = 0; t < 4; ++t) { const int vo = mb + 2048 + t * 512 + (n >> 3) * 256 + g * 64 + (n & 7) * 4; vm[t] = (u32x2){*(LAS unsigned*)(lds + vo), *(LAS unsigned*)(lds + vo + 32)}; }
        };
        if (s == s1) {
            TT(true, 3)
            unsigned t;
            const int d = min(wait_m(s - 1, t) + (int)(a.xa & 255), 500);
            st32((t + max(d, 0) + (a.xb >> 8 * ((s - FAST) >> 1) & 255)) & 0x7FFFFFFFu, rw, lane == 0 ? F_H + ((2 * (s - FAST) + tt) * NH + h) * 128 : OOB, 0);
            (void)wait_until(t + d);
            TT(true, 0)
            for (;;) {
                fetch_m(s);
                VM_DRAIN();
                frags();
                unsigned c = 0;
#pragma unroll
                for (int i = 0; i < 4; ++i) c = max(max(mx4(c, kf[i]), vm[i].x), vm[i].y);
                if (!__any(c == SENT) || --budget < 0) break;
            }
        } else {
            if (!pre) {
                seen();
                unsigned t;
                if (m_rdy < s) { const int d = wait_m(s, t); (void)wait_until(t + min(d + (int)(a.xa >> 24), 300)); }
                fetch_m(s);
                if (kv_rdy < s + 2) wait_prog(rw, fk, true, s + 2, budget);
                fetch(s + 2);
            }
            S4S(1)
            asm volatile("s_waitcnt vmcnt(8)" ::: "memory");
            frags();
        }
        TT(s == s1, 1)
        S4S(2)
        f32x4 sm;
        asm volatile(M16 "%0, %1, %5, 0\n\t" M16 "%0, %2, %6, %0\n\t" M16 "%0, %3, %7, %0\n\t" M16 "%0, %4, %8, %0\n\ts_nop 7"
                     : "=&v"(sm) : "v"(kf[0]), "v"(kf[1]), "v"(kf[2]), "v"(kf[3]), "v"(qf[0]), "v"(qf[1]), "v"(qf[2]), "v"(qf[3]));
        const float m2 = red_max_g(fmaxf(mx, fmaxf(sm[0], sm[1])));
        const float al = __builtin_amdgcn_exp2f((mx - m2) * c), p0 = __builtin_amdgcn_exp2f((sm[0] - m2) * c), p1 = __builtin_amdgcn_exp2f((sm[1] - m2) * c);
        const float inv = __builtin_amdgcn_rcpf(red_sum_g(l * al + p0 + p1)), ai = al * inv;
#pragma unroll
        for (int t = 0; t < 8; ++t) o[t] *= ai;
        const unsigned pm = pack2(p0 * inv, p1 * inv);
#define M1K "v_mfma_f32_16x16x16_bf16 "
        asm volatile("s_nop 1\n\t" M1K "%0, %8, %12, %0\n\t" M1K "%1, %8, %13, %1\n\t" M1K "%2, %9, %12, %2\n\t" M1K "%3, %9, %13, %3\n\t"
                     M1K "%4, %10, %12, %4\n\t" M1K "%5, %10, %13, %5\n\t" M1K "%6, %11, %12, %6\n\t" M1K "%7, %11, %13, %7\n\ts_nop 7"
                     : "+v"(o[0]), "+v"(o[1]), "+v"(o[2]), "+v"(o[3]), "+v"(o[4]), "+v"(o[5]), "+v"(o[6]), "+v"(o[7])
                     : "v"(vm[0]), "v"(vm[1]), "v"(vm[2]), "v"(vm[3]), "v"((u32x2){pm, 0u}), "v"((u32x2){0u, pm}));
#pragma unroll
        for (int b = 0; b < 2; ++b) {
            u32x4 x, y;
#pragma unroll
            for (int j = 0; j < 4; ++j) { x[j] = pack2(o[4 * b + (j >> 1)][2 * (j & 1)], o[4 * b + (j >> 1)][2 * (j & 1) + 1]); y[j] = pack2(o[4 * b + 2 + (j >> 1)][2 * (j & 1)], o[4 * b + 2 + (j >> 1)][2 * (j & 1) + 1]); }
            if (s == LATE - 1) { const u32x4 z = {x.x, x.z, y.x, y.z}; y = (u32x4){x.y, x.w, y.y, y.w}; x = z; }
            st128(x, rw, oo + b * 1024, s * 262144); st128(y, rw, oo + b * 1024 + 512, s * 262144);
        }
        TT(s == s1, 2)
        S4S(3)
        if (s == s1) break;
        asm volatile("s_waitcnt vmcnt(4)" ::: "memory");
        WG_BARRIER();
        S4S(4)
        if (s + 4 <= s1) { both(s); S4S(5) continue; }
        xpart();
        FENCE();
        VM_DRAIN();
        st32((unsigned)s, rw, fo, 0);
        TT(true, 4)
    }
}

#define STEP_ASM \
    "v_readfirstlane_b32 s36, v0\n" \
    "v_readfirstlane_b32 s37, v1\n" \
    "v_readfirstlane_b32 s38, v2\n" \
    "v_readfirstlane_b32 s39, v3\n" \
    "v_readfirstlane_b32 s40, v4\n" \
    "v_readfirstlane_b32 s41, v5\n" \
    "v_readfirstlane_b32 s42, v6\n" \
    "v_readfirstlane_b32 s43, v7\n" \
    "v_readfirstlane_b32 s28, v8\n" \
    "v_readfirstlane_b32 s29, v9\n" \
    "v_readfirstlane_b32 s30, v10\n" \
    "v_readfirstlane_b32 s31, v11\n" \
    "v_readfirstlane_b32 s32, v12\n" \
    "v_readfirstlane_b32 s33, v13\n" \
    "v_readfirstlane_b32 s34, v14\n" \
    "v_readfirstlane_b32 s35, v15\n" \
    "v_readfirstlane_b32 s24, v16\n" \
    "v_readfirstlane_b32 s25, v17\n" \
    "v_readfirstlane_b32 s26, v18\n" \
    "v_readfirstlane_b32 s27, v19\n" \
    "v_readfirstlane_b32 s60, v24\n" \
    "v_readfirstlane_b32 s61, v25\n" \
    "v_readfirstlane_b32 s62, v26\n" \
    "v_readfirstlane_b32 s63, v27\n" \
    "v_readfirstlane_b32 s64, v28\n" \
    "v_readfirstlane_b32 s65, v29\n" \
    "v_readfirstlane_b32 s66, v30\n" \
    "v_readfirstlane_b32 s67, v31\n" \
    "s_mov_b32 s68, m0\n" \
    "s_getpc_b64 s[96:97]\n" \
    "14:\n" \
    "s_add_u32 s96, s96, 2f-14b\n" \
    "s_addc_u32 s97, s97, 0\n" \
    "s_lshl_b32 s74, s60, 10\n" \
    "s_lshl_b32 s95, s74, 1\n" \
    "s_add_i32 s95, s95, 16384\n" \
    "s_mov_b32 s81, 0x3e0293ee\n" \
    "s_mov_b32 s82, 0x53800000\n" \
    "s_lshl_b32 s83, s60, 6\n" \
    "s_and_b32 s83, s83, 128\n" \
    "v_add_u32 v109, s83, v131\n" \
    "v_sub_u32 v111, v131, s83\n" \
    "v_xor_b32 v112, 0x2000, v133\n" \
    "v_and_b32 v113, 0x3c0, v127\n" \
    "v_mad_u32_u24 v113, v113, 7, v127\n" \
    "s_bfe_u32 s83, s61, 0x2000b\n" \
    "s_and_b32 s83, s83, 3\n" \
    "v_lshl_add_u32 v113, s83, 7, v113\n" \
    "v_lshlrev_b32 v114, 1, v127\n" \
    "v_add_u32 v106, s63, v133\n" \
    "s_bfe_u32 s83, s61, 0x40009\n" \
    "s_bfe_u32 s80, s61, 0x20004\n" \
    "s_lshl2_add_u32 s83, s83, s80\n" \
    "s_mov_b64 s[72:73], 0\n" \
    "s_cmp_eq_u32 s43, 4\n" \
    "s_cbranch_scc1 20f\n" \
    "0:\n" \
    "s_and_b32 s75, s36, 3\n" \
    "s_lshl_b32 s75, s75, 16\n" \
    "s_add_i32 s75, s75, s26\n" \
    "s_add_i32 s69, s75, s74\n" \
    "s_add_i32 s70, s69, 4096\n" \
    "s_add_i32 s71, s75, 32768\n" \
    "12:\n" \
    "ds_read_b128 v[72:75], v133 offset:0\n" \
    "ds_read_b128 v[76:79], v133 offset:1024\n" \
    "ds_read_b128 v[80:83], v133 offset:2048\n" \
    "ds_read_b128 v[84:87], v133 offset:3072\n" \
    "ds_read_b128 v[88:91], v133 offset:16384\n" \
    "ds_read_b128 v[92:95], v133 offset:17408\n" \
    "s_add_i32 s91, s36, 1\n" \
    "s_and_b32 s78, s36, 3\n" \
    "s_lshl_b32 s78, s78, 14\n" \
    "s_add_i32 s77, s78, s61\n" \
    "s_bitcmp1_b32 s36, 0\n" \
    "s_cselect_b32 s79, 0x8000, 0\n" \
    "s_lshl_b32 s92, s91, 18\n" \
    "s_add_i32 s92, s92, s64\n" \
    "s_movk_i32 m0, 0x2000\n" \
    "s_add_i32 s78, s78, 0x1a0000\n" \
    "s_add_i32 s93, s92, 0x1000000\n" \
    "v_mov_b64 v[40:41], 0\n" \
    "v_mov_b64 v[42:43], 0\n" \
    "v_mov_b64 v[44:45], 0\n" \
    "v_mov_b64 v[46:47], 0\n" \
    "v_mov_b64 v[48:49], 0\n" \
    "v_mov_b64 v[50:51], 0\n" \
    "v_mov_b64 v[52:53], 0\n" \
    "v_mov_b64 v[54:55], 0\n" \
    "v_mov_b64 v[56:57], 0\n" \
    "v_mov_b64 v[58:59], 0\n" \
    "v_mov_b64 v[60:61], 0\n" \
    "v_mov_b64 v[62:63], 0\n" \
    "v_mov_b64 v[64:65], 0\n" \
    "v_mov_b64 v[66:67], 0\n" \
    "v_mov_b64 v[68:69], 0\n" \
    "v_mov_b64 v[70:71], 0\n" \
    "v_add_u32 v108, s74, v127\n" \
    "v_xor_b32 v24, 0x400, v108\n" \
    "v_xor_b32 v28, 0x800, v108\n" \
    "v_xor_b32 v32, 0xc00, v108\n" \
    "s_sub_i32 s76, s33, s42\n" \
    "s_ashr_i32 s76, s76, s32\n" \
    "s_add_i32 s42, s42, s76\n" \
    "s_lshr_b32 s76, s32, 8\n" \
    "s_sub_i32 s76, s76, s40\n" \
    "s_ashr_i32 s76, s76, 2\n" \
    "s_add_i32 s40, s40, s76\n" \
    "s_sub_i32 s76, s40, s24\n" \
    "s_min_i32 s94, s91, s38\n" \
    "s_lshl_b32 s94, s94, 18\n" \
    "s_andn2_b32 s80, 0x1000000, s66\n" \
    "s_add_i32 s94, s94, s64\n" \
    "s_add_i32 s94, s94, s80\n" \
    "s_cmp_eq_u32 s36, 0\n" \
    "s_cbranch_scc1 17f\n" \
    "s_sub_i32 s90, s37, s29\n" \
    "s_mul_i32 s90, s90, s25\n" \
    "s_ashr_i32 s90, s90, 12\n" \
    "s_add_i32 s90, s90, s24\n" \
    "s_add_i32 s90, s90, -116\n" \
    "1:\n" \
    "s_memtime vcc\n" \
    "s_waitcnt lgkmcnt(0)\n" \
    "s_sub_i32 s75, s90, vcc_lo\n" \
    "s_cmp_gt_i32 s75, 112\n" \
    "s_cbranch_scc1 1b\n" \
    "s_max_i32 s75, s75, 0\n" \
    "s_and_b32 s75, s75, -4\n" \
    "s_sub_u32 vcc_lo, s96, s75\n" \
    "s_subb_u32 vcc_hi, s97, 0\n" \
    "s_setpc_b64 vcc\n" \
    "s_nop 0\n" \
    "s_nop 0\n" \
    "s_nop 0\n" \
    "s_nop 0\n" \
    "s_nop 0\n" \
    "s_nop 0\n" \
    "s_nop 0\n" \
    "s_nop 0\n" \
    "s_nop 0\n" \
    "s_nop 0\n" \
    "s_nop 0\n" \
    "s_nop 0\n" \
    "s_nop 0\n" \
    "s_nop 0\n" \
    "s_nop 0\n" \
    "s_nop 0\n" \
    "s_nop 0\n" \
    "s_nop 0\n" \
    "s_nop 0\n" \
    "s_nop 0\n" \
    "s_nop 0\n" \
    "s_nop 0\n" \
    "s_nop 0\n" \
    "s_nop 0\n" \
    "s_nop 0\n" \
    "s_nop 0\n" \
    "s_nop 0\n" \
    "s_nop 0\n" \
    "2:\n" \
    "buffer_load_dwordx4 v[0:3], v125, %[rs], s69 offen sc1\n" \
    "s_nop 3\n" \
    "s_nop 3\n" \
    "buffer_load_dwordx4 v[16:19], v125, %[rs], s70 offen sc1\n" \
    "s_mov_b32 exec_lo, s27\n" \
    "s_mov_b32 exec_hi, s27\n" \
    "buffer_load_dwordx4 v127, %[rs], s71 offen sc1 lds\n" \
    "s_mov_b64 exec, -1\n" \
    "v_bfe_u32 v20, v127, 8, 2\n" \
    "v_subrev_u32 v20, s60, v20\n" \
    "v_mul_i32_i24 v20, -1088, v20\n" \
    "v_add_u32 v20, v20, v129\n" \
    "v_lshrrev_b32 v21, 2, v127\n" \
    "s_mov_b32 exec_lo, s66\n" \
    "s_mov_b32 exec_hi, s66\n" \
    "ds_read2_b32 v[96:97], v20 offset0:0 offset1:144\n" \
    "s_mov_b64 exec, -1\n" \
    "s_lshl_b32 s80, s36, 16\n" \
    "s_add_i32 s80, s80, s67\n" \
    "s_cmp_eq_u32 s60, 0\n" \
    "s_cselect_b32 s72, 0x1010101, 0\n" \
    "s_waitcnt lgkmcnt(0)\n" \
    "buffer_store_dword v96, v21, %[rw], s80 offen sc1\n" \
    "buffer_store_dword v97, v21, %[rw], s80 offen offset:256 sc1\n" \
    "3:\n" \
    "s_mov_b32 m0, s95\n" \
    "s_add_i32 s75, s36, 2\n" \
    "s_cmp_lt_i32 s38, s75\n" \
    "s_cselect_b32 s75, s62, 0x1b0040\n" \
    "v_mov_b64 v[98:99], 0\n" \
    "v_mov_b32 v101, 0\n" \
    "s_pack_ll_b32_b16 s80, s91, s42\n" \
    "v_mov_b32 v103, s80\n" \
    "s_waitcnt vmcnt(4)\n" \
    "v_smfmac_f32_16x16x64_bf16 v[40:43], v[0:3], a[0:7], v124\n" \
    "ds_write_b128 v108, v[0:3] offset:0\n" \
    "v_smfmac_f32_16x16x64_bf16 v[44:47], v[0:3], a[8:15], v124\n" \
    "v_smfmac_f32_16x16x64_bf16 v[48:51], v[0:3], a[128:135], v124\n" \
    "v_smfmac_f32_16x16x64_bf16 v[52:55], v[0:3], a[136:143], v124\n" \
    "s_waitcnt vmcnt(3)\n" \
    "v_smfmac_f32_16x16x64_bf16 v[40:43], v[16:19], a[64:71], v124\n" \
    "ds_write_b128 v108, v[16:19] offset:4096\n" \
    "v_smfmac_f32_16x16x64_bf16 v[44:47], v[16:19], a[72:79], v124\n" \
    "s_waitcnt lgkmcnt(1)\n" \
    "s_barrier\n" \
    "v_smfmac_f32_16x16x64_bf16 v[48:51], v[16:19], a[192:199], v124\n" \
    "ds_read_b128 v[4:7], v24 offset:0\n" \
    "v_smfmac_f32_16x16x64_bf16 v[52:55], v[16:19], a[200:207], v124\n" \
    "ds_read_b128 v[8:11], v28 offset:0\n" \
    "v_smfmac_f32_16x16x64_bf16 v[56:59], v[0:3], v[72:79], v124\n" \
    "ds_read_b128 v[12:15], v32 offset:0\n" \
    "v_smfmac_f32_16x16x64_bf16 v[60:63], v[0:3], v[80:87], v124\n" \
    "s_waitcnt vmcnt(2) lgkmcnt(3)\n" \
    "s_barrier\n" \
    "s_memtime s[86:87]\n" \
    "v_smfmac_f32_16x16x64_bf16 v[56:59], v[16:19], v[168:175], v124\n" \
    "ds_read_b128 v[24:27], v24 offset:4096\n" \
    "v_smfmac_f32_16x16x64_bf16 v[60:63], v[16:19], v[176:183], v124\n" \
    "ds_read_b128 v[28:31], v28 offset:4096\n" \
    "s_waitcnt lgkmcnt(3)\n" \
    "v_smfmac_f32_16x16x64_bf16 v[40:43], v[4:7], a[16:23], v124\n" \
    "ds_read_b128 v[32:35], v32 offset:4096\n" \
    "v_smfmac_f32_16x16x64_bf16 v[40:43], v[8:11], a[32:39], v124\n" \
    "ds_read_b128 v[36:39], v126 offset:0\n" \
    "s_waitcnt lgkmcnt(4)\n" \
    "v_smfmac_f32_16x16x64_bf16 v[40:43], v[12:15], a[48:55], v124\n" \
    "buffer_load_dword v110, v114, %[rw], s94 offen sc1\n" \
    "v_smfmac_f32_16x16x64_bf16 v[44:47], v[4:7], a[24:31], v124\n" \
    "s_waitcnt lgkmcnt(2)\n" \
    "v_smfmac_f32_16x16x64_bf16 v[40:43], v[24:27], a[80:87], v124\n" \
    "v_smfmac_f32_16x16x64_bf16 v[40:43], v[28:31], a[96:103], v124\n" \
    "s_waitcnt lgkmcnt(0)\n" \
    "v_smfmac_f32_16x16x64_bf16 v[40:43], v[32:35], a[112:119], v124\n" \
    "ds_read_b128 v[72:75], v133 offset:18432\n" \
    "s_add_i32 s80, s86, s76\n" \
    "v_smfmac_f32_16x16x64_bf16 v[40:43], v[36:39], v[136:143], v124\n" \
    "ds_read_b128 v[76:79], v133 offset:19456\n" \
    "s_mul_hi_u32 s80, s80, s41\n" \
    "v_smfmac_f32_16x16x64_bf16 v[44:47], v[8:11], a[40:47], v124\n" \
    "ds_read_b128 v[80:83], v133 offset:32768\n" \
    "s_add_i32 s80, s80, s29\n" \
    "v_mov_b32 v102, s80\n" \
    "v_smfmac_f32_16x16x64_bf16 v[44:47], v[12:15], a[56:63], v124\n" \
    "ds_read_b128 v[84:87], v133 offset:33792\n" \
    "v_add_f32 v40, v40, v42\n" \
    "v_add_f32 v41, v41, v43\n" \
    "v_smfmac_f32_16x16x64_bf16 v[44:47], v[24:27], a[88:95], v124\n" \
    "ds_read_b128 v[20:23], v127 offset:19456\n" \
    "v_cmp_u_f32 vcc, v40, v41\n" \
    "v_smfmac_f32_16x16x64_bf16 v[44:47], v[28:31], a[104:111], v124\n" \
    "v_mul_f32 v40, s81, v40\n" \
    "v_smfmac_f32_16x16x64_bf16 v[44:47], v[32:35], a[120:127], v124\n" \
    "s_cbranch_vccnz 5f\n" \
    "6:\n" \
    "s_mov_b64 exec, s[72:73]\n" \
    "v_smfmac_f32_16x16x64_bf16 v[44:47], v[36:39], v[144:151], v124\n" \
    "buffer_store_dwordx2 v[102:103], v127, %[rs], s77 offen sc1\n" \
    "v_smfmac_f32_16x16x64_bf16 v[56:59], v[4:7], v[88:95], v124\n" \
    "s_mov_b64 exec, -1\n" \
    "s_waitcnt lgkmcnt(1)\n" \
    "v_mul_f32 v41, s81, v41\n" \
    "v_smfmac_f32_16x16x64_bf16 v[60:63], v[4:7], v[72:79], v124\n" \
    "ds_read_b128 v[88:91], v133 offset:34816\n" \
    "v_add_f32 v44, v44, v46\n" \
    "v_smfmac_f32_16x16x64_bf16 v[56:59], v[8:11], v[80:87], v124\n" \
    "ds_read_b128 v[92:95], v133 offset:35840\n" \
    "v_add_f32 v45, v45, v47\n" \
    "v_smfmac_f32_16x16x64_bf16 v[48:51], v[4:7], a[144:151], v124\n" \
    "ds_read_b128 v[72:75], v133 offset:49152\n" \
    "v_mul_f32 v44, s81, v44\n" \
    "v_smfmac_f32_16x16x64_bf16 v[52:55], v[4:7], a[152:159], v124\n" \
    "ds_read_b128 v[76:79], v133 offset:50176\n" \
    "v_mul_f32 v45, s81, v45\n" \
    "v_smfmac_f32_16x16x64_bf16 v[48:51], v[8:11], a[160:167], v124\n" \
    "ds_read_b128 v[0:3], v127 offset:16384\n" \
    "v_cvt_pk_bf16_f32 v107, v40, v44\n" \
    "v_smfmac_f32_16x16x64_bf16 v[52:55], v[8:11], a[168:175], v124\n" \
    "ds_read_b128 v[4:7], v127 offset:18432\n" \
    "s_waitcnt lgkmcnt(4)\n" \
    "v_cvt_pk_bf16_f32 v108, v41, v45\n" \
    "v_smfmac_f32_16x16x64_bf16 v[60:63], v[8:11], v[88:95], v124\n" \
    "ds_write2_b32 v128, v107, v108 offset0:0 offset1:80\n" \
    "v_smfmac_f32_16x16x64_bf16 v[60:63], v[12:15], v[248:255], v124\n" \
    "ds_read_b128 v[16:19], v127 offset:17408\n" \
    "s_waitcnt lgkmcnt(4)\n" \
    "v_smfmac_f32_16x16x64_bf16 v[56:59], v[12:15], v[72:79], v124\n" \
    "ds_read_b128 v[8:11], v127 offset:20480\n" \
    "v_smfmac_f32_16x16x64_bf16 v[48:51], v[12:15], a[176:183], v124\n" \
    "ds_read_b128 v[40:43], v127 offset:21504\n" \
    "v_smfmac_f32_16x16x64_bf16 v[52:55], v[12:15], a[184:191], v124\n" \
    "ds_read_b128 v[44:47], v127 offset:23552\n" \
    "v_smfmac_f32_16x16x64_bf16 v[48:51], v[24:27], a[208:215], v124\n" \
    "s_waitcnt lgkmcnt(4)\n" \
    "s_barrier\n" \
    "v_smfmac_f32_16x16x64_bf16 v[52:55], v[24:27], a[216:223], v124\n" \
    "ds_read_b128 v[72:75], v130 offset:0\n" \
    "v_smfmac_f32_16x16x64_bf16 v[48:51], v[28:31], a[224:231], v124\n" \
    "ds_read_b128 v[76:79], v130 offset:64\n" \
    "v_smfmac_f32_16x16x64_bf16 v[48:51], v[32:35], a[240:247], v124\n" \
    "ds_read_b128 v[80:83], v130 offset:128\n" \
    "v_smfmac_f32_16x16x64_bf16 v[48:51], v[36:39], v[152:159], v124\n" \
    "ds_read_b128 v[84:87], v130 offset:192\n" \
    "v_smfmac_f32_16x16x64_bf16 v[52:55], v[28:31], a[232:239], v124\n" \
    "ds_read_b128 v[12:15], v127 offset:22528\n" \
    "v_smfmac_f32_16x16x64_bf16 v[52:55], v[32:35], a[248:255], v124\n" \
    "v_add_f32 v48, v48, v50\n" \
    "v_smfmac_f32_16x16x64_bf16 v[52:55], v[36:39], v[160:167], v124\n" \
    "s_waitcnt lgkmcnt(3)\n" \
    "v_add_f32 v49, v49, v51\n" \
    "v_mfma_f32_16x16x32_bf16 v[88:91], v[0:3], v[72:75], 0\n" \
    "ds_read_b64 v[0:1], v132 offset:0\n" \
    "ds_read_b64 v[2:3], v132 offset:1024\n" \
    "v_mfma_f32_16x16x32_bf16 v[88:91], v[4:7], v[76:79], v[88:91]\n" \
    "s_waitcnt lgkmcnt(2)\n" \
    "v_add_f32 v52, v52, v54\n" \
    "v_add_f32 v53, v53, v55\n" \
    "v_mfma_f32_16x16x32_bf16 v[88:91], v[8:11], v[80:83], v[88:91]\n" \
    "v_cvt_pk_bf16_f32 v107, v48, v52\n" \
    "v_cvt_pk_bf16_f32 v108, v49, v53\n" \
    "v_mfma_f32_16x16x32_bf16 v[88:91], v[12:15], v[84:87], v[88:91]\n" \
    "ds_write2_b32 v129, v107, v108 offset0:0 offset1:144\n" \
    "v_mfma_f32_16x16x32_bf16 v[92:95], v[16:19], v[72:75], 0\n" \
    "ds_read_b64 v[4:5], v132 offset:512\n" \
    "ds_read_b64 v[6:7], v132 offset:1536\n" \
    "v_mfma_f32_16x16x32_bf16 v[92:95], v[20:23], v[76:79], v[92:95]\n" \
    "ds_read_b128 v[8:11], v106 offset:0\n" \
    "v_exp_f32 v88, v88\n" \
    "v_mfma_f32_16x16x32_bf16 v[92:95], v[40:43], v[80:83], v[92:95]\n" \
    "ds_read_b128 v[12:15], v106 offset:1024\n" \
    "v_exp_f32 v89, v89\n" \
    "v_mfma_f32_16x16x32_bf16 v[92:95], v[44:47], v[84:87], v[92:95]\n" \
    "s_waitcnt lgkmcnt(4)\n" \
    "s_barrier\n" \
    "v_smfmac_f32_16x16x64_bf16 v[56:59], v[24:27], v[184:191], v124\n" \
    "ds_read_b128 v[48:51], v131 offset:0\n" \
    "ds_read_b128 v[52:55], v131 offset:128\n" \
    "v_cvt_pk_bf16_f32 v88, v88, v89\n" \
    "v_smfmac_f32_16x16x64_bf16 v[56:59], v[28:31], v[200:207], v124\n" \
    "ds_read_b128 v[16:19], v106 offset:32768\n" \
    "v_exp_f32 v90, v90\n" \
    "v_smfmac_f32_16x16x64_bf16 v[56:59], v[32:35], v[216:223], v124\n" \
    "ds_read_b128 v[20:23], v106 offset:33792\n" \
    "v_exp_f32 v91, v91\n" \
    "v_smfmac_f32_16x16x64_bf16 v[56:59], v[36:39], v[232:239], v124\n" \
    "ds_read_b128 v[40:43], v106 offset:2048\n" \
    "s_waitcnt lgkmcnt(3)\n" \
    "v_exp_f32 v92, v92\n" \
    "v_cvt_pk_bf16_f32 v89, v90, v91\n" \
    "v_smfmac_f32_16x16x64_bf16 v[64:67], v[48:51], v[72:79], v124\n" \
    "ds_read_b128 v[44:47], v106 offset:3072\n" \
    "v_exp_f32 v93, v93\n" \
    "v_smfmac_f32_16x16x64_bf16 v[64:67], v[52:55], v[80:87], v124\n" \
    "ds_read_b128 v[72:75], v106 offset:34816\n" \
    "v_exp_f32 v94, v94\n" \
    "v_cvt_pk_bf16_f32 v90, v92, v93\n" \
    "v_smfmac_f32_16x16x64_bf16 v[60:63], v[24:27], v[192:199], v124\n" \
    "ds_read_b128 v[76:79], v106 offset:35840\n" \
    "v_exp_f32 v95, v95\n" \
    "v_smfmac_f32_16x16x64_bf16 v[60:63], v[28:31], v[208:215], v124\n" \
    "ds_read_b128 v[80:83], v106 offset:4096\n" \
    "s_cmp_eq_u32 s36, 62\n" \
    "v_add_f32 v64, v64, v66\n" \
    "v_add_f32 v65, v65, v67\n" \
    "v_smfmac_f32_16x16x64_bf16 v[60:63], v[32:35], v[224:231], v124\n" \
    "ds_read_b128 v[84:87], v106 offset:5120\n" \
    "v_exp_f32 v64, v64\n" \
    "v_cvt_pk_bf16_f32 v91, v94, v95\n" \
    "v_smfmac_f32_16x16x64_bf16 v[60:63], v[36:39], v[240:247], v124\n" \
    "ds_read_b128 v[48:51], v106 offset:36864\n" \
    "v_exp_f32 v65, v65\n" \
    "v_swap_b32 v1, v2\n" \
    "v_swap_b32 v5, v6\n" \
    "v_mfma_f32_16x16x32_bf16 v[102:105], v[88:91], v[120:123], 0\n" \
    "ds_read_b128 v[52:55], v106 offset:37888\n" \
    "v_cvt_pk_bf16_f32 v100, v64, v65\n" \
    "v_add_f32 v56, v56, v58\n" \
    "v_add_f32 v57, v57, v59\n" \
    "v_smfmac_f32_16x16x64_bf16 v[68:71], v[88:91], v[0:7], v124\n" \
    "ds_read_b128 v[24:27], v106 offset:6144\n" \
    "v_add_f32 v60, v60, v62\n" \
    "v_add_f32 v61, v61, v63\n" \
    "v_mfma_f32_16x16x16_bf16 v[102:105], v[100:101], v[120:121], v[102:105]\n" \
    "ds_read_b128 v[28:31], v106 offset:7168\n" \
    "v_cvt_pk_bf16_f32 v96, v56, v57\n" \
    "v_cvt_pk_bf16_f32 v97, v60, v61\n" \
    "s_nop 1\n" \
    "v_smfmac_f32_16x16x32_bf16 v[68:71], v[100:101], v[96:99], v124\n" \
    "ds_read_b128 v[32:35], v106 offset:38912\n" \
    "v_mov_b64 v[56:57], 0\n" \
    "v_mov_b64 v[58:59], 0\n" \
    "v_rcp_f32 v104, v102\n" \
    "v_rcp_f32 v105, v103\n" \
    "v_mul_f32 v68, v68, v104\n" \
    "v_mul_f32 v70, v70, v104\n" \
    "v_mul_f32 v69, v69, v105\n" \
    "v_mul_f32 v71, v71, v105\n" \
    "v_cvt_pk_bf16_f32 v107, v68, v70\n" \
    "v_cvt_pk_bf16_f32 v108, v69, v71\n" \
    "ds_write2_b32 v129, v107, v108 offset0:64 offset1:208\n" \
    "v_max3_f32 v102, v102, v103, v104\n" \
    "v_max_f32 v102, v102, v105\n" \
    "v_cmp_nge_f32 vcc, s82, v102\n" \
    "v_mov_b64 v[60:61], 0\n" \
    "v_mov_b64 v[62:63], 0\n" \
    "v_mov_b64 v[88:89], 0\n" \
    "v_mov_b64 v[90:91], 0\n" \
    "v_mov_b64 v[92:93], 0\n" \
    "v_mov_b64 v[94:95], 0\n" \
    "s_waitcnt vmcnt(2) lgkmcnt(0)\n" \
    "s_barrier\n" \
    "ds_read_b128 v[68:71], v109 offset:256\n" \
    "ds_read_b128 v[102:105], v111 offset:384\n" \
    "s_cbranch_vccnz 7f\n" \
    "buffer_load_dwordx2 v[98:99], v113, %[rs], s78 offen sc1\n" \
    "buffer_load_dword v100, off, %[rw], s75 sc1\n" \
    "v_mov_b64 v[64:65], 0\n" \
    "v_mov_b64 v[66:67], 0\n" \
    "s_waitcnt lgkmcnt(1)\n" \
    "v_smfmac_f32_16x16x64_bf16 v[56:59], v[68:71], v[8:15], v124\n" \
    "s_waitcnt lgkmcnt(0)\n" \
    "ds_read_b128 v[36:39], v106 offset:39936\n" \
    "v_smfmac_f32_16x16x64_bf16 v[56:59], v[102:105], v[16:23], v124\n" \
    "ds_read_b128 v[0:3], v133 offset:51200\n" \
    "v_smfmac_f32_16x16x64_bf16 v[60:63], v[68:71], v[40:47], v124\n" \
    "ds_read_b128 v[4:7], v133 offset:52224\n" \
    "v_smfmac_f32_16x16x64_bf16 v[60:63], v[102:105], v[72:79], v124\n" \
    "ds_read_b128 v[16:19], v112 offset:51200\n" \
    "v_add_f32 v56, v56, v58\n" \
    "v_smfmac_f32_16x16x64_bf16 v[88:91], v[68:71], v[80:87], v124\n" \
    "ds_read_b128 v[20:23], v112 offset:52224\n" \
    "v_add_f32 v57, v57, v59\n" \
    "v_smfmac_f32_16x16x64_bf16 v[88:91], v[102:105], v[48:55], v124\n" \
    "v_add_f32 v60, v60, v62\n" \
    "v_smfmac_f32_16x16x64_bf16 v[92:95], v[68:71], v[24:31], v124\n" \
    "s_waitcnt lgkmcnt(4)\n" \
    "v_add_f32 v61, v61, v63\n" \
    "v_smfmac_f32_16x16x64_bf16 v[92:95], v[102:105], v[32:39], v124\n" \
    "s_waitcnt lgkmcnt(2)\n" \
    "v_add_f32 v88, v88, v90\n" \
    "v_smfmac_f32_16x16x64_bf16 v[64:67], v[68:71], v[0:7], v124\n" \
    "s_waitcnt lgkmcnt(0)\n" \
    "v_add_f32 v89, v89, v91\n" \
    "v_smfmac_f32_16x16x64_bf16 v[64:67], v[102:105], v[16:23], v124\n" \
    "v_add_f32 v92, v92, v94\n" \
    "v_add_f32 v93, v93, v95\n" \
    "v_cvt_pk_bf16_f32 v8, v56, v60\n" \
    "v_cvt_pk_bf16_f32 v10, v57, v61\n" \
    "v_cvt_pk_bf16_f32 v9, v88, v92\n" \
    "v_cvt_pk_bf16_f32 v11, v89, v93\n" \
    "buffer_store_dwordx4 v[8:11], v134, %[rs], s79 offen sc1\n" \
    "v_add_f32 v64, v64, v66\n" \
    "v_add_f32 v65, v65, v67\n" \
    "v_cvt_pk_bf16_f32 v64, v64, v65\n" \
    "buffer_store_dword v64, v135, %[rs], s79 offen sc1\n" \
    "s_cbranch_scc1 16f\n" \
    "s_memtime s[88:89]\n" \
    "s_memrealtime s[44:45]\n" \
    "s_and_b32 s75, s91, 3\n" \
    "s_lshl_b32 s75, s75, 16\n" \
    "s_memrealtime s[46:47]\n" \
    "s_add_i32 s75, s75, s26\n" \
    "s_add_i32 s69, s75, s74\n" \
    "s_memrealtime s[48:49]\n" \
    "s_add_i32 s70, s69, 4096\n" \
    "s_add_i32 s71, s75, 32768\n" \
    "s_memrealtime s[50:51]\n" \
    "s_nop 1\n" \
    "s_memrealtime s[52:53]\n" \
    "s_nop 1\n" \
    "s_memrealtime s[54:55]\n" \
    "s_nop 1\n" \
    "s_memrealtime s[56:57]\n" \
    "s_nop 1\n" \
    "s_memrealtime vcc\n" \
    "v_and_b32 v18, 0x300, v127\n" \
    "v_add_u32 v18, v18, v127\n" \
    "v_and_b32 v19, 0x380, v127\n" \
    "v_add_u32 v19, v19, v127\n" \
    "s_waitcnt lgkmcnt(0)\n" \
    "s_add_i32 s44, s44, s46\n" \
    "s_add_i32 s48, s48, s50\n" \
    "s_add_i32 s52, s52, s54\n" \
    "s_add_i32 s44, s44, s48\n" \
    "s_add_i32 s52, s52, s56\n" \
    "s_add_i32 s44, s44, s52\n" \
    "s_add_i32 s80, s44, vcc_lo\n" \
    "s_sub_i32 s76, s80, s29\n" \
    "s_mov_b32 s29, s80\n" \
    "s_sub_i32 s80, s88, s24\n" \
    "s_mov_b32 s24, s88\n" \
    "s_mul_i32 s75, s76, s25\n" \
    "s_lshr_b32 s75, s75, 12\n" \
    "s_sub_i32 s75, s80, s75\n" \
    "s_mul_hi_u32 s80, s80, s41\n" \
    "s_sub_i32 s80, s76, s80\n" \
    "s_lshl_b32 s80, s80, 18\n" \
    "s_cmp_lt_u32 s76, 1480\n" \
    "s_cselect_b32 s75, s75, 0\n" \
    "s_cselect_b32 s80, s80, 0\n" \
    "s_add_i32 s25, s25, s75\n" \
    "s_add_i32 s41, s41, s80\n" \
    "s_waitcnt vmcnt(2)\n" \
    "v_readfirstlane_b32 s80, v100\n" \
    "v_cmp_ne_u16 vcc, s91, v99\n" \
    "s_mov_b32 s43, 0\n" \
    "s_max_i32 s38, s38, s80\n" \
    "s_cmp_lt_i32 s38, s91\n" \
    "s_cselect_b64 exec, 0, -1\n" \
    "s_cselect_b32 s94, 8, 0\n" \
    "buffer_load_dwordx4 v19, %[rw], s92 offen sc1 lds\n" \
    "s_add_i32 m0, s95, 896\n" \
    "s_nop 0\n" \
    "buffer_load_dwordx4 v19, %[rw], s92 offen offset:128 sc1 lds\n" \
    "s_add_i32 m0, s95, 8192\n" \
    "s_nop 0\n" \
    "buffer_load_dwordx4 v18, %[rw], s93 offen sc1 lds\n" \
    "s_add_i32 m0, s95, 8960\n" \
    "s_nop 0\n" \
    "buffer_load_dwordx4 v18, %[rw], s93 offen offset:256 sc1 lds\n" \
    "s_mov_b64 exec, -1\n" \
    "s_cbranch_vccnz 15f\n" \
    "10:\n" \
    "v_subrev_u32 v28, s29, v98\n" \
    "v_min_i32 v28, 0x4000, v28\n" \
    "v_max_i32 v28, 0xffffc000, v28\n" \
    "s_bitcmp1_b32 s30, 15\n" \
    "s_cbranch_scc1 18f\n" \
    "19:\n" \
    "v_readlane_b32 s75, v28, s83\n" \
    "v_max_i32_dpp v28, v28, v28 row_shr:1 row_mask:0xf bank_mask:0xf\n" \
    "s_nop 1\n" \
    "v_max_i32_dpp v28, v28, v28 row_shr:2 row_mask:0xf bank_mask:0xf\n" \
    "s_nop 1\n" \
    "v_max_i32_dpp v28, v28, v28 row_shr:4 row_mask:0xf bank_mask:0xf\n" \
    "s_nop 1\n" \
    "v_max_i32_dpp v28, v28, v28 row_shr:8 row_mask:0xf bank_mask:0xf\n" \
    "s_add_i32 s76, s29, s42\n" \
    "s_nop 0\n" \
    "v_max_i32_dpp v28, v28, v28 row_bcast:15 row_mask:0xa bank_mask:0xf\n" \
    "s_nop 1\n" \
    "v_max_i32_dpp v28, v28, v28 row_bcast:31 row_mask:0xc bank_mask:0xf\n" \
    "s_nop 0\n" \
    "v_readlane_b32 s37, v28, 63\n" \
    "s_sub_i32 s75, s37, s75\n" \
    "s_cmp_gt_i32 s75, 24\n" \
    "s_cselect_b32 s80, 2, 0\n" \
    "s_cmp_lt_i32 s75, 10\n" \
    "s_cselect_b32 s80, -6, s80\n" \
    "s_add_i32 s28, s28, s80\n" \
    "s_max_i32 s28, s28, 0\n" \
    "s_min_i32 s28, s28, 20\n" \
    "s_add_i32 s76, s76, s28\n" \
    "v_add_u32 v30, -1, s36\n" \
    "s_add_i32 s37, s37, s76\n" \
    "v_mov_b32 v31, s37\n" \
    "s_mov_b64 exec, s[72:73]\n" \
    "buffer_store_dwordx2 v[30:31], off, %[rw], s65 sc1\n" \
    "s_mov_b64 exec, -1\n" \
    "s_cmp_lg_u32 s94, 0\n" \
    "s_cbranch_scc1 9f\n" \
    "s_mov_b32 s36, s91\n" \
    "s_branch 12b\n" \
    "5:\n" \
    "v_mov_b32 v107, 0\n" \
    "v_max3_u32 v107, v107, v0, v1\n" \
    "v_max3_u32 v107, v107, v2, v3\n" \
    "v_max3_u32 v107, v107, v4, v5\n" \
    "v_max3_u32 v107, v107, v6, v7\n" \
    "v_max3_u32 v107, v107, v8, v9\n" \
    "v_max3_u32 v107, v107, v10, v11\n" \
    "v_max3_u32 v107, v107, v12, v13\n" \
    "v_max3_u32 v107, v107, v14, v15\n" \
    "v_max3_u32 v107, v107, v16, v17\n" \
    "v_max3_u32 v107, v107, v18, v19\n" \
    "v_max3_u32 v107, v107, v24, v25\n" \
    "v_max3_u32 v107, v107, v26, v27\n" \
    "v_max3_u32 v107, v107, v28, v29\n" \
    "v_max3_u32 v107, v107, v30, v31\n" \
    "v_max3_u32 v107, v107, v32, v33\n" \
    "v_max3_u32 v107, v107, v34, v35\n" \
    "v_max3_u32 v107, v107, v36, v37\n" \
    "v_max3_u32 v107, v107, v38, v39\n" \
    "v_cmp_eq_u32 vcc, -1, v107\n" \
    "s_nop 4\n" \
    "s_cbranch_vccz 6b\n" \
    "s_cmp_eq_u32 s36, 0\n" \
    "s_cbranch_scc1 6b\n" \
    "s_add_i32 s39, s39, -1\n" \
    "s_cmp_lt_i32 s39, 0\n" \
    "s_cbranch_scc1 6b\n" \
    "s_add_i32 s30, s30, 1\n" \
    "s_bitset1_b32 s30, 15\n" \
    "s_and_b32 s80, s31, 0xff\n" \
    "s_add_i32 s42, s42, s80\n" \
    "s_bfe_u32 s80, s31, 0x80008\n" \
    "s_add_i32 s40, s40, s80\n" \
    "s_waitcnt lgkmcnt(0)\n" \
    "s_mov_b32 s37, s29\n" \
    "s_nop 7\n" \
    "s_barrier\n" \
    "s_branch 0b\n" \
    "15:\n" \
    "s_add_i32 s30, s30, 0x10000\n" \
    "s_add_i32 s39, s39, -1\n" \
    "s_cmp_lt_i32 s39, 0\n" \
    "s_cbranch_scc1 10b\n" \
    "s_sleep 9\n" \
    "buffer_load_dwordx2 v[98:99], v113, %[rs], s78 offen sc1\n" \
    "s_waitcnt vmcnt(0)\n" \
    "v_cmp_ne_u16 vcc, s91, v99\n" \
    "s_nop 1\n" \
    "s_cbranch_vccnz 15b\n" \
    "s_memtime s[88:89]\n" \
    "s_memrealtime s[44:45]\n" \
    "s_nop 1\n" \
    "s_memrealtime s[46:47]\n" \
    "s_nop 1\n" \
    "s_memrealtime s[48:49]\n" \
    "s_nop 1\n" \
    "s_memrealtime s[50:51]\n" \
    "s_nop 1\n" \
    "s_memrealtime s[52:53]\n" \
    "s_nop 1\n" \
    "s_memrealtime s[54:55]\n" \
    "s_nop 1\n" \
    "s_memrealtime s[56:57]\n" \
    "s_nop 1\n" \
    "s_memrealtime vcc\n" \
    "s_waitcnt lgkmcnt(0)\n" \
    "s_add_i32 s44, s44, s46\n" \
    "s_add_i32 s48, s48, s50\n" \
    "s_add_i32 s52, s52, s54\n" \
    "s_add_i32 s44, s44, s48\n" \
    "s_add_i32 s52, s52, s56\n" \
    "s_add_i32 s44, s44, s52\n" \
    "s_add_i32 s29, s44, vcc_lo\n" \
    "s_mov_b32 s24, s88\n" \
    "s_branch 10b\n" \
    "18:\n" \
    "v_cmp_gt_i32 vcc, -320, v28\n" \
    "s_bitset0_b32 s30, 15\n" \
    "s_lshr_b32 s80, s31, 16\n" \
    "s_cmp_lt_u32 s36, 4\n" \
    "s_cselect_b32 s80, 0, s80\n" \
    "s_min_u32 s76, s80, 1\n" \
    "s_lshl_b32 s76, s76, 16\n" \
    "s_sub_i32 s31, s31, s76\n" \
    "s_mul_i32 s76, s80, 768\n" \
    "s_cmp_eq_u64 vcc, 0\n" \
    "s_cselect_b32 s80, 0, s80\n" \
    "s_cselect_b32 s76, s76, 0\n" \
    "s_add_i32 s33, s33, s80\n" \
    "s_add_i32 s32, s32, s76\n" \
    "s_branch 19b\n" \
    "17:\n" \
    "ds_read_b128 v[0:3], v108 offset:0\n" \
    "ds_read_b128 v[16:19], v108 offset:4096\n" \
    "s_waitcnt lgkmcnt(0)\n" \
    "s_branch 3b\n" \
    "20:\n" \
    "s_mov_b32 s91, 63\n" \
    "s_mov_b32 s78, 0x1a8000\n" \
    "buffer_load_dwordx2 v[98:99], v113, %[rs], s78 offen sc1\n" \
    "buffer_load_dword v100, off, %[rw], s62 sc1\n" \
    "s_waitcnt vmcnt(0)\n" \
    "16:\n" \
    "v_and_b32 v112, 0x300, v127\n" \
    "v_add_u32 v112, v112, v127\n" \
    "s_add_i32 s44, %[qb], 0\n" \
    "s_add_i32 s45, s44, 4096\n" \
    "buffer_load_dwordx4 v[136:139], v112, %[rw], s44 offen offset:0 sc1\n" \
    "v_lshrrev_b32 v103, 1, v127\n" \
    "s_lshr_b32 s76, s74, 1\n" \
    "v_add_u32 v101, s76, v103\n" \
    "ds_write_b64 v101, v[96:97] offset:9216\n" \
    "ds_read_b128 v[0:3], v127 offset:16384\n" \
    "v_lshrrev_b32 v42, 8, v127\n" \
    "v_bfe_u32 v40, v127, 4, 4\n" \
    "ds_read_b128 v[4:7], v127 offset:17408\n" \
    "v_and_b32 v41, 2, v42\n" \
    "v_add_u32 v43, v41, v42\n" \
    "ds_read_b128 v[8:11], v127 offset:18432\n" \
    "s_lshl_b32 s76, s60, 4\n" \
    "s_and_b32 s76, s76, 16\n" \
    "v_add_u32 v40, s76, v40\n" \
    "v_mad_i32_i24 v40, v43, -4, v40\n" \
    "buffer_load_dwordx4 v[140:143], v112, %[rw], s44 offen offset:2048 sc1\n" \
    "ds_read_b128 v[12:15], v127 offset:19456\n" \
    "v_mov_b32 v43, 0xff61b1e6\n" \
    "v_cmp_gt_i32 vcc, 0, v40\n" \
    "ds_read_b128 v[16:19], v127 offset:20480\n" \
    "v_cndmask_b32 v88, 0, v43, vcc\n" \
    "v_cmp_gt_i32 vcc, 1, v40\n" \
    "ds_read_b128 v[20:23], v127 offset:21504\n" \
    "v_cndmask_b32 v89, 0, v43, vcc\n" \
    "v_cmp_gt_i32 vcc, 2, v40\n" \
    "ds_read_b128 v[24:27], v127 offset:22528\n" \
    "v_cndmask_b32 v90, 0, v43, vcc\n" \
    "v_cmp_gt_i32 vcc, 3, v40\n" \
    "buffer_load_dwordx4 v[144:147], v112, %[rw], s45 offen offset:0 sc1\n" \
    "ds_read_b128 v[28:31], v127 offset:23552\n" \
    "v_cndmask_b32 v91, 0, v43, vcc\n" \
    "v_cmp_gt_i32 vcc, 8, v40\n" \
    "ds_read_b128 v[32:35], v131 offset:0\n" \
    "v_cndmask_b32 v92, 0, v43, vcc\n" \
    "v_cmp_gt_i32 vcc, 9, v40\n" \
    "ds_read_b128 v[36:39], v131 offset:128\n" \
    "v_cndmask_b32 v93, 0, v43, vcc\n" \
    "v_cmp_gt_i32 vcc, 10, v40\n" \
    "ds_read_b128 v[56:59], v127 offset:24576\n" \
    "v_cndmask_b32 v94, 0, v43, vcc\n" \
    "v_cmp_gt_i32 vcc, 11, v40\n" \
    "buffer_load_dwordx4 v[148:151], v112, %[rw], s45 offen offset:2048 sc1\n" \
    "ds_read_b128 v[60:63], v127 offset:25600\n" \
    "v_cndmask_b32 v95, 0, v43, vcc\n" \
    "v_mad_u32_u24 v44, v41, 3, v42\n" \
    "ds_read_b128 v[64:67], v127 offset:26624\n" \
    "v_bfe_u32 v45, v127, 6, 1\n" \
    "v_bfe_u32 v46, v127, 4, 2\n" \
    "s_waitcnt lgkmcnt(8)\n" \
    "ds_read_b128 v[68:71], v127 offset:27648\n" \
    "v_lshl_add_u32 v44, v46, 1, v44\n" \
    "v_bfe_u32 v46, v127, 7, 1\n" \
    "ds_read_b128 a[120:123], v127 offset:28672\n" \
    "v_lshl_add_u32 v44, v44, 1, v46\n" \
    "v_mul_u32_u24 v44, 4352, v44\n" \
    "ds_read_b128 a[124:127], v127 offset:29696\n" \
    "v_lshl_add_u32 v44, v45, 1, v44\n" \
    "v_lshlrev_b32 v45, 2, v45\n" \
    "ds_read_b128 v[80:83], v127 offset:30720\n" \
    "s_lshr_b32 s79, %[u], 4\n" \
    "s_mul_i32 s79, s79, 32 * 4352\n" \
    "s_and_b32 s94, %[u], 15\n" \
    "s_lshl2_add_u32 s79, s94, s79\n" \
    "s_add_i32 s79, s79, 0x3e00fc0\n" \
    "s_lshl3_add_u32 s94, s94, 0x4065f80\n" \
    "s_lshr_b32 s80, s60, 1\n" \
    "s_mul_i32 s76, s80, 49152\n" \
    "s_add_i32 s76, s76, 98304\n" \
    "v_add_u32 v128, s76, v127\n" \
    "v_xor_b32 v130, 0x38000, v128\n" \
    "ds_read_b128 v[84:87], v127 offset:31744\n" \
    "s_lshl_b32 s76, s80, 12\n" \
    "s_add_i32 s76, s76, 83968\n" \
    "v_add_u32 v132, s76, v127\n" \
    "s_add_i32 s92, s64, 16515072\n" \
    "s_add_i32 s93, s92, 0x1000000\n" \
    "s_mov_b32 m0, s95\n" \
    "v_and_b32 v97, 0x380, v127\n" \
    "v_add_u32 v97, v97, v127\n" \
    "s_waitcnt vmcnt(6)\n" \
    "v_readfirstlane_b32 s80, v100\n" \
    "v_cmp_ne_u16 vcc, s91, v99\n" \
    "s_max_i32 s38, s38, s80\n" \
    "s_cmp_lt_i32 s38, s91\n" \
    "s_cbranch_scc1 21f\n" \
    "22:\n" \
    "s_cbranch_vccnz 23f\n" \
    "24:\n" \
    "buffer_load_ushort v113, v44, %[rw], s79 offen sc1\n" \
    "buffer_load_dword v114, v45, %[rw], s94 offen sc1\n" \
    "v_subrev_u32 v96, s29, v98\n" \
    "v_min_i32 v96, 0x4000, v96\n" \
    "v_max_i32 v96, 0, v96\n" \
    "s_waitcnt lgkmcnt(0)\n" \
    "s_barrier\n" \
    "v_max_i32_dpp v96, v96, v96 row_shr:1 row_mask:0xf bank_mask:0xf\n" \
    "ds_read_b64 v[52:53], v103 offset:9216\n" \
    "s_and_b32 s75, s91, 3\n" \
    "v_max_i32_dpp v96, v96, v96 row_shr:2 row_mask:0xf bank_mask:0xf\n" \
    "ds_read_b64 v[54:55], v103 offset:9728\n" \
    "s_lshl_b32 s75, s75, 16\n" \
    "v_max_i32_dpp v96, v96, v96 row_shr:4 row_mask:0xf bank_mask:0xf\n" \
    "ds_read_b64 v[100:101], v103 offset:10240\n" \
    "s_add_i32 s75, s75, s26\n" \
    "v_max_i32_dpp v96, v96, v96 row_shr:8 row_mask:0xf bank_mask:0xf\n" \
    "ds_read_b64 v[102:103], v103 offset:10752\n" \
    "s_add_i32 s69, s75, s74\n" \
    "v_max_i32_dpp v96, v96, v96 row_bcast:15 row_mask:0xa bank_mask:0xf\n" \
    "ds_read_b128 a[0:3], v128 offset:0\n" \
    "s_add_i32 s70, s69, 4096\n" \
    "v_max_i32_dpp v96, v96, v96 row_bcast:31 row_mask:0xc bank_mask:0xf\n" \
    "ds_read_b128 a[16:19], v128 offset:2048\n" \
    "s_add_i32 s71, s75, 32768\n" \
    "v_readlane_b32 s90, v96, 63\n" \
    "s_add_i32 s90, s90, s42\n" \
    "s_mul_i32 s90, s90, s25\n" \
    "s_ashr_i32 s90, s90, 12\n" \
    "s_add_i32 s90, s90, s24\n" \
    "buffer_load_dwordx4 v97, %[rw], s92 offen sc1 lds\n" \
    "s_add_i32 m0, s95, 896\n" \
    "v_mov_b32 v99, 0\n" \
    "buffer_load_dwordx4 v97, %[rw], s92 offen offset:128 sc1 lds\n" \
    "s_add_i32 m0, s95, 8192\n" \
    "v_mov_b32 v96, 0\n" \
    "25:\n" \
    "v_mov_b64 v[48:49], 0\n" \
    "v_mov_b64 v[50:51], 0\n" \
    "s_waitcnt vmcnt(7)\n" \
    "v_mfma_f32_16x16x32_bf16 v[40:43], v[0:3], v[136:139], v[88:91]\n" \
    "ds_read_b128 a[4:7], v128 offset:1024\n" \
    "v_max3_u32 v98, v136, v137, v136\n" \
    "v_mfma_f32_16x16x32_bf16 v[44:47], v[4:7], v[136:139], v[92:95]\n" \
    "ds_read_b128 a[20:23], v128 offset:3072\n" \
    "v_max3_u32 v98, v138, v139, v98\n" \
    "s_waitcnt vmcnt(6)\n" \
    "v_mfma_f32_16x16x32_bf16 v[40:43], v[8:11], v[140:143], v[40:43]\n" \
    "ds_read_b128 a[8:11], v130 offset:0\n" \
    "v_max3_u32 v98, v140, v141, v98\n" \
    "v_mfma_f32_16x16x32_bf16 v[44:47], v[12:15], v[140:143], v[44:47]\n" \
    "ds_read_b128 a[24:27], v130 offset:2048\n" \
    "v_max3_u32 v98, v142, v143, v98\n" \
    "v_smfmac_f32_16x16x64_bf16 v[48:51], v[32:35], v[136:143], v124\n" \
    "ds_read_b128 a[12:15], v130 offset:1024\n" \
    "s_waitcnt vmcnt(5)\n" \
    "v_mfma_f32_16x16x32_bf16 v[40:43], v[16:19], v[144:147], v[40:43]\n" \
    "ds_read_b128 a[28:31], v130 offset:3072\n" \
    "v_max3_u32 v98, v144, v145, v98\n" \
    "v_mfma_f32_16x16x32_bf16 v[44:47], v[20:23], v[144:147], v[44:47]\n" \
    "ds_read_b128 a[32:35], v128 offset:4096\n" \
    "v_max3_u32 v98, v146, v147, v98\n" \
    "s_waitcnt vmcnt(4)\n" \
    "v_smfmac_f32_16x16x64_bf16 v[48:51], v[36:39], v[144:151], v124\n" \
    "ds_read_b128 a[48:51], v128 offset:6144\n" \
    "v_mfma_f32_16x16x32_bf16 v[40:43], v[24:27], v[148:151], v[40:43]\n" \
    "ds_read_b128 a[36:39], v128 offset:5120\n" \
    "v_max3_u32 v98, v148, v149, v98\n" \
    "v_max3_u32 v98, v150, v151, v98\n" \
    "v_mfma_f32_16x16x32_bf16 v[44:47], v[28:31], v[148:151], v[44:47]\n" \
    "v_cmp_eq_u32 vcc, -1, v98\n" \
    "s_add_i32 s44, %[qb], 262144\n" \
    "s_add_i32 s45, s44, 4096\n" \
    "s_waitcnt lgkmcnt(3)\n" \
    "v_add_f32 v48, v48, v50\n" \
    "v_add_f32 v49, v49, v51\n" \
    "s_nop 0\n" \
    "s_cbranch_vccnz 26f\n" \
    "27:\n" \
    "v_max3_f32 v0, v40, v41, v42\n" \
    "v_max3_f32 v1, v43, v44, v45\n" \
    "v_max3_f32 v0, v0, v46, v47\n" \
    "v_max3_f32 v1, v1, v48, v49\n" \
    "v_max_f32 v0, v0, v1\n" \
    "v_max_f32 v1, v0, v1\n" \
    "buffer_load_dwordx4 v112, %[rw], s93 offen sc1 lds\n" \
    "s_add_i32 m0, s95, 8960\n" \
    "s_nop 0\n" \
    "v_permlane16_swap_b32 v0, v1\n" \
    "v_max_f32 v0, v0, v1\n" \
    "v_max_f32 v1, v0, v1\n" \
    "buffer_load_dwordx4 v112, %[rw], s93 offen offset:256 sc1 lds\n" \
    "s_nop 0\n" \
    "v_permlane32_swap_b32 v0, v1\n" \
    "v_max_f32 v0, v0, v1\n" \
    "v_mul_f32 v0, s81, v0\n" \
    "v_fma_f32 v40, v40, s81, -v0\n" \
    "v_fma_f32 v41, v41, s81, -v0\n" \
    "ds_read_b128 v[136:139], v132 offset:0\n" \
    "v_exp_f32 v40, v40\n" \
    "v_exp_f32 v41, v41\n" \
    "v_fma_f32 v42, v42, s81, -v0\n" \
    "ds_read_b128 v[140:143], v132 offset:1024\n" \
    "v_fma_f32 v43, v43, s81, -v0\n" \
    "v_exp_f32 v42, v42\n" \
    "v_exp_f32 v43, v43\n" \
    "ds_read_b128 v[144:147], v132 offset:8192\n" \
    "v_cvt_pk_bf16_f32 v32, v40, v41\n" \
    "v_fma_f32 v44, v44, s81, -v0\n" \
    "v_fma_f32 v45, v45, s81, -v0\n" \
    "ds_read_b128 v[148:151], v132 offset:9216\n" \
    "v_exp_f32 v44, v44\n" \
    "v_exp_f32 v45, v45\n" \
    "v_cvt_pk_bf16_f32 v33, v42, v43\n" \
    "ds_read_b128 a[52:55], v128 offset:7168\n" \
    "v_fma_f32 v46, v46, s81, -v0\n" \
    "v_fma_f32 v47, v47, s81, -v0\n" \
    "v_exp_f32 v46, v46\n" \
    "s_memtime vcc\n" \
    "v_exp_f32 v47, v47\n" \
    "v_cvt_pk_bf16_f32 v34, v44, v45\n" \
    "v_fma_f32 v48, v48, s81, -v0\n" \
    "v_fma_f32 v49, v49, s81, -v0\n" \
    "v_exp_f32 v48, v48\n" \
    "v_exp_f32 v49, v49\n" \
    "v_cvt_pk_bf16_f32 v35, v46, v47\n" \
    "v_cvt_pk_bf16_f32 v98, v48, v49\n" \
    "v_cvt_pk_bf16_f32 v97, v48, v49\n" \
    "s_waitcnt lgkmcnt(0)\n" \
    "v_mfma_f32_16x16x32_bf16 v[36:39], v[120:123], v[32:35], 0\n" \
    "s_sub_i32 s75, s90, vcc_lo\n" \
    "s_cmp_gt_i32 s75, 100\n" \
    "v_mfma_f32_16x16x32_bf16 v[0:3], v[56:59], v[32:35], 0\n" \
    "s_cbranch_scc1 30f\n" \
    "31:\n" \
    "ds_read_b128 a[40:43], v130 offset:4096\n" \
    "v_bfe_u32 v40, v127, 6, 2\n" \
    "v_bfe_u32 v41, v127, 5, 1\n" \
    "v_mfma_f32_16x16x32_bf16 v[4:7], v[60:63], v[32:35], 0\n" \
    "s_mov_b32 m0, s74\n" \
    "s_add_i32 s76, s74, 4096\n" \
    "buffer_load_dwordx4 v125, %[rs], s69 offen sc1 lds\n" \
    "s_mov_b32 m0, s76\n" \
    "v_mfma_f32_16x16x32_bf16 v[8:11], v[64:67], v[32:35], 0\n" \
    "ds_read_b128 a[56:59], v130 offset:6144\n" \
    "v_bfe_u32 v42, v127, 4, 1\n" \
    "v_lshrrev_b32 v43, 8, v127\n" \
    "v_mfma_f32_16x16x32_bf16 v[12:15], v[68:71], v[32:35], 0\n" \
    "buffer_load_dwordx4 v125, %[rs], s70 offen sc1 lds\n" \
    "s_movk_i32 m0, 0x2000\n" \
    "v_mfma_f32_16x16x32_bf16 v[16:19], a[120:123], v[32:35], 0\n" \
    "ds_read_b128 a[44:47], v130 offset:5120\n" \
    "v_lshlrev_b32 v134, 14, v40\n" \
    "v_lshl_add_u32 v134, v41, 12, v134\n" \
    "v_mfma_f32_16x16x32_bf16 v[20:23], a[124:127], v[32:35], 0\n" \
    "s_mov_b32 exec_lo, s27\n" \
    "s_mov_b32 exec_hi, s27\n" \
    "buffer_load_dwordx4 v127, %[rs], s71 offen sc1 lds\n" \
    "s_mov_b64 exec, -1\n" \
    "v_mfma_f32_16x16x32_bf16 v[24:27], v[80:83], v[32:35], 0\n" \
    "ds_read_b128 a[60:63], v130 offset:7168\n" \
    "v_lshl_add_u32 v134, v42, 6, v134\n" \
    "v_lshl_add_u32 v134, v43, 4, v134\n" \
    "v_mfma_f32_16x16x32_bf16 v[28:31], v[84:87], v[32:35], 0\n" \
    "ds_read_b128 a[64:67], v128 offset:8192\n" \
    "v_and_b32 v41, 2, v40\n" \
    "v_add_lshl_u32 v135, v40, v41, 12\n" \
    "v_mfma_f32_16x16x16_bf16 v[36:39], v[120:121], v[98:99], v[36:39]\n" \
    "ds_read_b128 a[80:83], v128 offset:10240\n" \
    "v_bfe_u32 v42, v127, 4, 2\n" \
    "v_lshl_add_u32 v135, v42, 5, v135\n" \
    "v_mfma_f32_16x16x16_bf16 v[0:3], v[52:53], v[98:99], v[0:3]\n" \
    "buffer_load_dwordx4 v[72:75], v112, %[rw], s44 offen offset:0 sc1\n" \
    "v_mfma_f32_16x16x16_bf16 v[4:7], v[52:53], v[96:97], v[4:7]\n" \
    "ds_read_b128 a[68:71], v128 offset:9216\n" \
    "v_lshl_add_u32 v135, v43, 3, v135\n" \
    "v_mfma_f32_16x16x16_bf16 v[8:11], v[54:55], v[98:99], v[8:11]\n" \
    "buffer_load_dwordx4 v[76:79], v112, %[rw], s44 offen offset:2048 sc1\n" \
    "v_mfma_f32_16x16x16_bf16 v[12:15], v[54:55], v[96:97], v[12:15]\n" \
    "ds_read_b128 a[84:87], v128 offset:11264\n" \
    "v_mfma_f32_16x16x16_bf16 v[16:19], v[100:101], v[98:99], v[16:19]\n" \
    "buffer_load_dwordx4 v[104:107], v112, %[rw], s45 offen offset:0 sc1\n" \
    "v_mfma_f32_16x16x16_bf16 v[20:23], v[100:101], v[96:97], v[20:23]\n" \
    "ds_read_b128 a[72:75], v130 offset:8192\n" \
    "v_mfma_f32_16x16x16_bf16 v[24:27], v[102:103], v[98:99], v[24:27]\n" \
    "buffer_load_dwordx4 v[108:111], v112, %[rw], s45 offen offset:2048 sc1\n" \
    "v_mfma_f32_16x16x16_bf16 v[28:31], v[102:103], v[96:97], v[28:31]\n" \
    "ds_read_b128 a[88:91], v130 offset:10240\n" \
    "v_cvt_pk_bf16_f32 v40, v0, v4\n" \
    "v_cvt_pk_bf16_f32 v41, v1, v5\n" \
    "v_cvt_pk_bf16_f32 v42, v2, v6\n" \
    "v_cvt_pk_bf16_f32 v43, v3, v7\n" \
    "v_cvt_pk_bf16_f32 v44, v8, v12\n" \
    "v_cvt_pk_bf16_f32 v45, v9, v13\n" \
    "v_mfma_f32_16x16x32_bf16 v[64:67], v[136:139], v[40:43], 0\n" \
    "s_mov_b32 s46, %[tp]\n" \
    "ds_read_b128 a[76:79], v130 offset:9216\n" \
    "v_cvt_pk_bf16_f32 v46, v10, v14\n" \
    "v_cvt_pk_bf16_f32 v47, v11, v15\n" \
    "v_mfma_f32_16x16x32_bf16 v[56:59], a[0:3], v[40:43], 0\n" \
    "s_mov_b32 s47, %[tx]\n" \
    "ds_read_b128 a[92:95], v130 offset:11264\n" \
    "v_cvt_pk_bf16_f32 v48, v16, v20\n" \
    "v_cvt_pk_bf16_f32 v49, v17, v21\n" \
    "v_mfma_f32_16x16x32_bf16 v[64:67], v[140:143], v[44:47], v[64:67]\n" \
    "ds_read_b128 a[96:99], v128 offset:12288\n" \
    "v_cvt_pk_bf16_f32 v50, v18, v22\n" \
    "v_cvt_pk_bf16_f32 v51, v19, v23\n" \
    "v_mfma_f32_16x16x32_bf16 v[60:63], a[16:19], v[40:43], 0\n" \
    "s_waitcnt lgkmcnt(5)\n" \
    "ds_read_b128 a[112:115], v128 offset:14336\n" \
    "v_cvt_pk_bf16_f32 v52, v24, v28\n" \
    "v_cvt_pk_bf16_f32 v53, v25, v29\n" \
    "v_mfma_f32_16x16x32_bf16 v[64:67], v[144:147], v[48:51], v[64:67]\n" \
    "ds_read_b128 a[100:103], v128 offset:13312\n" \
    "v_cvt_pk_bf16_f32 v54, v26, v30\n" \
    "v_cvt_pk_bf16_f32 v55, v27, v31\n" \
    "v_mfma_f32_16x16x32_bf16 v[56:59], a[4:7], v[44:47], v[56:59]\n" \
    "ds_read_b128 a[116:119], v128 offset:15360\n" \
    "v_rcp_f32 v36, v36\n" \
    "v_mfma_f32_16x16x32_bf16 v[64:67], v[148:151], v[52:55], v[64:67]\n" \
    "ds_read_b128 a[104:107], v130 offset:12288\n" \
    "v_add_u32 v37, s74, v127\n" \
    "v_xor_b32 v38, 0x400, v37\n" \
    "v_mfma_f32_16x16x32_bf16 v[60:63], a[20:23], v[44:47], v[60:63]\n" \
    "ds_read_b128 a[120:123], v130 offset:14336\n" \
    "v_xor_b32 v39, 0x800, v37\n" \
    "v_xor_b32 v132, 0xc00, v37\n" \
    "v_mfma_f32_16x16x32_bf16 v[56:59], a[8:11], v[48:51], v[56:59]\n" \
    "ds_read_b128 a[108:111], v130 offset:13312\n" \
    "v_mul_f32 v64, v64, v36\n" \
    "v_mul_f32 v65, v65, v36\n" \
    "v_mfma_f32_16x16x32_bf16 v[60:63], a[24:27], v[48:51], v[60:63]\n" \
    "ds_read_b128 a[124:127], v130 offset:15360\n" \
    "v_mul_f32 v66, v66, v36\n" \
    "v_mul_f32 v67, v67, v36\n" \
    "v_mfma_f32_16x16x32_bf16 v[56:59], a[12:15], v[52:55], v[56:59]\n" \
    "v_cvt_pk_bf16_f32 v100, v64, v65\n" \
    "v_cvt_pk_bf16_f32 v101, v66, v67\n" \
    "v_mfma_f32_16x16x32_bf16 v[60:63], a[28:31], v[52:55], v[60:63]\n" \
    "buffer_store_dwordx2 v[100:101], v135, %[rw], s47 offen sc1\n" \
    "v_mfma_f32_16x16x32_bf16 v[64:67], a[32:35], v[40:43], 0\n" \
    "v_mul_f32 v56, v56, v36\n" \
    "v_mul_f32 v57, v57, v36\n" \
    "v_mfma_f32_16x16x32_bf16 v[68:71], a[48:51], v[40:43], 0\n" \
    "v_mul_f32 v58, v58, v36\n" \
    "v_mul_f32 v59, v59, v36\n" \
    "v_mfma_f32_16x16x32_bf16 v[64:67], a[36:39], v[44:47], v[64:67]\n" \
    "v_mul_f32 v60, v60, v36\n" \
    "v_mul_f32 v61, v61, v36\n" \
    "v_mfma_f32_16x16x32_bf16 v[68:71], a[52:55], v[44:47], v[68:71]\n" \
    "v_mul_f32 v62, v62, v36\n" \
    "v_mul_f32 v63, v63, v36\n" \
    "v_mfma_f32_16x16x32_bf16 v[64:67], a[40:43], v[48:51], v[64:67]\n" \
    "v_cvt_pk_bf16_f32 v96, v56, v57\n" \
    "v_cvt_pk_bf16_f32 v97, v58, v59\n" \
    "v_mfma_f32_16x16x32_bf16 v[68:71], a[56:59], v[48:51], v[68:71]\n" \
    "v_cvt_pk_bf16_f32 v98, v60, v61\n" \
    "v_cvt_pk_bf16_f32 v99, v62, v63\n" \
    "v_mfma_f32_16x16x32_bf16 v[64:67], a[44:47], v[52:55], v[64:67]\n" \
    "buffer_store_dwordx4 v[96:99], v134, %[rw], s46 offen sc1\n" \
    "s_add_i32 s46, s46, 0x20000\n" \
    "v_mfma_f32_16x16x32_bf16 v[68:71], a[60:63], v[52:55], v[68:71]\n" \
    "v_mov_b64 v[80:81], 0\n" \
    "v_mov_b64 v[82:83], 0\n" \
    "v_mfma_f32_16x16x32_bf16 v[56:59], a[64:67], v[40:43], 0\n" \
    "v_mul_f32 v64, v64, v36\n" \
    "v_mul_f32 v65, v65, v36\n" \
    "v_mfma_f32_16x16x32_bf16 v[60:63], a[80:83], v[40:43], 0\n" \
    "v_mul_f32 v66, v66, v36\n" \
    "v_mul_f32 v67, v67, v36\n" \
    "v_mfma_f32_16x16x32_bf16 v[56:59], a[68:71], v[44:47], v[56:59]\n" \
    "v_mul_f32 v68, v68, v36\n" \
    "v_mul_f32 v69, v69, v36\n" \
    "v_mfma_f32_16x16x32_bf16 v[60:63], a[84:87], v[44:47], v[60:63]\n" \
    "v_mul_f32 v70, v70, v36\n" \
    "v_mul_f32 v71, v71, v36\n" \
    "s_waitcnt lgkmcnt(11)\n" \
    "v_mfma_f32_16x16x32_bf16 v[56:59], a[72:75], v[48:51], v[56:59]\n" \
    "v_cvt_pk_bf16_f32 v100, v64, v65\n" \
    "v_cvt_pk_bf16_f32 v101, v66, v67\n" \
    "s_waitcnt lgkmcnt(10)\n" \
    "v_mfma_f32_16x16x32_bf16 v[60:63], a[88:91], v[48:51], v[60:63]\n" \
    "s_waitcnt vmcnt(6) lgkmcnt(0)\n" \
    "s_barrier\n" \
    "v_cvt_pk_bf16_f32 v102, v68, v69\n" \
    "v_cvt_pk_bf16_f32 v103, v70, v71\n" \
    "v_mfma_f32_16x16x32_bf16 v[56:59], a[76:79], v[52:55], v[56:59]\n" \
    "ds_read_b128 v[0:3], v37 offset:0\n" \
    "buffer_store_dwordx4 v[100:103], v134, %[rw], s46 offen sc1\n" \
    "s_add_i32 s46, s46, 0x20000\n" \
    "v_mfma_f32_16x16x32_bf16 v[60:63], a[92:95], v[52:55], v[60:63]\n" \
    "ds_read_b128 v[4:7], v38 offset:0\n" \
    "v_mov_b64 v[84:85], 0\n" \
    "v_mov_b64 v[86:87], 0\n" \
    "v_mfma_f32_16x16x32_bf16 v[64:67], a[96:99], v[40:43], 0\n" \
    "ds_read_b128 v[8:11], v39 offset:0\n" \
    "v_mul_f32 v56, v56, v36\n" \
    "v_mul_f32 v57, v57, v36\n" \
    "v_mfma_f32_16x16x32_bf16 v[68:71], a[112:115], v[40:43], 0\n" \
    "ds_read_b128 v[12:15], v132 offset:0\n" \
    "v_mul_f32 v58, v58, v36\n" \
    "v_mul_f32 v59, v59, v36\n" \
    "v_mfma_f32_16x16x32_bf16 v[64:67], a[100:103], v[44:47], v[64:67]\n" \
    "ds_read_b128 v[16:19], v37 offset:4096\n" \
    "v_mul_f32 v60, v60, v36\n" \
    "v_mul_f32 v61, v61, v36\n" \
    "v_mfma_f32_16x16x32_bf16 v[68:71], a[116:119], v[44:47], v[68:71]\n" \
    "ds_read_b128 v[20:23], v38 offset:4096\n" \
    "v_mul_f32 v62, v62, v36\n" \
    "v_mul_f32 v63, v63, v36\n" \
    "v_mfma_f32_16x16x32_bf16 v[64:67], a[104:107], v[48:51], v[64:67]\n" \
    "ds_read_b128 v[24:27], v39 offset:4096\n" \
    "v_cvt_pk_bf16_f32 v96, v56, v57\n" \
    "v_cvt_pk_bf16_f32 v97, v58, v59\n" \
    "v_mfma_f32_16x16x32_bf16 v[68:71], a[120:123], v[48:51], v[68:71]\n" \
    "ds_read_b128 v[28:31], v132 offset:4096\n" \
    "v_cvt_pk_bf16_f32 v98, v60, v61\n" \
    "v_cvt_pk_bf16_f32 v99, v62, v63\n" \
    "v_mfma_f32_16x16x32_bf16 v[64:67], a[108:111], v[52:55], v[64:67]\n" \
    "ds_read_b128 v[32:35], v126 offset:0\n" \
    "buffer_store_dwordx4 v[96:99], v134, %[rw], s46 offen sc1\n" \
    "s_add_i32 s46, s46, 0x20000\n" \
    "v_mfma_f32_16x16x32_bf16 v[68:71], a[124:127], v[52:55], v[68:71]\n" \
    "s_waitcnt lgkmcnt(3)\n" \
    "33:\n" \
    "v_smfmac_f32_16x16x64_bf16 v[80:83], v[0:3], a[128:135], v124\n" \
    "v_mul_f32 v64, v64, v36\n" \
    "v_smfmac_f32_16x16x64_bf16 v[80:83], v[4:7], a[144:151], v124\n" \
    "v_mul_f32 v65, v65, v36\n" \
    "v_smfmac_f32_16x16x64_bf16 v[80:83], v[8:11], a[160:167], v124\n" \
    "s_waitcnt lgkmcnt(0)\n" \
    "v_mul_f32 v66, v66, v36\n" \
    "v_smfmac_f32_16x16x64_bf16 v[80:83], v[12:15], a[176:183], v124\n" \
    "ds_read_b128 v[40:43], v127 offset:16384\n" \
    "v_mul_f32 v67, v67, v36\n" \
    "v_smfmac_f32_16x16x64_bf16 v[80:83], v[16:19], a[192:199], v124\n" \
    "ds_read_b128 v[44:47], v127 offset:17408\n" \
    "v_mul_f32 v68, v68, v36\n" \
    "v_smfmac_f32_16x16x64_bf16 v[80:83], v[20:23], a[208:215], v124\n" \
    "ds_read_b128 v[48:51], v127 offset:18432\n" \
    "v_mul_f32 v69, v69, v36\n" \
    "v_smfmac_f32_16x16x64_bf16 v[80:83], v[24:27], a[224:231], v124\n" \
    "ds_read_b128 v[52:55], v127 offset:19456\n" \
    "v_mul_f32 v70, v70, v36\n" \
    "v_mul_f32 v71, v71, v36\n" \
    "v_smfmac_f32_16x16x64_bf16 v[80:83], v[28:31], a[240:247], v124\n" \
    "ds_read_b128 v[56:59], v127 offset:20480\n" \
    "v_cvt_pk_bf16_f32 v100, v64, v65\n" \
    "v_cvt_pk_bf16_f32 v101, v66, v67\n" \
    "v_smfmac_f32_16x16x64_bf16 v[80:83], v[32:35], v[152:159], v124\n" \
    "ds_read_b128 v[60:63], v127 offset:21504\n" \
    "v_cvt_pk_bf16_f32 v102, v68, v69\n" \
    "v_cvt_pk_bf16_f32 v103, v70, v71\n" \
    "v_smfmac_f32_16x16x64_bf16 v[84:87], v[0:3], a[136:143], v124\n" \
    "ds_read_b128 v[64:67], v127 offset:22528\n" \
    "buffer_store_dwordx4 v[100:103], v134, %[rw], s46 offen sc1\n" \
    "v_smfmac_f32_16x16x64_bf16 v[84:87], v[4:7], a[152:159], v124\n" \
    "ds_read_b128 v[68:71], v127 offset:23552\n" \
    "v_add_f32 v80, v80, v82\n" \
    "v_add_f32 v81, v81, v83\n" \
    "v_smfmac_f32_16x16x64_bf16 v[84:87], v[8:11], a[168:175], v124\n" \
    "s_waitcnt vmcnt(5)\n" \
    "v_cmp_u_f32 vcc, v80, v81\n" \
    "v_max3_u32 v128, v72, v73, v72\n" \
    "v_smfmac_f32_16x16x64_bf16 v[84:87], v[12:15], a[184:191], v124\n" \
    "v_max3_u32 v128, v74, v75, v128\n" \
    "v_max3_u32 v128, v76, v77, v128\n" \
    "v_smfmac_f32_16x16x64_bf16 v[84:87], v[16:19], a[200:207], v124\n" \
    "s_cbranch_vccnz 32f\n" \
    "34:\n" \
    "v_max3_u32 v128, v78, v79, v128\n" \
    "v_max3_u32 v128, v104, v105, v128\n" \
    "v_smfmac_f32_16x16x64_bf16 v[84:87], v[20:23], a[216:223], v124\n" \
    "v_max3_u32 v128, v106, v107, v128\n" \
    "v_max3_u32 v128, v108, v109, v128\n" \
    "v_smfmac_f32_16x16x64_bf16 v[84:87], v[24:27], a[232:239], v124\n" \
    "v_max3_u32 v128, v110, v111, v128\n" \
    "v_cmp_eq_u32 vcc, -1, v128\n" \
    "v_smfmac_f32_16x16x64_bf16 v[84:87], v[28:31], a[248:255], v124\n" \
    "s_waitcnt lgkmcnt(0)\n" \
    "v_smfmac_f32_16x16x64_bf16 v[84:87], v[32:35], v[160:167], v124\n" \
    "s_cbranch_vccnz 28f\n" \
    "29:\n" \
    "v_mfma_f32_16x16x32_bf16 v[152:155], v[40:43], v[72:75], v[88:91]\n" \
    "ds_read_b128 a[128:131], v133 offset:0\n" \
    "v_mov_b64 v[96:97], 0\n" \
    "v_mov_b64 v[98:99], 0\n" \
    "v_mfma_f32_16x16x32_bf16 v[156:159], v[44:47], v[72:75], v[92:95]\n" \
    "ds_read_b128 a[132:135], v133 offset:1024\n" \
    "v_add_f32 v84, v84, v86\n" \
    "v_add_f32 v85, v85, v87\n" \
    "v_mfma_f32_16x16x32_bf16 v[152:155], v[48:51], v[76:79], v[152:155]\n" \
    "ds_read_b128 a[136:139], v133 offset:2048\n" \
    "v_cvt_pk_bf16_f32 v80, v80, v84\n" \
    "v_cvt_pk_bf16_f32 v81, v81, v85\n" \
    "v_mfma_f32_16x16x32_bf16 v[156:159], v[52:55], v[76:79], v[156:159]\n" \
    "ds_write2_b32 v129, v80, v81 offset0:0 offset1:144\n" \
    "v_mfma_f32_16x16x32_bf16 v[152:155], v[56:59], v[104:107], v[152:155]\n" \
    "ds_read_b128 a[140:143], v133 offset:3072\n" \
    "v_mov_b64 v[160:161], 0\n" \
    "v_mov_b64 v[162:163], 0\n" \
    "v_mfma_f32_16x16x32_bf16 v[156:159], v[60:63], v[104:107], v[156:159]\n" \
    "ds_read_b128 a[144:147], v133 offset:16384\n" \
    "v_mov_b64 v[100:101], 0\n" \
    "v_mov_b64 v[102:103], 0\n" \
    "v_mfma_f32_16x16x32_bf16 v[152:155], v[64:67], v[108:111], v[152:155]\n" \
    "ds_read_b128 a[148:151], v133 offset:17408\n" \
    "v_lshrrev_b32 v42, 1, v37\n" \
    "v_lshrrev_b32 v43, 1, v127\n" \
    "v_mfma_f32_16x16x32_bf16 v[156:159], v[68:71], v[108:111], v[156:159]\n" \
    "s_waitcnt lgkmcnt(3)\n" \
    "s_barrier\n" \
    "ds_read_b128 v[80:83], v131 offset:0\n" \
    "ds_read_b128 v[84:87], v131 offset:128\n" \
    "v_smfmac_f32_16x16x64_bf16 v[96:99], v[16:19], v[168:175], v124\n" \
    "ds_read_b128 a[152:155], v133 offset:18432\n" \
    "v_mov_b32 v61, 0\n" \
    "v_smfmac_f32_16x16x64_bf16 v[96:99], v[20:23], v[184:191], v124\n" \
    "ds_read_b128 a[156:159], v133 offset:19456\n" \
    "v_max3_f32 v40, v152, v153, v154\n" \
    "v_smfmac_f32_16x16x64_bf16 v[96:99], v[24:27], v[200:207], v124\n" \
    "ds_read_b128 a[160:163], v133 offset:32768\n" \
    "s_waitcnt lgkmcnt(3)\n" \
    "v_max3_f32 v41, v155, v156, v157\n" \
    "v_smfmac_f32_16x16x64_bf16 v[160:163], v[80:83], v[72:79], v124\n" \
    "ds_read_b128 a[164:167], v133 offset:33792\n" \
    "v_max3_f32 v40, v40, v158, v159\n" \
    "v_smfmac_f32_16x16x64_bf16 v[160:163], v[84:87], v[104:111], v124\n" \
    "ds_read_b128 a[168:171], v133 offset:34816\n" \
    "v_mov_b32 v62, 0\n" \
    "v_smfmac_f32_16x16x64_bf16 v[96:99], v[28:31], v[216:223], v124\n" \
    "ds_read_b128 a[172:175], v133 offset:35840\n" \
    "v_smfmac_f32_16x16x64_bf16 v[96:99], v[32:35], v[232:239], v124\n" \
    "ds_read_b128 a[176:179], v133 offset:49152\n" \
    "v_add_f32 v160, v160, v162\n" \
    "v_add_f32 v161, v161, v163\n" \
    "v_max3_f32 v41, v41, v160, v161\n" \
    "v_smfmac_f32_16x16x64_bf16 v[100:103], v[16:19], v[176:183], v124\n" \
    "ds_read_b128 a[180:183], v133 offset:50176\n" \
    "v_max_f32 v40, v40, v41\n" \
    "v_max_f32 v41, v40, v41\n" \
    "v_smfmac_f32_16x16x64_bf16 v[100:103], v[20:23], v[192:199], v124\n" \
    "ds_read_b128 a[192:195], v127 offset:24576\n" \
    "v_permlane16_swap_b32 v40, v41\n" \
    "v_max_f32 v40, v40, v41\n" \
    "v_max_f32 v41, v40, v41\n" \
    "v_smfmac_f32_16x16x64_bf16 v[96:99], v[0:3], a[128:135], v124\n" \
    "ds_read_b128 a[196:199], v127 offset:25600\n" \
    "v_permlane32_swap_b32 v40, v41\n" \
    "v_max_f32 v40, v40, v41\n" \
    "v_mul_f32 v40, s81, v40\n" \
    "v_smfmac_f32_16x16x64_bf16 v[96:99], v[4:7], a[144:151], v124\n" \
    "ds_read_b128 a[200:203], v127 offset:26624\n" \
    "s_waitcnt lgkmcnt(3)\n" \
    "v_fma_f32 v152, v152, s81, -v40\n" \
    "v_fma_f32 v153, v153, s81, -v40\n" \
    "v_exp_f32 v152, v152\n" \
    "v_smfmac_f32_16x16x64_bf16 v[96:99], v[8:11], a[160:167], v124\n" \
    "ds_read_b128 a[204:207], v127 offset:27648\n" \
    "v_exp_f32 v153, v153\n" \
    "v_fma_f32 v154, v154, s81, -v40\n" \
    "v_fma_f32 v155, v155, s81, -v40\n" \
    "v_smfmac_f32_16x16x64_bf16 v[96:99], v[12:15], a[176:183], v124\n" \
    "ds_read_b128 a[208:211], v127 offset:28672\n" \
    "v_exp_f32 v154, v154\n" \
    "v_exp_f32 v155, v155\n" \
    "v_cvt_pk_bf16_f32 v56, v152, v153\n" \
    "v_smfmac_f32_16x16x64_bf16 v[100:103], v[24:27], v[208:215], v124\n" \
    "ds_read_b128 a[212:215], v127 offset:29696\n" \
    "v_fma_f32 v156, v156, s81, -v40\n" \
    "v_fma_f32 v157, v157, s81, -v40\n" \
    "v_exp_f32 v156, v156\n" \
    "v_smfmac_f32_16x16x64_bf16 v[100:103], v[28:31], v[224:231], v124\n" \
    "ds_read_b128 a[216:219], v127 offset:30720\n" \
    "v_exp_f32 v157, v157\n" \
    "v_cvt_pk_bf16_f32 v57, v154, v155\n" \
    "v_fma_f32 v158, v158, s81, -v40\n" \
    "v_smfmac_f32_16x16x64_bf16 v[100:103], v[32:35], v[240:247], v124\n" \
    "ds_read_b128 a[220:223], v127 offset:31744\n" \
    "v_fma_f32 v159, v159, s81, -v40\n" \
    "v_exp_f32 v158, v158\n" \
    "v_exp_f32 v159, v159\n" \
    "v_smfmac_f32_16x16x64_bf16 v[100:103], v[12:15], v[248:255], v124\n" \
    "v_cvt_pk_bf16_f32 v58, v156, v157\n" \
    "v_fma_f32 v160, v160, s81, -v40\n" \
    "v_fma_f32 v161, v161, s81, -v40\n" \
    "v_smfmac_f32_16x16x64_bf16 v[100:103], v[0:3], a[136:143], v124\n" \
    "v_exp_f32 v160, v160\n" \
    "v_exp_f32 v161, v161\n" \
    "v_cvt_pk_bf16_f32 v59, v158, v159\n" \
    "v_smfmac_f32_16x16x64_bf16 v[100:103], v[4:7], a[152:159], v124\n" \
    "v_cvt_pk_bf16_f32 v60, v160, v161\n" \
    "v_cvt_pk_bf16_f32 v63, v160, v161\n" \
    "v_smfmac_f32_16x16x64_bf16 v[100:103], v[8:11], a[168:175], v124\n" \
    "s_waitcnt lgkmcnt(0)\n" \
    "v_mfma_f32_16x16x32_bf16 v[36:39], v[120:123], v[56:59], 0\n" \
    "v_add_f32 v96, v96, v98\n" \
    "v_add_f32 v97, v97, v99\n" \
    "v_mfma_f32_16x16x32_bf16 v[0:3], a[192:195], v[56:59], 0\n" \
    "v_add_f32 v100, v100, v102\n" \
    "v_add_f32 v101, v101, v103\n" \
    "v_mfma_f32_16x16x32_bf16 v[4:7], a[196:199], v[56:59], 0\n" \
    "v_cvt_pk_bf16_f32 v96, v96, v97\n" \
    "v_cvt_pk_bf16_f32 v97, v100, v101\n" \
    "v_mfma_f32_16x16x32_bf16 v[8:11], a[200:203], v[56:59], 0\n" \
    "ds_write_b64 v42, v[96:97] offset:9216\n" \
    "v_mfma_f32_16x16x32_bf16 v[12:15], a[204:207], v[56:59], 0\n" \
    "v_lshrrev_b32 v169, 8, v127\n" \
    "v_and_b32 v170, 2, v169\n" \
    "v_mfma_f32_16x16x32_bf16 v[16:19], a[208:211], v[56:59], 0\n" \
    "v_mad_u32_u24 v169, v170, 3, v169\n" \
    "v_bfe_u32 v168, v127, 6, 1\n" \
    "v_mfma_f32_16x16x32_bf16 v[20:23], a[212:215], v[56:59], 0\n" \
    "v_bfe_u32 v170, v127, 4, 2\n" \
    "v_lshl_add_u32 v169, v170, 1, v169\n" \
    "v_mfma_f32_16x16x32_bf16 v[24:27], a[216:219], v[56:59], 0\n" \
    "s_waitcnt lgkmcnt(0)\n" \
    "s_barrier\n" \
    "ds_read_b64 v[64:65], v43 offset:9216\n" \
    "ds_read_b64 v[66:67], v43 offset:9728\n" \
    "ds_read_b64 v[68:69], v43 offset:10240\n" \
    "ds_read_b64 v[70:71], v43 offset:10752\n" \
    "v_mfma_f32_16x16x32_bf16 v[28:31], a[220:223], v[56:59], 0\n" \
    "v_mul_u32_u24 v168, 1152, v168\n" \
    "v_lshl_add_u32 v168, v169, 2, v168\n" \
    "v_mfma_f32_16x16x16_bf16 v[36:39], v[120:121], v[60:61], v[36:39]\n" \
    "s_waitcnt lgkmcnt(2)\n" \
    "v_mfma_f32_16x16x16_bf16 v[0:3], v[64:65], v[60:61], v[0:3]\n" \
    "v_sub_f32 v169, 1.0, v114\n" \
    "v_lshlrev_b32 v172, 16, v113\n" \
    "v_mfma_f32_16x16x16_bf16 v[4:7], v[64:65], v[62:63], v[4:7]\n" \
    "s_waitcnt lgkmcnt(0)\n" \
    "v_mul_f32 v172, v172, v169\n" \
    "v_and_b32 v170, 16, v127\n" \
    "v_mfma_f32_16x16x16_bf16 v[8:11], v[66:67], v[60:61], v[8:11]\n" \
    "v_mov_b32 v169, 0x3f80\n" \
    "v_lshlrev_b32 v170, v170, v169\n" \
    "v_mfma_f32_16x16x16_bf16 v[12:15], v[66:67], v[62:63], v[12:15]\n" \
    "v_mov_b32_dpp v173, v172 row_ror:8 row_mask:0xf bank_mask:0xf\n" \
    "v_mov_b64 v[176:177], 0\n" \
    "v_mfma_f32_16x16x16_bf16 v[16:19], v[68:69], v[60:61], v[16:19]\n" \
    "v_mov_b64 v[178:179], 0\n" \
    "v_mov_b32_dpp v176, v170 quad_perm:[0,1,2,3] row_mask:0xf bank_mask:0x1\n" \
    "v_mfma_f32_16x16x16_bf16 v[20:23], v[68:69], v[62:63], v[20:23]\n" \
    "v_mov_b32_dpp v177, v170 quad_perm:[0,1,2,3] row_mask:0xf bank_mask:0x2\n" \
    "v_mov_b32_dpp v178, v170 quad_perm:[0,1,2,3] row_mask:0xf bank_mask:0x4\n" \
    "v_mfma_f32_16x16x16_bf16 v[24:27], v[70:71], v[60:61], v[24:27]\n" \
    "v_mov_b32_dpp v179, v170 quad_perm:[0,1,2,3] row_mask:0xf bank_mask:0x8\n" \
    "v_mfma_f32_16x16x16_bf16 v[28:31], v[70:71], v[62:63], v[28:31]\n" \
    "v_cvt_pk_bf16_f32 v40, v0, v4\n" \
    "v_cvt_pk_bf16_f32 v41, v1, v5\n" \
    "v_cvt_pk_bf16_f32 v42, v2, v6\n" \
    "v_cvt_pk_bf16_f32 v43, v3, v7\n" \
    "v_cvt_pk_bf16_f32 v44, v8, v12\n" \
    "v_cvt_pk_bf16_f32 v45, v9, v13\n" \
    "v_mfma_f32_16x16x32_bf16 v[80:83], v[136:139], v[40:43], 0\n" \
    "s_mov_b32 s46, %[tq]\n" \
    "v_cvt_pk_bf16_f32 v46, v10, v14\n" \
    "v_cvt_pk_bf16_f32 v47, v11, v15\n" \
    "v_mfma_f32_16x16x32_bf16 v[72:75], a[0:3], v[40:43], 0\n" \
    "s_add_i32 s47, %[tx], 0x40000\n" \
    "v_cvt_pk_bf16_f32 v48, v16, v20\n" \
    "v_cvt_pk_bf16_f32 v49, v17, v21\n" \
    "v_mfma_f32_16x16x32_bf16 v[80:83], v[140:143], v[44:47], v[80:83]\n" \
    "v_cvt_pk_bf16_f32 v50, v18, v22\n" \
    "v_cvt_pk_bf16_f32 v51, v19, v23\n" \
    "v_mfma_f32_16x16x32_bf16 v[76:79], a[16:19], v[40:43], 0\n" \
    "v_cvt_pk_bf16_f32 v52, v24, v28\n" \
    "v_cvt_pk_bf16_f32 v53, v25, v29\n" \
    "v_mfma_f32_16x16x32_bf16 v[80:83], v[144:147], v[48:51], v[80:83]\n" \
    "v_cvt_pk_bf16_f32 v54, v26, v30\n" \
    "v_cvt_pk_bf16_f32 v55, v27, v31\n" \
    "v_mfma_f32_16x16x32_bf16 v[72:75], a[4:7], v[44:47], v[72:75]\n" \
    "v_rcp_f32 v36, v36\n" \
    "v_mfma_f32_16x16x32_bf16 v[80:83], v[148:151], v[52:55], v[80:83]\n" \
    "s_and_b32 s94, %[u], 15\n" \
    "s_mul_i32 s94, s94, 2304\n" \
    "v_mfma_f32_16x16x32_bf16 v[76:79], a[20:23], v[44:47], v[76:79]\n" \
    "s_andn2_b32 s77, %[u], 15\n" \
    "s_lshl2_add_u32 s94, s77, s94\n" \
    "v_mfma_f32_16x16x32_bf16 v[72:75], a[8:11], v[48:51], v[72:75]\n" \
    "v_mul_f32 v80, v80, v36\n" \
    "v_mul_f32 v81, v81, v36\n" \
    "v_mfma_f32_16x16x32_bf16 v[76:79], a[24:27], v[48:51], v[76:79]\n" \
    "v_mul_f32 v82, v82, v36\n" \
    "v_mul_f32 v83, v83, v36\n" \
    "v_mfma_f32_16x16x32_bf16 v[72:75], a[12:15], v[52:55], v[72:75]\n" \
    "v_cvt_pk_bf16_f32 v100, v80, v81\n" \
    "v_cvt_pk_bf16_f32 v101, v82, v83\n" \
    "v_mfma_f32_16x16x32_bf16 v[76:79], a[28:31], v[52:55], v[76:79]\n" \
    "buffer_store_dwordx2 v[100:101], v135, %[rw], s47 offen sc1\n" \
    "v_mfma_f32_16x16x32_bf16 v[80:83], a[32:35], v[40:43], 0\n" \
    "v_mul_f32 v72, v72, v36\n" \
    "v_mul_f32 v73, v73, v36\n" \
    "v_mfma_f32_16x16x32_bf16 v[84:87], a[48:51], v[40:43], 0\n" \
    "v_mul_f32 v74, v74, v36\n" \
    "v_mul_f32 v75, v75, v36\n" \
    "v_mfma_f32_16x16x32_bf16 v[80:83], a[36:39], v[44:47], v[80:83]\n" \
    "v_mul_f32 v76, v76, v36\n" \
    "v_mul_f32 v77, v77, v36\n" \
    "v_mfma_f32_16x16x32_bf16 v[84:87], a[52:55], v[44:47], v[84:87]\n" \
    "v_mul_f32 v78, v78, v36\n" \
    "v_mul_f32 v79, v79, v36\n" \
    "v_mfma_f32_16x16x32_bf16 v[80:83], a[40:43], v[48:51], v[80:83]\n" \
    "v_cvt_pk_bf16_f32 v96, v72, v73\n" \
    "v_cvt_pk_bf16_f32 v97, v74, v75\n" \
    "v_mfma_f32_16x16x32_bf16 v[84:87], a[56:59], v[48:51], v[84:87]\n" \
    "v_cvt_pk_bf16_f32 v98, v76, v77\n" \
    "v_cvt_pk_bf16_f32 v99, v78, v79\n" \
    "v_mfma_f32_16x16x32_bf16 v[80:83], a[44:47], v[52:55], v[80:83]\n" \
    "buffer_store_dwordx4 v[96:99], v134, %[rw], s46 offen sc1\n" \
    "s_add_i32 s46, s46, 0x20000\n" \
    "v_mfma_f32_16x16x32_bf16 v[84:87], a[60:63], v[52:55], v[84:87]\n" \
    "s_add_i32 s94, s94, 2322432\n" \
    "v_mfma_f32_16x16x32_bf16 v[72:75], a[64:67], v[40:43], 0\n" \
    "v_mul_f32 v80, v80, v36\n" \
    "v_mul_f32 v81, v81, v36\n" \
    "v_mfma_f32_16x16x32_bf16 v[76:79], a[80:83], v[40:43], 0\n" \
    "v_mul_f32 v82, v82, v36\n" \
    "v_mul_f32 v83, v83, v36\n" \
    "v_mfma_f32_16x16x32_bf16 v[72:75], a[68:71], v[44:47], v[72:75]\n" \
    "v_mul_f32 v84, v84, v36\n" \
    "v_mul_f32 v85, v85, v36\n" \
    "v_mfma_f32_16x16x32_bf16 v[76:79], a[84:87], v[44:47], v[76:79]\n" \
    "v_mul_f32 v86, v86, v36\n" \
    "v_mul_f32 v87, v87, v36\n" \
    "v_mfma_f32_16x16x32_bf16 v[72:75], a[72:75], v[48:51], v[72:75]\n" \
    "v_cvt_pk_bf16_f32 v100, v80, v81\n" \
    "v_cvt_pk_bf16_f32 v101, v82, v83\n" \
    "v_mfma_f32_16x16x32_bf16 v[76:79], a[88:91], v[48:51], v[76:79]\n" \
    "v_cvt_pk_bf16_f32 v102, v84, v85\n" \
    "v_cvt_pk_bf16_f32 v103, v86, v87\n" \
    "v_mfma_f32_16x16x32_bf16 v[72:75], a[76:79], v[52:55], v[72:75]\n" \
    "buffer_store_dwordx4 v[100:103], v134, %[rw], s46 offen sc1\n" \
    "s_add_i32 s46, s46, 0x20000\n" \
    "v_mfma_f32_16x16x32_bf16 v[76:79], a[92:95], v[52:55], v[76:79]\n" \
    "v_mfma_f32_16x16x32_bf16 v[80:83], a[96:99], v[40:43], 0\n" \
    "v_mul_f32 v72, v72, v36\n" \
    "v_mul_f32 v73, v73, v36\n" \
    "v_mfma_f32_16x16x32_bf16 v[84:87], a[112:115], v[40:43], 0\n" \
    "v_mul_f32 v74, v74, v36\n" \
    "v_mul_f32 v75, v75, v36\n" \
    "v_mfma_f32_16x16x32_bf16 v[80:83], a[100:103], v[44:47], v[80:83]\n" \
    "v_mul_f32 v76, v76, v36\n" \
    "v_mul_f32 v77, v77, v36\n" \
    "v_mfma_f32_16x16x32_bf16 v[84:87], a[116:119], v[44:47], v[84:87]\n" \
    "v_mul_f32 v78, v78, v36\n" \
    "v_mul_f32 v79, v79, v36\n" \
    "v_mfma_f32_16x16x32_bf16 v[80:83], a[104:107], v[48:51], v[80:83]\n" \
    "v_cvt_pk_bf16_f32 v96, v72, v73\n" \
    "v_cvt_pk_bf16_f32 v97, v74, v75\n" \
    "v_mfma_f32_16x16x32_bf16 v[84:87], a[120:123], v[48:51], v[84:87]\n" \
    "v_cvt_pk_bf16_f32 v98, v76, v77\n" \
    "v_cvt_pk_bf16_f32 v99, v78, v79\n" \
    "v_mfma_f32_16x16x32_bf16 v[80:83], a[108:111], v[52:55], v[80:83]\n" \
    "buffer_store_dwordx4 v[96:99], v134, %[rw], s46 offen sc1\n" \
    "s_add_i32 s46, s46, 0x20000\n" \
    "v_mfma_f32_16x16x32_bf16 v[84:87], a[124:127], v[52:55], v[84:87]\n" \
    "s_memtime vcc\n" \
    "s_nop 2\n" \
    "v_mul_f32 v80, v80, v36\n" \
    "v_mul_f32 v81, v81, v36\n" \
    "v_mul_f32 v82, v82, v36\n" \
    "v_mul_f32 v83, v83, v36\n" \
    "v_mul_f32 v84, v84, v36\n" \
    "v_mul_f32 v85, v85, v36\n" \
    "v_mul_f32 v86, v86, v36\n" \
    "v_mul_f32 v87, v87, v36\n" \
    "v_cvt_pk_bf16_f32 v100, v80, v81\n" \
    "v_cvt_pk_bf16_f32 v101, v82, v83\n" \
    "v_cvt_pk_bf16_f32 v102, v84, v85\n" \
    "v_cvt_pk_bf16_f32 v103, v86, v87\n" \
    "buffer_store_dwordx4 v[100:103], v134, %[rw], s46 offen sc1\n" \
    "s_waitcnt lgkmcnt(0)\n" \
    "s_add_i32 s90, vcc_lo, %[fw]\n" \
    "35:\n" \
    "s_memtime vcc\n" \
    "s_waitcnt lgkmcnt(0)\n" \
    "s_sub_i32 s75, s90, vcc_lo\n" \
    "s_cmp_gt_i32 s75, 0\n" \
    "s_cbranch_scc1 35b\n" \
    "36:\n" \
    "buffer_load_dwordx4 v[0:3], v127, %[rw], %[ua] offen offset:0 sc1\n" \
    "buffer_load_dwordx4 v[4:7], v127, %[rw], %[ua] offen offset:1024 sc1\n" \
    "buffer_load_dwordx4 v[8:11], v127, %[rw], %[ua] offen offset:2048 sc1\n" \
    "buffer_load_dwordx4 v[12:15], v127, %[rw], %[ua] offen offset:3072 sc1\n" \
    "s_waitcnt vmcnt(3)\n" \
    "v_mfma_f32_16x16x32_bf16 v[32:35], v[176:179], v[0:3], 0\n" \
    "v_max3_u32 v40, v0, v1, v0\n" \
    "v_max3_u32 v40, v2, v3, v40\n" \
    "s_waitcnt vmcnt(2)\n" \
    "v_mfma_f32_16x16x32_bf16 v[32:35], v[176:179], v[4:7], v[32:35]\n" \
    "v_max3_u32 v40, v4, v5, v40\n" \
    "v_max3_u32 v40, v6, v7, v40\n" \
    "s_waitcnt vmcnt(1)\n" \
    "v_mfma_f32_16x16x32_bf16 v[32:35], v[176:179], v[8:11], v[32:35]\n" \
    "v_max3_u32 v40, v8, v9, v40\n" \
    "v_max3_u32 v40, v10, v11, v40\n" \
    "s_waitcnt vmcnt(0)\n" \
    "v_mfma_f32_16x16x32_bf16 v[32:35], v[176:179], v[12:15], v[32:35]\n" \
    "v_max3_u32 v40, v12, v13, v40\n" \
    "v_max3_u32 v40, v14, v15, v40\n" \
    "v_cmp_eq_u32 vcc, -1, v40\n" \
    "s_nop 3\n" \
    "s_cbranch_vccnz 38f\n" \
    "39:\n" \
    "v_add_f32_dpp v32, v32, v32 row_ror:8 row_mask:0xf bank_mask:0xf\n" \
    "v_add_f32_dpp v33, v33, v33 row_ror:8 row_mask:0xf bank_mask:0xf\n" \
    "s_mov_b32 exec_lo, 0x00ff00ff\n" \
    "s_mov_b32 exec_hi, 0x00ff00ff\n" \
    "v_fma_f32 v32, v32, v114, v172\n" \
    "v_fma_f32 v33, v33, v114, v173\n" \
    "v_cvt_pk_bf16_f32 v32, v32, v33\n" \
    "buffer_store_dword v32, v168, %[ru], s94 offen sc1\n" \
    "s_mov_b64 exec, -1\n" \
    "s_mov_b32 s36, 63\n" \
    "s_branch 9f\n" \
    "38:\n" \
    "s_add_i32 s39, s39, -1\n" \
    "s_cmp_lt_i32 s39, 0\n" \
    "s_cbranch_scc1 39b\n" \
    "s_add_i32 s30, s30, 0x10000\n" \
    "s_sleep 2\n" \
    "s_branch 36b\n" \
    "21:\n" \
    "s_add_i32 s39, s39, -1\n" \
    "s_cmp_lt_i32 s39, 0\n" \
    "s_cbranch_scc1 22b\n" \
    "buffer_load_dword v100, off, %[rw], s62 sc1\n" \
    "s_waitcnt vmcnt(0)\n" \
    "v_readfirstlane_b32 s38, v100\n" \
    "s_cmp_lt_i32 s38, s91\n" \
    "s_cbranch_scc1 21b\n" \
    "s_branch 22b\n" \
    "23:\n" \
    "s_add_i32 s39, s39, -1\n" \
    "s_cmp_lt_i32 s39, 0\n" \
    "s_cbranch_scc1 24b\n" \
    "s_sleep 4\n" \
    "buffer_load_dwordx2 v[98:99], v113, %[rs], s78 offen sc1\n" \
    "s_waitcnt vmcnt(0)\n" \
    "v_cmp_ne_u16 vcc, s91, v99\n" \
    "s_nop 1\n" \
    "s_cbranch_vccnz 23b\n" \
    "s_branch 24b\n" \
    "26:\n" \
    "s_add_i32 s39, s39, -1\n" \
    "s_cmp_lt_i32 s39, 0\n" \
    "s_cbranch_scc1 27b\n" \
    "s_sleep 4\n" \
    "s_add_i32 s44, %[qb], 0\n" \
    "s_add_i32 s45, s44, 4096\n" \
    "buffer_load_dwordx4 v[136:139], v112, %[rw], s44 offen offset:0 sc1\n" \
    "buffer_load_dwordx4 v[140:143], v112, %[rw], s44 offen offset:2048 sc1\n" \
    "buffer_load_dwordx4 v[144:147], v112, %[rw], s45 offen offset:0 sc1\n" \
    "buffer_load_dwordx4 v[148:151], v112, %[rw], s45 offen offset:2048 sc1\n" \
    "s_waitcnt vmcnt(0)\n" \
    "s_branch 25b\n" \
    "28:\n" \
    "s_add_i32 s39, s39, -1\n" \
    "s_cmp_lt_i32 s39, 0\n" \
    "s_cbranch_scc1 29b\n" \
    "s_sleep 4\n" \
    "s_add_i32 s44, %[qb], 262144\n" \
    "s_add_i32 s45, s44, 4096\n" \
    "buffer_load_dwordx4 v[72:75], v112, %[rw], s44 offen offset:0 sc1\n" \
    "buffer_load_dwordx4 v[76:79], v112, %[rw], s44 offen offset:2048 sc1\n" \
    "buffer_load_dwordx4 v[104:107], v112, %[rw], s45 offen offset:0 sc1\n" \
    "buffer_load_dwordx4 v[108:111], v112, %[rw], s45 offen offset:2048 sc1\n" \
    "s_waitcnt vmcnt(0)\n" \
    "v_max3_u32 v128, v72, v73, v72\n" \
    "v_max3_u32 v128, v74, v75, v128\n" \
    "v_max3_u32 v128, v76, v77, v128\n" \
    "v_max3_u32 v128, v78, v79, v128\n" \
    "v_max3_u32 v128, v104, v105, v128\n" \
    "v_max3_u32 v128, v106, v107, v128\n" \
    "v_max3_u32 v128, v108, v109, v128\n" \
    "v_max3_u32 v128, v110, v111, v128\n" \
    "v_cmp_eq_u32 vcc, -1, v128\n" \
    "s_nop 1\n" \
    "s_cbranch_vccnz 28b\n" \
    "s_branch 29b\n" \
    "30:\n" \
    "s_memtime vcc\n" \
    "s_waitcnt lgkmcnt(0)\n" \
    "s_sub_i32 s75, s90, vcc_lo\n" \
    "s_cmp_gt_i32 s75, 100\n" \
    "s_cbranch_scc1 30b\n" \
    "s_branch 31b\n" \
    "32:\n" \
    "v_max3_u32 v82, v0, v1, v0\n" \
    "v_max3_u32 v82, v2, v3, v82\n" \
    "v_max3_u32 v82, v4, v5, v82\n" \
    "v_max3_u32 v82, v6, v7, v82\n" \
    "v_max3_u32 v82, v8, v9, v82\n" \
    "v_max3_u32 v82, v10, v11, v82\n" \
    "v_max3_u32 v82, v12, v13, v82\n" \
    "v_max3_u32 v82, v14, v15, v82\n" \
    "v_max3_u32 v82, v16, v17, v82\n" \
    "v_max3_u32 v82, v18, v19, v82\n" \
    "v_max3_u32 v82, v20, v21, v82\n" \
    "v_max3_u32 v82, v22, v23, v82\n" \
    "v_max3_u32 v82, v24, v25, v82\n" \
    "v_max3_u32 v82, v26, v27, v82\n" \
    "v_max3_u32 v82, v28, v29, v82\n" \
    "v_max3_u32 v82, v30, v31, v82\n" \
    "v_max3_u32 v82, v32, v33, v82\n" \
    "v_max3_u32 v82, v34, v35, v82\n" \
    "v_cmp_eq_u32 vcc, -1, v82\n" \
    "s_nop 4\n" \
    "s_cbranch_vccz 34b\n" \
    "s_add_i32 s39, s39, -1\n" \
    "s_cmp_lt_i32 s39, 0\n" \
    "s_cbranch_scc1 34b\n" \
    "s_add_i32 s30, s30, 1\n" \
    "s_waitcnt lgkmcnt(0)\n" \
    "s_barrier\n" \
    "s_mov_b32 m0, s74\n" \
    "s_add_i32 s76, s74, 4096\n" \
    "buffer_load_dwordx4 v125, %[rs], s69 offen sc1 lds\n" \
    "s_mov_b32 m0, s76\n" \
    "v_mov_b64 v[80:81], 0\n" \
    "v_mov_b64 v[82:83], 0\n" \
    "buffer_load_dwordx4 v125, %[rs], s70 offen sc1 lds\n" \
    "s_movk_i32 m0, 0x2000\n" \
    "s_mov_b32 exec_lo, s27\n" \
    "s_mov_b32 exec_hi, s27\n" \
    "buffer_load_dwordx4 v127, %[rs], s71 offen sc1 lds\n" \
    "s_mov_b64 exec, -1\n" \
    "v_mov_b64 v[84:85], 0\n" \
    "v_mov_b64 v[86:87], 0\n" \
    "s_waitcnt vmcnt(0)\n" \
    "s_barrier\n" \
    "ds_read_b128 v[0:3], v37 offset:0\n" \
    "ds_read_b128 v[4:7], v38 offset:0\n" \
    "ds_read_b128 v[8:11], v39 offset:0\n" \
    "ds_read_b128 v[12:15], v132 offset:0\n" \
    "ds_read_b128 v[16:19], v37 offset:4096\n" \
    "ds_read_b128 v[20:23], v38 offset:4096\n" \
    "ds_read_b128 v[24:27], v39 offset:4096\n" \
    "ds_read_b128 v[28:31], v132 offset:4096\n" \
    "ds_read_b128 v[32:35], v126 offset:0\n" \
    "s_waitcnt lgkmcnt(8)\n" \
    "v_smfmac_f32_16x16x64_bf16 v[80:83], v[0:3], a[128:135], v124\n" \
    "s_waitcnt lgkmcnt(7)\n" \
    "v_smfmac_f32_16x16x64_bf16 v[80:83], v[4:7], a[144:151], v124\n" \
    "s_waitcnt lgkmcnt(6)\n" \
    "v_smfmac_f32_16x16x64_bf16 v[80:83], v[8:11], a[160:167], v124\n" \
    "s_waitcnt lgkmcnt(5)\n" \
    "v_smfmac_f32_16x16x64_bf16 v[80:83], v[12:15], a[176:183], v124\n" \
    "s_waitcnt lgkmcnt(4)\n" \
    "v_smfmac_f32_16x16x64_bf16 v[80:83], v[16:19], a[192:199], v124\n" \
    "s_waitcnt lgkmcnt(3)\n" \
    "v_smfmac_f32_16x16x64_bf16 v[80:83], v[20:23], a[208:215], v124\n" \
    "s_waitcnt lgkmcnt(2)\n" \
    "v_smfmac_f32_16x16x64_bf16 v[80:83], v[24:27], a[224:231], v124\n" \
    "s_waitcnt lgkmcnt(1)\n" \
    "v_smfmac_f32_16x16x64_bf16 v[80:83], v[28:31], a[240:247], v124\n" \
    "s_waitcnt lgkmcnt(0)\n" \
    "v_smfmac_f32_16x16x64_bf16 v[80:83], v[32:35], v[152:159], v124\n" \
    "v_smfmac_f32_16x16x64_bf16 v[84:87], v[0:3], a[136:143], v124\n" \
    "v_smfmac_f32_16x16x64_bf16 v[84:87], v[4:7], a[152:159], v124\n" \
    "v_smfmac_f32_16x16x64_bf16 v[84:87], v[8:11], a[168:175], v124\n" \
    "v_smfmac_f32_16x16x64_bf16 v[84:87], v[12:15], a[184:191], v124\n" \
    "v_smfmac_f32_16x16x64_bf16 v[84:87], v[16:19], a[200:207], v124\n" \
    "v_add_f32 v80, v80, v82\n" \
    "v_add_f32 v81, v81, v83\n" \
    "s_branch 32b\n" \
    "7:\n" \
    "s_mov_b32 s43, 2\n" \
    "s_nop 7\n" \
    "s_waitcnt vmcnt(0)\n" \
    "9:\n" \
    "s_mov_b32 m0, s68\n" \
    "s_waitcnt lgkmcnt(0)\n" \
    "v_mov_b32 v0, s36\n" \
    "v_mov_b32 v1, s37\n" \
    "v_mov_b32 v2, s38\n" \
    "v_mov_b32 v3, s39\n" \
    "v_mov_b32 v4, s40\n" \
    "v_mov_b32 v5, s41\n" \
    "v_mov_b32 v6, s42\n" \
    "v_mov_b32 v7, s43\n" \
    "v_mov_b32 v8, s28\n" \
    "v_mov_b32 v9, s29\n" \
    "v_mov_b32 v10, s30\n" \
    "v_mov_b32 v11, s31\n" \
    "v_mov_b32 v12, s32\n" \
    "v_mov_b32 v13, s33\n" \
    "v_mov_b32 v14, s34\n" \
    "v_mov_b32 v15, s35\n" \
    "v_mov_b32 v16, s24\n" \
    "v_mov_b32 v17, s25\n" \
    "v_mov_b32 v18, s26\n" \
    "v_mov_b32 v19, s27\n" \
    "v_mov_b32 v20, s84\n" \
    "v_mov_b32 v21, s86\n" \
    "v_mov_b32 v22, s88\n" \
    "v_mov_b32 v23, s84\n" \
    "v_mov_b32 v24, s60\n" \
    "v_mov_b32 v25, s61\n" \
    "v_mov_b32 v26, s62\n" \
    "v_mov_b32 v27, s63\n" \
    "v_mov_b32 v28, s64\n" \
    "v_mov_b32 v29, s65\n" \
    "v_mov_b32 v30, s66\n" \
    "v_mov_b32 v31, s67\n" \
    ".Lstep_end:\n" \
    ""
static_assert(2 * PART_STEP == 32768 && 2 * OFF_CAN == 1703936 && F_ERR == 2 * OFF_CAN + 65536 && MEMB_STRIDE == 32768 && OFF_VF - OFF_KF == 2048 * 4096 * 2 && OFF_BO == 65011712 && OFF_GT == 67518464 && BO_ROW == 4352, "constants of the step block");
#define STEP_CLOBBER "v32", "v33", "v34", "v35", "v36", "v37", "v38", "v39", "v40", "v41", "v42", "v43", "v44", "v45", "v46", "v47", "v48", "v49", "v50", "v51", "v52", "v53", "v54", "v55", "v56", "v57", "v58", "v59", "v60", "v61", "v62", "v63", "v64", "v65", "v66", "v67", "v68", "v69", "v70", "v71", "v72", "v73", "v74", "v75", "v76", "v77", "v78", "v79", "v80", "v81", "v82", "v83", "v84", "v85", "v86", "v87", "v88", "v89", "v90", "v91", "v92", "v93", "v94", "v95", "v98", "v99", "v100", "v101", "v102", "v103", "v104", "v105", "v106", "v107", "v108", "v109", "v110", "v111", "v112", "v113", "v114", "s24", "s25", "s26", "s27", "s28", "s29", "s30", "s31", "s32", "s33", "s34", "s35", "s36", "s37", "s38", "s39", "s40", "s41", "s42", "s43", "s44", "s45", "s46", "s47", "s48", "s49", "s50", "s51", "s52", "s53", "s54", "s55", "s56", "s57", "s58", "s59", "s60", "s61", "s62", "s63", "s64", "s65", "s66", "s67", "s68", "s69", "s70", "s71", "s72", "s73", "s74", "s75", "s76", "s77", "s78", "s79", "s80", "s81", "s82", "s83", "s84", "s85", "s86", "s87", "s88", "s89", "s90", "s91", "s92", "s93", "s94", "s95", "s96", "s97", "vcc", "scc", "memory"
#define OUT16_ASM \
    "s_mov_b32 exec_lo, %[tm]\n" \
    "s_mov_b32 exec_hi, 0\n" \
    "buffer_load_dword v79, %[tv], %[rw], %[so] offen sc1\n" \
    "s_mov_b64 exec, -1\n" \
    "s_add_i32 s44, %[so], 4096\n" \
    "s_add_i32 s45, %[so], 8192\n" \
    "s_add_i32 s46, %[so], 12288\n" \
    "buffer_load_dwordx4 v[16:19], %[vo], %[rw], %[so] offen offset:0 sc1\n" \
    "ds_read_b128 v[80:83], %[wl] offset:0\n" \
    "ds_read_b128 v[84:87], %[wl] offset:1024\n" \
    "s_nop 7\n" \
    "v_mov_b32 v104, 0\n" \
    "buffer_load_dwordx4 v[20:23], %[vo], %[rw], %[so] offen offset:256 sc1\n" \
    "s_nop 7\n" \
    "s_nop 3\n" \
    "buffer_load_dwordx4 v[24:27], %[vo], %[rw], %[so] offen offset:1024 sc1\n" \
    "s_nop 7\n" \
    "s_nop 3\n" \
    "buffer_load_dwordx4 v[28:31], %[vo], %[rw], %[so] offen offset:1280 sc1\n" \
    "s_nop 7\n" \
    "s_nop 3\n" \
    "buffer_load_dwordx4 v[32:35], %[vo], %[rw], %[so] offen offset:2048 sc1\n" \
    "s_nop 7\n" \
    "s_nop 3\n" \
    "buffer_load_dwordx4 v[36:39], %[vo], %[rw], %[so] offen offset:2304 sc1\n" \
    "s_nop 7\n" \
    "s_nop 3\n" \
    "buffer_load_dwordx4 v[40:43], %[vo], %[rw], %[so] offen offset:3072 sc1\n" \
    "s_nop 7\n" \
    "s_nop 3\n" \
    "buffer_load_dwordx4 v[44:47], %[vo], %[rw], %[so] offen offset:3328 sc1\n" \
    "s_nop 7\n" \
    "s_nop 3\n" \
    "buffer_load_dwordx4 v[48:51], %[vo], %[rw], s44 offen offset:0 sc1\n" \
    "s_nop 7\n" \
    "s_nop 3\n" \
    "buffer_load_dwordx4 v[52:55], %[vo], %[rw], s44 offen offset:256 sc1\n" \
    "s_nop 7\n" \
    "s_nop 3\n" \
    "buffer_load_dwordx4 v[56:59], %[vo], %[rw], s44 offen offset:1024 sc1\n" \
    "s_nop 7\n" \
    "s_nop 3\n" \
    "buffer_load_dwordx4 v[60:63], %[vo], %[rw], s44 offen offset:1280 sc1\n" \
    "ds_read_b128 v[88:91], %[wl] offset:2048\n" \
    "s_waitcnt vmcnt(11)\n" \
    "v_mfma_f32_16x16x32_bf16 v[0:3], a[0:3], v[16:19], 0\n" \
    "v_max3_u32 v104, v104, v16, v17\n" \
    "v_mfma_f32_16x16x32_bf16 v[4:7], a[128:131], v[16:19], 0\n" \
    "v_max3_u32 v104, v104, v18, v19\n" \
    "v_mfma_f32_16x16x32_bf16 v[8:11], v[128:131], v[16:19], 0\n" \
    "s_waitcnt lgkmcnt(2)\n" \
    "v_mfma_f32_16x16x32_bf16 v[12:15], v[80:83], v[16:19], 0\n" \
    "buffer_load_dwordx4 v[64:67], %[vo], %[rw], s44 offen offset:2048 sc1\n" \
    "ds_read_b128 v[80:83], %[wl] offset:3072\n" \
    "s_waitcnt vmcnt(11)\n" \
    "v_mfma_f32_16x16x32_bf16 v[0:3], a[4:7], v[20:23], v[0:3]\n" \
    "v_max3_u32 v104, v104, v20, v21\n" \
    "v_mfma_f32_16x16x32_bf16 v[4:7], a[132:135], v[20:23], v[4:7]\n" \
    "v_max3_u32 v104, v104, v22, v23\n" \
    "v_mfma_f32_16x16x32_bf16 v[8:11], v[132:135], v[20:23], v[8:11]\n" \
    "s_waitcnt lgkmcnt(2)\n" \
    "v_mfma_f32_16x16x32_bf16 v[12:15], v[84:87], v[20:23], v[12:15]\n" \
    "buffer_load_dwordx4 v[68:71], %[vo], %[rw], s44 offen offset:2304 sc1\n" \
    "ds_read_b128 v[84:87], %[wl] offset:4096\n" \
    "s_waitcnt vmcnt(11)\n" \
    "v_mfma_f32_16x16x32_bf16 v[0:3], a[8:11], v[24:27], v[0:3]\n" \
    "v_max3_u32 v104, v104, v24, v25\n" \
    "v_mfma_f32_16x16x32_bf16 v[4:7], a[136:139], v[24:27], v[4:7]\n" \
    "v_max3_u32 v104, v104, v26, v27\n" \
    "v_mfma_f32_16x16x32_bf16 v[8:11], v[136:139], v[24:27], v[8:11]\n" \
    "s_waitcnt lgkmcnt(2)\n" \
    "v_mfma_f32_16x16x32_bf16 v[12:15], v[88:91], v[24:27], v[12:15]\n" \
    "buffer_load_dwordx4 v[72:75], %[vo], %[rw], s44 offen offset:3072 sc1\n" \
    "ds_read_b128 v[88:91], %[wl] offset:5120\n" \
    "s_waitcnt vmcnt(11)\n" \
    "v_mfma_f32_16x16x32_bf16 v[0:3], a[12:15], v[28:31], v[0:3]\n" \
    "v_max3_u32 v104, v104, v28, v29\n" \
    "v_mfma_f32_16x16x32_bf16 v[4:7], a[140:143], v[28:31], v[4:7]\n" \
    "v_max3_u32 v104, v104, v30, v31\n" \
    "v_mfma_f32_16x16x32_bf16 v[8:11], v[140:143], v[28:31], v[8:11]\n" \
    "s_waitcnt lgkmcnt(2)\n" \
    "v_mfma_f32_16x16x32_bf16 v[12:15], v[80:83], v[28:31], v[12:15]\n" \
    "buffer_load_dwordx4 v[76:79], %[vo], %[rw], s44 offen offset:3328 sc1\n" \
    "ds_read_b128 v[80:83], %[wl] offset:6144\n" \
    "s_waitcnt vmcnt(11)\n" \
    "v_mfma_f32_16x16x32_bf16 v[0:3], a[16:19], v[32:35], v[0:3]\n" \
    "v_max3_u32 v104, v104, v32, v33\n" \
    "v_mfma_f32_16x16x32_bf16 v[4:7], a[144:147], v[32:35], v[4:7]\n" \
    "v_max3_u32 v104, v104, v34, v35\n" \
    "v_mfma_f32_16x16x32_bf16 v[8:11], v[144:147], v[32:35], v[8:11]\n" \
    "s_waitcnt lgkmcnt(2)\n" \
    "v_mfma_f32_16x16x32_bf16 v[12:15], v[84:87], v[32:35], v[12:15]\n" \
    "buffer_load_dwordx4 v[16:19], %[vo], %[rw], s45 offen offset:0 sc1\n" \
    "ds_read_b128 v[84:87], %[wl] offset:7168\n" \
    "s_waitcnt vmcnt(11)\n" \
    "v_mfma_f32_16x16x32_bf16 v[0:3], a[20:23], v[36:39], v[0:3]\n" \
    "v_max3_u32 v104, v104, v36, v37\n" \
    "v_mfma_f32_16x16x32_bf16 v[4:7], a[148:151], v[36:39], v[4:7]\n" \
    "v_max3_u32 v104, v104, v38, v39\n" \
    "v_mfma_f32_16x16x32_bf16 v[8:11], v[148:151], v[36:39], v[8:11]\n" \
    "s_waitcnt lgkmcnt(2)\n" \
    "v_mfma_f32_16x16x32_bf16 v[12:15], v[88:91], v[36:39], v[12:15]\n" \
    "buffer_load_dwordx4 v[20:23], %[vo], %[rw], s45 offen offset:256 sc1\n" \
    "ds_read_b128 v[88:91], %[wl] offset:8192\n" \
    "s_waitcnt vmcnt(11)\n" \
    "v_mfma_f32_16x16x32_bf16 v[0:3], a[24:27], v[40:43], v[0:3]\n" \
    "v_max3_u32 v104, v104, v40, v41\n" \
    "v_mfma_f32_16x16x32_bf16 v[4:7], a[152:155], v[40:43], v[4:7]\n" \
    "v_max3_u32 v104, v104, v42, v43\n" \
    "v_mfma_f32_16x16x32_bf16 v[8:11], v[152:155], v[40:43], v[8:11]\n" \
    "s_waitcnt lgkmcnt(2)\n" \
    "v_mfma_f32_16x16x32_bf16 v[12:15], v[80:83], v[40:43], v[12:15]\n" \
    "buffer_load_dwordx4 v[24:27], %[vo], %[rw], s45 offen offset:1024 sc1\n" \
    "ds_read_b128 v[80:83], %[wl] offset:9216\n" \
    "s_waitcnt vmcnt(11)\n" \
    "v_mfma_f32_16x16x32_bf16 v[0:3], a[28:31], v[44:47], v[0:3]\n" \
    "v_max3_u32 v104, v104, v44, v45\n" \
    "v_mfma_f32_16x16x32_bf16 v[4:7], a[156:159], v[44:47], v[4:7]\n" \
    "v_max3_u32 v104, v104, v46, v47\n" \
    "v_mfma_f32_16x16x32_bf16 v[8:11], v[156:159], v[44:47], v[8:11]\n" \
    "s_waitcnt lgkmcnt(2)\n" \
    "v_mfma_f32_16x16x32_bf16 v[12:15], v[84:87], v[44:47], v[12:15]\n" \
    "buffer_load_dwordx4 v[28:31], %[vo], %[rw], s45 offen offset:1280 sc1\n" \
    "ds_read_b128 v[84:87], %[wl] offset:10240\n" \
    "s_waitcnt vmcnt(11)\n" \
    "v_mfma_f32_16x16x32_bf16 v[0:3], a[32:35], v[48:51], v[0:3]\n" \
    "v_max3_u32 v104, v104, v48, v49\n" \
    "v_mfma_f32_16x16x32_bf16 v[4:7], a[160:163], v[48:51], v[4:7]\n" \
    "v_max3_u32 v104, v104, v50, v51\n" \
    "v_mfma_f32_16x16x32_bf16 v[8:11], v[160:163], v[48:51], v[8:11]\n" \
    "s_waitcnt lgkmcnt(2)\n" \
    "v_mfma_f32_16x16x32_bf16 v[12:15], v[88:91], v[48:51], v[12:15]\n" \
    "buffer_load_dwordx4 v[32:35], %[vo], %[rw], s45 offen offset:2048 sc1\n" \
    "ds_read_b128 v[88:91], %[wl] offset:11264\n" \
    "s_waitcnt vmcnt(11)\n" \
    "v_mfma_f32_16x16x32_bf16 v[0:3], a[36:39], v[52:55], v[0:3]\n" \
    "v_max3_u32 v104, v104, v52, v53\n" \
    "v_mfma_f32_16x16x32_bf16 v[4:7], a[164:167], v[52:55], v[4:7]\n" \
    "v_max3_u32 v104, v104, v54, v55\n" \
    "v_mfma_f32_16x16x32_bf16 v[8:11], v[164:167], v[52:55], v[8:11]\n" \
    "s_waitcnt lgkmcnt(2)\n" \
    "v_mfma_f32_16x16x32_bf16 v[12:15], v[80:83], v[52:55], v[12:15]\n" \
    "buffer_load_dwordx4 v[36:39], %[vo], %[rw], s45 offen offset:2304 sc1\n" \
    "ds_read_b128 v[80:83], %[wl] offset:12288\n" \
    "s_waitcnt vmcnt(11)\n" \
    "v_mfma_f32_16x16x32_bf16 v[0:3], a[40:43], v[56:59], v[0:3]\n" \
    "v_max3_u32 v104, v104, v56, v57\n" \
    "v_mfma_f32_16x16x32_bf16 v[4:7], a[168:171], v[56:59], v[4:7]\n" \
    "v_max3_u32 v104, v104, v58, v59\n" \
    "v_mfma_f32_16x16x32_bf16 v[8:11], v[168:171], v[56:59], v[8:11]\n" \
    "s_waitcnt lgkmcnt(2)\n" \
    "v_mfma_f32_16x16x32_bf16 v[12:15], v[84:87], v[56:59], v[12:15]\n" \
    "buffer_load_dwordx4 v[40:43], %[vo], %[rw], s45 offen offset:3072 sc1\n" \
    "ds_read_b128 v[84:87], %[wl] offset:13312\n" \
    "s_waitcnt vmcnt(11)\n" \
    "v_mfma_f32_16x16x32_bf16 v[0:3], a[44:47], v[60:63], v[0:3]\n" \
    "v_max3_u32 v104, v104, v60, v61\n" \
    "v_mfma_f32_16x16x32_bf16 v[4:7], a[172:175], v[60:63], v[4:7]\n" \
    "v_max3_u32 v104, v104, v62, v63\n" \
    "v_mfma_f32_16x16x32_bf16 v[8:11], v[172:175], v[60:63], v[8:11]\n" \
    "s_waitcnt lgkmcnt(2)\n" \
    "v_mfma_f32_16x16x32_bf16 v[12:15], v[88:91], v[60:63], v[12:15]\n" \
    "buffer_load_dwordx4 v[44:47], %[vo], %[rw], s45 offen offset:3328 sc1\n" \
    "ds_read_b128 v[88:91], %[wl] offset:14336\n" \
    "s_waitcnt vmcnt(11)\n" \
    "v_mfma_f32_16x16x32_bf16 v[0:3], a[48:51], v[64:67], v[0:3]\n" \
    "v_max3_u32 v104, v104, v64, v65\n" \
    "v_mfma_f32_16x16x32_bf16 v[4:7], a[176:179], v[64:67], v[4:7]\n" \
    "v_max3_u32 v104, v104, v66, v67\n" \
    "v_mfma_f32_16x16x32_bf16 v[8:11], v[176:179], v[64:67], v[8:11]\n" \
    "s_waitcnt lgkmcnt(2)\n" \
    "v_mfma_f32_16x16x32_bf16 v[12:15], v[80:83], v[64:67], v[12:15]\n" \
    "buffer_load_dwordx4 v[48:51], %[vo], %[rw], s46 offen offset:0 sc1\n" \
    "ds_read_b128 v[80:83], %[wl] offset:15360\n" \
    "s_waitcnt vmcnt(11)\n" \
    "v_mfma_f32_16x16x32_bf16 v[0:3], a[52:55], v[68:71], v[0:3]\n" \
    "v_max3_u32 v104, v104, v68, v69\n" \
    "v_mfma_f32_16x16x32_bf16 v[4:7], a[180:183], v[68:71], v[4:7]\n" \
    "v_max3_u32 v104, v104, v70, v71\n" \
    "v_mfma_f32_16x16x32_bf16 v[8:11], v[180:183], v[68:71], v[8:11]\n" \
    "s_waitcnt lgkmcnt(2)\n" \
    "v_mfma_f32_16x16x32_bf16 v[12:15], v[84:87], v[68:71], v[12:15]\n" \
    "buffer_load_dwordx4 v[52:55], %[vo], %[rw], s46 offen offset:256 sc1\n" \
    "ds_read_b128 v[84:87], %[wl] offset:16384\n" \
    "s_waitcnt vmcnt(11)\n" \
    "v_mfma_f32_16x16x32_bf16 v[0:3], a[56:59], v[72:75], v[0:3]\n" \
    "v_max3_u32 v104, v104, v72, v73\n" \
    "v_mfma_f32_16x16x32_bf16 v[4:7], a[184:187], v[72:75], v[4:7]\n" \
    "v_max3_u32 v104, v104, v74, v75\n" \
    "v_mfma_f32_16x16x32_bf16 v[8:11], v[184:187], v[72:75], v[8:11]\n" \
    "s_waitcnt lgkmcnt(2)\n" \
    "v_mfma_f32_16x16x32_bf16 v[12:15], v[88:91], v[72:75], v[12:15]\n" \
    "buffer_load_dwordx4 v[56:59], %[vo], %[rw], s46 offen offset:1024 sc1\n" \
    "ds_read_b128 v[88:91], %[wl] offset:17408\n" \
    "s_waitcnt vmcnt(11)\n" \
    "v_mfma_f32_16x16x32_bf16 v[0:3], a[60:63], v[76:79], v[0:3]\n" \
    "v_max3_u32 v104, v104, v76, v77\n" \
    "v_mfma_f32_16x16x32_bf16 v[4:7], a[188:191], v[76:79], v[4:7]\n" \
    "v_max3_u32 v104, v104, v78, v79\n" \
    "v_mfma_f32_16x16x32_bf16 v[8:11], v[188:191], v[76:79], v[8:11]\n" \
    "s_waitcnt lgkmcnt(2)\n" \
    "v_mfma_f32_16x16x32_bf16 v[12:15], v[80:83], v[76:79], v[12:15]\n" \
    "buffer_load_dwordx4 v[60:63], %[vo], %[rw], s46 offen offset:1280 sc1\n" \
    "ds_read_b128 v[80:83], %[wl] offset:18432\n" \
    "s_waitcnt vmcnt(11)\n" \
    "v_mfma_f32_16x16x32_bf16 v[0:3], a[64:67], v[16:19], v[0:3]\n" \
    "v_max3_u32 v104, v104, v16, v17\n" \
    "v_mfma_f32_16x16x32_bf16 v[4:7], a[192:195], v[16:19], v[4:7]\n" \
    "v_max3_u32 v104, v104, v18, v19\n" \
    "v_mfma_f32_16x16x32_bf16 v[8:11], v[192:195], v[16:19], v[8:11]\n" \
    "s_waitcnt lgkmcnt(2)\n" \
    "v_mfma_f32_16x16x32_bf16 v[12:15], v[84:87], v[16:19], v[12:15]\n" \
    "buffer_load_dwordx4 v[64:67], %[vo], %[rw], s46 offen offset:2048 sc1\n" \
    "ds_read_b128 v[84:87], %[wl] offset:19456\n" \
    "s_waitcnt vmcnt(11)\n" \
    "v_mfma_f32_16x16x32_bf16 v[0:3], a[68:71], v[20:23], v[0:3]\n" \
    "v_max3_u32 v104, v104, v20, v21\n" \
    "v_mfma_f32_16x16x32_bf16 v[4:7], a[196:199], v[20:23], v[4:7]\n" \
    "v_max3_u32 v104, v104, v22, v23\n" \
    "v_mfma_f32_16x16x32_bf16 v[8:11], v[196:199], v[20:23], v[8:11]\n" \
    "s_waitcnt lgkmcnt(2)\n" \
    "v_mfma_f32_16x16x32_bf16 v[12:15], v[88:91], v[20:23], v[12:15]\n" \
    "buffer_load_dwordx4 v[68:71], %[vo], %[rw], s46 offen offset:2304 sc1\n" \
    "ds_read_b128 v[88:91], %[wl] offset:20480\n" \
    "s_waitcnt vmcnt(11)\n" \
    "v_mfma_f32_16x16x32_bf16 v[0:3], a[72:75], v[24:27], v[0:3]\n" \
    "v_max3_u32 v104, v104, v24, v25\n" \
    "v_mfma_f32_16x16x32_bf16 v[4:7], a[200:203], v[24:27], v[4:7]\n" \
    "v_max3_u32 v104, v104, v26, v27\n" \
    "v_mfma_f32_16x16x32_bf16 v[8:11], v[200:203], v[24:27], v[8:11]\n" \
    "s_waitcnt lgkmcnt(2)\n" \
    "v_mfma_f32_16x16x32_bf16 v[12:15], v[80:83], v[24:27], v[12:15]\n" \
    "buffer_load_dwordx4 v[72:75], %[vo], %[rw], s46 offen offset:3072 sc1\n" \
    "ds_read_b128 v[80:83], %[wl] offset:21504\n" \
    "s_waitcnt vmcnt(11)\n" \
    "v_mfma_f32_16x16x32_bf16 v[0:3], a[76:79], v[28:31], v[0:3]\n" \
    "v_max3_u32 v104, v104, v28, v29\n" \
    "v_mfma_f32_16x16x32_bf16 v[4:7], a[204:207], v[28:31], v[4:7]\n" \
    "v_max3_u32 v104, v104, v30, v31\n" \
    "v_mfma_f32_16x16x32_bf16 v[8:11], v[204:207], v[28:31], v[8:11]\n" \
    "s_waitcnt lgkmcnt(2)\n" \
    "v_mfma_f32_16x16x32_bf16 v[12:15], v[84:87], v[28:31], v[12:15]\n" \
    "buffer_load_dwordx4 v[76:79], %[vo], %[rw], s46 offen offset:3328 sc1\n" \
    "ds_read_b128 v[84:87], %[wl] offset:22528\n" \
    "s_waitcnt vmcnt(11)\n" \
    "v_mfma_f32_16x16x32_bf16 v[0:3], a[80:83], v[32:35], v[0:3]\n" \
    "v_max3_u32 v104, v104, v32, v33\n" \
    "v_mfma_f32_16x16x32_bf16 v[4:7], a[208:211], v[32:35], v[4:7]\n" \
    "v_max3_u32 v104, v104, v34, v35\n" \
    "v_mfma_f32_16x16x32_bf16 v[8:11], v[208:211], v[32:35], v[8:11]\n" \
    "s_waitcnt lgkmcnt(2)\n" \
    "v_mfma_f32_16x16x32_bf16 v[12:15], v[88:91], v[32:35], v[12:15]\n" \
    "buffer_load_dwordx2 v[106:107], %[bv], %[rw], %[bs] offen sc1\n" \
    "ds_read_b128 v[88:91], %[wl] offset:23552\n" \
    "s_waitcnt vmcnt(11)\n" \
    "v_mfma_f32_16x16x32_bf16 v[0:3], a[84:87], v[36:39], v[0:3]\n" \
    "v_max3_u32 v104, v104, v36, v37\n" \
    "v_mfma_f32_16x16x32_bf16 v[4:7], a[212:215], v[36:39], v[4:7]\n" \
    "v_max3_u32 v104, v104, v38, v39\n" \
    "v_mfma_f32_16x16x32_bf16 v[8:11], v[212:215], v[36:39], v[8:11]\n" \
    "s_waitcnt lgkmcnt(2)\n" \
    "v_mfma_f32_16x16x32_bf16 v[12:15], v[80:83], v[36:39], v[12:15]\n" \
    "buffer_load_dword v108, %[gv], %[rw], %[gs] offen sc1\n" \
    "ds_read_b128 v[80:83], %[wl] offset:24576\n" \
    "s_waitcnt vmcnt(11)\n" \
    "v_mfma_f32_16x16x32_bf16 v[0:3], a[88:91], v[40:43], v[0:3]\n" \
    "v_max3_u32 v104, v104, v40, v41\n" \
    "v_mfma_f32_16x16x32_bf16 v[4:7], a[216:219], v[40:43], v[4:7]\n" \
    "v_max3_u32 v104, v104, v42, v43\n" \
    "v_mfma_f32_16x16x32_bf16 v[8:11], v[216:219], v[40:43], v[8:11]\n" \
    "s_waitcnt lgkmcnt(2)\n" \
    "v_mfma_f32_16x16x32_bf16 v[12:15], v[84:87], v[40:43], v[12:15]\n" \
    "buffer_load_dword v105, %[sv], %[rw], %[ss] offen sc1\n" \
    "ds_read_b128 v[84:87], %[wl] offset:25600\n" \
    "s_waitcnt vmcnt(11)\n" \
    "v_mfma_f32_16x16x32_bf16 v[0:3], a[92:95], v[44:47], v[0:3]\n" \
    "v_max3_u32 v104, v104, v44, v45\n" \
    "v_mfma_f32_16x16x32_bf16 v[4:7], a[220:223], v[44:47], v[4:7]\n" \
    "v_max3_u32 v104, v104, v46, v47\n" \
    "v_mfma_f32_16x16x32_bf16 v[8:11], v[220:223], v[44:47], v[8:11]\n" \
    "s_waitcnt lgkmcnt(2)\n" \
    "v_mfma_f32_16x16x32_bf16 v[12:15], v[88:91], v[44:47], v[12:15]\n" \
    "ds_read_b128 v[88:91], %[wl] offset:26624\n" \
    "s_waitcnt vmcnt(10)\n" \
    "v_mfma_f32_16x16x32_bf16 v[0:3], a[96:99], v[48:51], v[0:3]\n" \
    "v_max3_u32 v104, v104, v48, v49\n" \
    "v_mfma_f32_16x16x32_bf16 v[4:7], a[224:227], v[48:51], v[4:7]\n" \
    "v_max3_u32 v104, v104, v50, v51\n" \
    "v_mfma_f32_16x16x32_bf16 v[8:11], v[224:227], v[48:51], v[8:11]\n" \
    "s_waitcnt lgkmcnt(2)\n" \
    "v_mfma_f32_16x16x32_bf16 v[12:15], v[80:83], v[48:51], v[12:15]\n" \
    "ds_read_b128 v[80:83], %[wl] offset:27648\n" \
    "s_waitcnt vmcnt(9)\n" \
    "v_mfma_f32_16x16x32_bf16 v[0:3], a[100:103], v[52:55], v[0:3]\n" \
    "v_max3_u32 v104, v104, v52, v53\n" \
    "v_mfma_f32_16x16x32_bf16 v[4:7], a[228:231], v[52:55], v[4:7]\n" \
    "v_max3_u32 v104, v104, v54, v55\n" \
    "v_mfma_f32_16x16x32_bf16 v[8:11], v[228:231], v[52:55], v[8:11]\n" \
    "s_waitcnt lgkmcnt(2)\n" \
    "v_mfma_f32_16x16x32_bf16 v[12:15], v[84:87], v[52:55], v[12:15]\n" \
    "ds_read_b128 v[84:87], %[wl] offset:28672\n" \
    "s_waitcnt vmcnt(8)\n" \
    "v_mfma_f32_16x16x32_bf16 v[0:3], a[104:107], v[56:59], v[0:3]\n" \
    "v_max3_u32 v104, v104, v56, v57\n" \
    "v_mfma_f32_16x16x32_bf16 v[4:7], a[232:235], v[56:59], v[4:7]\n" \
    "v_max3_u32 v104, v104, v58, v59\n" \
    "v_mfma_f32_16x16x32_bf16 v[8:11], v[232:235], v[56:59], v[8:11]\n" \
    "s_waitcnt lgkmcnt(2)\n" \
    "v_mfma_f32_16x16x32_bf16 v[12:15], v[88:91], v[56:59], v[12:15]\n" \
    "ds_read_b128 v[88:91], %[wl] offset:29696\n" \
    "s_waitcnt vmcnt(7)\n" \
    "v_mfma_f32_16x16x32_bf16 v[0:3], a[108:111], v[60:63], v[0:3]\n" \
    "v_max3_u32 v104, v104, v60, v61\n" \
    "v_mfma_f32_16x16x32_bf16 v[4:7], a[236:239], v[60:63], v[4:7]\n" \
    "v_max3_u32 v104, v104, v62, v63\n" \
    "v_mfma_f32_16x16x32_bf16 v[8:11], v[236:239], v[60:63], v[8:11]\n" \
    "s_waitcnt lgkmcnt(2)\n" \
    "v_mfma_f32_16x16x32_bf16 v[12:15], v[80:83], v[60:63], v[12:15]\n" \
    "ds_read_b128 v[80:83], %[wl] offset:30720\n" \
    "s_waitcnt vmcnt(6)\n" \
    "v_mfma_f32_16x16x32_bf16 v[0:3], a[112:115], v[64:67], v[0:3]\n" \
    "v_max3_u32 v104, v104, v64, v65\n" \
    "v_mfma_f32_16x16x32_bf16 v[4:7], a[240:243], v[64:67], v[4:7]\n" \
    "v_max3_u32 v104, v104, v66, v67\n" \
    "v_mfma_f32_16x16x32_bf16 v[8:11], v[240:243], v[64:67], v[8:11]\n" \
    "s_waitcnt lgkmcnt(2)\n" \
    "v_mfma_f32_16x16x32_bf16 v[12:15], v[84:87], v[64:67], v[12:15]\n" \
    "ds_read_b128 v[84:87], %[wl] offset:31744\n" \
    "s_waitcnt vmcnt(5)\n" \
    "v_mfma_f32_16x16x32_bf16 v[0:3], a[116:119], v[68:71], v[0:3]\n" \
    "v_max3_u32 v104, v104, v68, v69\n" \
    "v_mfma_f32_16x16x32_bf16 v[4:7], a[244:247], v[68:71], v[4:7]\n" \
    "v_max3_u32 v104, v104, v70, v71\n" \
    "v_mfma_f32_16x16x32_bf16 v[8:11], v[244:247], v[68:71], v[8:11]\n" \
    "s_waitcnt lgkmcnt(2)\n" \
    "v_mfma_f32_16x16x32_bf16 v[12:15], v[88:91], v[68:71], v[12:15]\n" \
    "s_waitcnt vmcnt(4)\n" \
    "v_mfma_f32_16x16x32_bf16 v[0:3], a[120:123], v[72:75], v[0:3]\n" \
    "v_max3_u32 v104, v104, v72, v73\n" \
    "v_mfma_f32_16x16x32_bf16 v[4:7], a[248:251], v[72:75], v[4:7]\n" \
    "v_max3_u32 v104, v104, v74, v75\n" \
    "v_mfma_f32_16x16x32_bf16 v[8:11], v[248:251], v[72:75], v[8:11]\n" \
    "s_waitcnt lgkmcnt(1)\n" \
    "v_mfma_f32_16x16x32_bf16 v[12:15], v[80:83], v[72:75], v[12:15]\n" \
    "s_waitcnt vmcnt(3)\n" \
    "v_mfma_f32_16x16x32_bf16 v[0:3], a[124:127], v[76:79], v[0:3]\n" \
    "v_max3_u32 v104, v104, v76, v77\n" \
    "v_mfma_f32_16x16x32_bf16 v[4:7], a[252:255], v[76:79], v[4:7]\n" \
    "v_max3_u32 v104, v104, v78, v79\n" \
    "v_mfma_f32_16x16x32_bf16 v[8:11], v[252:255], v[76:79], v[8:11]\n" \
    "s_waitcnt lgkmcnt(0)\n" \
    "v_mfma_f32_16x16x32_bf16 v[12:15], v[84:87], v[76:79], v[12:15]\n" \
    "s_waitcnt vmcnt(0)\n" \
    "s_nop 7\n" \
    ""
#define OUT8_ASM \
    "s_mov_b32 exec_lo, %[tm]\n" \
    "s_mov_b32 exec_hi, 0\n" \
    "buffer_load_dword v79, %[tv], %[rw], %[so] offen sc1\n" \
    "s_mov_b64 exec, -1\n" \
    "s_add_i32 s44, %[so], 4096\n" \
    "s_add_i32 s45, %[so], 8192\n" \
    "s_add_i32 s46, %[so], 12288\n" \
    "buffer_load_dwordx4 v[16:19], %[vo], %[rw], %[so] offen offset:0 sc1\n" \
    "ds_read_b128 v[80:83], %[wl] offset:0\n" \
    "ds_read_b128 v[84:87], %[wl] offset:1024\n" \
    "ds_read_b128 v[88:91], %[wl] offset:2048\n" \
    "ds_read_b128 v[92:95], %[wl] offset:3072\n" \
    "v_mov_b64 v[0:1], 0\n" \
    "v_mov_b64 v[2:3], 0\n" \
    "v_mov_b64 v[4:5], 0\n" \
    "v_mov_b64 v[6:7], 0\n" \
    "v_mov_b64 v[8:9], 0\n" \
    "v_mov_b64 v[10:11], 0\n" \
    "v_mov_b64 v[12:13], 0\n" \
    "v_mov_b64 v[14:15], 0\n" \
    "v_mov_b32 v104, 0\n" \
    "buffer_load_dwordx4 v[20:23], %[vo], %[rw], %[so] offen offset:1024 sc1\n" \
    "s_nop 7\n" \
    "s_nop 3\n" \
    "buffer_load_dwordx4 v[24:27], %[vo], %[rw], %[so] offen offset:2048 sc1\n" \
    "s_nop 7\n" \
    "s_nop 3\n" \
    "buffer_load_dwordx4 v[28:31], %[vo], %[rw], %[so] offen offset:3072 sc1\n" \
    "s_nop 7\n" \
    "s_nop 3\n" \
    "buffer_load_dwordx4 v[32:35], %[vo], %[rw], s44 offen offset:0 sc1\n" \
    "s_nop 7\n" \
    "s_nop 3\n" \
    "buffer_load_dwordx4 v[36:39], %[vo], %[rw], s44 offen offset:1024 sc1\n" \
    "s_nop 7\n" \
    "s_nop 3\n" \
    "buffer_load_dwordx4 v[40:43], %[vo], %[rw], s44 offen offset:2048 sc1\n" \
    "s_nop 7\n" \
    "s_nop 3\n" \
    "buffer_load_dwordx4 v[44:47], %[vo], %[rw], s44 offen offset:3072 sc1\n" \
    "s_nop 7\n" \
    "s_nop 3\n" \
    "buffer_load_dwordx4 v[48:51], %[vo], %[rw], s45 offen offset:0 sc1\n" \
    "s_nop 7\n" \
    "s_nop 3\n" \
    "buffer_load_dwordx4 v[52:55], %[vo], %[rw], s45 offen offset:1024 sc1\n" \
    "ds_read_b128 v[96:99], %[wl] offset:4096\n" \
    "ds_read_b128 v[100:103], %[wl] offset:5120\n" \
    "s_waitcnt vmcnt(9)\n" \
    "v_smfmac_f32_16x16x64_bf16 v[0:3], v[16:19], a[0:7], %[idx]\n" \
    "v_max3_u32 v104, v104, v16, v17\n" \
    "v_smfmac_f32_16x16x64_bf16 v[4:7], v[16:19], a[128:135], %[idx]\n" \
    "v_max3_u32 v104, v104, v18, v19\n" \
    "v_smfmac_f32_16x16x64_bf16 v[8:11], v[16:19], v[128:135], %[idx]\n" \
    "s_waitcnt lgkmcnt(4)\n" \
    "v_smfmac_f32_16x16x64_bf16 v[12:15], v[16:19], v[80:87], %[idx]\n" \
    "buffer_load_dwordx4 v[56:59], %[vo], %[rw], s45 offen offset:2048 sc1\n" \
    "ds_read_b128 v[80:83], %[wl] offset:6144\n" \
    "ds_read_b128 v[84:87], %[wl] offset:7168\n" \
    "s_waitcnt vmcnt(9)\n" \
    "v_smfmac_f32_16x16x64_bf16 v[0:3], v[20:23], a[8:15], %[idx]\n" \
    "v_max3_u32 v104, v104, v20, v21\n" \
    "v_smfmac_f32_16x16x64_bf16 v[4:7], v[20:23], a[136:143], %[idx]\n" \
    "v_max3_u32 v104, v104, v22, v23\n" \
    "v_smfmac_f32_16x16x64_bf16 v[8:11], v[20:23], v[136:143], %[idx]\n" \
    "s_waitcnt lgkmcnt(4)\n" \
    "v_smfmac_f32_16x16x64_bf16 v[12:15], v[20:23], v[88:95], %[idx]\n" \
    "buffer_load_dwordx4 v[60:63], %[vo], %[rw], s45 offen offset:3072 sc1\n" \
    "ds_read_b128 v[88:91], %[wl] offset:8192\n" \
    "ds_read_b128 v[92:95], %[wl] offset:9216\n" \
    "s_waitcnt vmcnt(9)\n" \
    "v_smfmac_f32_16x16x64_bf16 v[0:3], v[24:27], a[16:23], %[idx]\n" \
    "v_max3_u32 v104, v104, v24, v25\n" \
    "v_smfmac_f32_16x16x64_bf16 v[4:7], v[24:27], a[144:151], %[idx]\n" \
    "v_max3_u32 v104, v104, v26, v27\n" \
    "v_smfmac_f32_16x16x64_bf16 v[8:11], v[24:27], v[144:151], %[idx]\n" \
    "s_waitcnt lgkmcnt(4)\n" \
    "v_smfmac_f32_16x16x64_bf16 v[12:15], v[24:27], v[96:103], %[idx]\n" \
    "buffer_load_dwordx4 v[64:67], %[vo], %[rw], s46 offen offset:0 sc1\n" \
    "ds_read_b128 v[96:99], %[wl] offset:10240\n" \
    "ds_read_b128 v[100:103], %[wl] offset:11264\n" \
    "s_waitcnt vmcnt(9)\n" \
    "v_smfmac_f32_16x16x64_bf16 v[0:3], v[28:31], a[24:31], %[idx]\n" \
    "v_max3_u32 v104, v104, v28, v29\n" \
    "v_smfmac_f32_16x16x64_bf16 v[4:7], v[28:31], a[152:159], %[idx]\n" \
    "v_max3_u32 v104, v104, v30, v31\n" \
    "v_smfmac_f32_16x16x64_bf16 v[8:11], v[28:31], v[152:159], %[idx]\n" \
    "s_waitcnt lgkmcnt(4)\n" \
    "v_smfmac_f32_16x16x64_bf16 v[12:15], v[28:31], v[80:87], %[idx]\n" \
    "buffer_load_dwordx4 v[68:71], %[vo], %[rw], s46 offen offset:1024 sc1\n" \
    "ds_read_b128 v[80:83], %[wl] offset:12288\n" \
    "ds_read_b128 v[84:87], %[wl] offset:13312\n" \
    "s_waitcnt vmcnt(9)\n" \
    "v_smfmac_f32_16x16x64_bf16 v[0:3], v[32:35], a[32:39], %[idx]\n" \
    "v_max3_u32 v104, v104, v32, v33\n" \
    "v_smfmac_f32_16x16x64_bf16 v[4:7], v[32:35], a[160:167], %[idx]\n" \
    "v_max3_u32 v104, v104, v34, v35\n" \
    "v_smfmac_f32_16x16x64_bf16 v[8:11], v[32:35], v[160:167], %[idx]\n" \
    "s_waitcnt lgkmcnt(4)\n" \
    "v_smfmac_f32_16x16x64_bf16 v[12:15], v[32:35], v[88:95], %[idx]\n" \
    "buffer_load_dwordx4 v[72:75], %[vo], %[rw], s46 offen offset:2048 sc1\n" \
    "ds_read_b128 v[88:91], %[wl] offset:14336\n" \
    "ds_read_b128 v[92:95], %[wl] offset:15360\n" \
    "s_waitcnt vmcnt(9)\n" \
    "v_smfmac_f32_16x16x64_bf16 v[0:3], v[36:39], a[40:47], %[idx]\n" \
    "v_max3_u32 v104, v104, v36, v37\n" \
    "v_smfmac_f32_16x16x64_bf16 v[4:7], v[36:39], a[168:175], %[idx]\n" \
    "v_max3_u32 v104, v104, v38, v39\n" \
    "v_smfmac_f32_16x16x64_bf16 v[8:11], v[36:39], v[168:175], %[idx]\n" \
    "s_waitcnt lgkmcnt(4)\n" \
    "v_smfmac_f32_16x16x64_bf16 v[12:15], v[36:39], v[96:103], %[idx]\n" \
    "buffer_load_dwordx4 v[76:79], %[vo], %[rw], s46 offen offset:3072 sc1\n" \
    "ds_read_b128 v[96:99], %[wl] offset:16384\n" \
    "ds_read_b128 v[100:103], %[wl] offset:17408\n" \
    "s_waitcnt vmcnt(9)\n" \
    "v_smfmac_f32_16x16x64_bf16 v[0:3], v[40:43], a[48:55], %[idx]\n" \
    "v_max3_u32 v104, v104, v40, v41\n" \
    "v_smfmac_f32_16x16x64_bf16 v[4:7], v[40:43], a[176:183], %[idx]\n" \
    "v_max3_u32 v104, v104, v42, v43\n" \
    "v_smfmac_f32_16x16x64_bf16 v[8:11], v[40:43], v[176:183], %[idx]\n" \
    "s_waitcnt lgkmcnt(4)\n" \
    "v_smfmac_f32_16x16x64_bf16 v[12:15], v[40:43], v[80:87], %[idx]\n" \
    "buffer_load_dwordx2 v[106:107], %[bv], %[rw], %[bs] offen sc1\n" \
    "ds_read_b128 v[80:83], %[wl] offset:18432\n" \
    "ds_read_b128 v[84:87], %[wl] offset:19456\n" \
    "s_waitcnt vmcnt(9)\n" \
    "v_smfmac_f32_16x16x64_bf16 v[0:3], v[44:47], a[56:63], %[idx]\n" \
    "v_max3_u32 v104, v104, v44, v45\n" \
    "v_smfmac_f32_16x16x64_bf16 v[4:7], v[44:47], a[184:191], %[idx]\n" \
    "v_max3_u32 v104, v104, v46, v47\n" \
    "v_smfmac_f32_16x16x64_bf16 v[8:11], v[44:47], v[184:191], %[idx]\n" \
    "s_waitcnt lgkmcnt(4)\n" \
    "v_smfmac_f32_16x16x64_bf16 v[12:15], v[44:47], v[88:95], %[idx]\n" \
    "buffer_load_dword v108, %[gv], %[rw], %[gs] offen sc1\n" \
    "ds_read_b128 v[88:91], %[wl] offset:20480\n" \
    "ds_read_b128 v[92:95], %[wl] offset:21504\n" \
    "s_waitcnt vmcnt(9)\n" \
    "v_smfmac_f32_16x16x64_bf16 v[0:3], v[48:51], a[64:71], %[idx]\n" \
    "v_max3_u32 v104, v104, v48, v49\n" \
    "v_smfmac_f32_16x16x64_bf16 v[4:7], v[48:51], a[192:199], %[idx]\n" \
    "v_max3_u32 v104, v104, v50, v51\n" \
    "v_smfmac_f32_16x16x64_bf16 v[8:11], v[48:51], v[192:199], %[idx]\n" \
    "s_waitcnt lgkmcnt(4)\n" \
    "v_smfmac_f32_16x16x64_bf16 v[12:15], v[48:51], v[96:103], %[idx]\n" \
    "buffer_load_dword v105, %[sv], %[rw], %[ss] offen sc1\n" \
    "ds_read_b128 v[96:99], %[wl] offset:22528\n" \
    "ds_read_b128 v[100:103], %[wl] offset:23552\n" \
    "s_waitcnt vmcnt(9)\n" \
    "v_smfmac_f32_16x16x64_bf16 v[0:3], v[52:55], a[72:79], %[idx]\n" \
    "v_max3_u32 v104, v104, v52, v53\n" \
    "v_smfmac_f32_16x16x64_bf16 v[4:7], v[52:55], a[200:207], %[idx]\n" \
    "v_max3_u32 v104, v104, v54, v55\n" \
    "v_smfmac_f32_16x16x64_bf16 v[8:11], v[52:55], v[200:207], %[idx]\n" \
    "s_waitcnt lgkmcnt(4)\n" \
    "v_smfmac_f32_16x16x64_bf16 v[12:15], v[52:55], v[80:87], %[idx]\n" \
    "ds_read_b128 v[80:83], %[wl] offset:24576\n" \
    "ds_read_b128 v[84:87], %[wl] offset:25600\n" \
    "s_waitcnt vmcnt(8)\n" \
    "v_smfmac_f32_16x16x64_bf16 v[0:3], v[56:59], a[80:87], %[idx]\n" \
    "v_max3_u32 v104, v104, v56, v57\n" \
    "v_smfmac_f32_16x16x64_bf16 v[4:7], v[56:59], a[208:215], %[idx]\n" \
    "v_max3_u32 v104, v104, v58, v59\n" \
    "v_smfmac_f32_16x16x64_bf16 v[8:11], v[56:59], v[208:215], %[idx]\n" \
    "s_waitcnt lgkmcnt(4)\n" \
    "v_smfmac_f32_16x16x64_bf16 v[12:15], v[56:59], v[88:95], %[idx]\n" \
    "ds_read_b128 v[88:91], %[wl] offset:26624\n" \
    "ds_read_b128 v[92:95], %[wl] offset:27648\n" \
    "s_waitcnt vmcnt(7)\n" \
    "v_smfmac_f32_16x16x64_bf16 v[0:3], v[60:63], a[88:95], %[idx]\n" \
    "v_max3_u32 v104, v104, v60, v61\n" \
    "v_smfmac_f32_16x16x64_bf16 v[4:7], v[60:63], a[216:223], %[idx]\n" \
    "v_max3_u32 v104, v104, v62, v63\n" \
    "v_smfmac_f32_16x16x64_bf16 v[8:11], v[60:63], v[216:223], %[idx]\n" \
    "s_waitcnt lgkmcnt(4)\n" \
    "v_smfmac_f32_16x16x64_bf16 v[12:15], v[60:63], v[96:103], %[idx]\n" \
    "ds_read_b128 v[96:99], %[wl] offset:28672\n" \
    "ds_read_b128 v[100:103], %[wl] offset:29696\n" \
    "s_waitcnt vmcnt(6)\n" \
    "v_smfmac_f32_16x16x64_bf16 v[0:3], v[64:67], a[96:103], %[idx]\n" \
    "v_max3_u32 v104, v104, v64, v65\n" \
    "v_smfmac_f32_16x16x64_bf16 v[4:7], v[64:67], a[224:231], %[idx]\n" \
    "v_max3_u32 v104, v104, v66, v67\n" \
    "v_smfmac_f32_16x16x64_bf16 v[8:11], v[64:67], v[224:231], %[idx]\n" \
    "s_waitcnt lgkmcnt(4)\n" \
    "v_smfmac_f32_16x16x64_bf16 v[12:15], v[64:67], v[80:87], %[idx]\n" \
    "ds_read_b128 v[80:83], %[wl] offset:30720\n" \
    "ds_read_b128 v[84:87], %[wl] offset:31744\n" \
    "s_waitcnt vmcnt(5)\n" \
    "v_smfmac_f32_16x16x64_bf16 v[0:3], v[68:71], a[104:111], %[idx]\n" \
    "v_max3_u32 v104, v104, v68, v69\n" \
    "v_smfmac_f32_16x16x64_bf16 v[4:7], v[68:71], a[232:239], %[idx]\n" \
    "v_max3_u32 v104, v104, v70, v71\n" \
    "v_smfmac_f32_16x16x64_bf16 v[8:11], v[68:71], v[232:239], %[idx]\n" \
    "s_waitcnt lgkmcnt(4)\n" \
    "v_smfmac_f32_16x16x64_bf16 v[12:15], v[68:71], v[88:95], %[idx]\n" \
    "s_waitcnt vmcnt(4)\n" \
    "v_smfmac_f32_16x16x64_bf16 v[0:3], v[72:75], a[112:119], %[idx]\n" \
    "v_max3_u32 v104, v104, v72, v73\n" \
    "v_smfmac_f32_16x16x64_bf16 v[4:7], v[72:75], a[240:247], %[idx]\n" \
    "v_max3_u32 v104, v104, v74, v75\n" \
    "v_smfmac_f32_16x16x64_bf16 v[8:11], v[72:75], v[240:247], %[idx]\n" \
    "s_waitcnt lgkmcnt(2)\n" \
    "v_smfmac_f32_16x16x64_bf16 v[12:15], v[72:75], v[96:103], %[idx]\n" \
    "s_waitcnt vmcnt(3)\n" \
    "v_smfmac_f32_16x16x64_bf16 v[0:3], v[76:79], a[120:127], %[idx]\n" \
    "v_max3_u32 v104, v104, v76, v77\n" \
    "v_smfmac_f32_16x16x64_bf16 v[4:7], v[76:79], a[248:255], %[idx]\n" \
    "v_max3_u32 v104, v104, v78, v79\n" \
    "v_smfmac_f32_16x16x64_bf16 v[8:11], v[76:79], v[248:255], %[idx]\n" \
    "s_waitcnt lgkmcnt(0)\n" \
    "v_smfmac_f32_16x16x64_bf16 v[12:15], v[76:79], v[80:87], %[idx]\n" \
    "s_waitcnt vmcnt(0)\n" \
    "s_nop 5\n" \
    "v_permlane32_swap_b32 v0, v8\n" \
    "v_add_f32 v0, v0, v8\n" \
    "v_permlane32_swap_b32 v1, v9\n" \
    "v_add_f32 v1, v1, v9\n" \
    "v_permlane32_swap_b32 v2, v10\n" \
    "v_add_f32 v2, v2, v10\n" \
    "v_permlane32_swap_b32 v3, v11\n" \
    "v_add_f32 v3, v3, v11\n" \
    "v_permlane32_swap_b32 v4, v12\n" \
    "v_add_f32 v4, v4, v12\n" \
    "v_permlane32_swap_b32 v5, v13\n" \
    "v_add_f32 v5, v5, v13\n" \
    "v_permlane32_swap_b32 v6, v14\n" \
    "v_add_f32 v6, v6, v14\n" \
    "v_permlane32_swap_b32 v7, v15\n" \
    "v_add_f32 v7, v7, v15\n" \
    ""
#define OUT_CLOBBER "v16", "v17", "v18", "v19", "v20", "v21", "v22", "v23", "v24", "v25", "v26", "v27", "v28", "v29", "v30", "v31", "v32", "v33", "v34", "v35", "v36", "v37", "v38", "v39", "v40", "v41", "v42", "v43", "v44", "v45", "v46", "v47", "v48", "v49", "v50", "v51", "v52", "v53", "v54", "v55", "v56", "v57", "v58", "v59", "v60", "v61", "v62", "v63", "v64", "v65", "v66", "v67", "v68", "v69", "v70", "v71", "v72", "v73", "v74", "v75", "v76", "v77", "v78", "v79", "v80", "v81", "v82", "v83", "v84", "v85", "v86", "v87", "v88", "v89", "v90", "v91", "v92", "v93", "v94", "v95", "v96", "v97", "v98", "v99", "v100", "v101", "v102", "v103", "s44", "s45", "s46", "scc", "memory"
constexpr int NREP = 2, NCP = 4;
#define OWNER_ASM \
    "s_getpc_b64 s[96:97]\n" \
    "14:\n" \
    "s_add_u32 s96, s96, 2f-14b\n" \
    "s_addc_u32 s97, s97, 0\n" \
    "s_sub_u32 s68, s96, 2f-11f\n" \
    "s_subb_u32 s69, s97, 0\n" \
    "s_mov_b32 %[np], 0\n" \
    "13:\n" \
    "s_memtime vcc\n" \
    "s_waitcnt lgkmcnt(0)\n" \
    "s_sub_i32 s70, %[lk], vcc_lo\n" \
    "s_cmp_gt_i32 s70, %[rd]\n" \
    "s_cbranch_scc1 13b\n" \
    "buffer_store_dwordx4 %[sv], %[ro], %[rs], %[pq] offen offset:0 sc1\n" \
    "buffer_store_dwordx4 %[sv], %[ro], %[rs], %[pq] offen offset:1024 sc1\n" \
    "buffer_store_dwordx4 %[sv], %[ro], %[rs], %[pq] offen offset:2048 sc1\n" \
    "buffer_store_dwordx4 %[sv], %[ro], %[rs], %[pq] offen offset:3072 sc1\n" \
    "v_mov_b32 v2, -1\n" \
    "buffer_store_dword v2, %[rm], %[rs], %[mq] offen sc1\n" \
    "buffer_store_dwordx4 %[sv], %[rt], %[rw], %[tq] offen sc1\n" \
    "s_mov_b32 s71, 4\n" \
    "10:\n" \
    "s_memtime vcc\n" \
    "s_waitcnt lgkmcnt(0)\n" \
    "s_sub_i32 s70, %[lk], vcc_lo\n" \
    "s_cmp_gt_i32 s70, 112\n" \
    "s_cbranch_scc1 10b\n" \
    "s_max_i32 s70, s70, 0\n" \
    "s_memtime s[58:59]\n" \
    "s_memrealtime s[44:45]\n" \
    "s_nop 1\n" \
    "s_memrealtime s[46:47]\n" \
    "s_nop 1\n" \
    "s_memrealtime s[48:49]\n" \
    "s_nop 1\n" \
    "s_memrealtime s[50:51]\n" \
    "s_nop 1\n" \
    "s_memrealtime s[52:53]\n" \
    "s_nop 1\n" \
    "s_memrealtime s[54:55]\n" \
    "s_nop 1\n" \
    "s_memrealtime s[56:57]\n" \
    "s_nop 1\n" \
    "s_memrealtime vcc\n" \
    "s_waitcnt lgkmcnt(0)\n" \
    "s_add_i32 s44, s44, s46\n" \
    "s_add_i32 s48, s48, s50\n" \
    "s_add_i32 s52, s52, s54\n" \
    "s_add_i32 s44, s44, s48\n" \
    "s_add_i32 s52, s52, s56\n" \
    "s_add_i32 s44, s44, s52\n" \
    "s_add_i32 s59, s44, vcc_lo\n" \
    "s_sub_i32 s44, s59, %[oT]\n" \
    "s_mov_b32 %[oT], s59\n" \
    "s_sub_i32 s45, s58, %[oC]\n" \
    "s_mov_b32 %[oC], s58\n" \
    "s_mul_i32 s46, s44, %[oR]\n" \
    "s_lshr_b32 s46, s46, 12\n" \
    "s_sub_i32 s46, s45, s46\n" \
    "s_sub_i32 s44, s44, 1000\n" \
    "s_cmp_lt_u32 s44, 480\n" \
    "s_cselect_b32 s46, s46, 0\n" \
    "s_add_i32 %[oR], %[oR], s46\n" \
    "s_sub_i32 s45, %[oT], %[c8]\n" \
    "s_and_b32 s70, s70, -4\n" \
    "s_sub_u32 vcc_lo, s68, s70\n" \
    "s_subb_u32 vcc_hi, s69, 0\n" \
    "s_setpc_b64 vcc\n" \
    "s_nop 0\n" \
    "s_nop 0\n" \
    "s_nop 0\n" \
    "s_nop 0\n" \
    "s_nop 0\n" \
    "s_nop 0\n" \
    "s_nop 0\n" \
    "s_nop 0\n" \
    "s_nop 0\n" \
    "s_nop 0\n" \
    "s_nop 0\n" \
    "s_nop 0\n" \
    "s_nop 0\n" \
    "s_nop 0\n" \
    "s_nop 0\n" \
    "s_nop 0\n" \
    "s_nop 0\n" \
    "s_nop 0\n" \
    "s_nop 0\n" \
    "s_nop 0\n" \
    "s_nop 0\n" \
    "s_nop 0\n" \
    "s_nop 0\n" \
    "s_nop 0\n" \
    "s_nop 0\n" \
    "s_nop 0\n" \
    "s_nop 0\n" \
    "s_nop 0\n" \
    "11:\n" \
    "buffer_load_dwordx2 v[0:1], %[ca], %[rs], %[cs] offen sc1\n" \
    "s_waitcnt vmcnt(0)\n" \
    "6:\n" \
    "s_memtime s[46:47]\n" \
    "v_subrev_u32 v0, s45, v0\n" \
    "v_cmp_ne_u16 vcc, %[tag], v1\n" \
    "s_add_i32 s48, %[oC], -136\n" \
    "v_max_i32_dpp v0, v0, v0 row_shr:1 row_mask:0xf bank_mask:0xf\n" \
    "v_max_u32_dpp v1, v1, v1 row_shr:1 row_mask:0xf bank_mask:0xf\n" \
    "s_cbranch_vccnz 7f\n" \
    "8:\n" \
    "v_max_i32_dpp v0, v0, v0 row_shr:2 row_mask:0xf bank_mask:0xf\n" \
    "v_max_u32_dpp v1, v1, v1 row_shr:2 row_mask:0xf bank_mask:0xf\n" \
    "s_nop 0\n" \
    "v_max_i32_dpp v0, v0, v0 row_shr:4 row_mask:0xf bank_mask:0xf\n" \
    "v_max_u32_dpp v1, v1, v1 row_shr:4 row_mask:0xf bank_mask:0xf\n" \
    "s_nop 0\n" \
    "v_max_i32_dpp v0, v0, v0 row_shr:8 row_mask:0xf bank_mask:0xf\n" \
    "v_max_u32_dpp v1, v1, v1 row_shr:8 row_mask:0xf bank_mask:0xf\n" \
    "s_waitcnt lgkmcnt(0)\n" \
    "v_max_i32_dpp v0, v0, v0 row_bcast:15 row_mask:0xa bank_mask:0xf\n" \
    "v_max_u32_dpp v1, v1, v1 row_bcast:15 row_mask:0xa bank_mask:0xf\n" \
    "s_add_i32 s46, s46, 44\n" \
    "v_max_i32_dpp v0, v0, v0 row_bcast:31 row_mask:0xc bank_mask:0xf\n" \
    "v_max_u32_dpp v1, v1, v1 row_bcast:31 row_mask:0xc bank_mask:0xf\n" \
    "v_readlane_b32 s44, v0, 63\n" \
    "v_readlane_b32 s49, v1, 63\n" \
    "s_mul_i32 s47, s44, %[oR]\n" \
    "s_ashr_i32 s47, s47, 12\n" \
    "s_add_i32 s47, s47, s48\n" \
    "s_sub_i32 s58, s47, s46\n" \
    "s_cmp_gt_i32 s58, 112\n" \
    "s_cbranch_scc1 1f\n" \
    "12:\n" \
    "s_max_i32 s58, s58, 0\n" \
    "s_and_b32 s58, s58, -4\n" \
    "s_sub_u32 vcc_lo, s96, s58\n" \
    "s_subb_u32 vcc_hi, s97, 0\n" \
    "s_setpc_b64 vcc\n" \
    "s_nop 0\n" \
    "s_nop 0\n" \
    "s_nop 0\n" \
    "s_nop 0\n" \
    "s_nop 0\n" \
    "s_nop 0\n" \
    "s_nop 0\n" \
    "s_nop 0\n" \
    "s_nop 0\n" \
    "s_nop 0\n" \
    "s_nop 0\n" \
    "s_nop 0\n" \
    "s_nop 0\n" \
    "s_nop 0\n" \
    "s_nop 0\n" \
    "s_nop 0\n" \
    "s_nop 0\n" \
    "s_nop 0\n" \
    "s_nop 0\n" \
    "s_nop 0\n" \
    "s_nop 0\n" \
    "s_nop 0\n" \
    "s_nop 0\n" \
    "s_nop 0\n" \
    "s_nop 0\n" \
    "s_nop 0\n" \
    "s_nop 0\n" \
    "s_nop 0\n" \
    "2:\n" \
    "buffer_load_dwordx4 v[0:3], %[o], %[rs], %[pb] offen offset:0 sc1\n" \
    "buffer_load_dwordx4 v[4:7], %[o], %[rs], %[pb] offen offset:1024 sc1\n" \
    "buffer_load_dwordx4 v[8:11], %[o], %[rs], %[pb] offen offset:2048 sc1\n" \
    "buffer_load_dwordx4 v[12:15], %[o], %[rs], %[pb] offen offset:3072 sc1\n" \
    "s_add_i32 %[np], %[np], 1\n" \
    "s_add_i32 %[tm], s44, s45\n" \
    "s_lshr_b32 s50, s49, 16\n" \
    "s_add_i32 s50, s50, s44\n" \
    "s_add_i32 s50, s50, %[cw]\n" \
    "s_mul_i32 s50, s50, %[oR]\n" \
    "s_ashr_i32 s50, s50, 12\n" \
    "s_add_i32 %[lk], s50, %[oC]\n" \
    "s_waitcnt vmcnt(3)\n" \
    "v_mfma_f32_16x16x32_bf16 v[16:19], %[sel], v[0:3], 0\n" \
    "v_max3_u32 v20, v0, v1, v1\n" \
    "v_max3_u32 v20, v2, v3, v20\n" \
    "s_waitcnt vmcnt(2)\n" \
    "v_mfma_f32_16x16x32_bf16 v[16:19], %[sel], v[4:7], v[16:19]\n" \
    "v_max3_u32 v20, v4, v5, v20\n" \
    "v_max3_u32 v20, v6, v7, v20\n" \
    "s_waitcnt vmcnt(1)\n" \
    "v_mfma_f32_16x16x32_bf16 v[16:19], %[sel], v[8:11], v[16:19]\n" \
    "v_max3_u32 v20, v8, v9, v20\n" \
    "v_max3_u32 v20, v10, v11, v20\n" \
    "s_waitcnt vmcnt(0)\n" \
    "v_mfma_f32_16x16x32_bf16 v[16:19], %[sel], v[12:15], v[16:19]\n" \
    "v_max3_u32 v20, v12, v13, v20\n" \
    "v_max3_u32 v20, v14, v15, v20\n" \
    "v_cmp_eq_u32 vcc, -1, v20\n" \
    "s_nop 3\n" \
    "s_cbranch_vccnz 4f\n" \
    "3:\n" \
    "v_add_f32_dpp v16, v16, v16 row_ror:8 row_mask:0xf bank_mask:0xf\n" \
    "v_add_f32_dpp v17, v17, v17 row_ror:8 row_mask:0xf bank_mask:0xf\n" \
    "v_cvt_pk_bf16_f32 v16, v16, v17\n" \
    "buffer_store_dword v16, %[so], %[rs], %[mb0] offen sc1\n" \
    "s_branch 5f\n" \
    "1:\n" \
    "s_memtime vcc\n" \
    "s_waitcnt lgkmcnt(0)\n" \
    "s_sub_i32 s58, s47, vcc_lo\n" \
    "s_cmp_gt_i32 s58, 112\n" \
    "s_cbranch_scc1 1b\n" \
    "s_branch 12b\n" \
    "4:\n" \
    "s_add_i32 %[bud], %[bud], -1\n" \
    "s_cmp_lt_i32 %[bud], 0\n" \
    "s_cbranch_scc0 2b\n" \
    "s_branch 3b\n" \
    "7:\n" \
    "s_bcnt1_i32_b64 s44, vcc\n" \
    "s_cmp_le_u32 s44, s71\n" \
    "s_mov_b32 s71, 0\n" \
    "s_cbranch_scc1 15f\n" \
    "s_cmp_eq_u32 %[tag], 1\n" \
    "s_cbranch_scc1 9f\n" \
    "s_sleep 15\n" \
    "9:\n" \
    "s_sleep 6\n" \
    "15:\n" \
    "s_add_i32 %[bud], %[bud], -1\n" \
    "s_cmp_lt_i32 %[bud], 0\n" \
    "s_cbranch_scc1 8b\n" \
    "buffer_load_dwordx2 v[0:1], %[ca], %[rs], %[cs] offen sc1\n" \
    "s_memtime s[58:59]\n" \
    "s_memrealtime s[44:45]\n" \
    "s_nop 1\n" \
    "s_memrealtime s[46:47]\n" \
    "s_nop 1\n" \
    "s_memrealtime s[48:49]\n" \
    "s_nop 1\n" \
    "s_memrealtime s[50:51]\n" \
    "s_nop 1\n" \
    "s_memrealtime s[52:53]\n" \
    "s_nop 1\n" \
    "s_memrealtime s[54:55]\n" \
    "s_nop 1\n" \
    "s_memrealtime s[56:57]\n" \
    "s_nop 1\n" \
    "s_memrealtime vcc\n" \
    "s_waitcnt lgkmcnt(0)\n" \
    "s_add_i32 s44, s44, s46\n" \
    "s_add_i32 s48, s48, s50\n" \
    "s_add_i32 s52, s52, s54\n" \
    "s_add_i32 s44, s44, s48\n" \
    "s_add_i32 s52, s52, s56\n" \
    "s_add_i32 s44, s44, s52\n" \
    "s_add_i32 s59, s44, vcc_lo\n" \
    "s_sub_i32 s44, s59, %[oT]\n" \
    "s_mov_b32 %[oT], s59\n" \
    "s_sub_i32 s45, s58, %[oC]\n" \
    "s_mov_b32 %[oC], s58\n" \
    "s_mul_i32 s46, s44, %[oR]\n" \
    "s_lshr_b32 s46, s46, 12\n" \
    "s_sub_i32 s46, s45, s46\n" \
    "s_sub_i32 s44, s44, 1000\n" \
    "s_cmp_lt_u32 s44, 480\n" \
    "s_cselect_b32 s46, s46, 0\n" \
    "s_add_i32 %[oR], %[oR], s46\n" \
    "s_sub_i32 s45, %[oT], %[c8]\n" \
    "s_waitcnt vmcnt(0)\n" \
    "s_branch 6b\n" \
    "5:\n" \
    ""
#define OWNER_CLOBBER "v0", "v1", "v2", "v3", "v4", "v5", "v6", "v7", "v8", "v9", "v10", "v11", "v12", "v13", "v14", "v15", "v16", "v17", "v18", "v19", "v20", "s44", "s45", "s46", "s47", "s48", "s49", "s50", "s51", "s52", "s53", "s54", "s55", "s56", "s57", "s58", "s59", "s68", "s69", "s70", "s71", "s96", "s97", "vcc", "scc", "memory"
constexpr int O_W = 0;
constexpr int O_STG = 131072;
constexpr int O_B = O_STG + 20480;
constexpr int OUT_REP = 7;
#define OUT_AUX 16
DI int opaque_lane() { int l; asm volatile("v_mbcnt_lo_u32_b32 %0, -1, 0\n\tv_mbcnt_hi_u32_b32 %0, -1, %0" : "=v"(l)); return l; }
template <int O, class V> DI void put4(V& d, u32x4 f) {
#pragma unroll
    for (int j = 0; j < 4; ++j) d[O + j] = f[j];
}
DI void role_outp(const Args& a, LAS char* lds, unsigned lds_base, int nb, int rep, rsrc_t rw, int& budget) {
    const int tid = threadIdx.x, lane = tid & 63, w = __builtin_amdgcn_readfirstlane(tid >> 6);
    WG_BARRIER();
    const int n = lane & 15, g = lane >> 4;
    const int wl = O_W + w * 32768 + lane * 16;
    u32x32 A[8], V[4];
    {
        const int stg = O_STG + w * 5120;
        const rsrc_t ro = __builtin_amdgcn_make_buffer_rsrc((void*)a.Wo, 0, HD * DM * 2, 0x00020000);
        const int so = (1024 * w + (lane >> 3)) * (DM * 2) + nb * 128 + (lane & 7) * 16, sw = stg + (lane >> 3) * 160 + (lane & 7) * 16;
        const int ra = stg + (16 * (g & 1) + 4 * (g >> 1) + (n >> 2)) * 160 + (n & 3) * 8;
        constexpr int PB = 4;
        u32x4 b[PB][4];
        auto fetch = [&](int ks) {
#pragma unroll
            for (int i = 0; i < 4; ++i) b[ks % PB][i] = __builtin_amdgcn_raw_buffer_load_b128(ro, so, (64 * (ks >> 1) + 16 * (ks & 1) + 8 * (i & 1) + 32 * (i >> 1)) * (DM * 2), 0);
        };
        unsigned tp = now();
        const int pc = a.d1 >> 16;
#pragma unroll
        for (int ks = 0; ks < PB; ++ks) { fetch(ks); FENCE(); tp += pc; while ((int)(now() - tp) < 0) {} }
        FENCE();
        static_for<32>([&](auto ic) {
            constexpr int ks = decltype(ic)::value;
#pragma unroll
            for (int i = 0; i < 4; ++i) *(LAS u32x4*)(lds + sw + i * 1280) = b[ks % PB][i];
            if (ks + PB < 32) { fetch(ks + PB); FENCE(); tp += pc; }
            u32x4 f[4];
#pragma unroll
            for (int t = 0; t < 4; ++t) { const u32x2 lo = tr_read(lds, ra + t * 32), hi = tr_read(lds, ra + t * 32 + 1280); f[t] = (u32x4){lo.x, lo.y, hi.x, hi.y}; }
            put4<4 * (ks & 7)>(A[ks >> 3], f[0]); put4<4 * (ks & 7)>(A[4 + (ks >> 3)], f[1]); put4<4 * (ks & 7)>(V[ks >> 3], f[2]);
            *(LAS u32x4*)(lds + wl + ks * 1024) = f[3];
            if constexpr (ks == 7) asm volatile("" : "+{a[0:31]}"(A[0]), "+{a[128:159]}"(A[4]), "+{v[128:159]}"(V[0]));
            if constexpr (ks == 15) asm volatile("" : "+{a[32:63]}"(A[1]), "+{a[160:191]}"(A[5]), "+{v[160:191]}"(V[1]));
            if constexpr (ks == 23) asm volatile("" : "+{a[64:95]}"(A[2]), "+{a[192:223]}"(A[6]), "+{v[192:223]}"(V[2]));
            if constexpr (ks == 31) asm volatile("" : "+{a[96:127]}"(A[3]), "+{a[224:255]}"(A[7]), "+{v[224:255]}"(V[3]));
            FENCE();
            if (ks + PB < 31) while ((int)(now() - tp) < 0) {}
        });
    }
    wait_prog(rw, F_S4 + lane * 4, true, 1, budget);
    const rsrc_t ru = __builtin_amdgcn_make_buffer_rsrc(a.out, 0, SEQ * DM * 2, 0x00020000);
    constexpr int SP = 2 * LATE - 2;
    auto first = [&](int u) { return u >= SP ? 8 * u + 8 * SP : 16 * u; };
    auto st_s = [&](int u) { return (u >= 2 * FAST ? F_H + ((first(u) >> 4) - 2 * FAST) * 4096 : F_O + (u & 3) * 4) + w * 1024; };
    auto bn_s = [&](int u) { return OFF_BO + (64 * nb + 16 * w) * BO_ROW + first(u) * 2; };
    auto gn_s = [&](int u) { return OFF_GT + (first(u) + 4 * w) * 4; };
    int st = -1;
    u32x2 bn = ld64(rw, (lane >> 2) * BO_ROW + (lane & 3) * 8, bn_s(rep));
    float gn = __uint_as_float(ld32(rw, g * 4, gn_s(rep)));
    __builtin_amdgcn_s_waitcnt(0);
#define OUT_OPS "=&{v[0:15]}"(acc), "=&{v104}"(top), "=&{v105}"(st), "=&{v[106:107]}"(bn), "=&{v108}"(gn) \
        : "{a[0:31]}"(A[0]), "{a[32:63]}"(A[1]), "{a[64:95]}"(A[2]), "{a[96:127]}"(A[3]), "{a[128:159]}"(A[4]), "{a[160:191]}"(A[5]), "{a[192:223]}"(A[6]), "{a[224:255]}"(A[7]), \
          "{v[128:159]}"(V[0]), "{v[160:191]}"(V[1]), "{v[192:223]}"(V[2]), "{v[224:255]}"(V[3]), [rw] "s"(rw), [so] "s"(t0 * 8192), [wl] "v"(lds_base + O_W + w * 32768 + lane * 16), \
          [tv] "v"(tv), [tm] "s"(tm), [vo] "v"(vo), [sv] "v"(sv), [bv] "v"((lane >> 2) * BO_ROW + (lane & 3) * 8), [gv] "v"(g * 4), \
          [ss] "s"(st_s(u + OUT_REP)), [bs] "s"(bn_s(u + OUT_REP)), [gs] "s"(gn_s(u + OUT_REP))
    for (int u = rep; u < SP + 4; u += OUT_REP) {
        const bool fast = u >= 2 * FAST, sp = u >= SP;
        const int t0 = first(u);
        WG_BARRIER();
        int lane = opaque_lane(), n = lane & 15, g = lane >> 4;
        if (u == 3 * OUT_REP && nb == 0) __builtin_amdgcn_raw_buffer_store_b64(launch_tag(a), rw, w + lane == 0 ? F_EPOCH : OOB, 0, 16);
        S4S(0)
        TT(u >= 119, 0)
        *(LAS u32x2*)(lds + O_B + (16 * w + (lane >> 2)) * 40 + (lane & 3) * 8) = bn;
        const float gt = gn;
        const int tv = OFF_OF + w * 16384 + nb * 2048 + g * 65536 + n * 128, tm = __builtin_amdgcn_readfirstlane(nb < 8 ? sp ? 0xFFFF : -1 : 0);
        const int vo = OFF_OF + w * 16384 + (n & 7) * 16 + (sp ? (n >> 3) * 512 + g * 128 : (n >> 3) * 65536 + (g & 1) * 512 + (g >> 1) * 128);
        const int thr = fast ? 0 : u >> 1, sv = (lane & 7) * 128;
        if (!__all(st >= thr)) do { __builtin_amdgcn_s_sleep(1); st = (int)ld32(rw, sv, st_s(u)); asm volatile("" ::: "memory"); } while (!__all(st >= thr) && --budget >= 0);
        if (fast) { const unsigned t = now(); (void)wait_until(t + wave_max32(min((int)(2 * (st - t)) >> 1, 500))); }
        S4S(1)
        TT(u >= 119, 1)
        f32x16 acc; unsigned top;
        if (sp) {
            do asm volatile(OUT8_ASM : OUT_OPS, [idx] "v"(n & 8 ? 0xEEEE : 0x4444) : OUT_CLOBBER); while (__any(top == SENT) && --budget >= 0);
            TT(u >= 119, 2)
            lane = opaque_lane(); n = lane & 15; g = lane >> 4;
#pragma unroll
            for (int i = 0; i < 8; ++i) *(LAS float*)(lds + O_STG + (w * 16 + 4 * (g & 1) + (i & 3)) * 272 + (g >> 1) * 128 + (i >> 2) * 64 + n * 4) = acc[i];
        } else {
            do asm volatile(OUT16_ASM : OUT_OPS : OUT_CLOBBER); while (__any(top == SENT) && --budget >= 0);
            S4S(2)
            TT(u >= 119, 2)
            lane = opaque_lane(); n = lane & 15; g = lane >> 4;
#pragma unroll
            for (int t = 0; t < 4; ++t) *(LAS f32x4*)(lds + O_STG + (w * 16 + n) * 272 + t * 64 + g * 16) = (f32x4){acc[4 * t], acc[4 * t + 1], acc[4 * t + 2], acc[4 * t + 3]};
        }
        WG_BARRIER();
        S4S(3)
        const int tok = 4 * w + g, c4 = 4 * n;
        f32x4 r[4]; u16 bv[4];
#pragma unroll
        for (int ww = 0; ww < 4; ++ww) { r[ww] = *(LAS f32x4*)(lds + O_STG + (ww * 16 + tok) * 272 + c4 * 4); bv[ww] = *(LAS u16*)(lds + O_B + (c4 + ww) * 40 + tok * 2); }
        FENCE();
        const f32x4 v = gt * ((r[0] + r[1]) + (r[2] + r[3])) + (1.0f - gt) * (f32x4){bf2f(bv[0]), bf2f(bv[1]), bf2f(bv[2]), bf2f(bv[3])};
        __builtin_amdgcn_raw_buffer_store_b64((u32x2){pack2(v[0], v[1]), pack2(v[2], v[3])}, ru, sp && tok >= 8 ? OOB : (tok * DM + 64 * nb + c4) * 2, t0 * (DM * 2), OUT_AUX);
        S4S(4)
        TT(u >= 119, 3)
        TT(u >= 105 && u < 112, 4)
    }
}

constexpr int L_MEMS = 0;
constexpr int L_Q = 9216;
constexpr int L_KO = L_Q + 2560;
constexpr int L_KX = 16384;
constexpr int L_W = 32768;
constexpr int C_RING = 8;
static_assert(S_TOTAL <= L_SINK && L_W + C_RING * 16384 <= L_TOTAL && L_KO + 8 * 576 <= L_KX, "LDS budget");
static_assert(P_STG + P_RING * 16384 <= L_TOTAL, "LDS budget");

DI int pos2col(int p) { return p < 512 ? (p & ~63) + 16 * (p & 3) + ((p & 63) >> 2) : p; }
DI void unit_rc(int u, int& i, int& p0) {
    if (u < 512) { i = 2 * ((u >> 2) & 3) + (u & 1); p0 = (u & ~15) + 4 * (u & 2); }
    else { const int v = u - 512; i = (v >> 1) & 7; p0 = 512 + 16 * (v >> 4) + 8 * (v & 1); }
}
DI void mem0_arrange(LAS char* lds, int raw, int tid) {
#pragma unroll
    for (int k = 0; k < 2; ++k) {
        int i, p0; unit_rc(tid + 256 * k, i, p0);
        unsigned r[4];
#pragma unroll
        for (int j = 0; j < 4; ++j) r[j] = *(LAS unsigned*)(lds + raw + (i * DM + pos2col(p0)) * 2 + 32 * j);
        *(LAS u32x4*)(lds + L_MEMS + (tid + 256 * k) * 16) = (u32x4){__builtin_amdgcn_perm(r[1], r[0], 0x05040100), __builtin_amdgcn_perm(r[3], r[2], 0x05040100),
                                                                     __builtin_amdgcn_perm(r[1], r[0], 0x07060302), __builtin_amdgcn_perm(r[3], r[2], 0x07060302)};
    }
    int i, p0; unit_rc(512 + (tid & 63), i, p0);
    *(LAS u32x4*)(lds + L_MEMS + 8192 + (tid & 63) * 16) = *(LAS u32x4*)(lds + raw + (i * DM + p0) * 2);
}
DI unsigned read_canaries(int s, rsrc_t rs, int lane, int& budget) {
    const int o = 2 * OFF_CAN + (s & 3) * 16384 + (lane >> 2) * 512 + (lane & 3) * 16;
    u32x2 v;
    for (;;) {
        v = ld64(rs, o, 0);
        asm volatile("" ::: "memory");
        if (__all((v.y & 0xffff) == (unsigned)(s + 1))) break;
        if (--budget < 0) break;
    }
    const unsigned t = 8 * now();
    return t + (unsigned)wave_max32(min(max((int)(v.x - t), -0x4000), 0x4000));
}
DI u32x8 sm_frag(LAS char* lds, int sl, int r0, int sa, int sH, int cb, int lo8) {
    auto rd = [&](int r) { return tr_read(lds, sl + r * 256 + ((cb ^ (2 * swz(r))) * 16) + lo8); };
    const u32x2 a = rd(r0), b = rd(r0 + sa), c = rd(r0 + sH), d = rd(r0 + sa + sH);
    return (u32x8){a.x, a.y, b.x, b.y, c.x, c.y, d.x, d.y};
}
#define GS(slot, i)
#define GV(slot, i, v)
DI int tp_unit(int U, int sel) { return OFF_TP + cmap((U & 511) * 4096) + ((sel >> (3 + (U >> 8 & 1)) ^ U >> 9) & 1) * 8192; }
template <int N> DI void finish(const Args& a, rsrc_t rw, int U, int sel, unsigned t, int& budget) {
    const int lane = opaque_lane(), n = lane & 7, g = lane >> 4;
    const rsrc_t ru = __builtin_amdgcn_make_buffer_rsrc(a.out, 0, SEQ * DM * 2, 0x00020000);
    constexpr int M = (N + 1) / 2;
    int at[M]; f32x2 bv[M]; float gt[M];
#pragma unroll
    for (int i = 0; i < M; ++i) {
        const int u = U + 2 * i + (lane >> 3 & 1 & (N - 1));
        const int tok = LATE * SEG + (U < 512 ? 2 * (u & 15) + (n >> 2) : (u & 32) + 4 * (u & 7) + (n >> 1));
        const int col = U < 512 ? 32 * (u >> 4) + 16 * (g >> 1) + 4 * (n & 3) + 2 * (g & 1) : 512 + 2 * (u & 24) + 8 * (n & 1) + 2 * g;
#pragma unroll
        for (int r = 0; r < 2; ++r) bv[i][r] = bf2f(__builtin_amdgcn_raw_buffer_load_b16(rw, col * BO_ROW + tok * 2, OFF_BO + r * BO_ROW, 16));
        gt[i] = __uint_as_float(ld32(rw, tok * 4, OFF_GT));
        at[i] = (tok * DM + col) * 2;
    }
    u32x4 one;
#pragma unroll
    for (int j = 0; j < 4; ++j) one[j] = (lane >> 2 & 3) == j ? 0x3F80u << 16 * (lane & 1) : 0u;
#pragma unroll
    for (int i = 0; i < M; ++i) { bv[i] *= 1.0f - gt[i]; asm volatile("" : "+v"(bv[i]), "+v"(one)); }
    (void)wait_until(t);
    f32x4 v[N];
    for (;;) {
        u32x4 d[4 * N];
#pragma unroll
        for (int k = 0; k < 4 * N; ++k) d[k] = ld128(rw, lane * 16 + (k & 3) * 1024, tp_unit(U + (k >> 2), sel));
        unsigned top = 0;
#pragma unroll
        for (int i = 0; i < N; ++i) {
            asm volatile(M16 "%0, %1, %2, 0\n\t" M16 "%0, %1, %3, %0\n\t" M16 "%0, %1, %4, %0\n\t" M16 "%0, %1, %5, %0\n\ts_nop 7" : "=&v"(v[i]) : "v"(one), "v"(d[4 * i]), "v"(d[4 * i + 1]), "v"(d[4 * i + 2]), "v"(d[4 * i + 3]));
            top = mx4(mx4(mx4(mx4(top, d[4 * i]), d[4 * i + 1]), d[4 * i + 2]), d[4 * i + 3]);
        }
        if (!__any(top == SENT) || --budget < 0) break;
        __builtin_amdgcn_s_sleep(4);
    }
#pragma unroll
    for (int i = 0; i < M; ++i) {
        f32x2 x;
#pragma unroll
        for (int r = 0; r < 2; ++r) {
            auto both = [](float y) { return y + __int_as_float(__builtin_amdgcn_update_dpp(0, __float_as_int(y), 0x128, 0xf, 0xf, true)); };
            const float p = both(v[2 * i][r]), q = both(v[(2 * i + 1) & (N - 1)][r]);
            x[r] = gt[i] * (lane & 8 ? q : p) + bv[i][r];
        }
        __builtin_amdgcn_raw_buffer_store_b32(pack2(x[0], x[1]), ru, at[i], 0, OUT_AUX);
    }
}
DI void owner_loop(const Args& a, int G, rsrc_t rs, rsrc_t rw, int sel, unsigned tbase, unsigned cbase, int& budget) {
    const int lane = threadIdx.x & 63, n = lane & 15, g = lane >> 4;
    const bool ex = G >= 64;
    const u32x4 sent4 = {SENT, SENT, SENT, SENT};
    const int js = (n & 2) ? 8 : ex ? (n & 4) + 2 * (n & 1) + (n >> 3) : 2 * (n >> 2) + (n & 1);
    u32x4 selu;
#pragma unroll
    for (int j = 0; j < 4; ++j) selu[j] = (js == 2 * j ? 0x3F80u : 0u) | (js == 2 * j + 1 ? 0x3F800000u : 0u);
    const int o = part_unit(G) + lane * 16;
    const int so = cmap((n >> 3) * 8192 + (G + (G & 64)) * 128 + (g & 1) * 4 + (ex ? (n & 4) * 16 + (g >> 1) * 32 + (n & 3) * 8 : (n & 6) * 16 + (g >> 1) * 16 + (n & 1) * 8));
    unsigned fails = 0;
    const int c8 = G >= 64 ? a.c1 >> 16 & 255 : G < 8 ? a.c1 >> 8 & 255 : a.c1 & 255;
    const bool gv_on = true; (void)gv_on;
    unsigned oT = now(), oC = (unsigned)__builtin_readcyclecounter(), oR;
    { const unsigned r = (unsigned)(512.0f * (float)(oC - cbase) * __builtin_amdgcn_rcpf((float)(oT - tbase))); asm volatile("s_nop 0\n\tv_readfirstlane_b32 %0, %1" : "=s"(oR) : "v"(r)); }
    oT = 8 * oT + 16;
    unsigned lk = oC;
    budget = __builtin_amdgcn_readfirstlane(budget);
    const int ca = 2 * OFF_CAN + (lane >> 2) * 512 + (blockIdx.x & (NCP - 1)) * 128 + (lane & 3) * 16;
    unsigned tc;
    for (int s = 0; s < NSEG - 1; ++s) {
        const int pb = (s & 1) * 2 * PART_STEP;
        int np;
        asm volatile(OWNER_ASM : [np] "=&s"(np), [tm] "=&s"(tc), [bud] "+s"(budget), [oT] "+s"(oT), [oC] "+s"(oC), [oR] "+s"(oR), [lk] "+s"(lk)
            : [c8] "s"(c8), [ca] "v"(ca), [cs] "s"((s & 3) * 16384), [tag] "s"(s + 1), [o] "v"(o), [so] "v"(so), [sel] "v"(selu), [rs] "s"(rs), [pb] "s"(pb), [mb0] "s"(2 * (OFF_MEMB + ((s + 1) & 3) * MEMB_STRIDE)),
              [cw] "s"(a.cw - c8), [rd] "s"(a.rd), [sv] "v"(sent4), [ro] "v"(s ? o : OOB), [pq] "s"(2 * PART_STEP - pb), [rm] "v"(s > 2 ? so : OOB), [mq] "s"(2 * (OFF_MEMB + ((s + 2) & 3) * MEMB_STRIDE)),
              [rw] "s"(rw), [rt] "v"(s >= 24 && s < 56 ? lane * 16 : OOB), [tq] "s"(tp_unit(8 * G + (s >> 2 & 7), sel) + (s & 3) * 1024) : OWNER_CLOBBER);
        fails += np > 1;
    }
    wait_prog(rw, F_S4 + lane * 4, true, 1, budget);
    const unsigned t = now(), t0 = t + min((int)(tc - 8 * t) >> 3, 100);
    finish<4>(a, rw, 4 * G + 4 * (G & 64), sel, t0 + (a.xa >> 8 & 255), budget);
    if (G >= 32 && G < 64) finish<1>(a, rw, 512 + G, sel, t0 + (a.xb >> 24), budget);
}
#define CORNER 4
DI void role_owner(const Args& a, unsigned lds_base, int ow, rsrc_t rw, int& budget) {
    const int tid = threadIdx.x, lane = tid & 63, w = __builtin_amdgcn_readfirstlane(tid >> 6);
    const unsigned tbase = now(), cbase = (unsigned)__builtin_readcyclecounter();
    if ((ow | 4) != 63 && w) { prefetch(a, lds_base, ow & 3, (ow >> 2) * 3 + w - 1, (ow & 3) == 3 ? 42 : 48); VM_DRAIN(); }
    if (w >= 2 || (w == 1 && ow >= 8)) return;
    const u32x2 tag = launch_tag(a);
    if ((ow | 4) == 63) {
        auto rtt = [&](int o) {
            unsigned long long t0, t1;
            asm volatile("s_memtime %0\n\ts_waitcnt lgkmcnt(0)" : "=s"(t0) :: "memory");
            st32(SENT, rw, lane == 0 ? 0 : OOB, o);
            asm volatile("s_waitcnt vmcnt(0)\n\ts_memtime %0\n\ts_waitcnt lgkmcnt(0)" : "=s"(t1) :: "memory");
            return (unsigned)(t1 - t0);
        };
        auto die = [&](int o) {
            unsigned t0 = ~0u, t1 = ~0u;
            for (int r = 0; r < 4; ++r) { t0 = min(t0, rtt(o + r * 128)); t1 = min(t1, rtt(o + 8192 + r * 128)); }
            return (unsigned)(t1 < t0);
        };
        unsigned best[4] = {~0u, ~0u, ~0u, ~0u}, near = die(ow & 4 ? 0 : OFF_TP);
        if (ow & 4) {
            for (int r = 4; r < 4 + CORNER; ++r)
#pragma unroll
                for (int i = 0; i < 4; ++i) best[i] = min(best[i], rtt(near * 8192 + cmap(i * 4096) + r * 128));
            near += 2 * (best[0] + best[2] < best[1] + best[3]) + 4 * (best[0] + best[1] < best[2] + best[3]);
        } else near += 2 * die(OFF_TP + (2 << 20));
        __builtin_amdgcn_raw_buffer_store_b64((u32x2){(tag.x & ~7u) | near, tag.y}, rw, lane == 0 ? F_INIT + (ow & 4 ? 32 : 161) * 128 : OOB, 0, 16);
    }
    const int sel = wait_init(rw, tag, budget, 32);
    const rsrc_t rs = __builtin_amdgcn_make_buffer_rsrc(a.ws + (sel & 1) * 8192, 0, COMM_BYTES - 8192, 0x00020000);
    const int cc = sel >> 1 ^ a.c1 >> 24;
    const int G = (ow & ~3) + 64 * w + ((cc ^ ow >> 1 ^ w) & 1) + ((cc ^ 2 * ow ^ ow >> 4) & 2);
    owner_loop(a, G, rs, rw, sel, tbase, cbase, budget);
}

template <int O, class V> DI void put8(V& d, u32x8 f) {
#pragma unroll
    for (int j = 0; j < 8; ++j) d[O + j] = f[j];
}
DI f32x4 SMF64(u32x4 a, u32x8 b, f32x4 c, int i) { asm volatile("s_nop 1\n\tv_smfmac_f32_16x16x64_bf16 %0, %1, %2, %3\n\ts_nop 7" : "+v"(c) : "v"(a), "v"(b), "v"(i)); return c; }
DI f32x4 SMF32(u32x2 a, u32x4 b, f32x4 c, int i) { asm volatile("s_nop 1\n\tv_smfmac_f32_16x16x32_bf16 %0, %1, %2, %3\n\ts_nop 7" : "+v"(c) : "v"(a), "v"(b), "v"(i)); return c; }
DI f32x4 MFV(u32x4 a, u32x4 b, f32x4 c) { asm volatile("s_nop 1\n\tv_mfma_f32_16x16x32_bf16 %0, %1, %2, %0\n\ts_nop 7" : "+v"(c) : "v"(a), "v"(b)); return c; }

DI void role_head(const Args& a, LAS char* lds, unsigned lds_base, int h, int tw, rsrc_t rw, int& budget) {
    const int tid = threadIdx.x, w = __builtin_amdgcn_readfirstlane(tid >> 6);
    char* ws = a.ws;
    const unsigned tbase = now(), cbase = (unsigned)__builtin_readcyclecounter();
    const bool gv_on = tw == 0; (void)gv_on;
    u32x32 A0, A1, A2, A3, A4, A5, A6, A7, R0, R1, R2, R3; u32x8 R4;
    u32x2 tag;
    {
        const int lane = tid & 63, n = lane & 15, g = lane >> 4;
#pragma unroll
        for (int j = 0; j < 3; ++j) glds16(a.mem0, min(4 * j + w, 8) * 1024 + lane * 16, lds_base + L_KX + min(4 * j + w, 8) * 1024);
        const int tq = n >> 2, tc = (n & 3) >> 1, lo8 = (n & 1) * 8;
        const int r_nat = 16 * (g >> 1) + 4 * (g & 1) + (tq & 1) + 8 * (tq >> 1);
        const int r_sig = 16 * (tq & 1) + 4 * (g >> 1) + (g & 1) + 2 * (tq >> 1);
        const int r_o = 16 * (tq & 1) + 8 * (g >> 1) + 4 * (g & 1) + (tq >> 1);
#define WFRAG(sl, kb, t) ((kb) < 8 ? sm_frag(lds, sl, r_sig, 32, 8, 4 * w + 2 * (t) + tc, lo8) : sm_frag(lds, sl, r_nat, 2, 32, 4 * w + 2 * (t) + tc, lo8))
        const u32x4 ro = ring_offs(w, lane, HD);
        auto issue = [&](int i) {
            const unsigned slot = lds_base + L_W + (i % C_RING) * 16384 + w * 4096;
            if (i == 23) ring_issue(a.Wo + (size_t)h * DH * DM + 512, ring_offs(w, lane, [](int r, int c) { return (r + 64 * (c >> 3)) * (DM * 2) + (c & 7) * 16; }), slot);
            else if (i < 28) ring_issue((i < 8 || i == 20 ? a.Wq : i < 16 || i == 21 ? a.Wk : a.Wv) + (size_t)(64 * (i < 16 ? i & 7 : i < 20 ? i - 12 : i < 24 ? 8 : i - 24)) * HD + h * DH, ro, slot);
            else ring_issue(a.Wo + (size_t)(h * DH + 64 * ((i & 1) ^ ((i - 28) >> 1))) * DM + 256 * tw + 128 * (i & 1), ring_offs(w, lane, DM), slot);
        };
        auto keep = [&](int at, u32x8 f) {
            *(LAS u32x4*)(lds + at + lane * 16) = (u32x4){f[0], f[1], f[2], f[3]};
            *(LAS u32x4*)(lds + at + 1024 + lane * 16) = (u32x4){f[4], f[5], f[6], f[7]};
        };
        u32x8 hold[8] = {};
#pragma unroll
        for (int i = 0; i < C_RING; ++i) issue(i);
        tag = launch_tag(a);
        ring_wait<4 * C_RING>();
        mem0_arrange(lds, L_KX, tid);
        static_for<9>([&](auto ic) {
            constexpr int G = decltype(ic)::value;
            ring_wait<(G < 7 ? 16 : G == 7 ? 8 : 0)>();
            const int sl = L_W + (G & 1) * 65536;
            auto fr = [&](int j, int t) { return WFRAG(sl + ((j ^ w) << 14), 0, t); };
            if constexpr (G < 4) {
                u32x32& A = G == 0 ? A0 : G == 1 ? A2 : G == 2 ? A4 : A6;
                u32x32& B = G == 0 ? A1 : G == 1 ? A3 : G == 2 ? A5 : A7;
                put8<0>(A, fr(0, 0)); put8<8>(A, fr(0, 1)); put8<16>(A, fr(1, 0)); put8<24>(A, fr(1, 1));
                put8<0>(B, fr(2, 0)); put8<8>(B, fr(2, 1)); put8<16>(B, fr(3, 0)); put8<24>(B, fr(3, 1));
                if constexpr (G == 0) asm volatile("" : "+{a[0:31]}"(A0), "+{a[32:63]}"(A1));
                if constexpr (G == 1) asm volatile("" : "+{a[64:95]}"(A2), "+{a[96:127]}"(A3));
                if constexpr (G == 2) asm volatile("" : "+{a[128:159]}"(A4), "+{a[160:191]}"(A5));
                if constexpr (G == 3) asm volatile("" : "+{a[192:223]}"(A6), "+{a[224:255]}"(A7));
            } else if constexpr (G == 4) {
                put8<16>(R1, fr(0, 0)); put8<24>(R1, fr(0, 1)); put8<0>(R2, fr(1, 0)); put8<8>(R2, fr(1, 1));
                put8<16>(R2, fr(2, 0)); put8<24>(R2, fr(2, 1)); put8<0>(R3, fr(3, 0)); put8<8>(R3, fr(3, 1));
            } else if constexpr (G == 5) {
                put8<16>(R0, WFRAG(sl, 8, 0)); put8<24>(R0, WFRAG(sl, 8, 1)); put8<0>(R1, WFRAG(sl + 16384, 8, 0)); put8<8>(R1, WFRAG(sl + 16384, 8, 1));
                put8<16>(R3, WFRAG(sl + 32768, 8, 0)); put8<24>(R3, WFRAG(sl + 32768, 8, 1));
                hold[7] = sm_frag(lds, sl + 49152, r_o, 2, 32, 8 * (w >> 1) + 4 * tw + 2 * (w & 1) + tc, lo8);
            } else if constexpr (G == 6) {
#pragma unroll
                for (int j = 0; j < 7; ++j) hold[j] = fr(j >> 1, j & 1);
                R4 = fr(3, 1);
            } else {
                if constexpr (G == 8)
#pragma unroll
                    for (int T = 0; T < 4; ++T) keep(L_W + 65536 + w * 8192 + T * 2048, hold[T]);
#pragma unroll
                for (int T = 0; T < 4; ++T) hold[T] = sm_frag(lds, L_W + (2 * G - 10 + (w >> 1)) * 16384, r_o, 2, 32, 8 * (w & 1) + 2 * T + tc, lo8);
            }
            if constexpr (G != 7) WG_BARRIER();
            if constexpr (G < 6)
#pragma unroll
                for (int i = 0; i < 4; ++i) issue(4 * G + 8 + i);
            if constexpr (G == 6)
#pragma unroll
                for (int j = 0; j < 8; ++j) keep(L_W + (j >> 1) * 16384 + w * 4096 + (j & 1) * 2048, hold[j]);
            if constexpr (G == 8)
#pragma unroll
                for (int T = 0; T < 4; ++T) keep(L_W + 98304 + w * 8192 + T * 2048, hold[T]);
        });
        const int ai = 2 * (n >> 2) + (n & 1), ahf = (n >> 1) & 1;
        R0[0] = R0[1] = R0[2] = R0[3] = 0x3F803F80u;
        R0[4] = ahf ? 0xEEEE : 0x4444;
        R0[5] = (g >> 1) * 512 + (n >> 2) * 128 + (g & 1) * 64 + (n & 3) * 16;
        R0[6] = lds_base + L_MEMS + 8192 + g * 256 + ai * 32 + ahf * 16;
        R0[7] = lane * 16;
        R0[8] = lds_base + L_Q + 2 * g * 320 + (16 * w + n) * 4;
        R0[9] = lds_base + L_KO + 2 * g * 576 + (16 * w + (n & 8) + 4 * (n & 1) + ((n >> 1) & 3)) * 4;
        R0[10] = lds_base + L_Q + ai * 320 + g * 16;
        R0[11] = lds_base + L_KO + ai * 576 + g * 32 + ahf * 16;
        R0[12] = lds_base + L_KX + 8192 + w * 2048 + (g >> 1) * 256 + n * 16 + (g & 1) * 8;
        R0[13] = lds_base + L_W + w * 4096 + lane * 16;
        R0[14] = part_unit((2 * (4 * tw + w) + (n >> 3)) * 4 + g) + h * 128 + (n & 7) * 16;
        R0[15] = (g >> 1) == (w >> 1) ? part_unit(64 + 2 * (2 * tw + (w & 1)) + (g >> 1)) + h * 128 + (g & 1) * 64 + n * 4 : OOB;
        asm volatile("" : "+{v[120:151]}"(R0), "+{v[152:183]}"(R1), "+{v[184:215]}"(R2), "+{v[216:247]}"(R3), "+{v[248:255]}"(R4));
    }
    const int sel = wait_init(rw, tag, budget, 33 + 4 * h + w);
    int kv_rdy = 0;
    const rsrc_t rs = __builtin_amdgcn_make_buffer_rsrc(ws + (sel & 1) * 8192, 0, COMM_BYTES - 8192, 0x00020000);
    auto dma = [&](int voff, int soff, unsigned dst) { bdma(rw, voff, soff, dst); };
    auto fetch_kv = [&](int s, int lane) {
        const int n = lane & 15, g = lane >> 4;
        const int kxs = (g >> 1) * 1024 + ((g & 1) * 32 + 16 * (n >> 3) + (n & 7)) * 16;
        const int vxs = (g >> 1) * 1024 + ((g & 1) * 32 + n) * 16;
        const int so_ = (s * 32 + h) * 8192 + w * 2048;
        const unsigned xdst = lds_base + L_KX + w * 2048;
        dma(kxs, OFF_KF + so_, xdst); dma(kxs, OFF_KF + so_ + 128, xdst + 1024);
        dma(vxs, OFF_VF + so_, xdst + 8192); dma(vxs, OFF_VF + so_ + 256, xdst + 8192 + 1024);
    };
    const int fo = F_KVM + h * 256 + tw * 128;
    u32x8 scon;
    scon[0] = w; scon[1] = 2 * OFF_CAN + (h >> 1) * 512 + (2 * (h & 1) + tw) * 16; scon[2] = F_KV + h * 512 + w * 128; scon[3] = 65536 + w * 4096;
    scon[4] = OFF_KF + h * 8192 + w * 2048; scon[5] = fo; scon[6] = tw - 1; scon[7] = (tw ? OFF_VM : OFF_KM) + (h - NH) * 2048 + w * 512;
    u32x8 S1 = {0u, 0u, (unsigned)kv_rdy, (unsigned)budget, (unsigned)a.d0, 0xFFFFFFFFu / 22, (unsigned)(a.c2 + a.b0), 0u};
    u32x8 S2 = {0u, 0u, 0u, (unsigned)a.up, (unsigned)a.dn, (unsigned)a.c2, 0u, 1u};
    fetch_kv(0, opaque_lane());
    __builtin_amdgcn_raw_buffer_store_b64((u32x2){8 * now() + a.e0, 1u | (a.c2 + a.b0) << 16}, rs, tid < NCP ? tid * 128 : OOB, scon[1], 16);
    asm volatile("s_waitcnt vmcnt(1)" ::: "memory");
    u32x2 vm = {};
    u32x8 S3;
    {
        const unsigned t = now(), c = (unsigned)__builtin_readcyclecounter();
        const float r = (float)(c - cbase) * __builtin_amdgcn_rcpf(8.0f * (float)(t - tbase));
        S2[1] = 8 * t + 16; S1[5] = (unsigned)(4294967296.0f * __builtin_amdgcn_rcpf(r));
        S3 = (u32x8){c, (unsigned)(4096.0f * r), 2u * OFF_MEMB + ((h >> 3 ^ sel >> 2 ^ a.c1 >> 26) & 1) * 16384, w == 3 ? ~0u : 0u};
    }
    const int U0 = (2 * tw + (w >> 1)) * 64 + 8 * (w & 1), U1 = 4 * (2 * h + tw) + w;
    for (;;) {
        asm volatile(STEP_ASM
            : "+{v[96:97]}"(vm), "+{v[0:7]}"(S1), "+{v[8:15]}"(S2), "+{v[16:23]}"(S3), "+{v[24:31]}"(scon)
              , "+{a[0:31]}"(A0), "+{a[32:63]}"(A1), "+{a[64:95]}"(A2), "+{a[96:127]}"(A3), "+{a[128:159]}"(A4), "+{a[160:191]}"(A5), "+{a[192:223]}"(A6), "+{v[120:151]}"(R0), "+{v[152:183]}"(R1)
            : "{a[224:255]}"(A7), "{v[184:215]}"(R2), "{v[216:247]}"(R3), "{v[248:255]}"(R4),
              [rs] "s"(rs), [rw] "s"(rw), [qb] "s"(OFF_OF + LATE * 262144 + h * 8192 + (w & 1) * 256),
              [tp] "s"(tp_unit(U0, sel) + h * 128), [tq] "s"(tp_unit(256 + U0, sel) + h * 128), [tx] "s"(tp_unit(512 + (U0 >> 3) + 3 * (w & 1), sel) + h * 128),
              [u] "s"(U1), [ua] "s"(tp_unit(256 + U1, sel)), [fw] "s"(8 * (a.xb >> 16 & 255)), [ru] "s"(__builtin_amdgcn_make_buffer_rsrc(a.out, 0, SEQ * DM * 2, 0x00020000))
            : STEP_CLOBBER);
        const int s = __builtin_amdgcn_readfirstlane(S1[0]);
        const unsigned st = __builtin_amdgcn_readfirstlane(S1[7]);
        kv_rdy = __builtin_amdgcn_readfirstlane(S1[2]); budget = __builtin_amdgcn_readfirstlane(S1[3]);
        if (s == NSEG - 1) break;
        if (__builtin_expect(st & 2, 0)) {
            const int lane = opaque_lane();
            LAS char* z = (LAS char*)0;
            const f32x4 zero = {0, 0, 0, 0};
            const u32x4 ones = __builtin_shufflevector(R0, R0, 0, 1, 2, 3);
            const unsigned zr = lane >> 6;
            const int idx = R0[4];
            u32x4 qf[4];
            f32x4 s1 = zero, s2 = zero;
#pragma unroll
            for (int ks = 0; ks < 4; ++ks) {
                qf[ks] = *(LAS u32x4*)(z + R0[10] + ks * 64);
                s1 = MFV(*(LAS u32x4*)(lds + L_KX + lane * 16 + ks * 2048), qf[ks], s1);
                s2 = MFV(*(LAS u32x4*)(lds + L_KX + lane * 16 + ks * 2048 + 1024), qf[ks], s2);
            }
            f32x4 s0 = SMF64(*(LAS u32x4*)(z + R0[11]), __builtin_shufflevector(qf[0], qf[1], 0, 1, 2, 3, 4, 5, 6, 7), zero, idx);
            s0 = SMF64(*(LAS u32x4*)(z + R0[11] + 128), __builtin_shufflevector(qf[2], qf[3], 0, 1, 2, 3, 4, 5, 6, 7), s0, idx);
            const float sa = s0[0] + s0[2], sb = s0[1] + s0[3];
            float mx = fmaxf(fmaxf(fmaxf(fmaxf(s1[0], s1[1]), fmaxf(s1[2], s1[3])), fmaxf(fmaxf(s2[0], s2[1]), fmaxf(s2[2], s2[3]))), fmaxf(sa, sb));
            mx = fmaxf(mx, __shfl_xor(mx, 16)); mx = fmaxf(mx, __shfl_xor(mx, 32));
            auto ex2 = [&](float x, float y) { return pack2(__builtin_amdgcn_exp2f(x - mx), __builtin_amdgcn_exp2f(y - mx)); };
            const u32x4 px = {ex2(s1[0], s1[1]), ex2(s1[2], s1[3]), ex2(s2[0], s2[1]), ex2(s2[2], s2[3])};
            const unsigned pm = ex2(sa, sb);
            const f32x4 l = MFV(((u32x4){pm, zr, zr, zr}), ones, MFV(px, ones, zero));
            const u32x2 x0 = *(LAS u32x2*)(z + R0[12]), y0 = *(LAS u32x2*)(z + R0[12] + 1024), x1 = *(LAS u32x2*)(z + R0[12] + 512), y1 = *(LAS u32x2*)(z + R0[12] + 1536);
            const f32x4 o = SMF64(px, ((u32x8){x0.x, y0.x, x0.y, y0.y, x1.x, y1.x, x1.y, y1.y}), zero, idx) + SMF32(((u32x2){pm, zr}), ((u32x4){vm.x, vm.y, zr, zr}), zero, idx);
            const float i0 = __builtin_amdgcn_rcpf(l[0]), i1 = __builtin_amdgcn_rcpf(l[1]);
            *(LAS unsigned*)(z + R0[9] + 256) = pack2(o[0] * i0, o[2] * i0);
            *(LAS unsigned*)(z + R0[9] + 256 + 576) = pack2(o[1] * i1, o[3] * i1);
            WG_BARRIER();
            const u32x4 oa = *(LAS u32x4*)(z + R0[11] + 256 + (w >> 1) * 128), ob = *(LAS u32x4*)(z + R0[11] + 384 - (w >> 1) * 128);
            auto frag = [&](int at) { return __builtin_shufflevector(*(LAS u32x4*)(z + at), *(LAS u32x4*)(z + at + 1024), 0, 1, 2, 3, 4, 5, 6, 7); };
            f32x2 f[4];
#pragma unroll
            for (int T = 0; T < 4; ++T) f[T] = fold(SMF64(ob, frag(R0[13] + scon[3] + 32768 + T * 2048), SMF64(oa, frag(R0[13] + scon[3] + T * 2048), zero, idx), idx));
            const f32x2 fx = fold(SMF64(ob, frag((R0[13] ^ 8192) + 49152 + 2048), SMF64(oa, frag(R0[13] + 49152 + 2048), zero, idx), idx));
            const int pb = (s & 1) * 2 * PART_STEP;
            st128((u32x4){pack2(f[0].x, f[1].x), pack2(f[2].x, f[3].x), pack2(f[0].y, f[1].y), pack2(f[2].y, f[3].y)}, rs, R0[14], pb);
            st32(pack2(fx.x, fx.y), rs, R0[15], pb);
            if (s == LATE) { S1[7] = 4; continue; }
        }
        if (__builtin_expect((st & 2) || kv_rdy <= s, 0)) {
            while (kv_rdy <= s) { kv_rdy = prog_min(rw, F_KV + h * 512 + w * 128); if (--budget < 0) break; }
            fetch_kv(s + 1, opaque_lane());
        }
        if (st & 2) { S1[1] = read_canaries(s, rs, opaque_lane(), budget) + S1[6]; S2[2] += 0x10000; }
        if ((int)(S1[1] - S2[1]) > 0x8000) { S2[1] = 8 * now() + 16; S3[0] = (unsigned)__builtin_readcyclecounter(); }
        S1[0] = s + 1; S1[2] = kv_rdy; S1[3] = budget;
    }
    TT(true, 0)
    st32((unsigned)(NSEG - 2), rw, opaque_lane() == 0 && w == 0 ? fo : OOB, 0);
    TT(true, 2)
}

__global__ void __launch_bounds__(256, 1) fused_kernel(Args a) {
    extern __shared__ __attribute__((aligned(16))) char smem[];
    LAS char* lds = (LAS char*)smem;
    const unsigned lds_base = (unsigned)(uintptr_t)lds;
    const int w = __builtin_amdgcn_readfirstlane(threadIdx.x >> 6);
    const int b = blockIdx.x, bx = b & 7, bi = b >> 3;
    if (bi >= 28) {
        const void* pc; unsigned len;
        asm volatile("s_getpc_b64 %0\n1:\ts_mov_b32 %1, .Lstep_end-1b" : "=s"(pc), "=s"(len));
        touch(pc, min((bi - 28) * 32768 + threadIdx.x * 128, len), lds_base);
    }
    asm volatile("" :: "s"(a.Wq), "s"(a.cw));
    const rsrc_t rw = __builtin_amdgcn_make_buffer_rsrc(a.ws, 0, WS_USED, 0x00020000);
    int budget = SPIN_MAX;
#define RT(i)
    if (bx < 4) {
        if (bi < 16) role_head(a, lds, lds_base, 8 * bx + (bi >> 1), bi & 1, rw, budget);
        else role_owner(a, lds_base, 4 * (bi - 16) + bx, rw, budget);
    } else {
        const int fx = bx - 4, no = fx < 3 ? 14 : 22, j = 14 * fx + bi;
        if (bi >= no || (j & 2)) {
            const unsigned t = now() + (a.d1 & 0xffff);
            if (bi >= no) { if (w) prefetch(a, lds_base, bx, (bi - no) * 3 + w - 1, 3 * (32 - no)); }
            else {
                const int sh = (j >> 2) * 2 + (j & 1);
#pragma unroll
                for (int i = 0; i < INIT_SHARE / 4096; ++i) st128((u32x4){SENT, SENT, SENT, SENT}, rw, threadIdx.x * 16 + i * 4096, sh * INIT_SHARE);
#pragma unroll
                for (int i = 0; i < 8; ++i) st128((u32x4){SENT, SENT, SENT, SENT}, rw, threadIdx.x * 16, i < 4 ? (i & 2 ? OFF_VM : OFF_KM) + FAST * 65536 + (2 * sh + (i & 1)) * 4096 : OFF_OF + LATE * 262144 + (4 * sh + i - 4) * 4096);
                const u32x2 tag = launch_tag(a);
                VM_DRAIN();
                WG_BARRIER();
                __builtin_amdgcn_raw_buffer_store_b64(tag, rw, threadIdx.x == 0 ? F_INIT + sh * 128 : OOB, 0, 16);
                RT(3)
            }
            (void)wait_until(t);
        }
        if (bi >= no) {
            const int t = 18 * fx + bi - no;
            role_s4d(a, lds, lds_base, t, rw, budget);
            RT(1)
            TT(true, 6)
            if (t < 9 * OUT_REP) role_outp(a, lds, lds_base, t % 9, t / 9, rw, budget);
        }
        else {
            if (!(j & 2)) role_kv(a, lds, lds_base, (j >> 2) * 2 + (j & 1), rw, budget);
            RT(1)
            TT(true, 6)
            role_qa(a, lds, lds_base, (j >> 2) * 2, (j & 2 ? 0 : FAST + 2) + (j & 1), (j & 2 ? FAST : FAST + 2) + (j & 1), rw, budget);
        }
    }
    RT(2)
    TT(true, 5)
    if (budget < 0 && threadIdx.x == 0) st32(0x1000u + b, rw, F_ERR, 0);
}

static int g_c1 = 56, g_c1m = 52, g_c1x = 70, g_c2 = 348, g_b0 = 64, g_up = 16, g_fl = 8, g_dn = 3, g_d0 = 1680, g_da = 100, g_ua = 48, g_d1 = 300, g_rd = 34, g_cw = 69, g_e0 = 165, g_x1 = 35, g_x2 = 140, g_x3 = 150, g_x4 = 75, g_x5 = 148, g_x6 = 150, g_x7 = 50, g_x8 = 210, g_pc = 45, g_cc = 0;
extern "C" void kernel_launch(void* const* d_in, const int* in_sizes, int n_in, void* d_out, int out_size, void* d_ws, size_t ws_size, hipStream_t stream) {
    Args a;
    a.x = (const u16*)d_in[0]; a.mem0 = (const u16*)d_in[1];
    a.Wq = (const u16*)d_in[2]; a.Wk = (const u16*)d_in[3]; a.Wv = (const u16*)d_in[4]; a.Wo = (const u16*)d_in[5];
    a.lAr = (const u16*)d_in[8]; a.Aim = (const u16*)d_in[9]; a.Cr = (const u16*)d_in[10]; a.Ci = (const u16*)d_in[11];
    a.ldt = (const u16*)d_in[12]; a.Dsk = (const u16*)d_in[13]; a.gw = (const u16*)d_in[14];
    a.out = (u16*)d_out;
    a.ws = (char*)(((uintptr_t)d_ws + ((2u << 20) - 1)) & ~(uintptr_t)((2u << 20) - 1));
    a.c1 = g_c1 | g_c1m << 8 | g_c1x << 16 | g_cc << 24; a.c2 = g_c2; a.d0 = g_d0 + g_da; a.b0 = g_b0; a.d1 = g_d1 | g_pc << 16; a.up = g_up | g_ua << 8 | g_fl << 16; a.dn = g_dn | g_d0 << 8; a.rd = 23 * g_rd - 300; a.cw = 8 * g_cw - 104; a.e0 = 8 * g_e0;
    a.xa = g_x1 | g_x2 << 8 | g_x5 << 16 | g_x7 << 24; a.xb = g_x6 | g_x3 << 8 | g_x4 << 16 | g_x8 << 24;
    (void)hipFuncSetAttribute((const void*)fused_kernel, hipFuncAttributeMaxDynamicSharedMemorySize, L_TOTAL);
    fused_kernel<<<256, 256, L_TOTAL, stream>>>(a);
}
```

```cpp
#include <hip/hip_runtime.h>
#include <hip/hip_bf16.h>
#include <stdint.h>

typedef unsigned short u16;
typedef __bf16 bf16x8 __attribute__((ext_vector_type(8)));
typedef __bf16 bf16x16 __attribute__((ext_vector_type(16)));
typedef _Float16 f16x2 __attribute__((ext_vector_type(2)));
typedef _Float16 f16x8 __attribute__((ext_vector_type(8)));
typedef short s16x4 __attribute__((ext_vector_type(4)));
typedef float f32x2 __attribute__((ext_vector_type(2)));
typedef float f32x4 __attribute__((ext_vector_type(4)));
typedef float f32x16 __attribute__((ext_vector_type(16)));
typedef unsigned u32x2 __attribute__((ext_vector_type(2)));
typedef unsigned u32x4 __attribute__((ext_vector_type(4)));
typedef unsigned u32x8 __attribute__((ext_vector_type(8)));
typedef unsigned u32x16 __attribute__((ext_vector_type(16)));
typedef unsigned u32x32 __attribute__((ext_vector_type(32)));
typedef short s16x8 __attribute__((ext_vector_type(8)));
#define LAS __attribute__((address_space(3)))
#define DI __device__ __forceinline__
typedef __amdgpu_buffer_rsrc_t rsrc_t;

constexpr int SEQ = 2048, DM = 576, NH = 32, DH = 128, HD = NH * DH, NM = 8, SEG = 32, NSEG = 64, NS = 128;
constexpr unsigned SENT = 0xFFFFFFFFu;
constexpr unsigned OOB = 0x7FFFFFF0u;
constexpr int SPIN_MAX = 1 << 20;

constexpr int PART_STEP = 16384;
constexpr int OFF_MEMB = 40 * PART_STEP;
constexpr int MEMB_STRIDE = 32768;
constexpr int OFF_CAN = 52 * PART_STEP;
constexpr int COMM_LOGICAL = OFF_CAN + 32768;
constexpr int COMM_BYTES = 2 * COMM_LOGICAL;
constexpr int F_ERR = COMM_BYTES;
constexpr int F_KV = F_ERR + 128;
constexpr int F_KVM = F_KV + 16384;
constexpr int F_O = F_KVM + 8192;
constexpr int F_H = F_O + 4096;
constexpr int F_S4 = F_H + 32768;
constexpr int F_DONE = F_S4 + 512;
constexpr int INIT_SHARE = 14 * 4096, INIT_BYTES = 32 * INIT_SHARE;
constexpr int F_INIT = INIT_BYTES;
constexpr int F_CL = F_INIT + 24576;
constexpr int F_EPOCH = F_CL + 16384;
static_assert(F_DONE + 128 <= INIT_BYTES, "");
static_assert(F_EPOCH + 128 <= (2 << 20), "hand-off region");
constexpr int OFF_TP = 2 << 20;
constexpr int OFF_KF = 6 << 20;
constexpr int OFF_VF = OFF_KF + SEQ * HD * 2;
constexpr int OFF_OF = OFF_VF + SEQ * HD * 2;
constexpr int OFF_KM = OFF_OF + SEQ * HD * 2;
constexpr int OFF_VM = OFF_KM + NSEG * NH * NM * DH * 2;
constexpr int FAST = NSEG - 6;
constexpr int LATE = NSEG - 2;
constexpr int OFF_BO = OFF_VM + NSEG * NH * NM * DH * 2;
constexpr int BO_ROW = 4352;
constexpr int OFF_GT = OFF_BO + DM * BO_ROW;
constexpr int OFF_DBG = OFF_GT + SEQ * 4;
constexpr int WS_USED = OFF_DBG + (4 << 20);

struct Args {
    const u16 *x, *mem0, *Wq, *Wk, *Wv, *Wo, *lAr, *Aim, *Cr, *Ci, *ldt, *Dsk, *gw;
    u16* out; char* ws;
    int c1, c2, d0, b0, d1, up, dn, rd, cw, e0;
    unsigned xa, xb;
};

DI float bf2f(u16 b) { return __uint_as_float((unsigned)b << 16); }
DI float bflo(unsigned w) { return __uint_as_float(w << 16); }
DI float bfhi(unsigned w) { return __uint_as_float(w & 0xFFFF0000u); }
DI u16 f2bf(float f) { return __builtin_bit_cast(u16, (__bf16)f); }
DI unsigned pack2(float lo, float hi) {
    typedef __bf16 bf16x2 __attribute__((ext_vector_type(2)));
    bf16x2 v = {(__bf16)lo, (__bf16)hi};
    return __builtin_bit_cast(unsigned, v);
}
#define WG_BARRIER() asm volatile("s_waitcnt lgkmcnt(0)\n\ts_barrier" ::: "memory")
#define VM_DRAIN() asm volatile("s_waitcnt vmcnt(0)" ::: "memory")
DI int cmap(int o) { return (o & 0x1FFF) | ((o >> 13) << 14); }
DI int part_unit(int G, int h) { const int p = G < 64 ? G & 31 : 32 + (G & 3); return (p >> 2) * 131072 + cmap((p & 3) * 4096) + ((G >> (G < 64 ? 5 : 2) ^ p >> 3) & 1) * 2048 + (h >> 4) * 65536 + (h & 15) * 128; }
DI unsigned mx4(unsigned a, u32x4 v) { a = max(max(a, v.x), v.y); return max(max(a, v.z), v.w); }
DI unsigned now() { return (unsigned)__builtin_amdgcn_s_memrealtime(); }
DI unsigned wait_until(unsigned t) {
    unsigned v; int guard = 8192;
    do { v = now(); } while ((int)(t - v) >= 4 && --guard > 0);
    const int r = __builtin_amdgcn_readfirstlane((int)(t - v));
    asm volatile("s_cmp_lt_i32 %0, 1\n\ts_cbranch_scc1 1f\n\ts_nop 3\n\ts_cmp_lt_i32 %0, 2\n\ts_cbranch_scc1 1f\n\ts_nop 3\n\ts_cmp_lt_i32 %0, 3\n\ts_cbranch_scc1 1f\n\ts_nop 7\n1:" :: "s"(r) : "scc");
    return r > 0 ? t : v + 2;
}
DI void st128(u32x4 v, rsrc_t rs, int off, int soff) {
    __builtin_amdgcn_raw_buffer_store_b128(v, rs, off, soff, 16);
    asm volatile("s_nop 1" :: "v"(v) : "memory");
}
DI void st32(unsigned v, rsrc_t rs, int off, int soff) { __builtin_amdgcn_raw_buffer_store_b32(v, rs, off, soff, 16); }
DI u32x4 ld128(rsrc_t rs, int off, int soff) { return __builtin_amdgcn_raw_buffer_load_b128(rs, off, soff, 16); }
DI u32x2 ld64(rsrc_t rs, int off, int soff) { return __builtin_amdgcn_raw_buffer_load_b64(rs, off, soff, 16); }
DI unsigned ld32(rsrc_t rs, int off, int soff) { return __builtin_amdgcn_raw_buffer_load_b32(rs, off, soff, 16); }
DI f32x2 fold(f32x4 a) { return (f32x2){a[0], a[1]} + (f32x2){a[2], a[3]}; }
DI float dpp_xor1(float v) { return __int_as_float(__builtin_amdgcn_update_dpp(0, __float_as_int(v), 0xB1, 0xf, 0xf, true)); }
DI float red_sum_g(float v) {
    auto a = __builtin_amdgcn_permlane16_swap(__float_as_uint(v), __float_as_uint(v), false, false);
    v = __uint_as_float(a[0]) + __uint_as_float(a[1]);
    auto b = __builtin_amdgcn_permlane32_swap(__float_as_uint(v), __float_as_uint(v), false, false);
    return __uint_as_float(b[0]) + __uint_as_float(b[1]);
}
DI int wave_max32(int d) {
    d = max(d, __builtin_amdgcn_update_dpp(d, d, 0x121, 0xf, 0xf, false));
    d = max(d, __builtin_amdgcn_update_dpp(d, d, 0x122, 0xf, 0xf, false));
    d = max(d, __builtin_amdgcn_update_dpp(d, d, 0x124, 0xf, 0xf, false));
    d = max(d, __builtin_amdgcn_update_dpp(d, d, 0x128, 0xf, 0xf, false));
    return __builtin_amdgcn_readfirstlane(max(max(__builtin_amdgcn_readlane(d, 0), __builtin_amdgcn_readlane(d, 16)), max(__builtin_amdgcn_readlane(d, 32), __builtin_amdgcn_readlane(d, 48))));
}
DI u32x2 tr_read(LAS char* lds, int addr) { return __builtin_bit_cast(u32x2, __builtin_amdgcn_ds_read_tr16_b64_v4i16((LAS s16x4*)(lds + addr))); }
DI void wait_prog(rsrc_t rw, int off, bool act, int thr, int& budget) {
    for (;;) {
        const int v = (int)ld32(rw, off, 0);
        asm volatile("" ::: "memory");
        if (__all(!act || v >= thr)) break;
        if (--budget < 0) break;
        __builtin_amdgcn_s_sleep(1);
    }
}
DI u32x2 launch_tag(const Args& a) {
    unsigned long long z;
    asm volatile("s_load_dwordx2 %0, %1, 0x0\n\ts_waitcnt lgkmcnt(0)" : "=s"(z) : "s"(a.ws + F_EPOCH) : "memory");
    z += 0x9E3779B97F4A7C15ull;
    z = (z ^ (z >> 30)) * 0xBF58476D1CE4E5B9ull; z = (z ^ (z >> 27)) * 0x94D049BB133111EBull; z ^= z >> 31;
    return (u32x2){(unsigned)z, (unsigned)(z >> 32)};
}
DI int wait_init(rsrc_t rw, u32x2 tag, int& budget, int more = 0) {
    const int l = threadIdx.x & 63, o = F_INIT + (l < 32 ? l : l < 62 || !more ? more : l == 63 ? 32 : 161) * 128;
    u32x2 v;
    for (;;) {
        v = ld64(rw, o, 0);
        asm volatile("" ::: "memory");
        if (__all(((v.x ^ tag.x) >> 3) == 0 && v.y == tag.y)) break;
        if (--budget < 0) break;
        __builtin_amdgcn_s_sleep(2);
    }
    return (__builtin_amdgcn_readlane(v.x, 63) & 7) + 8 * (__builtin_amdgcn_readlane(v.x, 62) & 7);
}
DI unsigned die_side() { return (__builtin_amdgcn_s_getreg(4 | 14 << 6) ^ blockIdx.x >> 1) & 1; }
DI int side_at(int x) { const int b = blockIdx.x ^ x; return F_CL + ((b >> 3) * 4 + (b & 3)) * 128; }
DI void tell_side(rsrc_t rw, u32x2 tag) { __builtin_amdgcn_raw_buffer_store_b64((u32x2){(tag.x & ~1u) | die_side(), tag.y}, rw, threadIdx.x == 0 ? 0 : OOB, side_at(0), 16); }
DI int agree_side(rsrc_t rw, u32x2 tag, u32x2 v, int& budget) {
    while (!__all(((v.x ^ tag.x) >> 1) == 0 && v.y == tag.y) && --budget >= 0) { v = ld64(rw, 0, side_at(16)); asm volatile("" ::: "memory"); }
    const unsigned me = die_side();
    return (__builtin_amdgcn_readfirstlane(v.x) ^ me) & 1 ? me : blockIdx.x >> 4 & 1;
}
DI void glds16(const void* base, int voff, unsigned lds_dst) {
    unsigned keep;
    asm volatile("s_mov_b32 %0, m0\n\ts_mov_b32 m0, %3\n\ts_nop 0\n\tglobal_load_lds_dwordx4 %1, %2\n\ts_mov_b32 m0, %0" : "=&s"(keep) : "v"(voff), "s"(base), "s"(lds_dst) : "memory");
}
DI void bdma(rsrc_t rs, int voff, int soff, unsigned lds_dst) {
    unsigned keep;
    asm volatile("s_mov_b32 %0, m0\n\ts_mov_b32 m0, %4\n\ts_nop 0\n\tbuffer_load_dwordx4 %1, %2, %3 offen sc1 lds\n\ts_mov_b32 m0, %0" : "=&s"(keep) : "v"(voff), "s"(rs), "s"(soff), "s"(lds_dst) : "memory");
}
DI int swz(int r) { return ((r & 3) ^ (((r >> 2) & 3) << 1) ^ (((r >> 4) & 1) << 2)) & 7; }
template <class F> DI u32x4 ring_offs(int w, int lane, F at) {
    u32x4 o;
#pragma unroll
    for (int j = 0; j < 4; ++j) { const int r = 16 * w + 4 * j + (lane >> 4); o[j] = at(r, (lane & 15) ^ (2 * swz(r))) - 1024 * j; }
    return o;
}
DI u32x4 ring_offs(int w, int lane, int ld) { return ring_offs(w, lane, [&](int r, int c) { return r * ld * 2 + c * 16; }); }
DI void ring_issue(const u16* src, u32x4 o, unsigned slot) {
    unsigned keep;
    asm volatile("s_mov_b32 %0, m0\n\ts_mov_b32 m0, %6\n\ts_nop 0\n\tglobal_load_lds_dwordx4 %1, %5\n\tglobal_load_lds_dwordx4 %2, %5 offset:1024\n\t"
                 "global_load_lds_dwordx4 %3, %5 offset:2048\n\tglobal_load_lds_dwordx4 %4, %5 offset:3072\n\ts_mov_b32 m0, %0"
                 : "=&s"(keep) : "v"(o.x), "v"(o.y), "v"(o.z), "v"(o.w), "s"(src), "s"(slot) : "memory");
}
#define PF_FIRST 2
constexpr int L_TOTAL = 163840, L_SINK = L_TOTAL - 256;
DI void touch(const void* base, int voff, unsigned lds_base) {
    unsigned keep;
    asm volatile("s_mov_b32 %0, m0\n\ts_mov_b32 m0, %3\n\ts_nop 0\n\tglobal_load_lds_dword %1, %2\n\ts_mov_b32 m0, %0" : "=&s"(keep) : "v"(voff), "s"(base), "s"(lds_base + L_SINK) : "memory");
}
DI void prefetch(const Args& a, unsigned lds_base, int x, int id, int n) {
    const int lane = threadIdx.x & 63;
    const int h0 = x < 4 ? 8 * x : (0x160E0800 >> (8 * (x - 4))) & 255, lpr = x == 5 ? 12 : x == 7 ? 20 : 16, per = 9 * lpr;
    const int first = PF_FIRST * lpr, end = (x < 4 ? 4 : 2) * per;
    const float rl = __builtin_amdgcn_rcpf((float)lpr);
    for (int i = first + id; i < end; i += n) {
        int m = (i >= per) + (i >= 2 * per) + (i >= 3 * per), j = i - m * per;
        if (x < 4 && m < 3) { const int b = i >> 4; m = b < 20 ? b >> 3 : min(b - 20, 2); j = (b < 16 ? b & 7 : b < 20 ? b - 12 : b < 23 ? 8 : b - 23) * 16 + (i & 15); }
        const int mm = m + (x >= 4);
        const int L = j * 64 + lane, row = (int)((L + 0.5f) * rl);
        touch(mm == 0 ? a.Wq : mm == 1 ? a.Wk : mm == 2 ? a.Wv : a.Wo, m == 3 ? h0 * (DH * DM * 2) + L * 128 : row * (HD * 2) + (L - row * lpr) * 128 + h0 * 256, lds_base);
    }
}
template <int N> DI void ring_wait() { asm volatile("s_waitcnt vmcnt(%0) lgkmcnt(0)\n\ts_barrier" :: "i"(N) : "memory"); }
template <int I> struct ic_t { static constexpr int value = I; };
template <int N, int I = 0, class F> DI void static_for(F&& f) { if constexpr (I < N) { f(ic_t<I>{}); static_for<N, I + 1>(f); } }
#define MFMA32(a, b, c) __builtin_amdgcn_mfma_f32_32x32x16_bf16(__builtin_bit_cast(bf16x8, a), __builtin_bit_cast(bf16x8, b), c, 0, 0, 0)
#define MFMA16(a, b, c) __builtin_amdgcn_mfma_f32_16x16x32_bf16(__builtin_bit_cast(bf16x8, a), __builtin_bit_cast(bf16x8, b), c, 0, 0, 0)

constexpr int S_CH = 9;
constexpr int S_XRS = 4096 + 32;
constexpr int S_XR = 0;
constexpr int S_RC = S_CH * S_XRS;
constexpr int S_RCS = 4192;
constexpr int S_KD = S_RC + 4 * S_RCS;
constexpr int S_K = S_KD + 48 * 128;
constexpr int S_KROW = 96;
constexpr int S_TAB = S_K + 79 * S_KROW;
constexpr int S_TROW = 544;
constexpr int S_RED = S_TAB + 57 * S_TROW;
constexpr int S_TOTAL = S_RED + 4 * 64 * 144;
static_assert(S_RC % 16 == 0 && S_TAB % 16 == 0 && S_RED % 16 == 0, "");
DI f32x2 crot(f32x2 z) { return (f32x2){-z.y, z.x}; }
DI f32x2 cmul(f32x2 a, f32x2 z) { return __builtin_elementwise_fma(a.yy, crot(z), a.xx * z); }
DI f32x2 cexp_p(float ar, float ai, float p) {
    const float x = ai * p;
    const float hi = x * 0.15915494309189535f;
    const float lo = __builtin_fmaf(x, 0.15915494309189535f, -hi) + x * 6.420638316725915e-9f;
    const float rev = (hi - __builtin_rintf(hi)) + lo;
    const float m = __builtin_amdgcn_exp2f(ar * p * 1.4426950408889634f);
    return (f32x2){m * __builtin_amdgcn_cosf(rev), m * __builtin_amdgcn_sinf(rev)};
}
#define FENCE() __builtin_amdgcn_sched_barrier(0)
#define TT(c, i)
DI void role_s4d(const Args& a, LAS char* lds, unsigned lds_base, int task, rsrc_t rw, int& budget) {
    const int tid = threadIdx.x, lane = tid & 63, w = __builtin_amdgcn_readfirstlane(tid >> 6);
    const int d0 = task * S_CH;
    touch(w == 0 ? a.lAr : w == 1 ? a.Aim : w == 2 ? a.Cr : a.Ci, min(d0 * (NS * 2) + min(lane, 2 * S_CH + 1) * 128, DM * NS * 2 - 4), lds_base);
    touch(w & 1 ? a.ldt : a.Dsk, min(lane * 128, DM * 2 - 4), lds_base);
#define S4S(i)
    {
        const rsrc_t rx = __builtin_amdgcn_make_buffer_rsrc((void*)a.x, 0, SEQ * DM * 2, 0x00020000);
        const unsigned sh = (d0 & 1) * 16;
        u32x4 v[8]; unsigned e[8];
#pragma unroll
        for (int i = 0; i < 8; ++i) {
            const int o = (tid + 256 * i) * (DM * 2) + (d0 & ~1) * 2;
            v[i] = __builtin_amdgcn_raw_buffer_load_b128(rx, o, 0, 0); e[i] = __builtin_amdgcn_raw_buffer_load_b32(rx, o, 16, 0);
        }
#pragma unroll
        for (int p = 0; p < 2; ++p) {
            const int tok = 32 * task + 8 * w + 4 * p + (lane >> 4);
            float acc = 0.f;
#pragma unroll
            for (int i = 0; i < 5; ++i) {
                const int ch = (lane & 15) + 16 * i;
                const u32x4 gv = ch < 72 ? *(const u32x4*)(a.gw + ch * 8) : (u32x4){0u, 0u, 0u, 0u};
                const u32x4 xv = *(const u32x4*)(a.x + (size_t)tok * DM + min(ch, 71) * 8);
#pragma unroll
                for (int j = 0; j < 4; ++j) acc += bflo(xv[j]) * bflo(gv[j]) + bfhi(xv[j]) * bfhi(gv[j]);
            }
            acc += __shfl_xor(acc, 1); acc += __shfl_xor(acc, 2); acc += __shfl_xor(acc, 4); acc += __shfl_xor(acc, 8);
            st32(__float_as_uint(1.0f / (1.0f + __expf(-acc))), rw, (lane & 15) == 0 ? OFF_GT + tok * 4 : OOB, 0);
        }
#pragma unroll
        for (int i = 0; i < 8; ++i) {
            const unsigned x5[5] = {__builtin_amdgcn_alignbit(v[i].y, v[i].x, sh), __builtin_amdgcn_alignbit(v[i].z, v[i].y, sh), __builtin_amdgcn_alignbit(v[i].w, v[i].z, sh),
                                    __builtin_amdgcn_alignbit(e[i], v[i].w, sh), e[i] >> sh};
#pragma unroll
            for (int c = 0; c < S_CH; ++c) *(LAS u16*)(lds + S_XR + c * S_XRS + (2047 - tid - 256 * i) * 2) = (u16)(x5[c >> 1] >> (16 * (c & 1)));
        }
    }
    if (tid < 8 * S_CH) *(LAS unsigned*)(lds + S_XR + (tid >> 3) * S_XRS + 4096 + (tid & 7) * 4) = 0;
    if (tid < 64) *(LAS unsigned*)(lds + S_RC + (tid >> 4) * S_RCS + 4096 + (tid & 15) * 4) = 0;
    for (int i = tid; i < 15 * S_KROW / 4; i += 256) *(LAS unsigned*)(lds + S_K + i * 4) = 0;
    int kd[8];
#pragma unroll
    for (int j = 0; j < 8; ++j) {
        const int p = (int)bf2f(f2bf((float)(8 * tid + j))), k = p >> 7, r = p & 7;
        const int row = p < 256 ? 8 * k + r : (p < 512 ? 8 + 4 * k + (r >> 1) : (p < 1024 ? 16 + 2 * k + (r >> 2) : 24 + k));
        kd[j] = S_KD + (row * 16 + ((p >> 3) & 15)) * 8;
    }
    const int n = tid & 127, nn = lane & 15, g = lane >> 4;
    u16 pA = a.lAr[d0 * NS + n], pI = a.Aim[d0 * NS + n], pCr = a.Cr[d0 * NS + n], pCi = a.Ci[d0 * NS + n], pT = a.ldt[d0], pD = a.Dsk[d0];
    WG_BARRIER();
    for (int ch = 0; ch < S_CH; ++ch) {
        S4S(0)
        const float dsk = bf2f(pD);
        {
            const u32x4 v = *(LAS u32x4*)(lds + S_XR + ch * S_XRS + tid * 16);
            const u32x2 e = *(LAS u32x2*)(lds + S_XR + ch * S_XRS + tid * 16 + 16);
            const unsigned s01 = __builtin_amdgcn_alignbit(v.y, v.x, 16), s12 = __builtin_amdgcn_alignbit(v.z, v.y, 16), s23 = __builtin_amdgcn_alignbit(v.w, v.z, 16);
            const unsigned s34 = __builtin_amdgcn_alignbit(e.x, v.w, 16), s45 = __builtin_amdgcn_alignbit(e.y, e.x, 16);
            *(LAS u32x4*)(lds + S_RC + tid * 16) = v;
            *(LAS u32x4*)(lds + S_RC + S_RCS + tid * 16) = (u32x4){s01, s12, s23, s34};
            *(LAS u32x4*)(lds + S_RC + 2 * S_RCS + tid * 16) = (u32x4){v.y, v.z, v.w, e.x};
            *(LAS u32x4*)(lds + S_RC + 3 * S_RCS + tid * 16) = (u32x4){s12, s23, s34, s45};
        }
        S4S(1)
        const float dt = bf2f(f2bf(expf(bf2f(pT))));
        const int sc = 140 - (wave_max32(max(pCr & 0x7FFF, pCi & 0x7FFF)) >> 7);
        {
            const float ar = -bf2f(f2bf(expf(bf2f(pA)))) * dt, ai = bf2f(pI) * dt;
            const f32x2 C = {__builtin_amdgcn_ldexpf(bf2f(pCr), sc), __builtin_amdgcn_ldexpf(bf2f(pCi), sc)};
            const int dn = min(d0 + ch + 1, DM - 1);
            pA = a.lAr[dn * NS + n]; pI = a.Aim[dn * NS + n]; pCr = a.Cr[dn * NS + n]; pCi = a.Ci[dn * NS + n]; pT = a.ldt[dn]; pD = a.Dsk[dn];
            const int to = S_TAB + n * 4;
            auto put = [&](int row, f32x2 v) { *(LAS f16x2*)(lds + to + row * S_TROW) = __builtin_convertvector(v, f16x2); };
            const f32x2 z128 = cexp_p(ar, ai, 128.f);
            if (w < 2) {
                f32x2 E[8], G[4];
                E[1] = cexp_p(ar, ai, 1.f); G[0] = C;
#pragma unroll
                for (int r = 2; r < 8; ++r) E[r] = cmul(E[r >> 1], E[r - (r >> 1)]);
#pragma unroll
                for (int k = 1; k < 4; ++k) G[k] = cmul(G[k - 1], z128);
#pragma unroll
                for (int k = 0; k < 4; ++k)
#pragma unroll
                    for (int r = 0; r < 8; r += 1 + (k >> 1)) put(k < 2 ? 8 * k + r : 8 + 4 * k + (r >> 1), r ? cmul(E[r], G[k]) : G[k]);
            } else {
                const f32x2 z4 = cexp_p(ar, ai, 4.f), z8 = cexp_p(ar, -ai, 8.f);
                f32x2 G = cmul(C, cexp_p(ar, ai, 512.f)), B = z8;
                put(41, (f32x2){1.f, 0.f});
#pragma unroll
                for (int k = 1; k < 16; ++k) {
                    put(41 + k, B); B = cmul(B, z8);
                    if (k < 5) { put(22 + 2 * k, G); put(23 + 2 * k, cmul(G, z4)); }
                    else if (k < 14) put(27 + k, G);
                    G = cmul(G, z128);
                }
            }
        }
        WG_BARRIER();
        S4S(2)
        {
            const int ko = (w & 1) * 256 + g * 16, t0 = w & 2;
            u32x4 av[2][4], bv[4];
#pragma unroll
            for (int j = 0; j < 4; ++j) {
                bv[j] = *(LAS u32x4*)(lds + S_TAB + (41 + nn) * S_TROW + ko + j * 64);
                av[0][j] = *(LAS u32x4*)(lds + S_TAB + min(16 * t0 + nn, 40) * S_TROW + ko + j * 64);
                av[1][j] = *(LAS u32x4*)(lds + S_TAB + (16 + nn) * S_TROW + ko + j * 64);
            }
            f32x4 acc[2] = {};
#pragma unroll
            for (int j = 0; j < 8; ++j) acc[j & 1] = __builtin_amdgcn_mfma_f32_16x16x32_f16(__builtin_bit_cast(f16x8, av[j & 1][j >> 1]), __builtin_bit_cast(f16x8, bv[j >> 1]), acc[j & 1], 0, 0, 0);
#pragma unroll
            for (int r = 0; r < 8; ++r) *(LAS float*)(lds + S_KD + (((r < 4 ? 16 * t0 : 16) + 4 * g + (r & 3)) * 16 + nn) * 8 + (w & 1) * 4) = __builtin_amdgcn_ldexpf(acc[r >> 2][r & 3] * dt, 1 - sc);
        }
        WG_BARRIER();
        S4S(3)
        {
            float kv[8];
#pragma unroll
            for (int j = 0; j < 8; ++j) { const f32x2 t = *(LAS f32x2*)(lds + kd[j]); kv[j] = t.x + t.y; }
            *(LAS u32x4*)(lds + S_K + (15 + (tid >> 2)) * S_KROW + (tid & 3) * 16) = (u32x4){pack2(kv[0], kv[1]), pack2(kv[2], kv[3]), pack2(kv[4], kv[5]), pack2(kv[6], kv[7])};
        }
        WG_BARRIER();
        S4S(4)
        {
            const int st0 = 2047 - nn + 8 * g - 32 * w, cc = st0 & 3;
            const int ub = S_RC + cc * S_RCS + (st0 - cc) * 2 - 3872, kb = S_K + (3 + nn - w) * S_KROW + g * 16;
            f32x4 y[4][2] = {};
            u32x2 U[2][16]; u32x4 F[2][4];
            auto rd = [&](int i, int f) {
                if (f < 4) F[i & 1][f] = *(LAS u32x4*)(lds + kb + (12 - 4 * i + 16 * f) * S_KROW);
                else U[i & 1][f - 4] = *(LAS u32x2*)(lds + ub + 3872 - 256 * i - 1024 * ((f - 4) >> 2) - 32 * ((f >> 1) & 1) + 8 * (f & 1));
            };
#pragma unroll
            for (int f = 0; f < 20; ++f) { rd(0, f); FENCE(); }
#pragma unroll
            for (int i = 0; i < 4; ++i) {
                int c = 0;
#pragma unroll
                for (int q = 0; q < 4; ++q)
#pragma unroll
                    for (int T = q; T < 4; ++T)
#pragma unroll
                        for (int bt = 0; bt < 2; ++bt) {
                            y[T][bt] = MFMA16(__builtin_shufflevector(U[i & 1][4 * q + 2 * bt], U[i & 1][4 * q + 2 * bt + 1], 0, 1, 2, 3), F[i & 1][T - q], y[T][bt]);
                            if (i < 3) rd(i + 1, c);
                            ++c;
                            FENCE();
                        }
            }
#pragma unroll
            for (int T = 0; T < 4; ++T)
#pragma unroll
                for (int bt = 0; bt < 2; ++bt) *(LAS f32x4*)(lds + S_RED + (w * 64 + 16 * T + nn) * 144 + bt * 64 + g * 16) = y[T][bt];
        }
        WG_BARRIER();
        S4S(5)
        {
            f32x4 y0 = {0, 0, 0, 0}, y1 = y0;
#pragma unroll
            for (int ww = 0; ww < 4; ++ww) {
                const int o = S_RED + (ww * 64 + (tid >> 2)) * 144 + (tid & 3) * 32;
                y0 += *(LAS f32x4*)(lds + o); y1 += *(LAS f32x4*)(lds + o + 16);
            }
            const u32x4 u = *(LAS u32x4*)(lds + S_XR + ch * S_XRS + (255 - tid) * 16);
            st128((u32x4){pack2(y0[0] + bfhi(u.w) * dsk, y0[1] + bflo(u.w) * dsk), pack2(y0[2] + bfhi(u.z) * dsk, y0[3] + bflo(u.z) * dsk),
                          pack2(y1[0] + bfhi(u.y) * dsk, y1[1] + bflo(u.y) * dsk), pack2(y1[2] + bfhi(u.x) * dsk, y1[3] + bflo(u.x) * dsk)}, rw, tid * 16, OFF_BO + (d0 + ch) * BO_ROW);
        }
        S4S(6)
    }
    wait_init(rw, launch_tag(a), budget);
    VM_DRAIN();
    WG_BARRIER();
    if (tid == 0) st32(1u, rw, F_S4 + task * 4, 0);
}

constexpr int XT_BYTES = 32 * 1152;
constexpr int P_XT = 0;
constexpr int P_STG = 2 * XT_BYTES;
struct WSlice { u32x4 f[36]; };
constexpr int P_RING = 5;
template <class F> DI void load_wslices(WSlice& W0, WSlice& W1, const u16* s0, const u16* s1, int w, int lane, LAS char* lds, unsigned lds_base, F also) {
    constexpr int NB = 18;
    const int r0 = 8 * (lane >> 5) + ((lane & 15) >> 2);
    const int cb = (4 * w + 2 * ((lane >> 4) & 1) + ((lane & 3) >> 1)), lo8 = (lane & 1) * 8;
    auto src = [&](int i) { return (i < 9 ? s0 : s1) + (size_t)(64 * (i % 9)) * HD; };
    const u32x4 ro = ring_offs(w, lane, HD);
#pragma unroll
    for (int i = 0; i < P_RING - 1; ++i) ring_issue(src(i), ro, lds_base + P_STG + i * 16384 + w * 4096);
    also();
    static_for<NB>([&](auto ic) {
        constexpr int i = decltype(ic)::value, rem = NB - 1 - i < P_RING - 2 ? NB - 1 - i : P_RING - 2;
        ring_wait<4 * rem>();
        if constexpr (i + P_RING - 1 < NB) ring_issue(src(i + P_RING - 1), ro, lds_base + P_STG + ((i + P_RING - 1) % P_RING) * 16384 + w * 4096);
        const int sl = P_STG + (i % P_RING) * 16384;
#pragma unroll
        for (int kk = 0; kk < 4; ++kk) {
            const int ra = r0 + 16 * kk, rb = ra + 4;
            const u32x2 lo = tr_read(lds, sl + ra * 256 + ((cb ^ (2 * swz(ra))) * 16) + lo8), hi = tr_read(lds, sl + rb * 256 + ((cb ^ (2 * swz(rb))) * 16) + lo8);
            (i < 9 ? W0 : W1).f[(i % 9) * 4 + kk] = (u32x4){lo.x, lo.y, hi.x, hi.y};
        }
        u32x4* f = (i < 9 ? W0 : W1).f + (i % 9) * 4;
        if constexpr (i < 16) asm volatile("" : "+a"(f[0]), "+a"(f[1]), "+a"(f[2]), "+a"(f[3]));
    });
}
DI u32x4 piece_pi(const f32x16& a, int qq) {
    return (u32x4){pack2(a[4 * qq], a[4 * qq + 8]), pack2(a[4 * qq + 1], a[4 * qq + 9]), pack2(a[4 * qq + 2], a[4 * qq + 10]), pack2(a[4 * qq + 3], a[4 * qq + 11])};
}
DI u32x4 piece_seq(const f32x16& a, int s2) {
    return (u32x4){pack2(a[8 * s2], a[8 * s2 + 1]), pack2(a[8 * s2 + 2], a[8 * s2 + 3]), pack2(a[8 * s2 + 4], a[8 * s2 + 5]), pack2(a[8 * s2 + 6], a[8 * s2 + 7])};
}
struct XTile {
    int xs[9], xa[4];
    DI void init(int w, int lane) {
#pragma unroll
        for (int i = 0; i < 9; ++i) { const int L = (i * 4 + w) * 64 + lane, r = L / 72, cs = L - 72 * r; xs[i] = r * 1152 + (cs ^ ((r >> 1) & 7)) * 16; }
        const int c32 = lane & 31, hf = lane >> 5;
#pragma unroll
        for (int j = 0; j < 4; ++j) xa[j] = P_XT + c32 * 1152 + (((2 * j + hf) ^ ((c32 >> 1) & 7)) * 16);
    }
    DI void dma(const Args& a, unsigned lds_base, int w, int s, int buf, int i) const {
        glds16((const char*)a.x + (size_t)min(s, NSEG - 1) * XT_BYTES, xs[i], lds_base + P_XT + buf * XT_BYTES + (i * 4 + w) * 1024);
    }
    DI void first(const Args& a, unsigned lds_base, int w, int lane, int s) {
        init(w, lane);
#pragma unroll
        for (int i = 0; i < 9; ++i) dma(a, lds_base, w, s, 0, i);
    }
};
template <class F> DI void project(f32x16& acc0, f32x16& acc1, const WSlice& W0, const WSlice& W1, const XTile& X, const Args& a, LAS char* lds, unsigned lds_base, int w, int buf, int sn, F gap) {
    const int bo = buf * XT_BYTES;
    constexpr int PD = 5;
    u32x4 xf[PD];
#pragma unroll
    for (int i = 0; i < PD; ++i) xf[i] = *(LAS u32x4*)(lds + X.xa[i & 3] + bo + (i >> 2) * 128);
    static_for<36>([&](auto ic) {
        constexpr int ks = decltype(ic)::value;
        if (ks == 0) asm volatile("v_mfma_f32_32x32x16_bf16 %0, %1, %2, 0" : "=&v"(acc0) : "a"(W0.f[0]), "v"(xf[0]) : "memory");
        else asm volatile("v_mfma_f32_32x32x16_bf16 %0, %1, %2, %0" : "+v"(acc0) : "a"(W0.f[ks]), "v"(xf[ks % PD]) : "memory");
        if (ks < 9) X.dma(a, lds_base, w, sn, buf ^ 1, ks);
        gap(ic_t<2 * ks>{});
        if (ks == 0) asm volatile("v_mfma_f32_32x32x16_bf16 %0, %1, %2, 0" : "=&v"(acc1) : "a"(W1.f[0]), "v"(xf[0]) : "memory");
        else if (ks < 28) asm volatile("v_mfma_f32_32x32x16_bf16 %0, %1, %2, %0" : "+v"(acc1) : "a"(W1.f[ks]), "v"(xf[ks % PD]) : "memory");
        else asm volatile("v_mfma_f32_32x32x16_bf16 %0, %1, %2, %0" : "+v"(acc1) : "v"(W1.f[ks]), "v"(xf[ks % PD]) : "memory");
        if (ks + PD < 36) xf[ks % PD] = *(LAS u32x4*)(lds + X.xa[(ks + PD) & 3] + bo + ((ks + PD) >> 2) * 128);
        gap(ic_t<2 * ks + 1>{});
    });
    asm volatile("s_nop 7\n\ts_nop 3" : "+v"(acc0), "+v"(acc1) :: "memory");
}
#define KV_T 7
#define KV_F 22
DI void role_kv(const Args& a, LAS char* lds, unsigned lds_base, int h, rsrc_t rw, int& budget) {
    const int tid = threadIdx.x, lane = tid & 63, w = __builtin_amdgcn_readfirstlane(tid >> 6);
    WSlice W0, W1;
    XTile X;
    u32x2 tag;
    load_wslices(W0, W1, a.Wk + h * DH, a.Wv + h * DH, w, lane, lds, lds_base, [&]() { X.first(a, lds_base, w, lane, 0); tag = launch_tag(a); });
    VM_DRAIN();
    WG_BARRIER();
    const int oo = h * 8192 + w * 2048 + lane * 16;
    const int fo = lane == 0 ? F_KV + h * 512 + w * 128 : OOB;
    u32x4 xf[6], P[4] = {};
    auto frag = [&](int buf, int ks) { xf[ks % 6] = *(LAS u32x4*)(lds + X.xa[ks & 3] + buf * XT_BYTES + (ks >> 2) * 128); };
    auto cvt = [&](const f32x16& k, const f32x16& v, int i) {
#pragma unroll
        for (int j = 0; j < 4; ++j) asm volatile("v_cvt_pk_bf16_f32 %0, %1, %2" : "=v"(P[i][j]) : "v"(i < 2 ? k[4 * i + j] : v[8 * i - 16 + 2 * j]), "v"(i < 2 ? k[4 * i + j + 8] : v[8 * i - 15 + 2 * j]));
    };
    auto put = [&](int o, int s, int i) { st128(P[i], rw, o + (i & 1) * 1024, (i < 2 ? OFF_KF : OFF_VF) + s * (NH * 8192)); };
    auto now_ = [&](f32x16& k, f32x16& v, int s) {
        asm volatile("s_nop 7\n\ts_nop 3" : "+v"(k), "+v"(v) :: "memory");
#pragma unroll
        for (int i = 0; i < 4; ++i) { cvt(k, v, i); put(oo, s, i); }
    };
    u32x2 in = {};
    auto seg = [&](f32x16& k, f32x16& v, const f32x16& pk, const f32x16& pv, int s, int buf) {
        const int o = s > 1 ? oo : OOB;
#pragma unroll
        for (int ks = 0; ks < 36; ++ks) {
            if (ks == 0) asm volatile("v_mfma_f32_32x32x16_bf16 %0, %1, %2, 0" : "=&v"(k) : "a"(W0.f[0]), "v"(xf[0]) : "memory");
            else asm volatile("v_mfma_f32_32x32x16_bf16 %0, %1, %2, %0" : "+v"(k) : "a"(W0.f[ks]), "v"(xf[ks % 6]) : "memory");
            if (ks >= 1 && ks < 5) cvt(pk, pv, ks - 1);
            if (buf && ks == KV_T && s == 1) {
                asm volatile("s_waitcnt vmcnt(%0)" :: "i"(4 + (KV_T - 4) / 2) : "memory");
                __builtin_amdgcn_raw_buffer_store_b64(tag, rw, lane == 0 ? F_INIT + (33 + 4 * h + w) * 128 : OOB, 0, 16);
                if (!__all(((in.x ^ tag.x) >> 1) == 0 && in.y == tag.y)) wait_init(rw, tag, budget);
            }
            if (ks >= 5 && ks < 13 && (ks & 1)) put(o, s - 1, (ks - 5) >> 1);
            if (ks == KV_F) { VM_DRAIN(); st32((unsigned)(s - 1), rw, s > 1 ? fo : OOB, 0); }
            if (ks == 0) asm volatile("v_mfma_f32_32x32x16_bf16 %0, %2, %1, 0" : "=&v"(v) : "a"(W1.f[0]), "v"(xf[0]) : "memory");
            else if (ks < 28) asm volatile("v_mfma_f32_32x32x16_bf16 %0, %2, %1, %0" : "+v"(v) : "a"(W1.f[ks]), "v"(xf[ks % 6]) : "memory");
            else asm volatile("v_mfma_f32_32x32x16_bf16 %0, %2, %1, %0" : "+v"(v) : "v"(W1.f[ks]), "v"(xf[ks % 6]) : "memory");
            if (ks == 30) WG_BARRIER();
            frag(buf ^ (ks >= 30), (ks + 6) % 36);
            if (ks < 4) X.dma(a, lds_base, w, s + 1, buf ^ 1, ks + 5);
            if (ks > 30) X.dma(a, lds_base, w, s + 2, buf, ks - 31);
        }
    };
#pragma unroll
    for (int i = 0; i < 6; ++i) frag(0, i);
#pragma unroll
    for (int i = 0; i < 5; ++i) X.dma(a, lds_base, w, 1, 1, i);
    f32x16 k0, v0, k1 = {}, v1 = {};
    for (int s = 0; s < NSEG; s += 2) {
        seg(k0, v0, k1, v1, s, 0);
        if (s == 0) { in = ld64(rw, F_INIT + (lane & 31) * 128, 0); now_(k0, v0, 0); }
        seg(k1, v1, k0, v0, s + 1, 1);
    }
    now_(k1, v1, NSEG - 1);
    wait_prog(rw, F_S4 + lane * 4, true, 1, budget);
    VM_DRAIN();
    st32(NSEG, rw, fo, 0);
}

DI int prog_min(rsrc_t rw, int off) {
    const int v = (int)ld32(rw, off, 0);
    asm volatile("" ::: "memory");
    return -wave_max32(-v);
}
DI float red_max_g(float v) {
    auto a = __builtin_amdgcn_permlane16_swap(__float_as_uint(v), __float_as_uint(v), false, false);
    v = fmaxf(__uint_as_float(a[0]), __uint_as_float(a[1]));
    auto b = __builtin_amdgcn_permlane32_swap(__float_as_uint(v), __float_as_uint(v), false, false);
    return fmaxf(__uint_as_float(b[0]), __uint_as_float(b[1]));
}
#define M16 "v_mfma_f32_16x16x32_bf16 "
DI void role_qa(const Args& a, LAS char* lds, unsigned lds_base, int h0, int s0, int s1, rsrc_t rw, int& budget) {
    const int tid = threadIdx.x, lane = tid & 63, w = __builtin_amdgcn_readfirstlane(tid >> 6);
    const int n = lane & 15, g = lane >> 4, h = h0 + (w >> 1), tt = w & 1;
    WSlice W0, W1;
    XTile X;
    load_wslices(W0, W1, a.Wq + h0 * DH, a.Wq + (h0 + 1) * DH, w, lane, lds, lds_base, [&]() { X.first(a, lds_base, w, lane, s0); });
    const int oo = OFF_OF + (2 * tt + (n >> 3)) * 65536 + h * 2048 + g * 128 + (n & 7) * 16;
#pragma unroll
    for (int t = 0; t < 4; ++t) st128((u32x4){SENT, SENT, SENT, SENT}, rw, oo + t * 512, s1 * 262144);
    VM_DRAIN();
    WG_BARRIER();
    constexpr int Q_Q = P_STG, Q_KV = Q_Q + 16384, Q_M = Q_KV + 32768;
    static_assert(Q_M + 16384 <= P_STG + P_RING * 16384, "");
    const int fk = F_KV + h * 512 + (lane & 3) * 128, fm = F_KVM + h * 256 + (lane & 1) * 128;
    int pm_, m_rdy = -1, kv_rdy = -1;
    auto look = [&]() { pm_ = (int)ld32(rw, lane & 4 ? fk : fm, 0); };
    auto seen = [&]() { auto rl = [&](int i) { return __builtin_amdgcn_readlane(pm_, i); }; m_rdy = min(rl(0), rl(1)); kv_rdy = min(min(rl(4), rl(5)), min(rl(6), rl(7))); };
    auto wait_m = [&](int thr, unsigned& t) {
        for (;;) {
            const u32x2 v = ld64(rw, fm, 0);
            asm volatile("" ::: "memory");
            t = now();
            const int p0 = __builtin_amdgcn_readlane(v.x, 0), p1 = __builtin_amdgcn_readlane(v.x, 1);
            const int d0 = (int)(__builtin_amdgcn_readlane(v.y, 0) - 8 * t) >> 3, d1 = (int)(__builtin_amdgcn_readlane(v.y, 1) - 8 * t) >> 3;
            if (min(p0, p1) >= thr || --budget < 0) return max(p0 == thr ? d0 : -1000, p1 == thr ? d1 : -1000);
            (void)wait_until(t + min(max(min(d0, d1) + (int)(a.xa >> 16 & 255), 10), 200));
        }
    };
    auto proj = [&](int s, auto gap) {
        f32x16 acc0, acc1;
        project(acc0, acc1, W0, W1, X, a, lds, lds_base, w, ((s - s0) >> 1) & 1, s + 2, [&](auto ic) { gap(ic); if (decltype(ic)::value == 55) look(); });
        if (s >= LATE) {
            const int o = OFF_OF + h0 * 8192 + w * 2048 + lane * 16;
            st128(piece_pi(acc0, 0), rw, o, s * 262144); st128(piece_pi(acc0, 1), rw, o + 1024, s * 262144);
            st128(piece_pi(acc1, 0), rw, o + 8192, s * 262144); st128(piece_pi(acc1, 1), rw, o + 8192 + 1024, s * 262144);
            return;
        }
        const int o = Q_Q + w * 2048 + lane * 16;
        *(LAS u32x4*)(lds + o) = piece_pi(acc0, 0); *(LAS u32x4*)(lds + o + 1024) = piece_pi(acc0, 1);
        *(LAS u32x4*)(lds + o + 8192) = piece_pi(acc1, 0); *(LAS u32x4*)(lds + o + 8192 + 1024) = piece_pi(acc1, 1);
    };
    const int fs = (tt ? OFF_VF : OFF_KF) + (g >> 1) * 1024 + ((g & 1) * 32 + 16 * (n >> 3) + (n & 7)) * 16;
    auto fetch = [&](int s) {
        const int so = (min(s, NSEG - 1) * NH + h) * 8192;
#pragma unroll
        for (int f = 0; f < 8; ++f) bdma(rw, fs, so + (f >> 1) * 2048 + (f & 1) * 128, lds_base + Q_KV + w * 8192 + f * 1024);
    };
    const int ms = OFF_KM + (lane & 7) * 256 + (lane >> 3) * 16;
    auto fetch_m = [&](int s) {
        const int so = (s * NH + h) * 2048;
        const unsigned d = lds_base + Q_M + w * 4096;
        bdma(rw, ms, so, d); bdma(rw, ms, so + 128, d + 1024); bdma(rw, lane * 16, OFF_VM + so, d + 2048); bdma(rw, lane * 16, OFF_VM + so + 1024, d + 3072);
    };
    const float c = 0.08838834764831845f * 1.4426950408889634f;
    u32x4 qf[4];
    f32x4 o[8]; float mx, l;
    auto xpart = [&]() {
        const int kb = Q_KV + (w >> 1) * 16384 + lane * 16;
        u32x4 kn[8], vn[8];
#pragma unroll
        for (int ks = 0; ks < 4; ++ks) qf[ks] = *(LAS u32x4*)(lds + Q_Q + (w >> 1) * 8192 + (2 * ks + (g >> 1)) * 1024 + ((g & 1) * 32 + 16 * tt + n) * 16);
#pragma unroll
        for (int f = 0; f < 8; ++f) kn[f] = *(LAS u32x4*)(lds + kb + f * 1024);
        FENCE();
#pragma unroll
        for (int f = 0; f < 8; ++f) vn[f] = *(LAS u32x4*)(lds + kb + 8192 + f * 1024);
        f32x4 s0, s1;
        asm volatile(M16 "%0, %2, %10, 0\n\t" M16 "%1, %3, %10, 0\n\t" M16 "%0, %4, %11, %0\n\t" M16 "%1, %5, %11, %1\n\t"
                     M16 "%0, %6, %12, %0\n\t" M16 "%1, %7, %12, %1\n\t" M16 "%0, %8, %13, %0\n\t" M16 "%1, %9, %13, %1\n\ts_nop 7"
                     : "=&v"(s0), "=&v"(s1) : "v"(kn[0]), "v"(kn[1]), "v"(kn[2]), "v"(kn[3]), "v"(kn[4]), "v"(kn[5]), "v"(kn[6]), "v"(kn[7]), "v"(qf[0]), "v"(qf[1]), "v"(qf[2]), "v"(qf[3]));
        float p[8];
        mx = -3.0e38f; l = 0.f;
#pragma unroll
        for (int r = 0; r < 8; ++r) { const int key = 16 * (g >> 1) + 4 * (g & 1) + (r & 3) + 8 * (r >> 2); p[r] = key <= 16 * tt + n ? (r < 4 ? s0[r & 3] : s1[r & 3]) : -3.0e38f; mx = fmaxf(mx, p[r]); }
        mx = red_max_g(mx);
#pragma unroll
        for (int r = 0; r < 8; ++r) { p[r] = __builtin_amdgcn_exp2f((p[r] - mx) * c); l += p[r]; }
        const u32x4 pf = {pack2(p[0], p[1]), pack2(p[2], p[3]), pack2(p[4], p[5]), pack2(p[6], p[7])};
        asm volatile("s_nop 1\n\t" M16 "%0, %8, %16, 0\n\t" M16 "%1, %9, %16, 0\n\t" M16 "%2, %10, %16, 0\n\t" M16 "%3, %11, %16, 0\n\t"
                     M16 "%4, %12, %16, 0\n\t" M16 "%5, %13, %16, 0\n\t" M16 "%6, %14, %16, 0\n\t" M16 "%7, %15, %16, 0\n\ts_nop 7"
                     : "=&v"(o[0]), "=&v"(o[1]), "=&v"(o[2]), "=&v"(o[3]), "=&v"(o[4]), "=&v"(o[5]), "=&v"(o[6]), "=&v"(o[7])
                     : "v"(vn[0]), "v"(vn[1]), "v"(vn[2]), "v"(vn[3]), "v"(vn[4]), "v"(vn[5]), "v"(vn[6]), "v"(vn[7]), "v"(pf));
    };
    const int fo = lane == 0 ? F_O + h * 128 + (2 * s0 + tt) * 4 : OOB;
    bool pre = false;
    auto both = [&](int s) {
        const int kb = Q_KV + (w >> 1) * 16384 + lane * 16;
        u32x4 kn[8], vn[8], pf;
        f32x4 sc[2];
        float p[8];
        int v0, v1, v2;
        asm volatile("buffer_load_dword %0, %1, %2, 0 offen sc1" : "=v"(pm_) : "v"(lane & 4 ? fk : fm), "s"(rw) : "memory");
#define PIN(...) asm volatile("" : __VA_ARGS__)
        proj(s + 4, [&](auto ic) {
            constexpr int i = decltype(ic)::value;
            if constexpr (i < 8) {
                if (!(i & 1)) qf[i >> 1] = *(LAS u32x4*)(lds + Q_Q + (w >> 1) * 8192 + (i + (g >> 1)) * 1024 + ((g & 1) * 32 + 16 * tt + n) * 16);
                kn[i] = *(LAS u32x4*)(lds + kb + i * 1024);
            }
            if constexpr (i >= 16 && i < 32 && !(i & 1)) vn[(i - 16) >> 1] = *(LAS u32x4*)(lds + kb + 8192 + (i - 16) * 512);
            if constexpr (i == 5 || i == 6) asm volatile(M16 "%0, %1, %2, 0" : "=&v"(sc[i - 5]) : "v"(kn[i - 5]), "v"(qf[0]));
            if constexpr (i > 6 && i < 13) asm volatile(M16 "%0, %1, %2, %0" : "+v"(sc[(i - 5) & 1]) : "v"(kn[i - 5]), "v"(qf[(i - 5) >> 1]));
            if constexpr (i == 15 || i == 16) {
                constexpr int r = 4 * (i - 15);
                PIN("+v"(sc[i - 15]));
#pragma unroll
                for (int j = 0; j < 4; ++j) p[r + j] = 16 * (g >> 1) + 4 * (g & 1) + j + 2 * r <= 16 * tt + n ? sc[i - 15][j] : -3.0e38f;
                PIN("+v"(p[r]), "+v"(p[r + 1]), "+v"(p[r + 2]), "+v"(p[r + 3]));
            }
            if constexpr (i == 17) { mx = fmaxf(fmaxf(fmaxf(fmaxf(p[0], p[1]), p[2]), fmaxf(fmaxf(p[3], p[4]), p[5])), fmaxf(p[6], p[7])); PIN("+v"(mx)); }
            if constexpr (i == 18) { auto x = __builtin_amdgcn_permlane16_swap(__float_as_uint(mx), __float_as_uint(mx), false, false); mx = fmaxf(__uint_as_float(x[0]), __uint_as_float(x[1])); PIN("+v"(mx)); }
            if constexpr (i == 19) { auto x = __builtin_amdgcn_permlane32_swap(__float_as_uint(mx), __float_as_uint(mx), false, false); mx = fmaxf(__uint_as_float(x[0]), __uint_as_float(x[1])); PIN("+v"(mx)); }
            if constexpr (i >= 20 && i < 24) {
                constexpr int r = 2 * (i - 20);
                PIN("+v"(p[r]), "+v"(p[r + 1]));
                p[r] = __builtin_amdgcn_exp2f((p[r] - mx) * c); p[r + 1] = __builtin_amdgcn_exp2f((p[r + 1] - mx) * c);
                PIN("+v"(p[r]), "+v"(p[r + 1]));
            }
            if constexpr (i == 24) { pf = (u32x4){pack2(p[0], p[1]), pack2(p[2], p[3]), pack2(p[4], p[5]), pack2(p[6], p[7])}; PIN("+v"(pf)); }
            if constexpr (i == 25) { l = ((p[0] + p[1]) + p[2]) + p[3]; PIN("+v"(l)); }
            if constexpr (i == 26) { l = (((l + p[4]) + p[5]) + p[6]) + p[7]; PIN("+v"(l)); }
            if constexpr (i == 27) { asm volatile("s_waitcnt vmcnt(9)" : "+v"(pm_) :: "memory"); st32((unsigned)s, rw, fo, 0); }
            if constexpr (i == 29) { seen(); pre = m_rdy >= s + 2 && kv_rdy >= s + 4; }
            if constexpr (i == 31) { v0 = pre ? ms : OOB; v1 = pre ? lane * 16 : OOB; v2 = pre ? fs : OOB; PIN("+v"(v0), "+v"(v1), "+v"(v2)); }
            if constexpr (i >= 28 && i < 36) asm volatile(M16 "%0, %1, %2, 0" : "=&v"(o[i - 28]) : "v"(vn[i - 28]), "v"(pf));
            if constexpr (i == 36) asm volatile("s_barrier" ::: "memory");
            if constexpr (i >= 38 && i < 42) bdma(rw, i < 40 ? v0 : v1, ((s + 2) * NH + h) * 2048 + (i < 40 ? (i & 1) * 128 : OFF_VM + (i & 1) * 1024), lds_base + Q_M + w * 4096 + (i - 38) * 1024);
            if constexpr (i >= 42 && i < 50) bdma(rw, v2, ((s + 4) * NH + h) * 8192 + ((i - 42) >> 1) * 2048 + (i & 1) * 128, lds_base + Q_KV + w * 8192 + (i - 42) * 1024);
        });
    };
    auto none = [](auto) {};
    proj(s0, none);
    if (s0 < FAST) wait_init(rw, launch_tag(a), budget);
    wait_prog(rw, fk, true, s0, budget);
    fetch(s0);
    VM_DRAIN();
    WG_BARRIER();
    xpart();
    WG_BARRIER();
    proj(s0 + 2, none);
    for (int s = s0; s <= s1; s += 2) {
        S4S(0)
        const int mb = Q_M + w * 4096;
        u32x4 kf[4]; u32x2 vm[4];
        auto frags = [&]() {
#pragma unroll
            for (int ks = 0; ks < 4; ++ks) kf[ks] = *(LAS u32x4*)(lds + mb + (4 * ks + g) * 128 + (2 * (n >> 2) + (n & 1)) * 16);
#pragma unroll
            for (int t = 0; t < 4; ++t) { const int vo = mb + 2048 + t * 512 + (n >> 3) * 256 + g * 64 + (n & 7) * 4; vm[t] = (u32x2){*(LAS unsigned*)(lds + vo), *(LAS unsigned*)(lds + vo + 32)}; }
        };
        if (s == s1) {
            TT(true, 3)
            unsigned t;
            const int d = min(wait_m(s - 1, t) + (int)(a.xa & 255), 500);
            st32((t + max(d, 0) + (a.xb >> 8 * ((s - FAST) >> 1) & 255)) & 0x7FFFFFFFu, rw, lane == 0 ? F_H + ((2 * (s - FAST) + tt) * NH + h) * 128 : OOB, 0);
            (void)wait_until(t + d);
            TT(true, 0)
            for (;;) {
                fetch_m(s);
                VM_DRAIN();
                frags();
                unsigned c = 0;
#pragma unroll
                for (int i = 0; i < 4; ++i) c = max(max(mx4(c, kf[i]), vm[i].x), vm[i].y);
                if (!__any(c == SENT) || --budget < 0) break;
            }
        } else {
            if (!pre) {
                seen();
                unsigned t;
                if (m_rdy < s) { const int d = wait_m(s, t); (void)wait_until(t + min(d + (int)(a.xa >> 24), 300)); }
                fetch_m(s);
                if (kv_rdy < s + 2) wait_prog(rw, fk, true, s + 2, budget);
                fetch(s + 2);
            }
            S4S(1)
            asm volatile("s_waitcnt vmcnt(8)" ::: "memory");
            frags();
        }
        TT(s == s1, 1)
        S4S(2)
        f32x4 sm;
        asm volatile(M16 "%0, %1, %5, 0\n\t" M16 "%0, %2, %6, %0\n\t" M16 "%0, %3, %7, %0\n\t" M16 "%0, %4, %8, %0\n\ts_nop 7"
                     : "=&v"(sm) : "v"(kf[0]), "v"(kf[1]), "v"(kf[2]), "v"(kf[3]), "v"(qf[0]), "v"(qf[1]), "v"(qf[2]), "v"(qf[3]));
        const float m2 = red_max_g(fmaxf(mx, fmaxf(sm[0], sm[1])));
        const float al = __builtin_amdgcn_exp2f((mx - m2) * c), p0 = __builtin_amdgcn_exp2f((sm[0] - m2) * c), p1 = __builtin_amdgcn_exp2f((sm[1] - m2) * c);
        const float inv = __builtin_amdgcn_rcpf(red_sum_g(l * al + p0 + p1)), ai = al * inv;
#pragma unroll
        for (int t = 0; t < 8; ++t) o[t] *= ai;
        const unsigned pm = pack2(p0 * inv, p1 * inv);
#define M1K "v_mfma_f32_16x16x16_bf16 "
        asm volatile("s_nop 1\n\t" M1K "%0, %8, %12, %0\n\t" M1K "%1, %8, %13, %1\n\t" M1K "%2, %9, %12, %2\n\t" M1K "%3, %9, %13, %3\n\t"
                     M1K "%4, %10, %12, %4\n\t" M1K "%5, %10, %13, %5\n\t" M1K "%6, %11, %12, %6\n\t" M1K "%7, %11, %13, %7\n\ts_nop 7"
                     : "+v"(o[0]), "+v"(o[1]), "+v"(o[2]), "+v"(o[3]), "+v"(o[4]), "+v"(o[5]), "+v"(o[6]), "+v"(o[7])
                     : "v"(vm[0]), "v"(vm[1]), "v"(vm[2]), "v"(vm[3]), "v"((u32x2){pm, 0u}), "v"((u32x2){0u, pm}));
#pragma unroll
        for (int b = 0; b < 2; ++b) {
            u32x4 x, y;
#pragma unroll
            for (int j = 0; j < 4; ++j) { x[j] = pack2(o[4 * b + (j >> 1)][2 * (j & 1)], o[4 * b + (j >> 1)][2 * (j & 1) + 1]); y[j] = pack2(o[4 * b + 2 + (j >> 1)][2 * (j & 1)], o[4 * b + 2 + (j >> 1)][2 * (j & 1) + 1]); }
            if (s == LATE - 1) { const u32x4 z = {x.x, x.z, y.x, y.z}; y = (u32x4){x.y, x.w, y.y, y.w}; x = z; }
            st128(x, rw, oo + b * 1024, s * 262144); st128(y, rw, oo + b * 1024 + 512, s * 262144);
        }
        TT(s == s1, 2)
        S4S(3)
        if (s == s1) break;
        asm volatile("s_waitcnt vmcnt(4)" ::: "memory");
        WG_BARRIER();
        S4S(4)
        if (s + 4 <= s1) { both(s); S4S(5) continue; }
        xpart();
        FENCE();
        VM_DRAIN();
        st32((unsigned)s, rw, fo, 0);
        TT(true, 4)
    }
}

#define STEP_ASM \
    "v_readfirstlane_b32 s36, v0\n" \
    "v_readfirstlane_b32 s37, v1\n" \
    "v_readfirstlane_b32 s38, v2\n" \
    "v_readfirstlane_b32 s39, v3\n" \
    "v_readfirstlane_b32 s40, v4\n" \
    "v_readfirstlane_b32 s41, v5\n" \
    "v_readfirstlane_b32 s42, v6\n" \
    "v_readfirstlane_b32 s43, v7\n" \
    "v_readfirstlane_b32 s28, v8\n" \
    "v_readfirstlane_b32 s29, v9\n" \
    "v_readfirstlane_b32 s30, v10\n" \
    "v_readfirstlane_b32 s31, v11\n" \
    "v_readfirstlane_b32 s32, v12\n" \
    "v_readfirstlane_b32 s33, v13\n" \
    "v_readfirstlane_b32 s34, v14\n" \
    "v_readfirstlane_b32 s35, v15\n" \
    "v_readfirstlane_b32 s24, v16\n" \
    "v_readfirstlane_b32 s25, v17\n" \
    "v_readfirstlane_b32 s26, v18\n" \
    "v_readfirstlane_b32 s27, v19\n" \
    "v_readfirstlane_b32 s60, v24\n" \
    "v_readfirstlane_b32 s61, v25\n" \
    "v_readfirstlane_b32 s62, v26\n" \
    "v_readfirstlane_b32 s63, v27\n" \
    "v_readfirstlane_b32 s64, v28\n" \
    "v_readfirstlane_b32 s65, v29\n" \
    "v_readfirstlane_b32 s66, v30\n" \
    "v_readfirstlane_b32 s67, v31\n" \
    "s_mov_b32 s68, m0\n" \
    "s_getpc_b64 s[96:97]\n" \
    "14:\n" \
    "s_add_u32 s96, s96, 2f-14b\n" \
    "s_addc_u32 s97, s97, 0\n" \
    "s_lshl_b32 s74, s60, 10\n" \
    "s_lshl_b32 s95, s74, 1\n" \
    "s_add_i32 s95, s95, 16384\n" \
    "s_mov_b32 s81, 0x3e0293ee\n" \
    "s_mov_b32 s82, 0x53800000\n" \
    "s_lshl_b32 s83, s60, 6\n" \
    "s_and_b32 s83, s83, 128\n" \
    "v_add_u32 v109, s83, v131\n" \
    "v_sub_u32 v111, v131, s83\n" \
    "v_xor_b32 v112, 0x2000, v133\n" \
    "v_and_b32 v113, 0x3c0, v127\n" \
    "v_mad_u32_u24 v113, v113, 7, v127\n" \
    "s_bfe_u32 s83, s61, 0x2000b\n" \
    "s_and_b32 s83, s83, 3\n" \
    "v_lshl_add_u32 v113, s83, 7, v113\n" \
    "v_lshlrev_b32 v114, 1, v127\n" \
    "v_add_u32 v106, s63, v133\n" \
    "s_bfe_u32 s83, s61, 0x40009\n" \
    "s_bfe_u32 s80, s61, 0x20004\n" \
    "s_lshl2_add_u32 s83, s83, s80\n" \
    "s_mov_b64 s[72:73], 0\n" \
    "s_cmp_eq_u32 s43, 4\n" \
    "s_cbranch_scc1 20f\n" \
    "0:\n" \
    "s_and_b32 s75, s36, 3\n" \
    "s_lshl_b32 s75, s75, 16\n" \
    "s_add_i32 s69, s75, s26\n" \
    "s_xor_b32 s70, s69, 2048\n" \
    "s_andn2_b32 s71, s69, 0x1fff\n" \
    "s_add_i32 s71, s71, 32768\n" \
    "ds_read_b128 v[72:75], v133 offset:0\n" \
    "ds_read_b128 v[76:79], v133 offset:1024\n" \
    "ds_read_b128 v[80:83], v133 offset:2048\n" \
    "ds_read_b128 v[84:87], v133 offset:3072\n" \
    "ds_read_b128 v[88:91], v133 offset:16384\n" \
    "ds_read_b128 v[92:95], v133 offset:17408\n" \
    "v_mov_b64 v[40:41], 0\n" \
    "v_mov_b64 v[42:43], 0\n" \
    "v_mov_b64 v[44:45], 0\n" \
    "v_mov_b64 v[46:47], 0\n" \
    "v_mov_b64 v[48:49], 0\n" \
    "v_mov_b64 v[50:51], 0\n" \
    "v_mov_b64 v[52:53], 0\n" \
    "v_mov_b64 v[54:55], 0\n" \
    "v_mov_b64 v[56:57], 0\n" \
    "v_mov_b64 v[58:59], 0\n" \
    "v_mov_b64 v[60:61], 0\n" \
    "v_mov_b64 v[62:63], 0\n" \
    "v_mov_b64 v[64:65], 0\n" \
    "v_mov_b64 v[66:67], 0\n" \
    "v_mov_b64 v[68:69], 0\n" \
    "v_mov_b64 v[70:71], 0\n" \
    "v_add_u32 v108, s74, v127\n" \
    "v_xor_b32 v24, 0x400, v108\n" \
    "v_xor_b32 v28, 0x800, v108\n" \
    "v_xor_b32 v32, 0xc00, v108\n" \
    "v_and_b32 v22, 0x200, v127\n" \
    "s_and_b32 s76, s26, 0x800\n" \
    "v_mad_u32_u24 v22, v22, 3, v127\n" \
    "v_xor_b32 v22, s76, v22\n" \
    "s_movk_i32 m0, 0x2000\n" \
    "s_cmp_eq_u32 s36, 0\n" \
    "s_cbranch_scc1 17f\n" \
    "s_sub_i32 s37, s37, s29\n" \
    "12:\n" \
    "1:\n" \
    "s_memtime vcc\n" \
    "s_mul_i32 s90, s37, s25\n" \
    "s_ashr_i32 s90, s90, 12\n" \
    "s_add_i32 s90, s90, s24\n" \
    "s_add_i32 s90, s90, -116\n" \
    "s_waitcnt lgkmcnt(0)\n" \
    "s_sub_i32 s75, s90, vcc_lo\n" \
    "s_cmp_gt_i32 s75, 112\n" \
    "s_cbranch_scc1 1b\n" \
    "s_max_i32 s75, s75, 0\n" \
    "s_and_b32 s75, s75, -4\n" \
    "s_sub_u32 vcc_lo, s96, s75\n" \
    "s_subb_u32 vcc_hi, s97, 0\n" \
    "s_setpc_b64 vcc\n" \
    "s_nop 0\n" \
    "s_nop 0\n" \
    "s_nop 0\n" \
    "s_nop 0\n" \
    "s_nop 0\n" \
    "s_nop 0\n" \
    "s_nop 0\n" \
    "s_nop 0\n" \
    "s_nop 0\n" \
    "s_nop 0\n" \
    "s_nop 0\n" \
    "s_nop 0\n" \
    "s_nop 0\n" \
    "s_nop 0\n" \
    "s_nop 0\n" \
    "s_nop 0\n" \
    "s_nop 0\n" \
    "s_nop 0\n" \
    "s_nop 0\n" \
    "s_nop 0\n" \
    "s_nop 0\n" \
    "s_nop 0\n" \
    "s_nop 0\n" \
    "s_nop 0\n" \
    "s_nop 0\n" \
    "s_nop 0\n" \
    "s_nop 0\n" \
    "s_nop 0\n" \
    "2:\n" \
    "buffer_load_dwordx4 v[0:3], v125, %[rs], s69 offen sc1\n" \
    "s_nop 3\n" \
    "s_nop 3\n" \
    "buffer_load_dwordx4 v[16:19], v125, %[rs], s70 offen sc1\n" \
    "s_mov_b32 exec_lo, s27\n" \
    "s_mov_b32 exec_hi, s27\n" \
    "buffer_load_dwordx4 v22, %[rs], s71 offen sc1 lds\n" \
    "s_mov_b64 exec, -1\n" \
    "v_bfe_u32 v20, v127, 8, 2\n" \
    "v_subrev_u32 v20, s60, v20\n" \
    "v_mul_i32_i24 v20, -1088, v20\n" \
    "v_add_u32 v20, v20, v129\n" \
    "v_lshrrev_b32 v21, 2, v127\n" \
    "s_mov_b32 exec_lo, s66\n" \
    "s_mov_b32 exec_hi, s66\n" \
    "ds_read2_b32 v[96:97], v20 offset0:0 offset1:144\n" \
    "s_mov_b64 exec, -1\n" \
    "s_lshl_b32 s80, s36, 16\n" \
    "s_add_i32 s80, s80, s67\n" \
    "s_sub_i32 s75, s37, s79\n" \
    "s_cmp_gt_i32 s75, 24\n" \
    "s_cselect_b32 s76, 2, 0\n" \
    "s_cmp_lt_i32 s75, 10\n" \
    "s_cselect_b32 s76, -6, s76\n" \
    "s_add_i32 s28, s28, s76\n" \
    "s_max_i32 s28, s28, 0\n" \
    "s_min_i32 s28, s28, 20\n" \
    "s_cmp_eq_u32 s60, 0\n" \
    "s_cselect_b32 s72, 0x1010101, 0\n" \
    "s_waitcnt lgkmcnt(0)\n" \
    "buffer_store_dword v96, v21, %[rw], s80 offen sc1\n" \
    "buffer_store_dword v97, v21, %[rw], s80 offen offset:256 sc1\n" \
    "3:\n" \
    "s_mov_b32 m0, s95\n" \
    "s_add_i32 s91, s36, 1\n" \
    "s_and_b32 s78, s36, 3\n" \
    "s_lshl_b32 s78, s78, 14\n" \
    "s_add_i32 s77, s78, s61\n" \
    "s_bitcmp1_b32 s36, 0\n" \
    "s_cselect_b32 s79, 0x8000, 0\n" \
    "s_lshl_b32 s92, s91, 18\n" \
    "s_add_i32 s92, s92, s64\n" \
    "s_add_i32 s78, s78, 0x1a0000\n" \
    "s_add_i32 s93, s92, 0x1000000\n" \
    "s_sub_i32 s76, s33, s42\n" \
    "s_ashr_i32 s76, s76, s32\n" \
    "s_add_i32 s42, s42, s76\n" \
    "s_lshr_b32 s76, s32, 8\n" \
    "s_sub_i32 s76, s76, s40\n" \
    "s_ashr_i32 s76, s76, 2\n" \
    "s_add_i32 s40, s40, s76\n" \
    "s_sub_i32 s76, s40, s24\n" \
    "s_min_i32 s94, s91, s38\n" \
    "s_lshl_b32 s94, s94, 18\n" \
    "s_andn2_b32 s80, 0x1000000, s66\n" \
    "s_add_i32 s94, s94, s64\n" \
    "s_add_i32 s94, s94, s80\n" \
    "s_add_i32 s75, s36, 2\n" \
    "s_cmp_lt_i32 s38, s75\n" \
    "s_cselect_b32 s75, s62, 0x1b0040\n" \
    "v_mov_b64 v[98:99], 0\n" \
    "v_mov_b32 v101, 0\n" \
    "s_pack_ll_b32_b16 s80, s91, s42\n" \
    "v_mov_b32 v103, s80\n" \
    "s_waitcnt vmcnt(4)\n" \
    "v_smfmac_f32_16x16x64_bf16 v[40:43], v[0:3], a[0:7], v124\n" \
    "ds_write_b128 v108, v[0:3] offset:0\n" \
    "v_smfmac_f32_16x16x64_bf16 v[44:47], v[0:3], a[8:15], v124\n" \
    "v_smfmac_f32_16x16x64_bf16 v[48:51], v[0:3], a[128:135], v124\n" \
    "v_smfmac_f32_16x16x64_bf16 v[52:55], v[0:3], a[136:143], v124\n" \
    "s_waitcnt vmcnt(3)\n" \
    "v_smfmac_f32_16x16x64_bf16 v[40:43], v[16:19], a[64:71], v124\n" \
    "ds_write_b128 v108, v[16:19] offset:4096\n" \
    "v_smfmac_f32_16x16x64_bf16 v[44:47], v[16:19], a[72:79], v124\n" \
    "s_waitcnt lgkmcnt(1)\n" \
    "s_barrier\n" \
    "v_smfmac_f32_16x16x64_bf16 v[48:51], v[16:19], a[192:199], v124\n" \
    "ds_read_b128 v[4:7], v24 offset:0\n" \
    "v_smfmac_f32_16x16x64_bf16 v[52:55], v[16:19], a[200:207], v124\n" \
    "ds_read_b128 v[8:11], v28 offset:0\n" \
    "v_smfmac_f32_16x16x64_bf16 v[56:59], v[0:3], v[72:79], v124\n" \
    "ds_read_b128 v[12:15], v32 offset:0\n" \
    "v_smfmac_f32_16x16x64_bf16 v[60:63], v[0:3], v[80:87], v124\n" \
    "s_waitcnt vmcnt(2) lgkmcnt(3)\n" \
    "s_barrier\n" \
    "s_memtime s[86:87]\n" \
    "v_smfmac_f32_16x16x64_bf16 v[56:59], v[16:19], v[168:175], v124\n" \
    "ds_read_b128 v[24:27], v24 offset:4096\n" \
    "v_smfmac_f32_16x16x64_bf16 v[60:63], v[16:19], v[176:183], v124\n" \
    "ds_read_b128 v[28:31], v28 offset:4096\n" \
    "s_waitcnt lgkmcnt(3)\n" \
    "v_smfmac_f32_16x16x64_bf16 v[40:43], v[4:7], a[16:23], v124\n" \
    "ds_read_b128 v[32:35], v32 offset:4096\n" \
    "v_smfmac_f32_16x16x64_bf16 v[40:43], v[8:11], a[32:39], v124\n" \
    "ds_read_b128 v[36:39], v126 offset:0\n" \
    "s_waitcnt lgkmcnt(4)\n" \
    "v_smfmac_f32_16x16x64_bf16 v[40:43], v[12:15], a[48:55], v124\n" \
    "buffer_load_dword v110, v114, %[rw], s94 offen sc1\n" \
    "v_smfmac_f32_16x16x64_bf16 v[44:47], v[4:7], a[24:31], v124\n" \
    "s_waitcnt lgkmcnt(2)\n" \
    "v_smfmac_f32_16x16x64_bf16 v[40:43], v[24:27], a[80:87], v124\n" \
    "v_smfmac_f32_16x16x64_bf16 v[40:43], v[28:31], a[96:103], v124\n" \
    "s_waitcnt lgkmcnt(0)\n" \
    "v_smfmac_f32_16x16x64_bf16 v[40:43], v[32:35], a[112:119], v124\n" \
    "ds_read_b128 v[72:75], v133 offset:18432\n" \
    "s_add_i32 s80, s86, s76\n" \
    "v_smfmac_f32_16x16x64_bf16 v[40:43], v[36:39], v[136:143], v124\n" \
    "ds_read_b128 v[76:79], v133 offset:19456\n" \
    "s_mul_hi_u32 s80, s80, s41\n" \
    "v_smfmac_f32_16x16x64_bf16 v[44:47], v[8:11], a[40:47], v124\n" \
    "ds_read_b128 v[80:83], v133 offset:32768\n" \
    "s_add_i32 s80, s80, s29\n" \
    "v_mov_b32 v102, s80\n" \
    "v_smfmac_f32_16x16x64_bf16 v[44:47], v[12:15], a[56:63], v124\n" \
    "ds_read_b128 v[84:87], v133 offset:33792\n" \
    "v_add_f32 v40, v40, v42\n" \
    "v_add_f32 v41, v41, v43\n" \
    "v_smfmac_f32_16x16x64_bf16 v[44:47], v[24:27], a[88:95], v124\n" \
    "ds_read_b128 v[20:23], v127 offset:19456\n" \
    "v_cmp_u_f32 vcc, v40, v41\n" \
    "v_smfmac_f32_16x16x64_bf16 v[44:47], v[28:31], a[104:111], v124\n" \
    "v_mul_f32 v40, s81, v40\n" \
    "v_smfmac_f32_16x16x64_bf16 v[44:47], v[32:35], a[120:127], v124\n" \
    "s_cbranch_vccnz 5f\n" \
    "6:\n" \
    "s_mov_b64 exec, s[72:73]\n" \
    "v_smfmac_f32_16x16x64_bf16 v[44:47], v[36:39], v[144:151], v124\n" \
    "buffer_store_dwordx2 v[102:103], v127, %[rs], s77 offen sc1\n" \
    "v_smfmac_f32_16x16x64_bf16 v[56:59], v[4:7], v[88:95], v124\n" \
    "s_mov_b64 exec, -1\n" \
    "s_waitcnt lgkmcnt(1)\n" \
    "v_mul_f32 v41, s81, v41\n" \
    "v_smfmac_f32_16x16x64_bf16 v[60:63], v[4:7], v[72:79], v124\n" \
    "ds_read_b128 v[88:91], v133 offset:34816\n" \
    "v_add_f32 v44, v44, v46\n" \
    "v_smfmac_f32_16x16x64_bf16 v[56:59], v[8:11], v[80:87], v124\n" \
    "ds_read_b128 v[92:95], v133 offset:35840\n" \
    "v_add_f32 v45, v45, v47\n" \
    "v_smfmac_f32_16x16x64_bf16 v[48:51], v[4:7], a[144:151], v124\n" \
    "ds_read_b128 v[72:75], v133 offset:49152\n" \
    "v_mul_f32 v44, s81, v44\n" \
    "v_smfmac_f32_16x16x64_bf16 v[52:55], v[4:7], a[152:159], v124\n" \
    "ds_read_b128 v[76:79], v133 offset:50176\n" \
    "v_mul_f32 v45, s81, v45\n" \
    "v_smfmac_f32_16x16x64_bf16 v[48:51], v[8:11], a[160:167], v124\n" \
    "ds_read_b128 v[0:3], v127 offset:16384\n" \
    "v_cvt_pk_bf16_f32 v107, v40, v44\n" \
    "v_smfmac_f32_16x16x64_bf16 v[52:55], v[8:11], a[168:175], v124\n" \
    "ds_read_b128 v[4:7], v127 offset:18432\n" \
    "s_waitcnt lgkmcnt(4)\n" \
    "v_cvt_pk_bf16_f32 v108, v41, v45\n" \
    "v_smfmac_f32_16x16x64_bf16 v[60:63], v[8:11], v[88:95], v124\n" \
    "ds_write2_b32 v128, v107, v108 offset0:0 offset1:80\n" \
    "v_smfmac_f32_16x16x64_bf16 v[60:63], v[12:15], v[248:255], v124\n" \
    "ds_read_b128 v[16:19], v127 offset:17408\n" \
    "s_waitcnt lgkmcnt(4)\n" \
    "v_smfmac_f32_16x16x64_bf16 v[56:59], v[12:15], v[72:79], v124\n" \
    "ds_read_b128 v[8:11], v127 offset:20480\n" \
    "v_smfmac_f32_16x16x64_bf16 v[48:51], v[12:15], a[176:183], v124\n" \
    "ds_read_b128 v[40:43], v127 offset:21504\n" \
    "v_smfmac_f32_16x16x64_bf16 v[52:55], v[12:15], a[184:191], v124\n" \
    "ds_read_b128 v[44:47], v127 offset:23552\n" \
    "v_smfmac_f32_16x16x64_bf16 v[48:51], v[24:27], a[208:215], v124\n" \
    "s_waitcnt lgkmcnt(4)\n" \
    "s_barrier\n" \
    "v_smfmac_f32_16x16x64_bf16 v[52:55], v[24:27], a[216:223], v124\n" \
    "ds_read_b128 v[72:75], v130 offset:0\n" \
    "v_smfmac_f32_16x16x64_bf16 v[48:51], v[28:31], a[224:231], v124\n" \
    "ds_read_b128 v[76:79], v130 offset:64\n" \
    "v_smfmac_f32_16x16x64_bf16 v[48:51], v[32:35], a[240:247], v124\n" \
    "ds_read_b128 v[80:83], v130 offset:128\n" \
    "v_smfmac_f32_16x16x64_bf16 v[48:51], v[36:39], v[152:159], v124\n" \
    "ds_read_b128 v[84:87], v130 offset:192\n" \
    "v_smfmac_f32_16x16x64_bf16 v[52:55], v[28:31], a[232:239], v124\n" \
    "ds_read_b128 v[12:15], v127 offset:22528\n" \
    "v_smfmac_f32_16x16x64_bf16 v[52:55], v[32:35], a[248:255], v124\n" \
    "v_add_f32 v48, v48, v50\n" \
    "v_smfmac_f32_16x16x64_bf16 v[52:55], v[36:39], v[160:167], v124\n" \
    "s_waitcnt lgkmcnt(3)\n" \
    "v_add_f32 v49, v49, v51\n" \
    "v_mfma_f32_16x16x32_bf16 v[88:91], v[0:3], v[72:75], 0\n" \
    "ds_read_b64 v[0:1], v132 offset:0\n" \
    "ds_read_b64 v[2:3], v132 offset:1024\n" \
    "v_mfma_f32_16x16x32_bf16 v[88:91], v[4:7], v[76:79], v[88:91]\n" \
    "s_waitcnt lgkmcnt(2)\n" \
    "v_add_f32 v52, v52, v54\n" \
    "v_add_f32 v53, v53, v55\n" \
    "v_mfma_f32_16x16x32_bf16 v[88:91], v[8:11], v[80:83], v[88:91]\n" \
    "v_cvt_pk_bf16_f32 v107, v48, v52\n" \
    "v_cvt_pk_bf16_f32 v108, v49, v53\n" \
    "v_mfma_f32_16x16x32_bf16 v[88:91], v[12:15], v[84:87], v[88:91]\n" \
    "ds_write2_b32 v129, v107, v108 offset0:0 offset1:144\n" \
    "v_mfma_f32_16x16x32_bf16 v[92:95], v[16:19], v[72:75], 0\n" \
    "ds_read_b64 v[4:5], v132 offset:512\n" \
    "ds_read_b64 v[6:7], v132 offset:1536\n" \
    "v_mfma_f32_16x16x32_bf16 v[92:95], v[20:23], v[76:79], v[92:95]\n" \
    "ds_read_b128 v[8:11], v106 offset:0\n" \
    "v_exp_f32 v88, v88\n" \
    "v_mfma_f32_16x16x32_bf16 v[92:95], v[40:43], v[80:83], v[92:95]\n" \
    "ds_read_b128 v[12:15], v106 offset:1024\n" \
    "v_exp_f32 v89, v89\n" \
    "v_mfma_f32_16x16x32_bf16 v[92:95], v[44:47], v[84:87], v[92:95]\n" \
    "s_waitcnt lgkmcnt(4)\n" \
    "s_barrier\n" \
    "v_smfmac_f32_16x16x64_bf16 v[56:59], v[24:27], v[184:191], v124\n" \
    "ds_read_b128 v[48:51], v131 offset:0\n" \
    "ds_read_b128 v[52:55], v131 offset:128\n" \
    "v_cvt_pk_bf16_f32 v88, v88, v89\n" \
    "v_smfmac_f32_16x16x64_bf16 v[56:59], v[28:31], v[200:207], v124\n" \
    "ds_read_b128 v[16:19], v106 offset:32768\n" \
    "v_exp_f32 v90, v90\n" \
    "v_smfmac_f32_16x16x64_bf16 v[56:59], v[32:35], v[216:223], v124\n" \
    "ds_read_b128 v[20:23], v106 offset:33792\n" \
    "v_exp_f32 v91, v91\n" \
    "v_smfmac_f32_16x16x64_bf16 v[56:59], v[36:39], v[232:239], v124\n" \
    "ds_read_b128 v[40:43], v106 offset:2048\n" \
    "s_waitcnt lgkmcnt(3)\n" \
    "v_exp_f32 v92, v92\n" \
    "v_cvt_pk_bf16_f32 v89, v90, v91\n" \
    "v_smfmac_f32_16x16x64_bf16 v[64:67], v[48:51], v[72:79], v124\n" \
    "ds_read_b128 v[44:47], v106 offset:3072\n" \
    "v_exp_f32 v93, v93\n" \
    "v_smfmac_f32_16x16x64_bf16 v[64:67], v[52:55], v[80:87], v124\n" \
    "ds_read_b128 v[72:75], v106 offset:34816\n" \
    "v_exp_f32 v94, v94\n" \
    "v_cvt_pk_bf16_f32 v90, v92, v93\n" \
    "v_smfmac_f32_16x16x64_bf16 v[60:63], v[24:27], v[192:199], v124\n" \
    "ds_read_b128 v[76:79], v106 offset:35840\n" \
    "v_exp_f32 v95, v95\n" \
    "v_smfmac_f32_16x16x64_bf16 v[60:63], v[28:31], v[208:215], v124\n" \
    "ds_read_b128 v[80:83], v106 offset:4096\n" \
    "s_cmp_eq_u32 s36, 62\n" \
    "v_add_f32 v64, v64, v66\n" \
    "v_add_f32 v65, v65, v67\n" \
    "v_smfmac_f32_16x16x64_bf16 v[60:63], v[32:35], v[224:231], v124\n" \
    "ds_read_b128 v[84:87], v106 offset:5120\n" \
    "v_exp_f32 v64, v64\n" \
    "v_cvt_pk_bf16_f32 v91, v94, v95\n" \
    "v_smfmac_f32_16x16x64_bf16 v[60:63], v[36:39], v[240:247], v124\n" \
    "ds_read_b128 v[48:51], v106 offset:36864\n" \
    "v_exp_f32 v65, v65\n" \
    "v_swap_b32 v1, v2\n" \
    "v_swap_b32 v5, v6\n" \
    "v_mfma_f32_16x16x32_bf16 v[102:105], v[88:91], v[120:123], 0\n" \
    "ds_read_b128 v[52:55], v106 offset:37888\n" \
    "v_cvt_pk_bf16_f32 v100, v64, v65\n" \
    "v_add_f32 v56, v56, v58\n" \
    "v_add_f32 v57, v57, v59\n" \
    "v_smfmac_f32_16x16x64_bf16 v[68:71], v[88:91], v[0:7], v124\n" \
    "ds_read_b128 v[24:27], v106 offset:6144\n" \
    "v_add_f32 v60, v60, v62\n" \
    "v_add_f32 v61, v61, v63\n" \
    "v_mfma_f32_16x16x16_bf16 v[102:105], v[100:101], v[120:121], v[102:105]\n" \
    "ds_read_b128 v[28:31], v106 offset:7168\n" \
    "v_cvt_pk_bf16_f32 v96, v56, v57\n" \
    "v_cvt_pk_bf16_f32 v97, v60, v61\n" \
    "s_nop 1\n" \
    "v_smfmac_f32_16x16x32_bf16 v[68:71], v[100:101], v[96:99], v124\n" \
    "ds_read_b128 v[32:35], v106 offset:38912\n" \
    "v_mov_b64 v[56:57], 0\n" \
    "v_mov_b64 v[58:59], 0\n" \
    "v_rcp_f32 v104, v102\n" \
    "v_rcp_f32 v105, v103\n" \
    "v_mul_f32 v68, v68, v104\n" \
    "v_mul_f32 v70, v70, v104\n" \
    "v_mul_f32 v69, v69, v105\n" \
    "v_mul_f32 v71, v71, v105\n" \
    "v_cvt_pk_bf16_f32 v107, v68, v70\n" \
    "v_cvt_pk_bf16_f32 v108, v69, v71\n" \
    "ds_write2_b32 v129, v107, v108 offset0:64 offset1:208\n" \
    "v_max3_f32 v102, v102, v103, v104\n" \
    "v_max_f32 v102, v102, v105\n" \
    "v_cmp_nge_f32 vcc, s82, v102\n" \
    "v_mov_b64 v[60:61], 0\n" \
    "v_mov_b64 v[62:63], 0\n" \
    "v_mov_b64 v[88:89], 0\n" \
    "v_mov_b64 v[90:91], 0\n" \
    "v_mov_b64 v[92:93], 0\n" \
    "v_mov_b64 v[94:95], 0\n" \
    "s_waitcnt vmcnt(2) lgkmcnt(0)\n" \
    "s_barrier\n" \
    "ds_read_b128 v[68:71], v109 offset:256\n" \
    "ds_read_b128 v[102:105], v111 offset:384\n" \
    "s_cbranch_vccnz 7f\n" \
    "buffer_load_dwordx2 v[98:99], v113, %[rs], s78 offen sc1\n" \
    "buffer_load_dword v100, off, %[rw], s75 sc1\n" \
    "v_mov_b64 v[64:65], 0\n" \
    "v_mov_b64 v[66:67], 0\n" \
    "s_waitcnt lgkmcnt(1)\n" \
    "v_smfmac_f32_16x16x64_bf16 v[56:59], v[68:71], v[8:15], v124\n" \
    "s_waitcnt lgkmcnt(0)\n" \
    "ds_read_b128 v[36:39], v106 offset:39936\n" \
    "v_smfmac_f32_16x16x64_bf16 v[56:59], v[102:105], v[16:23], v124\n" \
    "ds_read_b128 v[0:3], v133 offset:51200\n" \
    "v_smfmac_f32_16x16x64_bf16 v[60:63], v[68:71], v[40:47], v124\n" \
    "ds_read_b128 v[4:7], v133 offset:52224\n" \
    "v_smfmac_f32_16x16x64_bf16 v[60:63], v[102:105], v[72:79], v124\n" \
    "ds_read_b128 v[16:19], v112 offset:51200\n" \
    "v_add_f32 v56, v56, v58\n" \
    "v_smfmac_f32_16x16x64_bf16 v[88:91], v[68:71], v[80:87], v124\n" \
    "ds_read_b128 v[20:23], v112 offset:52224\n" \
    "v_add_f32 v57, v57, v59\n" \
    "v_smfmac_f32_16x16x64_bf16 v[88:91], v[102:105], v[48:55], v124\n" \
    "v_add_f32 v60, v60, v62\n" \
    "v_smfmac_f32_16x16x64_bf16 v[92:95], v[68:71], v[24:31], v124\n" \
    "s_waitcnt lgkmcnt(4)\n" \
    "v_add_f32 v61, v61, v63\n" \
    "v_smfmac_f32_16x16x64_bf16 v[92:95], v[102:105], v[32:39], v124\n" \
    "s_waitcnt lgkmcnt(2)\n" \
    "v_add_f32 v88, v88, v90\n" \
    "v_smfmac_f32_16x16x64_bf16 v[64:67], v[68:71], v[0:7], v124\n" \
    "s_waitcnt lgkmcnt(0)\n" \
    "v_add_f32 v89, v89, v91\n" \
    "v_smfmac_f32_16x16x64_bf16 v[64:67], v[102:105], v[16:23], v124\n" \
    "v_add_f32 v92, v92, v94\n" \
    "v_add_f32 v93, v93, v95\n" \
    "v_cvt_pk_bf16_f32 v8, v56, v60\n" \
    "v_cvt_pk_bf16_f32 v10, v57, v61\n" \
    "v_cvt_pk_bf16_f32 v9, v88, v92\n" \
    "v_cvt_pk_bf16_f32 v11, v89, v93\n" \
    "buffer_store_dwordx4 v[8:11], v134, %[rs], s79 offen sc1\n" \
    "v_add_f32 v64, v64, v66\n" \
    "v_add_f32 v65, v65, v67\n" \
    "v_cvt_pk_bf16_f32 v64, v64, v65\n" \
    "buffer_store_dword v64, v135, %[rs], s79 offen sc1\n" \
    "s_cbranch_scc1 16f\n" \
    "s_memtime s[88:89]\n" \
    "s_memrealtime s[44:45]\n" \
    "s_and_b32 s75, s91, 3\n" \
    "s_lshl_b32 s75, s75, 16\n" \
    "s_memrealtime s[46:47]\n" \
    "s_add_i32 s69, s75, s26\n" \
    "s_xor_b32 s70, s69, 2048\n" \
    "s_memrealtime s[48:49]\n" \
    "s_andn2_b32 s71, s69, 0x1fff\n" \
    "s_add_i32 s71, s71, 32768\n" \
    "s_memrealtime s[50:51]\n" \
    "s_mov_b32 s94, 0\n" \
    "s_mov_b32 s43, 0\n" \
    "s_memrealtime s[52:53]\n" \
    "s_cmp_lt_i32 s38, s91\n" \
    "s_nop 0\n" \
    "s_memrealtime s[54:55]\n" \
    "s_nop 1\n" \
    "s_memrealtime s[56:57]\n" \
    "s_nop 1\n" \
    "s_memrealtime vcc\n" \
    "v_and_b32 v19, 0x380, v127\n" \
    "v_add_u32 v19, v19, v127\n" \
    "v_and_b32 v18, 0x300, v127\n" \
    "v_add_u32 v18, v18, v127\n" \
    "s_cbranch_scc1 40f\n" \
    "41:\n" \
    "buffer_load_dwordx4 v19, %[rw], s92 offen sc1 lds\n" \
    "s_add_i32 m0, s95, 896\n" \
    "s_waitcnt lgkmcnt(0)\n" \
    "s_add_i32 s44, s44, s46\n" \
    "s_add_i32 s48, s48, s50\n" \
    "s_add_i32 s52, s52, s54\n" \
    "s_add_i32 s44, s44, s48\n" \
    "s_add_i32 s52, s52, s56\n" \
    "s_add_i32 s44, s44, s52\n" \
    "s_add_i32 s80, s44, vcc_lo\n" \
    "s_sub_i32 s45, s80, s29\n" \
    "s_mov_b32 s29, s80\n" \
    "s_sub_i32 s47, s88, s24\n" \
    "s_mov_b32 s24, s88\n" \
    "buffer_load_dwordx4 v19, %[rw], s92 offen offset:128 sc1 lds\n" \
    "s_add_i32 m0, s95, 8192\n" \
    "s_add_i32 s76, s29, s42\n" \
    "v_add_u32 v16, -1, s36\n" \
    "v_mov_b32 v17, s76\n" \
    "s_mov_b64 exec, s[72:73]\n" \
    "buffer_store_dwordx2 v[16:17], off, %[rw], s65 sc1\n" \
    "s_mov_b64 exec, -1\n" \
    "ds_read_b128 v[72:75], v133 offset:0\n" \
    "ds_read_b128 v[76:79], v133 offset:1024\n" \
    "ds_read_b128 v[80:83], v133 offset:2048\n" \
    "ds_read_b128 v[84:87], v133 offset:3072\n" \
    "ds_read_b128 v[88:91], v133 offset:16384\n" \
    "ds_read_b128 v[92:95], v133 offset:17408\n" \
    "buffer_load_dwordx4 v18, %[rw], s93 offen sc1 lds\n" \
    "s_add_i32 m0, s95, 8960\n" \
    "v_mov_b64 v[40:41], 0\n" \
    "v_mov_b64 v[42:43], 0\n" \
    "v_mov_b64 v[44:45], 0\n" \
    "v_mov_b64 v[46:47], 0\n" \
    "v_mov_b64 v[48:49], 0\n" \
    "v_mov_b64 v[50:51], 0\n" \
    "v_mov_b64 v[52:53], 0\n" \
    "v_mov_b64 v[54:55], 0\n" \
    "buffer_load_dwordx4 v18, %[rw], s93 offen offset:256 sc1 lds\n" \
    "s_movk_i32 m0, 0x2000\n" \
    "v_mov_b64 v[56:57], 0\n" \
    "v_mov_b64 v[58:59], 0\n" \
    "v_mov_b64 v[60:61], 0\n" \
    "v_mov_b64 v[62:63], 0\n" \
    "v_mov_b64 v[64:65], 0\n" \
    "v_mov_b64 v[66:67], 0\n" \
    "v_mov_b64 v[68:69], 0\n" \
    "v_mov_b64 v[70:71], 0\n" \
    "v_add_u32 v108, s74, v127\n" \
    "v_xor_b32 v24, 0x400, v108\n" \
    "v_xor_b32 v28, 0x800, v108\n" \
    "v_xor_b32 v32, 0xc00, v108\n" \
    "v_and_b32 v22, 0x200, v127\n" \
    "s_and_b32 s76, s26, 0x800\n" \
    "v_mad_u32_u24 v22, v22, 3, v127\n" \
    "v_xor_b32 v22, s76, v22\n" \
    "s_waitcnt vmcnt(7)\n" \
    "v_cmp_ne_u16 vcc, s91, v99\n" \
    "v_readfirstlane_b32 s80, v100\n" \
    "s_max_i32 s38, s38, s80\n" \
    "s_cbranch_vccnz 15f\n" \
    "10:\n" \
    "v_subrev_u32 v20, s29, v98\n" \
    "s_bitcmp1_b32 s30, 15\n" \
    "v_min_i32 v20, 0x4000, v20\n" \
    "v_max_i32 v20, 0xffffc000, v20\n" \
    "s_cbranch_scc1 18f\n" \
    "19:\n" \
    "v_readlane_b32 s79, v20, s83\n" \
    "v_max_i32_dpp v20, v20, v20 row_shr:1 row_mask:0xf bank_mask:0xf\n" \
    "s_mul_i32 s49, s45, s25\n" \
    "s_lshr_b32 s49, s49, 12\n" \
    "v_max_i32_dpp v20, v20, v20 row_shr:2 row_mask:0xf bank_mask:0xf\n" \
    "s_sub_i32 s49, s47, s49\n" \
    "s_mul_hi_u32 s47, s47, s41\n" \
    "v_max_i32_dpp v20, v20, v20 row_shr:4 row_mask:0xf bank_mask:0xf\n" \
    "s_sub_i32 s47, s45, s47\n" \
    "s_lshl_b32 s47, s47, 18\n" \
    "v_max_i32_dpp v20, v20, v20 row_shr:8 row_mask:0xf bank_mask:0xf\n" \
    "s_cmp_lt_u32 s45, 1480\n" \
    "s_cselect_b32 s49, s49, 0\n" \
    "v_max_i32_dpp v20, v20, v20 row_bcast:15 row_mask:0xa bank_mask:0xf\n" \
    "s_cselect_b32 s47, s47, 0\n" \
    "s_add_i32 s25, s25, s49\n" \
    "v_max_i32_dpp v20, v20, v20 row_bcast:31 row_mask:0xc bank_mask:0xf\n" \
    "s_add_i32 s41, s41, s47\n" \
    "s_add_i32 s76, s42, s28\n" \
    "s_add_i32 s79, s79, s76\n" \
    "v_readlane_b32 s37, v20, 63\n" \
    "s_add_i32 s37, s37, s76\n" \
    "s_cmp_lg_u32 s94, 0\n" \
    "s_cselect_b32 s36, s36, s91\n" \
    "s_cbranch_scc0 12b\n" \
    "s_branch 9f\n" \
    "5:\n" \
    "v_mov_b32 v107, 0\n" \
    "v_max3_u32 v107, v107, v0, v1\n" \
    "v_max3_u32 v107, v107, v2, v3\n" \
    "v_max3_u32 v107, v107, v4, v5\n" \
    "v_max3_u32 v107, v107, v6, v7\n" \
    "v_max3_u32 v107, v107, v8, v9\n" \
    "v_max3_u32 v107, v107, v10, v11\n" \
    "v_max3_u32 v107, v107, v12, v13\n" \
    "v_max3_u32 v107, v107, v14, v15\n" \
    "v_max3_u32 v107, v107, v16, v17\n" \
    "v_max3_u32 v107, v107, v18, v19\n" \
    "v_max3_u32 v107, v107, v24, v25\n" \
    "v_max3_u32 v107, v107, v26, v27\n" \
    "v_max3_u32 v107, v107, v28, v29\n" \
    "v_max3_u32 v107, v107, v30, v31\n" \
    "v_max3_u32 v107, v107, v32, v33\n" \
    "v_max3_u32 v107, v107, v34, v35\n" \
    "v_max3_u32 v107, v107, v36, v37\n" \
    "v_max3_u32 v107, v107, v38, v39\n" \
    "v_cmp_eq_u32 vcc, -1, v107\n" \
    "s_nop 4\n" \
    "s_cbranch_vccz 6b\n" \
    "s_cmp_eq_u32 s36, 0\n" \
    "s_cbranch_scc1 6b\n" \
    "s_add_i32 s39, s39, -1\n" \
    "s_cmp_lt_i32 s39, 0\n" \
    "s_cbranch_scc1 6b\n" \
    "s_add_i32 s30, s30, 1\n" \
    "s_bitset1_b32 s30, 15\n" \
    "s_and_b32 s80, s31, 0xff\n" \
    "s_add_i32 s42, s42, s80\n" \
    "s_bfe_u32 s80, s31, 0x80008\n" \
    "s_add_i32 s40, s40, s80\n" \
    "s_waitcnt lgkmcnt(0)\n" \
    "s_mov_b32 s37, s29\n" \
    "s_nop 7\n" \
    "s_barrier\n" \
    "s_branch 0b\n" \
    "15:\n" \
    "s_add_i32 s30, s30, 0x10000\n" \
    "s_add_i32 s39, s39, -1\n" \
    "s_cmp_lt_i32 s39, 0\n" \
    "s_cbranch_scc1 10b\n" \
    "s_sleep 9\n" \
    "buffer_load_dwordx2 v[98:99], v113, %[rs], s78 offen sc1\n" \
    "s_waitcnt vmcnt(0)\n" \
    "v_cmp_ne_u16 vcc, s91, v99\n" \
    "s_nop 1\n" \
    "s_cbranch_vccnz 15b\n" \
    "s_memtime s[88:89]\n" \
    "s_memrealtime s[44:45]\n" \
    "s_nop 1\n" \
    "s_memrealtime s[46:47]\n" \
    "s_nop 1\n" \
    "s_memrealtime s[48:49]\n" \
    "s_nop 1\n" \
    "s_memrealtime s[50:51]\n" \
    "s_nop 1\n" \
    "s_memrealtime s[52:53]\n" \
    "s_nop 1\n" \
    "s_memrealtime s[54:55]\n" \
    "s_nop 1\n" \
    "s_memrealtime s[56:57]\n" \
    "s_nop 1\n" \
    "s_memrealtime vcc\n" \
    "s_waitcnt lgkmcnt(0)\n" \
    "s_add_i32 s44, s44, s46\n" \
    "s_add_i32 s48, s48, s50\n" \
    "s_add_i32 s52, s52, s54\n" \
    "s_add_i32 s44, s44, s48\n" \
    "s_add_i32 s52, s52, s56\n" \
    "s_add_i32 s44, s44, s52\n" \
    "s_add_i32 s29, s44, vcc_lo\n" \
    "s_mov_b32 s24, s88\n" \
    "s_mov_b32 s45, -1\n" \
    "s_branch 10b\n" \
    "18:\n" \
    "v_cmp_gt_i32 vcc, -320, v20\n" \
    "s_bitset0_b32 s30, 15\n" \
    "s_lshr_b32 s80, s31, 16\n" \
    "s_cmp_lt_u32 s36, 4\n" \
    "s_cselect_b32 s80, 0, s80\n" \
    "s_min_u32 s76, s80, 1\n" \
    "s_lshl_b32 s76, s76, 16\n" \
    "s_sub_i32 s31, s31, s76\n" \
    "s_mul_i32 s76, s80, 768\n" \
    "s_cmp_eq_u64 vcc, 0\n" \
    "s_cselect_b32 s80, 0, s80\n" \
    "s_cselect_b32 s76, s76, 0\n" \
    "s_add_i32 s33, s33, s80\n" \
    "s_add_i32 s32, s32, s76\n" \
    "s_branch 19b\n" \
    "40:\n" \
    "s_waitcnt vmcnt(2)\n" \
    "v_readfirstlane_b32 s80, v100\n" \
    "s_max_i32 s38, s38, s80\n" \
    "s_cmp_lt_i32 s38, s91\n" \
    "s_cbranch_scc0 41b\n" \
    "s_mov_b32 s94, 8\n" \
    "v_mov_b32 v18, 0x7ffffff0\n" \
    "v_mov_b32 v19, 0x7ffffff0\n" \
    "s_branch 41b\n" \
    "17:\n" \
    "ds_read_b128 v[0:3], v108 offset:0\n" \
    "ds_read_b128 v[16:19], v108 offset:4096\n" \
    "s_waitcnt lgkmcnt(0)\n" \
    "s_branch 3b\n" \
    "20:\n" \
    "s_mov_b32 s91, 63\n" \
    "s_mov_b32 s78, 0x1a8000\n" \
    "buffer_load_dwordx2 v[98:99], v113, %[rs], s78 offen sc1\n" \
    "buffer_load_dword v100, off, %[rw], s62 sc1\n" \
    "s_waitcnt vmcnt(0)\n" \
    "16:\n" \
    "v_and_b32 v112, 0x300, v127\n" \
    "v_add_u32 v112, v112, v127\n" \
    "s_add_i32 s44, %[qb], 0\n" \
    "s_add_i32 s45, s44, 4096\n" \
    "buffer_load_dwordx4 v[136:139], v112, %[rw], s44 offen offset:0 sc1\n" \
    "v_lshrrev_b32 v103, 1, v127\n" \
    "s_lshr_b32 s76, s74, 1\n" \
    "v_add_u32 v101, s76, v103\n" \
    "ds_write_b64 v101, v[96:97] offset:9216\n" \
    "ds_read_b128 v[0:3], v127 offset:16384\n" \
    "v_lshrrev_b32 v42, 8, v127\n" \
    "v_bfe_u32 v40, v127, 4, 4\n" \
    "ds_read_b128 v[4:7], v127 offset:17408\n" \
    "v_and_b32 v41, 2, v42\n" \
    "v_add_u32 v43, v41, v42\n" \
    "ds_read_b128 v[8:11], v127 offset:18432\n" \
    "s_lshl_b32 s76, s60, 4\n" \
    "s_and_b32 s76, s76, 16\n" \
    "v_add_u32 v40, s76, v40\n" \
    "v_mad_i32_i24 v40, v43, -4, v40\n" \
    "buffer_load_dwordx4 v[140:143], v112, %[rw], s44 offen offset:2048 sc1\n" \
    "ds_read_b128 v[12:15], v127 offset:19456\n" \
    "v_mov_b32 v43, 0xff61b1e6\n" \
    "v_cmp_gt_i32 vcc, 0, v40\n" \
    "ds_read_b128 v[16:19], v127 offset:20480\n" \
    "v_cndmask_b32 v88, 0, v43, vcc\n" \
    "v_cmp_gt_i32 vcc, 1, v40\n" \
    "ds_read_b128 v[20:23], v127 offset:21504\n" \
    "v_cndmask_b32 v89, 0, v43, vcc\n" \
    "v_cmp_gt_i32 vcc, 2, v40\n" \
    "ds_read_b128 v[24:27], v127 offset:22528\n" \
    "v_cndmask_b32 v90, 0, v43, vcc\n" \
    "v_cmp_gt_i32 vcc, 3, v40\n" \
    "buffer_load_dwordx4 v[144:147], v112, %[rw], s45 offen offset:0 sc1\n" \
    "ds_read_b128 v[28:31], v127 offset:23552\n" \
    "v_cndmask_b32 v91, 0, v43, vcc\n" \
    "v_cmp_gt_i32 vcc, 8, v40\n" \
    "ds_read_b128 v[32:35], v131 offset:0\n" \
    "v_cndmask_b32 v92, 0, v43, vcc\n" \
    "v_cmp_gt_i32 vcc, 9, v40\n" \
    "ds_read_b128 v[36:39], v131 offset:128\n" \
    "v_cndmask_b32 v93, 0, v43, vcc\n" \
    "v_cmp_gt_i32 vcc, 10, v40\n" \
    "ds_read_b128 v[56:59], v127 offset:24576\n" \
    "v_cndmask_b32 v94, 0, v43, vcc\n" \
    "v_cmp_gt_i32 vcc, 11, v40\n" \
    "buffer_load_dwordx4 v[148:151], v112, %[rw], s45 offen offset:2048 sc1\n" \
    "ds_read_b128 v[60:63], v127 offset:25600\n" \
    "v_cndmask_b32 v95, 0, v43, vcc\n" \
    "v_mad_u32_u24 v44, v41, 3, v42\n" \
    "ds_read_b128 v[64:67], v127 offset:26624\n" \
    "v_bfe_u32 v45, v127, 6, 1\n" \
    "v_bfe_u32 v46, v127, 4, 2\n" \
    "s_waitcnt lgkmcnt(8)\n" \
    "ds_read_b128 v[68:71], v127 offset:27648\n" \
    "v_lshl_add_u32 v44, v46, 1, v44\n" \
    "v_bfe_u32 v46, v127, 7, 1\n" \
    "ds_read_b128 a[120:123], v127 offset:28672\n" \
    "v_lshl_add_u32 v44, v44, 1, v46\n" \
    "v_mul_u32_u24 v44, 4352, v44\n" \
    "ds_read_b128 a[124:127], v127 offset:29696\n" \
    "v_lshl_add_u32 v44, v45, 1, v44\n" \
    "v_lshlrev_b32 v45, 2, v45\n" \
    "ds_read_b128 v[80:83], v127 offset:30720\n" \
    "s_lshr_b32 s79, %[u], 4\n" \
    "s_mul_i32 s79, s79, 32 * 4352\n" \
    "s_and_b32 s94, %[u], 15\n" \
    "s_lshl2_add_u32 s79, s94, s79\n" \
    "s_add_i32 s79, s79, 0x3e00fc0\n" \
    "s_lshl3_add_u32 s94, s94, 0x4065f80\n" \
    "s_lshr_b32 s80, s60, 1\n" \
    "s_mul_i32 s76, s80, 49152\n" \
    "s_add_i32 s76, s76, 98304\n" \
    "v_add_u32 v128, s76, v127\n" \
    "v_xor_b32 v130, 0x38000, v128\n" \
    "ds_read_b128 v[84:87], v127 offset:31744\n" \
    "s_lshl_b32 s76, s80, 12\n" \
    "s_add_i32 s76, s76, 83968\n" \
    "v_add_u32 v132, s76, v127\n" \
    "s_add_i32 s92, s64, 16515072\n" \
    "s_add_i32 s93, s92, 0x1000000\n" \
    "s_mov_b32 m0, s95\n" \
    "v_and_b32 v97, 0x380, v127\n" \
    "v_add_u32 v97, v97, v127\n" \
    "s_waitcnt vmcnt(6)\n" \
    "v_readfirstlane_b32 s80, v100\n" \
    "v_cmp_ne_u16 vcc, s91, v99\n" \
    "s_max_i32 s38, s38, s80\n" \
    "s_cmp_lt_i32 s38, s91\n" \
    "s_cbranch_scc1 21f\n" \
    "22:\n" \
    "s_cbranch_vccnz 23f\n" \
    "24:\n" \
    "buffer_load_ushort v113, v44, %[rw], s79 offen sc1\n" \
    "buffer_load_dword v114, v45, %[rw], s94 offen sc1\n" \
    "v_subrev_u32 v96, s29, v98\n" \
    "v_min_i32 v96, 0x4000, v96\n" \
    "v_max_i32 v96, 0, v96\n" \
    "s_waitcnt lgkmcnt(0)\n" \
    "s_barrier\n" \
    "v_max_i32_dpp v96, v96, v96 row_shr:1 row_mask:0xf bank_mask:0xf\n" \
    "ds_read_b64 v[52:53], v103 offset:9216\n" \
    "s_and_b32 s75, s91, 3\n" \
    "v_max_i32_dpp v96, v96, v96 row_shr:2 row_mask:0xf bank_mask:0xf\n" \
    "ds_read_b64 v[54:55], v103 offset:9728\n" \
    "s_lshl_b32 s75, s75, 16\n" \
    "v_max_i32_dpp v96, v96, v96 row_shr:4 row_mask:0xf bank_mask:0xf\n" \
    "ds_read_b64 v[100:101], v103 offset:10240\n" \
    "s_add_i32 s69, s75, s26\n" \
    "v_max_i32_dpp v96, v96, v96 row_shr:8 row_mask:0xf bank_mask:0xf\n" \
    "ds_read_b64 v[102:103], v103 offset:10752\n" \
    "s_xor_b32 s70, s69, 2048\n" \
    "v_max_i32_dpp v96, v96, v96 row_bcast:15 row_mask:0xa bank_mask:0xf\n" \
    "ds_read_b128 a[0:3], v128 offset:0\n" \
    "s_andn2_b32 s71, s69, 0x1fff\n" \
    "v_max_i32_dpp v96, v96, v96 row_bcast:31 row_mask:0xc bank_mask:0xf\n" \
    "ds_read_b128 a[16:19], v128 offset:2048\n" \
    "s_add_i32 s71, s71, 32768\n" \
    "v_readlane_b32 s90, v96, 63\n" \
    "s_add_i32 s90, s90, s42\n" \
    "s_mul_i32 s90, s90, s25\n" \
    "s_ashr_i32 s90, s90, 12\n" \
    "s_add_i32 s90, s90, s24\n" \
    "buffer_load_dwordx4 v97, %[rw], s92 offen sc1 lds\n" \
    "s_add_i32 m0, s95, 896\n" \
    "v_mov_b32 v99, 0\n" \
    "buffer_load_dwordx4 v97, %[rw], s92 offen offset:128 sc1 lds\n" \
    "s_add_i32 m0, s95, 8192\n" \
    "v_mov_b32 v96, 0\n" \
    "25:\n" \
    "v_mov_b64 v[48:49], 0\n" \
    "v_mov_b64 v[50:51], 0\n" \
    "s_waitcnt vmcnt(7)\n" \
    "v_mfma_f32_16x16x32_bf16 v[40:43], v[0:3], v[136:139], v[88:91]\n" \
    "ds_read_b128 a[4:7], v128 offset:1024\n" \
    "v_max3_u32 v98, v136, v137, v136\n" \
    "v_mfma_f32_16x16x32_bf16 v[44:47], v[4:7], v[136:139], v[92:95]\n" \
    "ds_read_b128 a[20:23], v128 offset:3072\n" \
    "v_max3_u32 v98, v138, v139, v98\n" \
    "s_waitcnt vmcnt(6)\n" \
    "v_mfma_f32_16x16x32_bf16 v[40:43], v[8:11], v[140:143], v[40:43]\n" \
    "ds_read_b128 a[8:11], v130 offset:0\n" \
    "v_max3_u32 v98, v140, v141, v98\n" \
    "v_mfma_f32_16x16x32_bf16 v[44:47], v[12:15], v[140:143], v[44:47]\n" \
    "ds_read_b128 a[24:27], v130 offset:2048\n" \
    "v_max3_u32 v98, v142, v143, v98\n" \
    "v_smfmac_f32_16x16x64_bf16 v[48:51], v[32:35], v[136:143], v124\n" \
    "ds_read_b128 a[12:15], v130 offset:1024\n" \
    "s_waitcnt vmcnt(5)\n" \
    "v_mfma_f32_16x16x32_bf16 v[40:43], v[16:19], v[144:147], v[40:43]\n" \
    "ds_read_b128 a[28:31], v130 offset:3072\n" \
    "v_max3_u32 v98, v144, v145, v98\n" \
    "v_mfma_f32_16x16x32_bf16 v[44:47], v[20:23], v[144:147], v[44:47]\n" \
    "ds_read_b128 a[32:35], v128 offset:4096\n" \
    "v_max3_u32 v98, v146, v147, v98\n" \
    "s_waitcnt vmcnt(4)\n" \
    "v_smfmac_f32_16x16x64_bf16 v[48:51], v[36:39], v[144:151], v124\n" \
    "ds_read_b128 a[48:51], v128 offset:6144\n" \
    "v_mfma_f32_16x16x32_bf16 v[40:43], v[24:27], v[148:151], v[40:43]\n" \
    "ds_read_b128 a[36:39], v128 offset:5120\n" \
    "v_max3_u32 v98, v148, v149, v98\n" \
    "v_max3_u32 v98, v150, v151, v98\n" \
    "v_mfma_f32_16x16x32_bf16 v[44:47], v[28:31], v[148:151], v[44:47]\n" \
    "v_cmp_eq_u32 vcc, -1, v98\n" \
    "s_add_i32 s44, %[qb], 262144\n" \
    "s_add_i32 s45, s44, 4096\n" \
    "s_waitcnt lgkmcnt(3)\n" \
    "v_add_f32 v48, v48, v50\n" \
    "v_add_f32 v49, v49, v51\n" \
    "s_nop 0\n" \
    "s_cbranch_vccnz 26f\n" \
    "27:\n" \
    "v_max3_f32 v0, v40, v41, v42\n" \
    "v_max3_f32 v1, v43, v44, v45\n" \
    "v_max3_f32 v0, v0, v46, v47\n" \
    "v_max3_f32 v1, v1, v48, v49\n" \
    "v_max_f32 v0, v0, v1\n" \
    "v_max_f32 v1, v0, v1\n" \
    "buffer_load_dwordx4 v112, %[rw], s93 offen sc1 lds\n" \
    "s_add_i32 m0, s95, 8960\n" \
    "s_nop 0\n" \
    "v_permlane16_swap_b32 v0, v1\n" \
    "v_max_f32 v0, v0, v1\n" \
    "v_max_f32 v1, v0, v1\n" \
    "buffer_load_dwordx4 v112, %[rw], s93 offen offset:256 sc1 lds\n" \
    "s_nop 0\n" \
    "v_permlane32_swap_b32 v0, v1\n" \
    "v_max_f32 v0, v0, v1\n" \
    "v_mul_f32 v0, s81, v0\n" \
    "v_fma_f32 v40, v40, s81, -v0\n" \
    "v_fma_f32 v41, v41, s81, -v0\n" \
    "ds_read_b128 v[136:139], v132 offset:0\n" \
    "v_exp_f32 v40, v40\n" \
    "v_exp_f32 v41, v41\n" \
    "v_fma_f32 v42, v42, s81, -v0\n" \
    "ds_read_b128 v[140:143], v132 offset:1024\n" \
    "v_fma_f32 v43, v43, s81, -v0\n" \
    "v_exp_f32 v42, v42\n" \
    "v_exp_f32 v43, v43\n" \
    "ds_read_b128 v[144:147], v132 offset:8192\n" \
    "v_cvt_pk_bf16_f32 v32, v40, v41\n" \
    "v_fma_f32 v44, v44, s81, -v0\n" \
    "v_fma_f32 v45, v45, s81, -v0\n" \
    "ds_read_b128 v[148:151], v132 offset:9216\n" \
    "v_exp_f32 v44, v44\n" \
    "v_exp_f32 v45, v45\n" \
    "v_cvt_pk_bf16_f32 v33, v42, v43\n" \
    "ds_read_b128 a[52:55], v128 offset:7168\n" \
    "v_fma_f32 v46, v46, s81, -v0\n" \
    "v_fma_f32 v47, v47, s81, -v0\n" \
    "v_exp_f32 v46, v46\n" \
    "s_memtime vcc\n" \
    "v_exp_f32 v47, v47\n" \
    "v_cvt_pk_bf16_f32 v34, v44, v45\n" \
    "v_fma_f32 v48, v48, s81, -v0\n" \
    "v_fma_f32 v49, v49, s81, -v0\n" \
    "v_exp_f32 v48, v48\n" \
    "v_exp_f32 v49, v49\n" \
    "v_cvt_pk_bf16_f32 v35, v46, v47\n" \
    "v_cvt_pk_bf16_f32 v98, v48, v49\n" \
    "v_cvt_pk_bf16_f32 v97, v48, v49\n" \
    "s_waitcnt lgkmcnt(0)\n" \
    "v_mfma_f32_16x16x32_bf16 v[36:39], v[120:123], v[32:35], 0\n" \
    "s_sub_i32 s75, s90, vcc_lo\n" \
    "s_cmp_gt_i32 s75, 100\n" \
    "v_mfma_f32_16x16x32_bf16 v[0:3], v[56:59], v[32:35], 0\n" \
    "s_cbranch_scc1 30f\n" \
    "31:\n" \
    "ds_read_b128 a[40:43], v130 offset:4096\n" \
    "v_and_b32 v44, 0x200, v127\n" \
    "s_and_b32 s80, s26, 0x800\n" \
    "v_mad_u32_u24 v44, v44, 3, v127\n" \
    "v_mfma_f32_16x16x32_bf16 v[4:7], v[60:63], v[32:35], 0\n" \
    "s_mov_b32 m0, s74\n" \
    "s_add_i32 s76, s74, 4096\n" \
    "buffer_load_dwordx4 v125, %[rs], s69 offen sc1 lds\n" \
    "s_mov_b32 m0, s76\n" \
    "v_mfma_f32_16x16x32_bf16 v[8:11], v[64:67], v[32:35], 0\n" \
    "ds_read_b128 a[56:59], v130 offset:6144\n" \
    "v_xor_b32 v44, s80, v44\n" \
    "v_bfe_u32 v40, v127, 6, 2\n" \
    "v_mfma_f32_16x16x32_bf16 v[12:15], v[68:71], v[32:35], 0\n" \
    "buffer_load_dwordx4 v125, %[rs], s70 offen sc1 lds\n" \
    "s_movk_i32 m0, 0x2000\n" \
    "v_mfma_f32_16x16x32_bf16 v[16:19], a[120:123], v[32:35], 0\n" \
    "ds_read_b128 a[44:47], v130 offset:5120\n" \
    "v_bfe_u32 v41, v127, 5, 1\n" \
    "v_bfe_u32 v42, v127, 4, 1\n" \
    "v_mfma_f32_16x16x32_bf16 v[20:23], a[124:127], v[32:35], 0\n" \
    "s_mov_b32 exec_lo, s27\n" \
    "s_mov_b32 exec_hi, s27\n" \
    "buffer_load_dwordx4 v44, %[rs], s71 offen sc1 lds\n" \
    "s_mov_b64 exec, -1\n" \
    "v_mfma_f32_16x16x32_bf16 v[24:27], v[80:83], v[32:35], 0\n" \
    "ds_read_b128 a[60:63], v130 offset:7168\n" \
    "v_lshrrev_b32 v43, 8, v127\n" \
    "v_lshlrev_b32 v134, 14, v40\n" \
    "v_mfma_f32_16x16x32_bf16 v[28:31], v[84:87], v[32:35], 0\n" \
    "ds_read_b128 a[64:67], v128 offset:8192\n" \
    "v_lshl_add_u32 v134, v41, 12, v134\n" \
    "v_lshl_add_u32 v134, v42, 6, v134\n" \
    "v_mfma_f32_16x16x16_bf16 v[36:39], v[120:121], v[98:99], v[36:39]\n" \
    "ds_read_b128 a[80:83], v128 offset:10240\n" \
    "v_lshl_add_u32 v134, v43, 4, v134\n" \
    "v_and_b32 v41, 2, v40\n" \
    "v_mfma_f32_16x16x16_bf16 v[0:3], v[52:53], v[98:99], v[0:3]\n" \
    "buffer_load_dwordx4 v[72:75], v112, %[rw], s44 offen offset:0 sc1\n" \
    "v_mfma_f32_16x16x16_bf16 v[4:7], v[52:53], v[96:97], v[4:7]\n" \
    "ds_read_b128 a[68:71], v128 offset:9216\n" \
    "v_add_lshl_u32 v135, v40, v41, 12\n" \
    "v_bfe_u32 v42, v127, 4, 2\n" \
    "v_mfma_f32_16x16x16_bf16 v[8:11], v[54:55], v[98:99], v[8:11]\n" \
    "buffer_load_dwordx4 v[76:79], v112, %[rw], s44 offen offset:2048 sc1\n" \
    "v_mfma_f32_16x16x16_bf16 v[12:15], v[54:55], v[96:97], v[12:15]\n" \
    "ds_read_b128 a[84:87], v128 offset:11264\n" \
    "v_lshl_add_u32 v135, v42, 5, v135\n" \
    "v_lshl_add_u32 v135, v43, 3, v135\n" \
    "v_mfma_f32_16x16x16_bf16 v[16:19], v[100:101], v[98:99], v[16:19]\n" \
    "buffer_load_dwordx4 v[104:107], v112, %[rw], s45 offen offset:0 sc1\n" \
    "v_mfma_f32_16x16x16_bf16 v[20:23], v[100:101], v[96:97], v[20:23]\n" \
    "ds_read_b128 a[72:75], v130 offset:8192\n" \
    "v_mfma_f32_16x16x16_bf16 v[24:27], v[102:103], v[98:99], v[24:27]\n" \
    "buffer_load_dwordx4 v[108:111], v112, %[rw], s45 offen offset:2048 sc1\n" \
    "v_mfma_f32_16x16x16_bf16 v[28:31], v[102:103], v[96:97], v[28:31]\n" \
    "ds_read_b128 a[88:91], v130 offset:10240\n" \
    "v_cvt_pk_bf16_f32 v40, v0, v4\n" \
    "v_cvt_pk_bf16_f32 v41, v1, v5\n" \
    "v_cvt_pk_bf16_f32 v42, v2, v6\n" \
    "v_cvt_pk_bf16_f32 v43, v3, v7\n" \
    "v_cvt_pk_bf16_f32 v44, v8, v12\n" \
    "v_cvt_pk_bf16_f32 v45, v9, v13\n" \
    "v_mfma_f32_16x16x32_bf16 v[64:67], v[136:139], v[40:43], 0\n" \
    "s_mov_b32 s46, %[tp]\n" \
    "ds_read_b128 a[76:79], v130 offset:9216\n" \
    "v_cvt_pk_bf16_f32 v46, v10, v14\n" \
    "v_cvt_pk_bf16_f32 v47, v11, v15\n" \
    "v_mfma_f32_16x16x32_bf16 v[56:59], a[0:3], v[40:43], 0\n" \
    "s_mov_b32 s47, %[tx]\n" \
    "ds_read_b128 a[92:95], v130 offset:11264\n" \
    "v_cvt_pk_bf16_f32 v48, v16, v20\n" \
    "v_cvt_pk_bf16_f32 v49, v17, v21\n" \
    "v_mfma_f32_16x16x32_bf16 v[64:67], v[140:143], v[44:47], v[64:67]\n" \
    "ds_read_b128 a[96:99], v128 offset:12288\n" \
    "v_cvt_pk_bf16_f32 v50, v18, v22\n" \
    "v_cvt_pk_bf16_f32 v51, v19, v23\n" \
    "v_mfma_f32_16x16x32_bf16 v[60:63], a[16:19], v[40:43], 0\n" \
    "s_waitcnt lgkmcnt(5)\n" \
    "ds_read_b128 a[112:115], v128 offset:14336\n" \
    "v_cvt_pk_bf16_f32 v52, v24, v28\n" \
    "v_cvt_pk_bf16_f32 v53, v25, v29\n" \
    "v_mfma_f32_16x16x32_bf16 v[64:67], v[144:147], v[48:51], v[64:67]\n" \
    "ds_read_b128 a[100:103], v128 offset:13312\n" \
    "v_cvt_pk_bf16_f32 v54, v26, v30\n" \
    "v_cvt_pk_bf16_f32 v55, v27, v31\n" \
    "v_mfma_f32_16x16x32_bf16 v[56:59], a[4:7], v[44:47], v[56:59]\n" \
    "ds_read_b128 a[116:119], v128 offset:15360\n" \
    "v_rcp_f32 v36, v36\n" \
    "v_mfma_f32_16x16x32_bf16 v[64:67], v[148:151], v[52:55], v[64:67]\n" \
    "ds_read_b128 a[104:107], v130 offset:12288\n" \
    "v_add_u32 v37, s74, v127\n" \
    "v_xor_b32 v38, 0x400, v37\n" \
    "v_mfma_f32_16x16x32_bf16 v[60:63], a[20:23], v[44:47], v[60:63]\n" \
    "ds_read_b128 a[120:123], v130 offset:14336\n" \
    "v_xor_b32 v39, 0x800, v37\n" \
    "v_xor_b32 v132, 0xc00, v37\n" \
    "v_mfma_f32_16x16x32_bf16 v[56:59], a[8:11], v[48:51], v[56:59]\n" \
    "ds_read_b128 a[108:111], v130 offset:13312\n" \
    "v_mul_f32 v64, v64, v36\n" \
    "v_mul_f32 v65, v65, v36\n" \
    "v_mfma_f32_16x16x32_bf16 v[60:63], a[24:27], v[48:51], v[60:63]\n" \
    "ds_read_b128 a[124:127], v130 offset:15360\n" \
    "v_mul_f32 v66, v66, v36\n" \
    "v_mul_f32 v67, v67, v36\n" \
    "v_mfma_f32_16x16x32_bf16 v[56:59], a[12:15], v[52:55], v[56:59]\n" \
    "v_cvt_pk_bf16_f32 v100, v64, v65\n" \
    "v_cvt_pk_bf16_f32 v101, v66, v67\n" \
    "v_mfma_f32_16x16x32_bf16 v[60:63], a[28:31], v[52:55], v[60:63]\n" \
    "buffer_store_dwordx2 v[100:101], v135, %[rw], s47 offen sc1\n" \
    "v_mfma_f32_16x16x32_bf16 v[64:67], a[32:35], v[40:43], 0\n" \
    "v_mul_f32 v56, v56, v36\n" \
    "v_mul_f32 v57, v57, v36\n" \
    "v_mfma_f32_16x16x32_bf16 v[68:71], a[48:51], v[40:43], 0\n" \
    "v_mul_f32 v58, v58, v36\n" \
    "v_mul_f32 v59, v59, v36\n" \
    "v_mfma_f32_16x16x32_bf16 v[64:67], a[36:39], v[44:47], v[64:67]\n" \
    "v_mul_f32 v60, v60, v36\n" \
    "v_mul_f32 v61, v61, v36\n" \
    "v_mfma_f32_16x16x32_bf16 v[68:71], a[52:55], v[44:47], v[68:71]\n" \
    "v_mul_f32 v62, v62, v36\n" \
    "v_mul_f32 v63, v63, v36\n" \
    "v_mfma_f32_16x16x32_bf16 v[64:67], a[40:43], v[48:51], v[64:67]\n" \
    "v_cvt_pk_bf16_f32 v96, v56, v57\n" \
    "v_cvt_pk_bf16_f32 v97, v58, v59\n" \
    "v_mfma_f32_16x16x32_bf16 v[68:71], a[56:59], v[48:51], v[68:71]\n" \
    "v_cvt_pk_bf16_f32 v98, v60, v61\n" \
    "v_cvt_pk_bf16_f32 v99, v62, v63\n" \
    "v_mfma_f32_16x16x32_bf16 v[64:67], a[44:47], v[52:55], v[64:67]\n" \
    "buffer_store_dwordx4 v[96:99], v134, %[rw], s46 offen sc1\n" \
    "s_add_i32 s46, s46, 0x20000\n" \
    "v_mfma_f32_16x16x32_bf16 v[68:71], a[60:63], v[52:55], v[68:71]\n" \
    "v_mov_b64 v[80:81], 0\n" \
    "v_mov_b64 v[82:83], 0\n" \
    "v_mfma_f32_16x16x32_bf16 v[56:59], a[64:67], v[40:43], 0\n" \
    "v_mul_f32 v64, v64, v36\n" \
    "v_mul_f32 v65, v65, v36\n" \
    "v_mfma_f32_16x16x32_bf16 v[60:63], a[80:83], v[40:43], 0\n" \
    "v_mul_f32 v66, v66, v36\n" \
    "v_mul_f32 v67, v67, v36\n" \
    "v_mfma_f32_16x16x32_bf16 v[56:59], a[68:71], v[44:47], v[56:59]\n" \
    "v_mul_f32 v68, v68, v36\n" \
    "v_mul_f32 v69, v69, v36\n" \
    "v_mfma_f32_16x16x32_bf16 v[60:63], a[84:87], v[44:47], v[60:63]\n" \
    "v_mul_f32 v70, v70, v36\n" \
    "v_mul_f32 v71, v71, v36\n" \
    "s_waitcnt lgkmcnt(11)\n" \
    "v_mfma_f32_16x16x32_bf16 v[56:59], a[72:75], v[48:51], v[56:59]\n" \
    "v_cvt_pk_bf16_f32 v100, v64, v65\n" \
    "v_cvt_pk_bf16_f32 v101, v66, v67\n" \
    "s_waitcnt lgkmcnt(10)\n" \
    "v_mfma_f32_16x16x32_bf16 v[60:63], a[88:91], v[48:51], v[60:63]\n" \
    "s_waitcnt vmcnt(6) lgkmcnt(0)\n" \
    "s_barrier\n" \
    "v_cvt_pk_bf16_f32 v102, v68, v69\n" \
    "v_cvt_pk_bf16_f32 v103, v70, v71\n" \
    "v_mfma_f32_16x16x32_bf16 v[56:59], a[76:79], v[52:55], v[56:59]\n" \
    "ds_read_b128 v[0:3], v37 offset:0\n" \
    "buffer_store_dwordx4 v[100:103], v134, %[rw], s46 offen sc1\n" \
    "s_add_i32 s46, s46, 0x20000\n" \
    "v_mfma_f32_16x16x32_bf16 v[60:63], a[92:95], v[52:55], v[60:63]\n" \
    "ds_read_b128 v[4:7], v38 offset:0\n" \
    "v_mov_b64 v[84:85], 0\n" \
    "v_mov_b64 v[86:87], 0\n" \
    "v_mfma_f32_16x16x32_bf16 v[64:67], a[96:99], v[40:43], 0\n" \
    "ds_read_b128 v[8:11], v39 offset:0\n" \
    "v_mul_f32 v56, v56, v36\n" \
    "v_mul_f32 v57, v57, v36\n" \
    "v_mfma_f32_16x16x32_bf16 v[68:71], a[112:115], v[40:43], 0\n" \
    "ds_read_b128 v[12:15], v132 offset:0\n" \
    "v_mul_f32 v58, v58, v36\n" \
    "v_mul_f32 v59, v59, v36\n" \
    "v_mfma_f32_16x16x32_bf16 v[64:67], a[100:103], v[44:47], v[64:67]\n" \
    "ds_read_b128 v[16:19], v37 offset:4096\n" \
    "v_mul_f32 v60, v60, v36\n" \
    "v_mul_f32 v61, v61, v36\n" \
    "v_mfma_f32_16x16x32_bf16 v[68:71], a[116:119], v[44:47], v[68:71]\n" \
    "ds_read_b128 v[20:23], v38 offset:4096\n" \
    "v_mul_f32 v62, v62, v36\n" \
    "v_mul_f32 v63, v63, v36\n" \
    "v_mfma_f32_16x16x32_bf16 v[64:67], a[104:107], v[48:51], v[64:67]\n" \
    "ds_read_b128 v[24:27], v39 offset:4096\n" \
    "v_cvt_pk_bf16_f32 v96, v56, v57\n" \
    "v_cvt_pk_bf16_f32 v97, v58, v59\n" \
    "v_mfma_f32_16x16x32_bf16 v[68:71], a[120:123], v[48:51], v[68:71]\n" \
    "ds_read_b128 v[28:31], v132 offset:4096\n" \
    "v_cvt_pk_bf16_f32 v98, v60, v61\n" \
    "v_cvt_pk_bf16_f32 v99, v62, v63\n" \
    "v_mfma_f32_16x16x32_bf16 v[64:67], a[108:111], v[52:55], v[64:67]\n" \
    "ds_read_b128 v[32:35], v126 offset:0\n" \
    "buffer_store_dwordx4 v[96:99], v134, %[rw], s46 offen sc1\n" \
    "s_add_i32 s46, s46, 0x20000\n" \
    "v_mfma_f32_16x16x32_bf16 v[68:71], a[124:127], v[52:55], v[68:71]\n" \
    "s_waitcnt lgkmcnt(3)\n" \
    "33:\n" \
    "v_smfmac_f32_16x16x64_bf16 v[80:83], v[0:3], a[128:135], v124\n" \
    "v_mul_f32 v64, v64, v36\n" \
    "v_smfmac_f32_16x16x64_bf16 v[80:83], v[4:7], a[144:151], v124\n" \
    "v_mul_f32 v65, v65, v36\n" \
    "v_smfmac_f32_16x16x64_bf16 v[80:83], v[8:11], a[160:167], v124\n" \
    "s_waitcnt lgkmcnt(0)\n" \
    "v_mul_f32 v66, v66, v36\n" \
    "v_smfmac_f32_16x16x64_bf16 v[80:83], v[12:15], a[176:183], v124\n" \
    "ds_read_b128 v[40:43], v127 offset:16384\n" \
    "v_mul_f32 v67, v67, v36\n" \
    "v_smfmac_f32_16x16x64_bf16 v[80:83], v[16:19], a[192:199], v124\n" \
    "ds_read_b128 v[44:47], v127 offset:17408\n" \
    "v_mul_f32 v68, v68, v36\n" \
    "v_smfmac_f32_16x16x64_bf16 v[80:83], v[20:23], a[208:215], v124\n" \
    "ds_read_b128 v[48:51], v127 offset:18432\n" \
    "v_mul_f32 v69, v69, v36\n" \
    "v_smfmac_f32_16x16x64_bf16 v[80:83], v[24:27], a[224:231], v124\n" \
    "ds_read_b128 v[52:55], v127 offset:19456\n" \
    "v_mul_f32 v70, v70, v36\n" \
    "v_mul_f32 v71, v71, v36\n" \
    "v_smfmac_f32_16x16x64_bf16 v[80:83], v[28:31], a[240:247], v124\n" \
    "ds_read_b128 v[56:59], v127 offset:20480\n" \
    "v_cvt_pk_bf16_f32 v100, v64, v65\n" \
    "v_cvt_pk_bf16_f32 v101, v66, v67\n" \
    "v_smfmac_f32_16x16x64_bf16 v[80:83], v[32:35], v[152:159], v124\n" \
    "ds_read_b128 v[60:63], v127 offset:21504\n" \
    "v_cvt_pk_bf16_f32 v102, v68, v69\n" \
    "v_cvt_pk_bf16_f32 v103, v70, v71\n" \
    "v_smfmac_f32_16x16x64_bf16 v[84:87], v[0:3], a[136:143], v124\n" \
    "ds_read_b128 v[64:67], v127 offset:22528\n" \
    "buffer_store_dwordx4 v[100:103], v134, %[rw], s46 offen sc1\n" \
    "v_smfmac_f32_16x16x64_bf16 v[84:87], v[4:7], a[152:159], v124\n" \
    "ds_read_b128 v[68:71], v127 offset:23552\n" \
    "v_add_f32 v80, v80, v82\n" \
    "v_add_f32 v81, v81, v83\n" \
    "v_smfmac_f32_16x16x64_bf16 v[84:87], v[8:11], a[168:175], v124\n" \
    "s_waitcnt vmcnt(5)\n" \
    "v_cmp_u_f32 vcc, v80, v81\n" \
    "v_max3_u32 v128, v72, v73, v72\n" \
    "v_smfmac_f32_16x16x64_bf16 v[84:87], v[12:15], a[184:191], v124\n" \
    "v_max3_u32 v128, v74, v75, v128\n" \
    "v_max3_u32 v128, v76, v77, v128\n" \
    "v_smfmac_f32_16x16x64_bf16 v[84:87], v[16:19], a[200:207], v124\n" \
    "s_cbranch_vccnz 32f\n" \
    "34:\n" \
    "v_max3_u32 v128, v78, v79, v128\n" \
    "v_max3_u32 v128, v104, v105, v128\n" \
    "v_smfmac_f32_16x16x64_bf16 v[84:87], v[20:23], a[216:223], v124\n" \
    "v_max3_u32 v128, v106, v107, v128\n" \
    "v_max3_u32 v128, v108, v109, v128\n" \
    "v_smfmac_f32_16x16x64_bf16 v[84:87], v[24:27], a[232:239], v124\n" \
    "v_max3_u32 v128, v110, v111, v128\n" \
    "v_cmp_eq_u32 vcc, -1, v128\n" \
    "v_smfmac_f32_16x16x64_bf16 v[84:87], v[28:31], a[248:255], v124\n" \
    "s_waitcnt lgkmcnt(0)\n" \
    "v_smfmac_f32_16x16x64_bf16 v[84:87], v[32:35], v[160:167], v124\n" \
    "s_cbranch_vccnz 28f\n" \
    "29:\n" \
    "v_mfma_f32_16x16x32_bf16 v[152:155], v[40:43], v[72:75], v[88:91]\n" \
    "ds_read_b128 a[128:131], v133 offset:0\n" \
    "v_mov_b64 v[96:97], 0\n" \
    "v_mov_b64 v[98:99], 0\n" \
    "v_mfma_f32_16x16x32_bf16 v[156:159], v[44:47], v[72:75], v[92:95]\n" \
    "ds_read_b128 a[132:135], v133 offset:1024\n" \
    "v_add_f32 v84, v84, v86\n" \
    "v_add_f32 v85, v85, v87\n" \
    "v_mfma_f32_16x16x32_bf16 v[152:155], v[48:51], v[76:79], v[152:155]\n" \
    "ds_read_b128 a[136:139], v133 offset:2048\n" \
    "v_cvt_pk_bf16_f32 v80, v80, v84\n" \
    "v_cvt_pk_bf16_f32 v81, v81, v85\n" \
    "v_mfma_f32_16x16x32_bf16 v[156:159], v[52:55], v[76:79], v[156:159]\n" \
    "ds_write2_b32 v129, v80, v81 offset0:0 offset1:144\n" \
    "v_mfma_f32_16x16x32_bf16 v[152:155], v[56:59], v[104:107], v[152:155]\n" \
    "ds_read_b128 a[140:143], v133 offset:3072\n" \
    "v_mov_b64 v[160:161], 0\n" \
    "v_mov_b64 v[162:163], 0\n" \
    "v_mfma_f32_16x16x32_bf16 v[156:159], v[60:63], v[104:107], v[156:159]\n" \
    "ds_read_b128 a[144:147], v133 offset:16384\n" \
    "v_mov_b64 v[100:101], 0\n" \
    "v_mov_b64 v[102:103], 0\n" \
    "v_mfma_f32_16x16x32_bf16 v[152:155], v[64:67], v[108:111], v[152:155]\n" \
    "ds_read_b128 a[148:151], v133 offset:17408\n" \
    "v_lshrrev_b32 v42, 1, v37\n" \
    "v_lshrrev_b32 v43, 1, v127\n" \
    "v_mfma_f32_16x16x32_bf16 v[156:159], v[68:71], v[108:111], v[156:159]\n" \
    "s_waitcnt lgkmcnt(3)\n" \
    "s_barrier\n" \
    "ds_read_b128 v[80:83], v131 offset:0\n" \
    "ds_read_b128 v[84:87], v131 offset:128\n" \
    "v_smfmac_f32_16x16x64_bf16 v[96:99], v[16:19], v[168:175], v124\n" \
    "ds_read_b128 a[152:155], v133 offset:18432\n" \
    "v_mov_b32 v61, 0\n" \
    "v_smfmac_f32_16x16x64_bf16 v[96:99], v[20:23], v[184:191], v124\n" \
    "ds_read_b128 a[156:159], v133 offset:19456\n" \
    "v_max3_f32 v40, v152, v153, v154\n" \
    "v_smfmac_f32_16x16x64_bf16 v[96:99], v[24:27], v[200:207], v124\n" \
    "ds_read_b128 a[160:163], v133 offset:32768\n" \
    "s_waitcnt lgkmcnt(3)\n" \
    "v_max3_f32 v41, v155, v156, v157\n" \
    "v_smfmac_f32_16x16x64_bf16 v[160:163], v[80:83], v[72:79], v124\n" \
    "ds_read_b128 a[164:167], v133 offset:33792\n" \
    "v_max3_f32 v40, v40, v158, v159\n" \
    "v_smfmac_f32_16x16x64_bf16 v[160:163], v[84:87], v[104:111], v124\n" \
    "ds_read_b128 a[168:171], v133 offset:34816\n" \
    "v_mov_b32 v62, 0\n" \
    "v_smfmac_f32_16x16x64_bf16 v[96:99], v[28:31], v[216:223], v124\n" \
    "ds_read_b128 a[172:175], v133 offset:35840\n" \
    "v_smfmac_f32_16x16x64_bf16 v[96:99], v[32:35], v[232:239], v124\n" \
    "ds_read_b128 a[176:179], v133 offset:49152\n" \
    "v_add_f32 v160, v160, v162\n" \
    "v_add_f32 v161, v161, v163\n" \
    "v_max3_f32 v41, v41, v160, v161\n" \
    "v_smfmac_f32_16x16x64_bf16 v[100:103], v[16:19], v[176:183], v124\n" \
    "ds_read_b128 a[180:183], v133 offset:50176\n" \
    "v_max_f32 v40, v40, v41\n" \
    "v_max_f32 v41, v40, v41\n" \
    "v_smfmac_f32_16x16x64_bf16 v[100:103], v[20:23], v[192:199], v124\n" \
    "ds_read_b128 a[192:195], v127 offset:24576\n" \
    "v_permlane16_swap_b32 v40, v41\n" \
    "v_max_f32 v40, v40, v41\n" \
    "v_max_f32 v41, v40, v41\n" \
    "v_smfmac_f32_16x16x64_bf16 v[96:99], v[0:3], a[128:135], v124\n" \
    "ds_read_b128 a[196:199], v127 offset:25600\n" \
    "v_permlane32_swap_b32 v40, v41\n" \
    "v_max_f32 v40, v40, v41\n" \
    "v_mul_f32 v40, s81, v40\n" \
    "v_smfmac_f32_16x16x64_bf16 v[96:99], v[4:7], a[144:151], v124\n" \
    "ds_read_b128 a[200:203], v127 offset:26624\n" \
    "s_waitcnt lgkmcnt(3)\n" \
    "v_fma_f32 v152, v152, s81, -v40\n" \
    "v_fma_f32 v153, v153, s81, -v40\n" \
    "v_exp_f32 v152, v152\n" \
    "v_smfmac_f32_16x16x64_bf16 v[96:99], v[8:11], a[160:167], v124\n" \
    "ds_read_b128 a[204:207], v127 offset:27648\n" \
    "v_exp_f32 v153, v153\n" \
    "v_fma_f32 v154, v154, s81, -v40\n" \
    "v_fma_f32 v155, v155, s81, -v40\n" \
    "v_smfmac_f32_16x16x64_bf16 v[96:99], v[12:15], a[176:183], v124\n" \
    "ds_read_b128 a[208:211], v127 offset:28672\n" \
    "v_exp_f32 v154, v154\n" \
    "v_exp_f32 v155, v155\n" \
    "v_cvt_pk_bf16_f32 v56, v152, v153\n" \
    "v_smfmac_f32_16x16x64_bf16 v[100:103], v[24:27], v[208:215], v124\n" \
    "ds_read_b128 a[212:215], v127 offset:29696\n" \
    "v_fma_f32 v156, v156, s81, -v40\n" \
    "v_fma_f32 v157, v157, s81, -v40\n" \
    "v_exp_f32 v156, v156\n" \
    "v_smfmac_f32_16x16x64_bf16 v[100:103], v[28:31], v[224:231], v124\n" \
    "ds_read_b128 a[216:219], v127 offset:30720\n" \
    "v_exp_f32 v157, v157\n" \
    "v_cvt_pk_bf16_f32 v57, v154, v155\n" \
    "v_fma_f32 v158, v158, s81, -v40\n" \
    "v_smfmac_f32_16x16x64_bf16 v[100:103], v[32:35], v[240:247], v124\n" \
    "ds_read_b128 a[220:223], v127 offset:31744\n" \
    "v_fma_f32 v159, v159, s81, -v40\n" \
    "v_exp_f32 v158, v158\n" \
    "v_exp_f32 v159, v159\n" \
    "v_smfmac_f32_16x16x64_bf16 v[100:103], v[12:15], v[248:255], v124\n" \
    "v_cvt_pk_bf16_f32 v58, v156, v157\n" \
    "v_fma_f32 v160, v160, s81, -v40\n" \
    "v_fma_f32 v161, v161, s81, -v40\n" \
    "v_smfmac_f32_16x16x64_bf16 v[100:103], v[0:3], a[136:143], v124\n" \
    "v_exp_f32 v160, v160\n" \
    "v_exp_f32 v161, v161\n" \
    "v_cvt_pk_bf16_f32 v59, v158, v159\n" \
    "v_smfmac_f32_16x16x64_bf16 v[100:103], v[4:7], a[152:159], v124\n" \
    "v_cvt_pk_bf16_f32 v60, v160, v161\n" \
    "v_cvt_pk_bf16_f32 v63, v160, v161\n" \
    "v_smfmac_f32_16x16x64_bf16 v[100:103], v[8:11], a[168:175], v124\n" \
    "s_waitcnt lgkmcnt(0)\n" \
    "v_mfma_f32_16x16x32_bf16 v[36:39], v[120:123], v[56:59], 0\n" \
    "v_add_f32 v96, v96, v98\n" \
    "v_add_f32 v97, v97, v99\n" \
    "v_mfma_f32_16x16x32_bf16 v[0:3], a[192:195], v[56:59], 0\n" \
    "v_add_f32 v100, v100, v102\n" \
    "v_add_f32 v101, v101, v103\n" \
    "v_mfma_f32_16x16x32_bf16 v[4:7], a[196:199], v[56:59], 0\n" \
    "v_cvt_pk_bf16_f32 v96, v96, v97\n" \
    "v_cvt_pk_bf16_f32 v97, v100, v101\n" \
    "v_mfma_f32_16x16x32_bf16 v[8:11], a[200:203], v[56:59], 0\n" \
    "ds_write_b64 v42, v[96:97] offset:9216\n" \
    "v_mfma_f32_16x16x32_bf16 v[12:15], a[204:207], v[56:59], 0\n" \
    "v_lshrrev_b32 v169, 8, v127\n" \
    "v_and_b32 v170, 2, v169\n" \
    "v_mfma_f32_16x16x32_bf16 v[16:19], a[208:211], v[56:59], 0\n" \
    "v_mad_u32_u24 v169, v170, 3, v169\n" \
    "v_bfe_u32 v168, v127, 6, 1\n" \
    "v_mfma_f32_16x16x32_bf16 v[20:23], a[212:215], v[56:59], 0\n" \
    "v_bfe_u32 v170, v127, 4, 2\n" \
    "v_lshl_add_u32 v169, v170, 1, v169\n" \
    "v_mfma_f32_16x16x32_bf16 v[24:27], a[216:219], v[56:59], 0\n" \
    "s_waitcnt lgkmcnt(0)\n" \
    "s_barrier\n" \
    "ds_read_b64 v[64:65], v43 offset:9216\n" \
    "ds_read_b64 v[66:67], v43 offset:9728\n" \
    "ds_read_b64 v[68:69], v43 offset:10240\n" \
    "ds_read_b64 v[70:71], v43 offset:10752\n" \
    "v_mfma_f32_16x16x32_bf16 v[28:31], a[220:223], v[56:59], 0\n" \
    "v_mul_u32_u24 v168, 1152, v168\n" \
    "v_lshl_add_u32 v168, v169, 2, v168\n" \
    "v_mfma_f32_16x16x16_bf16 v[36:39], v[120:121], v[60:61], v[36:39]\n" \
    "s_waitcnt lgkmcnt(2)\n" \
    "v_mfma_f32_16x16x16_bf16 v[0:3], v[64:65], v[60:61], v[0:3]\n" \
    "v_sub_f32 v169, 1.0, v114\n" \
    "v_lshlrev_b32 v172, 16, v113\n" \
    "v_mfma_f32_16x16x16_bf16 v[4:7], v[64:65], v[62:63], v[4:7]\n" \
    "s_waitcnt lgkmcnt(0)\n" \
    "v_mul_f32 v172, v172, v169\n" \
    "v_and_b32 v170, 16, v127\n" \
    "v_mfma_f32_16x16x16_bf16 v[8:11], v[66:67], v[60:61], v[8:11]\n" \
    "v_mov_b32 v169, 0x3f80\n" \
    "v_lshlrev_b32 v170, v170, v169\n" \
    "v_mfma_f32_16x16x16_bf16 v[12:15], v[66:67], v[62:63], v[12:15]\n" \
    "v_mov_b32_dpp v173, v172 row_ror:8 row_mask:0xf bank_mask:0xf\n" \
    "v_mov_b64 v[176:177], 0\n" \
    "v_mfma_f32_16x16x16_bf16 v[16:19], v[68:69], v[60:61], v[16:19]\n" \
    "v_mov_b64 v[178:179], 0\n" \
    "v_mov_b32_dpp v176, v170 quad_perm:[0,1,2,3] row_mask:0xf bank_mask:0x1\n" \
    "v_mfma_f32_16x16x16_bf16 v[20:23], v[68:69], v[62:63], v[20:23]\n" \
    "v_mov_b32_dpp v177, v170 quad_perm:[0,1,2,3] row_mask:0xf bank_mask:0x2\n" \
    "v_mov_b32_dpp v178, v170 quad_perm:[0,1,2,3] row_mask:0xf bank_mask:0x4\n" \
    "v_mfma_f32_16x16x16_bf16 v[24:27], v[70:71], v[60:61], v[24:27]\n" \
    "v_mov_b32_dpp v179, v170 quad_perm:[0,1,2,3] row_mask:0xf bank_mask:0x8\n" \
    "v_mfma_f32_16x16x16_bf16 v[28:31], v[70:71], v[62:63], v[28:31]\n" \
    "v_cvt_pk_bf16_f32 v40, v0, v4\n" \
    "v_cvt_pk_bf16_f32 v41, v1, v5\n" \
    "v_cvt_pk_bf16_f32 v42, v2, v6\n" \
    "v_cvt_pk_bf16_f32 v43, v3, v7\n" \
    "v_cvt_pk_bf16_f32 v44, v8, v12\n" \
    "v_cvt_pk_bf16_f32 v45, v9, v13\n" \
    "v_mfma_f32_16x16x32_bf16 v[80:83], v[136:139], v[40:43], 0\n" \
    "s_mov_b32 s46, %[tq]\n" \
    "v_cvt_pk_bf16_f32 v46, v10, v14\n" \
    "v_cvt_pk_bf16_f32 v47, v11, v15\n" \
    "v_mfma_f32_16x16x32_bf16 v[72:75], a[0:3], v[40:43], 0\n" \
    "s_add_i32 s47, %[tx], 0x40000\n" \
    "v_cvt_pk_bf16_f32 v48, v16, v20\n" \
    "v_cvt_pk_bf16_f32 v49, v17, v21\n" \
    "v_mfma_f32_16x16x32_bf16 v[80:83], v[140:143], v[44:47], v[80:83]\n" \
    "v_cvt_pk_bf16_f32 v50, v18, v22\n" \
    "v_cvt_pk_bf16_f32 v51, v19, v23\n" \
    "v_mfma_f32_16x16x32_bf16 v[76:79], a[16:19], v[40:43], 0\n" \
    "v_cvt_pk_bf16_f32 v52, v24, v28\n" \
    "v_cvt_pk_bf16_f32 v53, v25, v29\n" \
    "v_mfma_f32_16x16x32_bf16 v[80:83], v[144:147], v[48:51], v[80:83]\n" \
    "v_cvt_pk_bf16_f32 v54, v26, v30\n" \
    "v_cvt_pk_bf16_f32 v55, v27, v31\n" \
    "v_mfma_f32_16x16x32_bf16 v[72:75], a[4:7], v[44:47], v[72:75]\n" \
    "v_rcp_f32 v36, v36\n" \
    "v_mfma_f32_16x16x32_bf16 v[80:83], v[148:151], v[52:55], v[80:83]\n" \
    "s_and_b32 s94, %[u], 15\n" \
    "s_mul_i32 s94, s94, 2304\n" \
    "v_mfma_f32_16x16x32_bf16 v[76:79], a[20:23], v[44:47], v[76:79]\n" \
    "s_andn2_b32 s77, %[u], 15\n" \
    "s_lshl2_add_u32 s94, s77, s94\n" \
    "v_mfma_f32_16x16x32_bf16 v[72:75], a[8:11], v[48:51], v[72:75]\n" \
    "v_mul_f32 v80, v80, v36\n" \
    "v_mul_f32 v81, v81, v36\n" \
    "v_mfma_f32_16x16x32_bf16 v[76:79], a[24:27], v[48:51], v[76:79]\n" \
    "v_mul_f32 v82, v82, v36\n" \
    "v_mul_f32 v83, v83, v36\n" \
    "v_mfma_f32_16x16x32_bf16 v[72:75], a[12:15], v[52:55], v[72:75]\n" \
    "v_cvt_pk_bf16_f32 v100, v80, v81\n" \
    "v_cvt_pk_bf16_f32 v101, v82, v83\n" \
    "v_mfma_f32_16x16x32_bf16 v[76:79], a[28:31], v[52:55], v[76:79]\n" \
    "buffer_store_dwordx2 v[100:101], v135, %[rw], s47 offen sc1\n" \
    "v_mfma_f32_16x16x32_bf16 v[80:83], a[32:35], v[40:43], 0\n" \
    "v_mul_f32 v72, v72, v36\n" \
    "v_mul_f32 v73, v73, v36\n" \
    "v_mfma_f32_16x16x32_bf16 v[84:87], a[48:51], v[40:43], 0\n" \
    "v_mul_f32 v74, v74, v36\n" \
    "v_mul_f32 v75, v75, v36\n" \
    "v_mfma_f32_16x16x32_bf16 v[80:83], a[36:39], v[44:47], v[80:83]\n" \
    "v_mul_f32 v76, v76, v36\n" \
    "v_mul_f32 v77, v77, v36\n" \
    "v_mfma_f32_16x16x32_bf16 v[84:87], a[52:55], v[44:47], v[84:87]\n" \
    "v_mul_f32 v78, v78, v36\n" \
    "v_mul_f32 v79, v79, v36\n" \
    "v_mfma_f32_16x16x32_bf16 v[80:83], a[40:43], v[48:51], v[80:83]\n" \
    "v_cvt_pk_bf16_f32 v96, v72, v73\n" \
    "v_cvt_pk_bf16_f32 v97, v74, v75\n" \
    "v_mfma_f32_16x16x32_bf16 v[84:87], a[56:59], v[48:51], v[84:87]\n" \
    "v_cvt_pk_bf16_f32 v98, v76, v77\n" \
    "v_cvt_pk_bf16_f32 v99, v78, v79\n" \
    "v_mfma_f32_16x16x32_bf16 v[80:83], a[44:47], v[52:55], v[80:83]\n" \
    "buffer_store_dwordx4 v[96:99], v134, %[rw], s46 offen sc1\n" \
    "s_add_i32 s46, s46, 0x20000\n" \
    "v_mfma_f32_16x16x32_bf16 v[84:87], a[60:63], v[52:55], v[84:87]\n" \
    "s_add_i32 s94, s94, 2322432\n" \
    "v_mfma_f32_16x16x32_bf16 v[72:75], a[64:67], v[40:43], 0\n" \
    "v_mul_f32 v80, v80, v36\n" \
    "v_mul_f32 v81, v81, v36\n" \
    "v_mfma_f32_16x16x32_bf16 v[76:79], a[80:83], v[40:43], 0\n" \
    "v_mul_f32 v82, v82, v36\n" \
    "v_mul_f32 v83, v83, v36\n" \
    "v_mfma_f32_16x16x32_bf16 v[72:75], a[68:71], v[44:47], v[72:75]\n" \
    "v_mul_f32 v84, v84, v36\n" \
    "v_mul_f32 v85, v85, v36\n" \
    "v_mfma_f32_16x16x32_bf16 v[76:79], a[84:87], v[44:47], v[76:79]\n" \
    "v_mul_f32 v86, v86, v36\n" \
    "v_mul_f32 v87, v87, v36\n" \
    "v_mfma_f32_16x16x32_bf16 v[72:75], a[72:75], v[48:51], v[72:75]\n" \
    "v_cvt_pk_bf16_f32 v100, v80, v81\n" \
    "v_cvt_pk_bf16_f32 v101, v82, v83\n" \
    "v_mfma_f32_16x16x32_bf16 v[76:79], a[88:91], v[48:51], v[76:79]\n" \
    "v_cvt_pk_bf16_f32 v102, v84, v85\n" \
    "v_cvt_pk_bf16_f32 v103, v86, v87\n" \
    "v_mfma_f32_16x16x32_bf16 v[72:75], a[76:79], v[52:55], v[72:75]\n" \
    "buffer_store_dwordx4 v[100:103], v134, %[rw], s46 offen sc1\n" \
    "s_add_i32 s46, s46, 0x20000\n" \
    "v_mfma_f32_16x16x32_bf16 v[76:79], a[92:95], v[52:55], v[76:79]\n" \
    "v_mfma_f32_16x16x32_bf16 v[80:83], a[96:99], v[40:43], 0\n" \
    "v_mul_f32 v72, v72, v36\n" \
    "v_mul_f32 v73, v73, v36\n" \
    "v_mfma_f32_16x16x32_bf16 v[84:87], a[112:115], v[40:43], 0\n" \
    "v_mul_f32 v74, v74, v36\n" \
    "v_mul_f32 v75, v75, v36\n" \
    "v_mfma_f32_16x16x32_bf16 v[80:83], a[100:103], v[44:47], v[80:83]\n" \
    "v_mul_f32 v76, v76, v36\n" \
    "v_mul_f32 v77, v77, v36\n" \
    "v_mfma_f32_16x16x32_bf16 v[84:87], a[116:119], v[44:47], v[84:87]\n" \
    "v_mul_f32 v78, v78, v36\n" \
    "v_mul_f32 v79, v79, v36\n" \
    "v_mfma_f32_16x16x32_bf16 v[80:83], a[104:107], v[48:51], v[80:83]\n" \
    "v_cvt_pk_bf16_f32 v96, v72, v73\n" \
    "v_cvt_pk_bf16_f32 v97, v74, v75\n" \
    "v_mfma_f32_16x16x32_bf16 v[84:87], a[120:123], v[48:51], v[84:87]\n" \
    "v_cvt_pk_bf16_f32 v98, v76, v77\n" \
    "v_cvt_pk_bf16_f32 v99, v78, v79\n" \
    "v_mfma_f32_16x16x32_bf16 v[80:83], a[108:111], v[52:55], v[80:83]\n" \
    "buffer_store_dwordx4 v[96:99], v134, %[rw], s46 offen sc1\n" \
    "s_add_i32 s46, s46, 0x20000\n" \
    "v_mfma_f32_16x16x32_bf16 v[84:87], a[124:127], v[52:55], v[84:87]\n" \
    "s_memtime vcc\n" \
    "s_nop 2\n" \
    "v_mul_f32 v80, v80, v36\n" \
    "v_mul_f32 v81, v81, v36\n" \
    "v_mul_f32 v82, v82, v36\n" \
    "v_mul_f32 v83, v83, v36\n" \
    "v_mul_f32 v84, v84, v36\n" \
    "v_mul_f32 v85, v85, v36\n" \
    "v_mul_f32 v86, v86, v36\n" \
    "v_mul_f32 v87, v87, v36\n" \
    "v_cvt_pk_bf16_f32 v100, v80, v81\n" \
    "v_cvt_pk_bf16_f32 v101, v82, v83\n" \
    "v_cvt_pk_bf16_f32 v102, v84, v85\n" \
    "v_cvt_pk_bf16_f32 v103, v86, v87\n" \
    "buffer_store_dwordx4 v[100:103], v134, %[rw], s46 offen sc1\n" \
    "s_waitcnt lgkmcnt(0)\n" \
    "s_add_i32 s90, vcc_lo, %[fw]\n" \
    "35:\n" \
    "s_memtime vcc\n" \
    "s_waitcnt lgkmcnt(0)\n" \
    "s_sub_i32 s75, s90, vcc_lo\n" \
    "s_cmp_gt_i32 s75, 0\n" \
    "s_cbranch_scc1 35b\n" \
    "36:\n" \
    "buffer_load_dwordx4 v[0:3], v127, %[rw], %[ua] offen offset:0 sc1\n" \
    "buffer_load_dwordx4 v[4:7], v127, %[rw], %[ua] offen offset:1024 sc1\n" \
    "buffer_load_dwordx4 v[8:11], v127, %[rw], %[ua] offen offset:2048 sc1\n" \
    "buffer_load_dwordx4 v[12:15], v127, %[rw], %[ua] offen offset:3072 sc1\n" \
    "s_waitcnt vmcnt(3)\n" \
    "v_mfma_f32_16x16x32_bf16 v[32:35], v[176:179], v[0:3], 0\n" \
    "v_max3_u32 v40, v0, v1, v0\n" \
    "v_max3_u32 v40, v2, v3, v40\n" \
    "s_waitcnt vmcnt(2)\n" \
    "v_mfma_f32_16x16x32_bf16 v[32:35], v[176:179], v[4:7], v[32:35]\n" \
    "v_max3_u32 v40, v4, v5, v40\n" \
    "v_max3_u32 v40, v6, v7, v40\n" \
    "s_waitcnt vmcnt(1)\n" \
    "v_mfma_f32_16x16x32_bf16 v[32:35], v[176:179], v[8:11], v[32:35]\n" \
    "v_max3_u32 v40, v8, v9, v40\n" \
    "v_max3_u32 v40, v10, v11, v40\n" \
    "s_waitcnt vmcnt(0)\n" \
    "v_mfma_f32_16x16x32_bf16 v[32:35], v[176:179], v[12:15], v[32:35]\n" \
    "v_max3_u32 v40, v12, v13, v40\n" \
    "v_max3_u32 v40, v14, v15, v40\n" \
    "v_cmp_eq_u32 vcc, -1, v40\n" \
    "s_nop 3\n" \
    "s_cbranch_vccnz 38f\n" \
    "39:\n" \
    "v_add_f32_dpp v32, v32, v32 row_ror:8 row_mask:0xf bank_mask:0xf\n" \
    "v_add_f32_dpp v33, v33, v33 row_ror:8 row_mask:0xf bank_mask:0xf\n" \
    "s_mov_b32 exec_lo, 0x00ff00ff\n" \
    "s_mov_b32 exec_hi, 0x00ff00ff\n" \
    "v_fma_f32 v32, v32, v114, v172\n" \
    "v_fma_f32 v33, v33, v114, v173\n" \
    "v_cvt_pk_bf16_f32 v32, v32, v33\n" \
    "buffer_store_dword v32, v168, %[ru], s94 offen sc1\n" \
    "s_mov_b64 exec, -1\n" \
    "s_mov_b32 s36, 63\n" \
    "s_branch 9f\n" \
    "38:\n" \
    "s_add_i32 s39, s39, -1\n" \
    "s_cmp_lt_i32 s39, 0\n" \
    "s_cbranch_scc1 39b\n" \
    "s_add_i32 s30, s30, 0x10000\n" \
    "s_sleep 2\n" \
    "s_branch 36b\n" \
    "21:\n" \
    "s_add_i32 s39, s39, -1\n" \
    "s_cmp_lt_i32 s39, 0\n" \
    "s_cbranch_scc1 22b\n" \
    "buffer_load_dword v100, off, %[rw], s62 sc1\n" \
    "s_waitcnt vmcnt(0)\n" \
    "v_readfirstlane_b32 s38, v100\n" \
    "s_cmp_lt_i32 s38, s91\n" \
    "s_cbranch_scc1 21b\n" \
    "s_branch 22b\n" \
    "23:\n" \
    "s_add_i32 s39, s39, -1\n" \
    "s_cmp_lt_i32 s39, 0\n" \
    "s_cbranch_scc1 24b\n" \
    "s_sleep 4\n" \
    "buffer_load_dwordx2 v[98:99], v113, %[rs], s78 offen sc1\n" \
    "s_waitcnt vmcnt(0)\n" \
    "v_cmp_ne_u16 vcc, s91, v99\n" \
    "s_nop 1\n" \
    "s_cbranch_vccnz 23b\n" \
    "s_branch 24b\n" \
    "26:\n" \
    "s_add_i32 s39, s39, -1\n" \
    "s_cmp_lt_i32 s39, 0\n" \
    "s_cbranch_scc1 27b\n" \
    "s_sleep 4\n" \
    "s_add_i32 s44, %[qb], 0\n" \
    "s_add_i32 s45, s44, 4096\n" \
    "buffer_load_dwordx4 v[136:139], v112, %[rw], s44 offen offset:0 sc1\n" \
    "buffer_load_dwordx4 v[140:143], v112, %[rw], s44 offen offset:2048 sc1\n" \
    "buffer_load_dwordx4 v[144:147], v112, %[rw], s45 offen offset:0 sc1\n" \
    "buffer_load_dwordx4 v[148:151], v112, %[rw], s45 offen offset:2048 sc1\n" \
    "s_waitcnt vmcnt(0)\n" \
    "s_branch 25b\n" \
    "28:\n" \
    "s_add_i32 s39, s39, -1\n" \
    "s_cmp_lt_i32 s39, 0\n" \
    "s_cbranch_scc1 29b\n" \
    "s_sleep 4\n" \
    "s_add_i32 s44, %[qb], 262144\n" \
    "s_add_i32 s45, s44, 4096\n" \
    "buffer_load_dwordx4 v[72:75], v112, %[rw], s44 offen offset:0 sc1\n" \
    "buffer_load_dwordx4 v[76:79], v112, %[rw], s44 offen offset:2048 sc1\n" \
    "buffer_load_dwordx4 v[104:107], v112, %[rw], s45 offen offset:0 sc1\n" \
    "buffer_load_dwordx4 v[108:111], v112, %[rw], s45 offen offset:2048 sc1\n" \
    "s_waitcnt vmcnt(0)\n" \
    "v_max3_u32 v128, v72, v73, v72\n" \
    "v_max3_u32 v128, v74, v75, v128\n" \
    "v_max3_u32 v128, v76, v77, v128\n" \
    "v_max3_u32 v128, v78, v79, v128\n" \
    "v_max3_u32 v128, v104, v105, v128\n" \
    "v_max3_u32 v128, v106, v107, v128\n" \
    "v_max3_u32 v128, v108, v109, v128\n" \
    "v_max3_u32 v128, v110, v111, v128\n" \
    "v_cmp_eq_u32 vcc, -1, v128\n" \
    "s_nop 1\n" \
    "s_cbranch_vccnz 28b\n" \
    "s_branch 29b\n" \
    "30:\n" \
    "s_memtime vcc\n" \
    "s_waitcnt lgkmcnt(0)\n" \
    "s_sub_i32 s75, s90, vcc_lo\n" \
    "s_cmp_gt_i32 s75, 100\n" \
    "s_cbranch_scc1 30b\n" \
    "s_branch 31b\n" \
    "32:\n" \
    "v_max3_u32 v82, v0, v1, v0\n" \
    "v_max3_u32 v82, v2, v3, v82\n" \
    "v_max3_u32 v82, v4, v5, v82\n" \
    "v_max3_u32 v82, v6, v7, v82\n" \
    "v_max3_u32 v82, v8, v9, v82\n" \
    "v_max3_u32 v82, v10, v11, v82\n" \
    "v_max3_u32 v82, v12, v13, v82\n" \
    "v_max3_u32 v82, v14, v15, v82\n" \
    "v_max3_u32 v82, v16, v17, v82\n" \
    "v_max3_u32 v82, v18, v19, v82\n" \
    "v_max3_u32 v82, v20, v21, v82\n" \
    "v_max3_u32 v82, v22, v23, v82\n" \
    "v_max3_u32 v82, v24, v25, v82\n" \
    "v_max3_u32 v82, v26, v27, v82\n" \
    "v_max3_u32 v82, v28, v29, v82\n" \
    "v_max3_u32 v82, v30, v31, v82\n" \
    "v_max3_u32 v82, v32, v33, v82\n" \
    "v_max3_u32 v82, v34, v35, v82\n" \
    "v_cmp_eq_u32 vcc, -1, v82\n" \
    "s_nop 4\n" \
    "s_cbranch_vccz 34b\n" \
    "s_add_i32 s39, s39, -1\n" \
    "s_cmp_lt_i32 s39, 0\n" \
    "s_cbranch_scc1 34b\n" \
    "s_add_i32 s30, s30, 1\n" \
    "s_waitcnt lgkmcnt(0)\n" \
    "s_barrier\n" \
    "v_and_b32 v96, 0x200, v127\n" \
    "s_and_b32 s80, s26, 0x800\n" \
    "v_mad_u32_u24 v96, v96, 3, v127\n" \
    "v_xor_b32 v96, s80, v96\n" \
    "s_mov_b32 m0, s74\n" \
    "s_add_i32 s76, s74, 4096\n" \
    "buffer_load_dwordx4 v125, %[rs], s69 offen sc1 lds\n" \
    "s_mov_b32 m0, s76\n" \
    "v_mov_b64 v[80:81], 0\n" \
    "v_mov_b64 v[82:83], 0\n" \
    "buffer_load_dwordx4 v125, %[rs], s70 offen sc1 lds\n" \
    "s_movk_i32 m0, 0x2000\n" \
    "s_mov_b32 exec_lo, s27\n" \
    "s_mov_b32 exec_hi, s27\n" \
    "buffer_load_dwordx4 v96, %[rs], s71 offen sc1 lds\n" \
    "s_mov_b64 exec, -1\n" \
    "v_mov_b64 v[84:85], 0\n" \
    "v_mov_b64 v[86:87], 0\n" \
    "s_waitcnt vmcnt(0)\n" \
    "s_barrier\n" \
    "ds_read_b128 v[0:3], v37 offset:0\n" \
    "ds_read_b128 v[4:7], v38 offset:0\n" \
    "ds_read_b128 v[8:11], v39 offset:0\n" \
    "ds_read_b128 v[12:15], v132 offset:0\n" \
    "ds_read_b128 v[16:19], v37 offset:4096\n" \
    "ds_read_b128 v[20:23], v38 offset:4096\n" \
    "ds_read_b128 v[24:27], v39 offset:4096\n" \
    "ds_read_b128 v[28:31], v132 offset:4096\n" \
    "ds_read_b128 v[32:35], v126 offset:0\n" \
    "s_waitcnt lgkmcnt(8)\n" \
    "v_smfmac_f32_16x16x64_bf16 v[80:83], v[0:3], a[128:135], v124\n" \
    "s_waitcnt lgkmcnt(7)\n" \
    "v_smfmac_f32_16x16x64_bf16 v[80:83], v[4:7], a[144:151], v124\n" \
    "s_waitcnt lgkmcnt(6)\n" \
    "v_smfmac_f32_16x16x64_bf16 v[80:83], v[8:11], a[160:167], v124\n" \
    "s_waitcnt lgkmcnt(5)\n" \
    "v_smfmac_f32_16x16x64_bf16 v[80:83], v[12:15], a[176:183], v124\n" \
    "s_waitcnt lgkmcnt(4)\n" \
    "v_smfmac_f32_16x16x64_bf16 v[80:83], v[16:19], a[192:199], v124\n" \
    "s_waitcnt lgkmcnt(3)\n" \
    "v_smfmac_f32_16x16x64_bf16 v[80:83], v[20:23], a[208:215], v124\n" \
    "s_waitcnt lgkmcnt(2)\n" \
    "v_smfmac_f32_16x16x64_bf16 v[80:83], v[24:27], a[224:231], v124\n" \
    "s_waitcnt lgkmcnt(1)\n" \
    "v_smfmac_f32_16x16x64_bf16 v[80:83], v[28:31], a[240:247], v124\n" \
    "s_waitcnt lgkmcnt(0)\n" \
    "v_smfmac_f32_16x16x64_bf16 v[80:83], v[32:35], v[152:159], v124\n" \
    "v_smfmac_f32_16x16x64_bf16 v[84:87], v[0:3], a[136:143], v124\n" \
    "v_smfmac_f32_16x16x64_bf16 v[84:87], v[4:7], a[152:159], v124\n" \
    "v_smfmac_f32_16x16x64_bf16 v[84:87], v[8:11], a[168:175], v124\n" \
    "v_smfmac_f32_16x16x64_bf16 v[84:87], v[12:15], a[184:191], v124\n" \
    "v_smfmac_f32_16x16x64_bf16 v[84:87], v[16:19], a[200:207], v124\n" \
    "v_add_f32 v80, v80, v82\n" \
    "v_add_f32 v81, v81, v83\n" \
    "s_branch 32b\n" \
    "7:\n" \
    "s_mov_b32 s43, 2\n" \
    "s_nop 7\n" \
    "s_waitcnt vmcnt(0)\n" \
    "9:\n" \
    "s_mov_b32 m0, s68\n" \
    "s_add_i32 s37, s37, s29\n" \
    "s_waitcnt lgkmcnt(0)\n" \
    "v_mov_b32 v0, s36\n" \
    "v_mov_b32 v1, s37\n" \
    "v_mov_b32 v2, s38\n" \
    "v_mov_b32 v3, s39\n" \
    "v_mov_b32 v4, s40\n" \
    "v_mov_b32 v5, s41\n" \
    "v_mov_b32 v6, s42\n" \
    "v_mov_b32 v7, s43\n" \
    "v_mov_b32 v8, s28\n" \
    "v_mov_b32 v9, s29\n" \
    "v_mov_b32 v10, s30\n" \
    "v_mov_b32 v11, s31\n" \
    "v_mov_b32 v12, s32\n" \
    "v_mov_b32 v13, s33\n" \
    "v_mov_b32 v14, s34\n" \
    "v_mov_b32 v15, s35\n" \
    "v_mov_b32 v16, s24\n" \
    "v_mov_b32 v17, s25\n" \
    "v_mov_b32 v18, s26\n" \
    "v_mov_b32 v19, s27\n" \
    "v_mov_b32 v20, s84\n" \
    "v_mov_b32 v21, s86\n" \
    "v_mov_b32 v22, s88\n" \
    "v_mov_b32 v23, s84\n" \
    "v_mov_b32 v24, s60\n" \
    "v_mov_b32 v25, s61\n" \
    "v_mov_b32 v26, s62\n" \
    "v_mov_b32 v27, s63\n" \
    "v_mov_b32 v28, s64\n" \
    "v_mov_b32 v29, s65\n" \
    "v_mov_b32 v30, s66\n" \
    "v_mov_b32 v31, s67\n" \
    ".Lstep_end:\n" \
    ""
static_assert(2 * PART_STEP == 32768 && 2 * OFF_CAN == 1703936 && F_ERR == 2 * OFF_CAN + 65536 && MEMB_STRIDE == 32768 && OFF_VF - OFF_KF == 2048 * 4096 * 2 && OFF_BO == 65011712 && OFF_GT == 67518464 && BO_ROW == 4352, "constants of the step block");
#define STEP_CLOBBER "v32", "v33", "v34", "v35", "v36", "v37", "v38", "v39", "v40", "v41", "v42", "v43", "v44", "v45", "v46", "v47", "v48", "v49", "v50", "v51", "v52", "v53", "v54", "v55", "v56", "v57", "v58", "v59", "v60", "v61", "v62", "v63", "v64", "v65", "v66", "v67", "v68", "v69", "v70", "v71", "v72", "v73", "v74", "v75", "v76", "v77", "v78", "v79", "v80", "v81", "v82", "v83", "v84", "v85", "v86", "v87", "v88", "v89", "v90", "v91", "v92", "v93", "v94", "v95", "v98", "v99", "v100", "v101", "v102", "v103", "v104", "v105", "v106", "v107", "v108", "v109", "v110", "v111", "v112", "v113", "v114", "s24", "s25", "s26", "s27", "s28", "s29", "s30", "s31", "s32", "s33", "s34", "s35", "s36", "s37", "s38", "s39", "s40", "s41", "s42", "s43", "s44", "s45", "s46", "s47", "s48", "s49", "s50", "s51", "s52", "s53", "s54", "s55", "s56", "s57", "s58", "s59", "s60", "s61", "s62", "s63", "s64", "s65", "s66", "s67", "s68", "s69", "s70", "s71", "s72", "s73", "s74", "s75", "s76", "s77", "s78", "s79", "s80", "s81", "s82", "s83", "s84", "s85", "s86", "s87", "s88", "s89", "s90", "s91", "s92", "s93", "s94", "s95", "s96", "s97", "vcc", "scc", "memory"
#define OUT16_ASM \
    "s_mov_b32 exec_lo, %[tm]\n" \
    "s_mov_b32 exec_hi, 0\n" \
    "buffer_load_dword v79, %[tv], %[rw], %[so] offen sc1\n" \
    "s_mov_b64 exec, -1\n" \
    "s_add_i32 s44, %[so], 4096\n" \
    "s_add_i32 s45, %[so], 8192\n" \
    "s_add_i32 s46, %[so], 12288\n" \
    "buffer_load_dwordx4 v[16:19], %[vo], %[rw], %[so] offen offset:0 sc1\n" \
    "ds_read_b128 v[80:83], %[wl] offset:0\n" \
    "ds_read_b128 v[84:87], %[wl] offset:1024\n" \
    "s_nop 7\n" \
    "v_mov_b32 v104, 0\n" \
    "buffer_load_dwordx4 v[20:23], %[vo], %[rw], %[so] offen offset:256 sc1\n" \
    "s_nop 7\n" \
    "s_nop 3\n" \
    "buffer_load_dwordx4 v[24:27], %[vo], %[rw], %[so] offen offset:1024 sc1\n" \
    "s_nop 7\n" \
    "s_nop 3\n" \
    "buffer_load_dwordx4 v[28:31], %[vo], %[rw], %[so] offen offset:1280 sc1\n" \
    "s_nop 7\n" \
    "s_nop 3\n" \
    "buffer_load_dwordx4 v[32:35], %[vo], %[rw], %[so] offen offset:2048 sc1\n" \
    "s_nop 7\n" \
    "s_nop 3\n" \
    "buffer_load_dwordx4 v[36:39], %[vo], %[rw], %[so] offen offset:2304 sc1\n" \
    "s_nop 7\n" \
    "s_nop 3\n" \
    "buffer_load_dwordx4 v[40:43], %[vo], %[rw], %[so] offen offset:3072 sc1\n" \
    "s_nop 7\n" \
    "s_nop 3\n" \
    "buffer_load_dwordx4 v[44:47], %[vo], %[rw], %[so] offen offset:3328 sc1\n" \
    "s_nop 7\n" \
    "s_nop 3\n" \
    "buffer_load_dwordx4 v[48:51], %[vo], %[rw], s44 offen offset:0 sc1\n" \
    "s_nop 7\n" \
    "s_nop 3\n" \
    "buffer_load_dwordx4 v[52:55], %[vo], %[rw], s44 offen offset:256 sc1\n" \
    "s_nop 7\n" \
    "s_nop 3\n" \
    "buffer_load_dwordx4 v[56:59], %[vo], %[rw], s44 offen offset:1024 sc1\n" \
    "s_nop 7\n" \
    "s_nop 3\n" \
    "buffer_load_dwordx4 v[60:63], %[vo], %[rw], s44 offen offset:1280 sc1\n" \
    "ds_read_b128 v[88:91], %[wl] offset:2048\n" \
    "s_waitcnt vmcnt(11)\n" \
    "v_mfma_f32_16x16x32_bf16 v[0:3], a[0:3], v[16:19], 0\n" \
    "v_max3_u32 v104, v104, v16, v17\n" \
    "v_mfma_f32_16x16x32_bf16 v[4:7], a[128:131], v[16:19], 0\n" \
    "v_max3_u32 v104, v104, v18, v19\n" \
    "v_mfma_f32_16x16x32_bf16 v[8:11], v[128:131], v[16:19], 0\n" \
    "s_waitcnt lgkmcnt(2)\n" \
    "v_mfma_f32_16x16x32_bf16 v[12:15], v[80:83], v[16:19], 0\n" \
    "buffer_load_dwordx4 v[64:67], %[vo], %[rw], s44 offen offset:2048 sc1\n" \
    "ds_read_b128 v[80:83], %[wl] offset:3072\n" \
    "s_waitcnt vmcnt(11)\n" \
    "v_mfma_f32_16x16x32_bf16 v[0:3], a[4:7], v[20:23], v[0:3]\n" \
    "v_max3_u32 v104, v104, v20, v21\n" \
    "v_mfma_f32_16x16x32_bf16 v[4:7], a[132:135], v[20:23], v[4:7]\n" \
    "v_max3_u32 v104, v104, v22, v23\n" \
    "v_mfma_f32_16x16x32_bf16 v[8:11], v[132:135], v[20:23], v[8:11]\n" \
    "s_waitcnt lgkmcnt(2)\n" \
    "v_mfma_f32_16x16x32_bf16 v[12:15], v[84:87], v[20:23], v[12:15]\n" \
    "buffer_load_dwordx4 v[68:71], %[vo], %[rw], s44 offen offset:2304 sc1\n" \
    "ds_read_b128 v[84:87], %[wl] offset:4096\n" \
    "s_waitcnt vmcnt(11)\n" \
    "v_mfma_f32_16x16x32_bf16 v[0:3], a[8:11], v[24:27], v[0:3]\n" \
    "v_max3_u32 v104, v104, v24, v25\n" \
    "v_mfma_f32_16x16x32_bf16 v[4:7], a[136:139], v[24:27], v[4:7]\n" \
    "v_max3_u32 v104, v104, v26, v27\n" \
    "v_mfma_f32_16x16x32_bf16 v[8:11], v[136:139], v[24:27], v[8:11]\n" \
    "s_waitcnt lgkmcnt(2)\n" \
    "v_mfma_f32_16x16x32_bf16 v[12:15], v[88:91], v[24:27], v[12:15]\n" \
    "buffer_load_dwordx4 v[72:75], %[vo], %[rw], s44 offen offset:3072 sc1\n" \
    "ds_read_b128 v[88:91], %[wl] offset:5120\n" \
    "s_waitcnt vmcnt(11)\n" \
    "v_mfma_f32_16x16x32_bf16 v[0:3], a[12:15], v[28:31], v[0:3]\n" \
    "v_max3_u32 v104, v104, v28, v29\n" \
    "v_mfma_f32_16x16x32_bf16 v[4:7], a[140:143], v[28:31], v[4:7]\n" \
    "v_max3_u32 v104, v104, v30, v31\n" \
    "v_mfma_f32_16x16x32_bf16 v[8:11], v[140:143], v[28:31], v[8:11]\n" \
    "s_waitcnt lgkmcnt(2)\n" \
    "v_mfma_f32_16x16x32_bf16 v[12:15], v[80:83], v[28:31], v[12:15]\n" \
    "buffer_load_dwordx4 v[76:79], %[vo], %[rw], s44 offen offset:3328 sc1\n" \
    "ds_read_b128 v[80:83], %[wl] offset:6144\n" \
    "s_waitcnt vmcnt(11)\n" \
    "v_mfma_f32_16x16x32_bf16 v[0:3], a[16:19], v[32:35], v[0:3]\n" \
    "v_max3_u32 v104, v104, v32, v33\n" \
    "v_mfma_f32_16x16x32_bf16 v[4:7], a[144:147], v[32:35], v[4:7]\n" \
    "v_max3_u32 v104, v104, v34, v35\n" \
    "v_mfma_f32_16x16x32_bf16 v[8:11], v[144:147], v[32:35], v[8:11]\n" \
    "s_waitcnt lgkmcnt(2)\n" \
    "v_mfma_f32_16x16x32_bf16 v[12:15], v[84:87], v[32:35], v[12:15]\n" \
    "buffer_load_dwordx4 v[16:19], %[vo], %[rw], s45 offen offset:0 sc1\n" \
    "ds_read_b128 v[84:87], %[wl] offset:7168\n" \
    "s_waitcnt vmcnt(11)\n" \
    "v_mfma_f32_16x16x32_bf16 v[0:3], a[20:23], v[36:39], v[0:3]\n" \
    "v_max3_u32 v104, v104, v36, v37\n" \
    "v_mfma_f32_16x16x32_bf16 v[4:7], a[148:151], v[36:39], v[4:7]\n" \
    "v_max3_u32 v104, v104, v38, v39\n" \
    "v_mfma_f32_16x16x32_bf16 v[8:11], v[148:151], v[36:39], v[8:11]\n" \
    "s_waitcnt lgkmcnt(2)\n" \
    "v_mfma_f32_16x16x32_bf16 v[12:15], v[88:91], v[36:39], v[12:15]\n" \
    "buffer_load_dwordx4 v[20:23], %[vo], %[rw], s45 offen offset:256 sc1\n" \
    "ds_read_b128 v[88:91], %[wl] offset:8192\n" \
    "s_waitcnt vmcnt(11)\n" \
    "v_mfma_f32_16x16x32_bf16 v[0:3], a[24:27], v[40:43], v[0:3]\n" \
    "v_max3_u32 v104, v104, v40, v41\n" \
    "v_mfma_f32_16x16x32_bf16 v[4:7], a[152:155], v[40:43], v[4:7]\n" \
    "v_max3_u32 v104, v104, v42, v43\n" \
    "v_mfma_f32_16x16x32_bf16 v[8:11], v[152:155], v[40:43], v[8:11]\n" \
    "s_waitcnt lgkmcnt(2)\n" \
    "v_mfma_f32_16x16x32_bf16 v[12:15], v[80:83], v[40:43], v[12:15]\n" \
    "buffer_load_dwordx4 v[24:27], %[vo], %[rw], s45 offen offset:1024 sc1\n" \
    "ds_read_b128 v[80:83], %[wl] offset:9216\n" \
    "s_waitcnt vmcnt(11)\n" \
    "v_mfma_f32_16x16x32_bf16 v[0:3], a[28:31], v[44:47], v[0:3]\n" \
    "v_max3_u32 v104, v104, v44, v45\n" \
    "v_mfma_f32_16x16x32_bf16 v[4:7], a[156:159], v[44:47], v[4:7]\n" \
    "v_max3_u32 v104, v104, v46, v47\n" \
    "v_mfma_f32_16x16x32_bf16 v[8:11], v[156:159], v[44:47], v[8:11]\n" \
    "s_waitcnt lgkmcnt(2)\n" \
    "v_mfma_f32_16x16x32_bf16 v[12:15], v[84:87], v[44:47], v[12:15]\n" \
    "buffer_load_dwordx4 v[28:31], %[vo], %[rw], s45 offen offset:1280 sc1\n" \
    "ds_read_b128 v[84:87], %[wl] offset:10240\n" \
    "s_waitcnt vmcnt(11)\n" \
    "v_mfma_f32_16x16x32_bf16 v[0:3], a[32:35], v[48:51], v[0:3]\n" \
    "v_max3_u32 v104, v104, v48, v49\n" \
    "v_mfma_f32_16x16x32_bf16 v[4:7], a[160:163], v[48:51], v[4:7]\n" \
    "v_max3_u32 v104, v104, v50, v51\n" \
    "v_mfma_f32_16x16x32_bf16 v[8:11], v[160:163], v[48:51], v[8:11]\n" \
    "s_waitcnt lgkmcnt(2)\n" \
    "v_mfma_f32_16x16x32_bf16 v[12:15], v[88:91], v[48:51], v[12:15]\n" \
    "buffer_load_dwordx4 v[32:35], %[vo], %[rw], s45 offen offset:2048 sc1\n" \
    "ds_read_b128 v[88:91], %[wl] offset:11264\n" \
    "s_waitcnt vmcnt(11)\n" \
    "v_mfma_f32_16x16x32_bf16 v[0:3], a[36:39], v[52:55], v[0:3]\n" \
    "v_max3_u32 v104, v104, v52, v53\n" \
    "v_mfma_f32_16x16x32_bf16 v[4:7], a[164:167], v[52:55], v[4:7]\n" \
    "v_max3_u32 v104, v104, v54, v55\n" \
    "v_mfma_f32_16x16x32_bf16 v[8:11], v[164:167], v[52:55], v[8:11]\n" \
    "s_waitcnt lgkmcnt(2)\n" \
    "v_mfma_f32_16x16x32_bf16 v[12:15], v[80:83], v[52:55], v[12:15]\n" \
    "buffer_load_dwordx4 v[36:39], %[vo], %[rw], s45 offen offset:2304 sc1\n" \
    "ds_read_b128 v[80:83], %[wl] offset:12288\n" \
    "s_waitcnt vmcnt(11)\n" \
    "v_mfma_f32_16x16x32_bf16 v[0:3], a[40:43], v[56:59], v[0:3]\n" \
    "v_max3_u32 v104, v104, v56, v57\n" \
    "v_mfma_f32_16x16x32_bf16 v[4:7], a[168:171], v[56:59], v[4:7]\n" \
    "v_max3_u32 v104, v104, v58, v59\n" \
    "v_mfma_f32_16x16x32_bf16 v[8:11], v[168:171], v[56:59], v[8:11]\n" \
    "s_waitcnt lgkmcnt(2)\n" \
    "v_mfma_f32_16x16x32_bf16 v[12:15], v[84:87], v[56:59], v[12:15]\n" \
    "buffer_load_dwordx4 v[40:43], %[vo], %[rw], s45 offen offset:3072 sc1\n" \
    "ds_read_b128 v[84:87], %[wl] offset:13312\n" \
    "s_waitcnt vmcnt(11)\n" \
    "v_mfma_f32_16x16x32_bf16 v[0:3], a[44:47], v[60:63], v[0:3]\n" \
    "v_max3_u32 v104, v104, v60, v61\n" \
    "v_mfma_f32_16x16x32_bf16 v[4:7], a[172:175], v[60:63], v[4:7]\n" \
    "v_max3_u32 v104, v104, v62, v63\n" \
    "v_mfma_f32_16x16x32_bf16 v[8:11], v[172:175], v[60:63], v[8:11]\n" \
    "s_waitcnt lgkmcnt(2)\n" \
    "v_mfma_f32_16x16x32_bf16 v[12:15], v[88:91], v[60:63], v[12:15]\n" \
    "buffer_load_dwordx4 v[44:47], %[vo], %[rw], s45 offen offset:3328 sc1\n" \
    "ds_read_b128 v[88:91], %[wl] offset:14336\n" \
    "s_waitcnt vmcnt(11)\n" \
    "v_mfma_f32_16x16x32_bf16 v[0:3], a[48:51], v[64:67], v[0:3]\n" \
    "v_max3_u32 v104, v104, v64, v65\n" \
    "v_mfma_f32_16x16x32_bf16 v[4:7], a[176:179], v[64:67], v[4:7]\n" \
    "v_max3_u32 v104, v104, v66, v67\n" \
    "v_mfma_f32_16x16x32_bf16 v[8:11], v[176:179], v[64:67], v[8:11]\n" \
    "s_waitcnt lgkmcnt(2)\n" \
    "v_mfma_f32_16x16x32_bf16 v[12:15], v[80:83], v[64:67], v[12:15]\n" \
    "buffer_load_dwordx4 v[48:51], %[vo], %[rw], s46 offen offset:0 sc1\n" \
    "ds_read_b128 v[80:83], %[wl] offset:15360\n" \
    "s_waitcnt vmcnt(11)\n" \
    "v_mfma_f32_16x16x32_bf16 v[0:3], a[52:55], v[68:71], v[0:3]\n" \
    "v_max3_u32 v104, v104, v68, v69\n" \
    "v_mfma_f32_16x16x32_bf16 v[4:7], a[180:183], v[68:71], v[4:7]\n" \
    "v_max3_u32 v104, v104, v70, v71\n" \
    "v_mfma_f32_16x16x32_bf16 v[8:11], v[180:183], v[68:71], v[8:11]\n" \
    "s_waitcnt lgkmcnt(2)\n" \
    "v_mfma_f32_16x16x32_bf16 v[12:15], v[84:87], v[68:71], v[12:15]\n" \
    "buffer_load_dwordx4 v[52:55], %[vo], %[rw], s46 offen offset:256 sc1\n" \
    "ds_read_b128 v[84:87], %[wl] offset:16384\n" \
    "s_waitcnt vmcnt(11)\n" \
    "v_mfma_f32_16x16x32_bf16 v[0:3], a[56:59], v[72:75], v[0:3]\n" \
    "v_max3_u32 v104, v104, v72, v73\n" \
    "v_mfma_f32_16x16x32_bf16 v[4:7], a[184:187], v[72:75], v[4:7]\n" \
    "v_max3_u32 v104, v104, v74, v75\n" \
    "v_mfma_f32_16x16x32_bf16 v[8:11], v[184:187], v[72:75], v[8:11]\n" \
    "s_waitcnt lgkmcnt(2)\n" \
    "v_mfma_f32_16x16x32_bf16 v[12:15], v[88:91], v[72:75], v[12:15]\n" \
    "buffer_load_dwordx4 v[56:59], %[vo], %[rw], s46 offen offset:1024 sc1\n" \
    "ds_read_b128 v[88:91], %[wl] offset:17408\n" \
    "s_waitcnt vmcnt(11)\n" \
    "v_mfma_f32_16x16x32_bf16 v[0:3], a[60:63], v[76:79], v[0:3]\n" \
    "v_max3_u32 v104, v104, v76, v77\n" \
    "v_mfma_f32_16x16x32_bf16 v[4:7], a[188:191], v[76:79], v[4:7]\n" \
    "v_max3_u32 v104, v104, v78, v79\n" \
    "v_mfma_f32_16x16x32_bf16 v[8:11], v[188:191], v[76:79], v[8:11]\n" \
    "s_waitcnt lgkmcnt(2)\n" \
    "v_mfma_f32_16x16x32_bf16 v[12:15], v[80:83], v[76:79], v[12:15]\n" \
    "buffer_load_dwordx4 v[60:63], %[vo], %[rw], s46 offen offset:1280 sc1\n" \
    "ds_read_b128 v[80:83], %[wl] offset:18432\n" \
    "s_waitcnt vmcnt(11)\n" \
    "v_mfma_f32_16x16x32_bf16 v[0:3], a[64:67], v[16:19], v[0:3]\n" \
    "v_max3_u32 v104, v104, v16, v17\n" \
    "v_mfma_f32_16x16x32_bf16 v[4:7], a[192:195], v[16:19], v[4:7]\n" \
    "v_max3_u32 v104, v104, v18, v19\n" \
    "v_mfma_f32_16x16x32_bf16 v[8:11], v[192:195], v[16:19], v[8:11]\n" \
    "s_waitcnt lgkmcnt(2)\n" \
    "v_mfma_f32_16x16x32_bf16 v[12:15], v[84:87], v[16:19], v[12:15]\n" \
    "buffer_load_dwordx4 v[64:67], %[vo], %[rw], s46 offen offset:2048 sc1\n" \
    "ds_read_b128 v[84:87], %[wl] offset:19456\n" \
    "s_waitcnt vmcnt(11)\n" \
    "v_mfma_f32_16x16x32_bf16 v[0:3], a[68:71], v[20:23], v[0:3]\n" \
    "v_max3_u32 v104, v104, v20, v21\n" \
    "v_mfma_f32_16x16x32_bf16 v[4:7], a[196:199], v[20:23], v[4:7]\n" \
    "v_max3_u32 v104, v104, v22, v23\n" \
    "v_mfma_f32_16x16x32_bf16 v[8:11], v[196:199], v[20:23], v[8:11]\n" \
    "s_waitcnt lgkmcnt(2)\n" \
    "v_mfma_f32_16x16x32_bf16 v[12:15], v[88:91], v[20:23], v[12:15]\n" \
    "buffer_load_dwordx4 v[68:71], %[vo], %[rw], s46 offen offset:2304 sc1\n" \
    "ds_read_b128 v[88:91], %[wl] offset:20480\n" \
    "s_waitcnt vmcnt(11)\n" \
    "v_mfma_f32_16x16x32_bf16 v[0:3], a[72:75], v[24:27], v[0:3]\n" \
    "v_max3_u32 v104, v104, v24, v25\n" \
    "v_mfma_f32_16x16x32_bf16 v[4:7], a[200:203], v[24:27], v[4:7]\n" \
    "v_max3_u32 v104, v104, v26, v27\n" \
    "v_mfma_f32_16x16x32_bf16 v[8:11], v[200:203], v[24:27], v[8:11]\n" \
    "s_waitcnt lgkmcnt(2)\n" \
    "v_mfma_f32_16x16x32_bf16 v[12:15], v[80:83], v[24:27], v[12:15]\n" \
    "buffer_load_dwordx4 v[72:75], %[vo], %[rw], s46 offen offset:3072 sc1\n" \
    "ds_read_b128 v[80:83], %[wl] offset:21504\n" \
    "s_waitcnt vmcnt(11)\n" \
    "v_mfma_f32_16x16x32_bf16 v[0:3], a[76:79], v[28:31], v[0:3]\n" \
    "v_max3_u32 v104, v104, v28, v29\n" \
    "v_mfma_f32_16x16x32_bf16 v[4:7], a[204:207], v[28:31], v[4:7]\n" \
    "v_max3_u32 v104, v104, v30, v31\n" \
    "v_mfma_f32_16x16x32_bf16 v[8:11], v[204:207], v[28:31], v[8:11]\n" \
    "s_waitcnt lgkmcnt(2)\n" \
    "v_mfma_f32_16x16x32_bf16 v[12:15], v[84:87], v[28:31], v[12:15]\n" \
    "buffer_load_dwordx4 v[76:79], %[vo], %[rw], s46 offen offset:3328 sc1\n" \
    "ds_read_b128 v[84:87], %[wl] offset:22528\n" \
    "s_waitcnt vmcnt(11)\n" \
    "v_mfma_f32_16x16x32_bf16 v[0:3], a[80:83], v[32:35], v[0:3]\n" \
    "v_max3_u32 v104, v104, v32, v33\n" \
    "v_mfma_f32_16x16x32_bf16 v[4:7], a[208:211], v[32:35], v[4:7]\n" \
    "v_max3_u32 v104, v104, v34, v35\n" \
    "v_mfma_f32_16x16x32_bf16 v[8:11], v[208:211], v[32:35], v[8:11]\n" \
    "s_waitcnt lgkmcnt(2)\n" \
    "v_mfma_f32_16x16x32_bf16 v[12:15], v[88:91], v[32:35], v[12:15]\n" \
    "buffer_load_dwordx2 v[106:107], %[bv], %[rw], %[bs] offen sc1\n" \
    "ds_read_b128 v[88:91], %[wl] offset:23552\n" \
    "s_waitcnt vmcnt(11)\n" \
    "v_mfma_f32_16x16x32_bf16 v[0:3], a[84:87], v[36:39], v[0:3]\n" \
    "v_max3_u32 v104, v104, v36, v37\n" \
    "v_mfma_f32_16x16x32_bf16 v[4:7], a[212:215], v[36:39], v[4:7]\n" \
    "v_max3_u32 v104, v104, v38, v39\n" \
    "v_mfma_f32_16x16x32_bf16 v[8:11], v[212:215], v[36:39], v[8:11]\n" \
    "s_waitcnt lgkmcnt(2)\n" \
    "v_mfma_f32_16x16x32_bf16 v[12:15], v[80:83], v[36:39], v[12:15]\n" \
    "buffer_load_dword v108, %[gv], %[rw], %[gs] offen sc1\n" \
    "ds_read_b128 v[80:83], %[wl] offset:24576\n" \
    "s_waitcnt vmcnt(11)\n" \
    "v_mfma_f32_16x16x32_bf16 v[0:3], a[88:91], v[40:43], v[0:3]\n" \
    "v_max3_u32 v104, v104, v40, v41\n" \
    "v_mfma_f32_16x16x32_bf16 v[4:7], a[216:219], v[40:43], v[4:7]\n" \
    "v_max3_u32 v104, v104, v42, v43\n" \
    "v_mfma_f32_16x16x32_bf16 v[8:11], v[216:219], v[40:43], v[8:11]\n" \
    "s_waitcnt lgkmcnt(2)\n" \
    "v_mfma_f32_16x16x32_bf16 v[12:15], v[84:87], v[40:43], v[12:15]\n" \
    "buffer_load_dword v105, %[sv], %[rw], %[ss] offen sc1\n" \
    "ds_read_b128 v[84:87], %[wl] offset:25600\n" \
    "s_waitcnt vmcnt(11)\n" \
    "v_mfma_f32_16x16x32_bf16 v[0:3], a[92:95], v[44:47], v[0:3]\n" \
    "v_max3_u32 v104, v104, v44, v45\n" \
    "v_mfma_f32_16x16x32_bf16 v[4:7], a[220:223], v[44:47], v[4:7]\n" \
    "v_max3_u32 v104, v104, v46, v47\n" \
    "v_mfma_f32_16x16x32_bf16 v[8:11], v[220:223], v[44:47], v[8:11]\n" \
    "s_waitcnt lgkmcnt(2)\n" \
    "v_mfma_f32_16x16x32_bf16 v[12:15], v[88:91], v[44:47], v[12:15]\n" \
    "ds_read_b128 v[88:91], %[wl] offset:26624\n" \
    "s_waitcnt vmcnt(10)\n" \
    "v_mfma_f32_16x16x32_bf16 v[0:3], a[96:99], v[48:51], v[0:3]\n" \
    "v_max3_u32 v104, v104, v48, v49\n" \
    "v_mfma_f32_16x16x32_bf16 v[4:7], a[224:227], v[48:51], v[4:7]\n" \
    "v_max3_u32 v104, v104, v50, v51\n" \
    "v_mfma_f32_16x16x32_bf16 v[8:11], v[224:227], v[48:51], v[8:11]\n" \
    "s_waitcnt lgkmcnt(2)\n" \
    "v_mfma_f32_16x16x32_bf16 v[12:15], v[80:83], v[48:51], v[12:15]\n" \
    "ds_read_b128 v[80:83], %[wl] offset:27648\n" \
    "s_waitcnt vmcnt(9)\n" \
    "v_mfma_f32_16x16x32_bf16 v[0:3], a[100:103], v[52:55], v[0:3]\n" \
    "v_max3_u32 v104, v104, v52, v53\n" \
    "v_mfma_f32_16x16x32_bf16 v[4:7], a[228:231], v[52:55], v[4:7]\n" \
    "v_max3_u32 v104, v104, v54, v55\n" \
    "v_mfma_f32_16x16x32_bf16 v[8:11], v[228:231], v[52:55], v[8:11]\n" \
    "s_waitcnt lgkmcnt(2)\n" \
    "v_mfma_f32_16x16x32_bf16 v[12:15], v[84:87], v[52:55], v[12:15]\n" \
    "ds_read_b128 v[84:87], %[wl] offset:28672\n" \
    "s_waitcnt vmcnt(8)\n" \
    "v_mfma_f32_16x16x32_bf16 v[0:3], a[104:107], v[56:59], v[0:3]\n" \
    "v_max3_u32 v104, v104, v56, v57\n" \
    "v_mfma_f32_16x16x32_bf16 v[4:7], a[232:235], v[56:59], v[4:7]\n" \
    "v_max3_u32 v104, v104, v58, v59\n" \
    "v_mfma_f32_16x16x32_bf16 v[8:11], v[232:235], v[56:59], v[8:11]\n" \
    "s_waitcnt lgkmcnt(2)\n" \
    "v_mfma_f32_16x16x32_bf16 v[12:15], v[88:91], v[56:59], v[12:15]\n" \
    "ds_read_b128 v[88:91], %[wl] offset:29696\n" \
    "s_waitcnt vmcnt(7)\n" \
    "v_mfma_f32_16x16x32_bf16 v[0:3], a[108:111], v[60:63], v[0:3]\n" \
    "v_max3_u32 v104, v104, v60, v61\n" \
    "v_mfma_f32_16x16x32_bf16 v[4:7], a[236:239], v[60:63], v[4:7]\n" \
    "v_max3_u32 v104, v104, v62, v63\n" \
    "v_mfma_f32_16x16x32_bf16 v[8:11], v[236:239], v[60:63], v[8:11]\n" \
    "s_waitcnt lgkmcnt(2)\n" \
    "v_mfma_f32_16x16x32_bf16 v[12:15], v[80:83], v[60:63], v[12:15]\n" \
    "ds_read_b128 v[80:83], %[wl] offset:30720\n" \
    "s_waitcnt vmcnt(6)\n" \
    "v_mfma_f32_16x16x32_bf16 v[0:3], a[112:115], v[64:67], v[0:3]\n" \
    "v_max3_u32 v104, v104, v64, v65\n" \
    "v_mfma_f32_16x16x32_bf16 v[4:7], a[240:243], v[64:67], v[4:7]\n" \
    "v_max3_u32 v104, v104, v66, v67\n" \
    "v_mfma_f32_16x16x32_bf16 v[8:11], v[240:243], v[64:67], v[8:11]\n" \
    "s_waitcnt lgkmcnt(2)\n" \
    "v_mfma_f32_16x16x32_bf16 v[12:15], v[84:87], v[64:67], v[12:15]\n" \
    "ds_read_b128 v[84:87], %[wl] offset:31744\n" \
    "s_waitcnt vmcnt(5)\n" \
    "v_mfma_f32_16x16x32_bf16 v[0:3], a[116:119], v[68:71], v[0:3]\n" \
    "v_max3_u32 v104, v104, v68, v69\n" \
    "v_mfma_f32_16x16x32_bf16 v[4:7], a[244:247], v[68:71], v[4:7]\n" \
    "v_max3_u32 v104, v104, v70, v71\n" \
    "v_mfma_f32_16x16x32_bf16 v[8:11], v[244:247], v[68:71], v[8:11]\n" \
    "s_waitcnt lgkmcnt(2)\n" \
    "v_mfma_f32_16x16x32_bf16 v[12:15], v[88:91], v[68:71], v[12:15]\n" \
    "s_waitcnt vmcnt(4)\n" \
    "v_mfma_f32_16x16x32_bf16 v[0:3], a[120:123], v[72:75], v[0:3]\n" \
    "v_max3_u32 v104, v104, v72, v73\n" \
    "v_mfma_f32_16x16x32_bf16 v[4:7], a[248:251], v[72:75], v[4:7]\n" \
    "v_max3_u32 v104, v104, v74, v75\n" \
    "v_mfma_f32_16x16x32_bf16 v[8:11], v[248:251], v[72:75], v[8:11]\n" \
    "s_waitcnt lgkmcnt(1)\n" \
    "v_mfma_f32_16x16x32_bf16 v[12:15], v[80:83], v[72:75], v[12:15]\n" \
    "s_waitcnt vmcnt(3)\n" \
    "v_mfma_f32_16x16x32_bf16 v[0:3], a[124:127], v[76:79], v[0:3]\n" \
    "v_max3_u32 v104, v104, v76, v77\n" \
    "v_mfma_f32_16x16x32_bf16 v[4:7], a[252:255], v[76:79], v[4:7]\n" \
    "v_max3_u32 v104, v104, v78, v79\n" \
    "v_mfma_f32_16x16x32_bf16 v[8:11], v[252:255], v[76:79], v[8:11]\n" \
    "s_waitcnt lgkmcnt(0)\n" \
    "v_mfma_f32_16x16x32_bf16 v[12:15], v[84:87], v[76:79], v[12:15]\n" \
    "s_waitcnt vmcnt(0)\n" \
    "s_nop 7\n" \
    ""
#define OUT8_ASM \
    "s_mov_b32 exec_lo, %[tm]\n" \
    "s_mov_b32 exec_hi, 0\n" \
    "buffer_load_dword v79, %[tv], %[rw], %[so] offen sc1\n" \
    "s_mov_b64 exec, -1\n" \
    "s_add_i32 s44, %[so], 4096\n" \
    "s_add_i32 s45, %[so], 8192\n" \
    "s_add_i32 s46, %[so], 12288\n" \
    "buffer_load_dwordx4 v[16:19], %[vo], %[rw], %[so] offen offset:0 sc1\n" \
    "ds_read_b128 v[80:83], %[wl] offset:0\n" \
    "ds_read_b128 v[84:87], %[wl] offset:1024\n" \
    "ds_read_b128 v[88:91], %[wl] offset:2048\n" \
    "ds_read_b128 v[92:95], %[wl] offset:3072\n" \
    "v_mov_b64 v[0:1], 0\n" \
    "v_mov_b64 v[2:3], 0\n" \
    "v_mov_b64 v[4:5], 0\n" \
    "v_mov_b64 v[6:7], 0\n" \
    "v_mov_b64 v[8:9], 0\n" \
    "v_mov_b64 v[10:11], 0\n" \
    "v_mov_b64 v[12:13], 0\n" \
    "v_mov_b64 v[14:15], 0\n" \
    "v_mov_b32 v104, 0\n" \
    "buffer_load_dwordx4 v[20:23], %[vo], %[rw], %[so] offen offset:1024 sc1\n" \
    "s_nop 7\n" \
    "s_nop 3\n" \
    "buffer_load_dwordx4 v[24:27], %[vo], %[rw], %[so] offen offset:2048 sc1\n" \
    "s_nop 7\n" \
    "s_nop 3\n" \
    "buffer_load_dwordx4 v[28:31], %[vo], %[rw], %[so] offen offset:3072 sc1\n" \
    "s_nop 7\n" \
    "s_nop 3\n" \
    "buffer_load_dwordx4 v[32:35], %[vo], %[rw], s44 offen offset:0 sc1\n" \
    "s_nop 7\n" \
    "s_nop 3\n" \
    "buffer_load_dwordx4 v[36:39], %[vo], %[rw], s44 offen offset:1024 sc1\n" \
    "s_nop 7\n" \
    "s_nop 3\n" \
    "buffer_load_dwordx4 v[40:43], %[vo], %[rw], s44 offen offset:2048 sc1\n" \
    "s_nop 7\n" \
    "s_nop 3\n" \
    "buffer_load_dwordx4 v[44:47], %[vo], %[rw], s44 offen offset:3072 sc1\n" \
    "s_nop 7\n" \
    "s_nop 3\n" \
    "buffer_load_dwordx4 v[48:51], %[vo], %[rw], s45 offen offset:0 sc1\n" \
    "s_nop 7\n" \
    "s_nop 3\n" \
    "buffer_load_dwordx4 v[52:55], %[vo], %[rw], s45 offen offset:1024 sc1\n" \
    "ds_read_b128 v[96:99], %[wl] offset:4096\n" \
    "ds_read_b128 v[100:103], %[wl] offset:5120\n" \
    "s_waitcnt vmcnt(9)\n" \
    "v_smfmac_f32_16x16x64_bf16 v[0:3], v[16:19], a[0:7], %[idx]\n" \
    "v_max3_u32 v104, v104, v16, v17\n" \
    "v_smfmac_f32_16x16x64_bf16 v[4:7], v[16:19], a[128:135], %[idx]\n" \
    "v_max3_u32 v104, v104, v18, v19\n" \
    "v_smfmac_f32_16x16x64_bf16 v[8:11], v[16:19], v[128:135], %[idx]\n" \
    "s_waitcnt lgkmcnt(4)\n" \
    "v_smfmac_f32_16x16x64_bf16 v[12:15], v[16:19], v[80:87], %[idx]\n" \
    "buffer_load_dwordx4 v[56:59], %[vo], %[rw], s45 offen offset:2048 sc1\n" \
    "ds_read_b128 v[80:83], %[wl] offset:6144\n" \
    "ds_read_b128 v[84:87], %[wl] offset:7168\n" \
    "s_waitcnt vmcnt(9)\n" \
    "v_smfmac_f32_16x16x64_bf16 v[0:3], v[20:23], a[8:15], %[idx]\n" \
    "v_max3_u32 v104, v104, v20, v21\n" \
    "v_smfmac_f32_16x16x64_bf16 v[4:7], v[20:23], a[136:143], %[idx]\n" \
    "v_max3_u32 v104, v104, v22, v23\n" \
    "v_smfmac_f32_16x16x64_bf16 v[8:11], v[20:23], v[136:143], %[idx]\n" \
    "s_waitcnt lgkmcnt(4)\n" \
    "v_smfmac_f32_16x16x64_bf16 v[12:15], v[20:23], v[88:95], %[idx]\n" \
    "buffer_load_dwordx4 v[60:63], %[vo], %[rw], s45 offen offset:3072 sc1\n" \
    "ds_read_b128 v[88:91], %[wl] offset:8192\n" \
    "ds_read_b128 v[92:95], %[wl] offset:9216\n" \
    "s_waitcnt vmcnt(9)\n" \
    "v_smfmac_f32_16x16x64_bf16 v[0:3], v[24:27], a[16:23], %[idx]\n" \
    "v_max3_u32 v104, v104, v24, v25\n" \
    "v_smfmac_f32_16x16x64_bf16 v[4:7], v[24:27], a[144:151], %[idx]\n" \
    "v_max3_u32 v104, v104, v26, v27\n" \
    "v_smfmac_f32_16x16x64_bf16 v[8:11], v[24:27], v[144:151], %[idx]\n" \
    "s_waitcnt lgkmcnt(4)\n" \
    "v_smfmac_f32_16x16x64_bf16 v[12:15], v[24:27], v[96:103], %[idx]\n" \
    "buffer_load_dwordx4 v[64:67], %[vo], %[rw], s46 offen offset:0 sc1\n" \
    "ds_read_b128 v[96:99], %[wl] offset:10240\n" \
    "ds_read_b128 v[100:103], %[wl] offset:11264\n" \
    "s_waitcnt vmcnt(9)\n" \
    "v_smfmac_f32_16x16x64_bf16 v[0:3], v[28:31], a[24:31], %[idx]\n" \
    "v_max3_u32 v104, v104, v28, v29\n" \
    "v_smfmac_f32_16x16x64_bf16 v[4:7], v[28:31], a[152:159], %[idx]\n" \
    "v_max3_u32 v104, v104, v30, v31\n" \
    "v_smfmac_f32_16x16x64_bf16 v[8:11], v[28:31], v[152:159], %[idx]\n" \
    "s_waitcnt lgkmcnt(4)\n" \
    "v_smfmac_f32_16x16x64_bf16 v[12:15], v[28:31], v[80:87], %[idx]\n" \
    "buffer_load_dwordx4 v[68:71], %[vo], %[rw], s46 offen offset:1024 sc1\n" \
    "ds_read_b128 v[80:83], %[wl] offset:12288\n" \
    "ds_read_b128 v[84:87], %[wl] offset:13312\n" \
    "s_waitcnt vmcnt(9)\n" \
    "v_smfmac_f32_16x16x64_bf16 v[0:3], v[32:35], a[32:39], %[idx]\n" \
    "v_max3_u32 v104, v104, v32, v33\n" \
    "v_smfmac_f32_16x16x64_bf16 v[4:7], v[32:35], a[160:167], %[idx]\n" \
    "v_max3_u32 v104, v104, v34, v35\n" \
    "v_smfmac_f32_16x16x64_bf16 v[8:11], v[32:35], v[160:167], %[idx]\n" \
    "s_waitcnt lgkmcnt(4)\n" \
    "v_smfmac_f32_16x16x64_bf16 v[12:15], v[32:35], v[88:95], %[idx]\n" \
    "buffer_load_dwordx4 v[72:75], %[vo], %[rw], s46 offen offset:2048 sc1\n" \
    "ds_read_b128 v[88:91], %[wl] offset:14336\n" \
    "ds_read_b128 v[92:95], %[wl] offset:15360\n" \
    "s_waitcnt vmcnt(9)\n" \
    "v_smfmac_f32_16x16x64_bf16 v[0:3], v[36:39], a[40:47], %[idx]\n" \
    "v_max3_u32 v104, v104, v36, v37\n" \
    "v_smfmac_f32_16x16x64_bf16 v[4:7], v[36:39], a[168:175], %[idx]\n" \
    "v_max3_u32 v104, v104, v38, v39\n" \
    "v_smfmac_f32_16x16x64_bf16 v[8:11], v[36:39], v[168:175], %[idx]\n" \
    "s_waitcnt lgkmcnt(4)\n" \
    "v_smfmac_f32_16x16x64_bf16 v[12:15], v[36:39], v[96:103], %[idx]\n" \
    "buffer_load_dwordx4 v[76:79], %[vo], %[rw], s46 offen offset:3072 sc1\n" \
    "ds_read_b128 v[96:99], %[wl] offset:16384\n" \
    "ds_read_b128 v[100:103], %[wl] offset:17408\n" \
    "s_waitcnt vmcnt(9)\n" \
    "v_smfmac_f32_16x16x64_bf16 v[0:3], v[40:43], a[48:55], %[idx]\n" \
    "v_max3_u32 v104, v104, v40, v41\n" \
    "v_smfmac_f32_16x16x64_bf16 v[4:7], v[40:43], a[176:183], %[idx]\n" \
    "v_max3_u32 v104, v104, v42, v43\n" \
    "v_smfmac_f32_16x16x64_bf16 v[8:11], v[40:43], v[176:183], %[idx]\n" \
    "s_waitcnt lgkmcnt(4)\n" \
    "v_smfmac_f32_16x16x64_bf16 v[12:15], v[40:43], v[80:87], %[idx]\n" \
    "buffer_load_dwordx2 v[106:107], %[bv], %[rw], %[bs] offen sc1\n" \
    "ds_read_b128 v[80:83], %[wl] offset:18432\n" \
    "ds_read_b128 v[84:87], %[wl] offset:19456\n" \
    "s_waitcnt vmcnt(9)\n" \
    "v_smfmac_f32_16x16x64_bf16 v[0:3], v[44:47], a[56:63], %[idx]\n" \
    "v_max3_u32 v104, v104, v44, v45\n" \
    "v_smfmac_f32_16x16x64_bf16 v[4:7], v[44:47], a[184:191], %[idx]\n" \
    "v_max3_u32 v104, v104, v46, v47\n" \
    "v_smfmac_f32_16x16x64_bf16 v[8:11], v[44:47], v[184:191], %[idx]\n" \
    "s_waitcnt lgkmcnt(4)\n" \
    "v_smfmac_f32_16x16x64_bf16 v[12:15], v[44:47], v[88:95], %[idx]\n" \
    "buffer_load_dword v108, %[gv], %[rw], %[gs] offen sc1\n" \
    "ds_read_b128 v[88:91], %[wl] offset:20480\n" \
    "ds_read_b128 v[92:95], %[wl] offset:21504\n" \
    "s_waitcnt vmcnt(9)\n" \
    "v_smfmac_f32_16x16x64_bf16 v[0:3], v[48:51], a[64:71], %[idx]\n" \
    "v_max3_u32 v104, v104, v48, v49\n" \
    "v_smfmac_f32_16x16x64_bf16 v[4:7], v[48:51], a[192:199], %[idx]\n" \
    "v_max3_u32 v104, v104, v50, v51\n" \
    "v_smfmac_f32_16x16x64_bf16 v[8:11], v[48:51], v[192:199], %[idx]\n" \
    "s_waitcnt lgkmcnt(4)\n" \
    "v_smfmac_f32_16x16x64_bf16 v[12:15], v[48:51], v[96:103], %[idx]\n" \
    "buffer_load_dword v105, %[sv], %[rw], %[ss] offen sc1\n" \
    "ds_read_b128 v[96:99], %[wl] offset:22528\n" \
    "ds_read_b128 v[100:103], %[wl] offset:23552\n" \
    "s_waitcnt vmcnt(9)\n" \
    "v_smfmac_f32_16x16x64_bf16 v[0:3], v[52:55], a[72:79], %[idx]\n" \
    "v_max3_u32 v104, v104, v52, v53\n" \
    "v_smfmac_f32_16x16x64_bf16 v[4:7], v[52:55], a[200:207], %[idx]\n" \
    "v_max3_u32 v104, v104, v54, v55\n" \
    "v_smfmac_f32_16x16x64_bf16 v[8:11], v[52:55], v[200:207], %[idx]\n" \
    "s_waitcnt lgkmcnt(4)\n" \
    "v_smfmac_f32_16x16x64_bf16 v[12:15], v[52:55], v[80:87], %[idx]\n" \
    "ds_read_b128 v[80:83], %[wl] offset:24576\n" \
    "ds_read_b128 v[84:87], %[wl] offset:25600\n" \
    "s_waitcnt vmcnt(8)\n" \
    "v_smfmac_f32_16x16x64_bf16 v[0:3], v[56:59], a[80:87], %[idx]\n" \
    "v_max3_u32 v104, v104, v56, v57\n" \
    "v_smfmac_f32_16x16x64_bf16 v[4:7], v[56:59], a[208:215], %[idx]\n" \
    "v_max3_u32 v104, v104, v58, v59\n" \
    "v_smfmac_f32_16x16x64_bf16 v[8:11], v[56:59], v[208:215], %[idx]\n" \
    "s_waitcnt lgkmcnt(4)\n" \
    "v_smfmac_f32_16x16x64_bf16 v[12:15], v[56:59], v[88:95], %[idx]\n" \
    "ds_read_b128 v[88:91], %[wl] offset:26624\n" \
    "ds_read_b128 v[92:95], %[wl] offset:27648\n" \
    "s_waitcnt vmcnt(7)\n" \
    "v_smfmac_f32_16x16x64_bf16 v[0:3], v[60:63], a[88:95], %[idx]\n" \
    "v_max3_u32 v104, v104, v60, v61\n" \
    "v_smfmac_f32_16x16x64_bf16 v[4:7], v[60:63], a[216:223], %[idx]\n" \
    "v_max3_u32 v104, v104, v62, v63\n" \
    "v_smfmac_f32_16x16x64_bf16 v[8:11], v[60:63], v[216:223], %[idx]\n" \
    "s_waitcnt lgkmcnt(4)\n" \
    "v_smfmac_f32_16x16x64_bf16 v[12:15], v[60:63], v[96:103], %[idx]\n" \
    "ds_read_b128 v[96:99], %[wl] offset:28672\n" \
    "ds_read_b128 v[100:103], %[wl] offset:29696\n" \
    "s_waitcnt vmcnt(6)\n" \
    "v_smfmac_f32_16x16x64_bf16 v[0:3], v[64:67], a[96:103], %[idx]\n" \
    "v_max3_u32 v104, v104, v64, v65\n" \
    "v_smfmac_f32_16x16x64_bf16 v[4:7], v[64:67], a[224:231], %[idx]\n" \
    "v_max3_u32 v104, v104, v66, v67\n" \
    "v_smfmac_f32_16x16x64_bf16 v[8:11], v[64:67], v[224:231], %[idx]\n" \
    "s_waitcnt lgkmcnt(4)\n" \
    "v_smfmac_f32_16x16x64_bf16 v[12:15], v[64:67], v[80:87], %[idx]\n" \
    "ds_read_b128 v[80:83], %[wl] offset:30720\n" \
    "ds_read_b128 v[84:87], %[wl] offset:31744\n" \
    "s_waitcnt vmcnt(5)\n" \
    "v_smfmac_f32_16x16x64_bf16 v[0:3], v[68:71], a[104:111], %[idx]\n" \
    "v_max3_u32 v104, v104, v68, v69\n" \
    "v_smfmac_f32_16x16x64_bf16 v[4:7], v[68:71], a[232:239], %[idx]\n" \
    "v_max3_u32 v104, v104, v70, v71\n" \
    "v_smfmac_f32_16x16x64_bf16 v[8:11], v[68:71], v[232:239], %[idx]\n" \
    "s_waitcnt lgkmcnt(4)\n" \
    "v_smfmac_f32_16x16x64_bf16 v[12:15], v[68:71], v[88:95], %[idx]\n" \
    "s_waitcnt vmcnt(4)\n" \
    "v_smfmac_f32_16x16x64_bf16 v[0:3], v[72:75], a[112:119], %[idx]\n" \
    "v_max3_u32 v104, v104, v72, v73\n" \
    "v_smfmac_f32_16x16x64_bf16 v[4:7], v[72:75], a[240:247], %[idx]\n" \
    "v_max3_u32 v104, v104, v74, v75\n" \
    "v_smfmac_f32_16x16x64_bf16 v[8:11], v[72:75], v[240:247], %[idx]\n" \
    "s_waitcnt lgkmcnt(2)\n" \
    "v_smfmac_f32_16x16x64_bf16 v[12:15], v[72:75], v[96:103], %[idx]\n" \
    "s_waitcnt vmcnt(3)\n" \
    "v_smfmac_f32_16x16x64_bf16 v[0:3], v[76:79], a[120:127], %[idx]\n" \
    "v_max3_u32 v104, v104, v76, v77\n" \
    "v_smfmac_f32_16x16x64_bf16 v[4:7], v[76:79], a[248:255], %[idx]\n" \
    "v_max3_u32 v104, v104, v78, v79\n" \
    "v_smfmac_f32_16x16x64_bf16 v[8:11], v[76:79], v[248:255], %[idx]\n" \
    "s_waitcnt lgkmcnt(0)\n" \
    "v_smfmac_f32_16x16x64_bf16 v[12:15], v[76:79], v[80:87], %[idx]\n" \
    "s_waitcnt vmcnt(0)\n" \
    "s_nop 5\n" \
    "v_permlane32_swap_b32 v0, v8\n" \
    "v_add_f32 v0, v0, v8\n" \
    "v_permlane32_swap_b32 v1, v9\n" \
    "v_add_f32 v1, v1, v9\n" \
    "v_permlane32_swap_b32 v2, v10\n" \
    "v_add_f32 v2, v2, v10\n" \
    "v_permlane32_swap_b32 v3, v11\n" \
    "v_add_f32 v3, v3, v11\n" \
    "v_permlane32_swap_b32 v4, v12\n" \
    "v_add_f32 v4, v4, v12\n" \
    "v_permlane32_swap_b32 v5, v13\n" \
    "v_add_f32 v5, v5, v13\n" \
    "v_permlane32_swap_b32 v6, v14\n" \
    "v_add_f32 v6, v6, v14\n" \
    "v_permlane32_swap_b32 v7, v15\n" \
    "v_add_f32 v7, v7, v15\n" \
    ""
#define OUT_CLOBBER "v16", "v17", "v18", "v19", "v20", "v21", "v22", "v23", "v24", "v25", "v26", "v27", "v28", "v29", "v30", "v31", "v32", "v33", "v34", "v35", "v36", "v37", "v38", "v39", "v40", "v41", "v42", "v43", "v44", "v45", "v46", "v47", "v48", "v49", "v50", "v51", "v52", "v53", "v54", "v55", "v56", "v57", "v58", "v59", "v60", "v61", "v62", "v63", "v64", "v65", "v66", "v67", "v68", "v69", "v70", "v71", "v72", "v73", "v74", "v75", "v76", "v77", "v78", "v79", "v80", "v81", "v82", "v83", "v84", "v85", "v86", "v87", "v88", "v89", "v90", "v91", "v92", "v93", "v94", "v95", "v96", "v97", "v98", "v99", "v100", "v101", "v102", "v103", "s44", "s45", "s46", "scc", "memory"
constexpr int NREP = 2, NCP = 4;
#define OWNER_ASM \
    "s_getpc_b64 s[96:97]\n" \
    "14:\n" \
    "s_add_u32 s96, s96, 2f-14b\n" \
    "s_addc_u32 s97, s97, 0\n" \
    "s_sub_u32 s68, s96, 2f-11f\n" \
    "s_subb_u32 s69, s97, 0\n" \
    "s_mov_b32 %[np], 0\n" \
    "s_add_i32 s72, %[pb], 0x10000\n" \
    "13:\n" \
    "s_memtime vcc\n" \
    "s_waitcnt lgkmcnt(0)\n" \
    "s_sub_i32 s70, %[lk], vcc_lo\n" \
    "s_cmp_gt_i32 s70, %[rd]\n" \
    "s_cbranch_scc1 13b\n" \
    "s_add_i32 s70, %[pq], 0x10000\n" \
    "buffer_store_dwordx4 %[sv], %[ro], %[rs], %[pq] offen offset:0 sc1\n" \
    "buffer_store_dwordx4 %[sv], %[ro], %[rs], %[pq] offen offset:1024 sc1\n" \
    "buffer_store_dwordx4 %[sv], %[ro], %[rs], s70 offen offset:0 sc1\n" \
    "buffer_store_dwordx4 %[sv], %[ro], %[rs], s70 offen offset:1024 sc1\n" \
    "v_mov_b32 v2, -1\n" \
    "buffer_store_dword v2, %[rm], %[rs], %[mq] offen sc1\n" \
    "buffer_store_dwordx4 %[sv], %[rt], %[rw], %[tq] offen sc1\n" \
    "s_mov_b32 s71, 4\n" \
    "10:\n" \
    "s_memtime vcc\n" \
    "s_waitcnt lgkmcnt(0)\n" \
    "s_sub_i32 s70, %[lk], vcc_lo\n" \
    "s_cmp_gt_i32 s70, 112\n" \
    "s_cbranch_scc1 10b\n" \
    "s_max_i32 s70, s70, 0\n" \
    "s_memtime s[58:59]\n" \
    "s_memrealtime s[44:45]\n" \
    "s_nop 1\n" \
    "s_memrealtime s[46:47]\n" \
    "s_nop 1\n" \
    "s_memrealtime s[48:49]\n" \
    "s_nop 1\n" \
    "s_memrealtime s[50:51]\n" \
    "s_nop 1\n" \
    "s_memrealtime s[52:53]\n" \
    "s_nop 1\n" \
    "s_memrealtime s[54:55]\n" \
    "s_nop 1\n" \
    "s_memrealtime s[56:57]\n" \
    "s_nop 1\n" \
    "s_memrealtime vcc\n" \
    "s_waitcnt lgkmcnt(0)\n" \
    "s_add_i32 s44, s44, s46\n" \
    "s_add_i32 s48, s48, s50\n" \
    "s_add_i32 s52, s52, s54\n" \
    "s_add_i32 s44, s44, s48\n" \
    "s_add_i32 s52, s52, s56\n" \
    "s_add_i32 s44, s44, s52\n" \
    "s_add_i32 s59, s44, vcc_lo\n" \
    "s_sub_i32 s44, s59, %[oT]\n" \
    "s_mov_b32 %[oT], s59\n" \
    "s_sub_i32 s45, s58, %[oC]\n" \
    "s_mov_b32 %[oC], s58\n" \
    "s_mul_i32 s46, s44, %[oR]\n" \
    "s_lshr_b32 s46, s46, 12\n" \
    "s_sub_i32 s46, s45, s46\n" \
    "s_sub_i32 s44, s44, 1000\n" \
    "s_cmp_lt_u32 s44, 480\n" \
    "s_cselect_b32 s46, s46, 0\n" \
    "s_add_i32 %[oR], %[oR], s46\n" \
    "s_sub_i32 s45, %[oT], %[c8]\n" \
    "s_and_b32 s70, s70, -4\n" \
    "s_sub_u32 vcc_lo, s68, s70\n" \
    "s_subb_u32 vcc_hi, s69, 0\n" \
    "s_setpc_b64 vcc\n" \
    "s_nop 0\n" \
    "s_nop 0\n" \
    "s_nop 0\n" \
    "s_nop 0\n" \
    "s_nop 0\n" \
    "s_nop 0\n" \
    "s_nop 0\n" \
    "s_nop 0\n" \
    "s_nop 0\n" \
    "s_nop 0\n" \
    "s_nop 0\n" \
    "s_nop 0\n" \
    "s_nop 0\n" \
    "s_nop 0\n" \
    "s_nop 0\n" \
    "s_nop 0\n" \
    "s_nop 0\n" \
    "s_nop 0\n" \
    "s_nop 0\n" \
    "s_nop 0\n" \
    "s_nop 0\n" \
    "s_nop 0\n" \
    "s_nop 0\n" \
    "s_nop 0\n" \
    "s_nop 0\n" \
    "s_nop 0\n" \
    "s_nop 0\n" \
    "s_nop 0\n" \
    "11:\n" \
    "buffer_load_dwordx2 v[0:1], %[ca], %[rs], %[cs] offen sc1\n" \
    "s_waitcnt vmcnt(0)\n" \
    "6:\n" \
    "s_memtime s[46:47]\n" \
    "v_subrev_u32 v0, s45, v0\n" \
    "v_cmp_ne_u16 vcc, %[tag], v1\n" \
    "s_add_i32 s48, %[oC], -136\n" \
    "v_max_i32_dpp v0, v0, v0 row_shr:1 row_mask:0xf bank_mask:0xf\n" \
    "v_max_u32_dpp v1, v1, v1 row_shr:1 row_mask:0xf bank_mask:0xf\n" \
    "s_cbranch_vccnz 7f\n" \
    "8:\n" \
    "v_max_i32_dpp v0, v0, v0 row_shr:2 row_mask:0xf bank_mask:0xf\n" \
    "v_max_u32_dpp v1, v1, v1 row_shr:2 row_mask:0xf bank_mask:0xf\n" \
    "s_nop 0\n" \
    "v_max_i32_dpp v0, v0, v0 row_shr:4 row_mask:0xf bank_mask:0xf\n" \
    "v_max_u32_dpp v1, v1, v1 row_shr:4 row_mask:0xf bank_mask:0xf\n" \
    "s_nop 0\n" \
    "v_max_i32_dpp v0, v0, v0 row_shr:8 row_mask:0xf bank_mask:0xf\n" \
    "v_max_u32_dpp v1, v1, v1 row_shr:8 row_mask:0xf bank_mask:0xf\n" \
    "s_waitcnt lgkmcnt(0)\n" \
    "v_max_i32_dpp v0, v0, v0 row_bcast:15 row_mask:0xa bank_mask:0xf\n" \
    "v_max_u32_dpp v1, v1, v1 row_bcast:15 row_mask:0xa bank_mask:0xf\n" \
    "s_add_i32 s46, s46, 44\n" \
    "v_max_i32_dpp v0, v0, v0 row_bcast:31 row_mask:0xc bank_mask:0xf\n" \
    "v_max_u32_dpp v1, v1, v1 row_bcast:31 row_mask:0xc bank_mask:0xf\n" \
    "v_readlane_b32 s44, v0, 63\n" \
    "v_readlane_b32 s49, v1, 63\n" \
    "s_mul_i32 s47, s44, %[oR]\n" \
    "s_ashr_i32 s47, s47, 12\n" \
    "s_add_i32 s47, s47, s48\n" \
    "s_sub_i32 s58, s47, s46\n" \
    "s_cmp_gt_i32 s58, 112\n" \
    "s_cbranch_scc1 1f\n" \
    "12:\n" \
    "s_max_i32 s58, s58, 0\n" \
    "s_and_b32 s58, s58, -4\n" \
    "s_sub_u32 vcc_lo, s96, s58\n" \
    "s_subb_u32 vcc_hi, s97, 0\n" \
    "s_setpc_b64 vcc\n" \
    "s_nop 0\n" \
    "s_nop 0\n" \
    "s_nop 0\n" \
    "s_nop 0\n" \
    "s_nop 0\n" \
    "s_nop 0\n" \
    "s_nop 0\n" \
    "s_nop 0\n" \
    "s_nop 0\n" \
    "s_nop 0\n" \
    "s_nop 0\n" \
    "s_nop 0\n" \
    "s_nop 0\n" \
    "s_nop 0\n" \
    "s_nop 0\n" \
    "s_nop 0\n" \
    "s_nop 0\n" \
    "s_nop 0\n" \
    "s_nop 0\n" \
    "s_nop 0\n" \
    "s_nop 0\n" \
    "s_nop 0\n" \
    "s_nop 0\n" \
    "s_nop 0\n" \
    "s_nop 0\n" \
    "s_nop 0\n" \
    "s_nop 0\n" \
    "s_nop 0\n" \
    "2:\n" \
    "buffer_load_dwordx4 v[0:3], %[o], %[rs], %[pb] offen offset:0 sc1\n" \
    "buffer_load_dwordx4 v[4:7], %[o], %[rs], %[pb] offen offset:1024 sc1\n" \
    "buffer_load_dwordx4 v[8:11], %[o], %[rs], s72 offen offset:0 sc1\n" \
    "buffer_load_dwordx4 v[12:15], %[o], %[rs], s72 offen offset:1024 sc1\n" \
    "s_add_i32 %[np], %[np], 1\n" \
    "s_add_i32 %[tm], s44, s45\n" \
    "s_lshr_b32 s50, s49, 16\n" \
    "s_add_i32 s50, s50, s44\n" \
    "s_add_i32 s50, s50, %[cw]\n" \
    "s_mul_i32 s50, s50, %[oR]\n" \
    "s_ashr_i32 s50, s50, 12\n" \
    "s_add_i32 %[lk], s50, %[oC]\n" \
    "s_waitcnt vmcnt(3)\n" \
    "v_mfma_f32_16x16x32_bf16 v[16:19], %[sel], v[0:3], 0\n" \
    "v_max3_u32 v20, v0, v1, v1\n" \
    "v_max3_u32 v20, v2, v3, v20\n" \
    "s_waitcnt vmcnt(2)\n" \
    "v_mfma_f32_16x16x32_bf16 v[16:19], %[sel], v[4:7], v[16:19]\n" \
    "v_max3_u32 v20, v4, v5, v20\n" \
    "v_max3_u32 v20, v6, v7, v20\n" \
    "s_waitcnt vmcnt(1)\n" \
    "v_mfma_f32_16x16x32_bf16 v[16:19], %[sel], v[8:11], v[16:19]\n" \
    "v_max3_u32 v20, v8, v9, v20\n" \
    "v_max3_u32 v20, v10, v11, v20\n" \
    "s_waitcnt vmcnt(0)\n" \
    "v_mfma_f32_16x16x32_bf16 v[16:19], %[sel], v[12:15], v[16:19]\n" \
    "v_max3_u32 v20, v12, v13, v20\n" \
    "v_max3_u32 v20, v14, v15, v20\n" \
    "v_cmp_eq_u32 vcc, -1, v20\n" \
    "s_nop 3\n" \
    "s_cbranch_vccnz 4f\n" \
    "3:\n" \
    "v_add_f32_dpp v16, v16, v16 row_ror:8 row_mask:0xf bank_mask:0xf\n" \
    "v_add_f32_dpp v17, v17, v17 row_ror:8 row_mask:0xf bank_mask:0xf\n" \
    "v_cvt_pk_bf16_f32 v16, v16, v17\n" \
    "buffer_store_dword v16, %[so], %[rs], %[mb0] offen sc1\n" \
    "s_branch 5f\n" \
    "1:\n" \
    "s_memtime vcc\n" \
    "s_waitcnt lgkmcnt(0)\n" \
    "s_sub_i32 s58, s47, vcc_lo\n" \
    "s_cmp_gt_i32 s58, 112\n" \
    "s_cbranch_scc1 1b\n" \
    "s_branch 12b\n" \
    "4:\n" \
    "s_add_i32 %[bud], %[bud], -1\n" \
    "s_cmp_lt_i32 %[bud], 0\n" \
    "s_cbranch_scc0 2b\n" \
    "s_branch 3b\n" \
    "7:\n" \
    "s_bcnt1_i32_b64 s44, vcc\n" \
    "s_cmp_le_u32 s44, s71\n" \
    "s_mov_b32 s71, 0\n" \
    "s_cbranch_scc1 15f\n" \
    "s_cmp_eq_u32 %[tag], 1\n" \
    "s_cbranch_scc1 9f\n" \
    "s_sleep 15\n" \
    "9:\n" \
    "s_sleep 6\n" \
    "15:\n" \
    "s_add_i32 %[bud], %[bud], -1\n" \
    "s_cmp_lt_i32 %[bud], 0\n" \
    "s_cbranch_scc1 8b\n" \
    "buffer_load_dwordx2 v[0:1], %[ca], %[rs], %[cs] offen sc1\n" \
    "s_memtime s[58:59]\n" \
    "s_memrealtime s[44:45]\n" \
    "s_nop 1\n" \
    "s_memrealtime s[46:47]\n" \
    "s_nop 1\n" \
    "s_memrealtime s[48:49]\n" \
    "s_nop 1\n" \
    "s_memrealtime s[50:51]\n" \
    "s_nop 1\n" \
    "s_memrealtime s[52:53]\n" \
    "s_nop 1\n" \
    "s_memrealtime s[54:55]\n" \
    "s_nop 1\n" \
    "s_memrealtime s[56:57]\n" \
    "s_nop 1\n" \
    "s_memrealtime vcc\n" \
    "s_waitcnt lgkmcnt(0)\n" \
    "s_add_i32 s44, s44, s46\n" \
    "s_add_i32 s48, s48, s50\n" \
    "s_add_i32 s52, s52, s54\n" \
    "s_add_i32 s44, s44, s48\n" \
    "s_add_i32 s52, s52, s56\n" \
    "s_add_i32 s44, s44, s52\n" \
    "s_add_i32 s59, s44, vcc_lo\n" \
    "s_sub_i32 s44, s59, %[oT]\n" \
    "s_mov_b32 %[oT], s59\n" \
    "s_sub_i32 s45, s58, %[oC]\n" \
    "s_mov_b32 %[oC], s58\n" \
    "s_mul_i32 s46, s44, %[oR]\n" \
    "s_lshr_b32 s46, s46, 12\n" \
    "s_sub_i32 s46, s45, s46\n" \
    "s_sub_i32 s44, s44, 1000\n" \
    "s_cmp_lt_u32 s44, 480\n" \
    "s_cselect_b32 s46, s46, 0\n" \
    "s_add_i32 %[oR], %[oR], s46\n" \
    "s_sub_i32 s45, %[oT], %[c8]\n" \
    "s_waitcnt vmcnt(0)\n" \
    "s_branch 6b\n" \
    "5:\n" \
    ""
#define OWNER_CLOBBER "v0", "v1", "v2", "v3", "v4", "v5", "v6", "v7", "v8", "v9", "v10", "v11", "v12", "v13", "v14", "v15", "v16", "v17", "v18", "v19", "v20", "s44", "s45", "s46", "s47", "s48", "s49", "s50", "s51", "s52", "s53", "s54", "s55", "s56", "s57", "s58", "s59", "s68", "s69", "s70", "s71", "s72", "s96", "s97", "vcc", "scc", "memory"
constexpr int O_W = 0;
constexpr int O_STG = 131072;
constexpr int O_B = O_STG + 20480;
constexpr int OUT_REP = 7;
#define OUT_AUX 16
DI int opaque_lane() { int l; asm volatile("v_mbcnt_lo_u32_b32 %0, -1, 0\n\tv_mbcnt_hi_u32_b32 %0, -1, %0" : "=v"(l)); return l; }
template <int O, class V> DI void put4(V& d, u32x4 f) {
#pragma unroll
    for (int j = 0; j < 4; ++j) d[O + j] = f[j];
}
DI void role_outp(const Args& a, LAS char* lds, unsigned lds_base, int nb, int rep, rsrc_t rw, int& budget) {
    const int tid = threadIdx.x, lane = tid & 63, w = __builtin_amdgcn_readfirstlane(tid >> 6);
    WG_BARRIER();
    const int n = lane & 15, g = lane >> 4;
    const int wl = O_W + w * 32768 + lane * 16;
    u32x32 A[8], V[4];
    {
        const int stg = O_STG + w * 5120;
        const rsrc_t ro = __builtin_amdgcn_make_buffer_rsrc((void*)a.Wo, 0, HD * DM * 2, 0x00020000);
        const int so = (1024 * w + (lane >> 3)) * (DM * 2) + nb * 128 + (lane & 7) * 16, sw = stg + (lane >> 3) * 160 + (lane & 7) * 16;
        const int ra = stg + (16 * (g & 1) + 4 * (g >> 1) + (n >> 2)) * 160 + (n & 3) * 8;
        constexpr int PB = 4;
        u32x4 b[PB][4];
        auto fetch = [&](int ks) {
#pragma unroll
            for (int i = 0; i < 4; ++i) b[ks % PB][i] = __builtin_amdgcn_raw_buffer_load_b128(ro, so, (64 * (ks >> 1) + 16 * (ks & 1) + 8 * (i & 1) + 32 * (i >> 1)) * (DM * 2), 0);
        };
        unsigned tp = now();
        const int pc = a.d1 >> 16;
#pragma unroll
        for (int ks = 0; ks < PB; ++ks) { fetch(ks); FENCE(); tp += pc; while ((int)(now() - tp) < 0) {} }
        FENCE();
        static_for<32>([&](auto ic) {
            constexpr int ks = decltype(ic)::value;
#pragma unroll
            for (int i = 0; i < 4; ++i) *(LAS u32x4*)(lds + sw + i * 1280) = b[ks % PB][i];
            if (ks + PB < 32) { fetch(ks + PB); FENCE(); tp += pc; }
            u32x4 f[4];
#pragma unroll
            for (int t = 0; t < 4; ++t) { const u32x2 lo = tr_read(lds, ra + t * 32), hi = tr_read(lds, ra + t * 32 + 1280); f[t] = (u32x4){lo.x, lo.y, hi.x, hi.y}; }
            put4<4 * (ks & 7)>(A[ks >> 3], f[0]); put4<4 * (ks & 7)>(A[4 + (ks >> 3)], f[1]); put4<4 * (ks & 7)>(V[ks >> 3], f[2]);
            *(LAS u32x4*)(lds + wl + ks * 1024) = f[3];
            if constexpr (ks == 7) asm volatile("" : "+{a[0:31]}"(A[0]), "+{a[128:159]}"(A[4]), "+{v[128:159]}"(V[0]));
            if constexpr (ks == 15) asm volatile("" : "+{a[32:63]}"(A[1]), "+{a[160:191]}"(A[5]), "+{v[160:191]}"(V[1]));
            if constexpr (ks == 23) asm volatile("" : "+{a[64:95]}"(A[2]), "+{a[192:223]}"(A[6]), "+{v[192:223]}"(V[2]));
            if constexpr (ks == 31) asm volatile("" : "+{a[96:127]}"(A[3]), "+{a[224:255]}"(A[7]), "+{v[224:255]}"(V[3]));
            FENCE();
            if (ks + PB < 31) while ((int)(now() - tp) < 0) {}
        });
    }
    wait_prog(rw, F_S4 + lane * 4, true, 1, budget);
    const rsrc_t ru = __builtin_amdgcn_make_buffer_rsrc(a.out, 0, SEQ * DM * 2, 0x00020000);
    constexpr int SP = 2 * LATE - 2;
    auto first = [&](int u) { return u >= SP ? 8 * u + 8 * SP : 16 * u; };
    auto st_s = [&](int u) { return (u >= 2 * FAST ? F_H + ((first(u) >> 4) - 2 * FAST) * 4096 : F_O + (u & 3) * 4) + w * 1024; };
    auto bn_s = [&](int u) { return OFF_BO + (64 * nb + 16 * w) * BO_ROW + first(u) * 2; };
    auto gn_s = [&](int u) { return OFF_GT + (first(u) + 4 * w) * 4; };
    int st = -1;
    u32x2 bn = ld64(rw, (lane >> 2) * BO_ROW + (lane & 3) * 8, bn_s(rep));
    float gn = __uint_as_float(ld32(rw, g * 4, gn_s(rep)));
    __builtin_amdgcn_s_waitcnt(0);
#define OUT_OPS "=&{v[0:15]}"(acc), "=&{v104}"(top), "=&{v105}"(st), "=&{v[106:107]}"(bn), "=&{v108}"(gn) \
        : "{a[0:31]}"(A[0]), "{a[32:63]}"(A[1]), "{a[64:95]}"(A[2]), "{a[96:127]}"(A[3]), "{a[128:159]}"(A[4]), "{a[160:191]}"(A[5]), "{a[192:223]}"(A[6]), "{a[224:255]}"(A[7]), \
          "{v[128:159]}"(V[0]), "{v[160:191]}"(V[1]), "{v[192:223]}"(V[2]), "{v[224:255]}"(V[3]), [rw] "s"(rw), [so] "s"(t0 * 8192), [wl] "v"(lds_base + O_W + w * 32768 + lane * 16), \
          [tv] "v"(tv), [tm] "s"(tm), [vo] "v"(vo), [sv] "v"(sv), [bv] "v"((lane >> 2) * BO_ROW + (lane & 3) * 8), [gv] "v"(g * 4), \
          [ss] "s"(st_s(u + OUT_REP)), [bs] "s"(bn_s(u + OUT_REP)), [gs] "s"(gn_s(u + OUT_REP))
    for (int u = rep; u < SP + 4; u += OUT_REP) {
        const bool fast = u >= 2 * FAST, sp = u >= SP;
        const int t0 = first(u);
        WG_BARRIER();
        int lane = opaque_lane(), n = lane & 15, g = lane >> 4;
        if (u == 3 * OUT_REP && nb == 0) __builtin_amdgcn_raw_buffer_store_b64(launch_tag(a), rw, w + lane == 0 ? F_EPOCH : OOB, 0, 16);
        S4S(0)
        TT(u >= 119, 0)
        *(LAS u32x2*)(lds + O_B + (16 * w + (lane >> 2)) * 40 + (lane & 3) * 8) = bn;
        const float gt = gn;
        const int tv = OFF_OF + w * 16384 + nb * 2048 + g * 65536 + n * 128, tm = __builtin_amdgcn_readfirstlane(nb < 8 ? sp ? 0xFFFF : -1 : 0);
        const int vo = OFF_OF + w * 16384 + (n & 7) * 16 + (sp ? (n >> 3) * 512 + g * 128 : (n >> 3) * 65536 + (g & 1) * 512 + (g >> 1) * 128);
        const int thr = fast ? 0 : u >> 1, sv = (lane & 7) * 128;
        if (!__all(st >= thr)) do { __builtin_amdgcn_s_sleep(1); st = (int)ld32(rw, sv, st_s(u)); asm volatile("" ::: "memory"); } while (!__all(st >= thr) && --budget >= 0);
        if (fast) { const unsigned t = now(); (void)wait_until(t + wave_max32(min((int)(2 * (st - t)) >> 1, 500))); }
        S4S(1)
        TT(u >= 119, 1)
        f32x16 acc; unsigned top;
        if (sp) {
            do asm volatile(OUT8_ASM : OUT_OPS, [idx] "v"(n & 8 ? 0xEEEE : 0x4444) : OUT_CLOBBER); while (__any(top == SENT) && --budget >= 0);
            TT(u >= 119, 2)
            lane = opaque_lane(); n = lane & 15; g = lane >> 4;
#pragma unroll
            for (int i = 0; i < 8; ++i) *(LAS float*)(lds + O_STG + (w * 16 + 4 * (g & 1) + (i & 3)) * 272 + (g >> 1) * 128 + (i >> 2) * 64 + n * 4) = acc[i];
        } else {
            do asm volatile(OUT16_ASM : OUT_OPS : OUT_CLOBBER); while (__any(top == SENT) && --budget >= 0);
            S4S(2)
            TT(u >= 119, 2)
            lane = opaque_lane(); n = lane & 15; g = lane >> 4;
#pragma unroll
            for (int t = 0; t < 4; ++t) *(LAS f32x4*)(lds + O_STG + (w * 16 + n) * 272 + t * 64 + g * 16) = (f32x4){acc[4 * t], acc[4 * t + 1], acc[4 * t + 2], acc[4 * t + 3]};
        }
        WG_BARRIER();
        S4S(3)
        const int tok = 4 * w + g, c4 = 4 * n;
        f32x4 r[4]; u16 bv[4];
#pragma unroll
        for (int ww = 0; ww < 4; ++ww) { r[ww] = *(LAS f32x4*)(lds + O_STG + (ww * 16 + tok) * 272 + c4 * 4); bv[ww] = *(LAS u16*)(lds + O_B + (c4 + ww) * 40 + tok * 2); }
        FENCE();
        const f32x4 v = gt * ((r[0] + r[1]) + (r[2] + r[3])) + (1.0f - gt) * (f32x4){bf2f(bv[0]), bf2f(bv[1]), bf2f(bv[2]), bf2f(bv[3])};
        __builtin_amdgcn_raw_buffer_store_b64((u32x2){pack2(v[0], v[1]), pack2(v[2], v[3])}, ru, sp && tok >= 8 ? OOB : (tok * DM + 64 * nb + c4) * 2, t0 * (DM * 2), OUT_AUX);
        S4S(4)
        TT(u >= 119, 3)
        TT(u >= 105 && u < 112, 4)
    }
}

constexpr int L_MEMS = 0;
constexpr int L_Q = 9216;
constexpr int L_KO = L_Q + 2560;
constexpr int L_KX = 16384;
constexpr int L_W = 32768;
constexpr int C_RING = 8;
static_assert(S_TOTAL <= L_SINK && L_W + C_RING * 16384 <= L_TOTAL && L_KO + 8 * 576 <= L_KX, "LDS budget");
static_assert(P_STG + P_RING * 16384 <= L_TOTAL, "LDS budget");

DI int pos2col(int p) { return p < 512 ? (p & ~63) + 16 * (p & 3) + ((p & 63) >> 2) : p; }
DI void unit_rc(int u, int& i, int& p0) {
    if (u < 512) { i = 2 * ((u >> 2) & 3) + (u & 1); p0 = (u & ~15) + 4 * (u & 2); }
    else { const int v = u - 512; i = (v >> 1) & 7; p0 = 512 + 16 * (v >> 4) + 8 * (v & 1); }
}
DI void mem0_arrange(LAS char* lds, int raw, int tid) {
#pragma unroll
    for (int k = 0; k < 2; ++k) {
        int i, p0; unit_rc(tid + 256 * k, i, p0);
        unsigned r[4];
#pragma unroll
        for (int j = 0; j < 4; ++j) r[j] = *(LAS unsigned*)(lds + raw + (i * DM + pos2col(p0)) * 2 + 32 * j);
        *(LAS u32x4*)(lds + L_MEMS + (tid + 256 * k) * 16) = (u32x4){__builtin_amdgcn_perm(r[1], r[0], 0x05040100), __builtin_amdgcn_perm(r[3], r[2], 0x05040100),
                                                                     __builtin_amdgcn_perm(r[1], r[0], 0x07060302), __builtin_amdgcn_perm(r[3], r[2], 0x07060302)};
    }
    int i, p0; unit_rc(512 + (tid & 63), i, p0);
    *(LAS u32x4*)(lds + L_MEMS + 8192 + (tid & 63) * 16) = *(LAS u32x4*)(lds + raw + (i * DM + p0) * 2);
}
DI unsigned read_canaries(int s, rsrc_t rs, int lane, int& budget) {
    const int o = 2 * OFF_CAN + (s & 3) * 16384 + (lane >> 2) * 512 + (lane & 3) * 16;
    u32x2 v;
    for (;;) {
        v = ld64(rs, o, 0);
        asm volatile("" ::: "memory");
        if (__all((v.y & 0xffff) == (unsigned)(s + 1))) break;
        if (--budget < 0) break;
    }
    const unsigned t = 8 * now();
    return t + (unsigned)wave_max32(min(max((int)(v.x - t), -0x4000), 0x4000));
}
DI u32x8 sm_frag(LAS char* lds, int sl, int r0, int sa, int sH, int cb, int lo8) {
    auto rd = [&](int r) { return tr_read(lds, sl + r * 256 + ((cb ^ (2 * swz(r))) * 16) + lo8); };
    const u32x2 a = rd(r0), b = rd(r0 + sa), c = rd(r0 + sH), d = rd(r0 + sa + sH);
    return (u32x8){a.x, a.y, b.x, b.y, c.x, c.y, d.x, d.y};
}
#define GS(slot, i)
#define GV(slot, i, v)
DI int tp_unit(int U, int sel) { return OFF_TP + cmap((U & 511) * 4096) + ((sel >> (4 + (U >> 8 & 1)) ^ U >> 9) & 1) * 8192; }
template <int N> DI void finish(const Args& a, rsrc_t rw, int U, int sel, unsigned t, int& budget) {
    const int lane = opaque_lane(), n = lane & 7, g = lane >> 4;
    const rsrc_t ru = __builtin_amdgcn_make_buffer_rsrc(a.out, 0, SEQ * DM * 2, 0x00020000);
    constexpr int M = (N + 1) / 2;
    int at[M]; f32x2 bv[M]; float gt[M];
#pragma unroll
    for (int i = 0; i < M; ++i) {
        const int u = U + 2 * i + (lane >> 3 & 1 & (N - 1));
        const int tok = LATE * SEG + (U < 512 ? 2 * (u & 15) + (n >> 2) : (u & 32) + 4 * (u & 7) + (n >> 1));
        const int col = U < 512 ? 32 * (u >> 4) + 16 * (g >> 1) + 4 * (n & 3) + 2 * (g & 1) : 512 + 2 * (u & 24) + 8 * (n & 1) + 2 * g;
#pragma unroll
        for (int r = 0; r < 2; ++r) bv[i][r] = bf2f(__builtin_amdgcn_raw_buffer_load_b16(rw, col * BO_ROW + tok * 2, OFF_BO + r * BO_ROW, 16));
        gt[i] = __uint_as_float(ld32(rw, tok * 4, OFF_GT));
        at[i] = (tok * DM + col) * 2;
    }
    u32x4 one;
#pragma unroll
    for (int j = 0; j < 4; ++j) one[j] = (lane >> 2 & 3) == j ? 0x3F80u << 16 * (lane & 1) : 0u;
#pragma unroll
    for (int i = 0; i < M; ++i) { bv[i] *= 1.0f - gt[i]; asm volatile("" : "+v"(bv[i]), "+v"(one)); }
    (void)wait_until(t);
    f32x4 v[N];
    for (;;) {
        u32x4 d[4 * N];
#pragma unroll
        for (int k = 0; k < 4 * N; ++k) d[k] = ld128(rw, lane * 16 + (k & 3) * 1024, tp_unit(U + (k >> 2), sel));
        unsigned top = 0;
#pragma unroll
        for (int i = 0; i < N; ++i) {
            asm volatile(M16 "%0, %1, %2, 0\n\t" M16 "%0, %1, %3, %0\n\t" M16 "%0, %1, %4, %0\n\t" M16 "%0, %1, %5, %0\n\ts_nop 7" : "=&v"(v[i]) : "v"(one), "v"(d[4 * i]), "v"(d[4 * i + 1]), "v"(d[4 * i + 2]), "v"(d[4 * i + 3]));
            top = mx4(mx4(mx4(mx4(top, d[4 * i]), d[4 * i + 1]), d[4 * i + 2]), d[4 * i + 3]);
        }
        if (!__any(top == SENT) || --budget < 0) break;
        __builtin_amdgcn_s_sleep(4);
    }
#pragma unroll
    for (int i = 0; i < M; ++i) {
        f32x2 x;
#pragma unroll
        for (int r = 0; r < 2; ++r) {
            auto both = [](float y) { return y + __int_as_float(__builtin_amdgcn_update_dpp(0, __float_as_int(y), 0x128, 0xf, 0xf, true)); };
            const float p = both(v[2 * i][r]), q = both(v[(2 * i + 1) & (N - 1)][r]);
            x[r] = gt[i] * (lane & 8 ? q : p) + bv[i][r];
        }
        __builtin_amdgcn_raw_buffer_store_b32(pack2(x[0], x[1]), ru, at[i], 0, OUT_AUX);
    }
}
DI void owner_loop(const Args& a, int G, rsrc_t rs, rsrc_t rw, int sel, unsigned tbase, unsigned cbase, int& budget) {
    const int lane = threadIdx.x & 63, n = lane & 15, g = lane >> 4;
    const bool ex = G >= 64;
    const u32x4 sent4 = {SENT, SENT, SENT, SENT};
    const int js = (n & 2) ? 8 : ex ? (n & 4) + 2 * (n & 1) + (n >> 3) : 2 * (n >> 2) + (n & 1);
    u32x4 selu;
#pragma unroll
    for (int j = 0; j < 4; ++j) selu[j] = (js == 2 * j ? 0x3F80u : 0u) | (js == 2 * j + 1 ? 0x3F800000u : 0u);
    const int o = (part_unit(G, 0) ^ (sel & 8) * 256) + lane * 16;
    const int so = (n >> 3) * 16384 + ((ex ? 32768 + (G & 4) * 512 + (G & 3) * 128 : (G & 16) * 256 + (G & 32) * 64 + (G & 15) * 128) ^ (~sel & 8) * 256) + (g & 1) * 4 +
                   (ex ? (n & 4) * 16 + (g >> 1) * 32 + (n & 3) * 8 : (n & 6) * 16 + (g >> 1) * 16 + (n & 1) * 8);
    unsigned fails = 0;
    const int c8 = G >= 64 ? a.c1 >> 16 & 255 : (G & 28) == 0 ? a.c1 >> 8 & 255 : a.c1 & 255;
    const bool gv_on = true; (void)gv_on;
    unsigned oT = now(), oC = (unsigned)__builtin_readcyclecounter(), oR;
    { const unsigned r = (unsigned)(512.0f * (float)(oC - cbase) * __builtin_amdgcn_rcpf((float)(oT - tbase))); asm volatile("s_nop 0\n\tv_readfirstlane_b32 %0, %1" : "=s"(oR) : "v"(r)); }
    oT = 8 * oT + 16;
    unsigned lk = oC;
    budget = __builtin_amdgcn_readfirstlane(budget);
    const int ca = 2 * OFF_CAN + (lane >> 2) * 512 + (blockIdx.x & (NCP - 1)) * 128 + (lane & 3) * 16;
    unsigned tc;
    for (int s = 0; s < NSEG - 1; ++s) {
        const int pb = (s & 1) * 2 * PART_STEP;
        int np;
        asm volatile(OWNER_ASM : [np] "=&s"(np), [tm] "=&s"(tc), [bud] "+s"(budget), [oT] "+s"(oT), [oC] "+s"(oC), [oR] "+s"(oR), [lk] "+s"(lk)
            : [c8] "s"(c8), [ca] "v"(ca), [cs] "s"((s & 3) * 16384), [tag] "s"(s + 1), [o] "v"(o), [so] "v"(so), [sel] "v"(selu), [rs] "s"(rs), [pb] "s"(pb), [mb0] "s"(2 * (OFF_MEMB + ((s + 1) & 3) * MEMB_STRIDE)),
              [cw] "s"(a.cw - c8), [rd] "s"(a.rd), [sv] "v"(sent4), [ro] "v"(s ? o : OOB), [pq] "s"(2 * PART_STEP - pb), [rm] "v"(s > 2 ? so : OOB), [mq] "s"(2 * (OFF_MEMB + ((s + 2) & 3) * MEMB_STRIDE)),
              [rw] "s"(rw), [rt] "v"(s >= 24 && s < 56 ? lane * 16 : OOB), [tq] "s"(tp_unit(8 * G + (s >> 2 & 7), sel) + (s & 3) * 1024) : OWNER_CLOBBER);
        fails += np > 1;
    }
    wait_prog(rw, F_S4 + lane * 4, true, 1, budget);
    const unsigned t = now(), t0 = t + min((int)(tc - 8 * t) >> 3, 100);
    finish<4>(a, rw, 4 * G + 4 * (G & 64), sel, t0 + (a.xa >> 8 & 255), budget);
    if (G >= 32 && G < 64) finish<1>(a, rw, 512 + G, sel, t0 + (a.xb >> 24), budget);
}
#define CORNER 4
DI void role_owner(const Args& a, unsigned lds_base, int ow, rsrc_t rw, int& budget) {
    const int tid = threadIdx.x, lane = tid & 63, w = __builtin_amdgcn_readfirstlane(tid >> 6);
    const unsigned tbase = now(), cbase = (unsigned)__builtin_readcyclecounter();
    if ((ow | 4) != 63 && w) { prefetch(a, lds_base, ow & 3, (ow >> 2) * 3 + w - 1, (ow & 3) == 3 ? 42 : 48); VM_DRAIN(); }
    if (w >= 2 || (w == 1 && (ow & ~11))) return;
    const u32x2 tag = launch_tag(a);
    tell_side(rw, tag);
    if ((ow | 4) == 63) {
        auto rtt = [&](int o) {
            unsigned long long t0, t1;
            asm volatile("s_memtime %0\n\ts_waitcnt lgkmcnt(0)" : "=s"(t0) :: "memory");
            st32(SENT, rw, lane == 0 ? 0 : OOB, o);
            asm volatile("s_waitcnt vmcnt(0)\n\ts_memtime %0\n\ts_waitcnt lgkmcnt(0)" : "=s"(t1) :: "memory");
            return (unsigned)(t1 - t0);
        };
        auto die = [&](int o, int d = 8192) {
            unsigned t0 = ~0u, t1 = ~0u;
            for (int r = 0; r < 4; ++r) { t0 = min(t0, rtt(o + r * 128)); t1 = min(t1, rtt(o + d + r * 128)); }
            return (unsigned)(t1 < t0);
        };
        unsigned best[4] = {~0u, ~0u, ~0u, ~0u}, near = die(ow & 4 ? 0 : OFF_TP);
        if (ow & 4) {
            for (int r = 4; r < 4 + CORNER; ++r)
#pragma unroll
                for (int i = 0; i < 4; ++i) best[i] = min(best[i], rtt(near * 8192 + cmap(i * 4096) + r * 128));
            near += 2 * (best[0] + best[2] < best[1] + best[3]) + 4 * (best[0] + best[1] < best[2] + best[3]);
        } else near = 2 * near + 4 * die(OFF_TP + (2 << 20)) + (die(32768, 2048) ^ die_side());
        __builtin_amdgcn_raw_buffer_store_b64((u32x2){(tag.x & ~7u) | near, tag.y}, rw, lane == 0 ? F_INIT + (ow & 4 ? 32 : 161) * 128 : OOB, 0, 16);
    }
    const int sel = wait_init(rw, tag, budget, 32) ^ a.c1 >> 24;
    const rsrc_t rs = __builtin_amdgcn_make_buffer_rsrc(a.ws + (sel & 1) * 8192, 0, COMM_BYTES - 8192, 0x00020000);
    const int side = agree_side(rw, tag, ~tag, budget), cc = sel >> 1;
    const int G = (w ? 64 + 4 * side : 32 * side + (ow >> 1 & 24) + (ow & 4)) + ((cc ^ ow >> 1 ^ w) & 1) + ((cc ^ 2 * ow ^ ow >> 4) & 2);
    owner_loop(a, G, rs, rw, sel, tbase, cbase, budget);
}

template <int O, class V> DI void put8(V& d, u32x8 f) {
#pragma unroll
    for (int j = 0; j < 8; ++j) d[O + j] = f[j];
}
DI f32x4 SMF64(u32x4 a, u32x8 b, f32x4 c, int i) { asm volatile("s_nop 1\n\tv_smfmac_f32_16x16x64_bf16 %0, %1, %2, %3\n\ts_nop 7" : "+v"(c) : "v"(a), "v"(b), "v"(i)); return c; }
DI f32x4 SMF32(u32x2 a, u32x4 b, f32x4 c, int i) { asm volatile("s_nop 1\n\tv_smfmac_f32_16x16x32_bf16 %0, %1, %2, %3\n\ts_nop 7" : "+v"(c) : "v"(a), "v"(b), "v"(i)); return c; }
DI f32x4 MFV(u32x4 a, u32x4 b, f32x4 c) { asm volatile("s_nop 1\n\tv_mfma_f32_16x16x32_bf16 %0, %1, %2, %0\n\ts_nop 7" : "+v"(c) : "v"(a), "v"(b)); return c; }

DI void role_head(const Args& a, LAS char* lds, unsigned lds_base, int h, rsrc_t rw, int& budget) {
    const int tid = threadIdx.x, w = __builtin_amdgcn_readfirstlane(tid >> 6);
    char* ws = a.ws;
    const unsigned tbase = now(), cbase = (unsigned)__builtin_readcyclecounter();
    int tw = 0;
    u32x32 A0, A1, A2, A3, A4, A5, A6, A7, R0, R1, R2, R3; u32x8 R4;
    u32x2 tag;
    {
        const int lane = tid & 63, n = lane & 15, g = lane >> 4;
#pragma unroll
        for (int j = 0; j < 3; ++j) glds16(a.mem0, min(4 * j + w, 8) * 1024 + lane * 16, lds_base + L_KX + min(4 * j + w, 8) * 1024);
        const int tq = n >> 2, tc = (n & 3) >> 1, lo8 = (n & 1) * 8;
        const int r_nat = 16 * (g >> 1) + 4 * (g & 1) + (tq & 1) + 8 * (tq >> 1);
        const int r_sig = 16 * (tq & 1) + 4 * (g >> 1) + (g & 1) + 2 * (tq >> 1);
        const int r_o = 16 * (tq & 1) + 8 * (g >> 1) + 4 * (g & 1) + (tq >> 1);
#define WFRAG(sl, kb, t) ((kb) < 8 ? sm_frag(lds, sl, r_sig, 32, 8, 4 * w + 2 * (t) + tc, lo8) : sm_frag(lds, sl, r_nat, 2, 32, 4 * w + 2 * (t) + tc, lo8))
        const u32x4 ro = ring_offs(w, lane, HD);
        auto issue = [&](int i) {
            const unsigned slot = lds_base + L_W + (i % C_RING) * 16384 + w * 4096;
            if (i == 23) ring_issue(a.Wo + (size_t)h * DH * DM + 512, ring_offs(w, lane, [](int r, int c) { return (r + 64 * (c >> 3)) * (DM * 2) + (c & 7) * 16; }), slot);
            else if (i < 28) ring_issue((i < 8 || i == 20 ? a.Wq : i < 16 || i == 21 ? a.Wk : a.Wv) + (size_t)(64 * (i < 16 ? i & 7 : i < 20 ? i - 12 : i < 24 ? 8 : i - 24)) * HD + h * DH, ro, slot);
            else ring_issue(a.Wo + (size_t)(h * DH + 64 * ((i & 1) ^ ((i - 28) >> 1))) * DM + 256 * tw + 128 * (i & 1), ring_offs(w, lane, DM), slot);
        };
        auto keep = [&](int at, u32x8 f) {
            *(LAS u32x4*)(lds + at + lane * 16) = (u32x4){f[0], f[1], f[2], f[3]};
            *(LAS u32x4*)(lds + at + 1024 + lane * 16) = (u32x4){f[4], f[5], f[6], f[7]};
        };
        u32x8 hold[8] = {};
#pragma unroll
        for (int i = 0; i < C_RING; ++i) issue(i);
        tag = launch_tag(a);
        tell_side(rw, tag);
        u32x2 other;
        ring_wait<4 * C_RING + 1>();
        mem0_arrange(lds, L_KX, tid);
        static_for<9>([&](auto ic) {
            constexpr int G = decltype(ic)::value;
            ring_wait<(G == 2 || G == 5 || G == 6 ? 16 : G < 5 ? 17 : G == 7 ? 8 : 0)>();
            if constexpr (G == 5) { asm volatile("" : "+v"(other)); tw = agree_side(rw, tag, other, budget); }
            const int sl = L_W + (G & 1) * 65536;
            auto fr = [&](int j, int t) { return WFRAG(sl + ((j ^ w) << 14), 0, t); };
            if constexpr (G < 4) {
                u32x32& A = G == 0 ? A0 : G == 1 ? A2 : G == 2 ? A4 : A6;
                u32x32& B = G == 0 ? A1 : G == 1 ? A3 : G == 2 ? A5 : A7;
                put8<0>(A, fr(0, 0)); put8<8>(A, fr(0, 1)); put8<16>(A, fr(1, 0)); put8<24>(A, fr(1, 1));
                put8<0>(B, fr(2, 0)); put8<8>(B, fr(2, 1)); put8<16>(B, fr(3, 0)); put8<24>(B, fr(3, 1));
                if constexpr (G == 0) asm volatile("" : "+{a[0:31]}"(A0), "+{a[32:63]}"(A1));
                if constexpr (G == 1) asm volatile("" : "+{a[64:95]}"(A2), "+{a[96:127]}"(A3));
                if constexpr (G == 2) asm volatile("" : "+{a[128:159]}"(A4), "+{a[160:191]}"(A5));
                if constexpr (G == 3) asm volatile("" : "+{a[192:223]}"(A6), "+{a[224:255]}"(A7));
            } else if constexpr (G == 4) {
                put8<16>(R1, fr(0, 0)); put8<24>(R1, fr(0, 1)); put8<0>(R2, fr(1, 0)); put8<8>(R2, fr(1, 1));
                put8<16>(R2, fr(2, 0)); put8<24>(R2, fr(2, 1)); put8<0>(R3, fr(3, 0)); put8<8>(R3, fr(3, 1));
            } else if constexpr (G == 5) {
                put8<16>(R0, WFRAG(sl, 8, 0)); put8<24>(R0, WFRAG(sl, 8, 1)); put8<0>(R1, WFRAG(sl + 16384, 8, 0)); put8<8>(R1, WFRAG(sl + 16384, 8, 1));
                put8<16>(R3, WFRAG(sl + 32768, 8, 0)); put8<24>(R3, WFRAG(sl + 32768, 8, 1));
                hold[7] = sm_frag(lds, sl + 49152, r_o, 2, 32, 8 * (w >> 1) + 4 * tw + 2 * (w & 1) + tc, lo8);
            } else if constexpr (G == 6) {
#pragma unroll
                for (int j = 0; j < 7; ++j) hold[j] = fr(j >> 1, j & 1);
                R4 = fr(3, 1);
            } else {
                if constexpr (G == 8)
#pragma unroll
                    for (int T = 0; T < 4; ++T) keep(L_W + 65536 + w * 8192 + T * 2048, hold[T]);
#pragma unroll
                for (int T = 0; T < 4; ++T) hold[T] = sm_frag(lds, L_W + (2 * G - 10 + (w >> 1)) * 16384, r_o, 2, 32, 8 * (w & 1) + 2 * T + tc, lo8);
            }
            if constexpr (G != 7) WG_BARRIER();
            if constexpr (G < 6)
#pragma unroll
                for (int i = 0; i < 4; ++i) issue(4 * G + 8 + i);
            if constexpr (G == 2) asm volatile("buffer_load_dwordx2 %0, off, %1, %2 sc1" : "=v"(other) : "s"(rw), "s"(side_at(16)) : "memory");
            if constexpr (G == 6)
#pragma unroll
                for (int j = 0; j < 8; ++j) keep(L_W + (j >> 1) * 16384 + w * 4096 + (j & 1) * 2048, hold[j]);
            if constexpr (G == 8)
#pragma unroll
                for (int T = 0; T < 4; ++T) keep(L_W + 98304 + w * 8192 + T * 2048, hold[T]);
        });
        const int ai = 2 * (n >> 2) + (n & 1), ahf = (n >> 1) & 1;
        R0[0] = R0[1] = R0[2] = R0[3] = 0x3F803F80u;
        R0[4] = ahf ? 0xEEEE : 0x4444;
        R0[5] = (g >> 1) * 512 + (n >> 2) * 128 + (g & 1) * 64 + (n & 3) * 16;
        R0[6] = lds_base + L_MEMS + 8192 + g * 256 + ai * 32 + ahf * 16;
        R0[7] = lane * 16;
        R0[8] = lds_base + L_Q + 2 * g * 320 + (16 * w + n) * 4;
        R0[9] = lds_base + L_KO + 2 * g * 576 + (16 * w + (n & 8) + 4 * (n & 1) + ((n >> 1) & 3)) * 4;
        R0[10] = lds_base + L_Q + ai * 320 + g * 16;
        R0[11] = lds_base + L_KO + ai * 576 + g * 32 + ahf * 16;
        R0[12] = lds_base + L_KX + 8192 + w * 2048 + (g >> 1) * 256 + n * 16 + (g & 1) * 8;
        R0[13] = lds_base + L_W + w * 4096 + lane * 16;
        R0[14] = part_unit((2 * (4 * tw + w) + (n >> 3)) * 4 + g, h) + (n & 7) * 16;
        R0[15] = (g >> 1) == (w >> 1) ? part_unit(64 + 2 * (2 * tw + (w & 1)) + (g >> 1), h) + (g & 1) * 64 + n * 4 : OOB;
        asm volatile("" : "+{v[120:151]}"(R0), "+{v[152:183]}"(R1), "+{v[184:215]}"(R2), "+{v[216:247]}"(R3), "+{v[248:255]}"(R4));
    }
    TT(true, 7)
    const bool gv_on = tw == 0; (void)gv_on;
    const int sel = wait_init(rw, tag, budget, 33 + 4 * h + w) ^ a.c1 >> 24;
    R0[14] ^= (sel & 8) * 256; R0[15] ^= (sel & 8) * 256;
    TT(true, 8)
    int kv_rdy = 0;
    const rsrc_t rs = __builtin_amdgcn_make_buffer_rsrc(ws + (sel & 1) * 8192, 0, COMM_BYTES - 8192, 0x00020000);
    auto dma = [&](int voff, int soff, unsigned dst) { bdma(rw, voff, soff, dst); };
    auto fetch_kv = [&](int s, int lane) {
        const int n = lane & 15, g = lane >> 4;
        const int kxs = (g >> 1) * 1024 + ((g & 1) * 32 + 16 * (n >> 3) + (n & 7)) * 16;
        const int vxs = (g >> 1) * 1024 + ((g & 1) * 32 + n) * 16;
        const int so_ = (s * 32 + h) * 8192 + w * 2048;
        const unsigned xdst = lds_base + L_KX + w * 2048;
        dma(kxs, OFF_KF + so_, xdst); dma(kxs, OFF_KF + so_ + 128, xdst + 1024);
        dma(vxs, OFF_VF + so_, xdst + 8192); dma(vxs, OFF_VF + so_ + 256, xdst + 8192 + 1024);
    };
    const int fo = F_KVM + h * 256 + tw * 128;
    u32x8 scon;
    scon[0] = w; scon[1] = 2 * OFF_CAN + (h >> 1) * 512 + (2 * (h & 1) + tw) * 16; scon[2] = F_KV + h * 512 + w * 128; scon[3] = 65536 + w * 4096;
    scon[4] = OFF_KF + h * 8192 + w * 2048; scon[5] = fo; scon[6] = tw - 1; scon[7] = (tw ? OFF_VM : OFF_KM) + (h - NH) * 2048 + w * 512;
    u32x8 S1 = {0u, 0u, (unsigned)kv_rdy, (unsigned)budget, (unsigned)a.d0, 0xFFFFFFFFu / 22, (unsigned)(a.c2 + a.b0), 0u};
    u32x8 S2 = {0u, 0u, 0u, (unsigned)a.up, (unsigned)a.dn, (unsigned)a.c2, 0u, 1u};
    fetch_kv(0, opaque_lane());
    __builtin_amdgcn_raw_buffer_store_b64((u32x2){8 * now() + a.e0, 1u | (a.c2 + a.b0) << 16}, rs, tid < NCP ? tid * 128 : OOB, scon[1], 16);
    asm volatile("s_waitcnt vmcnt(1)" ::: "memory");
    TT(true, 9)
    u32x2 vm = {};
    u32x8 S3;
    {
        const unsigned t = now(), c = (unsigned)__builtin_readcyclecounter();
        const float r = (float)(c - cbase) * __builtin_amdgcn_rcpf(8.0f * (float)(t - tbase));
        S2[1] = 8 * t + 16; S1[5] = (unsigned)(4294967296.0f * __builtin_amdgcn_rcpf(r));
        S3 = (u32x8){c, (unsigned)(4096.0f * r), 2u * OFF_MEMB + ((h >> 3 ^ sel >> 2) & 1) * 16384 + (w & 2) * 2048 + (~sel & 8) * 256 + (w & 1) * 1024, w == 3 ? ~0u : 0u};
    }
    const int U0 = (2 * tw + (w >> 1)) * 64 + 8 * (w & 1), U1 = 4 * (2 * h + tw) + w;
    for (;;) {
        asm volatile(STEP_ASM
            : "+{v[96:97]}"(vm), "+{v[0:7]}"(S1), "+{v[8:15]}"(S2), "+{v[16:23]}"(S3), "+{v[24:31]}"(scon)
              , "+{a[0:31]}"(A0), "+{a[32:63]}"(A1), "+{a[64:95]}"(A2), "+{a[96:127]}"(A3), "+{a[128:159]}"(A4), "+{a[160:191]}"(A5), "+{a[192:223]}"(A6), "+{v[120:151]}"(R0), "+{v[152:183]}"(R1)
            : "{a[224:255]}"(A7), "{v[184:215]}"(R2), "{v[216:247]}"(R3), "{v[248:255]}"(R4),
              [rs] "s"(rs), [rw] "s"(rw), [qb] "s"(OFF_OF + LATE * 262144 + h * 8192 + (w & 1) * 256),
              [tp] "s"(tp_unit(U0, sel) + h * 128), [tq] "s"(tp_unit(256 + U0, sel) + h * 128), [tx] "s"(tp_unit(512 + (U0 >> 3) + 3 * (w & 1), sel) + h * 128),
              [u] "s"(U1), [ua] "s"(tp_unit(256 + U1, sel)), [fw] "s"(8 * (a.xb >> 16 & 255)), [ru] "s"(__builtin_amdgcn_make_buffer_rsrc(a.out, 0, SEQ * DM * 2, 0x00020000))
            : STEP_CLOBBER);
        const int s = __builtin_amdgcn_readfirstlane(S1[0]);
        const unsigned st = __builtin_amdgcn_readfirstlane(S1[7]);
        kv_rdy = __builtin_amdgcn_readfirstlane(S1[2]); budget = __builtin_amdgcn_readfirstlane(S1[3]);
        if (s == NSEG - 1) break;
        if (__builtin_expect(st & 2, 0)) {
            const int lane = opaque_lane();
            LAS char* z = (LAS char*)0;
            const f32x4 zero = {0, 0, 0, 0};
            const u32x4 ones = __builtin_shufflevector(R0, R0, 0, 1, 2, 3);
            const unsigned zr = lane >> 6;
            const int idx = R0[4];
            u32x4 qf[4];
            f32x4 s1 = zero, s2 = zero;
#pragma unroll
            for (int ks = 0; ks < 4; ++ks) {
                qf[ks] = *(LAS u32x4*)(z + R0[10] + ks * 64);
                s1 = MFV(*(LAS u32x4*)(lds + L_KX + lane * 16 + ks * 2048), qf[ks], s1);
                s2 = MFV(*(LAS u32x4*)(lds + L_KX + lane * 16 + ks * 2048 + 1024), qf[ks], s2);
            }
            f32x4 s0 = SMF64(*(LAS u32x4*)(z + R0[11]), __builtin_shufflevector(qf[0], qf[1], 0, 1, 2, 3, 4, 5, 6, 7), zero, idx);
            s0 = SMF64(*(LAS u32x4*)(z + R0[11] + 128), __builtin_shufflevector(qf[2], qf[3], 0, 1, 2, 3, 4, 5, 6, 7), s0, idx);
            const float sa = s0[0] + s0[2], sb = s0[1] + s0[3];
            float mx = fmaxf(fmaxf(fmaxf(fmaxf(s1[0], s1[1]), fmaxf(s1[2], s1[3])), fmaxf(fmaxf(s2[0], s2[1]), fmaxf(s2[2], s2[3]))), fmaxf(sa, sb));
            mx = fmaxf(mx, __shfl_xor(mx, 16)); mx = fmaxf(mx, __shfl_xor(mx, 32));
            auto ex2 = [&](float x, float y) { return pack2(__builtin_amdgcn_exp2f(x - mx), __builtin_amdgcn_exp2f(y - mx)); };
            const u32x4 px = {ex2(s1[0], s1[1]), ex2(s1[2], s1[3]), ex2(s2[0], s2[1]), ex2(s2[2], s2[3])};
            const unsigned pm = ex2(sa, sb);
            const f32x4 l = MFV(((u32x4){pm, zr, zr, zr}), ones, MFV(px, ones, zero));
            const u32x2 x0 = *(LAS u32x2*)(z + R0[12]), y0 = *(LAS u32x2*)(z + R0[12] + 1024), x1 = *(LAS u32x2*)(z + R0[12] + 512), y1 = *(LAS u32x2*)(z + R0[12] + 1536);
            const f32x4 o = SMF64(px, ((u32x8){x0.x, y0.x, x0.y, y0.y, x1.x, y1.x, x1.y, y1.y}), zero, idx) + SMF32(((u32x2){pm, zr}), ((u32x4){vm.x, vm.y, zr, zr}), zero, idx);
            const float i0 = __builtin_amdgcn_rcpf(l[0]), i1 = __builtin_amdgcn_rcpf(l[1]);
            *(LAS unsigned*)(z + R0[9] + 256) = pack2(o[0] * i0, o[2] * i0);
            *(LAS unsigned*)(z + R0[9] + 256 + 576) = pack2(o[1] * i1, o[3] * i1);
            WG_BARRIER();
            const u32x4 oa = *(LAS u32x4*)(z + R0[11] + 256 + (w >> 1) * 128), ob = *(LAS u32x4*)(z + R0[11] + 384 - (w >> 1) * 128);
            auto frag = [&](int at) { return __builtin_shufflevector(*(LAS u32x4*)(z + at), *(LAS u32x4*)(z + at + 1024), 0, 1, 2, 3, 4, 5, 6, 7); };
            f32x2 f[4];
#pragma unroll
            for (int T = 0; T < 4; ++T) f[T] = fold(SMF64(ob, frag(R0[13] + scon[3] + 32768 + T * 2048), SMF64(oa, frag(R0[13] + scon[3] + T * 2048), zero, idx), idx));
            const f32x2 fx = fold(SMF64(ob, frag((R0[13] ^ 8192) + 49152 + 2048), SMF64(oa, frag(R0[13] + 49152 + 2048), zero, idx), idx));
            const int pb = (s & 1) * 2 * PART_STEP;
            st128((u32x4){pack2(f[0].x, f[1].x), pack2(f[2].x, f[3].x), pack2(f[0].y, f[1].y), pack2(f[2].y, f[3].y)}, rs, R0[14], pb);
            st32(pack2(fx.x, fx.y), rs, R0[15], pb);
            if (s == LATE) { S1[7] = 4; continue; }
        }
        if (__builtin_expect((st & 2) || kv_rdy <= s, 0)) {
            while (kv_rdy <= s) { kv_rdy = prog_min(rw, F_KV + h * 512 + w * 128); if (--budget < 0) break; }
            fetch_kv(s + 1, opaque_lane());
        }
        if (st & 2) { S1[1] = read_canaries(s, rs, opaque_lane(), budget) + S1[6]; S2[2] += 0x10000; }
        if ((int)(S1[1] - S2[1]) > 0x8000) { S2[1] = 8 * now() + 16; S3[0] = (unsigned)__builtin_readcyclecounter(); }
        S1[0] = s + 1; S1[2] = kv_rdy; S1[3] = budget;
    }
    TT(true, 0)
    st32((unsigned)(NSEG - 2), rw, opaque_lane() == 0 && w == 0 ? fo : OOB, 0);
    TT(true, 2)
}

__global__ void __launch_bounds__(256, 1) fused_kernel(Args a) {
    extern __shared__ __attribute__((aligned(16))) char smem[];
    LAS char* lds = (LAS char*)smem;
    const unsigned lds_base = (unsigned)(uintptr_t)lds;
    const int w = __builtin_amdgcn_readfirstlane(threadIdx.x >> 6);
    const int b = blockIdx.x, bx = b & 7, bi = b >> 3;
    if (bi >= 28) {
        const void* pc; unsigned len;
        asm volatile("s_getpc_b64 %0\n1:\ts_mov_b32 %1, .Lstep_end-1b" : "=s"(pc), "=s"(len));
        touch(pc, min((bi - 28) * 32768 + threadIdx.x * 128, len), lds_base);
    }
    asm volatile("" :: "s"(a.Wq), "s"(a.cw));
    const rsrc_t rw = __builtin_amdgcn_make_buffer_rsrc(a.ws, 0, WS_USED, 0x00020000);
    int budget = SPIN_MAX;
#define RT(i)
    if (bx < 4) {
        if (bi < 16) role_head(a, lds, lds_base, 8 * bx + (bi >> 2) * 2 + (bi & 1), rw, budget);
        else role_owner(a, lds_base, 4 * (bi - 16) + bx, rw, budget);
    } else {
        const int fx = bx - 4, no = fx < 3 ? 14 : 22, j = 14 * fx + bi;
        if (bi >= no || (j & 2)) {
            const unsigned t = now() + (a.d1 & 0xffff);
            if (bi >= no) { if (w) prefetch(a, lds_base, bx, (bi - no) * 3 + w - 1, 3 * (32 - no)); }
            else {
                const int sh = (j >> 2) * 2 + (j & 1);
#pragma unroll
                for (int i = 0; i < INIT_SHARE / 4096; ++i) st128((u32x4){SENT, SENT, SENT, SENT}, rw, threadIdx.x * 16 + i * 4096, sh * INIT_SHARE);
#pragma unroll
                for (int i = 0; i < 8; ++i) st128((u32x4){SENT, SENT, SENT, SENT}, rw, threadIdx.x * 16, i < 4 ? (i & 2 ? OFF_VM : OFF_KM) + FAST * 65536 + (2 * sh + (i & 1)) * 4096 : OFF_OF + LATE * 262144 + (4 * sh + i - 4) * 4096);
                const u32x2 tag = launch_tag(a);
                VM_DRAIN();
                WG_BARRIER();
                __builtin_amdgcn_raw_buffer_store_b64(tag, rw, threadIdx.x == 0 ? F_INIT + sh * 128 : OOB, 0, 16);
                RT(3)
            }
            (void)wait_until(t);
        }
        if (bi >= no) {
            const int t = 18 * fx + bi - no;
            role_s4d(a, lds, lds_base, t, rw, budget);
            RT(1)
            TT(true, 6)
            if (t < 9 * OUT_REP) role_outp(a, lds, lds_base, t % 9, t / 9, rw, budget);
        }
        else {
            if (!(j & 2)) role_kv(a, lds, lds_base, (j >> 2) * 2 + (j & 1), rw, budget);
            RT(1)
            TT(true, 6)
            role_qa(a, lds, lds_base, (j >> 2) * 2, (j & 2 ? 0 : FAST + 2) + (j & 1), (j & 2 ? FAST : FAST + 2) + (j & 1), rw, budget);
        }
    }
    RT(2)
    TT(true, 5)
    if (budget < 0 && threadIdx.x == 0) st32(0x1000u + b, rw, F_ERR, 0);
}

static int g_c1 = 34, g_c1m = 30, g_c1x = 46, g_c2 = 316, g_b0 = 32, g_up = 8, g_fl = 8, g_dn = 2, g_d0 = 1680, g_da = 100, g_ua = 48, g_d1 = 300, g_rd = 34, g_cw = 69, g_e0 = 190, g_x1 = 40, g_x2 = 140, g_x3 = 150, g_x4 = 75, g_x5 = 135, g_x6 = 157, g_x7 = 35, g_x8 = 210, g_pc = 45, g_cc = 0;
extern "C" void kernel_launch(void* const* d_in, const int* in_sizes, int n_in, void* d_out, int out_size, void* d_ws, size_t ws_size, hipStream_t stream) {
    Args a;
    a.x = (const u16*)d_in[0]; a.mem0 = (const u16*)d_in[1];
    a.Wq = (const u16*)d_in[2]; a.Wk = (const u16*)d_in[3]; a.Wv = (const u16*)d_in[4]; a.Wo = (const u16*)d_in[5];
    a.lAr = (const u16*)d_in[8]; a.Aim = (const u16*)d_in[9]; a.Cr = (const u16*)d_in[10]; a.Ci = (const u16*)d_in[11];
    a.ldt = (const u16*)d_in[12]; a.Dsk = (const u16*)d_in[13]; a.gw = (const u16*)d_in[14];
    a.out = (u16*)d_out;
    a.ws = (char*)(((uintptr_t)d_ws + ((2u << 20) - 1)) & ~(uintptr_t)((2u << 20) - 1));
    a.c1 = g_c1 | g_c1m << 8 | g_c1x << 16 | g_cc << 24; a.c2 = g_c2; a.d0 = g_d0 + g_da; a.b0 = g_b0; a.d1 = g_d1 | g_pc << 16; a.up = g_up | g_ua << 8 | g_fl << 16; a.dn = g_dn | g_d0 << 8; a.rd = 23 * g_rd - 300; a.cw = 8 * g_cw - 104; a.e0 = 8 * g_e0;
    a.xa = g_x1 | g_x2 << 8 | g_x5 << 16 | g_x7 << 24; a.xb = g_x6 | g_x3 << 8 | g_x4 << 16 | g_x8 << 24;
    (void)hipFuncSetAttribute((const void*)fused_kernel, hipFuncAttributeMaxDynamicSharedMemorySize, L_TOTAL);
    fused_kernel<<<256, 256, L_TOTAL, stream>>>(a);
}
```

```cpp
#include <hip/hip_runtime.h>
#include <hip/hip_bf16.h>
#include <stdint.h>

typedef unsigned short u16;
typedef __bf16 bf16x8 __attribute__((ext_vector_type(8)));
typedef __bf16 bf16x16 __attribute__((ext_vector_type(16)));
typedef _Float16 f16x2 __attribute__((ext_vector_type(2)));
typedef _Float16 f16x8 __attribute__((ext_vector_type(8)));
typedef short s16x4 __attribute__((ext_vector_type(4)));
typedef float f32x2 __attribute__((ext_vector_type(2)));
typedef float f32x4 __attribute__((ext_vector_type(4)));
typedef float f32x16 __attribute__((ext_vector_type(16)));
typedef unsigned u32x2 __attribute__((ext_vector_type(2)));
typedef unsigned u32x4 __attribute__((ext_vector_type(4)));
typedef unsigned u32x8 __attribute__((ext_vector_type(8)));
typedef unsigned u32x16 __attribute__((ext_vector_type(16)));
typedef unsigned u32x32 __attribute__((ext_vector_type(32)));
typedef short s16x8 __attribute__((ext_vector_type(8)));
#define LAS __attribute__((address_space(3)))
#define DI __device__ __forceinline__
typedef __amdgpu_buffer_rsrc_t rsrc_t;

constexpr int SEQ = 2048, DM = 576, NH = 32, DH = 128, HD = NH * DH, NM = 8, SEG = 32, NSEG = 64, NS = 128;
constexpr unsigned SENT = 0xFFFFFFFFu;
constexpr unsigned OOB = 0x7FFFFFF0u;
constexpr int SPIN_MAX = 1 << 20;

constexpr int PART_STEP = 16384;
constexpr int OFF_MEMB = 40 * PART_STEP;
constexpr int MEMB_STRIDE = 32768;
constexpr int OFF_CAN = 52 * PART_STEP;
constexpr int COMM_LOGICAL = OFF_CAN + 32768;
constexpr int COMM_BYTES = 2 * COMM_LOGICAL;
constexpr int F_ERR = COMM_BYTES;
constexpr int F_KV = F_ERR + 128;
constexpr int F_KVM = F_KV + 16384;
constexpr int F_O = F_KVM + 8192;
constexpr int F_H = F_O + 4096;
constexpr int F_S4 = F_H + 32768;
constexpr int F_DONE = F_S4 + 512;
constexpr int INIT_SHARE = 14 * 4096, INIT_BYTES = 32 * INIT_SHARE;
constexpr int F_INIT = INIT_BYTES;
constexpr int F_CL = F_INIT + 24576;
constexpr int F_EPOCH = F_CL + 16384;
static_assert(F_DONE + 128 <= INIT_BYTES, "");
static_assert(F_EPOCH + 128 <= (2 << 20), "hand-off region");
constexpr int OFF_TP = 2 << 20;
constexpr int OFF_KF = 6 << 20;
constexpr int OFF_VF = OFF_KF + SEQ * HD * 2;
constexpr int OFF_OF = OFF_VF + SEQ * HD * 2;
constexpr int OFF_KM = OFF_OF + SEQ * HD * 2;
constexpr int OFF_VM = OFF_KM + NSEG * NH * NM * DH * 2;
constexpr int FAST = NSEG - 6;
constexpr int LATE = NSEG - 2;
constexpr int OFF_BO = OFF_VM + NSEG * NH * NM * DH * 2;
constexpr int BO_ROW = 4352;
constexpr int OFF_GT = OFF_BO + DM * BO_ROW;
constexpr int OFF_DBG = OFF_GT + SEQ * 4;
constexpr int WS_USED = OFF_DBG + (4 << 20);

struct Args {
    const u16 *x, *mem0, *Wq, *Wk, *Wv, *Wo, *lAr, *Aim, *Cr, *Ci, *ldt, *Dsk, *gw;
    u16* out; char* ws;
    int c1, c2, d0, b0, d1, up, dn, rd, cw, e0;
    unsigned xa, xb;
};

DI float bf2f(u16 b) { return __uint_as_float((unsigned)b << 16); }
DI float bflo(unsigned w) { return __uint_as_float(w << 16); }
DI float bfhi(unsigned w) { return __uint_as_float(w & 0xFFFF0000u); }
DI u16 f2bf(float f) { return __builtin_bit_cast(u16, (__bf16)f); }
DI unsigned pack2(float lo, float hi) {
    typedef __bf16 bf16x2 __attribute__((ext_vector_type(2)));
    bf16x2 v = {(__bf16)lo, (__bf16)hi};
    return __builtin_bit_cast(unsigned, v);
}
#define WG_BARRIER() asm volatile("s_waitcnt lgkmcnt(0)\n\ts_barrier" ::: "memory")
#define VM_DRAIN() asm volatile("s_waitcnt vmcnt(0)" ::: "memory")
DI int cmap(int o) { return (o & 0x1FFF) | ((o >> 13) << 14); }
DI int part_unit(int G, int h) { const int p = G < 64 ? G & 31 : 32 + (G & 3); return (p >> 2) * 131072 + cmap((p & 3) * 4096) + ((G >> (G < 64 ? 5 : 2) ^ p >> 3) & 1) * 2048 + (h >> 4) * 65536 + (h & 15) * 128; }
DI unsigned mx4(unsigned a, u32x4 v) { a = max(max(a, v.x), v.y); return max(max(a, v.z), v.w); }
DI unsigned now() { return (unsigned)__builtin_amdgcn_s_memrealtime(); }
DI unsigned wait_until(unsigned t) {
    unsigned v; int guard = 8192;
    do { v = now(); } while ((int)(t - v) >= 4 && --guard > 0);
    const int r = __builtin_amdgcn_readfirstlane((int)(t - v));
    asm volatile("s_cmp_lt_i32 %0, 1\n\ts_cbranch_scc1 1f\n\ts_nop 3\n\ts_cmp_lt_i32 %0, 2\n\ts_cbranch_scc1 1f\n\ts_nop 3\n\ts_cmp_lt_i32 %0, 3\n\ts_cbranch_scc1 1f\n\ts_nop 7\n1:" :: "s"(r) : "scc");
    return r > 0 ? t : v + 2;
}
DI void st128(u32x4 v, rsrc_t rs, int off, int soff) {
    __builtin_amdgcn_raw_buffer_store_b128(v, rs, off, soff, 16);
    asm volatile("s_nop 1" :: "v"(v) : "memory");
}
DI void st32(unsigned v, rsrc_t rs, int off, int soff) { __builtin_amdgcn_raw_buffer_store_b32(v, rs, off, soff, 16); }
DI u32x4 ld128(rsrc_t rs, int off, int soff) { return __builtin_amdgcn_raw_buffer_load_b128(rs, off, soff, 16); }
DI u32x2 ld64(rsrc_t rs, int off, int soff) { return __builtin_amdgcn_raw_buffer_load_b64(rs, off, soff, 16); }
DI unsigned ld32(rsrc_t rs, int off, int soff) { return __builtin_amdgcn_raw_buffer_load_b32(rs, off, soff, 16); }
DI f32x2 fold(f32x4 a) { return (f32x2){a[0], a[1]} + (f32x2){a[2], a[3]}; }
DI float dpp_xor1(float v) { return __int_as_float(__builtin_amdgcn_update_dpp(0, __float_as_int(v), 0xB1, 0xf, 0xf, true)); }
DI float red_sum_g(float v) {
    auto a = __builtin_amdgcn_permlane16_swap(__float_as_uint(v), __float_as_uint(v), false, false);
    v = __uint_as_float(a[0]) + __uint_as_float(a[1]);
    auto b = __builtin_amdgcn_permlane32_swap(__float_as_uint(v), __float_as_uint(v), false, false);
    return __uint_as_float(b[0]) + __uint_as_float(b[1]);
}
DI int wave_max32(int d) {
    d = max(d, __builtin_amdgcn_update_dpp(d, d, 0x121, 0xf, 0xf, false));
    d = max(d, __builtin_amdgcn_update_dpp(d, d, 0x122, 0xf, 0xf, false));
    d = max(d, __builtin_amdgcn_update_dpp(d, d, 0x124, 0xf, 0xf, false));
    d = max(d, __builtin_amdgcn_update_dpp(d, d, 0x128, 0xf, 0xf, false));
    return __builtin_amdgcn_readfirstlane(max(max(__builtin_amdgcn_readlane(d, 0), __builtin_amdgcn_readlane(d, 16)), max(__builtin_amdgcn_readlane(d, 32), __builtin_amdgcn_readlane(d, 48))));
}
DI u32x2 tr_read(LAS char* lds, int addr) { return __builtin_bit_cast(u32x2, __builtin_amdgcn_ds_read_tr16_b64_v4i16((LAS s16x4*)(lds + addr))); }
DI void wait_prog(rsrc_t rw, int off, bool act, int thr, int& budget) {
    for (;;) {
        const int v = (int)ld32(rw, off, 0);
        asm volatile("" ::: "memory");
        if (__all(!act || v >= thr)) break;
        if (--budget < 0) break;
        __builtin_amdgcn_s_sleep(1);
    }
}
DI u32x2 launch_tag(const Args& a) {
    unsigned long long z;
    asm volatile("s_load_dwordx2 %0, %1, 0x0\n\ts_waitcnt lgkmcnt(0)" : "=s"(z) : "s"(a.ws + F_EPOCH) : "memory");
    z += 0x9E3779B97F4A7C15ull;
    z = (z ^ (z >> 30)) * 0xBF58476D1CE4E5B9ull; z = (z ^ (z >> 27)) * 0x94D049BB133111EBull; z ^= z >> 31;
    return (u32x2){(unsigned)z, (unsigned)(z >> 32)};
}
DI int wait_init(rsrc_t rw, u32x2 tag, int& budget, int more = 0) {
    const int l = threadIdx.x & 63, o = F_INIT + (l < 32 ? l : l < 62 || !more ? more : l == 63 ? 32 : 161) * 128;
    u32x2 v;
    for (;;) {
        v = ld64(rw, o, 0);
        asm volatile("" ::: "memory");
        if (__all(((v.x ^ tag.x) >> 3) == 0 && v.y == tag.y)) break;
        if (--budget < 0) break;
        __builtin_amdgcn_s_sleep(2);
    }
    return (__builtin_amdgcn_readlane(v.x, 63) & 7) + 8 * (__builtin_amdgcn_readlane(v.x, 62) & 7);
}
DI unsigned die_side() { return (__builtin_amdgcn_s_getreg(4 | 14 << 6) ^ blockIdx.x >> 1) & 1; }
DI int side_at(int x) { const int b = blockIdx.x ^ x; return F_CL + ((b >> 3) * 4 + (b & 3)) * 128; }
DI void tell_side(rsrc_t rw, u32x2 tag) { __builtin_amdgcn_raw_buffer_store_b64((u32x2){(tag.x & ~1u) | die_side(), tag.y}, rw, threadIdx.x == 0 ? 0 : OOB, side_at(0), 16); }
DI int agree_side(rsrc_t rw, u32x2 tag, u32x2 v, int& budget) {
    while (!__all(((v.x ^ tag.x) >> 1) == 0 && v.y == tag.y) && --budget >= 0) { v = ld64(rw, 0, side_at(16)); asm volatile("" ::: "memory"); }
    const unsigned me = die_side();
    return (__builtin_amdgcn_readfirstlane(v.x) ^ me) & 1 ? me : blockIdx.x >> 4 & 1;
}
DI void glds16(const void* base, int voff, unsigned lds_dst) {
    unsigned keep;
    asm volatile("s_mov_b32 %0, m0\n\ts_mov_b32 m0, %3\n\ts_nop 0\n\tglobal_load_lds_dwordx4 %1, %2\n\ts_mov_b32 m0, %0" : "=&s"(keep) : "v"(voff), "s"(base), "s"(lds_dst) : "memory");
}
DI void bdma(rsrc_t rs, int voff, int soff, unsigned lds_dst) {
    unsigned keep;
    asm volatile("s_mov_b32 %0, m0\n\ts_mov_b32 m0, %4\n\ts_nop 0\n\tbuffer_load_dwordx4 %1, %2, %3 offen sc1 lds\n\ts_mov_b32 m0, %0" : "=&s"(keep) : "v"(voff), "s"(rs), "s"(soff), "s"(lds_dst) : "memory");
}
DI int swz(int r) { return ((r & 3) ^ (((r >> 2) & 3) << 1) ^ (((r >> 4) & 1) << 2)) & 7; }
template <class F> DI u32x4 ring_offs(int w, int lane, F at) {
    u32x4 o;
#pragma unroll
    for (int j = 0; j < 4; ++j) { const int r = 16 * w + 4 * j + (lane >> 4); o[j] = at(r, (lane & 15) ^ (2 * swz(r))) - 1024 * j; }
    return o;
}
DI u32x4 ring_offs(int w, int lane, int ld) { return ring_offs(w, lane, [&](int r, int c) { return r * ld * 2 + c * 16; }); }
DI void ring_issue(const u16* src, u32x4 o, unsigned slot) {
    unsigned keep;
    asm volatile("s_mov_b32 %0, m0\n\ts_mov_b32 m0, %6\n\ts_nop 0\n\tglobal_load_lds_dwordx4 %1, %5\n\tglobal_load_lds_dwordx4 %2, %5 offset:1024\n\t"
                 "global_load_lds_dwordx4 %3, %5 offset:2048\n\tglobal_load_lds_dwordx4 %4, %5 offset:3072\n\ts_mov_b32 m0, %0"
                 : "=&s"(keep) : "v"(o.x), "v"(o.y), "v"(o.z), "v"(o.w), "s"(src), "s"(slot) : "memory");
}
#define PF_FIRST 2
constexpr int L_TOTAL = 163840, L_SINK = L_TOTAL - 256;
DI void touch(const void* base, int voff, unsigned lds_base) {
    unsigned keep;
    asm volatile("s_mov_b32 %0, m0\n\ts_mov_b32 m0, %3\n\ts_nop 0\n\tglobal_load_lds_dword %1, %2\n\ts_mov_b32 m0, %0" : "=&s"(keep) : "v"(voff), "s"(base), "s"(lds_base + L_SINK) : "memory");
}
DI void prefetch(const Args& a, unsigned lds_base, int x, int id, int n) {
    const int lane = threadIdx.x & 63;
    const int h0 = x < 4 ? 8 * x : (0x160E0800 >> (8 * (x - 4))) & 255, lpr = x == 5 ? 12 : x == 7 ? 20 : 16, per = 9 * lpr;
    const int first = PF_FIRST * lpr, end = (x < 4 ? 4 : 2) * per;
    const float rl = __builtin_amdgcn_rcpf((float)lpr);
    for (int i = first + id; i < end; i += n) {
        int m = (i >= per) + (i >= 2 * per) + (i >= 3 * per), j = i - m * per;
        if (x < 4 && m < 3) { const int b = i >> 4; m = b < 20 ? b >> 3 : min(b - 20, 2); j = (b < 16 ? b & 7 : b < 20 ? b - 12 : b < 23 ? 8 : b - 23) * 16 + (i & 15); }
        const int mm = m + (x >= 4);
        const int L = j * 64 + lane, row = (int)((L + 0.5f) * rl);
        touch(mm == 0 ? a.Wq : mm == 1 ? a.Wk : mm == 2 ? a.Wv : a.Wo, m == 3 ? h0 * (DH * DM * 2) + L * 128 : row * (HD * 2) + (L - row * lpr) * 128 + h0 * 256, lds_base);
    }
}
template <int N> DI void ring_wait() { asm volatile("s_waitcnt vmcnt(%0) lgkmcnt(0)\n\ts_barrier" :: "i"(N) : "memory"); }
template <int I> struct ic_t { static constexpr int value = I; };
template <int N, int I = 0, class F> DI void static_for(F&& f) { if constexpr (I < N) { f(ic_t<I>{}); static_for<N, I + 1>(f); } }
#define MFMA32(a, b, c) __builtin_amdgcn_mfma_f32_32x32x16_bf16(__builtin_bit_cast(bf16x8, a), __builtin_bit_cast(bf16x8, b), c, 0, 0, 0)
#define MFMA16(a, b, c) __builtin_amdgcn_mfma_f32_16x16x32_bf16(__builtin_bit_cast(bf16x8, a), __builtin_bit_cast(bf16x8, b), c, 0, 0, 0)

constexpr int S_CH = 9;
constexpr int S_XRS = 4096 + 32;
constexpr int S_XR = 0;
constexpr int S_RC = S_CH * S_XRS;
constexpr int S_RCS = 4192;
constexpr int S_KD = S_RC + 4 * S_RCS;
constexpr int S_K = S_KD + 48 * 128;
constexpr int S_KROW = 96;
constexpr int S_TAB = S_K + 79 * S_KROW;
constexpr int S_TROW = 544;
constexpr int S_RED = S_TAB + 57 * S_TROW;
constexpr int S_TOTAL = S_RED + 4 * 64 * 144;
static_assert(S_RC % 16 == 0 && S_TAB % 16 == 0 && S_RED % 16 == 0, "");
DI f32x2 crot(f32x2 z) { return (f32x2){-z.y, z.x}; }
DI f32x2 cmul(f32x2 a, f32x2 z) { return __builtin_elementwise_fma(a.yy, crot(z), a.xx * z); }
DI f32x2 cexp_p(float ar, float ai, float p) {
    const float x = ai * p;
    const float hi = x * 0.15915494309189535f;
    const float lo = __builtin_fmaf(x, 0.15915494309189535f, -hi) + x * 6.420638316725915e-9f;
    const float rev = (hi - __builtin_rintf(hi)) + lo;
    const float m = __builtin_amdgcn_exp2f(ar * p * 1.4426950408889634f);
    return (f32x2){m * __builtin_amdgcn_cosf(rev), m * __builtin_amdgcn_sinf(rev)};
}
#define FENCE() __builtin_amdgcn_sched_barrier(0)
#define TT(c, i)
DI void role_s4d(const Args& a, LAS char* lds, unsigned lds_base, int task, rsrc_t rw, int& budget) {
    const int tid = threadIdx.x, lane = tid & 63, w = __builtin_amdgcn_readfirstlane(tid >> 6);
    const int d0 = task * S_CH;
    touch(w == 0 ? a.lAr : w == 1 ? a.Aim : w == 2 ? a.Cr : a.Ci, min(d0 * (NS * 2) + min(lane, 2 * S_CH + 1) * 128, DM * NS * 2 - 4), lds_base);
    touch(w & 1 ? a.ldt : a.Dsk, min(lane * 128, DM * 2 - 4), lds_base);
#define S4S(i)
    {
        const rsrc_t rx = __builtin_amdgcn_make_buffer_rsrc((void*)a.x, 0, SEQ * DM * 2, 0x00020000);
        const unsigned sh = (d0 & 1) * 16;
        u32x4 v[8]; unsigned e[8];
#pragma unroll
        for (int i = 0; i < 8; ++i) {
            const int o = (tid + 256 * i) * (DM * 2) + (d0 & ~1) * 2;
            v[i] = __builtin_amdgcn_raw_buffer_load_b128(rx, o, 0, 0); e[i] = __builtin_amdgcn_raw_buffer_load_b32(rx, o, 16, 0);
        }
#pragma unroll
        for (int p = 0; p < 2; ++p) {
            const int tok = 32 * task + 8 * w + 4 * p + (lane >> 4);
            float acc = 0.f;
#pragma unroll
            for (int i = 0; i < 5; ++i) {
                const int ch = (lane & 15) + 16 * i;
                const u32x4 gv = ch < 72 ? *(const u32x4*)(a.gw + ch * 8) : (u32x4){0u, 0u, 0u, 0u};
                const u32x4 xv = *(const u32x4*)(a.x + (size_t)tok * DM + min(ch, 71) * 8);
#pragma unroll
                for (int j = 0; j < 4; ++j) acc += bflo(xv[j]) * bflo(gv[j]) + bfhi(xv[j]) * bfhi(gv[j]);
            }
            acc += __shfl_xor(acc, 1); acc += __shfl_xor(acc, 2); acc += __shfl_xor(acc, 4); acc += __shfl_xor(acc, 8);
            st32(__float_as_uint(1.0f / (1.0f + __expf(-acc))), rw, (lane & 15) == 0 ? OFF_GT + tok * 4 : OOB, 0);
        }
#pragma unroll
        for (int i = 0; i < 8; ++i) {
            const unsigned x5[5] = {__builtin_amdgcn_alignbit(v[i].y, v[i].x, sh), __builtin_amdgcn_alignbit(v[i].z, v[i].y, sh), __builtin_amdgcn_alignbit(v[i].w, v[i].z, sh),
                                    __builtin_amdgcn_alignbit(e[i], v[i].w, sh), e[i] >> sh};
#pragma unroll
            for (int c = 0; c < S_CH; ++c) *(LAS u16*)(lds + S_XR + c * S_XRS + (2047 - tid - 256 * i) * 2) = (u16)(x5[c >> 1] >> (16 * (c & 1)));
        }
    }
    if (tid < 8 * S_CH) *(LAS unsigned*)(lds + S_XR + (tid >> 3) * S_XRS + 4096 + (tid & 7) * 4) = 0;
    if (tid < 64) *(LAS unsigned*)(lds + S_RC + (tid >> 4) * S_RCS + 4096 + (tid & 15) * 4) = 0;
    for (int i = tid; i < 15 * S_KROW / 4; i += 256) *(LAS unsigned*)(lds + S_K + i * 4) = 0;
    int kd[8];
#pragma unroll
    for (int j = 0; j < 8; ++j) {
        const int p = (int)bf2f(f2bf((float)(8 * tid + j))), k = p >> 7, r = p & 7;
        const int row = p < 256 ? 8 * k + r : (p < 512 ? 8 + 4 * k + (r >> 1) : (p < 1024 ? 16 + 2 * k + (r >> 2) : 24 + k));
        kd[j] = S_KD + (row * 16 + ((p >> 3) & 15)) * 8;
    }
    const int n = tid & 127, nn = lane & 15, g = lane >> 4;
    u16 pA = a.lAr[d0 * NS + n], pI = a.Aim[d0 * NS + n], pCr = a.Cr[d0 * NS + n], pCi = a.Ci[d0 * NS + n], pT = a.ldt[d0], pD = a.Dsk[d0];
    WG_BARRIER();
    for (int ch = 0; ch < S_CH; ++ch) {
        S4S(0)
        const float dsk = bf2f(pD);
        {
            const u32x4 v = *(LAS u32x4*)(lds + S_XR + ch * S_XRS + tid * 16);
            const u32x2 e = *(LAS u32x2*)(lds + S_XR + ch * S_XRS + tid * 16 + 16);
            const unsigned s01 = __builtin_amdgcn_alignbit(v.y, v.x, 16), s12 = __builtin_amdgcn_alignbit(v.z, v.y, 16), s23 = __builtin_amdgcn_alignbit(v.w, v.z, 16);
            const unsigned s34 = __builtin_amdgcn_alignbit(e.x, v.w, 16), s45 = __builtin_amdgcn_alignbit(e.y, e.x, 16);
            *(LAS u32x4*)(lds + S_RC + tid * 16) = v;
            *(LAS u32x4*)(lds + S_RC + S_RCS + tid * 16) = (u32x4){s01, s12, s23, s34};
            *(LAS u32x4*)(lds + S_RC + 2 * S_RCS + tid * 16) = (u32x4){v.y, v.z, v.w, e.x};
            *(LAS u32x4*)(lds + S_RC + 3 * S_RCS + tid * 16) = (u32x4){s12, s23, s34, s45};
        }
        S4S(1)
        const float dt = bf2f(f2bf(expf(bf2f(pT))));
        const int sc = 140 - (wave_max32(max(pCr & 0x7FFF, pCi & 0x7FFF)) >> 7);
        {
            const float ar = -bf2f(f2bf(expf(bf2f(pA)))) * dt, ai = bf2f(pI) * dt;
            const f32x2 C = {__builtin_amdgcn_ldexpf(bf2f(pCr), sc), __builtin_amdgcn_ldexpf(bf2f(pCi), sc)};
            const int dn = min(d0 + ch + 1, DM - 1);
            pA = a.lAr[dn * NS + n]; pI = a.Aim[dn * NS + n]; pCr = a.Cr[dn * NS + n]; pCi = a.Ci[dn * NS + n]; pT = a.ldt[dn]; pD = a.Dsk[dn];
            const int to = S_TAB + n * 4;
            auto put = [&](int row, f32x2 v) { *(LAS f16x2*)(lds + to + row * S_TROW) = __builtin_convertvector(v, f16x2); };
            const f32x2 z128 = cexp_p(ar, ai, 128.f);
            if (w < 2) {
                f32x2 E[8], G[4];
                E[1] = cexp_p(ar, ai, 1.f); G[0] = C;
#pragma unroll
                for (int r = 2; r < 8; ++r) E[r] = cmul(E[r >> 1], E[r - (r >> 1)]);
#pragma unroll
                for (int k = 1; k < 4; ++k) G[k] = cmul(G[k - 1], z128);
#pragma unroll
                for (int k = 0; k < 4; ++k)
#pragma unroll
                    for (int r = 0; r < 8; r += 1 + (k >> 1)) put(k < 2 ? 8 * k + r : 8 + 4 * k + (r >> 1), r ? cmul(E[r], G[k]) : G[k]);
            } else {
                const f32x2 z4 = cexp_p(ar, ai, 4.f), z8 = cexp_p(ar, -ai, 8.f);
                f32x2 G = cmul(C, cexp_p(ar, ai, 512.f)), B = z8;
                put(41, (f32x2){1.f, 0.f});
#pragma unroll
                for (int k = 1; k < 16; ++k) {
                    put(41 + k, B); B = cmul(B, z8);
                    if (k < 5) { put(22 + 2 * k, G); put(23 + 2 * k, cmul(G, z4)); }
                    else if (k < 14) put(27 + k, G);
                    G = cmul(G, z128);
                }
            }
        }
        WG_BARRIER();
        S4S(2)
        {
            const int ko = (w & 1) * 256 + g * 16, t0 = w & 2;
            u32x4 av[2][4], bv[4];
#pragma unroll
            for (int j = 0; j < 4; ++j) {
                bv[j] = *(LAS u32x4*)(lds + S_TAB + (41 + nn) * S_TROW + ko + j * 64);
                av[0][j] = *(LAS u32x4*)(lds + S_TAB + min(16 * t0 + nn, 40) * S_TROW + ko + j * 64);
                av[1][j] = *(LAS u32x4*)(lds + S_TAB + (16 + nn) * S_TROW + ko + j * 64);
            }
            f32x4 acc[2] = {};
#pragma unroll
            for (int j = 0; j < 8; ++j) acc[j & 1] = __builtin_amdgcn_mfma_f32_16x16x32_f16(__builtin_bit_cast(f16x8, av[j & 1][j >> 1]), __builtin_bit_cast(f16x8, bv[j >> 1]), acc[j & 1], 0, 0, 0);
#pragma unroll
            for (int r = 0; r < 8; ++r) *(LAS float*)(lds + S_KD + (((r < 4 ? 16 * t0 : 16) + 4 * g + (r & 3)) * 16 + nn) * 8 + (w & 1) * 4) = __builtin_amdgcn_ldexpf(acc[r >> 2][r & 3] * dt, 1 - sc);
        }
        WG_BARRIER();
        S4S(3)
        {
            float kv[8];
#pragma unroll
            for (int j = 0; j < 8; ++j) { const f32x2 t = *(LAS f32x2*)(lds + kd[j]); kv[j] = t.x + t.y; }
            *(LAS u32x4*)(lds + S_K + (15 + (tid >> 2)) * S_KROW + (tid & 3) * 16) = (u32x4){pack2(kv[0], kv[1]), pack2(kv[2], kv[3]), pack2(kv[4], kv[5]), pack2(kv[6], kv[7])};
        }
        WG_BARRIER();
        S4S(4)
        {
            const int st0 = 2047 - nn + 8 * g - 32 * w, cc = st0 & 3;
            const int ub = S_RC + cc * S_RCS + (st0 - cc) * 2 - 3872, kb = S_K + (3 + nn - w) * S_KROW + g * 16;
            f32x4 y[4][2] = {};
            u32x2 U[2][16]; u32x4 F[2][4];
            auto rd = [&](int i, int f) {
                if (f < 4) F[i & 1][f] = *(LAS u32x4*)(lds + kb + (12 - 4 * i + 16 * f) * S_KROW);
                else U[i & 1][f - 4] = *(LAS u32x2*)(lds + ub + 3872 - 256 * i - 1024 * ((f - 4) >> 2) - 32 * ((f >> 1) & 1) + 8 * (f & 1));
            };
#pragma unroll
            for (int f = 0; f < 20; ++f) { rd(0, f); FENCE(); }
#pragma unroll
            for (int i = 0; i < 4; ++i) {
                int c = 0;
#pragma unroll
                for (int q = 0; q < 4; ++q)
#pragma unroll
                    for (int T = q; T < 4; ++T)
#pragma unroll
                        for (int bt = 0; bt < 2; ++bt) {
                            y[T][bt] = MFMA16(__builtin_shufflevector(U[i & 1][4 * q + 2 * bt], U[i & 1][4 * q + 2 * bt + 1], 0, 1, 2, 3), F[i & 1][T - q], y[T][bt]);
                            if (i < 3) rd(i + 1, c);
                            ++c;
                            FENCE();
                        }
            }
#pragma unroll
            for (int T = 0; T < 4; ++T)
#pragma unroll
                for (int bt = 0; bt < 2; ++bt) *(LAS f32x4*)(lds + S_RED + (w * 64 + 16 * T + nn) * 144 + bt * 64 + g * 16) = y[T][bt];
        }
        WG_BARRIER();
        S4S(5)
        {
            f32x4 y0 = {0, 0, 0, 0}, y1 = y0;
#pragma unroll
            for (int ww = 0; ww < 4; ++ww) {
                const int o = S_RED + (ww * 64 + (tid >> 2)) * 144 + (tid & 3) * 32;
                y0 += *(LAS f32x4*)(lds + o); y1 += *(LAS f32x4*)(lds + o + 16);
            }
            const u32x4 u = *(LAS u32x4*)(lds + S_XR + ch * S_XRS + (255 - tid) * 16);
            st128((u32x4){pack2(y0[0] + bfhi(u.w) * dsk, y0[1] + bflo(u.w) * dsk), pack2(y0[2] + bfhi(u.z) * dsk, y0[3] + bflo(u.z) * dsk),
                          pack2(y1[0] + bfhi(u.y) * dsk, y1[1] + bflo(u.y) * dsk), pack2(y1[2] + bfhi(u.x) * dsk, y1[3] + bflo(u.x) * dsk)}, rw, tid * 16, OFF_BO + (d0 + ch) * BO_ROW);
        }
        S4S(6)
    }
    wait_init(rw, launch_tag(a), budget);
    VM_DRAIN();
    WG_BARRIER();
    if (tid == 0) st32(1u, rw, F_S4 + task * 4, 0);
}

constexpr int XT_BYTES = 32 * 1152;
constexpr int P_XT = 0;
constexpr int P_STG = 2 * XT_BYTES;
struct WSlice { u32x4 f[36]; };
constexpr int P_RING = 5;
template <class F> DI void load_wslices(WSlice& W0, WSlice& W1, const u16* s0, const u16* s1, int w, int lane, LAS char* lds, unsigned lds_base, F also) {
    constexpr int NB = 18;
    const int r0 = 8 * (lane >> 5) + ((lane & 15) >> 2);
    const int cb = (4 * w + 2 * ((lane >> 4) & 1) + ((lane & 3) >> 1)), lo8 = (lane & 1) * 8;
    auto src = [&](int i) { return (i < 9 ? s0 : s1) + (size_t)(64 * (i % 9)) * HD; };
    const u32x4 ro = ring_offs(w, lane, HD);
#pragma unroll
    for (int i = 0; i < P_RING - 1; ++i) ring_issue(src(i), ro, lds_base + P_STG + i * 16384 + w * 4096);
    also();
    static_for<NB>([&](auto ic) {
        constexpr int i = decltype(ic)::value, rem = NB - 1 - i < P_RING - 2 ? NB - 1 - i : P_RING - 2;
        ring_wait<4 * rem>();
        if constexpr (i + P_RING - 1 < NB) ring_issue(src(i + P_RING - 1), ro, lds_base + P_STG + ((i + P_RING - 1) % P_RING) * 16384 + w * 4096);
        const int sl = P_STG + (i % P_RING) * 16384;
#pragma unroll
        for (int kk = 0; kk < 4; ++kk) {
            const int ra = r0 + 16 * kk, rb = ra + 4;
            const u32x2 lo = tr_read(lds, sl + ra * 256 + ((cb ^ (2 * swz(ra))) * 16) + lo8), hi = tr_read(lds, sl + rb * 256 + ((cb ^ (2 * swz(rb))) * 16) + lo8);
            (i < 9 ? W0 : W1).f[(i % 9) * 4 + kk] = (u32x4){lo.x, lo.y, hi.x, hi.y};
        }
        u32x4* f = (i < 9 ? W0 : W1).f + (i % 9) * 4;
        if constexpr (i < 16) asm volatile("" : "+a"(f[0]), "+a"(f[1]), "+a"(f[2]), "+a"(f[3]));
    });
}
DI u32x4 piece_pi(const f32x16& a, int qq) {
    return (u32x4){pack2(a[4 * qq], a[4 * qq + 8]), pack2(a[4 * qq + 1], a[4 * qq + 9]), pack2(a[4 * qq + 2], a[4 * qq + 10]), pack2(a[4 * qq + 3], a[4 * qq + 11])};
}
DI u32x4 piece_seq(const f32x16& a, int s2) {
    return (u32x4){pack2(a[8 * s2], a[8 * s2 + 1]), pack2(a[8 * s2 + 2], a[8 * s2 + 3]), pack2(a[8 * s2 + 4], a[8 * s2 + 5]), pack2(a[8 * s2 + 6], a[8 * s2 + 7])};
}
struct XTile {
    int xs[9], xa[4];
    DI void init(int w, int lane) {
#pragma unroll
        for (int i = 0; i < 9; ++i) { const int L = (i * 4 + w) * 64 + lane, r = L / 72, cs = L - 72 * r; xs[i] = r * 1152 + (cs ^ ((r >> 1) & 7)) * 16; }
        const int c32 = lane & 31, hf = lane >> 5;
#pragma unroll
        for (int j = 0; j < 4; ++j) xa[j] = P_XT + c32 * 1152 + (((2 * j + hf) ^ ((c32 >> 1) & 7)) * 16);
    }
    DI void dma(const Args& a, unsigned lds_base, int w, int s, int buf, int i) const {
        glds16((const char*)a.x + (size_t)min(s, NSEG - 1) * XT_BYTES, xs[i], lds_base + P_XT + buf * XT_BYTES + (i * 4 + w) * 1024);
    }
    DI void first(const Args& a, unsigned lds_base, int w, int lane, int s) {
        init(w, lane);
#pragma unroll
        for (int i = 0; i < 9; ++i) dma(a, lds_base, w, s, 0, i);
    }
};
template <class F> DI void project(f32x16& acc0, f32x16& acc1, const WSlice& W0, const WSlice& W1, const XTile& X, const Args& a, LAS char* lds, unsigned lds_base, int w, int buf, int sn, F gap) {
    const int bo = buf * XT_BYTES;
    constexpr int PD = 5;
    u32x4 xf[PD];
#pragma unroll
    for (int i = 0; i < PD; ++i) xf[i] = *(LAS u32x4*)(lds + X.xa[i & 3] + bo + (i >> 2) * 128);
    static_for<36>([&](auto ic) {
        constexpr int ks = decltype(ic)::value;
        if (ks == 0) asm volatile("v_mfma_f32_32x32x16_bf16 %0, %1, %2, 0" : "=&v"(acc0) : "a"(W0.f[0]), "v"(xf[0]) : "memory");
        else asm volatile("v_mfma_f32_32x32x16_bf16 %0, %1, %2, %0" : "+v"(acc0) : "a"(W0.f[ks]), "v"(xf[ks % PD]) : "memory");
        if (ks < 9) X.dma(a, lds_base, w, sn, buf ^ 1, ks);
        gap(ic_t<2 * ks>{});
        if (ks == 0) asm volatile("v_mfma_f32_32x32x16_bf16 %0, %1, %2, 0" : "=&v"(acc1) : "a"(W1.f[0]), "v"(xf[0]) : "memory");
        else if (ks < 28) asm volatile("v_mfma_f32_32x32x16_bf16 %0, %1, %2, %0" : "+v"(acc1) : "a"(W1.f[ks]), "v"(xf[ks % PD]) : "memory");
        else asm volatile("v_mfma_f32_32x32x16_bf16 %0, %1, %2, %0" : "+v"(acc1) : "v"(W1.f[ks]), "v"(xf[ks % PD]) : "memory");
        if (ks + PD < 36) xf[ks % PD] = *(LAS u32x4*)(lds + X.xa[(ks + PD) & 3] + bo + ((ks + PD) >> 2) * 128);
        gap(ic_t<2 * ks + 1>{});
    });
    asm volatile("s_nop 7\n\ts_nop 3" : "+v"(acc0), "+v"(acc1) :: "memory");
}
#define KV_T 7
#define KV_F 22
DI void role_kv(const Args& a, LAS char* lds, unsigned lds_base, int h, rsrc_t rw, int& budget) {
    const int tid = threadIdx.x, lane = tid & 63, w = __builtin_amdgcn_readfirstlane(tid >> 6);
    WSlice W0, W1;
    XTile X;
    u32x2 tag;
    load_wslices(W0, W1, a.Wk + h * DH, a.Wv + h * DH, w, lane, lds, lds_base, [&]() { X.first(a, lds_base, w, lane, 0); tag = launch_tag(a); });
    VM_DRAIN();
    WG_BARRIER();
    const int oo = h * 8192 + w * 2048 + lane * 16;
    const int fo = lane == 0 ? F_KV + h * 512 + w * 128 : OOB;
    u32x4 xf[6], P[4] = {};
    auto frag = [&](int buf, int ks) { xf[ks % 6] = *(LAS u32x4*)(lds + X.xa[ks & 3] + buf * XT_BYTES + (ks >> 2) * 128); };
    auto cvt = [&](const f32x16& k, const f32x16& v, int i) {
#pragma unroll
        for (int j = 0; j < 4; ++j) asm volatile("v_cvt_pk_bf16_f32 %0, %1, %2" : "=v"(P[i][j]) : "v"(i < 2 ? k[4 * i + j] : v[8 * i - 16 + 2 * j]), "v"(i < 2 ? k[4 * i + j + 8] : v[8 * i - 15 + 2 * j]));
    };
    auto put = [&](int o, int s, int i) { st128(P[i], rw, o + (i & 1) * 1024, (i < 2 ? OFF_KF : OFF_VF) + s * (NH * 8192)); };
    auto now_ = [&](f32x16& k, f32x16& v, int s) {
        asm volatile("s_nop 7\n\ts_nop 3" : "+v"(k), "+v"(v) :: "memory");
#pragma unroll
        for (int i = 0; i < 4; ++i) { cvt(k, v, i); put(oo, s, i); }
    };
    u32x2 in = {};
    auto seg = [&](f32x16& k, f32x16& v, const f32x16& pk, const f32x16& pv, int s, int buf) {
        const int o = s > 1 ? oo : OOB;
#pragma unroll
        for (int ks = 0; ks < 36; ++ks) {
            if (ks == 0) asm volatile("v_mfma_f32_32x32x16_bf16 %0, %1, %2, 0" : "=&v"(k) : "a"(W0.f[0]), "v"(xf[0]) : "memory");
            else asm volatile("v_mfma_f32_32x32x16_bf16 %0, %1, %2, %0" : "+v"(k) : "a"(W0.f[ks]), "v"(xf[ks % 6]) : "memory");
            if (ks >= 1 && ks < 5) cvt(pk, pv, ks - 1);
            if (buf && ks == KV_T && s == 1) {
                asm volatile("s_waitcnt vmcnt(%0)" :: "i"(4 + (KV_T - 4) / 2) : "memory");
                __builtin_amdgcn_raw_buffer_store_b64(tag, rw, lane == 0 ? F_INIT + (33 + 4 * h + w) * 128 : OOB, 0, 16);
                if (!__all(((in.x ^ tag.x) >> 1) == 0 && in.y == tag.y)) wait_init(rw, tag, budget);
            }
            if (ks >= 5 && ks < 13 && (ks & 1)) put(o, s - 1, (ks - 5) >> 1);
            if (ks == KV_F) { VM_DRAIN(); st32((unsigned)(s - 1), rw, s > 1 ? fo : OOB, 0); }
            if (ks == 0) asm volatile("v_mfma_f32_32x32x16_bf16 %0, %2, %1, 0" : "=&v"(v) : "a"(W1.f[0]), "v"(xf[0]) : "memory");
            else if (ks < 28) asm volatile("v_mfma_f32_32x32x16_bf16 %0, %2, %1, %0" : "+v"(v) : "a"(W1.f[ks]), "v"(xf[ks % 6]) : "memory");
            else asm volatile("v_mfma_f32_32x32x16_bf16 %0, %2, %1, %0" : "+v"(v) : "v"(W1.f[ks]), "v"(xf[ks % 6]) : "memory");
            if (ks == 30) WG_BARRIER();
            frag(buf ^ (ks >= 30), (ks + 6) % 36);
            if (ks < 4) X.dma(a, lds_base, w, s + 1, buf ^ 1, ks + 5);
            if (ks > 30) X.dma(a, lds_base, w, s + 2, buf, ks - 31);
        }
    };
#pragma unroll
    for (int i = 0; i < 6; ++i) frag(0, i);
#pragma unroll
    for (int i = 0; i < 5; ++i) X.dma(a, lds_base, w, 1, 1, i);
    f32x16 k0, v0, k1 = {}, v1 = {};
    for (int s = 0; s < NSEG; s += 2) {
        seg(k0, v0, k1, v1, s, 0);
        if (s == 0) { in = ld64(rw, F_INIT + (lane & 31) * 128, 0); now_(k0, v0, 0); }
        seg(k1, v1, k0, v0, s + 1, 1);
    }
    now_(k1, v1, NSEG - 1);
    wait_prog(rw, F_S4 + lane * 4, true, 1, budget);
    VM_DRAIN();
    st32(NSEG, rw, fo, 0);
}

DI int prog_min(rsrc_t rw, int off) {
    const int v = (int)ld32(rw, off, 0);
    asm volatile("" ::: "memory");
    return -wave_max32(-v);
}
DI float red_max_g(float v) {
    auto a = __builtin_amdgcn_permlane16_swap(__float_as_uint(v), __float_as_uint(v), false, false);
    v = fmaxf(__uint_as_float(a[0]), __uint_as_float(a[1]));
    auto b = __builtin_amdgcn_permlane32_swap(__float_as_uint(v), __float_as_uint(v), false, false);
    return fmaxf(__uint_as_float(b[0]), __uint_as_float(b[1]));
}
#define M16 "v_mfma_f32_16x16x32_bf16 "
DI void role_qa(const Args& a, LAS char* lds, unsigned lds_base, int h0, int s0, int s1, rsrc_t rw, int& budget) {
    const int tid = threadIdx.x, lane = tid & 63, w = __builtin_amdgcn_readfirstlane(tid >> 6);
    const int n = lane & 15, g = lane >> 4, h = h0 + (w >> 1), tt = w & 1;
    WSlice W0, W1;
    XTile X;
    load_wslices(W0, W1, a.Wq + h0 * DH, a.Wq + (h0 + 1) * DH, w, lane, lds, lds_base, [&]() { X.first(a, lds_base, w, lane, s0); });
    const int oo = OFF_OF + (2 * tt + (n >> 3)) * 65536 + h * 2048 + g * 128 + (n & 7) * 16;
#pragma unroll
    for (int t = 0; t < 4; ++t) st128((u32x4){SENT, SENT, SENT, SENT}, rw, oo + t * 512, s1 * 262144);
    VM_DRAIN();
    WG_BARRIER();
    constexpr int Q_Q = P_STG, Q_KV = Q_Q + 16384, Q_M = Q_KV + 32768;
    static_assert(Q_M + 16384 <= P_STG + P_RING * 16384, "");
    const int fk = F_KV + h * 512 + (lane & 3) * 128, fm = F_KVM + h * 256 + (lane & 1) * 128;
    int pm_, m_rdy = -1, kv_rdy = -1;
    auto look = [&]() { pm_ = (int)ld32(rw, lane & 4 ? fk : fm, 0); };
    auto seen = [&]() { auto rl = [&](int i) { return __builtin_amdgcn_readlane(pm_, i); }; m_rdy = min(rl(0), rl(1)); kv_rdy = min(min(rl(4), rl(5)), min(rl(6), rl(7))); };
    auto wait_m = [&](int thr, unsigned& t) {
        for (;;) {
            const u32x2 v = ld64(rw, fm, 0);
            asm volatile("" ::: "memory");
            t = now();
            const int p0 = __builtin_amdgcn_readlane(v.x, 0), p1 = __builtin_amdgcn_readlane(v.x, 1);
            const int d0 = (int)(__builtin_amdgcn_readlane(v.y, 0) - 8 * t) >> 3, d1 = (int)(__builtin_amdgcn_readlane(v.y, 1) - 8 * t) >> 3;
            if (min(p0, p1) >= thr || --budget < 0) return max(p0 == thr ? d0 : -1000, p1 == thr ? d1 : -1000);
            (void)wait_until(t + min(max(min(d0, d1) + (int)(a.xa >> 16 & 255), 10), 200));
        }
    };
    auto proj = [&](int s, auto gap) {
        f32x16 acc0, acc1;
        project(acc0, acc1, W0, W1, X, a, lds, lds_base, w, ((s - s0) >> 1) & 1, s + 2, [&](auto ic) { gap(ic); if (decltype(ic)::value == 55) look(); });
        if (s >= LATE) {
            const int o = OFF_OF + h0 * 8192 + w * 2048 + lane * 16;
            st128(piece_pi(acc0, 0), rw, o, s * 262144); st128(piece_pi(acc0, 1), rw, o + 1024, s * 262144);
            st128(piece_pi(acc1, 0), rw, o + 8192, s * 262144); st128(piece_pi(acc1, 1), rw, o + 8192 + 1024, s * 262144);
            return;
        }
        const int o = Q_Q + w * 2048 + lane * 16;
        *(LAS u32x4*)(lds + o) = piece_pi(acc0, 0); *(LAS u32x4*)(lds + o + 1024) = piece_pi(acc0, 1);
        *(LAS u32x4*)(lds + o + 8192) = piece_pi(acc1, 0); *(LAS u32x4*)(lds + o + 8192 + 1024) = piece_pi(acc1, 1);
    };
    const int fs = (tt ? OFF_VF : OFF_KF) + (g >> 1) * 1024 + ((g & 1) * 32 + 16 * (n >> 3) + (n & 7)) * 16;
    auto fetch = [&](int s) {
        const int so = (min(s, NSEG - 1) * NH + h) * 8192;
#pragma unroll
        for (int f = 0; f < 8; ++f) bdma(rw, fs, so + (f >> 1) * 2048 + (f & 1) * 128, lds_base + Q_KV + w * 8192 + f * 1024);
    };
    const int ms = OFF_KM + (lane & 7) * 256 + (lane >> 3) * 16;
    auto fetch_m = [&](int s) {
        const int so = (s * NH + h) * 2048;
        const unsigned d = lds_base + Q_M + w * 4096;
        bdma(rw, ms, so, d); bdma(rw, ms, so + 128, d + 1024); bdma(rw, lane * 16, OFF_VM + so, d + 2048); bdma(rw, lane * 16, OFF_VM + so + 1024, d + 3072);
    };
    const float c = 0.08838834764831845f * 1.4426950408889634f;
    u32x4 qf[4];
    f32x4 o[8]; float mx, l;
    auto xpart = [&]() {
        const int kb = Q_KV + (w >> 1) * 16384 + lane * 16;
        u32x4 kn[8], vn[8];
#pragma unroll
        for (int ks = 0; ks < 4; ++ks) qf[ks] = *(LAS u32x4*)(lds + Q_Q + (w >> 1) * 8192 + (2 * ks + (g >> 1)) * 1024 + ((g & 1) * 32 + 16 * tt + n) * 16);
#pragma unroll
        for (int f = 0; f < 8; ++f) kn[f] = *(LAS u32x4*)(lds + kb + f * 1024);
        FENCE();
#pragma unroll
        for (int f = 0; f < 8; ++f) vn[f] = *(LAS u32x4*)(lds + kb + 8192 + f * 1024);
        f32x4 s0, s1;
        asm volatile(M16 "%0, %2, %10, 0\n\t" M16 "%1, %3, %10, 0\n\t" M16 "%0, %4, %11, %0\n\t" M16 "%1, %5, %11, %1\n\t"
                     M16 "%0, %6, %12, %0\n\t" M16 "%1, %7, %12, %1\n\t" M16 "%0, %8, %13, %0\n\t" M16 "%1, %9, %13, %1\n\ts_nop 7"
                     : "=&v"(s0), "=&v"(s1) : "v"(kn[0]), "v"(kn[1]), "v"(kn[2]), "v"(kn[3]), "v"(kn[4]), "v"(kn[5]), "v"(kn[6]), "v"(kn[7]), "v"(qf[0]), "v"(qf[1]), "v"(qf[2]), "v"(qf[3]));
        float p[8];
        mx = -3.0e38f; l = 0.f;
#pragma unroll
        for (int r = 0; r < 8; ++r) { const int key = 16 * (g >> 1) + 4 * (g & 1) + (r & 3) + 8 * (r >> 2); p[r] = key <= 16 * tt + n ? (r < 4 ? s0[r & 3] : s1[r & 3]) : -3.0e38f; mx = fmaxf(mx, p[r]); }
        mx = red_max_g(mx);
#pragma unroll
        for (int r = 0; r < 8; ++r) { p[r] = __builtin_amdgcn_exp2f((p[r] - mx) * c); l += p[r]; }
        const u32x4 pf = {pack2(p[0], p[1]), pack2(p[2], p[3]), pack2(p[4], p[5]), pack2(p[6], p[7])};
        asm volatile("s_nop 1\n\t" M16 "%0, %8, %16, 0\n\t" M16 "%1, %9, %16, 0\n\t" M16 "%2, %10, %16, 0\n\t" M16 "%3, %11, %16, 0\n\t"
                     M16 "%4, %12, %16, 0\n\t" M16 "%5, %13, %16, 0\n\t" M16 "%6, %14, %16, 0\n\t" M16 "%7, %15, %16, 0\n\ts_nop 7"
                     : "=&v"(o[0]), "=&v"(o[1]), "=&v"(o[2]), "=&v"(o[3]), "=&v"(o[4]), "=&v"(o[5]), "=&v"(o[6]), "=&v"(o[7])
                     : "v"(vn[0]), "v"(vn[1]), "v"(vn[2]), "v"(vn[3]), "v"(vn[4]), "v"(vn[5]), "v"(vn[6]), "v"(vn[7]), "v"(pf));
    };
    const int fo = lane == 0 ? F_O + h * 128 + (2 * s0 + tt) * 4 : OOB;
    bool pre = false;
    auto both = [&](int s) {
        const int kb = Q_KV + (w >> 1) * 16384 + lane * 16;
        u32x4 kn[8], vn[8], pf;
        f32x4 sc[2];
        float p[8];
        int v0, v1, v2;
        asm volatile("buffer_load_dword %0, %1, %2, 0 offen sc1" : "=v"(pm_) : "v"(lane & 4 ? fk : fm), "s"(rw) : "memory");
#define PIN(...) asm volatile("" : __VA_ARGS__)
        proj(s + 4, [&](auto ic) {
            constexpr int i = decltype(ic)::value;
            if constexpr (i < 8) {
                if (!(i & 1)) qf[i >> 1] = *(LAS u32x4*)(lds + Q_Q + (w >> 1) * 8192 + (i + (g >> 1)) * 1024 + ((g & 1) * 32 + 16 * tt + n) * 16);
                kn[i] = *(LAS u32x4*)(lds + kb + i * 1024);
            }
            if constexpr (i >= 16 && i < 32 && !(i & 1)) vn[(i - 16) >> 1] = *(LAS u32x4*)(lds + kb + 8192 + (i - 16) * 512);
            if constexpr (i == 5 || i == 6) asm volatile(M16 "%0, %1, %2, 0" : "=&v"(sc[i - 5]) : "v"(kn[i - 5]), "v"(qf[0]));
            if constexpr (i > 6 && i < 13) asm volatile(M16 "%0, %1, %2, %0" : "+v"(sc[(i - 5) & 1]) : "v"(kn[i - 5]), "v"(qf[(i - 5) >> 1]));
            if constexpr (i == 15 || i == 16) {
                constexpr int r = 4 * (i - 15);
                PIN("+v"(sc[i - 15]));
#pragma unroll
                for (int j = 0; j < 4; ++j) p[r + j] = 16 * (g >> 1) + 4 * (g & 1) + j + 2 * r <= 16 * tt + n ? sc[i - 15][j] : -3.0e38f;
                PIN("+v"(p[r]), "+v"(p[r + 1]), "+v"(p[r + 2]), "+v"(p[r + 3]));
            }
            if constexpr (i == 17) { mx = fmaxf(fmaxf(fmaxf(fmaxf(p[0], p[1]), p[2]), fmaxf(fmaxf(p[3], p[4]), p[5])), fmaxf(p[6], p[7])); PIN("+v"(mx)); }
            if constexpr (i == 18) { auto x = __builtin_amdgcn_permlane16_swap(__float_as_uint(mx), __float_as_uint(mx), false, false); mx = fmaxf(__uint_as_float(x[0]), __uint_as_float(x[1])); PIN("+v"(mx)); }
            if constexpr (i == 19) { auto x = __builtin_amdgcn_permlane32_swap(__float_as_uint(mx), __float_as_uint(mx), false, false); mx = fmaxf(__uint_as_float(x[0]), __uint_as_float(x[1])); PIN("+v"(mx)); }
            if constexpr (i >= 20 && i < 24) {
                constexpr int r = 2 * (i - 20);
                PIN("+v"(p[r]), "+v"(p[r + 1]));
                p[r] = __builtin_amdgcn_exp2f((p[r] - mx) * c); p[r + 1] = __builtin_amdgcn_exp2f((p[r + 1] - mx) * c);
                PIN("+v"(p[r]), "+v"(p[r + 1]));
            }
            if constexpr (i == 24) { pf = (u32x4){pack2(p[0], p[1]), pack2(p[2], p[3]), pack2(p[4], p[5]), pack2(p[6], p[7])}; PIN("+v"(pf)); }
            if constexpr (i == 25) { l = ((p[0] + p[1]) + p[2]) + p[3]; PIN("+v"(l)); }
            if constexpr (i == 26) { l = (((l + p[4]) + p[5]) + p[6]) + p[7]; PIN("+v"(l)); }
            if constexpr (i == 27) { asm volatile("s_waitcnt vmcnt(9)" : "+v"(pm_) :: "memory"); st32((unsigned)s, rw, fo, 0); }
            if constexpr (i == 29) { seen(); pre = m_rdy >= s + 2 && kv_rdy >= s + 4; }
            if constexpr (i == 31) { v0 = pre ? ms : OOB; v1 = pre ? lane * 16 : OOB; v2 = pre ? fs : OOB; PIN("+v"(v0), "+v"(v1), "+v"(v2)); }
            if constexpr (i >= 28 && i < 36) asm volatile(M16 "%0, %1, %2, 0" : "=&v"(o[i - 28]) : "v"(vn[i - 28]), "v"(pf));
            if constexpr (i == 36) asm volatile("s_barrier" ::: "memory");
            if constexpr (i >= 38 && i < 42) bdma(rw, i < 40 ? v0 : v1, ((s + 2) * NH + h) * 2048 + (i < 40 ? (i & 1) * 128 : OFF_VM + (i & 1) * 1024), lds_base + Q_M + w * 4096 + (i - 38) * 1024);
            if constexpr (i >= 42 && i < 50) bdma(rw, v2, ((s + 4) * NH + h) * 8192 + ((i - 42) >> 1) * 2048 + (i & 1) * 128, lds_base + Q_KV + w * 8192 + (i - 42) * 1024);
        });
    };
    auto none = [](auto) {};
    proj(s0, none);
    if (s0 < FAST) wait_init(rw, launch_tag(a), budget);
    wait_prog(rw, fk, true, s0, budget);
    fetch(s0);
    VM_DRAIN();
    WG_BARRIER();
    xpart();
    WG_BARRIER();
    proj(s0 + 2, none);
    for (int s = s0; s <= s1; s += 2) {
        S4S(0)
        const int mb = Q_M + w * 4096;
        u32x4 kf[4]; u32x2 vm[4];
        auto frags = [&]() {
#pragma unroll
            for (int ks = 0; ks < 4; ++ks) kf[ks] = *(LAS u32x4*)(lds + mb + (4 * ks + g) * 128 + (2 * (n >> 2) + (n & 1)) * 16);
#pragma unroll
            for (int t = 0; t < 4; ++t) { const int vo = mb + 2048 + t * 512 + (n >> 3) * 256 + g * 64 + (n & 7) * 4; vm[t] = (u32x2){*(LAS unsigned*)(lds + vo), *(LAS unsigned*)(lds + vo + 32)}; }
        };
        if (s == s1) {
            TT(true, 3)
            unsigned t;
            const int d = min(wait_m(s - 1, t) + (int)(a.xa & 255), 500);
            st32((t + max(d, 0) + (a.xb >> 8 * ((s - FAST) >> 1) & 255)) & 0x7FFFFFFFu, rw, lane == 0 ? F_H + ((2 * (s - FAST) + tt) * NH + h) * 128 : OOB, 0);
            (void)wait_until(t + d);
            TT(true, 0)
            for (;;) {
                fetch_m(s);
                VM_DRAIN();
                frags();
                unsigned c = 0;
#pragma unroll
                for (int i = 0; i < 4; ++i) c = max(max(mx4(c, kf[i]), vm[i].x), vm[i].y);
                if (!__any(c == SENT) || --budget < 0) break;
            }
        } else {
            if (!pre) {
                seen();
                unsigned t;
                if (m_rdy < s) { const int d = wait_m(s, t); (void)wait_until(t + min(d + (int)(a.xa >> 24), 300)); }
                fetch_m(s);
                if (kv_rdy < s + 2) wait_prog(rw, fk, true, s + 2, budget);
                fetch(s + 2);
            }
            S4S(1)
            asm volatile("s_waitcnt vmcnt(8)" ::: "memory");
            frags();
        }
        TT(s == s1, 1)
        S4S(2)
        f32x4 sm;
        asm volatile(M16 "%0, %1, %5, 0\n\t" M16 "%0, %2, %6, %0\n\t" M16 "%0, %3, %7, %0\n\t" M16 "%0, %4, %8, %0\n\ts_nop 7"
                     : "=&v"(sm) : "v"(kf[0]), "v"(kf[1]), "v"(kf[2]), "v"(kf[3]), "v"(qf[0]), "v"(qf[1]), "v"(qf[2]), "v"(qf[3]));
        const float m2 = red_max_g(fmaxf(mx, fmaxf(sm[0], sm[1])));
        const float al = __builtin_amdgcn_exp2f((mx - m2) * c), p0 = __builtin_amdgcn_exp2f((sm[0] - m2) * c), p1 = __builtin_amdgcn_exp2f((sm[1] - m2) * c);
        const float inv = __builtin_amdgcn_rcpf(red_sum_g(l * al + p0 + p1)), ai = al * inv;
#pragma unroll
        for (int t = 0; t < 8; ++t) o[t] *= ai;
        const unsigned pm = pack2(p0 * inv, p1 * inv);
#define M1K "v_mfma_f32_16x16x16_bf16 "
        asm volatile("s_nop 1\n\t" M1K "%0, %8, %12, %0\n\t" M1K "%1, %8, %13, %1\n\t" M1K "%2, %9, %12, %2\n\t" M1K "%3, %9, %13, %3\n\t"
                     M1K "%4, %10, %12, %4\n\t" M1K "%5, %10, %13, %5\n\t" M1K "%6, %11, %12, %6\n\t" M1K "%7, %11, %13, %7\n\ts_nop 7"
                     : "+v"(o[0]), "+v"(o[1]), "+v"(o[2]), "+v"(o[3]), "+v"(o[4]), "+v"(o[5]), "+v"(o[6]), "+v"(o[7])
                     : "v"(vm[0]), "v"(vm[1]), "v"(vm[2]), "v"(vm[3]), "v"((u32x2){pm, 0u}), "v"((u32x2){0u, pm}));
#pragma unroll
        for (int b = 0; b < 2; ++b) {
            u32x4 x, y;
#pragma unroll
            for (int j = 0; j < 4; ++j) { x[j] = pack2(o[4 * b + (j >> 1)][2 * (j & 1)], o[4 * b + (j >> 1)][2 * (j & 1) + 1]); y[j] = pack2(o[4 * b + 2 + (j >> 1)][2 * (j & 1)], o[4 * b + 2 + (j >> 1)][2 * (j & 1) + 1]); }
            if (s == LATE - 1) { const u32x4 z = {x.x, x.z, y.x, y.z}; y = (u32x4){x.y, x.w, y.y, y.w}; x = z; }
            st128(x, rw, oo + b * 1024, s * 262144); st128(y, rw, oo + b * 1024 + 512, s * 262144);
        }
        TT(s == s1, 2)
        S4S(3)
        if (s == s1) break;
        asm volatile("s_waitcnt vmcnt(4)" ::: "memory");
        WG_BARRIER();
        S4S(4)
        if (s + 4 <= s1) { both(s); S4S(5) continue; }
        xpart();
        FENCE();
        VM_DRAIN();
        st32((unsigned)s, rw, fo, 0);
        TT(true, 4)
    }
}

#define STEP_ASM \
    "v_readfirstlane_b32 s36, v0\n" \
    "v_readfirstlane_b32 s37, v1\n" \
    "v_readfirstlane_b32 s38, v2\n" \
    "v_readfirstlane_b32 s39, v3\n" \
    "v_readfirstlane_b32 s40, v4\n" \
    "v_readfirstlane_b32 s41, v5\n" \
    "v_readfirstlane_b32 s42, v6\n" \
    "v_readfirstlane_b32 s43, v7\n" \
    "v_readfirstlane_b32 s28, v8\n" \
    "v_readfirstlane_b32 s29, v9\n" \
    "v_readfirstlane_b32 s30, v10\n" \
    "v_readfirstlane_b32 s31, v11\n" \
    "v_readfirstlane_b32 s32, v12\n" \
    "v_readfirstlane_b32 s33, v13\n" \
    "v_readfirstlane_b32 s34, v14\n" \
    "v_readfirstlane_b32 s35, v15\n" \
    "v_readfirstlane_b32 s24, v16\n" \
    "v_readfirstlane_b32 s25, v17\n" \
    "v_readfirstlane_b32 s26, v18\n" \
    "v_readfirstlane_b32 s27, v19\n" \
    "v_readfirstlane_b32 s60, v24\n" \
    "v_readfirstlane_b32 s61, v25\n" \
    "v_readfirstlane_b32 s62, v26\n" \
    "v_readfirstlane_b32 s63, v27\n" \
    "v_readfirstlane_b32 s64, v28\n" \
    "v_readfirstlane_b32 s65, v29\n" \
    "v_readfirstlane_b32 s66, v30\n" \
    "v_readfirstlane_b32 s67, v31\n" \
    "s_mov_b32 s68, m0\n" \
    "s_getpc_b64 s[96:97]\n" \
    "14:\n" \
    "s_add_u32 s96, s96, 2f-14b\n" \
    "s_addc_u32 s97, s97, 0\n" \
    "s_lshl_b32 s74, s60, 10\n" \
    "s_lshl_b32 s95, s74, 1\n" \
    "s_add_i32 s95, s95, 16384\n" \
    "s_mov_b32 s81, 0x3e0293ee\n" \
    "s_mov_b32 s82, 0x53800000\n" \
    "s_lshl_b32 s83, s60, 6\n" \
    "s_and_b32 s83, s83, 128\n" \
    "v_add_u32 v109, s83, v131\n" \
    "v_sub_u32 v111, v131, s83\n" \
    "v_xor_b32 v112, 0x2000, v133\n" \
    "v_and_b32 v113, 0x3c0, v127\n" \
    "v_mad_u32_u24 v113, v113, 7, v127\n" \
    "s_bfe_u32 s83, s61, 0x2000b\n" \
    "s_and_b32 s83, s83, 3\n" \
    "v_lshl_add_u32 v113, s83, 7, v113\n" \
    "v_lshlrev_b32 v114, 1, v127\n" \
    "v_add_u32 v106, s63, v133\n" \
    "s_bfe_u32 s83, s61, 0x40009\n" \
    "s_bfe_u32 s80, s61, 0x20004\n" \
    "s_lshl2_add_u32 s83, s83, s80\n" \
    "s_mov_b64 s[72:73], 0\n" \
    "s_cmp_eq_u32 s43, 4\n" \
    "s_cbranch_scc1 20f\n" \
    "0:\n" \
    "s_and_b32 s75, s36, 3\n" \
    "s_lshl_b32 s75, s75, 16\n" \
    "s_add_i32 s69, s75, s26\n" \
    "s_xor_b32 s70, s69, 2048\n" \
    "s_andn2_b32 s71, s69, 0x1fff\n" \
    "s_add_i32 s71, s71, 32768\n" \
    "ds_read_b128 v[72:75], v133 offset:0\n" \
    "ds_read_b128 v[76:79], v133 offset:1024\n" \
    "ds_read_b128 v[80:83], v133 offset:2048\n" \
    "ds_read_b128 v[84:87], v133 offset:3072\n" \
    "ds_read_b128 v[88:91], v133 offset:16384\n" \
    "ds_read_b128 v[92:95], v133 offset:17408\n" \
    "v_mov_b64 v[40:41], 0\n" \
    "v_mov_b64 v[42:43], 0\n" \
    "v_mov_b64 v[44:45], 0\n" \
    "v_mov_b64 v[46:47], 0\n" \
    "v_mov_b64 v[48:49], 0\n" \
    "v_mov_b64 v[50:51], 0\n" \
    "v_mov_b64 v[52:53], 0\n" \
    "v_mov_b64 v[54:55], 0\n" \
    "v_mov_b64 v[56:57], 0\n" \
    "v_mov_b64 v[58:59], 0\n" \
    "v_mov_b64 v[60:61], 0\n" \
    "v_mov_b64 v[62:63], 0\n" \
    "v_mov_b64 v[64:65], 0\n" \
    "v_mov_b64 v[66:67], 0\n" \
    "v_mov_b64 v[68:69], 0\n" \
    "v_mov_b64 v[70:71], 0\n" \
    "v_add_u32 v108, s74, v127\n" \
    "v_xor_b32 v24, 0x400, v108\n" \
    "v_xor_b32 v28, 0x800, v108\n" \
    "v_xor_b32 v32, 0xc00, v108\n" \
    "v_and_b32 v22, 0x200, v127\n" \
    "s_and_b32 s76, s26, 0x800\n" \
    "v_mad_u32_u24 v22, v22, 3, v127\n" \
    "v_xor_b32 v22, s76, v22\n" \
    "s_movk_i32 m0, 0x2000\n" \
    "s_cmp_eq_u32 s36, 0\n" \
    "s_cbranch_scc1 17f\n" \
    "s_sub_i32 s37, s37, s29\n" \
    "12:\n" \
    "1:\n" \
    "s_memtime vcc\n" \
    "s_mul_i32 s90, s37, s25\n" \
    "s_ashr_i32 s90, s90, 12\n" \
    "s_add_i32 s90, s90, s24\n" \
    "s_add_i32 s90, s90, -116\n" \
    "s_waitcnt lgkmcnt(0)\n" \
    "s_sub_i32 s75, s90, vcc_lo\n" \
    "s_cmp_gt_i32 s75, 112\n" \
    "s_cbranch_scc1 1b\n" \
    "s_max_i32 s75, s75, 0\n" \
    "s_and_b32 s75, s75, -4\n" \
    "s_sub_u32 vcc_lo, s96, s75\n" \
    "s_subb_u32 vcc_hi, s97, 0\n" \
    "s_setpc_b64 vcc\n" \
    "s_nop 0\n" \
    "s_nop 0\n" \
    "s_nop 0\n" \
    "s_nop 0\n" \
    "s_nop 0\n" \
    "s_nop 0\n" \
    "s_nop 0\n" \
    "s_nop 0\n" \
    "s_nop 0\n" \
    "s_nop 0\n" \
    "s_nop 0\n" \
    "s_nop 0\n" \
    "s_nop 0\n" \
    "s_nop 0\n" \
    "s_nop 0\n" \
    "s_nop 0\n" \
    "s_nop 0\n" \
    "s_nop 0\n" \
    "s_nop 0\n" \
    "s_nop 0\n" \
    "s_nop 0\n" \
    "s_nop 0\n" \
    "s_nop 0\n" \
    "s_nop 0\n" \
    "s_nop 0\n" \
    "s_nop 0\n" \
    "s_nop 0\n" \
    "s_nop 0\n" \
    "2:\n" \
    "buffer_load_dwordx4 v[0:3], v125, %[rs], s69 offen sc1\n" \
    "s_nop 3\n" \
    "s_nop 3\n" \
    "buffer_load_dwordx4 v[16:19], v125, %[rs], s70 offen sc1\n" \
    "s_mov_b32 exec_lo, s27\n" \
    "s_mov_b32 exec_hi, s27\n" \
    "buffer_load_dwordx4 v22, %[rs], s71 offen sc1 lds\n" \
    "s_mov_b64 exec, -1\n" \
    "v_bfe_u32 v20, v127, 8, 2\n" \
    "v_subrev_u32 v20, s60, v20\n" \
    "v_mul_i32_i24 v20, -1088, v20\n" \
    "v_add_u32 v20, v20, v129\n" \
    "v_lshrrev_b32 v21, 2, v127\n" \
    "s_mov_b32 exec_lo, s66\n" \
    "s_mov_b32 exec_hi, s66\n" \
    "ds_read2_b32 v[96:97], v20 offset0:0 offset1:144\n" \
    "s_mov_b64 exec, -1\n" \
    "s_lshl_b32 s80, s36, 16\n" \
    "s_add_i32 s80, s80, s67\n" \
    "s_sub_i32 s75, s37, s79\n" \
    "s_cmp_gt_i32 s75, 24\n" \
    "s_cselect_b32 s76, 2, 0\n" \
    "s_cmp_lt_i32 s75, 10\n" \
    "s_cselect_b32 s76, -6, s76\n" \
    "s_add_i32 s28, s28, s76\n" \
    "s_max_i32 s28, s28, 0\n" \
    "s_min_i32 s28, s28, 20\n" \
    "s_cmp_eq_u32 s60, 0\n" \
    "s_cselect_b32 s72, 0x1010101, 0\n" \
    "s_waitcnt lgkmcnt(0)\n" \
    "buffer_store_dword v96, v21, %[rw], s80 offen sc1\n" \
    "buffer_store_dword v97, v21, %[rw], s80 offen offset:256 sc1\n" \
    "3:\n" \
    "s_mov_b32 m0, s95\n" \
    "s_add_i32 s91, s36, 1\n" \
    "s_and_b32 s78, s36, 3\n" \
    "s_lshl_b32 s78, s78, 14\n" \
    "s_add_i32 s77, s78, s61\n" \
    "s_bitcmp1_b32 s36, 0\n" \
    "s_cselect_b32 s79, 0x8000, 0\n" \
    "s_lshl_b32 s92, s91, 18\n" \
    "s_add_i32 s92, s92, s64\n" \
    "s_add_i32 s78, s78, 0x1a0000\n" \
    "s_add_i32 s93, s92, 0x1000000\n" \
    "s_sub_i32 s76, s33, s42\n" \
    "s_ashr_i32 s76, s76, s32\n" \
    "s_add_i32 s42, s42, s76\n" \
    "s_lshr_b32 s76, s32, 8\n" \
    "s_sub_i32 s76, s76, s40\n" \
    "s_ashr_i32 s76, s76, 2\n" \
    "s_add_i32 s40, s40, s76\n" \
    "s_sub_i32 s76, s40, s24\n" \
    "s_min_i32 s94, s91, s38\n" \
    "s_lshl_b32 s94, s94, 18\n" \
    "s_andn2_b32 s80, 0x1000000, s66\n" \
    "s_add_i32 s94, s94, s64\n" \
    "s_add_i32 s94, s94, s80\n" \
    "s_add_i32 s75, s36, 2\n" \
    "s_cmp_lt_i32 s38, s75\n" \
    "s_cselect_b32 s75, s62, 0x1b0040\n" \
    "v_mov_b64 v[98:99], 0\n" \
    "v_mov_b32 v101, 0\n" \
    "s_pack_ll_b32_b16 s80, s91, s42\n" \
    "v_mov_b32 v103, s80\n" \
    "s_waitcnt vmcnt(4)\n" \
    "v_smfmac_f32_16x16x64_bf16 v[40:43], v[0:3], a[0:7], v124\n" \
    "ds_write_b128 v108, v[0:3] offset:0\n" \
    "v_smfmac_f32_16x16x64_bf16 v[44:47], v[0:3], a[8:15], v124\n" \
    "v_smfmac_f32_16x16x64_bf16 v[48:51], v[0:3], a[128:135], v124\n" \
    "v_smfmac_f32_16x16x64_bf16 v[52:55], v[0:3], a[136:143], v124\n" \
    "s_waitcnt vmcnt(3)\n" \
    "v_smfmac_f32_16x16x64_bf16 v[40:43], v[16:19], a[64:71], v124\n" \
    "ds_write_b128 v108, v[16:19] offset:4096\n" \
    "v_smfmac_f32_16x16x64_bf16 v[44:47], v[16:19], a[72:79], v124\n" \
    "s_waitcnt lgkmcnt(1)\n" \
    "s_barrier\n" \
    "v_smfmac_f32_16x16x64_bf16 v[48:51], v[16:19], a[192:199], v124\n" \
    "ds_read_b128 v[4:7], v24 offset:0\n" \
    "v_smfmac_f32_16x16x64_bf16 v[52:55], v[16:19], a[200:207], v124\n" \
    "ds_read_b128 v[8:11], v28 offset:0\n" \
    "v_smfmac_f32_16x16x64_bf16 v[56:59], v[0:3], v[72:79], v124\n" \
    "ds_read_b128 v[12:15], v32 offset:0\n" \
    "v_smfmac_f32_16x16x64_bf16 v[60:63], v[0:3], v[80:87], v124\n" \
    "s_waitcnt vmcnt(2) lgkmcnt(3)\n" \
    "s_barrier\n" \
    "s_memtime s[86:87]\n" \
    "v_smfmac_f32_16x16x64_bf16 v[56:59], v[16:19], v[168:175], v124\n" \
    "ds_read_b128 v[24:27], v24 offset:4096\n" \
    "v_smfmac_f32_16x16x64_bf16 v[60:63], v[16:19], v[176:183], v124\n" \
    "ds_read_b128 v[28:31], v28 offset:4096\n" \
    "s_waitcnt lgkmcnt(3)\n" \
    "v_smfmac_f32_16x16x64_bf16 v[40:43], v[4:7], a[16:23], v124\n" \
    "ds_read_b128 v[32:35], v32 offset:4096\n" \
    "v_smfmac_f32_16x16x64_bf16 v[40:43], v[8:11], a[32:39], v124\n" \
    "ds_read_b128 v[36:39], v126 offset:0\n" \
    "s_waitcnt lgkmcnt(4)\n" \
    "v_smfmac_f32_16x16x64_bf16 v[40:43], v[12:15], a[48:55], v124\n" \
    "buffer_load_dword v110, v114, %[rw], s94 offen sc1\n" \
    "v_smfmac_f32_16x16x64_bf16 v[44:47], v[4:7], a[24:31], v124\n" \
    "s_waitcnt lgkmcnt(2)\n" \
    "v_smfmac_f32_16x16x64_bf16 v[40:43], v[24:27], a[80:87], v124\n" \
    "v_smfmac_f32_16x16x64_bf16 v[40:43], v[28:31], a[96:103], v124\n" \
    "s_waitcnt lgkmcnt(0)\n" \
    "v_smfmac_f32_16x16x64_bf16 v[40:43], v[32:35], a[112:119], v124\n" \
    "ds_read_b128 v[72:75], v133 offset:18432\n" \
    "s_add_i32 s80, s86, s76\n" \
    "v_smfmac_f32_16x16x64_bf16 v[40:43], v[36:39], v[136:143], v124\n" \
    "ds_read_b128 v[76:79], v133 offset:19456\n" \
    "s_mul_hi_u32 s80, s80, s41\n" \
    "v_smfmac_f32_16x16x64_bf16 v[44:47], v[8:11], a[40:47], v124\n" \
    "ds_read_b128 v[80:83], v133 offset:32768\n" \
    "s_add_i32 s80, s80, s29\n" \
    "v_mov_b32 v102, s80\n" \
    "v_smfmac_f32_16x16x64_bf16 v[44:47], v[12:15], a[56:63], v124\n" \
    "ds_read_b128 v[84:87], v133 offset:33792\n" \
    "v_add_f32 v40, v40, v42\n" \
    "v_add_f32 v41, v41, v43\n" \
    "v_smfmac_f32_16x16x64_bf16 v[44:47], v[24:27], a[88:95], v124\n" \
    "ds_read_b128 v[20:23], v127 offset:19456\n" \
    "v_cmp_u_f32 vcc, v40, v41\n" \
    "v_smfmac_f32_16x16x64_bf16 v[44:47], v[28:31], a[104:111], v124\n" \
    "v_mul_f32 v40, s81, v40\n" \
    "v_smfmac_f32_16x16x64_bf16 v[44:47], v[32:35], a[120:127], v124\n" \
    "s_cbranch_vccnz 5f\n" \
    "6:\n" \
    "s_mov_b64 exec, s[72:73]\n" \
    "v_smfmac_f32_16x16x64_bf16 v[44:47], v[36:39], v[144:151], v124\n" \
    "buffer_store_dwordx2 v[102:103], v127, %[rs], s77 offen sc1\n" \
    "v_smfmac_f32_16x16x64_bf16 v[56:59], v[4:7], v[88:95], v124\n" \
    "s_mov_b64 exec, -1\n" \
    "s_waitcnt lgkmcnt(1)\n" \
    "v_mul_f32 v41, s81, v41\n" \
    "v_smfmac_f32_16x16x64_bf16 v[60:63], v[4:7], v[72:79], v124\n" \
    "ds_read_b128 v[88:91], v133 offset:34816\n" \
    "v_add_f32 v44, v44, v46\n" \
    "v_smfmac_f32_16x16x64_bf16 v[56:59], v[8:11], v[80:87], v124\n" \
    "ds_read_b128 v[92:95], v133 offset:35840\n" \
    "v_add_f32 v45, v45, v47\n" \
    "v_smfmac_f32_16x16x64_bf16 v[48:51], v[4:7], a[144:151], v124\n" \
    "ds_read_b128 v[72:75], v133 offset:49152\n" \
    "v_mul_f32 v44, s81, v44\n" \
    "v_smfmac_f32_16x16x64_bf16 v[52:55], v[4:7], a[152:159], v124\n" \
    "ds_read_b128 v[76:79], v133 offset:50176\n" \
    "v_mul_f32 v45, s81, v45\n" \
    "v_smfmac_f32_16x16x64_bf16 v[48:51], v[8:11], a[160:167], v124\n" \
    "ds_read_b128 v[0:3], v127 offset:16384\n" \
    "v_cvt_pk_bf16_f32 v107, v40, v44\n" \
    "v_smfmac_f32_16x16x64_bf16 v[52:55], v[8:11], a[168:175], v124\n" \
    "ds_read_b128 v[4:7], v127 offset:18432\n" \
    "s_waitcnt lgkmcnt(4)\n" \
    "v_cvt_pk_bf16_f32 v108, v41, v45\n" \
    "v_smfmac_f32_16x16x64_bf16 v[60:63], v[8:11], v[88:95], v124\n" \
    "ds_write2_b32 v128, v107, v108 offset0:0 offset1:80\n" \
    "v_smfmac_f32_16x16x64_bf16 v[60:63], v[12:15], v[248:255], v124\n" \
    "ds_read_b128 v[16:19], v127 offset:17408\n" \
    "s_waitcnt lgkmcnt(4)\n" \
    "v_smfmac_f32_16x16x64_bf16 v[56:59], v[12:15], v[72:79], v124\n" \
    "ds_read_b128 v[8:11], v127 offset:20480\n" \
    "v_smfmac_f32_16x16x64_bf16 v[48:51], v[12:15], a[176:183], v124\n" \
    "ds_read_b128 v[40:43], v127 offset:21504\n" \
    "v_smfmac_f32_16x16x64_bf16 v[52:55], v[12:15], a[184:191], v124\n" \
    "ds_read_b128 v[44:47], v127 offset:23552\n" \
    "v_smfmac_f32_16x16x64_bf16 v[48:51], v[24:27], a[208:215], v124\n" \
    "s_waitcnt lgkmcnt(4)\n" \
    "s_barrier\n" \
    "v_smfmac_f32_16x16x64_bf16 v[52:55], v[24:27], a[216:223], v124\n" \
    "ds_read_b128 v[72:75], v130 offset:0\n" \
    "v_smfmac_f32_16x16x64_bf16 v[48:51], v[28:31], a[224:231], v124\n" \
    "ds_read_b128 v[76:79], v130 offset:64\n" \
    "v_smfmac_f32_16x16x64_bf16 v[48:51], v[32:35], a[240:247], v124\n" \
    "ds_read_b128 v[80:83], v130 offset:128\n" \
    "v_smfmac_f32_16x16x64_bf16 v[48:51], v[36:39], v[152:159], v124\n" \
    "ds_read_b128 v[84:87], v130 offset:192\n" \
    "v_smfmac_f32_16x16x64_bf16 v[52:55], v[28:31], a[232:239], v124\n" \
    "ds_read_b128 v[12:15], v127 offset:22528\n" \
    "v_smfmac_f32_16x16x64_bf16 v[52:55], v[32:35], a[248:255], v124\n" \
    "v_add_f32 v48, v48, v50\n" \
    "v_smfmac_f32_16x16x64_bf16 v[52:55], v[36:39], v[160:167], v124\n" \
    "s_waitcnt lgkmcnt(3)\n" \
    "v_add_f32 v49, v49, v51\n" \
    "v_mfma_f32_16x16x32_bf16 v[88:91], v[0:3], v[72:75], 0\n" \
    "ds_read_b64 v[0:1], v132 offset:0\n" \
    "ds_read_b64 v[2:3], v132 offset:1024\n" \
    "v_mfma_f32_16x16x32_bf16 v[88:91], v[4:7], v[76:79], v[88:91]\n" \
    "s_waitcnt lgkmcnt(2)\n" \
    "v_add_f32 v52, v52, v54\n" \
    "v_add_f32 v53, v53, v55\n" \
    "v_mfma_f32_16x16x32_bf16 v[88:91], v[8:11], v[80:83], v[88:91]\n" \
    "v_cvt_pk_bf16_f32 v107, v48, v52\n" \
    "v_cvt_pk_bf16_f32 v108, v49, v53\n" \
    "v_mfma_f32_16x16x32_bf16 v[88:91], v[12:15], v[84:87], v[88:91]\n" \
    "ds_write2_b32 v129, v107, v108 offset0:0 offset1:144\n" \
    "v_mfma_f32_16x16x32_bf16 v[92:95], v[16:19], v[72:75], 0\n" \
    "ds_read_b64 v[4:5], v132 offset:512\n" \
    "ds_read_b64 v[6:7], v132 offset:1536\n" \
    "v_mfma_f32_16x16x32_bf16 v[92:95], v[20:23], v[76:79], v[92:95]\n" \
    "ds_read_b128 v[8:11], v106 offset:0\n" \
    "v_exp_f32 v88, v88\n" \
    "v_mfma_f32_16x16x32_bf16 v[92:95], v[40:43], v[80:83], v[92:95]\n" \
    "ds_read_b128 v[12:15], v106 offset:1024\n" \
    "v_exp_f32 v89, v89\n" \
    "v_mfma_f32_16x16x32_bf16 v[92:95], v[44:47], v[84:87], v[92:95]\n" \
    "s_waitcnt lgkmcnt(4)\n" \
    "s_barrier\n" \
    "v_smfmac_f32_16x16x64_bf16 v[56:59], v[24:27], v[184:191], v124\n" \
    "ds_read_b128 v[48:51], v131 offset:0\n" \
    "ds_read_b128 v[52:55], v131 offset:128\n" \
    "v_cvt_pk_bf16_f32 v88, v88, v89\n" \
    "v_smfmac_f32_16x16x64_bf16 v[56:59], v[28:31], v[200:207], v124\n" \
    "ds_read_b128 v[16:19], v106 offset:32768\n" \
    "v_exp_f32 v90, v90\n" \
    "v_smfmac_f32_16x16x64_bf16 v[56:59], v[32:35], v[216:223], v124\n" \
    "ds_read_b128 v[20:23], v106 offset:33792\n" \
    "v_exp_f32 v91, v91\n" \
    "v_smfmac_f32_16x16x64_bf16 v[56:59], v[36:39], v[232:239], v124\n" \
    "ds_read_b128 v[40:43], v106 offset:2048\n" \
    "s_waitcnt lgkmcnt(3)\n" \
    "v_exp_f32 v92, v92\n" \
    "v_cvt_pk_bf16_f32 v89, v90, v91\n" \
    "v_smfmac_f32_16x16x64_bf16 v[64:67], v[48:51], v[72:79], v124\n" \
    "ds_read_b128 v[44:47], v106 offset:3072\n" \
    "v_exp_f32 v93, v93\n" \
    "v_smfmac_f32_16x16x64_bf16 v[64:67], v[52:55], v[80:87], v124\n" \
    "ds_read_b128 v[72:75], v106 offset:34816\n" \
    "v_exp_f32 v94, v94\n" \
    "v_cvt_pk_bf16_f32 v90, v92, v93\n" \
    "v_smfmac_f32_16x16x64_bf16 v[60:63], v[24:27], v[192:199], v124\n" \
    "ds_read_b128 v[76:79], v106 offset:35840\n" \
    "v_exp_f32 v95, v95\n" \
    "v_smfmac_f32_16x16x64_bf16 v[60:63], v[28:31], v[208:215], v124\n" \
    "ds_read_b128 v[80:83], v106 offset:4096\n" \
    "s_cmp_eq_u32 s36, 62\n" \
    "v_add_f32 v64, v64, v66\n" \
    "v_add_f32 v65, v65, v67\n" \
    "v_smfmac_f32_16x16x64_bf16 v[60:63], v[32:35], v[224:231], v124\n" \
    "ds_read_b128 v[84:87], v106 offset:5120\n" \
    "v_exp_f32 v64, v64\n" \
    "v_cvt_pk_bf16_f32 v91, v94, v95\n" \
    "v_smfmac_f32_16x16x64_bf16 v[60:63], v[36:39], v[240:247], v124\n" \
    "ds_read_b128 v[48:51], v106 offset:36864\n" \
    "v_exp_f32 v65, v65\n" \
    "v_swap_b32 v1, v2\n" \
    "v_swap_b32 v5, v6\n" \
    "v_mfma_f32_16x16x32_bf16 v[102:105], v[88:91], v[120:123], 0\n" \
    "ds_read_b128 v[52:55], v106 offset:37888\n" \
    "v_cvt_pk_bf16_f32 v100, v64, v65\n" \
    "v_add_f32 v56, v56, v58\n" \
    "v_add_f32 v57, v57, v59\n" \
    "v_smfmac_f32_16x16x64_bf16 v[68:71], v[88:91], v[0:7], v124\n" \
    "ds_read_b128 v[24:27], v106 offset:6144\n" \
    "v_add_f32 v60, v60, v62\n" \
    "v_add_f32 v61, v61, v63\n" \
    "v_mfma_f32_16x16x16_bf16 v[102:105], v[100:101], v[120:121], v[102:105]\n" \
    "ds_read_b128 v[28:31], v106 offset:7168\n" \
    "v_cvt_pk_bf16_f32 v96, v56, v57\n" \
    "v_cvt_pk_bf16_f32 v97, v60, v61\n" \
    "s_nop 1\n" \
    "v_smfmac_f32_16x16x32_bf16 v[68:71], v[100:101], v[96:99], v124\n" \
    "ds_read_b128 v[32:35], v106 offset:38912\n" \
    "v_mov_b64 v[56:57], 0\n" \
    "v_mov_b64 v[58:59], 0\n" \
    "v_rcp_f32 v104, v102\n" \
    "v_rcp_f32 v105, v103\n" \
    "v_mul_f32 v68, v68, v104\n" \
    "v_mul_f32 v70, v70, v104\n" \
    "v_mul_f32 v69, v69, v105\n" \
    "v_mul_f32 v71, v71, v105\n" \
    "v_cvt_pk_bf16_f32 v107, v68, v70\n" \
    "v_cvt_pk_bf16_f32 v108, v69, v71\n" \
    "ds_write2_b32 v129, v107, v108 offset0:64 offset1:208\n" \
    "v_max3_f32 v102, v102, v103, v104\n" \
    "v_max_f32 v102, v102, v105\n" \
    "v_cmp_nge_f32 vcc, s82, v102\n" \
    "v_mov_b64 v[60:61], 0\n" \
    "v_mov_b64 v[62:63], 0\n" \
    "v_mov_b64 v[88:89], 0\n" \
    "v_mov_b64 v[90:91], 0\n" \
    "v_mov_b64 v[92:93], 0\n" \
    "v_mov_b64 v[94:95], 0\n" \
    "s_waitcnt vmcnt(2) lgkmcnt(0)\n" \
    "s_barrier\n" \
    "ds_read_b128 v[68:71], v109 offset:256\n" \
    "ds_read_b128 v[102:105], v111 offset:384\n" \
    "s_cbranch_vccnz 7f\n" \
    "buffer_load_dwordx2 v[98:99], v113, %[rs], s78 offen sc1\n" \
    "buffer_load_dword v100, off, %[rw], s75 sc1\n" \
    "v_mov_b64 v[64:65], 0\n" \
    "v_mov_b64 v[66:67], 0\n" \
    "s_waitcnt lgkmcnt(1)\n" \
    "v_smfmac_f32_16x16x64_bf16 v[56:59], v[68:71], v[8:15], v124\n" \
    "s_waitcnt lgkmcnt(0)\n" \
    "ds_read_b128 v[36:39], v106 offset:39936\n" \
    "v_smfmac_f32_16x16x64_bf16 v[56:59], v[102:105], v[16:23], v124\n" \
    "ds_read_b128 v[0:3], v133 offset:51200\n" \
    "v_smfmac_f32_16x16x64_bf16 v[60:63], v[68:71], v[40:47], v124\n" \
    "ds_read_b128 v[4:7], v133 offset:52224\n" \
    "v_smfmac_f32_16x16x64_bf16 v[60:63], v[102:105], v[72:79], v124\n" \
    "ds_read_b128 v[16:19], v112 offset:51200\n" \
    "v_add_f32 v56, v56, v58\n" \
    "v_smfmac_f32_16x16x64_bf16 v[88:91], v[68:71], v[80:87], v124\n" \
    "ds_read_b128 v[20:23], v112 offset:52224\n" \
    "v_add_f32 v57, v57, v59\n" \
    "v_smfmac_f32_16x16x64_bf16 v[88:91], v[102:105], v[48:55], v124\n" \
    "v_add_f32 v60, v60, v62\n" \
    "v_smfmac_f32_16x16x64_bf16 v[92:95], v[68:71], v[24:31], v124\n" \
    "s_waitcnt lgkmcnt(4)\n" \
    "v_add_f32 v61, v61, v63\n" \
    "v_smfmac_f32_16x16x64_bf16 v[92:95], v[102:105], v[32:39], v124\n" \
    "s_waitcnt lgkmcnt(2)\n" \
    "v_add_f32 v88, v88, v90\n" \
    "v_smfmac_f32_16x16x64_bf16 v[64:67], v[68:71], v[0:7], v124\n" \
    "s_waitcnt lgkmcnt(0)\n" \
    "v_add_f32 v89, v89, v91\n" \
    "v_smfmac_f32_16x16x64_bf16 v[64:67], v[102:105], v[16:23], v124\n" \
    "v_add_f32 v92, v92, v94\n" \
    "v_add_f32 v93, v93, v95\n" \
    "v_cvt_pk_bf16_f32 v8, v56, v60\n" \
    "v_cvt_pk_bf16_f32 v10, v57, v61\n" \
    "v_cvt_pk_bf16_f32 v9, v88, v92\n" \
    "v_cvt_pk_bf16_f32 v11, v89, v93\n" \
    "buffer_store_dwordx4 v[8:11], v134, %[rs], s79 offen sc1\n" \
    "v_add_f32 v64, v64, v66\n" \
    "v_add_f32 v65, v65, v67\n" \
    "v_cvt_pk_bf16_f32 v64, v64, v65\n" \
    "buffer_store_dword v64, v135, %[rs], s79 offen sc1\n" \
    "s_cbranch_scc1 16f\n" \
    "s_memtime s[88:89]\n" \
    "s_memrealtime s[44:45]\n" \
    "s_and_b32 s75, s91, 3\n" \
    "s_lshl_b32 s75, s75, 16\n" \
    "s_memrealtime s[46:47]\n" \
    "s_add_i32 s69, s75, s26\n" \
    "s_xor_b32 s70, s69, 2048\n" \
    "s_memrealtime s[48:49]\n" \
    "s_andn2_b32 s71, s69, 0x1fff\n" \
    "s_add_i32 s71, s71, 32768\n" \
    "s_memrealtime s[50:51]\n" \
    "s_mov_b32 s94, 0\n" \
    "s_mov_b32 s43, 0\n" \
    "s_memrealtime s[52:53]\n" \
    "s_cmp_lt_i32 s38, s91\n" \
    "s_nop 0\n" \
    "s_memrealtime s[54:55]\n" \
    "s_nop 1\n" \
    "s_memrealtime s[56:57]\n" \
    "s_nop 1\n" \
    "s_memrealtime vcc\n" \
    "v_and_b32 v19, 0x380, v127\n" \
    "v_add_u32 v19, v19, v127\n" \
    "v_and_b32 v18, 0x300, v127\n" \
    "v_add_u32 v18, v18, v127\n" \
    "s_cbranch_scc1 40f\n" \
    "41:\n" \
    "buffer_load_dwordx4 v19, %[rw], s92 offen sc1 lds\n" \
    "s_add_i32 m0, s95, 896\n" \
    "s_waitcnt lgkmcnt(0)\n" \
    "s_add_i32 s44, s44, s46\n" \
    "s_add_i32 s48, s48, s50\n" \
    "s_add_i32 s52, s52, s54\n" \
    "s_add_i32 s44, s44, s48\n" \
    "s_add_i32 s52, s52, s56\n" \
    "s_add_i32 s44, s44, s52\n" \
    "s_add_i32 s80, s44, vcc_lo\n" \
    "s_sub_i32 s45, s80, s29\n" \
    "s_mov_b32 s29, s80\n" \
    "s_sub_i32 s47, s88, s24\n" \
    "s_mov_b32 s24, s88\n" \
    "buffer_load_dwordx4 v19, %[rw], s92 offen offset:128 sc1 lds\n" \
    "s_add_i32 m0, s95, 8192\n" \
    "s_add_i32 s76, s29, s42\n" \
    "v_add_u32 v16, -1, s36\n" \
    "v_mov_b32 v17, s76\n" \
    "s_mov_b64 exec, s[72:73]\n" \
    "buffer_store_dwordx2 v[16:17], off, %[rw], s65 sc1\n" \
    "s_mov_b64 exec, -1\n" \
    "ds_read_b128 v[72:75], v133 offset:0\n" \
    "ds_read_b128 v[76:79], v133 offset:1024\n" \
    "ds_read_b128 v[80:83], v133 offset:2048\n" \
    "ds_read_b128 v[84:87], v133 offset:3072\n" \
    "ds_read_b128 v[88:91], v133 offset:16384\n" \
    "ds_read_b128 v[92:95], v133 offset:17408\n" \
    "buffer_load_dwordx4 v18, %[rw], s93 offen sc1 lds\n" \
    "s_add_i32 m0, s95, 8960\n" \
    "v_mov_b64 v[40:41], 0\n" \
    "v_mov_b64 v[42:43], 0\n" \
    "v_mov_b64 v[44:45], 0\n" \
    "v_mov_b64 v[46:47], 0\n" \
    "v_mov_b64 v[48:49], 0\n" \
    "v_mov_b64 v[50:51], 0\n" \
    "v_mov_b64 v[52:53], 0\n" \
    "v_mov_b64 v[54:55], 0\n" \
    "buffer_load_dwordx4 v18, %[rw], s93 offen offset:256 sc1 lds\n" \
    "s_movk_i32 m0, 0x2000\n" \
    "v_mov_b64 v[56:57], 0\n" \
    "v_mov_b64 v[58:59], 0\n" \
    "v_mov_b64 v[60:61], 0\n" \
    "v_mov_b64 v[62:63], 0\n" \
    "v_mov_b64 v[64:65], 0\n" \
    "v_mov_b64 v[66:67], 0\n" \
    "v_mov_b64 v[68:69], 0\n" \
    "v_mov_b64 v[70:71], 0\n" \
    "v_add_u32 v108, s74, v127\n" \
    "v_xor_b32 v24, 0x400, v108\n" \
    "v_xor_b32 v28, 0x800, v108\n" \
    "v_xor_b32 v32, 0xc00, v108\n" \
    "v_and_b32 v22, 0x200, v127\n" \
    "s_and_b32 s76, s26, 0x800\n" \
    "v_mad_u32_u24 v22, v22, 3, v127\n" \
    "v_xor_b32 v22, s76, v22\n" \
    "s_waitcnt vmcnt(7)\n" \
    "v_cmp_ne_u16 vcc, s91, v99\n" \
    "v_readfirstlane_b32 s80, v100\n" \
    "s_max_i32 s38, s38, s80\n" \
    "s_cbranch_vccnz 15f\n" \
    "10:\n" \
    "v_subrev_u32 v20, s29, v98\n" \
    "s_bitcmp1_b32 s30, 15\n" \
    "v_min_i32 v20, 0x4000, v20\n" \
    "v_max_i32 v20, 0xffffc000, v20\n" \
    "s_cbranch_scc1 18f\n" \
    "19:\n" \
    "v_readlane_b32 s79, v20, s83\n" \
    "v_max_i32_dpp v20, v20, v20 row_shr:1 row_mask:0xf bank_mask:0xf\n" \
    "s_mul_i32 s49, s45, s25\n" \
    "s_lshr_b32 s49, s49, 12\n" \
    "v_max_i32_dpp v20, v20, v20 row_shr:2 row_mask:0xf bank_mask:0xf\n" \
    "s_sub_i32 s49, s47, s49\n" \
    "s_mul_hi_u32 s47, s47, s41\n" \
    "v_max_i32_dpp v20, v20, v20 row_shr:4 row_mask:0xf bank_mask:0xf\n" \
    "s_sub_i32 s47, s45, s47\n" \
    "s_lshl_b32 s47, s47, 18\n" \
    "v_max_i32_dpp v20, v20, v20 row_shr:8 row_mask:0xf bank_mask:0xf\n" \
    "s_cmp_lt_u32 s45, 3000\n" \
    "s_cselect_b32 s49, s49, 0\n" \
    "v_max_i32_dpp v20, v20, v20 row_bcast:15 row_mask:0xa bank_mask:0xf\n" \
    "s_cselect_b32 s47, s47, 0\n" \
    "s_add_i32 s25, s25, s49\n" \
    "v_max_i32_dpp v20, v20, v20 row_bcast:31 row_mask:0xc bank_mask:0xf\n" \
    "s_add_i32 s41, s41, s47\n" \
    "s_add_i32 s76, s42, s28\n" \
    "s_add_i32 s79, s79, s76\n" \
    "v_readlane_b32 s37, v20, 63\n" \
    "s_add_i32 s37, s37, s76\n" \
    "s_cmp_lg_u32 s94, 0\n" \
    "s_cselect_b32 s36, s36, s91\n" \
    "s_cbranch_scc0 12b\n" \
    "s_branch 9f\n" \
    "5:\n" \
    "v_mov_b32 v107, 0\n" \
    "v_max3_u32 v107, v107, v0, v1\n" \
    "v_max3_u32 v107, v107, v2, v3\n" \
    "v_max3_u32 v107, v107, v4, v5\n" \
    "v_max3_u32 v107, v107, v6, v7\n" \
    "v_max3_u32 v107, v107, v8, v9\n" \
    "v_max3_u32 v107, v107, v10, v11\n" \
    "v_max3_u32 v107, v107, v12, v13\n" \
    "v_max3_u32 v107, v107, v14, v15\n" \
    "v_max3_u32 v107, v107, v16, v17\n" \
    "v_max3_u32 v107, v107, v18, v19\n" \
    "v_max3_u32 v107, v107, v24, v25\n" \
    "v_max3_u32 v107, v107, v26, v27\n" \
    "v_max3_u32 v107, v107, v28, v29\n" \
    "v_max3_u32 v107, v107, v30, v31\n" \
    "v_max3_u32 v107, v107, v32, v33\n" \
    "v_max3_u32 v107, v107, v34, v35\n" \
    "v_max3_u32 v107, v107, v36, v37\n" \
    "v_max3_u32 v107, v107, v38, v39\n" \
    "v_cmp_eq_u32 vcc, -1, v107\n" \
    "s_nop 4\n" \
    "s_cbranch_vccz 6b\n" \
    "s_cmp_eq_u32 s36, 0\n" \
    "s_cbranch_scc1 6b\n" \
    "s_add_i32 s39, s39, -1\n" \
    "s_cmp_lt_i32 s39, 0\n" \
    "s_cbranch_scc1 6b\n" \
    "s_add_i32 s30, s30, 1\n" \
    "s_bitset1_b32 s30, 15\n" \
    "s_and_b32 s80, s31, 0xff\n" \
    "s_add_i32 s42, s42, s80\n" \
    "s_bfe_u32 s80, s31, 0x80008\n" \
    "s_add_i32 s40, s40, s80\n" \
    "s_waitcnt lgkmcnt(0)\n" \
    "s_mov_b32 s37, s29\n" \
    "s_nop 7\n" \
    "s_barrier\n" \
    "s_branch 0b\n" \
    "15:\n" \
    "s_add_i32 s30, s30, 0x10000\n" \
    "s_add_i32 s39, s39, -1\n" \
    "s_cmp_lt_i32 s39, 0\n" \
    "s_cbranch_scc1 10b\n" \
    "s_sleep 9\n" \
    "buffer_load_dwordx2 v[98:99], v113, %[rs], s78 offen sc1\n" \
    "s_waitcnt vmcnt(0)\n" \
    "v_cmp_ne_u16 vcc, s91, v99\n" \
    "s_nop 1\n" \
    "s_cbranch_vccnz 15b\n" \
    "s_memtime s[88:89]\n" \
    "s_memrealtime s[44:45]\n" \
    "s_nop 1\n" \
    "s_memrealtime s[46:47]\n" \
    "s_nop 1\n" \
    "s_memrealtime s[48:49]\n" \
    "s_nop 1\n" \
    "s_memrealtime s[50:51]\n" \
    "s_nop 1\n" \
    "s_memrealtime s[52:53]\n" \
    "s_nop 1\n" \
    "s_memrealtime s[54:55]\n" \
    "s_nop 1\n" \
    "s_memrealtime s[56:57]\n" \
    "s_nop 1\n" \
    "s_memrealtime vcc\n" \
    "s_waitcnt lgkmcnt(0)\n" \
    "s_add_i32 s44, s44, s46\n" \
    "s_add_i32 s48, s48, s50\n" \
    "s_add_i32 s52, s52, s54\n" \
    "s_add_i32 s44, s44, s48\n" \
    "s_add_i32 s52, s52, s56\n" \
    "s_add_i32 s44, s44, s52\n" \
    "s_add_i32 s29, s44, vcc_lo\n" \
    "s_mov_b32 s24, s88\n" \
    "s_mov_b32 s45, -1\n" \
    "s_branch 10b\n" \
    "18:\n" \
    "v_cmp_gt_i32 vcc, -320, v20\n" \
    "s_bitset0_b32 s30, 15\n" \
    "s_lshr_b32 s80, s31, 16\n" \
    "s_cmp_lt_u32 s36, 4\n" \
    "s_cselect_b32 s80, 0, s80\n" \
    "s_min_u32 s76, s80, 1\n" \
    "s_lshl_b32 s76, s76, 16\n" \
    "s_sub_i32 s31, s31, s76\n" \
    "s_mul_i32 s76, s80, 768\n" \
    "s_cmp_eq_u64 vcc, 0\n" \
    "s_cselect_b32 s80, 0, s80\n" \
    "s_cselect_b32 s76, s76, 0\n" \
    "s_add_i32 s33, s33, s80\n" \
    "s_add_i32 s32, s32, s76\n" \
    "s_branch 19b\n" \
    "40:\n" \
    "s_waitcnt vmcnt(2)\n" \
    "v_readfirstlane_b32 s80, v100\n" \
    "s_max_i32 s38, s38, s80\n" \
    "s_cmp_lt_i32 s38, s91\n" \
    "s_cbranch_scc0 41b\n" \
    "s_mov_b32 s94, 8\n" \
    "v_mov_b32 v18, 0x7ffffff0\n" \
    "v_mov_b32 v19, 0x7ffffff0\n" \
    "s_branch 41b\n" \
    "17:\n" \
    "ds_read_b128 v[0:3], v108 offset:0\n" \
    "ds_read_b128 v[16:19], v108 offset:4096\n" \
    "s_waitcnt lgkmcnt(0)\n" \
    "s_branch 3b\n" \
    "20:\n" \
    "s_mov_b32 s91, 63\n" \
    "s_mov_b32 s78, 0x1a8000\n" \
    "buffer_load_dwordx2 v[98:99], v113, %[rs], s78 offen sc1\n" \
    "buffer_load_dword v100, off, %[rw], s62 sc1\n" \
    "s_waitcnt vmcnt(0)\n" \
    "16:\n" \
    "v_and_b32 v112, 0x300, v127\n" \
    "v_add_u32 v112, v112, v127\n" \
    "s_add_i32 s44, %[qb], 0\n" \
    "s_add_i32 s45, s44, 4096\n" \
    "buffer_load_dwordx4 v[136:139], v112, %[rw], s44 offen offset:0 sc1\n" \
    "v_lshrrev_b32 v103, 1, v127\n" \
    "s_lshr_b32 s76, s74, 1\n" \
    "v_add_u32 v101, s76, v103\n" \
    "ds_write_b64 v101, v[96:97] offset:9216\n" \
    "ds_read_b128 v[0:3], v127 offset:16384\n" \
    "v_lshrrev_b32 v42, 8, v127\n" \
    "v_bfe_u32 v40, v127, 4, 4\n" \
    "ds_read_b128 v[4:7], v127 offset:17408\n" \
    "v_and_b32 v41, 2, v42\n" \
    "v_add_u32 v43, v41, v42\n" \
    "ds_read_b128 v[8:11], v127 offset:18432\n" \
    "s_lshl_b32 s76, s60, 4\n" \
    "s_and_b32 s76, s76, 16\n" \
    "v_add_u32 v40, s76, v40\n" \
    "v_mad_i32_i24 v40, v43, -4, v40\n" \
    "buffer_load_dwordx4 v[140:143], v112, %[rw], s44 offen offset:2048 sc1\n" \
    "ds_read_b128 v[12:15], v127 offset:19456\n" \
    "v_mov_b32 v43, 0xff61b1e6\n" \
    "v_cmp_gt_i32 vcc, 0, v40\n" \
    "ds_read_b128 v[16:19], v127 offset:20480\n" \
    "v_cndmask_b32 v88, 0, v43, vcc\n" \
    "v_cmp_gt_i32 vcc, 1, v40\n" \
    "ds_read_b128 v[20:23], v127 offset:21504\n" \
    "v_cndmask_b32 v89, 0, v43, vcc\n" \
    "v_cmp_gt_i32 vcc, 2, v40\n" \
    "ds_read_b128 v[24:27], v127 offset:22528\n" \
    "v_cndmask_b32 v90, 0, v43, vcc\n" \
    "v_cmp_gt_i32 vcc, 3, v40\n" \
    "buffer_load_dwordx4 v[144:147], v112, %[rw], s45 offen offset:0 sc1\n" \
    "ds_read_b128 v[28:31], v127 offset:23552\n" \
    "v_cndmask_b32 v91, 0, v43, vcc\n" \
    "v_cmp_gt_i32 vcc, 8, v40\n" \
    "ds_read_b128 v[32:35], v131 offset:0\n" \
    "v_cndmask_b32 v92, 0, v43, vcc\n" \
    "v_cmp_gt_i32 vcc, 9, v40\n" \
    "ds_read_b128 v[36:39], v131 offset:128\n" \
    "v_cndmask_b32 v93, 0, v43, vcc\n" \
    "v_cmp_gt_i32 vcc, 10, v40\n" \
    "ds_read_b128 v[56:59], v127 offset:24576\n" \
    "v_cndmask_b32 v94, 0, v43, vcc\n" \
    "v_cmp_gt_i32 vcc, 11, v40\n" \
    "buffer_load_dwordx4 v[148:151], v112, %[rw], s45 offen offset:2048 sc1\n" \
    "ds_read_b128 v[60:63], v127 offset:25600\n" \
    "v_cndmask_b32 v95, 0, v43, vcc\n" \
    "v_mad_u32_u24 v44, v41, 3, v42\n" \
    "ds_read_b128 v[64:67], v127 offset:26624\n" \
    "v_bfe_u32 v45, v127, 6, 1\n" \
    "v_bfe_u32 v46, v127, 4, 2\n" \
    "s_waitcnt lgkmcnt(8)\n" \
    "ds_read_b128 v[68:71], v127 offset:27648\n" \
    "v_lshl_add_u32 v44, v46, 1, v44\n" \
    "v_bfe_u32 v46, v127, 7, 1\n" \
    "ds_read_b128 a[120:123], v127 offset:28672\n" \
    "v_lshl_add_u32 v44, v44, 1, v46\n" \
    "v_mul_u32_u24 v44, 4352, v44\n" \
    "ds_read_b128 a[124:127], v127 offset:29696\n" \
    "v_lshl_add_u32 v44, v45, 1, v44\n" \
    "v_lshlrev_b32 v45, 2, v45\n" \
    "ds_read_b128 v[80:83], v127 offset:30720\n" \
    "s_lshr_b32 s79, %[u], 4\n" \
    "s_mul_i32 s79, s79, 32 * 4352\n" \
    "s_and_b32 s94, %[u], 15\n" \
    "s_lshl2_add_u32 s79, s94, s79\n" \
    "s_add_i32 s79, s79, 0x3e00fc0\n" \
    "s_lshl3_add_u32 s94, s94, 0x4065f80\n" \
    "s_lshr_b32 s80, s60, 1\n" \
    "s_mul_i32 s76, s80, 49152\n" \
    "s_add_i32 s76, s76, 98304\n" \
    "v_add_u32 v128, s76, v127\n" \
    "v_xor_b32 v130, 0x38000, v128\n" \
    "ds_read_b128 v[84:87], v127 offset:31744\n" \
    "s_lshl_b32 s76, s80, 12\n" \
    "s_add_i32 s76, s76, 83968\n" \
    "v_add_u32 v132, s76, v127\n" \
    "s_add_i32 s92, s64, 16515072\n" \
    "s_add_i32 s93, s92, 0x1000000\n" \
    "s_mov_b32 m0, s95\n" \
    "v_and_b32 v97, 0x380, v127\n" \
    "v_add_u32 v97, v97, v127\n" \
    "s_waitcnt vmcnt(6)\n" \
    "v_readfirstlane_b32 s80, v100\n" \
    "v_cmp_ne_u16 vcc, s91, v99\n" \
    "s_max_i32 s38, s38, s80\n" \
    "s_cmp_lt_i32 s38, s91\n" \
    "s_cbranch_scc1 21f\n" \
    "22:\n" \
    "s_cbranch_vccnz 23f\n" \
    "24:\n" \
    "buffer_load_ushort v113, v44, %[rw], s79 offen sc1\n" \
    "buffer_load_dword v114, v45, %[rw], s94 offen sc1\n" \
    "v_subrev_u32 v96, s29, v98\n" \
    "v_min_i32 v96, 0x4000, v96\n" \
    "v_max_i32 v96, 0, v96\n" \
    "s_waitcnt lgkmcnt(0)\n" \
    "s_barrier\n" \
    "v_max_i32_dpp v96, v96, v96 row_shr:1 row_mask:0xf bank_mask:0xf\n" \
    "ds_read_b64 v[52:53], v103 offset:9216\n" \
    "s_and_b32 s75, s91, 3\n" \
    "v_max_i32_dpp v96, v96, v96 row_shr:2 row_mask:0xf bank_mask:0xf\n" \
    "ds_read_b64 v[54:55], v103 offset:9728\n" \
    "s_lshl_b32 s75, s75, 16\n" \
    "v_max_i32_dpp v96, v96, v96 row_shr:4 row_mask:0xf bank_mask:0xf\n" \
    "ds_read_b64 v[100:101], v103 offset:10240\n" \
    "s_add_i32 s69, s75, s26\n" \
    "v_max_i32_dpp v96, v96, v96 row_shr:8 row_mask:0xf bank_mask:0xf\n" \
    "ds_read_b64 v[102:103], v103 offset:10752\n" \
    "s_xor_b32 s70, s69, 2048\n" \
    "v_max_i32_dpp v96, v96, v96 row_bcast:15 row_mask:0xa bank_mask:0xf\n" \
    "ds_read_b128 a[0:3], v128 offset:0\n" \
    "s_andn2_b32 s71, s69, 0x1fff\n" \
    "v_max_i32_dpp v96, v96, v96 row_bcast:31 row_mask:0xc bank_mask:0xf\n" \
    "ds_read_b128 a[16:19], v128 offset:2048\n" \
    "s_add_i32 s71, s71, 32768\n" \
    "v_readlane_b32 s90, v96, 63\n" \
    "s_add_i32 s90, s90, s42\n" \
    "s_mul_i32 s90, s90, s25\n" \
    "s_ashr_i32 s90, s90, 12\n" \
    "s_add_i32 s90, s90, s24\n" \
    "buffer_load_dwordx4 v97, %[rw], s92 offen sc1 lds\n" \
    "s_add_i32 m0, s95, 896\n" \
    "v_mov_b32 v99, 0\n" \
    "buffer_load_dwordx4 v97, %[rw], s92 offen offset:128 sc1 lds\n" \
    "s_add_i32 m0, s95, 8192\n" \
    "v_mov_b32 v96, 0\n" \
    "25:\n" \
    "v_mov_b64 v[48:49], 0\n" \
    "v_mov_b64 v[50:51], 0\n" \
    "s_waitcnt vmcnt(7)\n" \
    "v_mfma_f32_16x16x32_bf16 v[40:43], v[0:3], v[136:139], v[88:91]\n" \
    "ds_read_b128 a[4:7], v128 offset:1024\n" \
    "v_max3_u32 v98, v136, v137, v136\n" \
    "v_mfma_f32_16x16x32_bf16 v[44:47], v[4:7], v[136:139], v[92:95]\n" \
    "ds_read_b128 a[20:23], v128 offset:3072\n" \
    "v_max3_u32 v98, v138, v139, v98\n" \
    "s_waitcnt vmcnt(6)\n" \
    "v_mfma_f32_16x16x32_bf16 v[40:43], v[8:11], v[140:143], v[40:43]\n" \
    "ds_read_b128 a[8:11], v130 offset:0\n" \
    "v_max3_u32 v98, v140, v141, v98\n" \
    "v_mfma_f32_16x16x32_bf16 v[44:47], v[12:15], v[140:143], v[44:47]\n" \
    "ds_read_b128 a[24:27], v130 offset:2048\n" \
    "v_max3_u32 v98, v142, v143, v98\n" \
    "v_smfmac_f32_16x16x64_bf16 v[48:51], v[32:35], v[136:143], v124\n" \
    "ds_read_b128 a[12:15], v130 offset:1024\n" \
    "s_waitcnt vmcnt(5)\n" \
    "v_mfma_f32_16x16x32_bf16 v[40:43], v[16:19], v[144:147], v[40:43]\n" \
    "ds_read_b128 a[28:31], v130 offset:3072\n" \
    "v_max3_u32 v98, v144, v145, v98\n" \
    "v_mfma_f32_16x16x32_bf16 v[44:47], v[20:23], v[144:147], v[44:47]\n" \
    "ds_read_b128 a[32:35], v128 offset:4096\n" \
    "v_max3_u32 v98, v146, v147, v98\n" \
    "s_waitcnt vmcnt(4)\n" \
    "v_smfmac_f32_16x16x64_bf16 v[48:51], v[36:39], v[144:151], v124\n" \
    "ds_read_b128 a[48:51], v128 offset:6144\n" \
    "v_mfma_f32_16x16x32_bf16 v[40:43], v[24:27], v[148:151], v[40:43]\n" \
    "ds_read_b128 a[36:39], v128 offset:5120\n" \
    "v_max3_u32 v98, v148, v149, v98\n" \
    "v_max3_u32 v98, v150, v151, v98\n" \
    "v_mfma_f32_16x16x32_bf16 v[44:47], v[28:31], v[148:151], v[44:47]\n" \
    "v_cmp_eq_u32 vcc, -1, v98\n" \
    "s_add_i32 s44, %[qb], 262144\n" \
    "s_add_i32 s45, s44, 4096\n" \
    "s_waitcnt lgkmcnt(3)\n" \
    "v_add_f32 v48, v48, v50\n" \
    "v_add_f32 v49, v49, v51\n" \
    "s_nop 0\n" \
    "s_cbranch_vccnz 26f\n" \
    "27:\n" \
    "v_max3_f32 v0, v40, v41, v42\n" \
    "v_max3_f32 v1, v43, v44, v45\n" \
    "v_max3_f32 v0, v0, v46, v47\n" \
    "v_max3_f32 v1, v1, v48, v49\n" \
    "v_max_f32 v0, v0, v1\n" \
    "v_max_f32 v1, v0, v1\n" \
    "buffer_load_dwordx4 v112, %[rw], s93 offen sc1 lds\n" \
    "s_add_i32 m0, s95, 8960\n" \
    "s_nop 0\n" \
    "v_permlane16_swap_b32 v0, v1\n" \
    "v_max_f32 v0, v0, v1\n" \
    "v_max_f32 v1, v0, v1\n" \
    "buffer_load_dwordx4 v112, %[rw], s93 offen offset:256 sc1 lds\n" \
    "s_nop 0\n" \
    "v_permlane32_swap_b32 v0, v1\n" \
    "v_max_f32 v0, v0, v1\n" \
    "v_mul_f32 v0, s81, v0\n" \
    "v_fma_f32 v40, v40, s81, -v0\n" \
    "v_fma_f32 v41, v41, s81, -v0\n" \
    "ds_read_b128 v[136:139], v132 offset:0\n" \
    "v_exp_f32 v40, v40\n" \
    "v_exp_f32 v41, v41\n" \
    "v_fma_f32 v42, v42, s81, -v0\n" \
    "ds_read_b128 v[140:143], v132 offset:1024\n" \
    "v_fma_f32 v43, v43, s81, -v0\n" \
    "v_exp_f32 v42, v42\n" \
    "v_exp_f32 v43, v43\n" \
    "ds_read_b128 v[144:147], v132 offset:8192\n" \
    "v_cvt_pk_bf16_f32 v32, v40, v41\n" \
    "v_fma_f32 v44, v44, s81, -v0\n" \
    "v_fma_f32 v45, v45, s81, -v0\n" \
    "ds_read_b128 v[148:151], v132 offset:9216\n" \
    "v_exp_f32 v44, v44\n" \
    "v_exp_f32 v45, v45\n" \
    "v_cvt_pk_bf16_f32 v33, v42, v43\n" \
    "ds_read_b128 a[52:55], v128 offset:7168\n" \
    "v_fma_f32 v46, v46, s81, -v0\n" \
    "v_fma_f32 v47, v47, s81, -v0\n" \
    "v_exp_f32 v46, v46\n" \
    "s_memtime vcc\n" \
    "v_exp_f32 v47, v47\n" \
    "v_cvt_pk_bf16_f32 v34, v44, v45\n" \
    "v_fma_f32 v48, v48, s81, -v0\n" \
    "v_fma_f32 v49, v49, s81, -v0\n" \
    "v_exp_f32 v48, v48\n" \
    "v_exp_f32 v49, v49\n" \
    "v_cvt_pk_bf16_f32 v35, v46, v47\n" \
    "v_cvt_pk_bf16_f32 v98, v48, v49\n" \
    "v_cvt_pk_bf16_f32 v97, v48, v49\n" \
    "s_waitcnt lgkmcnt(0)\n" \
    "v_mfma_f32_16x16x32_bf16 v[36:39], v[120:123], v[32:35], 0\n" \
    "s_sub_i32 s75, s90, vcc_lo\n" \
    "s_cmp_gt_i32 s75, 100\n" \
    "v_mfma_f32_16x16x32_bf16 v[0:3], v[56:59], v[32:35], 0\n" \
    "s_cbranch_scc1 30f\n" \
    "31:\n" \
    "ds_read_b128 a[40:43], v130 offset:4096\n" \
    "v_and_b32 v44, 0x200, v127\n" \
    "s_and_b32 s80, s26, 0x800\n" \
    "v_mad_u32_u24 v44, v44, 3, v127\n" \
    "v_mfma_f32_16x16x32_bf16 v[4:7], v[60:63], v[32:35], 0\n" \
    "s_mov_b32 m0, s74\n" \
    "s_add_i32 s76, s74, 4096\n" \
    "buffer_load_dwordx4 v125, %[rs], s69 offen sc1 lds\n" \
    "s_mov_b32 m0, s76\n" \
    "v_mfma_f32_16x16x32_bf16 v[8:11], v[64:67], v[32:35], 0\n" \
    "ds_read_b128 a[56:59], v130 offset:6144\n" \
    "v_xor_b32 v44, s80, v44\n" \
    "v_bfe_u32 v40, v127, 6, 2\n" \
    "v_mfma_f32_16x16x32_bf16 v[12:15], v[68:71], v[32:35], 0\n" \
    "buffer_load_dwordx4 v125, %[rs], s70 offen sc1 lds\n" \
    "s_movk_i32 m0, 0x2000\n" \
    "v_mfma_f32_16x16x32_bf16 v[16:19], a[120:123], v[32:35], 0\n" \
    "ds_read_b128 a[44:47], v130 offset:5120\n" \
    "v_bfe_u32 v41, v127, 5, 1\n" \
    "v_bfe_u32 v42, v127, 4, 1\n" \
    "v_mfma_f32_16x16x32_bf16 v[20:23], a[124:127], v[32:35], 0\n" \
    "s_mov_b32 exec_lo, s27\n" \
    "s_mov_b32 exec_hi, s27\n" \
    "buffer_load_dwordx4 v44, %[rs], s71 offen sc1 lds\n" \
    "s_mov_b64 exec, -1\n" \
    "v_mfma_f32_16x16x32_bf16 v[24:27], v[80:83], v[32:35], 0\n" \
    "ds_read_b128 a[60:63], v130 offset:7168\n" \
    "v_lshrrev_b32 v43, 8, v127\n" \
    "v_lshlrev_b32 v134, 14, v40\n" \
    "v_mfma_f32_16x16x32_bf16 v[28:31], v[84:87], v[32:35], 0\n" \
    "ds_read_b128 a[64:67], v128 offset:8192\n" \
    "v_lshl_add_u32 v134, v41, 12, v134\n" \
    "v_lshl_add_u32 v134, v42, 6, v134\n" \
    "v_mfma_f32_16x16x16_bf16 v[36:39], v[120:121], v[98:99], v[36:39]\n" \
    "ds_read_b128 a[80:83], v128 offset:10240\n" \
    "v_lshl_add_u32 v134, v43, 4, v134\n" \
    "v_and_b32 v41, 2, v40\n" \
    "v_mfma_f32_16x16x16_bf16 v[0:3], v[52:53], v[98:99], v[0:3]\n" \
    "buffer_load_dwordx4 v[72:75], v112, %[rw], s44 offen offset:0 sc1\n" \
    "v_mfma_f32_16x16x16_bf16 v[4:7], v[52:53], v[96:97], v[4:7]\n" \
    "ds_read_b128 a[68:71], v128 offset:9216\n" \
    "v_add_lshl_u32 v135, v40, v41, 12\n" \
    "v_bfe_u32 v42, v127, 4, 2\n" \
    "v_mfma_f32_16x16x16_bf16 v[8:11], v[54:55], v[98:99], v[8:11]\n" \
    "buffer_load_dwordx4 v[76:79], v112, %[rw], s44 offen offset:2048 sc1\n" \
    "v_mfma_f32_16x16x16_bf16 v[12:15], v[54:55], v[96:97], v[12:15]\n" \
    "ds_read_b128 a[84:87], v128 offset:11264\n" \
    "v_lshl_add_u32 v135, v42, 5, v135\n" \
    "v_lshl_add_u32 v135, v43, 3, v135\n" \
    "v_mfma_f32_16x16x16_bf16 v[16:19], v[100:101], v[98:99], v[16:19]\n" \
    "buffer_load_dwordx4 v[104:107], v112, %[rw], s45 offen offset:0 sc1\n" \
    "v_mfma_f32_16x16x16_bf16 v[20:23], v[100:101], v[96:97], v[20:23]\n" \
    "ds_read_b128 a[72:75], v130 offset:8192\n" \
    "v_mfma_f32_16x16x16_bf16 v[24:27], v[102:103], v[98:99], v[24:27]\n" \
    "buffer_load_dwordx4 v[108:111], v112, %[rw], s45 offen offset:2048 sc1\n" \
    "v_mfma_f32_16x16x16_bf16 v[28:31], v[102:103], v[96:97], v[28:31]\n" \
    "ds_read_b128 a[88:91], v130 offset:10240\n" \
    "v_cvt_pk_bf16_f32 v40, v0, v4\n" \
    "v_cvt_pk_bf16_f32 v41, v1, v5\n" \
    "v_cvt_pk_bf16_f32 v42, v2, v6\n" \
    "v_cvt_pk_bf16_f32 v43, v3, v7\n" \
    "v_cvt_pk_bf16_f32 v44, v8, v12\n" \
    "v_cvt_pk_bf16_f32 v45, v9, v13\n" \
    "v_mfma_f32_16x16x32_bf16 v[64:67], v[136:139], v[40:43], 0\n" \
    "s_mov_b32 s46, %[tp]\n" \
    "ds_read_b128 a[76:79], v130 offset:9216\n" \
    "v_cvt_pk_bf16_f32 v46, v10, v14\n" \
    "v_cvt_pk_bf16_f32 v47, v11, v15\n" \
    "v_mfma_f32_16x16x32_bf16 v[56:59], a[0:3], v[40:43], 0\n" \
    "s_mov_b32 s47, %[tx]\n" \
    "ds_read_b128 a[92:95], v130 offset:11264\n" \
    "v_cvt_pk_bf16_f32 v48, v16, v20\n" \
    "v_cvt_pk_bf16_f32 v49, v17, v21\n" \
    "v_mfma_f32_16x16x32_bf16 v[64:67], v[140:143], v[44:47], v[64:67]\n" \
    "ds_read_b128 a[96:99], v128 offset:12288\n" \
    "v_cvt_pk_bf16_f32 v50, v18, v22\n" \
    "v_cvt_pk_bf16_f32 v51, v19, v23\n" \
    "v_mfma_f32_16x16x32_bf16 v[60:63], a[16:19], v[40:43], 0\n" \
    "s_waitcnt lgkmcnt(5)\n" \
    "ds_read_b128 a[112:115], v128 offset:14336\n" \
    "v_cvt_pk_bf16_f32 v52, v24, v28\n" \
    "v_cvt_pk_bf16_f32 v53, v25, v29\n" \
    "v_mfma_f32_16x16x32_bf16 v[64:67], v[144:147], v[48:51], v[64:67]\n" \
    "ds_read_b128 a[100:103], v128 offset:13312\n" \
    "v_cvt_pk_bf16_f32 v54, v26, v30\n" \
    "v_cvt_pk_bf16_f32 v55, v27, v31\n" \
    "v_mfma_f32_16x16x32_bf16 v[56:59], a[4:7], v[44:47], v[56:59]\n" \
    "ds_read_b128 a[116:119], v128 offset:15360\n" \
    "v_rcp_f32 v36, v36\n" \
    "v_mfma_f32_16x16x32_bf16 v[64:67], v[148:151], v[52:55], v[64:67]\n" \
    "ds_read_b128 a[104:107], v130 offset:12288\n" \
    "v_add_u32 v37, s74, v127\n" \
    "v_xor_b32 v38, 0x400, v37\n" \
    "v_mfma_f32_16x16x32_bf16 v[60:63], a[20:23], v[44:47], v[60:63]\n" \
    "ds_read_b128 a[120:123], v130 offset:14336\n" \
    "v_xor_b32 v39, 0x800, v37\n" \
    "v_xor_b32 v132, 0xc00, v37\n" \
    "v_mfma_f32_16x16x32_bf16 v[56:59], a[8:11], v[48:51], v[56:59]\n" \
    "ds_read_b128 a[108:111], v130 offset:13312\n" \
    "v_mul_f32 v64, v64, v36\n" \
    "v_mul_f32 v65, v65, v36\n" \
    "v_mfma_f32_16x16x32_bf16 v[60:63], a[24:27], v[48:51], v[60:63]\n" \
    "ds_read_b128 a[124:127], v130 offset:15360\n" \
    "v_mul_f32 v66, v66, v36\n" \
    "v_mul_f32 v67, v67, v36\n" \
    "v_mfma_f32_16x16x32_bf16 v[56:59], a[12:15], v[52:55], v[56:59]\n" \
    "v_cvt_pk_bf16_f32 v100, v64, v65\n" \
    "v_cvt_pk_bf16_f32 v101, v66, v67\n" \
    "v_mfma_f32_16x16x32_bf16 v[60:63], a[28:31], v[52:55], v[60:63]\n" \
    "buffer_store_dwordx2 v[100:101], v135, %[rw], s47 offen sc1\n" \
    "v_mfma_f32_16x16x32_bf16 v[64:67], a[32:35], v[40:43], 0\n" \
    "v_mul_f32 v56, v56, v36\n" \
    "v_mul_f32 v57, v57, v36\n" \
    "v_mfma_f32_16x16x32_bf16 v[68:71], a[48:51], v[40:43], 0\n" \
    "v_mul_f32 v58, v58, v36\n" \
    "v_mul_f32 v59, v59, v36\n" \
    "v_mfma_f32_16x16x32_bf16 v[64:67], a[36:39], v[44:47], v[64:67]\n" \
    "v_mul_f32 v60, v60, v36\n" \
    "v_mul_f32 v61, v61, v36\n" \
    "v_mfma_f32_16x16x32_bf16 v[68:71], a[52:55], v[44:47], v[68:71]\n" \
    "v_mul_f32 v62, v62, v36\n" \
    "v_mul_f32 v63, v63, v36\n" \
    "v_mfma_f32_16x16x32_bf16 v[64:67], a[40:43], v[48:51], v[64:67]\n" \
    "v_cvt_pk_bf16_f32 v96, v56, v57\n" \
    "v_cvt_pk_bf16_f32 v97, v58, v59\n" \
    "v_mfma_f32_16x16x32_bf16 v[68:71], a[56:59], v[48:51], v[68:71]\n" \
    "v_cvt_pk_bf16_f32 v98, v60, v61\n" \
    "v_cvt_pk_bf16_f32 v99, v62, v63\n" \
    "v_mfma_f32_16x16x32_bf16 v[64:67], a[44:47], v[52:55], v[64:67]\n" \
    "buffer_store_dwordx4 v[96:99], v134, %[rw], s46 offen sc1\n" \
    "s_add_i32 s46, s46, 0x20000\n" \
    "v_mfma_f32_16x16x32_bf16 v[68:71], a[60:63], v[52:55], v[68:71]\n" \
    "v_mov_b64 v[80:81], 0\n" \
    "v_mov_b64 v[82:83], 0\n" \
    "v_mfma_f32_16x16x32_bf16 v[56:59], a[64:67], v[40:43], 0\n" \
    "v_mul_f32 v64, v64, v36\n" \
    "v_mul_f32 v65, v65, v36\n" \
    "v_mfma_f32_16x16x32_bf16 v[60:63], a[80:83], v[40:43], 0\n" \
    "v_mul_f32 v66, v66, v36\n" \
    "v_mul_f32 v67, v67, v36\n" \
    "v_mfma_f32_16x16x32_bf16 v[56:59], a[68:71], v[44:47], v[56:59]\n" \
    "v_mul_f32 v68, v68, v36\n" \
    "v_mul_f32 v69, v69, v36\n" \
    "v_mfma_f32_16x16x32_bf16 v[60:63], a[84:87], v[44:47], v[60:63]\n" \
    "v_mul_f32 v70, v70, v36\n" \
    "v_mul_f32 v71, v71, v36\n" \
    "s_waitcnt lgkmcnt(11)\n" \
    "v_mfma_f32_16x16x32_bf16 v[56:59], a[72:75], v[48:51], v[56:59]\n" \
    "v_cvt_pk_bf16_f32 v100, v64, v65\n" \
    "v_cvt_pk_bf16_f32 v101, v66, v67\n" \
    "s_waitcnt lgkmcnt(10)\n" \
    "v_mfma_f32_16x16x32_bf16 v[60:63], a[88:91], v[48:51], v[60:63]\n" \
    "s_waitcnt vmcnt(6) lgkmcnt(0)\n" \
    "s_barrier\n" \
    "v_cvt_pk_bf16_f32 v102, v68, v69\n" \
    "v_cvt_pk_bf16_f32 v103, v70, v71\n" \
    "v_mfma_f32_16x16x32_bf16 v[56:59], a[76:79], v[52:55], v[56:59]\n" \
    "ds_read_b128 v[0:3], v37 offset:0\n" \
    "buffer_store_dwordx4 v[100:103], v134, %[rw], s46 offen sc1\n" \
    "s_add_i32 s46, s46, 0x20000\n" \
    "v_mfma_f32_16x16x32_bf16 v[60:63], a[92:95], v[52:55], v[60:63]\n" \
    "ds_read_b128 v[4:7], v38 offset:0\n" \
    "v_mov_b64 v[84:85], 0\n" \
    "v_mov_b64 v[86:87], 0\n" \
    "v_mfma_f32_16x16x32_bf16 v[64:67], a[96:99], v[40:43], 0\n" \
    "ds_read_b128 v[8:11], v39 offset:0\n" \
    "v_mul_f32 v56, v56, v36\n" \
    "v_mul_f32 v57, v57, v36\n" \
    "v_mfma_f32_16x16x32_bf16 v[68:71], a[112:115], v[40:43], 0\n" \
    "ds_read_b128 v[12:15], v132 offset:0\n" \
    "v_mul_f32 v58, v58, v36\n" \
    "v_mul_f32 v59, v59, v36\n" \
    "v_mfma_f32_16x16x32_bf16 v[64:67], a[100:103], v[44:47], v[64:67]\n" \
    "ds_read_b128 v[16:19], v37 offset:4096\n" \
    "v_mul_f32 v60, v60, v36\n" \
    "v_mul_f32 v61, v61, v36\n" \
    "v_mfma_f32_16x16x32_bf16 v[68:71], a[116:119], v[44:47], v[68:71]\n" \
    "ds_read_b128 v[20:23], v38 offset:4096\n" \
    "v_mul_f32 v62, v62, v36\n" \
    "v_mul_f32 v63, v63, v36\n" \
    "v_mfma_f32_16x16x32_bf16 v[64:67], a[104:107], v[48:51], v[64:67]\n" \
    "ds_read_b128 v[24:27], v39 offset:4096\n" \
    "v_cvt_pk_bf16_f32 v96, v56, v57\n" \
    "v_cvt_pk_bf16_f32 v97, v58, v59\n" \
    "v_mfma_f32_16x16x32_bf16 v[68:71], a[120:123], v[48:51], v[68:71]\n" \
    "ds_read_b128 v[28:31], v132 offset:4096\n" \
    "v_cvt_pk_bf16_f32 v98, v60, v61\n" \
    "v_cvt_pk_bf16_f32 v99, v62, v63\n" \
    "v_mfma_f32_16x16x32_bf16 v[64:67], a[108:111], v[52:55], v[64:67]\n" \
    "ds_read_b128 v[32:35], v126 offset:0\n" \
    "buffer_store_dwordx4 v[96:99], v134, %[rw], s46 offen sc1\n" \
    "s_add_i32 s46, s46, 0x20000\n" \
    "v_mfma_f32_16x16x32_bf16 v[68:71], a[124:127], v[52:55], v[68:71]\n" \
    "s_waitcnt lgkmcnt(3)\n" \
    "33:\n" \
    "v_smfmac_f32_16x16x64_bf16 v[80:83], v[0:3], a[128:135], v124\n" \
    "v_mul_f32 v64, v64, v36\n" \
    "v_smfmac_f32_16x16x64_bf16 v[80:83], v[4:7], a[144:151], v124\n" \
    "v_mul_f32 v65, v65, v36\n" \
    "v_smfmac_f32_16x16x64_bf16 v[80:83], v[8:11], a[160:167], v124\n" \
    "s_waitcnt lgkmcnt(0)\n" \
    "v_mul_f32 v66, v66, v36\n" \
    "v_smfmac_f32_16x16x64_bf16 v[80:83], v[12:15], a[176:183], v124\n" \
    "ds_read_b128 v[40:43], v127 offset:16384\n" \
    "v_mul_f32 v67, v67, v36\n" \
    "v_smfmac_f32_16x16x64_bf16 v[80:83], v[16:19], a[192:199], v124\n" \
    "ds_read_b128 v[44:47], v127 offset:17408\n" \
    "v_mul_f32 v68, v68, v36\n" \
    "v_smfmac_f32_16x16x64_bf16 v[80:83], v[20:23], a[208:215], v124\n" \
    "ds_read_b128 v[48:51], v127 offset:18432\n" \
    "v_mul_f32 v69, v69, v36\n" \
    "v_smfmac_f32_16x16x64_bf16 v[80:83], v[24:27], a[224:231], v124\n" \
    "ds_read_b128 v[52:55], v127 offset:19456\n" \
    "v_mul_f32 v70, v70, v36\n" \
    "v_mul_f32 v71, v71, v36\n" \
    "v_smfmac_f32_16x16x64_bf16 v[80:83], v[28:31], a[240:247], v124\n" \
    "ds_read_b128 v[56:59], v127 offset:20480\n" \
    "v_cvt_pk_bf16_f32 v100, v64, v65\n" \
    "v_cvt_pk_bf16_f32 v101, v66, v67\n" \
    "v_smfmac_f32_16x16x64_bf16 v[80:83], v[32:35], v[152:159], v124\n" \
    "ds_read_b128 v[60:63], v127 offset:21504\n" \
    "v_cvt_pk_bf16_f32 v102, v68, v69\n" \
    "v_cvt_pk_bf16_f32 v103, v70, v71\n" \
    "v_smfmac_f32_16x16x64_bf16 v[84:87], v[0:3], a[136:143], v124\n" \
    "ds_read_b128 v[64:67], v127 offset:22528\n" \
    "buffer_store_dwordx4 v[100:103], v134, %[rw], s46 offen sc1\n" \
    "v_smfmac_f32_16x16x64_bf16 v[84:87], v[4:7], a[152:159], v124\n" \
    "ds_read_b128 v[68:71], v127 offset:23552\n" \
    "v_add_f32 v80, v80, v82\n" \
    "v_add_f32 v81, v81, v83\n" \
    "v_smfmac_f32_16x16x64_bf16 v[84:87], v[8:11], a[168:175], v124\n" \
    "s_waitcnt vmcnt(5)\n" \
    "v_cmp_u_f32 vcc, v80, v81\n" \
    "v_max3_u32 v128, v72, v73, v72\n" \
    "v_smfmac_f32_16x16x64_bf16 v[84:87], v[12:15], a[184:191], v124\n" \
    "v_max3_u32 v128, v74, v75, v128\n" \
    "v_max3_u32 v128, v76, v77, v128\n" \
    "v_smfmac_f32_16x16x64_bf16 v[84:87], v[16:19], a[200:207], v124\n" \
    "s_cbranch_vccnz 32f\n" \
    "34:\n" \
    "v_max3_u32 v128, v78, v79, v128\n" \
    "v_max3_u32 v128, v104, v105, v128\n" \
    "v_smfmac_f32_16x16x64_bf16 v[84:87], v[20:23], a[216:223], v124\n" \
    "v_max3_u32 v128, v106, v107, v128\n" \
    "v_max3_u32 v128, v108, v109, v128\n" \
    "v_smfmac_f32_16x16x64_bf16 v[84:87], v[24:27], a[232:239], v124\n" \
    "v_max3_u32 v128, v110, v111, v128\n" \
    "v_cmp_eq_u32 vcc, -1, v128\n" \
    "v_smfmac_f32_16x16x64_bf16 v[84:87], v[28:31], a[248:255], v124\n" \
    "s_waitcnt lgkmcnt(0)\n" \
    "v_smfmac_f32_16x16x64_bf16 v[84:87], v[32:35], v[160:167], v124\n" \
    "s_cbranch_vccnz 28f\n" \
    "29:\n" \
    "v_mfma_f32_16x16x32_bf16 v[152:155], v[40:43], v[72:75], v[88:91]\n" \
    "ds_read_b128 a[128:131], v133 offset:0\n" \
    "v_mov_b64 v[96:97], 0\n" \
    "v_mov_b64 v[98:99], 0\n" \
    "v_mfma_f32_16x16x32_bf16 v[156:159], v[44:47], v[72:75], v[92:95]\n" \
    "ds_read_b128 a[132:135], v133 offset:1024\n" \
    "v_add_f32 v84, v84, v86\n" \
    "v_add_f32 v85, v85, v87\n" \
    "v_mfma_f32_16x16x32_bf16 v[152:155], v[48:51], v[76:79], v[152:155]\n" \
    "ds_read_b128 a[136:139], v133 offset:2048\n" \
    "v_cvt_pk_bf16_f32 v80, v80, v84\n" \
    "v_cvt_pk_bf16_f32 v81, v81, v85\n" \
    "v_mfma_f32_16x16x32_bf16 v[156:159], v[52:55], v[76:79], v[156:159]\n" \
    "ds_write2_b32 v129, v80, v81 offset0:0 offset1:144\n" \
    "v_mfma_f32_16x16x32_bf16 v[152:155], v[56:59], v[104:107], v[152:155]\n" \
    "ds_read_b128 a[140:143], v133 offset:3072\n" \
    "v_mov_b64 v[160:161], 0\n" \
    "v_mov_b64 v[162:163], 0\n" \
    "v_mfma_f32_16x16x32_bf16 v[156:159], v[60:63], v[104:107], v[156:159]\n" \
    "ds_read_b128 a[144:147], v133 offset:16384\n" \
    "v_mov_b64 v[100:101], 0\n" \
    "v_mov_b64 v[102:103], 0\n" \
    "v_mfma_f32_16x16x32_bf16 v[152:155], v[64:67], v[108:111], v[152:155]\n" \
    "ds_read_b128 a[148:151], v133 offset:17408\n" \
    "v_lshrrev_b32 v42, 1, v37\n" \
    "v_lshrrev_b32 v43, 1, v127\n" \
    "v_mfma_f32_16x16x32_bf16 v[156:159], v[68:71], v[108:111], v[156:159]\n" \
    "s_waitcnt lgkmcnt(3)\n" \
    "s_barrier\n" \
    "ds_read_b128 v[80:83], v131 offset:0\n" \
    "ds_read_b128 v[84:87], v131 offset:128\n" \
    "v_smfmac_f32_16x16x64_bf16 v[96:99], v[16:19], v[168:175], v124\n" \
    "ds_read_b128 a[152:155], v133 offset:18432\n" \
    "v_mov_b32 v61, 0\n" \
    "v_smfmac_f32_16x16x64_bf16 v[96:99], v[20:23], v[184:191], v124\n" \
    "ds_read_b128 a[156:159], v133 offset:19456\n" \
    "v_max3_f32 v40, v152, v153, v154\n" \
    "v_smfmac_f32_16x16x64_bf16 v[96:99], v[24:27], v[200:207], v124\n" \
    "ds_read_b128 a[160:163], v133 offset:32768\n" \
    "s_waitcnt lgkmcnt(3)\n" \
    "v_max3_f32 v41, v155, v156, v157\n" \
    "v_smfmac_f32_16x16x64_bf16 v[160:163], v[80:83], v[72:79], v124\n" \
    "ds_read_b128 a[164:167], v133 offset:33792\n" \
    "v_max3_f32 v40, v40, v158, v159\n" \
    "v_smfmac_f32_16x16x64_bf16 v[160:163], v[84:87], v[104:111], v124\n" \
    "ds_read_b128 a[168:171], v133 offset:34816\n" \
    "v_mov_b32 v62, 0\n" \
    "v_smfmac_f32_16x16x64_bf16 v[96:99], v[28:31], v[216:223], v124\n" \
    "ds_read_b128 a[172:175], v133 offset:35840\n" \
    "v_smfmac_f32_16x16x64_bf16 v[96:99], v[32:35], v[232:239], v124\n" \
    "ds_read_b128 a[176:179], v133 offset:49152\n" \
    "v_add_f32 v160, v160, v162\n" \
    "v_add_f32 v161, v161, v163\n" \
    "v_max3_f32 v41, v41, v160, v161\n" \
    "v_smfmac_f32_16x16x64_bf16 v[100:103], v[16:19], v[176:183], v124\n" \
    "ds_read_b128 a[180:183], v133 offset:50176\n" \
    "v_max_f32 v40, v40, v41\n" \
    "v_max_f32 v41, v40, v41\n" \
    "v_smfmac_f32_16x16x64_bf16 v[100:103], v[20:23], v[192:199], v124\n" \
    "ds_read_b128 a[192:195], v127 offset:24576\n" \
    "v_permlane16_swap_b32 v40, v41\n" \
    "v_max_f32 v40, v40, v41\n" \
    "v_max_f32 v41, v40, v41\n" \
    "v_smfmac_f32_16x16x64_bf16 v[96:99], v[0:3], a[128:135], v124\n" \
    "ds_read_b128 a[196:199], v127 offset:25600\n" \
    "v_permlane32_swap_b32 v40, v41\n" \
    "v_max_f32 v40, v40, v41\n" \
    "v_mul_f32 v40, s81, v40\n" \
    "v_smfmac_f32_16x16x64_bf16 v[96:99], v[4:7], a[144:151], v124\n" \
    "ds_read_b128 a[200:203], v127 offset:26624\n" \
    "s_waitcnt lgkmcnt(3)\n" \
    "v_fma_f32 v152, v152, s81, -v40\n" \
    "v_fma_f32 v153, v153, s81, -v40\n" \
    "v_exp_f32 v152, v152\n" \
    "v_smfmac_f32_16x16x64_bf16 v[96:99], v[8:11], a[160:167], v124\n" \
    "ds_read_b128 a[204:207], v127 offset:27648\n" \
    "v_exp_f32 v153, v153\n" \
    "v_fma_f32 v154, v154, s81, -v40\n" \
    "v_fma_f32 v155, v155, s81, -v40\n" \
    "v_smfmac_f32_16x16x64_bf16 v[96:99], v[12:15], a[176:183], v124\n" \
    "ds_read_b128 a[208:211], v127 offset:28672\n" \
    "v_exp_f32 v154, v154\n" \
    "v_exp_f32 v155, v155\n" \
    "v_cvt_pk_bf16_f32 v56, v152, v153\n" \
    "v_smfmac_f32_16x16x64_bf16 v[100:103], v[24:27], v[208:215], v124\n" \
    "ds_read_b128 a[212:215], v127 offset:29696\n" \
    "v_fma_f32 v156, v156, s81, -v40\n" \
    "v_fma_f32 v157, v157, s81, -v40\n" \
    "v_exp_f32 v156, v156\n" \
    "v_smfmac_f32_16x16x64_bf16 v[100:103], v[28:31], v[224:231], v124\n" \
    "ds_read_b128 a[216:219], v127 offset:30720\n" \
    "v_exp_f32 v157, v157\n" \
    "v_cvt_pk_bf16_f32 v57, v154, v155\n" \
    "v_fma_f32 v158, v158, s81, -v40\n" \
    "v_smfmac_f32_16x16x64_bf16 v[100:103], v[32:35], v[240:247], v124\n" \
    "ds_read_b128 a[220:223], v127 offset:31744\n" \
    "v_fma_f32 v159, v159, s81, -v40\n" \
    "v_exp_f32 v158, v158\n" \
    "v_exp_f32 v159, v159\n" \
    "v_smfmac_f32_16x16x64_bf16 v[100:103], v[12:15], v[248:255], v124\n" \
    "v_cvt_pk_bf16_f32 v58, v156, v157\n" \
    "v_fma_f32 v160, v160, s81, -v40\n" \
    "v_fma_f32 v161, v161, s81, -v40\n" \
    "v_smfmac_f32_16x16x64_bf16 v[100:103], v[0:3], a[136:143], v124\n" \
    "v_exp_f32 v160, v160\n" \
    "v_exp_f32 v161, v161\n" \
    "v_cvt_pk_bf16_f32 v59, v158, v159\n" \
    "v_smfmac_f32_16x16x64_bf16 v[100:103], v[4:7], a[152:159], v124\n" \
    "v_cvt_pk_bf16_f32 v60, v160, v161\n" \
    "v_cvt_pk_bf16_f32 v63, v160, v161\n" \
    "v_smfmac_f32_16x16x64_bf16 v[100:103], v[8:11], a[168:175], v124\n" \
    "s_waitcnt lgkmcnt(0)\n" \
    "v_mfma_f32_16x16x32_bf16 v[36:39], v[120:123], v[56:59], 0\n" \
    "v_add_f32 v96, v96, v98\n" \
    "v_add_f32 v97, v97, v99\n" \
    "v_mfma_f32_16x16x32_bf16 v[0:3], a[192:195], v[56:59], 0\n" \
    "v_add_f32 v100, v100, v102\n" \
    "v_add_f32 v101, v101, v103\n" \
    "v_mfma_f32_16x16x32_bf16 v[4:7], a[196:199], v[56:59], 0\n" \
    "v_cvt_pk_bf16_f32 v96, v96, v97\n" \
    "v_cvt_pk_bf16_f32 v97, v100, v101\n" \
    "v_mfma_f32_16x16x32_bf16 v[8:11], a[200:203], v[56:59], 0\n" \
    "ds_write_b64 v42, v[96:97] offset:9216\n" \
    "v_mfma_f32_16x16x32_bf16 v[12:15], a[204:207], v[56:59], 0\n" \
    "v_lshrrev_b32 v169, 8, v127\n" \
    "v_and_b32 v170, 2, v169\n" \
    "v_mfma_f32_16x16x32_bf16 v[16:19], a[208:211], v[56:59], 0\n" \
    "v_mad_u32_u24 v169, v170, 3, v169\n" \
    "v_bfe_u32 v168, v127, 6, 1\n" \
    "v_mfma_f32_16x16x32_bf16 v[20:23], a[212:215], v[56:59], 0\n" \
    "v_bfe_u32 v170, v127, 4, 2\n" \
    "v_lshl_add_u32 v169, v170, 1, v169\n" \
    "v_mfma_f32_16x16x32_bf16 v[24:27], a[216:219], v[56:59], 0\n" \
    "s_waitcnt lgkmcnt(0)\n" \
    "s_barrier\n" \
    "ds_read_b64 v[64:65], v43 offset:9216\n" \
    "ds_read_b64 v[66:67], v43 offset:9728\n" \
    "ds_read_b64 v[68:69], v43 offset:10240\n" \
    "ds_read_b64 v[70:71], v43 offset:10752\n" \
    "v_mfma_f32_16x16x32_bf16 v[28:31], a[220:223], v[56:59], 0\n" \
    "v_mul_u32_u24 v168, 1152, v168\n" \
    "v_lshl_add_u32 v168, v169, 2, v168\n" \
    "v_mfma_f32_16x16x16_bf16 v[36:39], v[120:121], v[60:61], v[36:39]\n" \
    "s_waitcnt lgkmcnt(2)\n" \
    "v_mfma_f32_16x16x16_bf16 v[0:3], v[64:65], v[60:61], v[0:3]\n" \
    "v_sub_f32 v169, 1.0, v114\n" \
    "v_lshlrev_b32 v172, 16, v113\n" \
    "v_mfma_f32_16x16x16_bf16 v[4:7], v[64:65], v[62:63], v[4:7]\n" \
    "s_waitcnt lgkmcnt(0)\n" \
    "v_mul_f32 v172, v172, v169\n" \
    "v_and_b32 v170, 16, v127\n" \
    "v_mfma_f32_16x16x16_bf16 v[8:11], v[66:67], v[60:61], v[8:11]\n" \
    "v_mov_b32 v169, 0x3f80\n" \
    "v_lshlrev_b32 v170, v170, v169\n" \
    "v_mfma_f32_16x16x16_bf16 v[12:15], v[66:67], v[62:63], v[12:15]\n" \
    "v_mov_b32_dpp v173, v172 row_ror:8 row_mask:0xf bank_mask:0xf\n" \
    "v_mov_b64 v[176:177], 0\n" \
    "v_mfma_f32_16x16x16_bf16 v[16:19], v[68:69], v[60:61], v[16:19]\n" \
    "v_mov_b64 v[178:179], 0\n" \
    "v_mov_b32_dpp v176, v170 quad_perm:[0,1,2,3] row_mask:0xf bank_mask:0x1\n" \
    "v_mfma_f32_16x16x16_bf16 v[20:23], v[68:69], v[62:63], v[20:23]\n" \
    "v_mov_b32_dpp v177, v170 quad_perm:[0,1,2,3] row_mask:0xf bank_mask:0x2\n" \
    "v_mov_b32_dpp v178, v170 quad_perm:[0,1,2,3] row_mask:0xf bank_mask:0x4\n" \
    "v_mfma_f32_16x16x16_bf16 v[24:27], v[70:71], v[60:61], v[24:27]\n" \
    "v_mov_b32_dpp v179, v170 quad_perm:[0,1,2,3] row_mask:0xf bank_mask:0x8\n" \
    "v_mfma_f32_16x16x16_bf16 v[28:31], v[70:71], v[62:63], v[28:31]\n" \
    "v_cvt_pk_bf16_f32 v40, v0, v4\n" \
    "v_cvt_pk_bf16_f32 v41, v1, v5\n" \
    "v_cvt_pk_bf16_f32 v42, v2, v6\n" \
    "v_cvt_pk_bf16_f32 v43, v3, v7\n" \
    "v_cvt_pk_bf16_f32 v44, v8, v12\n" \
    "v_cvt_pk_bf16_f32 v45, v9, v13\n" \
    "v_mfma_f32_16x16x32_bf16 v[80:83], v[136:139], v[40:43], 0\n" \
    "s_mov_b32 s46, %[tq]\n" \
    "v_cvt_pk_bf16_f32 v46, v10, v14\n" \
    "v_cvt_pk_bf16_f32 v47, v11, v15\n" \
    "v_mfma_f32_16x16x32_bf16 v[72:75], a[0:3], v[40:43], 0\n" \
    "s_add_i32 s47, %[tx], 0x40000\n" \
    "v_cvt_pk_bf16_f32 v48, v16, v20\n" \
    "v_cvt_pk_bf16_f32 v49, v17, v21\n" \
    "v_mfma_f32_16x16x32_bf16 v[80:83], v[140:143], v[44:47], v[80:83]\n" \
    "v_cvt_pk_bf16_f32 v50, v18, v22\n" \
    "v_cvt_pk_bf16_f32 v51, v19, v23\n" \
    "v_mfma_f32_16x16x32_bf16 v[76:79], a[16:19], v[40:43], 0\n" \
    "v_cvt_pk_bf16_f32 v52, v24, v28\n" \
    "v_cvt_pk_bf16_f32 v53, v25, v29\n" \
    "v_mfma_f32_16x16x32_bf16 v[80:83], v[144:147], v[48:51], v[80:83]\n" \
    "v_cvt_pk_bf16_f32 v54, v26, v30\n" \
    "v_cvt_pk_bf16_f32 v55, v27, v31\n" \
    "v_mfma_f32_16x16x32_bf16 v[72:75], a[4:7], v[44:47], v[72:75]\n" \
    "v_rcp_f32 v36, v36\n" \
    "v_mfma_f32_16x16x32_bf16 v[80:83], v[148:151], v[52:55], v[80:83]\n" \
    "s_and_b32 s94, %[u], 15\n" \
    "s_mul_i32 s94, s94, 2304\n" \
    "v_mfma_f32_16x16x32_bf16 v[76:79], a[20:23], v[44:47], v[76:79]\n" \
    "s_andn2_b32 s77, %[u], 15\n" \
    "s_lshl2_add_u32 s94, s77, s94\n" \
    "v_mfma_f32_16x16x32_bf16 v[72:75], a[8:11], v[48:51], v[72:75]\n" \
    "v_mul_f32 v80, v80, v36\n" \
    "v_mul_f32 v81, v81, v36\n" \
    "v_mfma_f32_16x16x32_bf16 v[76:79], a[24:27], v[48:51], v[76:79]\n" \
    "v_mul_f32 v82, v82, v36\n" \
    "v_mul_f32 v83, v83, v36\n" \
    "v_mfma_f32_16x16x32_bf16 v[72:75], a[12:15], v[52:55], v[72:75]\n" \
    "v_cvt_pk_bf16_f32 v100, v80, v81\n" \
    "v_cvt_pk_bf16_f32 v101, v82, v83\n" \
    "v_mfma_f32_16x16x32_bf16 v[76:79], a[28:31], v[52:55], v[76:79]\n" \
    "buffer_store_dwordx2 v[100:101], v135, %[rw], s47 offen sc1\n" \
    "v_mfma_f32_16x16x32_bf16 v[80:83], a[32:35], v[40:43], 0\n" \
    "v_mul_f32 v72, v72, v36\n" \
    "v_mul_f32 v73, v73, v36\n" \
    "v_mfma_f32_16x16x32_bf16 v[84:87], a[48:51], v[40:43], 0\n" \
    "v_mul_f32 v74, v74, v36\n" \
    "v_mul_f32 v75, v75, v36\n" \
    "v_mfma_f32_16x16x32_bf16 v[80:83], a[36:39], v[44:47], v[80:83]\n" \
    "v_mul_f32 v76, v76, v36\n" \
    "v_mul_f32 v77, v77, v36\n" \
    "v_mfma_f32_16x16x32_bf16 v[84:87], a[52:55], v[44:47], v[84:87]\n" \
    "v_mul_f32 v78, v78, v36\n" \
    "v_mul_f32 v79, v79, v36\n" \
    "v_mfma_f32_16x16x32_bf16 v[80:83], a[40:43], v[48:51], v[80:83]\n" \
    "v_cvt_pk_bf16_f32 v96, v72, v73\n" \
    "v_cvt_pk_bf16_f32 v97, v74, v75\n" \
    "v_mfma_f32_16x16x32_bf16 v[84:87], a[56:59], v[48:51], v[84:87]\n" \
    "v_cvt_pk_bf16_f32 v98, v76, v77\n" \
    "v_cvt_pk_bf16_f32 v99, v78, v79\n" \
    "v_mfma_f32_16x16x32_bf16 v[80:83], a[44:47], v[52:55], v[80:83]\n" \
    "buffer_store_dwordx4 v[96:99], v134, %[rw], s46 offen sc1\n" \
    "s_add_i32 s46, s46, 0x20000\n" \
    "v_mfma_f32_16x16x32_bf16 v[84:87], a[60:63], v[52:55], v[84:87]\n" \
    "s_add_i32 s94, s94, 2322432\n" \
    "v_mfma_f32_16x16x32_bf16 v[72:75], a[64:67], v[40:43], 0\n" \
    "v_mul_f32 v80, v80, v36\n" \
    "v_mul_f32 v81, v81, v36\n" \
    "v_mfma_f32_16x16x32_bf16 v[76:79], a[80:83], v[40:43], 0\n" \
    "v_mul_f32 v82, v82, v36\n" \
    "v_mul_f32 v83, v83, v36\n" \
    "v_mfma_f32_16x16x32_bf16 v[72:75], a[68:71], v[44:47], v[72:75]\n" \
    "v_mul_f32 v84, v84, v36\n" \
    "v_mul_f32 v85, v85, v36\n" \
    "v_mfma_f32_16x16x32_bf16 v[76:79], a[84:87], v[44:47], v[76:79]\n" \
    "v_mul_f32 v86, v86, v36\n" \
    "v_mul_f32 v87, v87, v36\n" \
    "v_mfma_f32_16x16x32_bf16 v[72:75], a[72:75], v[48:51], v[72:75]\n" \
    "v_cvt_pk_bf16_f32 v100, v80, v81\n" \
    "v_cvt_pk_bf16_f32 v101, v82, v83\n" \
    "v_mfma_f32_16x16x32_bf16 v[76:79], a[88:91], v[48:51], v[76:79]\n" \
    "v_cvt_pk_bf16_f32 v102, v84, v85\n" \
    "v_cvt_pk_bf16_f32 v103, v86, v87\n" \
    "v_mfma_f32_16x16x32_bf16 v[72:75], a[76:79], v[52:55], v[72:75]\n" \
    "buffer_store_dwordx4 v[100:103], v134, %[rw], s46 offen sc1\n" \
    "s_add_i32 s46, s46, 0x20000\n" \
    "v_mfma_f32_16x16x32_bf16 v[76:79], a[92:95], v[52:55], v[76:79]\n" \
    "v_mfma_f32_16x16x32_bf16 v[80:83], a[96:99], v[40:43], 0\n" \
    "v_mul_f32 v72, v72, v36\n" \
    "v_mul_f32 v73, v73, v36\n" \
    "v_mfma_f32_16x16x32_bf16 v[84:87], a[112:115], v[40:43], 0\n" \
    "v_mul_f32 v74, v74, v36\n" \
    "v_mul_f32 v75, v75, v36\n" \
    "v_mfma_f32_16x16x32_bf16 v[80:83], a[100:103], v[44:47], v[80:83]\n" \
    "v_mul_f32 v76, v76, v36\n" \
    "v_mul_f32 v77, v77, v36\n" \
    "v_mfma_f32_16x16x32_bf16 v[84:87], a[116:119], v[44:47], v[84:87]\n" \
    "v_mul_f32 v78, v78, v36\n" \
    "v_mul_f32 v79, v79, v36\n" \
    "v_mfma_f32_16x16x32_bf16 v[80:83], a[104:107], v[48:51], v[80:83]\n" \
    "v_cvt_pk_bf16_f32 v96, v72, v73\n" \
    "v_cvt_pk_bf16_f32 v97, v74, v75\n" \
    "v_mfma_f32_16x16x32_bf16 v[84:87], a[120:123], v[48:51], v[84:87]\n" \
    "v_cvt_pk_bf16_f32 v98, v76, v77\n" \
    "v_cvt_pk_bf16_f32 v99, v78, v79\n" \
    "v_mfma_f32_16x16x32_bf16 v[80:83], a[108:111], v[52:55], v[80:83]\n" \
    "buffer_store_dwordx4 v[96:99], v134, %[rw], s46 offen sc1\n" \
    "s_add_i32 s46, s46, 0x20000\n" \
    "v_mfma_f32_16x16x32_bf16 v[84:87], a[124:127], v[52:55], v[84:87]\n" \
    "s_memtime vcc\n" \
    "s_nop 2\n" \
    "v_mul_f32 v80, v80, v36\n" \
    "v_mul_f32 v81, v81, v36\n" \
    "v_mul_f32 v82, v82, v36\n" \
    "v_mul_f32 v83, v83, v36\n" \
    "v_mul_f32 v84, v84, v36\n" \
    "v_mul_f32 v85, v85, v36\n" \
    "v_mul_f32 v86, v86, v36\n" \
    "v_mul_f32 v87, v87, v36\n" \
    "v_cvt_pk_bf16_f32 v100, v80, v81\n" \
    "v_cvt_pk_bf16_f32 v101, v82, v83\n" \
    "v_cvt_pk_bf16_f32 v102, v84, v85\n" \
    "v_cvt_pk_bf16_f32 v103, v86, v87\n" \
    "buffer_store_dwordx4 v[100:103], v134, %[rw], s46 offen sc1\n" \
    "s_waitcnt lgkmcnt(0)\n" \
    "s_add_i32 s90, vcc_lo, %[fw]\n" \
    "35:\n" \
    "s_memtime vcc\n" \
    "s_waitcnt lgkmcnt(0)\n" \
    "s_sub_i32 s75, s90, vcc_lo\n" \
    "s_cmp_gt_i32 s75, 0\n" \
    "s_cbranch_scc1 35b\n" \
    "36:\n" \
    "buffer_load_dwordx4 v[0:3], v127, %[rw], %[ua] offen offset:0 sc1\n" \
    "buffer_load_dwordx4 v[4:7], v127, %[rw], %[ua] offen offset:1024 sc1\n" \
    "buffer_load_dwordx4 v[8:11], v127, %[rw], %[ua] offen offset:2048 sc1\n" \
    "buffer_load_dwordx4 v[12:15], v127, %[rw], %[ua] offen offset:3072 sc1\n" \
    "s_waitcnt vmcnt(3)\n" \
    "v_mfma_f32_16x16x32_bf16 v[32:35], v[176:179], v[0:3], 0\n" \
    "v_max3_u32 v40, v0, v1, v0\n" \
    "v_max3_u32 v40, v2, v3, v40\n" \
    "s_waitcnt vmcnt(2)\n" \
    "v_mfma_f32_16x16x32_bf16 v[32:35], v[176:179], v[4:7], v[32:35]\n" \
    "v_max3_u32 v40, v4, v5, v40\n" \
    "v_max3_u32 v40, v6, v7, v40\n" \
    "s_waitcnt vmcnt(1)\n" \
    "v_mfma_f32_16x16x32_bf16 v[32:35], v[176:179], v[8:11], v[32:35]\n" \
    "v_max3_u32 v40, v8, v9, v40\n" \
    "v_max3_u32 v40, v10, v11, v40\n" \
    "s_waitcnt vmcnt(0)\n" \
    "v_mfma_f32_16x16x32_bf16 v[32:35], v[176:179], v[12:15], v[32:35]\n" \
    "v_max3_u32 v40, v12, v13, v40\n" \
    "v_max3_u32 v40, v14, v15, v40\n" \
    "v_cmp_eq_u32 vcc, -1, v40\n" \
    "s_nop 3\n" \
    "s_cbranch_vccnz 38f\n" \
    "39:\n" \
    "v_add_f32_dpp v32, v32, v32 row_ror:8 row_mask:0xf bank_mask:0xf\n" \
    "v_add_f32_dpp v33, v33, v33 row_ror:8 row_mask:0xf bank_mask:0xf\n" \
    "s_mov_b32 exec_lo, 0x00ff00ff\n" \
    "s_mov_b32 exec_hi, 0x00ff00ff\n" \
    "v_fma_f32 v32, v32, v114, v172\n" \
    "v_fma_f32 v33, v33, v114, v173\n" \
    "v_cvt_pk_bf16_f32 v32, v32, v33\n" \
    "buffer_store_dword v32, v168, %[ru], s94 offen sc1\n" \
    "s_mov_b64 exec, -1\n" \
    "s_mov_b32 s36, 63\n" \
    "s_branch 9f\n" \
    "38:\n" \
    "s_add_i32 s39, s39, -1\n" \
    "s_cmp_lt_i32 s39, 0\n" \
    "s_cbranch_scc1 39b\n" \
    "s_add_i32 s30, s30, 0x10000\n" \
    "s_sleep 2\n" \
    "s_branch 36b\n" \
    "21:\n" \
    "s_add_i32 s39, s39, -1\n" \
    "s_cmp_lt_i32 s39, 0\n" \
    "s_cbranch_scc1 22b\n" \
    "buffer_load_dword v100, off, %[rw], s62 sc1\n" \
    "s_waitcnt vmcnt(0)\n" \
    "v_readfirstlane_b32 s38, v100\n" \
    "s_cmp_lt_i32 s38, s91\n" \
    "s_cbranch_scc1 21b\n" \
    "s_branch 22b\n" \
    "23:\n" \
    "s_add_i32 s39, s39, -1\n" \
    "s_cmp_lt_i32 s39, 0\n" \
    "s_cbranch_scc1 24b\n" \
    "s_sleep 4\n" \
    "buffer_load_dwordx2 v[98:99], v113, %[rs], s78 offen sc1\n" \
    "s_waitcnt vmcnt(0)\n" \
    "v_cmp_ne_u16 vcc, s91, v99\n" \
    "s_nop 1\n" \
    "s_cbranch_vccnz 23b\n" \
    "s_branch 24b\n" \
    "26:\n" \
    "s_add_i32 s39, s39, -1\n" \
    "s_cmp_lt_i32 s39, 0\n" \
    "s_cbranch_scc1 27b\n" \
    "s_sleep 4\n" \
    "s_add_i32 s44, %[qb], 0\n" \
    "s_add_i32 s45, s44, 4096\n" \
    "buffer_load_dwordx4 v[136:139], v112, %[rw], s44 offen offset:0 sc1\n" \
    "buffer_load_dwordx4 v[140:143], v112, %[rw], s44 offen offset:2048 sc1\n" \
    "buffer_load_dwordx4 v[144:147], v112, %[rw], s45 offen offset:0 sc1\n" \
    "buffer_load_dwordx4 v[148:151], v112, %[rw], s45 offen offset:2048 sc1\n" \
    "s_waitcnt vmcnt(0)\n" \
    "s_branch 25b\n" \
    "28:\n" \
    "s_add_i32 s39, s39, -1\n" \
    "s_cmp_lt_i32 s39, 0\n" \
    "s_cbranch_scc1 29b\n" \
    "s_sleep 4\n" \
    "s_add_i32 s44, %[qb], 262144\n" \
    "s_add_i32 s45, s44, 4096\n" \
    "buffer_load_dwordx4 v[72:75], v112, %[rw], s44 offen offset:0 sc1\n" \
    "buffer_load_dwordx4 v[76:79], v112, %[rw], s44 offen offset:2048 sc1\n" \
    "buffer_load_dwordx4 v[104:107], v112, %[rw], s45 offen offset:0 sc1\n" \
    "buffer_load_dwordx4 v[108:111], v112, %[rw], s45 offen offset:2048 sc1\n" \
    "s_waitcnt vmcnt(0)\n" \
    "v_max3_u32 v128, v72, v73, v72\n" \
    "v_max3_u32 v128, v74, v75, v128\n" \
    "v_max3_u32 v128, v76, v77, v128\n" \
    "v_max3_u32 v128, v78, v79, v128\n" \
    "v_max3_u32 v128, v104, v105, v128\n" \
    "v_max3_u32 v128, v106, v107, v128\n" \
    "v_max3_u32 v128, v108, v109, v128\n" \
    "v_max3_u32 v128, v110, v111, v128\n" \
    "v_cmp_eq_u32 vcc, -1, v128\n" \
    "s_nop 1\n" \
    "s_cbranch_vccnz 28b\n" \
    "s_branch 29b\n" \
    "30:\n" \
    "s_memtime vcc\n" \
    "s_waitcnt lgkmcnt(0)\n" \
    "s_sub_i32 s75, s90, vcc_lo\n" \
    "s_cmp_gt_i32 s75, 100\n" \
    "s_cbranch_scc1 30b\n" \
    "s_branch 31b\n" \
    "32:\n" \
    "v_max3_u32 v82, v0, v1, v0\n" \
    "v_max3_u32 v82, v2, v3, v82\n" \
    "v_max3_u32 v82, v4, v5, v82\n" \
    "v_max3_u32 v82, v6, v7, v82\n" \
    "v_max3_u32 v82, v8, v9, v82\n" \
    "v_max3_u32 v82, v10, v11, v82\n" \
    "v_max3_u32 v82, v12, v13, v82\n" \
    "v_max3_u32 v82, v14, v15, v82\n" \
    "v_max3_u32 v82, v16, v17, v82\n" \
    "v_max3_u32 v82, v18, v19, v82\n" \
    "v_max3_u32 v82, v20, v21, v82\n" \
    "v_max3_u32 v82, v22, v23, v82\n" \
    "v_max3_u32 v82, v24, v25, v82\n" \
    "v_max3_u32 v82, v26, v27, v82\n" \
    "v_max3_u32 v82, v28, v29, v82\n" \
    "v_max3_u32 v82, v30, v31, v82\n" \
    "v_max3_u32 v82, v32, v33, v82\n" \
    "v_max3_u32 v82, v34, v35, v82\n" \
    "v_cmp_eq_u32 vcc, -1, v82\n" \
    "s_nop 4\n" \
    "s_cbranch_vccz 34b\n" \
    "s_add_i32 s39, s39, -1\n" \
    "s_cmp_lt_i32 s39, 0\n" \
    "s_cbranch_scc1 34b\n" \
    "s_add_i32 s30, s30, 1\n" \
    "s_waitcnt lgkmcnt(0)\n" \
    "s_barrier\n" \
    "v_and_b32 v96, 0x200, v127\n" \
    "s_and_b32 s80, s26, 0x800\n" \
    "v_mad_u32_u24 v96, v96, 3, v127\n" \
    "v_xor_b32 v96, s80, v96\n" \
    "s_mov_b32 m0, s74\n" \
    "s_add_i32 s76, s74, 4096\n" \
    "buffer_load_dwordx4 v125, %[rs], s69 offen sc1 lds\n" \
    "s_mov_b32 m0, s76\n" \
    "v_mov_b64 v[80:81], 0\n" \
    "v_mov_b64 v[82:83], 0\n" \
    "buffer_load_dwordx4 v125, %[rs], s70 offen sc1 lds\n" \
    "s_movk_i32 m0, 0x2000\n" \
    "s_mov_b32 exec_lo, s27\n" \
    "s_mov_b32 exec_hi, s27\n" \
    "buffer_load_dwordx4 v96, %[rs], s71 offen sc1 lds\n" \
    "s_mov_b64 exec, -1\n" \
    "v_mov_b64 v[84:85], 0\n" \
    "v_mov_b64 v[86:87], 0\n" \
    "s_waitcnt vmcnt(0)\n" \
    "s_barrier\n" \
    "ds_read_b128 v[0:3], v37 offset:0\n" \
    "ds_read_b128 v[4:7], v38 offset:0\n" \
    "ds_read_b128 v[8:11], v39 offset:0\n" \
    "ds_read_b128 v[12:15], v132 offset:0\n" \
    "ds_read_b128 v[16:19], v37 offset:4096\n" \
    "ds_read_b128 v[20:23], v38 offset:4096\n" \
    "ds_read_b128 v[24:27], v39 offset:4096\n" \
    "ds_read_b128 v[28:31], v132 offset:4096\n" \
    "ds_read_b128 v[32:35], v126 offset:0\n" \
    "s_waitcnt lgkmcnt(8)\n" \
    "v_smfmac_f32_16x16x64_bf16 v[80:83], v[0:3], a[128:135], v124\n" \
    "s_waitcnt lgkmcnt(7)\n" \
    "v_smfmac_f32_16x16x64_bf16 v[80:83], v[4:7], a[144:151], v124\n" \
    "s_waitcnt lgkmcnt(6)\n" \
    "v_smfmac_f32_16x16x64_bf16 v[80:83], v[8:11], a[160:167], v124\n" \
    "s_waitcnt lgkmcnt(5)\n" \
    "v_smfmac_f32_16x16x64_bf16 v[80:83], v[12:15], a[176:183], v124\n" \
    "s_waitcnt lgkmcnt(4)\n" \
    "v_smfmac_f32_16x16x64_bf16 v[80:83], v[16:19], a[192:199], v124\n" \
    "s_waitcnt lgkmcnt(3)\n" \
    "v_smfmac_f32_16x16x64_bf16 v[80:83], v[20:23], a[208:215], v124\n" \
    "s_waitcnt lgkmcnt(2)\n" \
    "v_smfmac_f32_16x16x64_bf16 v[80:83], v[24:27], a[224:231], v124\n" \
    "s_waitcnt lgkmcnt(1)\n" \
    "v_smfmac_f32_16x16x64_bf16 v[80:83], v[28:31], a[240:247], v124\n" \
    "s_waitcnt lgkmcnt(0)\n" \
    "v_smfmac_f32_16x16x64_bf16 v[80:83], v[32:35], v[152:159], v124\n" \
    "v_smfmac_f32_16x16x64_bf16 v[84:87], v[0:3], a[136:143], v124\n" \
    "v_smfmac_f32_16x16x64_bf16 v[84:87], v[4:7], a[152:159], v124\n" \
    "v_smfmac_f32_16x16x64_bf16 v[84:87], v[8:11], a[168:175], v124\n" \
    "v_smfmac_f32_16x16x64_bf16 v[84:87], v[12:15], a[184:191], v124\n" \
    "v_smfmac_f32_16x16x64_bf16 v[84:87], v[16:19], a[200:207], v124\n" \
    "v_add_f32 v80, v80, v82\n" \
    "v_add_f32 v81, v81, v83\n" \
    "s_branch 32b\n" \
    "7:\n" \
    "s_mov_b32 s43, 2\n" \
    "s_nop 7\n" \
    "s_waitcnt vmcnt(0)\n" \
    "9:\n" \
    "s_mov_b32 m0, s68\n" \
    "s_add_i32 s37, s37, s29\n" \
    "s_waitcnt lgkmcnt(0)\n" \
    "v_mov_b32 v0, s36\n" \
    "v_mov_b32 v1, s37\n" \
    "v_mov_b32 v2, s38\n" \
    "v_mov_b32 v3, s39\n" \
    "v_mov_b32 v4, s40\n" \
    "v_mov_b32 v5, s41\n" \
    "v_mov_b32 v6, s42\n" \
    "v_mov_b32 v7, s43\n" \
    "v_mov_b32 v8, s28\n" \
    "v_mov_b32 v9, s29\n" \
    "v_mov_b32 v10, s30\n" \
    "v_mov_b32 v11, s31\n" \
    "v_mov_b32 v12, s32\n" \
    "v_mov_b32 v13, s33\n" \
    "v_mov_b32 v14, s34\n" \
    "v_mov_b32 v15, s35\n" \
    "v_mov_b32 v16, s24\n" \
    "v_mov_b32 v17, s25\n" \
    "v_mov_b32 v18, s26\n" \
    "v_mov_b32 v19, s27\n" \
    "v_mov_b32 v20, s84\n" \
    "v_mov_b32 v21, s86\n" \
    "v_mov_b32 v22, s88\n" \
    "v_mov_b32 v23, s84\n" \
    "v_mov_b32 v24, s60\n" \
    "v_mov_b32 v25, s61\n" \
    "v_mov_b32 v26, s62\n" \
    "v_mov_b32 v27, s63\n" \
    "v_mov_b32 v28, s64\n" \
    "v_mov_b32 v29, s65\n" \
    "v_mov_b32 v30, s66\n" \
    "v_mov_b32 v31, s67\n" \
    ".Lstep_end:\n" \
    ""
static_assert(2 * PART_STEP == 32768 && 2 * OFF_CAN == 1703936 && F_ERR == 2 * OFF_CAN + 65536 && MEMB_STRIDE == 32768 && OFF_VF - OFF_KF == 2048 * 4096 * 2 && OFF_BO == 65011712 && OFF_GT == 67518464 && BO_ROW == 4352, "constants of the step block");
#define STEP_CLOBBER "v32", "v33", "v34", "v35", "v36", "v37", "v38", "v39", "v40", "v41", "v42", "v43", "v44", "v45", "v46", "v47", "v48", "v49", "v50", "v51", "v52", "v53", "v54", "v55", "v56", "v57", "v58", "v59", "v60", "v61", "v62", "v63", "v64", "v65", "v66", "v67", "v68", "v69", "v70", "v71", "v72", "v73", "v74", "v75", "v76", "v77", "v78", "v79", "v80", "v81", "v82", "v83", "v84", "v85", "v86", "v87", "v88", "v89", "v90", "v91", "v92", "v93", "v94", "v95", "v98", "v99", "v100", "v101", "v102", "v103", "v104", "v105", "v106", "v107", "v108", "v109", "v110", "v111", "v112", "v113", "v114", "s24", "s25", "s26", "s27", "s28", "s29", "s30", "s31", "s32", "s33", "s34", "s35", "s36", "s37", "s38", "s39", "s40", "s41", "s42", "s43", "s44", "s45", "s46", "s47", "s48", "s49", "s50", "s51", "s52", "s53", "s54", "s55", "s56", "s57", "s58", "s59", "s60", "s61", "s62", "s63", "s64", "s65", "s66", "s67", "s68", "s69", "s70", "s71", "s72", "s73", "s74", "s75", "s76", "s77", "s78", "s79", "s80", "s81", "s82", "s83", "s84", "s85", "s86", "s87", "s88", "s89", "s90", "s91", "s92", "s93", "s94", "s95", "s96", "s97", "vcc", "scc", "memory"
#define OUT16_ASM \
    "s_mov_b32 exec_lo, %[tm]\n" \
    "s_mov_b32 exec_hi, 0\n" \
    "buffer_load_dword v79, %[tv], %[rw], %[so] offen sc1\n" \
    "s_mov_b64 exec, -1\n" \
    "s_add_i32 s44, %[so], 4096\n" \
    "s_add_i32 s45, %[so], 8192\n" \
    "s_add_i32 s46, %[so], 12288\n" \
    "buffer_load_dwordx4 v[16:19], %[vo], %[rw], %[so] offen offset:0 sc1\n" \
    "ds_read_b128 v[80:83], %[wl] offset:0\n" \
    "ds_read_b128 v[84:87], %[wl] offset:1024\n" \
    "s_nop 7\n" \
    "v_mov_b32 v104, 0\n" \
    "buffer_load_dwordx4 v[20:23], %[vo], %[rw], %[so] offen offset:256 sc1\n" \
    "s_nop 7\n" \
    "s_nop 3\n" \
    "buffer_load_dwordx4 v[24:27], %[vo], %[rw], %[so] offen offset:1024 sc1\n" \
    "s_nop 7\n" \
    "s_nop 3\n" \
    "buffer_load_dwordx4 v[28:31], %[vo], %[rw], %[so] offen offset:1280 sc1\n" \
    "s_nop 7\n" \
    "s_nop 3\n" \
    "buffer_load_dwordx4 v[32:35], %[vo], %[rw], %[so] offen offset:2048 sc1\n" \
    "s_nop 7\n" \
    "s_nop 3\n" \
    "buffer_load_dwordx4 v[36:39], %[vo], %[rw], %[so] offen offset:2304 sc1\n" \
    "s_nop 7\n" \
    "s_nop 3\n" \
    "buffer_load_dwordx4 v[40:43], %[vo], %[rw], %[so] offen offset:3072 sc1\n" \
    "s_nop 7\n" \
    "s_nop 3\n" \
    "buffer_load_dwordx4 v[44:47], %[vo], %[rw], %[so] offen offset:3328 sc1\n" \
    "s_nop 7\n" \
    "s_nop 3\n" \
    "buffer_load_dwordx4 v[48:51], %[vo], %[rw], s44 offen offset:0 sc1\n" \
    "s_nop 7\n" \
    "s_nop 3\n" \
    "buffer_load_dwordx4 v[52:55], %[vo], %[rw], s44 offen offset:256 sc1\n" \
    "s_nop 7\n" \
    "s_nop 3\n" \
    "buffer_load_dwordx4 v[56:59], %[vo], %[rw], s44 offen offset:1024 sc1\n" \
    "s_nop 7\n" \
    "s_nop 3\n" \
    "buffer_load_dwordx4 v[60:63], %[vo], %[rw], s44 offen offset:1280 sc1\n" \
    "ds_read_b128 v[88:91], %[wl] offset:2048\n" \
    "s_waitcnt vmcnt(11)\n" \
    "v_mfma_f32_16x16x32_bf16 v[0:3], a[0:3], v[16:19], 0\n" \
    "v_max3_u32 v104, v104, v16, v17\n" \
    "v_mfma_f32_16x16x32_bf16 v[4:7], a[128:131], v[16:19], 0\n" \
    "v_max3_u32 v104, v104, v18, v19\n" \
    "v_mfma_f32_16x16x32_bf16 v[8:11], v[128:131], v[16:19], 0\n" \
    "s_waitcnt lgkmcnt(2)\n" \
    "v_mfma_f32_16x16x32_bf16 v[12:15], v[80:83], v[16:19], 0\n" \
    "buffer_load_dwordx4 v[64:67], %[vo], %[rw], s44 offen offset:2048 sc1\n" \
    "ds_read_b128 v[80:83], %[wl] offset:3072\n" \
    "s_waitcnt vmcnt(11)\n" \
    "v_mfma_f32_16x16x32_bf16 v[0:3], a[4:7], v[20:23], v[0:3]\n" \
    "v_max3_u32 v104, v104, v20, v21\n" \
    "v_mfma_f32_16x16x32_bf16 v[4:7], a[132:135], v[20:23], v[4:7]\n" \
    "v_max3_u32 v104, v104, v22, v23\n" \
    "v_mfma_f32_16x16x32_bf16 v[8:11], v[132:135], v[20:23], v[8:11]\n" \
    "s_waitcnt lgkmcnt(2)\n" \
    "v_mfma_f32_16x16x32_bf16 v[12:15], v[84:87], v[20:23], v[12:15]\n" \
    "buffer_load_dwordx4 v[68:71], %[vo], %[rw], s44 offen offset:2304 sc1\n" \
    "ds_read_b128 v[84:87], %[wl] offset:4096\n" \
    "s_waitcnt vmcnt(11)\n" \
    "v_mfma_f32_16x16x32_bf16 v[0:3], a[8:11], v[24:27], v[0:3]\n" \
    "v_max3_u32 v104, v104, v24, v25\n" \
    "v_mfma_f32_16x16x32_bf16 v[4:7], a[136:139], v[24:27], v[4:7]\n" \
    "v_max3_u32 v104, v104, v26, v27\n" \
    "v_mfma_f32_16x16x32_bf16 v[8:11], v[136:139], v[24:27], v[8:11]\n" \
    "s_waitcnt lgkmcnt(2)\n" \
    "v_mfma_f32_16x16x32_bf16 v[12:15], v[88:91], v[24:27], v[12:15]\n" \
    "buffer_load_dwordx4 v[72:75], %[vo], %[rw], s44 offen offset:3072 sc1\n" \
    "ds_read_b128 v[88:91], %[wl] offset:5120\n" \
    "s_waitcnt vmcnt(11)\n" \
    "v_mfma_f32_16x16x32_bf16 v[0:3], a[12:15], v[28:31], v[0:3]\n" \
    "v_max3_u32 v104, v104, v28, v29\n" \
    "v_mfma_f32_16x16x32_bf16 v[4:7], a[140:143], v[28:31], v[4:7]\n" \
    "v_max3_u32 v104, v104, v30, v31\n" \
    "v_mfma_f32_16x16x32_bf16 v[8:11], v[140:143], v[28:31], v[8:11]\n" \
    "s_waitcnt lgkmcnt(2)\n" \
    "v_mfma_f32_16x16x32_bf16 v[12:15], v[80:83], v[28:31], v[12:15]\n" \
    "buffer_load_dwordx4 v[76:79], %[vo], %[rw], s44 offen offset:3328 sc1\n" \
    "ds_read_b128 v[80:83], %[wl] offset:6144\n" \
    "s_waitcnt vmcnt(11)\n" \
    "v_mfma_f32_16x16x32_bf16 v[0:3], a[16:19], v[32:35], v[0:3]\n" \
    "v_max3_u32 v104, v104, v32, v33\n" \
    "v_mfma_f32_16x16x32_bf16 v[4:7], a[144:147], v[32:35], v[4:7]\n" \
    "v_max3_u32 v104, v104, v34, v35\n" \
    "v_mfma_f32_16x16x32_bf16 v[8:11], v[144:147], v[32:35], v[8:11]\n" \
    "s_waitcnt lgkmcnt(2)\n" \
    "v_mfma_f32_16x16x32_bf16 v[12:15], v[84:87], v[32:35], v[12:15]\n" \
    "buffer_load_dwordx4 v[16:19], %[vo], %[rw], s45 offen offset:0 sc1\n" \
    "ds_read_b128 v[84:87], %[wl] offset:7168\n" \
    "s_waitcnt vmcnt(11)\n" \
    "v_mfma_f32_16x16x32_bf16 v[0:3], a[20:23], v[36:39], v[0:3]\n" \
    "v_max3_u32 v104, v104, v36, v37\n" \
    "v_mfma_f32_16x16x32_bf16 v[4:7], a[148:151], v[36:39], v[4:7]\n" \
    "v_max3_u32 v104, v104, v38, v39\n" \
    "v_mfma_f32_16x16x32_bf16 v[8:11], v[148:151], v[36:39], v[8:11]\n" \
    "s_waitcnt lgkmcnt(2)\n" \
    "v_mfma_f32_16x16x32_bf16 v[12:15], v[88:91], v[36:39], v[12:15]\n" \
    "buffer_load_dwordx4 v[20:23], %[vo], %[rw], s45 offen offset:256 sc1\n" \
    "ds_read_b128 v[88:91], %[wl] offset:8192\n" \
    "s_waitcnt vmcnt(11)\n" \
    "v_mfma_f32_16x16x32_bf16 v[0:3], a[24:27], v[40:43], v[0:3]\n" \
    "v_max3_u32 v104, v104, v40, v41\n" \
    "v_mfma_f32_16x16x32_bf16 v[4:7], a[152:155], v[40:43], v[4:7]\n" \
    "v_max3_u32 v104, v104, v42, v43\n" \
    "v_mfma_f32_16x16x32_bf16 v[8:11], v[152:155], v[40:43], v[8:11]\n" \
    "s_waitcnt lgkmcnt(2)\n" \
    "v_mfma_f32_16x16x32_bf16 v[12:15], v[80:83], v[40:43], v[12:15]\n" \
    "buffer_load_dwordx4 v[24:27], %[vo], %[rw], s45 offen offset:1024 sc1\n" \
    "ds_read_b128 v[80:83], %[wl] offset:9216\n" \
    "s_waitcnt vmcnt(11)\n" \
    "v_mfma_f32_16x16x32_bf16 v[0:3], a[28:31], v[44:47], v[0:3]\n" \
    "v_max3_u32 v104, v104, v44, v45\n" \
    "v_mfma_f32_16x16x32_bf16 v[4:7], a[156:159], v[44:47], v[4:7]\n" \
    "v_max3_u32 v104, v104, v46, v47\n" \
    "v_mfma_f32_16x16x32_bf16 v[8:11], v[156:159], v[44:47], v[8:11]\n" \
    "s_waitcnt lgkmcnt(2)\n" \
    "v_mfma_f32_16x16x32_bf16 v[12:15], v[84:87], v[44:47], v[12:15]\n" \
    "buffer_load_dwordx4 v[28:31], %[vo], %[rw], s45 offen offset:1280 sc1\n" \
    "ds_read_b128 v[84:87], %[wl] offset:10240\n" \
    "s_waitcnt vmcnt(11)\n" \
    "v_mfma_f32_16x16x32_bf16 v[0:3], a[32:35], v[48:51], v[0:3]\n" \
    "v_max3_u32 v104, v104, v48, v49\n" \
    "v_mfma_f32_16x16x32_bf16 v[4:7], a[160:163], v[48:51], v[4:7]\n" \
    "v_max3_u32 v104, v104, v50, v51\n" \
    "v_mfma_f32_16x16x32_bf16 v[8:11], v[160:163], v[48:51], v[8:11]\n" \
    "s_waitcnt lgkmcnt(2)\n" \
    "v_mfma_f32_16x16x32_bf16 v[12:15], v[88:91], v[48:51], v[12:15]\n" \
    "buffer_load_dwordx4 v[32:35], %[vo], %[rw], s45 offen offset:2048 sc1\n" \
    "ds_read_b128 v[88:91], %[wl] offset:11264\n" \
    "s_waitcnt vmcnt(11)\n" \
    "v_mfma_f32_16x16x32_bf16 v[0:3], a[36:39], v[52:55], v[0:3]\n" \
    "v_max3_u32 v104, v104, v52, v53\n" \
    "v_mfma_f32_16x16x32_bf16 v[4:7], a[164:167], v[52:55], v[4:7]\n" \
    "v_max3_u32 v104, v104, v54, v55\n" \
    "v_mfma_f32_16x16x32_bf16 v[8:11], v[164:167], v[52:55], v[8:11]\n" \
    "s_waitcnt lgkmcnt(2)\n" \
    "v_mfma_f32_16x16x32_bf16 v[12:15], v[80:83], v[52:55], v[12:15]\n" \
    "buffer_load_dwordx4 v[36:39], %[vo], %[rw], s45 offen offset:2304 sc1\n" \
    "ds_read_b128 v[80:83], %[wl] offset:12288\n" \
    "s_waitcnt vmcnt(11)\n" \
    "v_mfma_f32_16x16x32_bf16 v[0:3], a[40:43], v[56:59], v[0:3]\n" \
    "v_max3_u32 v104, v104, v56, v57\n" \
    "v_mfma_f32_16x16x32_bf16 v[4:7], a[168:171], v[56:59], v[4:7]\n" \
    "v_max3_u32 v104, v104, v58, v59\n" \
    "v_mfma_f32_16x16x32_bf16 v[8:11], v[168:171], v[56:59], v[8:11]\n" \
    "s_waitcnt lgkmcnt(2)\n" \
    "v_mfma_f32_16x16x32_bf16 v[12:15], v[84:87], v[56:59], v[12:15]\n" \
    "buffer_load_dwordx4 v[40:43], %[vo], %[rw], s45 offen offset:3072 sc1\n" \
    "ds_read_b128 v[84:87], %[wl] offset:13312\n" \
    "s_waitcnt vmcnt(11)\n" \
    "v_mfma_f32_16x16x32_bf16 v[0:3], a[44:47], v[60:63], v[0:3]\n" \
    "v_max3_u32 v104, v104, v60, v61\n" \
    "v_mfma_f32_16x16x32_bf16 v[4:7], a[172:175], v[60:63], v[4:7]\n" \
    "v_max3_u32 v104, v104, v62, v63\n" \
    "v_mfma_f32_16x16x32_bf16 v[8:11], v[172:175], v[60:63], v[8:11]\n" \
    "s_waitcnt lgkmcnt(2)\n" \
    "v_mfma_f32_16x16x32_bf16 v[12:15], v[88:91], v[60:63], v[12:15]\n" \
    "buffer_load_dwordx4 v[44:47], %[vo], %[rw], s45 offen offset:3328 sc1\n" \
    "ds_read_b128 v[88:91], %[wl] offset:14336\n" \
    "s_waitcnt vmcnt(11)\n" \
    "v_mfma_f32_16x16x32_bf16 v[0:3], a[48:51], v[64:67], v[0:3]\n" \
    "v_max3_u32 v104, v104, v64, v65\n" \
    "v_mfma_f32_16x16x32_bf16 v[4:7], a[176:179], v[64:67], v[4:7]\n" \
    "v_max3_u32 v104, v104, v66, v67\n" \
    "v_mfma_f32_16x16x32_bf16 v[8:11], v[176:179], v[64:67], v[8:11]\n" \
    "s_waitcnt lgkmcnt(2)\n" \
    "v_mfma_f32_16x16x32_bf16 v[12:15], v[80:83], v[64:67], v[12:15]\n" \
    "buffer_load_dwordx4 v[48:51], %[vo], %[rw], s46 offen offset:0 sc1\n" \
    "ds_read_b128 v[80:83], %[wl] offset:15360\n" \
    "s_waitcnt vmcnt(11)\n" \
    "v_mfma_f32_16x16x32_bf16 v[0:3], a[52:55], v[68:71], v[0:3]\n" \
    "v_max3_u32 v104, v104, v68, v69\n" \
    "v_mfma_f32_16x16x32_bf16 v[4:7], a[180:183], v[68:71], v[4:7]\n" \
    "v_max3_u32 v104, v104, v70, v71\n" \
    "v_mfma_f32_16x16x32_bf16 v[8:11], v[180:183], v[68:71], v[8:11]\n" \
    "s_waitcnt lgkmcnt(2)\n" \
    "v_mfma_f32_16x16x32_bf16 v[12:15], v[84:87], v[68:71], v[12:15]\n" \
    "buffer_load_dwordx4 v[52:55], %[vo], %[rw], s46 offen offset:256 sc1\n" \
    "ds_read_b128 v[84:87], %[wl] offset:16384\n" \
    "s_waitcnt vmcnt(11)\n" \
    "v_mfma_f32_16x16x32_bf16 v[0:3], a[56:59], v[72:75], v[0:3]\n" \
    "v_max3_u32 v104, v104, v72, v73\n" \
    "v_mfma_f32_16x16x32_bf16 v[4:7], a[184:187], v[72:75], v[4:7]\n" \
    "v_max3_u32 v104, v104, v74, v75\n" \
    "v_mfma_f32_16x16x32_bf16 v[8:11], v[184:187], v[72:75], v[8:11]\n" \
    "s_waitcnt lgkmcnt(2)\n" \
    "v_mfma_f32_16x16x32_bf16 v[12:15], v[88:91], v[72:75], v[12:15]\n" \
    "buffer_load_dwordx4 v[56:59], %[vo], %[rw], s46 offen offset:1024 sc1\n" \
    "ds_read_b128 v[88:91], %[wl] offset:17408\n" \
    "s_waitcnt vmcnt(11)\n" \
    "v_mfma_f32_16x16x32_bf16 v[0:3], a[60:63], v[76:79], v[0:3]\n" \
    "v_max3_u32 v104, v104, v76, v77\n" \
    "v_mfma_f32_16x16x32_bf16 v[4:7], a[188:191], v[76:79], v[4:7]\n" \
    "v_max3_u32 v104, v104, v78, v79\n" \
    "v_mfma_f32_16x16x32_bf16 v[8:11], v[188:191], v[76:79], v[8:11]\n" \
    "s_waitcnt lgkmcnt(2)\n" \
    "v_mfma_f32_16x16x32_bf16 v[12:15], v[80:83], v[76:79], v[12:15]\n" \
    "buffer_load_dwordx4 v[60:63], %[vo], %[rw], s46 offen offset:1280 sc1\n" \
    "ds_read_b128 v[80:83], %[wl] offset:18432\n" \
    "s_waitcnt vmcnt(11)\n" \
    "v_mfma_f32_16x16x32_bf16 v[0:3], a[64:67], v[16:19], v[0:3]\n" \
    "v_max3_u32 v104, v104, v16, v17\n" \
    "v_mfma_f32_16x16x32_bf16 v[4:7], a[192:195], v[16:19], v[4:7]\n" \
    "v_max3_u32 v104, v104, v18, v19\n" \
    "v_mfma_f32_16x16x32_bf16 v[8:11], v[192:195], v[16:19], v[8:11]\n" \
    "s_waitcnt lgkmcnt(2)\n" \
    "v_mfma_f32_16x16x32_bf16 v[12:15], v[84:87], v[16:19], v[12:15]\n" \
    "buffer_load_dwordx4 v[64:67], %[vo], %[rw], s46 offen offset:2048 sc1\n" \
    "ds_read_b128 v[84:87], %[wl] offset:19456\n" \
    "s_waitcnt vmcnt(11)\n" \
    "v_mfma_f32_16x16x32_bf16 v[0:3], a[68:71], v[20:23], v[0:3]\n" \
    "v_max3_u32 v104, v104, v20, v21\n" \
    "v_mfma_f32_16x16x32_bf16 v[4:7], a[196:199], v[20:23], v[4:7]\n" \
    "v_max3_u32 v104, v104, v22, v23\n" \
    "v_mfma_f32_16x16x32_bf16 v[8:11], v[196:199], v[20:23], v[8:11]\n" \
    "s_waitcnt lgkmcnt(2)\n" \
    "v_mfma_f32_16x16x32_bf16 v[12:15], v[88:91], v[20:23], v[12:15]\n" \
    "buffer_load_dwordx4 v[68:71], %[vo], %[rw], s46 offen offset:2304 sc1\n" \
    "ds_read_b128 v[88:91], %[wl] offset:20480\n" \
    "s_waitcnt vmcnt(11)\n" \
    "v_mfma_f32_16x16x32_bf16 v[0:3], a[72:75], v[24:27], v[0:3]\n" \
    "v_max3_u32 v104, v104, v24, v25\n" \
    "v_mfma_f32_16x16x32_bf16 v[4:7], a[200:203], v[24:27], v[4:7]\n" \
    "v_max3_u32 v104, v104, v26, v27\n" \
    "v_mfma_f32_16x16x32_bf16 v[8:11], v[200:203], v[24:27], v[8:11]\n" \
    "s_waitcnt lgkmcnt(2)\n" \
    "v_mfma_f32_16x16x32_bf16 v[12:15], v[80:83], v[24:27], v[12:15]\n" \
    "buffer_load_dwordx4 v[72:75], %[vo], %[rw], s46 offen offset:3072 sc1\n" \
    "ds_read_b128 v[80:83], %[wl] offset:21504\n" \
    "s_waitcnt vmcnt(11)\n" \
    "v_mfma_f32_16x16x32_bf16 v[0:3], a[76:79], v[28:31], v[0:3]\n" \
    "v_max3_u32 v104, v104, v28, v29\n" \
    "v_mfma_f32_16x16x32_bf16 v[4:7], a[204:207], v[28:31], v[4:7]\n" \
    "v_max3_u32 v104, v104, v30, v31\n" \
    "v_mfma_f32_16x16x32_bf16 v[8:11], v[204:207], v[28:31], v[8:11]\n" \
    "s_waitcnt lgkmcnt(2)\n" \
    "v_mfma_f32_16x16x32_bf16 v[12:15], v[84:87], v[28:31], v[12:15]\n" \
    "buffer_load_dwordx4 v[76:79], %[vo], %[rw], s46 offen offset:3328 sc1\n" \
    "ds_read_b128 v[84:87], %[wl] offset:22528\n" \
    "s_waitcnt vmcnt(11)\n" \
    "v_mfma_f32_16x16x32_bf16 v[0:3], a[80:83], v[32:35], v[0:3]\n" \
    "v_max3_u32 v104, v104, v32, v33\n" \
    "v_mfma_f32_16x16x32_bf16 v[4:7], a[208:211], v[32:35], v[4:7]\n" \
    "v_max3_u32 v104, v104, v34, v35\n" \
    "v_mfma_f32_16x16x32_bf16 v[8:11], v[208:211], v[32:35], v[8:11]\n" \
    "s_waitcnt lgkmcnt(2)\n" \
    "v_mfma_f32_16x16x32_bf16 v[12:15], v[88:91], v[32:35], v[12:15]\n" \
    "buffer_load_dwordx2 v[106:107], %[bv], %[rw], %[bs] offen sc1\n" \
    "ds_read_b128 v[88:91], %[wl] offset:23552\n" \
    "s_waitcnt vmcnt(11)\n" \
    "v_mfma_f32_16x16x32_bf16 v[0:3], a[84:87], v[36:39], v[0:3]\n" \
    "v_max3_u32 v104, v104, v36, v37\n" \
    "v_mfma_f32_16x16x32_bf16 v[4:7], a[212:215], v[36:39], v[4:7]\n" \
    "v_max3_u32 v104, v104, v38, v39\n" \
    "v_mfma_f32_16x16x32_bf16 v[8:11], v[212:215], v[36:39], v[8:11]\n" \
    "s_waitcnt lgkmcnt(2)\n" \
    "v_mfma_f32_16x16x32_bf16 v[12:15], v[80:83], v[36:39], v[12:15]\n" \
    "buffer_load_dword v108, %[gv], %[rw], %[gs] offen sc1\n" \
    "ds_read_b128 v[80:83], %[wl] offset:24576\n" \
    "s_waitcnt vmcnt(11)\n" \
    "v_mfma_f32_16x16x32_bf16 v[0:3], a[88:91], v[40:43], v[0:3]\n" \
    "v_max3_u32 v104, v104, v40, v41\n" \
    "v_mfma_f32_16x16x32_bf16 v[4:7], a[216:219], v[40:43], v[4:7]\n" \
    "v_max3_u32 v104, v104, v42, v43\n" \
    "v_mfma_f32_16x16x32_bf16 v[8:11], v[216:219], v[40:43], v[8:11]\n" \
    "s_waitcnt lgkmcnt(2)\n" \
    "v_mfma_f32_16x16x32_bf16 v[12:15], v[84:87], v[40:43], v[12:15]\n" \
    "buffer_load_dword v105, %[sv], %[rw], %[ss] offen sc1\n" \
    "ds_read_b128 v[84:87], %[wl] offset:25600\n" \
    "s_waitcnt vmcnt(11)\n" \
    "v_mfma_f32_16x16x32_bf16 v[0:3], a[92:95], v[44:47], v[0:3]\n" \
    "v_max3_u32 v104, v104, v44, v45\n" \
    "v_mfma_f32_16x16x32_bf16 v[4:7], a[220:223], v[44:47], v[4:7]\n" \
    "v_max3_u32 v104, v104, v46, v47\n" \
    "v_mfma_f32_16x16x32_bf16 v[8:11], v[220:223], v[44:47], v[8:11]\n" \
    "s_waitcnt lgkmcnt(2)\n" \
    "v_mfma_f32_16x16x32_bf16 v[12:15], v[88:91], v[44:47], v[12:15]\n" \
    "ds_read_b128 v[88:91], %[wl] offset:26624\n" \
    "s_waitcnt vmcnt(10)\n" \
    "v_mfma_f32_16x16x32_bf16 v[0:3], a[96:99], v[48:51], v[0:3]\n" \
    "v_max3_u32 v104, v104, v48, v49\n" \
    "v_mfma_f32_16x16x32_bf16 v[4:7], a[224:227], v[48:51], v[4:7]\n" \
    "v_max3_u32 v104, v104, v50, v51\n" \
    "v_mfma_f32_16x16x32_bf16 v[8:11], v[224:227], v[48:51], v[8:11]\n" \
    "s_waitcnt lgkmcnt(2)\n" \
    "v_mfma_f32_16x16x32_bf16 v[12:15], v[80:83], v[48:51], v[12:15]\n" \
    "ds_read_b128 v[80:83], %[wl] offset:27648\n" \
    "s_waitcnt vmcnt(9)\n" \
    "v_mfma_f32_16x16x32_bf16 v[0:3], a[100:103], v[52:55], v[0:3]\n" \
    "v_max3_u32 v104, v104, v52, v53\n" \
    "v_mfma_f32_16x16x32_bf16 v[4:7], a[228:231], v[52:55], v[4:7]\n" \
    "v_max3_u32 v104, v104, v54, v55\n" \
    "v_mfma_f32_16x16x32_bf16 v[8:11], v[228:231], v[52:55], v[8:11]\n" \
    "s_waitcnt lgkmcnt(2)\n" \
    "v_mfma_f32_16x16x32_bf16 v[12:15], v[84:87], v[52:55], v[12:15]\n" \
    "ds_read_b128 v[84:87], %[wl] offset:28672\n" \
    "s_waitcnt vmcnt(8)\n" \
    "v_mfma_f32_16x16x32_bf16 v[0:3], a[104:107], v[56:59], v[0:3]\n" \
    "v_max3_u32 v104, v104, v56, v57\n" \
    "v_mfma_f32_16x16x32_bf16 v[4:7], a[232:235], v[56:59], v[4:7]\n" \
    "v_max3_u32 v104, v104, v58, v59\n" \
    "v_mfma_f32_16x16x32_bf16 v[8:11], v[232:235], v[56:59], v[8:11]\n" \
    "s_waitcnt lgkmcnt(2)\n" \
    "v_mfma_f32_16x16x32_bf16 v[12:15], v[88:91], v[56:59], v[12:15]\n" \
    "ds_read_b128 v[88:91], %[wl] offset:29696\n" \
    "s_waitcnt vmcnt(7)\n" \
    "v_mfma_f32_16x16x32_bf16 v[0:3], a[108:111], v[60:63], v[0:3]\n" \
    "v_max3_u32 v104, v104, v60, v61\n" \
    "v_mfma_f32_16x16x32_bf16 v[4:7], a[236:239], v[60:63], v[4:7]\n" \
    "v_max3_u32 v104, v104, v62, v63\n" \
    "v_mfma_f32_16x16x32_bf16 v[8:11], v[236:239], v[60:63], v[8:11]\n" \
    "s_waitcnt lgkmcnt(2)\n" \
    "v_mfma_f32_16x16x32_bf16 v[12:15], v[80:83], v[60:63], v[12:15]\n" \
    "ds_read_b128 v[80:83], %[wl] offset:30720\n" \
    "s_waitcnt vmcnt(6)\n" \
    "v_mfma_f32_16x16x32_bf16 v[0:3], a[112:115], v[64:67], v[0:3]\n" \
    "v_max3_u32 v104, v104, v64, v65\n" \
    "v_mfma_f32_16x16x32_bf16 v[4:7], a[240:243], v[64:67], v[4:7]\n" \
    "v_max3_u32 v104, v104, v66, v67\n" \
    "v_mfma_f32_16x16x32_bf16 v[8:11], v[240:243], v[64:67], v[8:11]\n" \
    "s_waitcnt lgkmcnt(2)\n" \
    "v_mfma_f32_16x16x32_bf16 v[12:15], v[84:87], v[64:67], v[12:15]\n" \
    "ds_read_b128 v[84:87], %[wl] offset:31744\n" \
    "s_waitcnt vmcnt(5)\n" \
    "v_mfma_f32_16x16x32_bf16 v[0:3], a[116:119], v[68:71], v[0:3]\n" \
    "v_max3_u32 v104, v104, v68, v69\n" \
    "v_mfma_f32_16x16x32_bf16 v[4:7], a[244:247], v[68:71], v[4:7]\n" \
    "v_max3_u32 v104, v104, v70, v71\n" \
    "v_mfma_f32_16x16x32_bf16 v[8:11], v[244:247], v[68:71], v[8:11]\n" \
    "s_waitcnt lgkmcnt(2)\n" \
    "v_mfma_f32_16x16x32_bf16 v[12:15], v[88:91], v[68:71], v[12:15]\n" \
    "s_waitcnt vmcnt(4)\n" \
    "v_mfma_f32_16x16x32_bf16 v[0:3], a[120:123], v[72:75], v[0:3]\n" \
    "v_max3_u32 v104, v104, v72, v73\n" \
    "v_mfma_f32_16x16x32_bf16 v[4:7], a[248:251], v[72:75], v[4:7]\n" \
    "v_max3_u32 v104, v104, v74, v75\n" \
    "v_mfma_f32_16x16x32_bf16 v[8:11], v[248:251], v[72:75], v[8:11]\n" \
    "s_waitcnt lgkmcnt(1)\n" \
    "v_mfma_f32_16x16x32_bf16 v[12:15], v[80:83], v[72:75], v[12:15]\n" \
    "s_waitcnt vmcnt(3)\n" \
    "v_mfma_f32_16x16x32_bf16 v[0:3], a[124:127], v[76:79], v[0:3]\n" \
    "v_max3_u32 v104, v104, v76, v77\n" \
    "v_mfma_f32_16x16x32_bf16 v[4:7], a[252:255], v[76:79], v[4:7]\n" \
    "v_max3_u32 v104, v104, v78, v79\n" \
    "v_mfma_f32_16x16x32_bf16 v[8:11], v[252:255], v[76:79], v[8:11]\n" \
    "s_waitcnt lgkmcnt(0)\n" \
    "v_mfma_f32_16x16x32_bf16 v[12:15], v[84:87], v[76:79], v[12:15]\n" \
    "s_waitcnt vmcnt(0)\n" \
    "s_nop 7\n" \
    ""
#define OUT8_ASM \
    "s_mov_b32 exec_lo, %[tm]\n" \
    "s_mov_b32 exec_hi, 0\n" \
    "buffer_load_dword v79, %[tv], %[rw], %[so] offen sc1\n" \
    "s_mov_b64 exec, -1\n" \
    "s_add_i32 s44, %[so], 4096\n" \
    "s_add_i32 s45, %[so], 8192\n" \
    "s_add_i32 s46, %[so], 12288\n" \
    "buffer_load_dwordx4 v[16:19], %[vo], %[rw], %[so] offen offset:0 sc1\n" \
    "ds_read_b128 v[80:83], %[wl] offset:0\n" \
    "ds_read_b128 v[84:87], %[wl] offset:1024\n" \
    "ds_read_b128 v[88:91], %[wl] offset:2048\n" \
    "ds_read_b128 v[92:95], %[wl] offset:3072\n" \
    "v_mov_b64 v[0:1], 0\n" \
    "v_mov_b64 v[2:3], 0\n" \
    "v_mov_b64 v[4:5], 0\n" \
    "v_mov_b64 v[6:7], 0\n" \
    "v_mov_b64 v[8:9], 0\n" \
    "v_mov_b64 v[10:11], 0\n" \
    "v_mov_b64 v[12:13], 0\n" \
    "v_mov_b64 v[14:15], 0\n" \
    "v_mov_b32 v104, 0\n" \
    "buffer_load_dwordx4 v[20:23], %[vo], %[rw], %[so] offen offset:1024 sc1\n" \
    "s_nop 7\n" \
    "s_nop 3\n" \
    "buffer_load_dwordx4 v[24:27], %[vo], %[rw], %[so] offen offset:2048 sc1\n" \
    "s_nop 7\n" \
    "s_nop 3\n" \
    "buffer_load_dwordx4 v[28:31], %[vo], %[rw], %[so] offen offset:3072 sc1\n" \
    "s_nop 7\n" \
    "s_nop 3\n" \
    "buffer_load_dwordx4 v[32:35], %[vo], %[rw], s44 offen offset:0 sc1\n" \
    "s_nop 7\n" \
    "s_nop 3\n" \
    "buffer_load_dwordx4 v[36:39], %[vo], %[rw], s44 offen offset:1024 sc1\n" \
    "s_nop 7\n" \
    "s_nop 3\n" \
    "buffer_load_dwordx4 v[40:43], %[vo], %[rw], s44 offen offset:2048 sc1\n" \
    "s_nop 7\n" \
    "s_nop 3\n" \
    "buffer_load_dwordx4 v[44:47], %[vo], %[rw], s44 offen offset:3072 sc1\n" \
    "s_nop 7\n" \
    "s_nop 3\n" \
    "buffer_load_dwordx4 v[48:51], %[vo], %[rw], s45 offen offset:0 sc1\n" \
    "s_nop 7\n" \
    "s_nop 3\n" \
    "buffer_load_dwordx4 v[52:55], %[vo], %[rw], s45 offen offset:1024 sc1\n" \
    "ds_read_b128 v[96:99], %[wl] offset:4096\n" \
    "ds_read_b128 v[100:103], %[wl] offset:5120\n" \
    "s_waitcnt vmcnt(9)\n" \
    "v_smfmac_f32_16x16x64_bf16 v[0:3], v[16:19], a[0:7], %[idx]\n" \
    "v_max3_u32 v104, v104, v16, v17\n" \
    "v_smfmac_f32_16x16x64_bf16 v[4:7], v[16:19], a[128:135], %[idx]\n" \
    "v_max3_u32 v104, v104, v18, v19\n" \
    "v_smfmac_f32_16x16x64_bf16 v[8:11], v[16:19], v[128:135], %[idx]\n" \
    "s_waitcnt lgkmcnt(4)\n" \
    "v_smfmac_f32_16x16x64_bf16 v[12:15], v[16:19], v[80:87], %[idx]\n" \
    "buffer_load_dwordx4 v[56:59], %[vo], %[rw], s45 offen offset:2048 sc1\n" \
    "ds_read_b128 v[80:83], %[wl] offset:6144\n" \
    "ds_read_b128 v[84:87], %[wl] offset:7168\n" \
    "s_waitcnt vmcnt(9)\n" \
    "v_smfmac_f32_16x16x64_bf16 v[0:3], v[20:23], a[8:15], %[idx]\n" \
    "v_max3_u32 v104, v104, v20, v21\n" \
    "v_smfmac_f32_16x16x64_bf16 v[4:7], v[20:23], a[136:143], %[idx]\n" \
    "v_max3_u32 v104, v104, v22, v23\n" \
    "v_smfmac_f32_16x16x64_bf16 v[8:11], v[20:23], v[136:143], %[idx]\n" \
    "s_waitcnt lgkmcnt(4)\n" \
    "v_smfmac_f32_16x16x64_bf16 v[12:15], v[20:23], v[88:95], %[idx]\n" \
    "buffer_load_dwordx4 v[60:63], %[vo], %[rw], s45 offen offset:3072 sc1\n" \
    "ds_read_b128 v[88:91], %[wl] offset:8192\n" \
    "ds_read_b128 v[92:95], %[wl] offset:9216\n" \
    "s_waitcnt vmcnt(9)\n" \
    "v_smfmac_f32_16x16x64_bf16 v[0:3], v[24:27], a[16:23], %[idx]\n" \
    "v_max3_u32 v104, v104, v24, v25\n" \
    "v_smfmac_f32_16x16x64_bf16 v[4:7], v[24:27], a[144:151], %[idx]\n" \
    "v_max3_u32 v104, v104, v26, v27\n" \
    "v_smfmac_f32_16x16x64_bf16 v[8:11], v[24:27], v[144:151], %[idx]\n" \
    "s_waitcnt lgkmcnt(4)\n" \
    "v_smfmac_f32_16x16x64_bf16 v[12:15], v[24:27], v[96:103], %[idx]\n" \
    "buffer_load_dwordx4 v[64:67], %[vo], %[rw], s46 offen offset:0 sc1\n" \
    "ds_read_b128 v[96:99], %[wl] offset:10240\n" \
    "ds_read_b128 v[100:103], %[wl] offset:11264\n" \
    "s_waitcnt vmcnt(9)\n" \
    "v_smfmac_f32_16x16x64_bf16 v[0:3], v[28:31], a[24:31], %[idx]\n" \
    "v_max3_u32 v104, v104, v28, v29\n" \
    "v_smfmac_f32_16x16x64_bf16 v[4:7], v[28:31], a[152:159], %[idx]\n" \
    "v_max3_u32 v104, v104, v30, v31\n" \
    "v_smfmac_f32_16x16x64_bf16 v[8:11], v[28:31], v[152:159], %[idx]\n" \
    "s_waitcnt lgkmcnt(4)\n" \
    "v_smfmac_f32_16x16x64_bf16 v[12:15], v[28:31], v[80:87], %[idx]\n" \
    "buffer_load_dwordx4 v[68:71], %[vo], %[rw], s46 offen offset:1024 sc1\n" \
    "ds_read_b128 v[80:83], %[wl] offset:12288\n" \
    "ds_read_b128 v[84:87], %[wl] offset:13312\n" \
    "s_waitcnt vmcnt(9)\n" \
    "v_smfmac_f32_16x16x64_bf16 v[0:3], v[32:35], a[32:39], %[idx]\n" \
    "v_max3_u32 v104, v104, v32, v33\n" \
    "v_smfmac_f32_16x16x64_bf16 v[4:7], v[32:35], a[160:167], %[idx]\n" \
    "v_max3_u32 v104, v104, v34, v35\n" \
    "v_smfmac_f32_16x16x64_bf16 v[8:11], v[32:35], v[160:167], %[idx]\n" \
    "s_waitcnt lgkmcnt(4)\n" \
    "v_smfmac_f32_16x16x64_bf16 v[12:15], v[32:35], v[88:95], %[idx]\n" \
    "buffer_load_dwordx4 v[72:75], %[vo], %[rw], s46 offen offset:2048 sc1\n" \
    "ds_read_b128 v[88:91], %[wl] offset:14336\n" \
    "ds_read_b128 v[92:95], %[wl] offset:15360\n" \
    "s_waitcnt vmcnt(9)\n" \
    "v_smfmac_f32_16x16x64_bf16 v[0:3], v[36:39], a[40:47], %[idx]\n" \
    "v_max3_u32 v104, v104, v36, v37\n" \
    "v_smfmac_f32_16x16x64_bf16 v[4:7], v[36:39], a[168:175], %[idx]\n" \
    "v_max3_u32 v104, v104, v38, v39\n" \
    "v_smfmac_f32_16x16x64_bf16 v[8:11], v[36:39], v[168:175], %[idx]\n" \
    "s_waitcnt lgkmcnt(4)\n" \
    "v_smfmac_f32_16x16x64_bf16 v[12:15], v[36:39], v[96:103], %[idx]\n" \
    "buffer_load_dwordx4 v[76:79], %[vo], %[rw], s46 offen offset:3072 sc1\n" \
    "ds_read_b128 v[96:99], %[wl] offset:16384\n" \
    "ds_read_b128 v[100:103], %[wl] offset:17408\n" \
    "s_waitcnt vmcnt(9)\n" \
    "v_smfmac_f32_16x16x64_bf16 v[0:3], v[40:43], a[48:55], %[idx]\n" \
    "v_max3_u32 v104, v104, v40, v41\n" \
    "v_smfmac_f32_16x16x64_bf16 v[4:7], v[40:43], a[176:183], %[idx]\n" \
    "v_max3_u32 v104, v104, v42, v43\n" \
    "v_smfmac_f32_16x16x64_bf16 v[8:11], v[40:43], v[176:183], %[idx]\n" \
    "s_waitcnt lgkmcnt(4)\n" \
    "v_smfmac_f32_16x16x64_bf16 v[12:15], v[40:43], v[80:87], %[idx]\n" \
    "buffer_load_dwordx2 v[106:107], %[bv], %[rw], %[bs] offen sc1\n" \
    "ds_read_b128 v[80:83], %[wl] offset:18432\n" \
    "ds_read_b128 v[84:87], %[wl] offset:19456\n" \
    "s_waitcnt vmcnt(9)\n" \
    "v_smfmac_f32_16x16x64_bf16 v[0:3], v[44:47], a[56:63], %[idx]\n" \
    "v_max3_u32 v104, v104, v44, v45\n" \
    "v_smfmac_f32_16x16x64_bf16 v[4:7], v[44:47], a[184:191], %[idx]\n" \
    "v_max3_u32 v104, v104, v46, v47\n" \
    "v_smfmac_f32_16x16x64_bf16 v[8:11], v[44:47], v[184:191], %[idx]\n" \
    "s_waitcnt lgkmcnt(4)\n" \
    "v_smfmac_f32_16x16x64_bf16 v[12:15], v[44:47], v[88:95], %[idx]\n" \
    "buffer_load_dword v108, %[gv], %[rw], %[gs] offen sc1\n" \
    "ds_read_b128 v[88:91], %[wl] offset:20480\n" \
    "ds_read_b128 v[92:95], %[wl] offset:21504\n" \
    "s_waitcnt vmcnt(9)\n" \
    "v_smfmac_f32_16x16x64_bf16 v[0:3], v[48:51], a[64:71], %[idx]\n" \
    "v_max3_u32 v104, v104, v48, v49\n" \
    "v_smfmac_f32_16x16x64_bf16 v[4:7], v[48:51], a[192:199], %[idx]\n" \
    "v_max3_u32 v104, v104, v50, v51\n" \
    "v_smfmac_f32_16x16x64_bf16 v[8:11], v[48:51], v[192:199], %[idx]\n" \
    "s_waitcnt lgkmcnt(4)\n" \
    "v_smfmac_f32_16x16x64_bf16 v[12:15], v[48:51], v[96:103], %[idx]\n" \
    "buffer_load_dword v105, %[sv], %[rw], %[ss] offen sc1\n" \
    "ds_read_b128 v[96:99], %[wl] offset:22528\n" \
    "ds_read_b128 v[100:103], %[wl] offset:23552\n" \
    "s_waitcnt vmcnt(9)\n" \
    "v_smfmac_f32_16x16x64_bf16 v[0:3], v[52:55], a[72:79], %[idx]\n" \
    "v_max3_u32 v104, v104, v52, v53\n" \
    "v_smfmac_f32_16x16x64_bf16 v[4:7], v[52:55], a[200:207], %[idx]\n" \
    "v_max3_u32 v104, v104, v54, v55\n" \
    "v_smfmac_f32_16x16x64_bf16 v[8:11], v[52:55], v[200:207], %[idx]\n" \
    "s_waitcnt lgkmcnt(4)\n" \
    "v_smfmac_f32_16x16x64_bf16 v[12:15], v[52:55], v[80:87], %[idx]\n" \
    "ds_read_b128 v[80:83], %[wl] offset:24576\n" \
    "ds_read_b128 v[84:87], %[wl] offset:25600\n" \
    "s_waitcnt vmcnt(8)\n" \
    "v_smfmac_f32_16x16x64_bf16 v[0:3], v[56:59], a[80:87], %[idx]\n" \
    "v_max3_u32 v104, v104, v56, v57\n" \
    "v_smfmac_f32_16x16x64_bf16 v[4:7], v[56:59], a[208:215], %[idx]\n" \
    "v_max3_u32 v104, v104, v58, v59\n" \
    "v_smfmac_f32_16x16x64_bf16 v[8:11], v[56:59], v[208:215], %[idx]\n" \
    "s_waitcnt lgkmcnt(4)\n" \
    "v_smfmac_f32_16x16x64_bf16 v[12:15], v[56:59], v[88:95], %[idx]\n" \
    "ds_read_b128 v[88:91], %[wl] offset:26624\n" \
    "ds_read_b128 v[92:95], %[wl] offset:27648\n" \
    "s_waitcnt vmcnt(7)\n" \
    "v_smfmac_f32_16x16x64_bf16 v[0:3], v[60:63], a[88:95], %[idx]\n" \
    "v_max3_u32 v104, v104, v60, v61\n" \
    "v_smfmac_f32_16x16x64_bf16 v[4:7], v[60:63], a[216:223], %[idx]\n" \
    "v_max3_u32 v104, v104, v62, v63\n" \
    "v_smfmac_f32_16x16x64_bf16 v[8:11], v[60:63], v[216:223], %[idx]\n" \
    "s_waitcnt lgkmcnt(4)\n" \
    "v_smfmac_f32_16x16x64_bf16 v[12:15], v[60:63], v[96:103], %[idx]\n" \
    "ds_read_b128 v[96:99], %[wl] offset:28672\n" \
    "ds_read_b128 v[100:103], %[wl] offset:29696\n" \
    "s_waitcnt vmcnt(6)\n" \
    "v_smfmac_f32_16x16x64_bf16 v[0:3], v[64:67], a[96:103], %[idx]\n" \
    "v_max3_u32 v104, v104, v64, v65\n" \
    "v_smfmac_f32_16x16x64_bf16 v[4:7], v[64:67], a[224:231], %[idx]\n" \
    "v_max3_u32 v104, v104, v66, v67\n" \
    "v_smfmac_f32_16x16x64_bf16 v[8:11], v[64:67], v[224:231], %[idx]\n" \
    "s_waitcnt lgkmcnt(4)\n" \
    "v_smfmac_f32_16x16x64_bf16 v[12:15], v[64:67], v[80:87], %[idx]\n" \
    "ds_read_b128 v[80:83], %[wl] offset:30720\n" \
    "ds_read_b128 v[84:87], %[wl] offset:31744\n" \
    "s_waitcnt vmcnt(5)\n" \
    "v_smfmac_f32_16x16x64_bf16 v[0:3], v[68:71], a[104:111], %[idx]\n" \
    "v_max3_u32 v104, v104, v68, v69\n" \
    "v_smfmac_f32_16x16x64_bf16 v[4:7], v[68:71], a[232:239], %[idx]\n" \
    "v_max3_u32 v104, v104, v70, v71\n" \
    "v_smfmac_f32_16x16x64_bf16 v[8:11], v[68:71], v[232:239], %[idx]\n" \
    "s_waitcnt lgkmcnt(4)\n" \
    "v_smfmac_f32_16x16x64_bf16 v[12:15], v[68:71], v[88:95], %[idx]\n" \
    "s_waitcnt vmcnt(4)\n" \
    "v_smfmac_f32_16x16x64_bf16 v[0:3], v[72:75], a[112:119], %[idx]\n" \
    "v_max3_u32 v104, v104, v72, v73\n" \
    "v_smfmac_f32_16x16x64_bf16 v[4:7], v[72:75], a[240:247], %[idx]\n" \
    "v_max3_u32 v104, v104, v74, v75\n" \
    "v_smfmac_f32_16x16x64_bf16 v[8:11], v[72:75], v[240:247], %[idx]\n" \
    "s_waitcnt lgkmcnt(2)\n" \
    "v_smfmac_f32_16x16x64_bf16 v[12:15], v[72:75], v[96:103], %[idx]\n" \
    "s_waitcnt vmcnt(3)\n" \
    "v_smfmac_f32_16x16x64_bf16 v[0:3], v[76:79], a[120:127], %[idx]\n" \
    "v_max3_u32 v104, v104, v76, v77\n" \
    "v_smfmac_f32_16x16x64_bf16 v[4:7], v[76:79], a[248:255], %[idx]\n" \
    "v_max3_u32 v104, v104, v78, v79\n" \
    "v_smfmac_f32_16x16x64_bf16 v[8:11], v[76:79], v[248:255], %[idx]\n" \
    "s_waitcnt lgkmcnt(0)\n" \
    "v_smfmac_f32_16x16x64_bf16 v[12:15], v[76:79], v[80:87], %[idx]\n" \
    "s_waitcnt vmcnt(0)\n" \
    "s_nop 5\n" \
    "v_permlane32_swap_b32 v0, v8\n" \
    "v_add_f32 v0, v0, v8\n" \
    "v_permlane32_swap_b32 v1, v9\n" \
    "v_add_f32 v1, v1, v9\n" \
    "v_permlane32_swap_b32 v2, v10\n" \
    "v_add_f32 v2, v2, v10\n" \
    "v_permlane32_swap_b32 v3, v11\n" \
    "v_add_f32 v3, v3, v11\n" \
    "v_permlane32_swap_b32 v4, v12\n" \
    "v_add_f32 v4, v4, v12\n" \
    "v_permlane32_swap_b32 v5, v13\n" \
    "v_add_f32 v5, v5, v13\n" \
    "v_permlane32_swap_b32 v6, v14\n" \
    "v_add_f32 v6, v6, v14\n" \
    "v_permlane32_swap_b32 v7, v15\n" \
    "v_add_f32 v7, v7, v15\n" \
    ""
#define OUT_CLOBBER "v16", "v17", "v18", "v19", "v20", "v21", "v22", "v23", "v24", "v25", "v26", "v27", "v28", "v29", "v30", "v31", "v32", "v33", "v34", "v35", "v36", "v37", "v38", "v39", "v40", "v41", "v42", "v43", "v44", "v45", "v46", "v47", "v48", "v49", "v50", "v51", "v52", "v53", "v54", "v55", "v56", "v57", "v58", "v59", "v60", "v61", "v62", "v63", "v64", "v65", "v66", "v67", "v68", "v69", "v70", "v71", "v72", "v73", "v74", "v75", "v76", "v77", "v78", "v79", "v80", "v81", "v82", "v83", "v84", "v85", "v86", "v87", "v88", "v89", "v90", "v91", "v92", "v93", "v94", "v95", "v96", "v97", "v98", "v99", "v100", "v101", "v102", "v103", "s44", "s45", "s46", "scc", "memory"
constexpr int NREP = 2, NCP = 4;
#define OWNER_ASM \
    "s_getpc_b64 s[96:97]\n" \
    "14:\n" \
    "s_add_u32 s96, s96, 2f-14b\n" \
    "s_addc_u32 s97, s97, 0\n" \
    "s_sub_u32 s68, s96, 2f-11f\n" \
    "s_subb_u32 s69, s97, 0\n" \
    "s_mov_b32 %[np], 0\n" \
    "s_add_i32 s72, %[pb], 0x10000\n" \
    "13:\n" \
    "s_memtime vcc\n" \
    "s_waitcnt lgkmcnt(0)\n" \
    "s_sub_i32 s70, %[lk], vcc_lo\n" \
    "s_cmp_gt_i32 s70, %[rd]\n" \
    "s_cbranch_scc1 13b\n" \
    "s_add_i32 s70, %[pq], 0x10000\n" \
    "buffer_store_dwordx4 %[sv], %[ro], %[rs], %[pq] offen offset:0 sc1\n" \
    "buffer_store_dwordx4 %[sv], %[ro], %[rs], %[pq] offen offset:1024 sc1\n" \
    "buffer_store_dwordx4 %[sv], %[ro], %[rs], s70 offen offset:0 sc1\n" \
    "buffer_store_dwordx4 %[sv], %[ro], %[rs], s70 offen offset:1024 sc1\n" \
    "v_mov_b32 v2, -1\n" \
    "buffer_store_dword v2, %[rm], %[rs], %[mq] offen sc1\n" \
    "buffer_store_dwordx4 %[sv], %[rt], %[rw], %[tq] offen sc1\n" \
    "s_mov_b32 s71, 4\n" \
    "10:\n" \
    "s_memtime vcc\n" \
    "s_waitcnt lgkmcnt(0)\n" \
    "s_sub_i32 s70, %[lk], vcc_lo\n" \
    "s_cmp_gt_i32 s70, 112\n" \
    "s_cbranch_scc1 10b\n" \
    "s_max_i32 s70, s70, 0\n" \
    "s_memtime s[58:59]\n" \
    "s_memrealtime s[44:45]\n" \
    "s_nop 1\n" \
    "s_memrealtime s[46:47]\n" \
    "s_nop 1\n" \
    "s_memrealtime s[48:49]\n" \
    "s_nop 1\n" \
    "s_memrealtime s[50:51]\n" \
    "s_nop 1\n" \
    "s_memrealtime s[52:53]\n" \
    "s_nop 1\n" \
    "s_memrealtime s[54:55]\n" \
    "s_nop 1\n" \
    "s_memrealtime s[56:57]\n" \
    "s_nop 1\n" \
    "s_memrealtime vcc\n" \
    "s_waitcnt lgkmcnt(0)\n" \
    "s_add_i32 s44, s44, s46\n" \
    "s_add_i32 s48, s48, s50\n" \
    "s_add_i32 s52, s52, s54\n" \
    "s_add_i32 s44, s44, s48\n" \
    "s_add_i32 s52, s52, s56\n" \
    "s_add_i32 s44, s44, s52\n" \
    "s_add_i32 s59, s44, vcc_lo\n" \
    "s_sub_i32 s44, s59, %[oT]\n" \
    "s_mov_b32 %[oT], s59\n" \
    "s_sub_i32 s45, s58, %[oC]\n" \
    "s_mov_b32 %[oC], s58\n" \
    "s_mul_i32 s46, s44, %[oR]\n" \
    "s_lshr_b32 s46, s46, 12\n" \
    "s_sub_i32 s46, s45, s46\n" \
    "s_sub_i32 s44, s44, 1000\n" \
    "s_cmp_lt_u32 s44, 2000\n" \
    "s_cselect_b32 s46, s46, 0\n" \
    "s_add_i32 %[oR], %[oR], s46\n" \
    "s_sub_i32 s45, %[oT], %[c8]\n" \
    "s_and_b32 s70, s70, -4\n" \
    "s_sub_u32 vcc_lo, s68, s70\n" \
    "s_subb_u32 vcc_hi, s69, 0\n" \
    "s_setpc_b64 vcc\n" \
    "s_nop 0\n" \
    "s_nop 0\n" \
    "s_nop 0\n" \
    "s_nop 0\n" \
    "s_nop 0\n" \
    "s_nop 0\n" \
    "s_nop 0\n" \
    "s_nop 0\n" \
    "s_nop 0\n" \
    "s_nop 0\n" \
    "s_nop 0\n" \
    "s_nop 0\n" \
    "s_nop 0\n" \
    "s_nop 0\n" \
    "s_nop 0\n" \
    "s_nop 0\n" \
    "s_nop 0\n" \
    "s_nop 0\n" \
    "s_nop 0\n" \
    "s_nop 0\n" \
    "s_nop 0\n" \
    "s_nop 0\n" \
    "s_nop 0\n" \
    "s_nop 0\n" \
    "s_nop 0\n" \
    "s_nop 0\n" \
    "s_nop 0\n" \
    "s_nop 0\n" \
    "11:\n" \
    "buffer_load_dwordx2 v[0:1], %[ca], %[rs], %[cs] offen sc1\n" \
    "s_waitcnt vmcnt(0)\n" \
    "6:\n" \
    "s_memtime s[46:47]\n" \
    "v_subrev_u32 v0, s45, v0\n" \
    "v_cmp_ne_u16 vcc, %[tag], v1\n" \
    "s_add_i32 s48, %[oC], -136\n" \
    "v_max_i32_dpp v0, v0, v0 row_shr:1 row_mask:0xf bank_mask:0xf\n" \
    "v_max_u32_dpp v1, v1, v1 row_shr:1 row_mask:0xf bank_mask:0xf\n" \
    "s_cbranch_vccnz 7f\n" \
    "8:\n" \
    "v_max_i32_dpp v0, v0, v0 row_shr:2 row_mask:0xf bank_mask:0xf\n" \
    "v_max_u32_dpp v1, v1, v1 row_shr:2 row_mask:0xf bank_mask:0xf\n" \
    "s_nop 0\n" \
    "v_max_i32_dpp v0, v0, v0 row_shr:4 row_mask:0xf bank_mask:0xf\n" \
    "v_max_u32_dpp v1, v1, v1 row_shr:4 row_mask:0xf bank_mask:0xf\n" \
    "s_nop 0\n" \
    "v_max_i32_dpp v0, v0, v0 row_shr:8 row_mask:0xf bank_mask:0xf\n" \
    "v_max_u32_dpp v1, v1, v1 row_shr:8 row_mask:0xf bank_mask:0xf\n" \
    "s_waitcnt lgkmcnt(0)\n" \
    "v_max_i32_dpp v0, v0, v0 row_bcast:15 row_mask:0xa bank_mask:0xf\n" \
    "v_max_u32_dpp v1, v1, v1 row_bcast:15 row_mask:0xa bank_mask:0xf\n" \
    "s_add_i32 s46, s46, 44\n" \
    "v_max_i32_dpp v0, v0, v0 row_bcast:31 row_mask:0xc bank_mask:0xf\n" \
    "v_max_u32_dpp v1, v1, v1 row_bcast:31 row_mask:0xc bank_mask:0xf\n" \
    "v_readlane_b32 s44, v0, 63\n" \
    "v_readlane_b32 s49, v1, 63\n" \
    "s_mul_i32 s47, s44, %[oR]\n" \
    "s_ashr_i32 s47, s47, 12\n" \
    "s_add_i32 s47, s47, s48\n" \
    "s_sub_i32 s58, s47, s46\n" \
    "s_cmp_gt_i32 s58, 112\n" \
    "s_cbranch_scc1 1f\n" \
    "12:\n" \
    "s_max_i32 s58, s58, 0\n" \
    "s_and_b32 s58, s58, -4\n" \
    "s_sub_u32 vcc_lo, s96, s58\n" \
    "s_subb_u32 vcc_hi, s97, 0\n" \
    "s_setpc_b64 vcc\n" \
    "s_nop 0\n" \
    "s_nop 0\n" \
    "s_nop 0\n" \
    "s_nop 0\n" \
    "s_nop 0\n" \
    "s_nop 0\n" \
    "s_nop 0\n" \
    "s_nop 0\n" \
    "s_nop 0\n" \
    "s_nop 0\n" \
    "s_nop 0\n" \
    "s_nop 0\n" \
    "s_nop 0\n" \
    "s_nop 0\n" \
    "s_nop 0\n" \
    "s_nop 0\n" \
    "s_nop 0\n" \
    "s_nop 0\n" \
    "s_nop 0\n" \
    "s_nop 0\n" \
    "s_nop 0\n" \
    "s_nop 0\n" \
    "s_nop 0\n" \
    "s_nop 0\n" \
    "s_nop 0\n" \
    "s_nop 0\n" \
    "s_nop 0\n" \
    "s_nop 0\n" \
    "2:\n" \
    "buffer_load_dwordx4 v[0:3], %[o], %[rs], %[pb] offen offset:0 sc1\n" \
    "buffer_load_dwordx4 v[4:7], %[o], %[rs], %[pb] offen offset:1024 sc1\n" \
    "buffer_load_dwordx4 v[8:11], %[o], %[rs], s72 offen offset:0 sc1\n" \
    "buffer_load_dwordx4 v[12:15], %[o], %[rs], s72 offen offset:1024 sc1\n" \
    "s_add_i32 %[np], %[np], 1\n" \
    "s_add_i32 %[tm], s44, s45\n" \
    "s_lshr_b32 s50, s49, 16\n" \
    "s_add_i32 s50, s50, s44\n" \
    "s_add_i32 s50, s50, %[cw]\n" \
    "s_mul_i32 s50, s50, %[oR]\n" \
    "s_ashr_i32 s50, s50, 12\n" \
    "s_add_i32 %[lk], s50, %[oC]\n" \
    "s_waitcnt vmcnt(3)\n" \
    "v_mfma_f32_16x16x32_bf16 v[16:19], %[sel], v[0:3], 0\n" \
    "v_max3_u32 v20, v0, v1, v1\n" \
    "v_max3_u32 v20, v2, v3, v20\n" \
    "s_waitcnt vmcnt(2)\n" \
    "v_mfma_f32_16x16x32_bf16 v[16:19], %[sel], v[4:7], v[16:19]\n" \
    "v_max3_u32 v20, v4, v5, v20\n" \
    "v_max3_u32 v20, v6, v7, v20\n" \
    "s_waitcnt vmcnt(1)\n" \
    "v_mfma_f32_16x16x32_bf16 v[16:19], %[sel], v[8:11], v[16:19]\n" \
    "v_max3_u32 v20, v8, v9, v20\n" \
    "v_max3_u32 v20, v10, v11, v20\n" \
    "s_waitcnt vmcnt(0)\n" \
    "v_mfma_f32_16x16x32_bf16 v[16:19], %[sel], v[12:15], v[16:19]\n" \
    "v_max3_u32 v20, v12, v13, v20\n" \
    "v_max3_u32 v20, v14, v15, v20\n" \
    "v_cmp_eq_u32 vcc, -1, v20\n" \
    "s_nop 3\n" \
    "s_cbranch_vccnz 4f\n" \
    "3:\n" \
    "v_add_f32_dpp v16, v16, v16 row_ror:8 row_mask:0xf bank_mask:0xf\n" \
    "v_add_f32_dpp v17, v17, v17 row_ror:8 row_mask:0xf bank_mask:0xf\n" \
    "v_cvt_pk_bf16_f32 v16, v16, v17\n" \
    "buffer_store_dword v16, %[so], %[rs], %[mb0] offen sc1\n" \
    "s_branch 5f\n" \
    "1:\n" \
    "s_memtime vcc\n" \
    "s_waitcnt lgkmcnt(0)\n" \
    "s_sub_i32 s58, s47, vcc_lo\n" \
    "s_cmp_gt_i32 s58, 112\n" \
    "s_cbranch_scc1 1b\n" \
    "s_branch 12b\n" \
    "4:\n" \
    "s_add_i32 %[bud], %[bud], -1\n" \
    "s_cmp_lt_i32 %[bud], 0\n" \
    "s_cbranch_scc0 2b\n" \
    "s_branch 3b\n" \
    "7:\n" \
    "s_bcnt1_i32_b64 s44, vcc\n" \
    "s_cmp_le_u32 s44, s71\n" \
    "s_mov_b32 s71, 0\n" \
    "s_cbranch_scc1 15f\n" \
    "s_cmp_eq_u32 %[tag], 1\n" \
    "s_cbranch_scc1 9f\n" \
    "s_sleep 15\n" \
    "9:\n" \
    "s_sleep 6\n" \
    "15:\n" \
    "s_add_i32 %[bud], %[bud], -1\n" \
    "s_cmp_lt_i32 %[bud], 0\n" \
    "s_cbranch_scc1 8b\n" \
    "buffer_load_dwordx2 v[0:1], %[ca], %[rs], %[cs] offen sc1\n" \
    "s_memtime s[58:59]\n" \
    "s_memrealtime s[44:45]\n" \
    "s_nop 1\n" \
    "s_memrealtime s[46:47]\n" \
    "s_nop 1\n" \
    "s_memrealtime s[48:49]\n" \
    "s_nop 1\n" \
    "s_memrealtime s[50:51]\n" \
    "s_nop 1\n" \
    "s_memrealtime s[52:53]\n" \
    "s_nop 1\n" \
    "s_memrealtime s[54:55]\n" \
    "s_nop 1\n" \
    "s_memrealtime s[56:57]\n" \
    "s_nop 1\n" \
    "s_memrealtime vcc\n" \
    "s_waitcnt lgkmcnt(0)\n" \
    "s_add_i32 s44, s44, s46\n" \
    "s_add_i32 s48, s48, s50\n" \
    "s_add_i32 s52, s52, s54\n" \
    "s_add_i32 s44, s44, s48\n" \
    "s_add_i32 s52, s52, s56\n" \
    "s_add_i32 s44, s44, s52\n" \
    "s_add_i32 s59, s44, vcc_lo\n" \
    "s_sub_i32 s44, s59, %[oT]\n" \
    "s_mov_b32 %[oT], s59\n" \
    "s_sub_i32 s45, s58, %[oC]\n" \
    "s_mov_b32 %[oC], s58\n" \
    "s_mul_i32 s46, s44, %[oR]\n" \
    "s_lshr_b32 s46, s46, 12\n" \
    "s_sub_i32 s46, s45, s46\n" \
    "s_sub_i32 s44, s44, 1000\n" \
    "s_cmp_lt_u32 s44, 2000\n" \
    "s_cselect_b32 s46, s46, 0\n" \
    "s_add_i32 %[oR], %[oR], s46\n" \
    "s_sub_i32 s45, %[oT], %[c8]\n" \
    "s_waitcnt vmcnt(0)\n" \
    "s_branch 6b\n" \
    "5:\n" \
    ""
#define OWNER_CLOBBER "v0", "v1", "v2", "v3", "v4", "v5", "v6", "v7", "v8", "v9", "v10", "v11", "v12", "v13", "v14", "v15", "v16", "v17", "v18", "v19", "v20", "s44", "s45", "s46", "s47", "s48", "s49", "s50", "s51", "s52", "s53", "s54", "s55", "s56", "s57", "s58", "s59", "s68", "s69", "s70", "s71", "s72", "s96", "s97", "vcc", "scc", "memory"
constexpr int O_W = 0;
constexpr int O_STG = 131072;
constexpr int O_B = O_STG + 20480;
constexpr int OUT_REP = 7;
#define OUT_AUX 16
DI int opaque_lane() { int l; asm volatile("v_mbcnt_lo_u32_b32 %0, -1, 0\n\tv_mbcnt_hi_u32_b32 %0, -1, %0" : "=v"(l)); return l; }
template <int O, class V> DI void put4(V& d, u32x4 f) {
#pragma unroll
    for (int j = 0; j < 4; ++j) d[O + j] = f[j];
}
DI void role_outp(const Args& a, LAS char* lds, unsigned lds_base, int nb, int rep, rsrc_t rw, int& budget) {
    const int tid = threadIdx.x, lane = tid & 63, w = __builtin_amdgcn_readfirstlane(tid >> 6);
    WG_BARRIER();
    const int n = lane & 15, g = lane >> 4;
    const int wl = O_W + w * 32768 + lane * 16;
    u32x32 A[8], V[4];
    {
        const int stg = O_STG + w * 5120;
        const rsrc_t ro = __builtin_amdgcn_make_buffer_rsrc((void*)a.Wo, 0, HD * DM * 2, 0x00020000);
        const int so = (1024 * w + (lane >> 3)) * (DM * 2) + nb * 128 + (lane & 7) * 16, sw = stg + (lane >> 3) * 160 + (lane & 7) * 16;
        const int ra = stg + (16 * (g & 1) + 4 * (g >> 1) + (n >> 2)) * 160 + (n & 3) * 8;
        constexpr int PB = 4;
        u32x4 b[PB][4];
        auto fetch = [&](int ks) {
#pragma unroll
            for (int i = 0; i < 4; ++i) b[ks % PB][i] = __builtin_amdgcn_raw_buffer_load_b128(ro, so, (64 * (ks >> 1) + 16 * (ks & 1) + 8 * (i & 1) + 32 * (i >> 1)) * (DM * 2), 0);
        };
        unsigned tp = now();
        const int pc = a.d1 >> 16;
#pragma unroll
        for (int ks = 0; ks < PB; ++ks) { fetch(ks); FENCE(); tp += pc; while ((int)(now() - tp) < 0) {} }
        FENCE();
        static_for<32>([&](auto ic) {
            constexpr int ks = decltype(ic)::value;
#pragma unroll
            for (int i = 0; i < 4; ++i) *(LAS u32x4*)(lds + sw + i * 1280) = b[ks % PB][i];
            if (ks + PB < 32) { fetch(ks + PB); FENCE(); tp += pc; }
            u32x4 f[4];
#pragma unroll
            for (int t = 0; t < 4; ++t) { const u32x2 lo = tr_read(lds, ra + t * 32), hi = tr_read(lds, ra + t * 32 + 1280); f[t] = (u32x4){lo.x, lo.y, hi.x, hi.y}; }
            put4<4 * (ks & 7)>(A[ks >> 3], f[0]); put4<4 * (ks & 7)>(A[4 + (ks >> 3)], f[1]); put4<4 * (ks & 7)>(V[ks >> 3], f[2]);
            *(LAS u32x4*)(lds + wl + ks * 1024) = f[3];
            if constexpr (ks == 7) asm volatile("" : "+{a[0:31]}"(A[0]), "+{a[128:159]}"(A[4]), "+{v[128:159]}"(V[0]));
            if constexpr (ks == 15) asm volatile("" : "+{a[32:63]}"(A[1]), "+{a[160:191]}"(A[5]), "+{v[160:191]}"(V[1]));
            if constexpr (ks == 23) asm volatile("" : "+{a[64:95]}"(A[2]), "+{a[192:223]}"(A[6]), "+{v[192:223]}"(V[2]));
            if constexpr (ks == 31) asm volatile("" : "+{a[96:127]}"(A[3]), "+{a[224:255]}"(A[7]), "+{v[224:255]}"(V[3]));
            FENCE();
            if (ks + PB < 31) while ((int)(now() - tp) < 0) {}
        });
    }
    wait_prog(rw, F_S4 + lane * 4, true, 1, budget);
    const rsrc_t ru = __builtin_amdgcn_make_buffer_rsrc(a.out, 0, SEQ * DM * 2, 0x00020000);
    constexpr int SP = 2 * LATE - 2;
    auto first = [&](int u) { return u >= SP ? 8 * u + 8 * SP : 16 * u; };
    auto st_s = [&](int u) { return (u >= 2 * FAST ? F_H + ((first(u) >> 4) - 2 * FAST) * 4096 : F_O + (u & 3) * 4) + w * 1024; };
    auto bn_s = [&](int u) { return OFF_BO + (64 * nb + 16 * w) * BO_ROW + first(u) * 2; };
    auto gn_s = [&](int u) { return OFF_GT + (first(u) + 4 * w) * 4; };
    int st = -1;
    u32x2 bn = ld64(rw, (lane >> 2) * BO_ROW + (lane & 3) * 8, bn_s(rep));
    float gn = __uint_as_float(ld32(rw, g * 4, gn_s(rep)));
    __builtin_amdgcn_s_waitcnt(0);
#define OUT_OPS "=&{v[0:15]}"(acc), "=&{v104}"(top), "=&{v105}"(st), "=&{v[106:107]}"(bn), "=&{v108}"(gn) \
        : "{a[0:31]}"(A[0]), "{a[32:63]}"(A[1]), "{a[64:95]}"(A[2]), "{a[96:127]}"(A[3]), "{a[128:159]}"(A[4]), "{a[160:191]}"(A[5]), "{a[192:223]}"(A[6]), "{a[224:255]}"(A[7]), \
          "{v[128:159]}"(V[0]), "{v[160:191]}"(V[1]), "{v[192:223]}"(V[2]), "{v[224:255]}"(V[3]), [rw] "s"(rw), [so] "s"(t0 * 8192), [wl] "v"(lds_base + O_W + w * 32768 + lane * 16), \
          [tv] "v"(tv), [tm] "s"(tm), [vo] "v"(vo), [sv] "v"(sv), [bv] "v"((lane >> 2) * BO_ROW + (lane & 3) * 8), [gv] "v"(g * 4), \
          [ss] "s"(st_s(u + OUT_REP)), [bs] "s"(bn_s(u + OUT_REP)), [gs] "s"(gn_s(u + OUT_REP))
    for (int u = rep; u < SP + 4; u += OUT_REP) {
        const bool fast = u >= 2 * FAST, sp = u >= SP;
        const int t0 = first(u);
        WG_BARRIER();
        int lane = opaque_lane(), n = lane & 15, g = lane >> 4;
        if (u == 3 * OUT_REP && nb == 0) __builtin_amdgcn_raw_buffer_store_b64(launch_tag(a), rw, w + lane == 0 ? F_EPOCH : OOB, 0, 16);
        S4S(0)
        TT(u >= 119, 0)
        *(LAS u32x2*)(lds + O_B + (16 * w + (lane >> 2)) * 40 + (lane & 3) * 8) = bn;
        const float gt = gn;
        const int tv = OFF_OF + w * 16384 + nb * 2048 + g * 65536 + n * 128, tm = __builtin_amdgcn_readfirstlane(nb < 8 ? sp ? 0xFFFF : -1 : 0);
        const int vo = OFF_OF + w * 16384 + (n & 7) * 16 + (sp ? (n >> 3) * 512 + g * 128 : (n >> 3) * 65536 + (g & 1) * 512 + (g >> 1) * 128);
        const int thr = fast ? 0 : u >> 1, sv = (lane & 7) * 128;
        if (!__all(st >= thr)) do { __builtin_amdgcn_s_sleep(1); st = (int)ld32(rw, sv, st_s(u)); asm volatile("" ::: "memory"); } while (!__all(st >= thr) && --budget >= 0);
        if (fast) { const unsigned t = now(); (void)wait_until(t + wave_max32(min((int)(2 * (st - t)) >> 1, 500))); }
        S4S(1)
        TT(u >= 119, 1)
        f32x16 acc; unsigned top;
        if (sp) {
            do asm volatile(OUT8_ASM : OUT_OPS, [idx] "v"(n & 8 ? 0xEEEE : 0x4444) : OUT_CLOBBER); while (__any(top == SENT) && --budget >= 0);
            TT(u >= 119, 2)
            lane = opaque_lane(); n = lane & 15; g = lane >> 4;
#pragma unroll
            for (int i = 0; i < 8; ++i) *(LAS float*)(lds + O_STG + (w * 16 + 4 * (g & 1) + (i & 3)) * 272 + (g >> 1) * 128 + (i >> 2) * 64 + n * 4) = acc[i];
        } else {
            do asm volatile(OUT16_ASM : OUT_OPS : OUT_CLOBBER); while (__any(top == SENT) && --budget >= 0);
            S4S(2)
            TT(u >= 119, 2)
            lane = opaque_lane(); n = lane & 15; g = lane >> 4;
#pragma unroll
            for (int t = 0; t < 4; ++t) *(LAS f32x4*)(lds + O_STG + (w * 16 + n) * 272 + t * 64 + g * 16) = (f32x4){acc[4 * t], acc[4 * t + 1], acc[4 * t + 2], acc[4 * t + 3]};
        }
        WG_BARRIER();
        S4S(3)
        const int tok = 4 * w + g, c4 = 4 * n;
        f32x4 r[4]; u16 bv[4];
#pragma unroll
        for (int ww = 0; ww < 4; ++ww) { r[ww] = *(LAS f32x4*)(lds + O_STG + (ww * 16 + tok) * 272 + c4 * 4); bv[ww] = *(LAS u16*)(lds + O_B + (c4 + ww) * 40 + tok * 2); }
        FENCE();
        const f32x4 v = gt * ((r[0] + r[1]) + (r[2] + r[3])) + (1.0f - gt) * (f32x4){bf2f(bv[0]), bf2f(bv[1]), bf2f(bv[2]), bf2f(bv[3])};
        __builtin_amdgcn_raw_buffer_store_b64((u32x2){pack2(v[0], v[1]), pack2(v[2], v[3])}, ru, sp && tok >= 8 ? OOB : (tok * DM + 64 * nb + c4) * 2, t0 * (DM * 2), OUT_AUX);
        S4S(4)
        TT(u >= 119, 3)
        TT(u >= 105 && u < 112, 4)
    }
}

constexpr int L_MEMS = 0;
constexpr int L_Q = 9216;
constexpr int L_KO = L_Q + 2560;
constexpr int L_KX = 16384;
constexpr int L_W = 32768;
constexpr int C_RING = 8;
static_assert(S_TOTAL <= L_SINK && L_W + C_RING * 16384 <= L_TOTAL && L_KO + 8 * 576 <= L_KX, "LDS budget");
static_assert(P_STG + P_RING * 16384 <= L_TOTAL, "LDS budget");

DI int pos2col(int p) { return p < 512 ? (p & ~63) + 16 * (p & 3) + ((p & 63) >> 2) : p; }
DI void unit_rc(int u, int& i, int& p0) {
    if (u < 512) { i = 2 * ((u >> 2) & 3) + (u & 1); p0 = (u & ~15) + 4 * (u & 2); }
    else { const int v = u - 512; i = (v >> 1) & 7; p0 = 512 + 16 * (v >> 4) + 8 * (v & 1); }
}
DI void mem0_arrange(LAS char* lds, int raw, int tid) {
#pragma unroll
    for (int k = 0; k < 2; ++k) {
        int i, p0; unit_rc(tid + 256 * k, i, p0);
        unsigned r[4];
#pragma unroll
        for (int j = 0; j < 4; ++j) r[j] = *(LAS unsigned*)(lds + raw + (i * DM + pos2col(p0)) * 2 + 32 * j);
        *(LAS u32x4*)(lds + L_MEMS + (tid + 256 * k) * 16) = (u32x4){__builtin_amdgcn_perm(r[1], r[0], 0x05040100), __builtin_amdgcn_perm(r[3], r[2], 0x05040100),
                                                                     __builtin_amdgcn_perm(r[1], r[0], 0x07060302), __builtin_amdgcn_perm(r[3], r[2], 0x07060302)};
    }
    int i, p0; unit_rc(512 + (tid & 63), i, p0);
    *(LAS u32x4*)(lds + L_MEMS + 8192 + (tid & 63) * 16) = *(LAS u32x4*)(lds + raw + (i * DM + p0) * 2);
}
DI unsigned read_canaries(int s, rsrc_t rs, int lane, int& budget) {
    const int o = 2 * OFF_CAN + (s & 3) * 16384 + (lane >> 2) * 512 + (lane & 3) * 16;
    u32x2 v;
    for (;;) {
        v = ld64(rs, o, 0);
        asm volatile("" ::: "memory");
        if (__all((v.y & 0xffff) == (unsigned)(s + 1))) break;
        if (--budget < 0) break;
    }
    const unsigned t = 8 * now();
    return t + (unsigned)wave_max32(min(max((int)(v.x - t), -0x4000), 0x4000));
}
DI u32x8 sm_frag(LAS char* lds, int sl, int r0, int sa, int sH, int cb, int lo8) {
    auto rd = [&](int r) { return tr_read(lds, sl + r * 256 + ((cb ^ (2 * swz(r))) * 16) + lo8); };
    const u32x2 a = rd(r0), b = rd(r0 + sa), c = rd(r0 + sH), d = rd(r0 + sa + sH);
    return (u32x8){a.x, a.y, b.x, b.y, c.x, c.y, d.x, d.y};
}
#define GS(slot, i)
#define GV(slot, i, v)
DI int tp_unit(int U, int sel) { return OFF_TP + cmap((U & 511) * 4096) + ((sel >> (4 + (U >> 8 & 1)) ^ U >> 9) & 1) * 8192; }
template <int N> DI void finish(const Args& a, rsrc_t rw, int U, int sel, unsigned t, int& budget) {
    const int lane = opaque_lane(), n = lane & 7, g = lane >> 4;
    const rsrc_t ru = __builtin_amdgcn_make_buffer_rsrc(a.out, 0, SEQ * DM * 2, 0x00020000);
    constexpr int M = (N + 1) / 2;
    int at[M]; f32x2 bv[M]; float gt[M];
#pragma unroll
    for (int i = 0; i < M; ++i) {
        const int u = U + 2 * i + (lane >> 3 & 1 & (N - 1));
        const int tok = LATE * SEG + (U < 512 ? 2 * (u & 15) + (n >> 2) : (u & 32) + 4 * (u & 7) + (n >> 1));
        const int col = U < 512 ? 32 * (u >> 4) + 16 * (g >> 1) + 4 * (n & 3) + 2 * (g & 1) : 512 + 2 * (u & 24) + 8 * (n & 1) + 2 * g;
#pragma unroll
        for (int r = 0; r < 2; ++r) bv[i][r] = bf2f(__builtin_amdgcn_raw_buffer_load_b16(rw, col * BO_ROW + tok * 2, OFF_BO + r * BO_ROW, 16));
        gt[i] = __uint_as_float(ld32(rw, tok * 4, OFF_GT));
        at[i] = (tok * DM + col) * 2;
    }
    u32x4 one;
#pragma unroll
    for (int j = 0; j < 4; ++j) one[j] = (lane >> 2 & 3) == j ? 0x3F80u << 16 * (lane & 1) : 0u;
#pragma unroll
    for (int i = 0; i < M; ++i) { bv[i] *= 1.0f - gt[i]; asm volatile("" : "+v"(bv[i]), "+v"(one)); }
    (void)wait_until(t);
    f32x4 v[N];
    for (;;) {
        u32x4 d[4 * N];
#pragma unroll
        for (int k = 0; k < 4 * N; ++k) d[k] = ld128(rw, lane * 16 + (k & 3) * 1024, tp_unit(U + (k >> 2), sel));
        unsigned top = 0;
#pragma unroll
        for (int i = 0; i < N; ++i) {
            asm volatile(M16 "%0, %1, %2, 0\n\t" M16 "%0, %1, %3, %0\n\t" M16 "%0, %1, %4, %0\n\t" M16 "%0, %1, %5, %0\n\ts_nop 7" : "=&v"(v[i]) : "v"(one), "v"(d[4 * i]), "v"(d[4 * i + 1]), "v"(d[4 * i + 2]), "v"(d[4 * i + 3]));
            top = mx4(mx4(mx4(mx4(top, d[4 * i]), d[4 * i + 1]), d[4 * i + 2]), d[4 * i + 3]);
        }
        if (!__any(top == SENT) || --budget < 0) break;
        __builtin_amdgcn_s_sleep(4);
    }
#pragma unroll
    for (int i = 0; i < M; ++i) {
        f32x2 x;
#pragma unroll
        for (int r = 0; r < 2; ++r) {
            auto both = [](float y) { return y + __int_as_float(__builtin_amdgcn_update_dpp(0, __float_as_int(y), 0x128, 0xf, 0xf, true)); };
            const float p = both(v[2 * i][r]), q = both(v[(2 * i + 1) & (N - 1)][r]);
            x[r] = gt[i] * (lane & 8 ? q : p) + bv[i][r];
        }
        __builtin_amdgcn_raw_buffer_store_b32(pack2(x[0], x[1]), ru, at[i], 0, OUT_AUX);
    }
}
DI void owner_loop(const Args& a, int G, rsrc_t rs, rsrc_t rw, int sel, unsigned tbase, unsigned cbase, int& budget) {
    const int lane = threadIdx.x & 63, n = lane & 15, g = lane >> 4;
    const bool ex = G >= 64;
    const u32x4 sent4 = {SENT, SENT, SENT, SENT};
    const int js = (n & 2) ? 8 : ex ? (n & 4) + 2 * (n & 1) + (n >> 3) : 2 * (n >> 2) + (n & 1);
    u32x4 selu;
#pragma unroll
    for (int j = 0; j < 4; ++j) selu[j] = (js == 2 * j ? 0x3F80u : 0u) | (js == 2 * j + 1 ? 0x3F800000u : 0u);
    const int o = (part_unit(G, 0) ^ (sel & 8) * 256) + lane * 16;
    const int so = (n >> 3) * 16384 + ((ex ? 32768 + (G & 4) * 512 + (G & 3) * 128 : (G & 16) * 256 + (G & 32) * 64 + (G & 15) * 128) ^ (~sel & 8) * 256) + (g & 1) * 4 +
                   (ex ? (n & 4) * 16 + (g >> 1) * 32 + (n & 3) * 8 : (n & 6) * 16 + (g >> 1) * 16 + (n & 1) * 8);
    unsigned fails = 0;
    const int c8 = G >= 64 ? a.c1 >> 16 & 255 : (G & 28) == 0 ? a.c1 >> 8 & 255 : a.c1 & 255;
    const bool gv_on = true; (void)gv_on;
    unsigned oT = now(), oC = (unsigned)__builtin_readcyclecounter(), oR;
    { const unsigned r = (unsigned)(512.0f * (float)(oC - cbase) * __builtin_amdgcn_rcpf((float)(oT - tbase))); asm volatile("s_nop 0\n\tv_readfirstlane_b32 %0, %1" : "=s"(oR) : "v"(r)); }
    oT = 8 * oT + 16;
    unsigned lk = oC;
    budget = __builtin_amdgcn_readfirstlane(budget);
    const int ca = 2 * OFF_CAN + (lane >> 2) * 512 + (blockIdx.x & (NCP - 1)) * 128 + (lane & 3) * 16;
    unsigned tc;
    const unsigned oR0 = oR;
    oR = 0;
#pragma unroll 1
    for (int s = -1; s < NSEG - 1; ++s) {
        asm volatile("" : "+s"(s));
        const int pb = (s & 1) * 2 * PART_STEP;
        int np;
        asm volatile(OWNER_ASM : [np] "=&s"(np), [tm] "=&s"(tc), [bud] "+s"(budget), [oT] "+s"(oT), [oC] "+s"(oC), [oR] "+s"(oR), [lk] "+s"(lk)
            : [c8] "s"(c8), [ca] "v"(s < 0 ? OOB : ca), [cs] "s"((s & 3) * 16384), [tag] "s"(s + 1), [o] "v"(s < 0 ? OOB : o), [so] "v"(s < 0 ? OOB : so), [sel] "v"(selu), [rs] "s"(rs), [pb] "s"(pb), [mb0] "s"(2 * (OFF_MEMB + ((s + 1) & 3) * MEMB_STRIDE)),
              [cw] "s"(a.cw - c8), [rd] "s"(a.rd), [sv] "v"(sent4), [ro] "v"(s > 0 ? o : OOB), [pq] "s"(2 * PART_STEP - pb), [rm] "v"(s > 2 ? so : OOB), [mq] "s"(2 * (OFF_MEMB + ((s + 2) & 3) * MEMB_STRIDE)),
              [rw] "s"(rw), [rt] "v"(s >= 24 && s < 56 ? lane * 16 : OOB), [tq] "s"(tp_unit(8 * G + (s >> 2 & 7), sel) + (s & 3) * 1024) : OWNER_CLOBBER);
        if (s < 0) { oR = oR0; continue; }
        fails += np > 1;
    }
    wait_prog(rw, F_S4 + lane * 4, true, 1, budget);
    const unsigned t = now(), t0 = t + min((int)(tc - 8 * t) >> 3, 100);
    finish<4>(a, rw, 4 * G + 4 * (G & 64), sel, t0 + (a.xa >> 8 & 255), budget);
    if (G >= 32 && G < 64) finish<1>(a, rw, 512 + G, sel, t0 + (a.xb >> 24), budget);
}
#define CORNER 4
DI void role_owner(const Args& a, unsigned lds_base, int ow, rsrc_t rw, int& budget) {
    const int tid = threadIdx.x, lane = tid & 63, w = __builtin_amdgcn_readfirstlane(tid >> 6);
    const unsigned tbase = now(), cbase = (unsigned)__builtin_readcyclecounter();
    if ((ow | 4) != 63 && w) { prefetch(a, lds_base, ow & 3, (ow >> 2) * 3 + w - 1, (ow & 3) == 3 ? 42 : 48); VM_DRAIN(); }
    if (w >= 2 || (w == 1 && (ow & ~11))) return;
    const u32x2 tag = launch_tag(a);
    tell_side(rw, tag);
    if ((ow | 4) == 63) {
        auto rtt = [&](int o) {
            unsigned long long t0, t1;
            asm volatile("s_memtime %0\n\ts_waitcnt lgkmcnt(0)" : "=s"(t0) :: "memory");
            st32(SENT, rw, lane == 0 ? 0 : OOB, o);
            asm volatile("s_waitcnt vmcnt(0)\n\ts_memtime %0\n\ts_waitcnt lgkmcnt(0)" : "=s"(t1) :: "memory");
            return (unsigned)(t1 - t0);
        };
        auto die = [&](int o, int d = 8192) {
            unsigned t0 = ~0u, t1 = ~0u;
            for (int r = 0; r < 4; ++r) { t0 = min(t0, rtt(o + r * 128)); t1 = min(t1, rtt(o + d + r * 128)); }
            return (unsigned)(t1 < t0);
        };
        unsigned best[4] = {~0u, ~0u, ~0u, ~0u}, near = die(ow & 4 ? 0 : OFF_TP);
        if (ow & 4) {
            for (int r = 4; r < 4 + CORNER; ++r)
#pragma unroll
                for (int i = 0; i < 4; ++i) best[i] = min(best[i], rtt(near * 8192 + cmap(i * 4096) + r * 128));
            near += 2 * (best[0] + best[2] < best[1] + best[3]) + 4 * (best[0] + best[1] < best[2] + best[3]);
        } else near = 2 * near + 4 * die(OFF_TP + (2 << 20)) + (die(32768, 2048) ^ die_side());
        __builtin_amdgcn_raw_buffer_store_b64((u32x2){(tag.x & ~7u) | near, tag.y}, rw, lane == 0 ? F_INIT + (ow & 4 ? 32 : 161) * 128 : OOB, 0, 16);
    }
    const int sel = wait_init(rw, tag, budget, 32) ^ a.c1 >> 24;
    const rsrc_t rs = __builtin_amdgcn_make_buffer_rsrc(a.ws + (sel & 1) * 8192, 0, COMM_BYTES - 8192, 0x00020000);
    const int side = agree_side(rw, tag, ~tag, budget), cc = sel >> 1;
    const int G = (w ? 64 + 4 * side : 32 * side + (ow >> 1 & 24) + (ow & 4)) + ((cc ^ ow >> 1 ^ w) & 1) + ((cc ^ 2 * ow ^ ow >> 4) & 2);
    owner_loop(a, G, rs, rw, sel, tbase, cbase, budget);
}

template <int O, class V> DI void put8(V& d, u32x8 f) {
#pragma unroll
    for (int j = 0; j < 8; ++j) d[O + j] = f[j];
}
DI f32x4 SMF64(u32x4 a, u32x8 b, f32x4 c, int i) { asm volatile("s_nop 1\n\tv_smfmac_f32_16x16x64_bf16 %0, %1, %2, %3\n\ts_nop 7" : "+v"(c) : "v"(a), "v"(b), "v"(i)); return c; }
DI f32x4 SMF32(u32x2 a, u32x4 b, f32x4 c, int i) { asm volatile("s_nop 1\n\tv_smfmac_f32_16x16x32_bf16 %0, %1, %2, %3\n\ts_nop 7" : "+v"(c) : "v"(a), "v"(b), "v"(i)); return c; }
DI f32x4 MFV(u32x4 a, u32x4 b, f32x4 c) { asm volatile("s_nop 1\n\tv_mfma_f32_16x16x32_bf16 %0, %1, %2, %0\n\ts_nop 7" : "+v"(c) : "v"(a), "v"(b)); return c; }

DI void role_head(const Args& a, LAS char* lds, unsigned lds_base, int h, rsrc_t rw, int& budget) {
    const int tid = threadIdx.x, w = __builtin_amdgcn_readfirstlane(tid >> 6);
    char* ws = a.ws;
    const unsigned tbase = now(), cbase = (unsigned)__builtin_readcyclecounter();
    int tw = 0;
    u32x32 A0, A1, A2, A3, A4, A5, A6, A7, R0, R1, R2, R3; u32x8 R4;
    u32x2 tag;
    {
        const int lane = tid & 63, n = lane & 15, g = lane >> 4;
#pragma unroll
        for (int j = 0; j < 3; ++j) glds16(a.mem0, min(4 * j + w, 8) * 1024 + lane * 16, lds_base + L_KX + min(4 * j + w, 8) * 1024);
        const int tq = n >> 2, tc = (n & 3) >> 1, lo8 = (n & 1) * 8;
        const int r_nat = 16 * (g >> 1) + 4 * (g & 1) + (tq & 1) + 8 * (tq >> 1);
        const int r_sig = 16 * (tq & 1) + 4 * (g >> 1) + (g & 1) + 2 * (tq >> 1);
        const int r_o = 16 * (tq & 1) + 8 * (g >> 1) + 4 * (g & 1) + (tq >> 1);
#define WFRAG(sl, kb, t) ((kb) < 8 ? sm_frag(lds, sl, r_sig, 32, 8, 4 * w + 2 * (t) + tc, lo8) : sm_frag(lds, sl, r_nat, 2, 32, 4 * w + 2 * (t) + tc, lo8))
        const u32x4 ro = ring_offs(w, lane, HD);
        auto issue = [&](int i) {
            const unsigned slot = lds_base + L_W + (i % C_RING) * 16384 + w * 4096;
            if (i == 23) ring_issue(a.Wo + (size_t)h * DH * DM + 512, ring_offs(w, lane, [](int r, int c) { return (r + 64 * (c >> 3)) * (DM * 2) + (c & 7) * 16; }), slot);
            else if (i < 28) ring_issue((i < 8 || i == 20 ? a.Wq : i < 16 || i == 21 ? a.Wk : a.Wv) + (size_t)(64 * (i < 16 ? i & 7 : i < 20 ? i - 12 : i < 24 ? 8 : i - 24)) * HD + h * DH, ro, slot);
            else ring_issue(a.Wo + (size_t)(h * DH + 64 * ((i & 1) ^ ((i - 28) >> 1))) * DM + 256 * tw + 128 * (i & 1), ring_offs(w, lane, DM), slot);
        };
        auto keep = [&](int at, u32x8 f) {
            *(LAS u32x4*)(lds + at + lane * 16) = (u32x4){f[0], f[1], f[2], f[3]};
            *(LAS u32x4*)(lds + at + 1024 + lane * 16) = (u32x4){f[4], f[5], f[6], f[7]};
        };
        u32x8 hold[8] = {};
#pragma unroll
        for (int i = 0; i < C_RING; ++i) issue(i);
        tag = launch_tag(a);
        tell_side(rw, tag);
        u32x2 other;
        ring_wait<4 * C_RING + 1>();
        mem0_arrange(lds, L_KX, tid);
        static_for<9>([&](auto ic) {
            constexpr int G = decltype(ic)::value;
            ring_wait<(G == 2 || G == 5 || G == 6 ? 16 : G < 5 ? 17 : G == 7 ? 8 : 0)>();
            if constexpr (G == 5) { asm volatile("" : "+v"(other)); tw = agree_side(rw, tag, other, budget); }
            const int sl = L_W + (G & 1) * 65536;
            auto fr = [&](int j, int t) { return WFRAG(sl + ((j ^ w) << 14), 0, t); };
            if constexpr (G < 4) {
                u32x32& A = G == 0 ? A0 : G == 1 ? A2 : G == 2 ? A4 : A6;
                u32x32& B = G == 0 ? A1 : G == 1 ? A3 : G == 2 ? A5 : A7;
                put8<0>(A, fr(0, 0)); put8<8>(A, fr(0, 1)); put8<16>(A, fr(1, 0)); put8<24>(A, fr(1, 1));
                put8<0>(B, fr(2, 0)); put8<8>(B, fr(2, 1)); put8<16>(B, fr(3, 0)); put8<24>(B, fr(3, 1));
                if constexpr (G == 0) asm volatile("" : "+{a[0:31]}"(A0), "+{a[32:63]}"(A1));
                if constexpr (G == 1) asm volatile("" : "+{a[64:95]}"(A2), "+{a[96:127]}"(A3));
                if constexpr (G == 2) asm volatile("" : "+{a[128:159]}"(A4), "+{a[160:191]}"(A5));
                if constexpr (G == 3) asm volatile("" : "+{a[192:223]}"(A6), "+{a[224:255]}"(A7));
            } else if constexpr (G == 4) {
                put8<16>(R1, fr(0, 0)); put8<24>(R1, fr(0, 1)); put8<0>(R2, fr(1, 0)); put8<8>(R2, fr(1, 1));
                put8<16>(R2, fr(2, 0)); put8<24>(R2, fr(2, 1)); put8<0>(R3, fr(3, 0)); put8<8>(R3, fr(3, 1));
            } else if constexpr (G == 5) {
                put8<16>(R0, WFRAG(sl, 8, 0)); put8<24>(R0, WFRAG(sl, 8, 1)); put8<0>(R1, WFRAG(sl + 16384, 8, 0)); put8<8>(R1, WFRAG(sl + 16384, 8, 1));
                put8<16>(R3, WFRAG(sl + 32768, 8, 0)); put8<24>(R3, WFRAG(sl + 32768, 8, 1));
                hold[7] = sm_frag(lds, sl + 49152, r_o, 2, 32, 8 * (w >> 1) + 4 * tw + 2 * (w & 1) + tc, lo8);
            } else if constexpr (G == 6) {
#pragma unroll
                for (int j = 0; j < 7; ++j) hold[j] = fr(j >> 1, j & 1);
                R4 = fr(3, 1);
            } else {
                if constexpr (G == 8)
#pragma unroll
                    for (int T = 0; T < 4; ++T) keep(L_W + 65536 + w * 8192 + T * 2048, hold[T]);
#pragma unroll
                for (int T = 0; T < 4; ++T) hold[T] = sm_frag(lds, L_W + (2 * G - 10 + (w >> 1)) * 16384, r_o, 2, 32, 8 * (w & 1) + 2 * T + tc, lo8);
            }
            if constexpr (G != 7) WG_BARRIER();
            if constexpr (G < 6)
#pragma unroll
                for (int i = 0; i < 4; ++i) issue(4 * G + 8 + i);
            if constexpr (G == 2) asm volatile("buffer_load_dwordx2 %0, off, %1, %2 sc1" : "=v"(other) : "s"(rw), "s"(side_at(16)) : "memory");
            if constexpr (G == 6)
#pragma unroll
                for (int j = 0; j < 8; ++j) keep(L_W + (j >> 1) * 16384 + w * 4096 + (j & 1) * 2048, hold[j]);
            if constexpr (G == 8)
#pragma unroll
                for (int T = 0; T < 4; ++T) keep(L_W + 98304 + w * 8192 + T * 2048, hold[T]);
        });
        const int ai = 2 * (n >> 2) + (n & 1), ahf = (n >> 1) & 1;
        R0[0] = R0[1] = R0[2] = R0[3] = 0x3F803F80u;
        R0[4] = ahf ? 0xEEEE : 0x4444;
        R0[5] = (g >> 1) * 512 + (n >> 2) * 128 + (g & 1) * 64 + (n & 3) * 16;
        R0[6] = lds_base + L_MEMS + 8192 + g * 256 + ai * 32 + ahf * 16;
        R0[7] = lane * 16;
        R0[8] = lds_base + L_Q + 2 * g * 320 + (16 * w + n) * 4;
        R0[9] = lds_base + L_KO + 2 * g * 576 + (16 * w + (n & 8) + 4 * (n & 1) + ((n >> 1) & 3)) * 4;
        R0[10] = lds_base + L_Q + ai * 320 + g * 16;
        R0[11] = lds_base + L_KO + ai * 576 + g * 32 + ahf * 16;
        R0[12] = lds_base + L_KX + 8192 + w * 2048 + (g >> 1) * 256 + n * 16 + (g & 1) * 8;
        R0[13] = lds_base + L_W + w * 4096 + lane * 16;
        R0[14] = part_unit((2 * (4 * tw + w) + (n >> 3)) * 4 + g, h) + (n & 7) * 16;
        R0[15] = (g >> 1) == (w >> 1) ? part_unit(64 + 2 * (2 * tw + (w & 1)) + (g >> 1), h) + (g & 1) * 64 + n * 4 : OOB;
        asm volatile("" : "+{v[120:151]}"(R0), "+{v[152:183]}"(R1), "+{v[184:215]}"(R2), "+{v[216:247]}"(R3), "+{v[248:255]}"(R4));
    }
    TT(true, 7)
    const bool gv_on = tw == 0; (void)gv_on;
    const int sel = wait_init(rw, tag, budget, 33 + 4 * h + w) ^ a.c1 >> 24;
    R0[14] ^= (sel & 8) * 256; R0[15] ^= (sel & 8) * 256;
    TT(true, 8)
    int kv_rdy = 0;
    const rsrc_t rs = __builtin_amdgcn_make_buffer_rsrc(ws + (sel & 1) * 8192, 0, COMM_BYTES - 8192, 0x00020000);
    auto dma = [&](int voff, int soff, unsigned dst) { bdma(rw, voff, soff, dst); };
    auto fetch_kv = [&](int s, int lane) {
        const int n = lane & 15, g = lane >> 4;
        const int kxs = (g >> 1) * 1024 + ((g & 1) * 32 + 16 * (n >> 3) + (n & 7)) * 16;
        const int vxs = (g >> 1) * 1024 + ((g & 1) * 32 + n) * 16;
        const int so_ = (s * 32 + h) * 8192 + w * 2048;
        const unsigned xdst = lds_base + L_KX + w * 2048;
        dma(kxs, OFF_KF + so_, xdst); dma(kxs, OFF_KF + so_ + 128, xdst + 1024);
        dma(vxs, OFF_VF + so_, xdst + 8192); dma(vxs, OFF_VF + so_ + 256, xdst + 8192 + 1024);
    };
    const int fo = F_KVM + h * 256 + tw * 128;
    u32x8 scon;
    scon[0] = w; scon[1] = 2 * OFF_CAN + (h >> 1) * 512 + (2 * (h & 1) + tw) * 16; scon[2] = F_KV + h * 512 + w * 128; scon[3] = 65536 + w * 4096;
    scon[4] = OFF_KF + h * 8192 + w * 2048; scon[5] = fo; scon[6] = tw - 1; scon[7] = (tw ? OFF_VM : OFF_KM) + (h - NH) * 2048 + w * 512;
    u32x8 S1 = {0u, 0u, (unsigned)kv_rdy, (unsigned)budget, (unsigned)a.d0, 0xFFFFFFFFu / 22, (unsigned)(a.c2 + a.b0), 0u};
    u32x8 S2 = {0u, 0u, 0u, (unsigned)a.up, (unsigned)a.dn, (unsigned)a.c2, 0u, 1u};
    fetch_kv(0, opaque_lane());
    __builtin_amdgcn_raw_buffer_store_b64((u32x2){8 * now() + a.e0, 1u | (a.c2 + a.b0) << 16}, rs, tid < NCP ? tid * 128 : OOB, scon[1], 16);
    asm volatile("s_waitcnt vmcnt(1)" ::: "memory");
    TT(true, 9)
    u32x2 vm = {};
    u32x8 S3;
    {
        const unsigned t = now(), c = (unsigned)__builtin_readcyclecounter();
        const float r = (float)(c - cbase) * __builtin_amdgcn_rcpf(8.0f * (float)(t - tbase));
        S2[1] = 8 * t + 16; S1[5] = (unsigned)(4294967296.0f * __builtin_amdgcn_rcpf(r));
        S3 = (u32x8){c, (unsigned)(4096.0f * r), 2u * OFF_MEMB + ((h >> 3 ^ sel >> 2) & 1) * 16384 + (w & 2) * 2048 + (~sel & 8) * 256 + (w & 1) * 1024, w == 3 ? ~0u : 0u};
    }
    const int U0 = (2 * tw + (w >> 1)) * 64 + 8 * (w & 1), U1 = 4 * (2 * h + tw) + w;
    for (;;) {
        asm volatile(STEP_ASM
            : "+{v[96:97]}"(vm), "+{v[0:7]}"(S1), "+{v[8:15]}"(S2), "+{v[16:23]}"(S3), "+{v[24:31]}"(scon)
              , "+{a[0:31]}"(A0), "+{a[32:63]}"(A1), "+{a[64:95]}"(A2), "+{a[96:127]}"(A3), "+{a[128:159]}"(A4), "+{a[160:191]}"(A5), "+{a[192:223]}"(A6), "+{v[120:151]}"(R0), "+{v[152:183]}"(R1)
            : "{a[224:255]}"(A7), "{v[184:215]}"(R2), "{v[216:247]}"(R3), "{v[248:255]}"(R4),
              [rs] "s"(rs), [rw] "s"(rw), [qb] "s"(OFF_OF + LATE * 262144 + h * 8192 + (w & 1) * 256),
              [tp] "s"(tp_unit(U0, sel) + h * 128), [tq] "s"(tp_unit(256 + U0, sel) + h * 128), [tx] "s"(tp_unit(512 + (U0 >> 3) + 3 * (w & 1), sel) + h * 128),
              [u] "s"(U1), [ua] "s"(tp_unit(256 + U1, sel)), [fw] "s"(8 * (a.xb >> 16 & 255)), [ru] "s"(__builtin_amdgcn_make_buffer_rsrc(a.out, 0, SEQ * DM * 2, 0x00020000))
            : STEP_CLOBBER);
        const int s = __builtin_amdgcn_readfirstlane(S1[0]);
        const unsigned st = __builtin_amdgcn_readfirstlane(S1[7]);
        kv_rdy = __builtin_amdgcn_readfirstlane(S1[2]); budget = __builtin_amdgcn_readfirstlane(S1[3]);
        if (s == NSEG - 1) break;
        if (__builtin_expect(st & 2, 0)) {
            const int lane = opaque_lane();
            LAS char* z = (LAS char*)0;
            const f32x4 zero = {0, 0, 0, 0};
            const u32x4 ones = __builtin_shufflevector(R0, R0, 0, 1, 2, 3);
            const unsigned zr = lane >> 6;
            const int idx = R0[4];
            u32x4 qf[4];
            f32x4 s1 = zero, s2 = zero;
#pragma unroll
            for (int ks = 0; ks < 4; ++ks) {
                qf[ks] = *(LAS u32x4*)(z + R0[10] + ks * 64);
                s1 = MFV(*(LAS u32x4*)(lds + L_KX + lane * 16 + ks * 2048), qf[ks], s1);
                s2 = MFV(*(LAS u32x4*)(lds + L_KX + lane * 16 + ks * 2048 + 1024), qf[ks], s2);
            }
            f32x4 s0 = SMF64(*(LAS u32x4*)(z + R0[11]), __builtin_shufflevector(qf[0], qf[1], 0, 1, 2, 3, 4, 5, 6, 7), zero, idx);
            s0 = SMF64(*(LAS u32x4*)(z + R0[11] + 128), __builtin_shufflevector(qf[2], qf[3], 0, 1, 2, 3, 4, 5, 6, 7), s0, idx);
            const float sa = s0[0] + s0[2], sb = s0[1] + s0[3];
            float mx = fmaxf(fmaxf(fmaxf(fmaxf(s1[0], s1[1]), fmaxf(s1[2], s1[3])), fmaxf(fmaxf(s2[0], s2[1]), fmaxf(s2[2], s2[3]))), fmaxf(sa, sb));
            mx = fmaxf(mx, __shfl_xor(mx, 16)); mx = fmaxf(mx, __shfl_xor(mx, 32));
            auto ex2 = [&](float x, float y) { return pack2(__builtin_amdgcn_exp2f(x - mx), __builtin_amdgcn_exp2f(y - mx)); };
            const u32x4 px = {ex2(s1[0], s1[1]), ex2(s1[2], s1[3]), ex2(s2[0], s2[1]), ex2(s2[2], s2[3])};
            const unsigned pm = ex2(sa, sb);
            const f32x4 l = MFV(((u32x4){pm, zr, zr, zr}), ones, MFV(px, ones, zero));
            const u32x2 x0 = *(LAS u32x2*)(z + R0[12]), y0 = *(LAS u32x2*)(z + R0[12] + 1024), x1 = *(LAS u32x2*)(z + R0[12] + 512), y1 = *(LAS u32x2*)(z + R0[12] + 1536);
            const f32x4 o = SMF64(px, ((u32x8){x0.x, y0.x, x0.y, y0.y, x1.x, y1.x, x1.y, y1.y}), zero, idx) + SMF32(((u32x2){pm, zr}), ((u32x4){vm.x, vm.y, zr, zr}), zero, idx);
            const float i0 = __builtin_amdgcn_rcpf(l[0]), i1 = __builtin_amdgcn_rcpf(l[1]);
            *(LAS unsigned*)(z + R0[9] + 256) = pack2(o[0] * i0, o[2] * i0);
            *(LAS unsigned*)(z + R0[9] + 256 + 576) = pack2(o[1] * i1, o[3] * i1);
            WG_BARRIER();
            const u32x4 oa = *(LAS u32x4*)(z + R0[11] + 256 + (w >> 1) * 128), ob = *(LAS u32x4*)(z + R0[11] + 384 - (w >> 1) * 128);
            auto frag = [&](int at) { return __builtin_shufflevector(*(LAS u32x4*)(z + at), *(LAS u32x4*)(z + at + 1024), 0, 1, 2, 3, 4, 5, 6, 7); };
            f32x2 f[4];
#pragma unroll
            for (int T = 0; T < 4; ++T) f[T] = fold(SMF64(ob, frag(R0[13] + scon[3] + 32768 + T * 2048), SMF64(oa, frag(R0[13] + scon[3] + T * 2048), zero, idx), idx));
            const f32x2 fx = fold(SMF64(ob, frag((R0[13] ^ 8192) + 49152 + 2048), SMF64(oa, frag(R0[13] + 49152 + 2048), zero, idx), idx));
            const int pb = (s & 1) * 2 * PART_STEP;
            st128((u32x4){pack2(f[0].x, f[1].x), pack2(f[2].x, f[3].x), pack2(f[0].y, f[1].y), pack2(f[2].y, f[3].y)}, rs, R0[14], pb);
            st32(pack2(fx.x, fx.y), rs, R0[15], pb);
            if (s == LATE) { S1[7] = 4; continue; }
        }
        if (__builtin_expect((st & 2) || kv_rdy <= s, 0)) {
            while (kv_rdy <= s) { kv_rdy = prog_min(rw, F_KV + h * 512 + w * 128); if (--budget < 0) break; }
            fetch_kv(s + 1, opaque_lane());
        }
        if (st & 2) { S1[1] = read_canaries(s, rs, opaque_lane(), budget) + S1[6]; S2[2] += 0x10000; }
        if ((int)(S1[1] - S2[1]) > 0x8000) { S2[1] = 8 * now() + 16; S3[0] = (unsigned)__builtin_readcyclecounter(); }
        S1[0] = s + 1; S1[2] = kv_rdy; S1[3] = budget;
    }
    TT(true, 0)
    st32((unsigned)(NSEG - 2), rw, opaque_lane() == 0 && w == 0 ? fo : OOB, 0);
    TT(true, 2)
}

__global__ void __launch_bounds__(256, 1) fused_kernel(Args a) {
    extern __shared__ __attribute__((aligned(16))) char smem[];
    LAS char* lds = (LAS char*)smem;
    const unsigned lds_base = (unsigned)(uintptr_t)lds;
    const int w = __builtin_amdgcn_readfirstlane(threadIdx.x >> 6);
    const int b = blockIdx.x, bx = b & 7, bi = b >> 3;
    if (bi >= 28) {
        const void* pc; unsigned len;
        asm volatile("s_getpc_b64 %0\n1:\ts_mov_b32 %1, .Lstep_end-1b" : "=s"(pc), "=s"(len));
        touch(pc, min((bi - 28) * 32768 + threadIdx.x * 128, len), lds_base);
    }
    asm volatile("" :: "s"(a.Wq), "s"(a.cw));
    const rsrc_t rw = __builtin_amdgcn_make_buffer_rsrc(a.ws, 0, WS_USED, 0x00020000);
    int budget = SPIN_MAX;
#define RT(i)
    if (bx < 4) {
        if (bi < 16) role_head(a, lds, lds_base, 8 * bx + (bi >> 2) * 2 + (bi & 1), rw, budget);
        else role_owner(a, lds_base, 4 * (bi - 16) + bx, rw, budget);
    } else {
        const int fx = bx - 4, no = fx < 3 ? 14 : 22, j = 14 * fx + bi;
        if (bi >= no || (j & 2)) {
            const unsigned t = now() + (a.d1 & 0xffff);
            if (bi >= no) { if (w) prefetch(a, lds_base, bx, (bi - no) * 3 + w - 1, 3 * (32 - no)); }
            else {
                const int sh = (j >> 2) * 2 + (j & 1);
#pragma unroll
                for (int i = 0; i < INIT_SHARE / 4096; ++i) st128((u32x4){SENT, SENT, SENT, SENT}, rw, threadIdx.x * 16 + i * 4096, sh * INIT_SHARE);
#pragma unroll
                for (int i = 0; i < 8; ++i) st128((u32x4){SENT, SENT, SENT, SENT}, rw, threadIdx.x * 16, i < 4 ? (i & 2 ? OFF_VM : OFF_KM) + FAST * 65536 + (2 * sh + (i & 1)) * 4096 : OFF_OF + LATE * 262144 + (4 * sh + i - 4) * 4096);
                const u32x2 tag = launch_tag(a);
                VM_DRAIN();
                WG_BARRIER();
                __builtin_amdgcn_raw_buffer_store_b64(tag, rw, threadIdx.x == 0 ? F_INIT + sh * 128 : OOB, 0, 16);
                RT(3)
            }
            (void)wait_until(t);
        }
        if (bi >= no) {
            const int t = 18 * fx + bi - no;
            role_s4d(a, lds, lds_base, t, rw, budget);
            RT(1)
            TT(true, 6)
            if (t < 9 * OUT_REP) role_outp(a, lds, lds_base, t % 9, t / 9, rw, budget);
        }
        else {
            if (!(j & 2)) role_kv(a, lds, lds_base, (j >> 2) * 2 + (j & 1), rw, budget);
            RT(1)
            TT(true, 6)
            role_qa(a, lds, lds_base, (j >> 2) * 2, (j & 2 ? 0 : FAST + 2) + (j & 1), (j & 2 ? FAST : FAST + 2) + (j & 1), rw, budget);
        }
    }
    RT(2)
    TT(true, 5)
    if (budget < 0 && threadIdx.x == 0) st32(0x1000u + b, rw, F_ERR, 0);
}

static int g_c1 = 34, g_c1m = 30, g_c1x = 46, g_c2 = 316, g_b0 = 32, g_up = 8, g_fl = 8, g_dn = 2, g_d0 = 1680, g_da = 100, g_ua = 48, g_d1 = 300, g_rd = 34, g_cw = 69, g_e0 = 190, g_x1 = 40, g_x2 = 140, g_x3 = 150, g_x4 = 75, g_x5 = 135, g_x6 = 157, g_x7 = 35, g_x8 = 210, g_pc = 45, g_cc = 0;
extern "C" void kernel_launch(void* const* d_in, const int* in_sizes, int n_in, void* d_out, int out_size, void* d_ws, size_t ws_size, hipStream_t stream) {
    Args a;
    a.x = (const u16*)d_in[0]; a.mem0 = (const u16*)d_in[1];
    a.Wq = (const u16*)d_in[2]; a.Wk = (const u16*)d_in[3]; a.Wv = (const u16*)d_in[4]; a.Wo = (const u16*)d_in[5];
    a.lAr = (const u16*)d_in[8]; a.Aim = (const u16*)d_in[9]; a.Cr = (const u16*)d_in[10]; a.Ci = (const u16*)d_in[11];
    a.ldt = (const u16*)d_in[12]; a.Dsk = (const u16*)d_in[13]; a.gw = (const u16*)d_in[14];
    a.out = (u16*)d_out;
    a.ws = (char*)(((uintptr_t)d_ws + ((2u << 20) - 1)) & ~(uintptr_t)((2u << 20) - 1));
    a.c1 = g_c1 | g_c1m << 8 | g_c1x << 16 | g_cc << 24; a.c2 = g_c2; a.d0 = g_d0 + g_da; a.b0 = g_b0; a.d1 = g_d1 | g_pc << 16; a.up = g_up | g_ua << 8 | g_fl << 16; a.dn = g_dn | g_d0 << 8; a.rd = 23 * g_rd - 300; a.cw = 8 * g_cw - 104; a.e0 = 8 * g_e0;
    a.xa = g_x1 | g_x2 << 8 | g_x5 << 16 | g_x7 << 24; a.xb = g_x6 | g_x3 << 8 | g_x4 << 16 | g_x8 << 24;
    (void)hipFuncSetAttribute((const void*)fused_kernel, hipFuncAttributeMaxDynamicSharedMemorySize, L_TOTAL);
    fused_kernel<<<256, 256, L_TOTAL, stream>>>(a);
}
```

```cpp
#include <hip/hip_runtime.h>
#include <hip/hip_bf16.h>
#include <stdint.h>

typedef unsigned short u16;
typedef __bf16 bf16x8 __attribute__((ext_vector_type(8)));
typedef __bf16 bf16x16 __attribute__((ext_vector_type(16)));
typedef _Float16 f16x2 __attribute__((ext_vector_type(2)));
typedef _Float16 f16x8 __attribute__((ext_vector_type(8)));
typedef short s16x4 __attribute__((ext_vector_type(4)));
typedef float f32x2 __attribute__((ext_vector_type(2)));
typedef float f32x4 __attribute__((ext_vector_type(4)));
typedef float f32x16 __attribute__((ext_vector_type(16)));
typedef unsigned u32x2 __attribute__((ext_vector_type(2)));
typedef unsigned u32x4 __attribute__((ext_vector_type(4)));
typedef unsigned u32x8 __attribute__((ext_vector_type(8)));
typedef unsigned u32x16 __attribute__((ext_vector_type(16)));
typedef unsigned u32x32 __attribute__((ext_vector_type(32)));
typedef short s16x8 __attribute__((ext_vector_type(8)));
#define LAS __attribute__((address_space(3)))
#define DI __device__ __forceinline__
typedef __amdgpu_buffer_rsrc_t rsrc_t;

constexpr int SEQ = 2048, DM = 576, NH = 32, DH = 128, HD = NH * DH, NM = 8, SEG = 32, NSEG = 64, NS = 128;
constexpr unsigned SENT = 0xFFFFFFFFu;
constexpr unsigned OOB = 0x7FFFFFF0u;
constexpr int SPIN_MAX = 1 << 20;

constexpr int PART_STEP = 16384;
constexpr int OFF_MEMB = 40 * PART_STEP;
constexpr int MEMB_STRIDE = 32768;
constexpr int OFF_CAN = 52 * PART_STEP;
constexpr int COMM_LOGICAL = OFF_CAN + 32768;
constexpr int COMM_BYTES = 2 * COMM_LOGICAL;
constexpr int F_ERR = COMM_BYTES;
constexpr int F_KV = F_ERR + 128;
constexpr int F_KVM = F_KV + 16384;
constexpr int F_O = F_KVM + 8192;
constexpr int F_H = F_O + 4096;
constexpr int F_S4 = F_H + 32768;
constexpr int F_DONE = F_S4 + 512;
constexpr int INIT_BYTES = 2 * OFF_CAN + 131072;
constexpr int F_INIT = INIT_BYTES;
constexpr int F_CL = F_INIT + 24576;
constexpr int F_EPOCH = F_CL + 16384;
static_assert(F_DONE + 128 <= INIT_BYTES, "");
static_assert(F_EPOCH + 128 <= (2 << 20), "hand-off region");
constexpr int OFF_TP = 2 << 20;
constexpr int OFF_KF = 6 << 20;
constexpr int OFF_VF = OFF_KF + SEQ * HD * 2;
constexpr int OFF_OF = OFF_VF + SEQ * HD * 2;
constexpr int OFF_KM = OFF_OF + SEQ * HD * 2;
constexpr int OFF_VM = OFF_KM + NSEG * NH * NM * DH * 2;
constexpr int FAST = NSEG - 6;
constexpr int LATE = NSEG - 2;
constexpr int OFF_BO = OFF_VM + NSEG * NH * NM * DH * 2;
constexpr int BO_ROW = 4352;
constexpr int OFF_GT = OFF_BO + DM * BO_ROW;
constexpr int OFF_DBG = OFF_GT + SEQ * 4;
constexpr int WS_USED = OFF_DBG + (4 << 20);

struct Args {
    const u16 *x, *mem0, *Wq, *Wk, *Wv, *Wo, *lAr, *Aim, *Cr, *Ci, *ldt, *Dsk, *gw;
    u16* out; char* ws;
    int c1, c2, d0, b0, d1, up, dn, rd, cw, e0;
    unsigned xa, xb;
};

DI float bf2f(u16 b) { return __uint_as_float((unsigned)b << 16); }
DI float bflo(unsigned w) { return __uint_as_float(w << 16); }
DI float bfhi(unsigned w) { return __uint_as_float(w & 0xFFFF0000u); }
DI u16 f2bf(float f) { return __builtin_bit_cast(u16, (__bf16)f); }
DI unsigned pack2(float lo, float hi) {
    typedef __bf16 bf16x2 __attribute__((ext_vector_type(2)));
    bf16x2 v = {(__bf16)lo, (__bf16)hi};
    return __builtin_bit_cast(unsigned, v);
}
#define WG_BARRIER() asm volatile("s_waitcnt lgkmcnt(0)\n\ts_barrier" ::: "memory")
#define VM_DRAIN() asm volatile("s_waitcnt vmcnt(0)" ::: "memory")
DI int opaque_lane() { int l; asm volatile("v_mbcnt_lo_u32_b32 %0, -1, 0\n\tv_mbcnt_hi_u32_b32 %0, -1, %0" : "=v"(l)); return l; }
DI int cmap(int o) { return (o & 0x1FFF) | ((o >> 13) << 14); }
DI int part_unit(int G, int h) { const int p = G < 64 ? G & 31 : 32 + (G & 3); return (p >> 2) * 131072 + cmap((p & 3) * 4096) + ((G >> (G < 64 ? 5 : 2) ^ p >> 3) & 1) * 2048 + (h >> 4) * 65536 + (h & 15) * 128; }
DI unsigned mx4(unsigned a, u32x4 v) { a = max(max(a, v.x), v.y); return max(max(a, v.z), v.w); }
DI unsigned now() { return (unsigned)__builtin_amdgcn_s_memrealtime(); }
DI unsigned wait_until(unsigned t) {
    unsigned v; int guard = 8192;
    do { v = now(); } while ((int)(t - v) >= 4 && --guard > 0);
    const int r = __builtin_amdgcn_readfirstlane((int)(t - v));
    asm volatile("s_cmp_lt_i32 %0, 1\n\ts_cbranch_scc1 1f\n\ts_nop 3\n\ts_cmp_lt_i32 %0, 2\n\ts_cbranch_scc1 1f\n\ts_nop 3\n\ts_cmp_lt_i32 %0, 3\n\ts_cbranch_scc1 1f\n\ts_nop 7\n1:" :: "s"(r) : "scc");
    return r > 0 ? t : v + 2;
}
DI void st128(u32x4 v, rsrc_t rs, int off, int soff) {
    __builtin_amdgcn_raw_buffer_store_b128(v, rs, off, soff, 16);
    asm volatile("s_nop 1" :: "v"(v) : "memory");
}
DI void st32(unsigned v, rsrc_t rs, int off, int soff) { __builtin_amdgcn_raw_buffer_store_b32(v, rs, off, soff, 16); }
DI u32x4 ld128(rsrc_t rs, int off, int soff) { return __builtin_amdgcn_raw_buffer_load_b128(rs, off, soff, 16); }
DI u32x2 ld64(rsrc_t rs, int off, int soff) { return __builtin_amdgcn_raw_buffer_load_b64(rs, off, soff, 16); }
DI unsigned ld32(rsrc_t rs, int off, int soff) { return __builtin_amdgcn_raw_buffer_load_b32(rs, off, soff, 16); }
DI f32x2 fold(f32x4 a) { return (f32x2){a[0], a[1]} + (f32x2){a[2], a[3]}; }
DI float dpp_xor1(float v) { return __int_as_float(__builtin_amdgcn_update_dpp(0, __float_as_int(v), 0xB1, 0xf, 0xf, true)); }
DI float red_sum_g(float v) {
    auto a = __builtin_amdgcn_permlane16_swap(__float_as_uint(v), __float_as_uint(v), false, false);
    v = __uint_as_float(a[0]) + __uint_as_float(a[1]);
    auto b = __builtin_amdgcn_permlane32_swap(__float_as_uint(v), __float_as_uint(v), false, false);
    return __uint_as_float(b[0]) + __uint_as_float(b[1]);
}
DI int wave_max32(int d) {
    d = max(d, __builtin_amdgcn_update_dpp(d, d, 0x121, 0xf, 0xf, false));
    d = max(d, __builtin_amdgcn_update_dpp(d, d, 0x122, 0xf, 0xf, false));
    d = max(d, __builtin_amdgcn_update_dpp(d, d, 0x124, 0xf, 0xf, false));
    d = max(d, __builtin_amdgcn_update_dpp(d, d, 0x128, 0xf, 0xf, false));
    return __builtin_amdgcn_readfirstlane(max(max(__builtin_amdgcn_readlane(d, 0), __builtin_amdgcn_readlane(d, 16)), max(__builtin_amdgcn_readlane(d, 32), __builtin_amdgcn_readlane(d, 48))));
}
DI u32x2 tr_read(LAS char* lds, int addr) { return __builtin_bit_cast(u32x2, __builtin_amdgcn_ds_read_tr16_b64_v4i16((LAS s16x4*)(lds + addr))); }
DI void wait_prog(rsrc_t rw, int off, bool act, int thr, int& budget) {
    for (;;) {
        const int v = (int)ld32(rw, off, 0);
        asm volatile("" ::: "memory");
        if (__all(!act || v >= thr)) break;
        if (--budget < 0) break;
        __builtin_amdgcn_s_sleep(1);
    }
}
DI u32x2 launch_tag(const Args& a) {
    unsigned long long z;
    asm volatile("s_load_dwordx2 %0, %1, 0x0\n\ts_waitcnt lgkmcnt(0)" : "=s"(z) : "s"(a.ws + F_EPOCH) : "memory");
    z += 0x9E3779B97F4A7C15ull;
    z = (z ^ (z >> 30)) * 0xBF58476D1CE4E5B9ull; z = (z ^ (z >> 27)) * 0x94D049BB133111EBull; z ^= z >> 31;
    return (u32x2){(unsigned)z, (unsigned)(z >> 32)};
}
DI int init_at(int more) { const int l = threadIdx.x & 63; return F_INIT + (l < 32 ? l : l < 56 || !more ? more : l + 105) * 128; }
DI bool init_ok(u32x2 v, u32x2 tag) { return __all(((v.x ^ tag.x) >> 3) == 0 && v.y == tag.y); }
DI int init_sel(u32x2 v) { const int m = __ballot(v.x & 1) >> 56; return (m & 1) + 2 * (m >> (4 + 2 * (m & 1)) & 3) + 4 * (m & 14); }
DI int wait_init(rsrc_t rw, u32x2 tag, int& budget, int more = 0) {
    u32x2 v;
    for (;;) {
        v = ld64(rw, init_at(more), 0);
        asm volatile("" ::: "memory");
        if (init_ok(v, tag) || --budget < 0) break;
        __builtin_amdgcn_s_sleep(2);
    }
    return init_sel(v);
}
DI unsigned die_side() { return (__builtin_amdgcn_s_getreg(4 | 14 << 6) ^ blockIdx.x >> 1) & 1; }
DI int side_at(int x) { const int b = blockIdx.x ^ x; return F_CL + ((b >> 3) * 4 + (b & 3)) * 128; }
DI void tell_side(rsrc_t rw, u32x2 tag) { __builtin_amdgcn_raw_buffer_store_b64((u32x2){(tag.x & ~1u) | die_side(), tag.y}, rw, threadIdx.x == 0 ? 0 : OOB, side_at(0), 16); }
DI int agree_side(rsrc_t rw, u32x2 tag, u32x2 v, int& budget) {
    while (!__all(((v.x ^ tag.x) >> 1) == 0 && v.y == tag.y) && --budget >= 0) { v = ld64(rw, 0, side_at(16)); asm volatile("" ::: "memory"); }
    const unsigned me = die_side();
    return (__builtin_amdgcn_readfirstlane(v.x) ^ me) & 1 ? me : blockIdx.x >> 4 & 1;
}
DI void glds16(const void* base, int voff, unsigned lds_dst) {
    unsigned keep;
    asm volatile("s_mov_b32 %0, m0\n\ts_mov_b32 m0, %3\n\ts_nop 0\n\tglobal_load_lds_dwordx4 %1, %2\n\ts_mov_b32 m0, %0" : "=&s"(keep) : "v"(voff), "s"(base), "s"(lds_dst) : "memory");
}
DI void bdma(rsrc_t rs, int voff, int soff, unsigned lds_dst) {
    unsigned keep;
    asm volatile("s_mov_b32 %0, m0\n\ts_mov_b32 m0, %4\n\ts_nop 0\n\tbuffer_load_dwordx4 %1, %2, %3 offen sc1 lds\n\ts_mov_b32 m0, %0" : "=&s"(keep) : "v"(voff), "s"(rs), "s"(soff), "s"(lds_dst) : "memory");
}
DI int swz(int r) { return ((r & 3) ^ (((r >> 2) & 3) << 1) ^ (((r >> 4) & 1) << 2)) & 7; }
template <class F> DI u32x4 ring_offs(int w, int lane, F at) {
    u32x4 o;
#pragma unroll
    for (int j = 0; j < 4; ++j) { const int r = 16 * w + 4 * j + (lane >> 4); o[j] = at(r, (lane & 15) ^ (2 * swz(r))) - 1024 * j; }
    return o;
}
DI u32x4 ring_offs(int w, int lane, int ld) { return ring_offs(w, lane, [&](int r, int c) { return r * ld * 2 + c * 16; }); }
DI void ring_issue(const u16* src, u32x4 o, unsigned slot) {
    unsigned keep;
    asm volatile("s_mov_b32 %0, m0\n\ts_mov_b32 m0, %6\n\ts_nop 0\n\tglobal_load_lds_dwordx4 %1, %5\n\tglobal_load_lds_dwordx4 %2, %5 offset:1024\n\t"
                 "global_load_lds_dwordx4 %3, %5 offset:2048\n\tglobal_load_lds_dwordx4 %4, %5 offset:3072\n\ts_mov_b32 m0, %0"
                 : "=&s"(keep) : "v"(o.x), "v"(o.y), "v"(o.z), "v"(o.w), "s"(src), "s"(slot) : "memory");
}
#define PF_FIRST 2
constexpr int L_TOTAL = 163840, L_SINK = L_TOTAL - 256;
DI void touch(const void* base, int voff, unsigned lds_base) {
    unsigned keep;
    asm volatile("s_mov_b32 %0, m0\n\ts_mov_b32 m0, %3\n\ts_nop 0\n\tglobal_load_lds_dword %1, %2\n\ts_mov_b32 m0, %0" : "=&s"(keep) : "v"(voff), "s"(base), "s"(lds_base + L_SINK) : "memory");
}
DI void prefetch(const Args& a, unsigned lds_base, int x, int id, int n) {
    const int lane = threadIdx.x & 63;
    const int h0 = x < 4 ? 8 * x : (0x160E0800 >> (8 * (x - 4))) & 255, lpr = x == 5 ? 12 : x == 7 ? 20 : 16, per = 9 * lpr;
    const int first = PF_FIRST * lpr, end = (x < 4 ? 4 : 2) * per;
    const float rl = __builtin_amdgcn_rcpf((float)lpr);
    if (x >= 4 && id < 18) touch(a.x, (id * 64 + lane) * 128, lds_base);
    for (int i = first + id; i < end; i += n) {
        int m = (i >= per) + (i >= 2 * per) + (i >= 3 * per), j = i - m * per;
        if (x < 4 && m < 3) { const int b = i >> 4; m = b < 20 ? b >> 3 : min(b - 20, 2); j = (b < 16 ? b & 7 : b < 20 ? b - 12 : b < 23 ? 8 : b - 23) * 16 + (i & 15); }
        const int mm = m + (x >= 4);
        const int L = j * 64 + lane, row = (int)((L + 0.5f) * rl);
        touch(mm == 0 ? a.Wq : mm == 1 ? a.Wk : mm == 2 ? a.Wv : a.Wo, m == 3 ? h0 * (DH * DM * 2) + L * 128 : row * (HD * 2) + (L - row * lpr) * 128 + h0 * 256, lds_base);
    }
}
template <int N> DI void ring_wait() { asm volatile("s_waitcnt vmcnt(%0) lgkmcnt(0)\n\ts_barrier" :: "i"(N) : "memory"); }
template <int I> struct ic_t { static constexpr int value = I; };
template <int N, int I = 0, class F> DI void static_for(F&& f) { if constexpr (I < N) { f(ic_t<I>{}); static_for<N, I + 1>(f); } }
#define MFMA32(a, b, c) __builtin_amdgcn_mfma_f32_32x32x16_bf16(__builtin_bit_cast(bf16x8, a), __builtin_bit_cast(bf16x8, b), c, 0, 0, 0)
#define MFMA16(a, b, c) __builtin_amdgcn_mfma_f32_16x16x32_bf16(__builtin_bit_cast(bf16x8, a), __builtin_bit_cast(bf16x8, b), c, 0, 0, 0)

constexpr int S_CH = 9;
constexpr int S_XRS = 4096 + 32;
constexpr int S_XR = 0;
constexpr int S_RC = S_CH * S_XRS;
constexpr int S_RCS = 4192;
constexpr int S_KD = S_RC + 4 * S_RCS;
constexpr int S_K = S_KD + 48 * 128;
constexpr int S_KROW = 96;
constexpr int S_TAB = S_K + 79 * S_KROW;
constexpr int S_TROW = 544;
constexpr int S_RED = S_TAB + 57 * S_TROW;
constexpr int S_TOTAL = S_RED + 4 * 64 * 144;
static_assert(S_RC % 16 == 0 && S_TAB % 16 == 0 && S_RED % 16 == 0, "");
DI f32x2 crot(f32x2 z) { return (f32x2){-z.y, z.x}; }
DI f32x2 cmul(f32x2 a, f32x2 z) { return __builtin_elementwise_fma(a.yy, crot(z), a.xx * z); }
DI f32x2 cexp_p(float ar, float ai, float p) {
    const float x = ai * p;
    const float hi = x * 0.15915494309189535f;
    const float lo = __builtin_fmaf(x, 0.15915494309189535f, -hi) + x * 6.420638316725915e-9f;
    const float rev = (hi - __builtin_rintf(hi)) + lo;
    const float m = __builtin_amdgcn_exp2f(ar * p * 1.4426950408889634f);
    return (f32x2){m * __builtin_amdgcn_cosf(rev), m * __builtin_amdgcn_sinf(rev)};
}
#define FENCE() __builtin_amdgcn_sched_barrier(0)
#define TT(c, i)
DI void role_s4d(const Args& a, LAS char* lds, unsigned lds_base, int task, rsrc_t rw, int& budget) {
    const int tid = threadIdx.x, lane = tid & 63, w = __builtin_amdgcn_readfirstlane(tid >> 6);
    const int d0 = task * S_CH;
    touch(w == 0 ? a.lAr : w == 1 ? a.Aim : w == 2 ? a.Cr : a.Ci, min(d0 * (NS * 2) + min(lane, 2 * S_CH + 1) * 128, DM * NS * 2 - 4), lds_base);
    touch(w & 1 ? a.ldt : a.Dsk, min(lane * 128, DM * 2 - 4), lds_base);
#define S4S(i)
    {
        const rsrc_t rx = __builtin_amdgcn_make_buffer_rsrc((void*)a.x, 0, SEQ * DM * 2, 0x00020000);
        const unsigned sh = (d0 & 1) * 16;
        u32x4 v[8]; unsigned e[8];
#pragma unroll
        for (int i = 0; i < 8; ++i) {
            const int o = (tid + 256 * i) * (DM * 2) + (d0 & ~1) * 2;
            v[i] = __builtin_amdgcn_raw_buffer_load_b128(rx, o, 0, 0); e[i] = __builtin_amdgcn_raw_buffer_load_b32(rx, o, 16, 0);
        }
#pragma unroll
        for (int p = 0; p < 2; ++p) {
            const int tok = 32 * task + 8 * w + 4 * p + (lane >> 4);
            float acc = 0.f;
#pragma unroll
            for (int i = 0; i < 5; ++i) {
                const int ch = (lane & 15) + 16 * i;
                const u32x4 gv = ch < 72 ? *(const u32x4*)(a.gw + ch * 8) : (u32x4){0u, 0u, 0u, 0u};
                const u32x4 xv = *(const u32x4*)(a.x + (size_t)tok * DM + min(ch, 71) * 8);
#pragma unroll
                for (int j = 0; j < 4; ++j) acc += bflo(xv[j]) * bflo(gv[j]) + bfhi(xv[j]) * bfhi(gv[j]);
            }
            acc += __shfl_xor(acc, 1); acc += __shfl_xor(acc, 2); acc += __shfl_xor(acc, 4); acc += __shfl_xor(acc, 8);
            st32(__float_as_uint(1.0f / (1.0f + __expf(-acc))), rw, (lane & 15) == 0 ? OFF_GT + tok * 4 : OOB, 0);
        }
#pragma unroll
        for (int i = 0; i < 8; ++i) {
            const unsigned x5[5] = {__builtin_amdgcn_alignbit(v[i].y, v[i].x, sh), __builtin_amdgcn_alignbit(v[i].z, v[i].y, sh), __builtin_amdgcn_alignbit(v[i].w, v[i].z, sh),
                                    __builtin_amdgcn_alignbit(e[i], v[i].w, sh), e[i] >> sh};
#pragma unroll
            for (int c = 0; c < S_CH; ++c) *(LAS u16*)(lds + S_XR + c * S_XRS + (2047 - tid - 256 * i) * 2) = (u16)(x5[c >> 1] >> (16 * (c & 1)));
        }
    }
    if (tid < 8 * S_CH) *(LAS unsigned*)(lds + S_XR + (tid >> 3) * S_XRS + 4096 + (tid & 7) * 4) = 0;
    if (tid < 64) *(LAS unsigned*)(lds + S_RC + (tid >> 4) * S_RCS + 4096 + (tid & 15) * 4) = 0;
    for (int i = tid; i < 15 * S_KROW / 4; i += 256) *(LAS unsigned*)(lds + S_K + i * 4) = 0;
    int kd[8];
#pragma unroll
    for (int j = 0; j < 8; ++j) {
        const int p = (int)bf2f(f2bf((float)(8 * tid + j))), k = p >> 7, r = p & 7;
        const int row = p < 256 ? 8 * k + r : (p < 512 ? 8 + 4 * k + (r >> 1) : (p < 1024 ? 16 + 2 * k + (r >> 2) : 24 + k));
        kd[j] = S_KD + (row * 16 + ((p >> 3) & 15)) * 8;
    }
    const int n = tid & 127, nn = lane & 15, g = lane >> 4;
    u16 pA = a.lAr[d0 * NS + n], pI = a.Aim[d0 * NS + n], pCr = a.Cr[d0 * NS + n], pCi = a.Ci[d0 * NS + n], pT = a.ldt[d0], pD = a.Dsk[d0];
    WG_BARRIER();
    for (int ch = 0; ch < S_CH; ++ch) {
        S4S(0)
        const float dsk = bf2f(pD);
        {
            const u32x4 v = *(LAS u32x4*)(lds + S_XR + ch * S_XRS + tid * 16);
            const u32x2 e = *(LAS u32x2*)(lds + S_XR + ch * S_XRS + tid * 16 + 16);
            const unsigned s01 = __builtin_amdgcn_alignbit(v.y, v.x, 16), s12 = __builtin_amdgcn_alignbit(v.z, v.y, 16), s23 = __builtin_amdgcn_alignbit(v.w, v.z, 16);
            const unsigned s34 = __builtin_amdgcn_alignbit(e.x, v.w, 16), s45 = __builtin_amdgcn_alignbit(e.y, e.x, 16);
            *(LAS u32x4*)(lds + S_RC + tid * 16) = v;
            *(LAS u32x4*)(lds + S_RC + S_RCS + tid * 16) = (u32x4){s01, s12, s23, s34};
            *(LAS u32x4*)(lds + S_RC + 2 * S_RCS + tid * 16) = (u32x4){v.y, v.z, v.w, e.x};
            *(LAS u32x4*)(lds + S_RC + 3 * S_RCS + tid * 16) = (u32x4){s12, s23, s34, s45};
        }
        S4S(1)
        const float dt = bf2f(f2bf(expf(bf2f(pT))));
        const int sc = 140 - (wave_max32(max(pCr & 0x7FFF, pCi & 0x7FFF)) >> 7);
        {
            const float ar = -bf2f(f2bf(expf(bf2f(pA)))) * dt, ai = bf2f(pI) * dt;
            const f32x2 C = {__builtin_amdgcn_ldexpf(bf2f(pCr), sc), __builtin_amdgcn_ldexpf(bf2f(pCi), sc)};
            const int dn = min(d0 + ch + 1, DM - 1);
            pA = a.lAr[dn * NS + n]; pI = a.Aim[dn * NS + n]; pCr = a.Cr[dn * NS + n]; pCi = a.Ci[dn * NS + n]; pT = a.ldt[dn]; pD = a.Dsk[dn];
            const int to = S_TAB + n * 4;
            auto put = [&](int row, f32x2 v) { *(LAS f16x2*)(lds + to + row * S_TROW) = __builtin_convertvector(v, f16x2); };
            const f32x2 z128 = cexp_p(ar, ai, 128.f);
            if (w < 2) {
                f32x2 E[8], G[4];
                E[1] = cexp_p(ar, ai, 1.f); G[0] = C;
#pragma unroll
                for (int r = 2; r < 8; ++r) E[r] = cmul(E[r >> 1], E[r - (r >> 1)]);
#pragma unroll
                for (int k = 1; k < 4; ++k) G[k] = cmul(G[k - 1], z128);
#pragma unroll
                for (int k = 0; k < 4; ++k)
#pragma unroll
                    for (int r = 0; r < 8; r += 1 + (k >> 1)) put(k < 2 ? 8 * k + r : 8 + 4 * k + (r >> 1), r ? cmul(E[r], G[k]) : G[k]);
            } else {
                const f32x2 z4 = cexp_p(ar, ai, 4.f), z8 = cexp_p(ar, -ai, 8.f);
                f32x2 G = cmul(C, cexp_p(ar, ai, 512.f)), B = z8;
                put(41, (f32x2){1.f, 0.f});
#pragma unroll
                for (int k = 1; k < 16; ++k) {
                    put(41 + k, B); B = cmul(B, z8);
                    if (k < 5) { put(22 + 2 * k, G); put(23 + 2 * k, cmul(G, z4)); }
                    else if (k < 14) put(27 + k, G);
                    G = cmul(G, z128);
                }
            }
        }
        WG_BARRIER();
        S4S(2)
        {
            const int ko = (w & 1) * 256 + g * 16, t0 = w & 2;
            u32x4 av[2][4], bv[4];
#pragma unroll
            for (int j = 0; j < 4; ++j) {
                bv[j] = *(LAS u32x4*)(lds + S_TAB + (41 + nn) * S_TROW + ko + j * 64);
                av[0][j] = *(LAS u32x4*)(lds + S_TAB + min(16 * t0 + nn, 40) * S_TROW + ko + j * 64);
                av[1][j] = *(LAS u32x4*)(lds + S_TAB + (16 + nn) * S_TROW + ko + j * 64);
            }
            f32x4 acc[2] = {};
#pragma unroll
            for (int j = 0; j < 8; ++j) acc[j & 1] = __builtin_amdgcn_mfma_f32_16x16x32_f16(__builtin_bit_cast(f16x8, av[j & 1][j >> 1]), __builtin_bit_cast(f16x8, bv[j >> 1]), acc[j & 1], 0, 0, 0);
#pragma unroll
            for (int r = 0; r < 8; ++r) *(LAS float*)(lds + S_KD + (((r < 4 ? 16 * t0 : 16) + 4 * g + (r & 3)) * 16 + nn) * 8 + (w & 1) * 4) = __builtin_amdgcn_ldexpf(acc[r >> 2][r & 3] * dt, 1 - sc);
        }
        WG_BARRIER();
        S4S(3)
        {
            float kv[8];
#pragma unroll
            for (int j = 0; j < 8; ++j) { const f32x2 t = *(LAS f32x2*)(lds + kd[j]); kv[j] = t.x + t.y; }
            *(LAS u32x4*)(lds + S_K + (15 + (tid >> 2)) * S_KROW + (tid & 3) * 16) = (u32x4){pack2(kv[0], kv[1]), pack2(kv[2], kv[3]), pack2(kv[4], kv[5]), pack2(kv[6], kv[7])};
        }
        WG_BARRIER();
        S4S(4)
        {
            const int st0 = 2047 - nn + 8 * g - 32 * w, cc = st0 & 3;
            const int ub = S_RC + cc * S_RCS + (st0 - cc) * 2 - 3872, kb = S_K + (3 + nn - w) * S_KROW + g * 16;
            f32x4 y[4][2] = {};
            u32x2 U[2][16]; u32x4 F[2][4];
            auto rd = [&](int i, int f) {
                if (f < 4) F[i & 1][f] = *(LAS u32x4*)(lds + kb + (12 - 4 * i + 16 * f) * S_KROW);
                else U[i & 1][f - 4] = *(LAS u32x2*)(lds + ub + 3872 - 256 * i - 1024 * ((f - 4) >> 2) - 32 * ((f >> 1) & 1) + 8 * (f & 1));
            };
#pragma unroll
            for (int f = 0; f < 20; ++f) { rd(0, f); FENCE(); }
#pragma unroll
            for (int i = 0; i < 4; ++i) {
                int c = 0;
#pragma unroll
                for (int q = 0; q < 4; ++q)
#pragma unroll
                    for (int T = q; T < 4; ++T)
#pragma unroll
                        for (int bt = 0; bt < 2; ++bt) {
                            y[T][bt] = MFMA16(__builtin_shufflevector(U[i & 1][4 * q + 2 * bt], U[i & 1][4 * q + 2 * bt + 1], 0, 1, 2, 3), F[i & 1][T - q], y[T][bt]);
                            if (i < 3) rd(i + 1, c);
                            ++c;
                            FENCE();
                        }
            }
#pragma unroll
            for (int T = 0; T < 4; ++T)
#pragma unroll
                for (int bt = 0; bt < 2; ++bt) *(LAS f32x4*)(lds + S_RED + (w * 64 + 16 * T + nn) * 144 + bt * 64 + g * 16) = y[T][bt];
        }
        WG_BARRIER();
        S4S(5)
        {
            f32x4 y0 = {0, 0, 0, 0}, y1 = y0;
#pragma unroll
            for (int ww = 0; ww < 4; ++ww) {
                const int o = S_RED + (ww * 64 + (tid >> 2)) * 144 + (tid & 3) * 32;
                y0 += *(LAS f32x4*)(lds + o); y1 += *(LAS f32x4*)(lds + o + 16);
            }
            const u32x4 u = *(LAS u32x4*)(lds + S_XR + ch * S_XRS + (255 - tid) * 16);
            st128((u32x4){pack2(y0[0] + bfhi(u.w) * dsk, y0[1] + bflo(u.w) * dsk), pack2(y0[2] + bfhi(u.z) * dsk, y0[3] + bflo(u.z) * dsk),
                          pack2(y1[0] + bfhi(u.y) * dsk, y1[1] + bflo(u.y) * dsk), pack2(y1[2] + bfhi(u.x) * dsk, y1[3] + bflo(u.x) * dsk)}, rw, tid * 16, OFF_BO + (d0 + ch) * BO_ROW);
        }
        S4S(6)
    }
    wait_init(rw, launch_tag(a), budget);
    VM_DRAIN();
    WG_BARRIER();
    if (tid == 0) st32(1u, rw, F_S4 + task * 4, 0);
}

constexpr int XT_BYTES = 32 * 1152;
constexpr int P_XT = 0;
constexpr int P_STG = 2 * XT_BYTES;
struct WSlice { u32x4 f[36]; };
constexpr int P_RING = 5;
template <int E1, int E3, class E, class F> DI void load_wslices(WSlice& W0, WSlice& W1, const u16* s0, const u16* s1, int w, int lane, LAS char* lds, unsigned lds_base, E also, F got) {
    constexpr int NB = 18;
    const int r0 = 8 * (lane >> 5) + ((lane & 15) >> 2);
    const int cb = (4 * w + 2 * ((lane >> 4) & 1) + ((lane & 3) >> 1)), lo8 = (lane & 1) * 8;
    auto src = [&](int i) { return (i < 9 ? s0 : s1) + (size_t)(64 * (i % 9)) * HD; };
    const u32x4 ro = ring_offs(w, lane, HD);
#pragma unroll
    for (int i = 0; i < P_RING - 1; ++i) { ring_issue(src(i), ro, lds_base + P_STG + i * 16384 + w * 4096); also(i); }
    static_for<NB>([&](auto ic) {
        constexpr int i = decltype(ic)::value, rem = NB - 1 - i < P_RING - 2 ? NB - 1 - i : P_RING - 2;
        ring_wait<4 * rem + (i < 2) * E1 + (i < 4) * E3 + (i > 0 && i < 5 && E1) * 4>();
        got(ic, ic_t<0>{});
        if constexpr (i + P_RING - 1 < NB) ring_issue(src(i + P_RING - 1), ro, lds_base + P_STG + ((i + P_RING - 1) % P_RING) * 16384 + w * 4096);
        got(ic, ic_t<1>{});
        const int sl = P_STG + (i % P_RING) * 16384;
#pragma unroll
        for (int kk = 0; kk < 4; ++kk) {
            const int ra = r0 + 16 * kk, rb = ra + 4;
            const u32x2 lo = tr_read(lds, sl + ra * 256 + ((cb ^ (2 * swz(ra))) * 16) + lo8), hi = tr_read(lds, sl + rb * 256 + ((cb ^ (2 * swz(rb))) * 16) + lo8);
            (i < 9 ? W0 : W1).f[(i % 9) * 4 + kk] = (u32x4){lo.x, lo.y, hi.x, hi.y};
        }
        got(ic, ic_t<2>{});
        u32x4* f = (i < 9 ? W0 : W1).f + (i % 9) * 4;
        if constexpr (i < 16) asm volatile("" : "+a"(f[0]), "+a"(f[1]), "+a"(f[2]), "+a"(f[3]));
    });
    static_for<3>([&](auto jc) { got(ic_t<NB>{}, jc); });
}
DI u32x4 piece_pi(const f32x16& a, int qq) {
    return (u32x4){pack2(a[4 * qq], a[4 * qq + 8]), pack2(a[4 * qq + 1], a[4 * qq + 9]), pack2(a[4 * qq + 2], a[4 * qq + 10]), pack2(a[4 * qq + 3], a[4 * qq + 11])};
}
DI u32x4 piece_seq(const f32x16& a, int s2) {
    return (u32x4){pack2(a[8 * s2], a[8 * s2 + 1]), pack2(a[8 * s2 + 2], a[8 * s2 + 3]), pack2(a[8 * s2 + 4], a[8 * s2 + 5]), pack2(a[8 * s2 + 6], a[8 * s2 + 7])};
}
struct XTile {
    int xs[9], xa[4];
    DI void init(int w, int lane) {
#pragma unroll
        for (int i = 0; i < 9; ++i) { const int L = (i * 4 + w) * 64 + lane, r = L / 72, cs = L - 72 * r; xs[i] = r * 1152 + (cs ^ ((r >> 1) & 7)) * 16; }
        const int c32 = lane & 31, hf = lane >> 5;
#pragma unroll
        for (int j = 0; j < 4; ++j) xa[j] = P_XT + c32 * 1152 + (((2 * j + hf) ^ ((c32 >> 1) & 7)) * 16);
    }
    DI void dma(const Args& a, unsigned lds_base, int w, int s, int buf, int i) const {
        glds16((const char*)a.x + (size_t)min(s, NSEG - 1) * XT_BYTES, xs[i], lds_base + P_XT + buf * XT_BYTES + (i * 4 + w) * 1024);
    }
    DI void first(const Args& a, unsigned lds_base, int w, int lane, int s) {
        init(w, lane);
#pragma unroll
        for (int i = 0; i < 9; ++i) dma(a, lds_base, w, s, 0, i);
    }
};
template <class F> DI void project(f32x16& acc0, f32x16& acc1, const WSlice& W0, const WSlice& W1, const XTile& X, const Args& a, LAS char* lds, unsigned lds_base, int w, int buf, int sn, F gap) {
    const int bo = buf * XT_BYTES;
    constexpr int PD = 5;
    u32x4 xf[PD];
#pragma unroll
    for (int i = 0; i < PD; ++i) xf[i] = *(LAS u32x4*)(lds + X.xa[i & 3] + bo + (i >> 2) * 128);
    static_for<36>([&](auto ic) {
        constexpr int ks = decltype(ic)::value;
        if (ks == 0) asm volatile("v_mfma_f32_32x32x16_bf16 %0, %1, %2, 0" : "=&v"(acc0) : "a"(W0.f[0]), "v"(xf[0]) : "memory");
        else asm volatile("v_mfma_f32_32x32x16_bf16 %0, %1, %2, %0" : "+v"(acc0) : "a"(W0.f[ks]), "v"(xf[ks % PD]) : "memory");
        if (ks < 9) X.dma(a, lds_base, w, sn, buf ^ 1, ks);
        gap(ic_t<2 * ks>{});
        if (ks == 0) asm volatile("v_mfma_f32_32x32x16_bf16 %0, %1, %2, 0" : "=&v"(acc1) : "a"(W1.f[0]), "v"(xf[0]) : "memory");
        else if (ks < 28) asm volatile("v_mfma_f32_32x32x16_bf16 %0, %1, %2, %0" : "+v"(acc1) : "a"(W1.f[ks]), "v"(xf[ks % PD]) : "memory");
        else asm volatile("v_mfma_f32_32x32x16_bf16 %0, %1, %2, %0" : "+v"(acc1) : "v"(W1.f[ks]), "v"(xf[ks % PD]) : "memory");
        if (ks + PD < 36) xf[ks % PD] = *(LAS u32x4*)(lds + X.xa[(ks + PD) & 3] + bo + ((ks + PD) >> 2) * 128);
        gap(ic_t<2 * ks + 1>{});
    });
    asm volatile("s_nop 7\n\ts_nop 3" : "+v"(acc0), "+v"(acc1) :: "memory");
}
#define KV_T 12
#define KV_F 22
DI void role_kv(const Args& a, LAS char* lds, unsigned lds_base, int h, rsrc_t rw, int& budget) {
    const int tid = threadIdx.x, lane = tid & 63, w = __builtin_amdgcn_readfirstlane(tid >> 6);
    WSlice W0, W1;
    XTile X;
    const int oo = h * 8192 + w * 2048 + lane * 16;
    const int fo = lane == 0 ? F_KV + h * 512 + w * 128 : OOB;
    u32x4 xf[6], P[4] = {};
    auto frag = [&](int buf, int ks) { xf[ks % 6] = *(LAS u32x4*)(lds + X.xa[ks & 3] + buf * XT_BYTES + (ks >> 2) * 128); };
    auto cvt = [&](const f32x16& k, const f32x16& v, int i) {
#pragma unroll
        for (int j = 0; j < 4; ++j) asm volatile("v_cvt_pk_bf16_f32 %0, %1, %2" : "=v"(P[i][j]) : "v"(i < 2 ? k[4 * i + j] : v[8 * i - 16 + 2 * j]), "v"(i < 2 ? k[4 * i + j + 8] : v[8 * i - 15 + 2 * j]));
    };
    auto put = [&](int o, int s, int i) { st128(P[i], rw, o + (i & 1) * 1024, (i < 2 ? OFF_KF : OFF_VF) + s * (NH * 8192)); };
    auto now_ = [&](f32x16& k, f32x16& v, int s) {
        asm volatile("s_nop 7\n\ts_nop 3" : "+v"(k), "+v"(v) :: "memory");
#pragma unroll
        for (int i = 0; i < 4; ++i) { cvt(k, v, i); put(oo, s, i); }
    };
    auto seg = [&](f32x16& k, f32x16& v, const f32x16& pk, const f32x16& pv, int s, int buf) {
        const int o = s > 2 ? oo : OOB;
#pragma unroll
        for (int ks = 0; ks < 36; ++ks) {
            if (ks == 0) asm volatile("v_mfma_f32_32x32x16_bf16 %0, %1, %2, 0" : "=&v"(k) : "a"(W0.f[0]), "v"(xf[0]) : "memory");
            else asm volatile("v_mfma_f32_32x32x16_bf16 %0, %1, %2, %0" : "+v"(k) : "a"(W0.f[ks]), "v"(xf[ks % 6]) : "memory");
            if (ks >= 1 && ks < 5) cvt(pk, pv, ks - 1);
            if (ks >= 5 && ks < 13 && (ks & 1)) put(o, s - 1, (ks - 5) >> 1);
            if (ks == KV_T && !buf && s == 2) { asm volatile("s_waitcnt vmcnt(8)" ::: "memory"); st32(1u, rw, fo, 0); }
            if (ks == 14 && (unsigned)(s - 4) < 8) {
                const int i = s - 4, t = opaque_lane() + 64 * w;
                st128((u32x4){SENT, SENT, SENT, SENT}, rw, i < 4 ? (t >> 7) * 65536 + (t & 127) * 16 : t * 16,
                      i < 4 ? (i & 2 ? OFF_VM : OFF_KM) + (FAST + 2 * (i & 1)) * 65536 + h * 2048 : OFF_OF + (LATE - 2 + (i >> 1)) * 262144 + h * 8192 + (i & 1) * 4096);
            }
            if (ks == KV_F) { VM_DRAIN(); st32((unsigned)(s - 1), rw, fo, 0); }
            if (ks == 0) asm volatile("v_mfma_f32_32x32x16_bf16 %0, %2, %1, 0" : "=&v"(v) : "a"(W1.f[0]), "v"(xf[0]) : "memory");
            else if (ks < 28) asm volatile("v_mfma_f32_32x32x16_bf16 %0, %2, %1, %0" : "+v"(v) : "a"(W1.f[ks]), "v"(xf[ks % 6]) : "memory");
            else asm volatile("v_mfma_f32_32x32x16_bf16 %0, %2, %1, %0" : "+v"(v) : "v"(W1.f[ks]), "v"(xf[ks % 6]) : "memory");
            if (ks == 30) WG_BARRIER();
            frag(buf ^ (ks >= 30), (ks + 6) % 36);
            if (ks < 4) X.dma(a, lds_base, w, s + 1, buf ^ 1, ks + 5);
            if (ks > 30) X.dma(a, lds_base, w, s + 2, buf, ks - 31);
        }
    };
    f32x16 k0 = {}, v0 = {}, k1 = {}, v1 = {};
    u32x2 tag, in;
    u32x4 xa[2][4], xb[2][4];
    auto mm = [&](f32x16& k, f32x16& v, int t, int i, int j) {
        if (t) {}
        else if (i >= 16) asm volatile("s_nop 1\n\tv_mfma_f32_32x32x16_bf16 %0, %2, %1, %0" : "+v"(v) : "v"(W1.f[4 * i % 36 + j]), "v"(xa[t][j]) : "memory");
        else if (i >= 9) asm volatile("s_nop 1\n\tv_mfma_f32_32x32x16_bf16 %0, %2, %1, %0" : "+v"(v) : "a"(W1.f[4 * i % 36 + j]), "v"(xa[t][j]) : "memory");
        if (i > 1 + 2 * t && i < 11 + 2 * t) asm volatile("s_nop 1\n\tv_mfma_f32_32x32x16_bf16 %0, %1, %2, %0" : "+v"(k) : "a"(W0.f[4 * i % 36 + j]), "v"(xa[t][j]) : "memory");
    };
    load_wslices<9, 9>(W0, W1, a.Wk + h * DH, a.Wv + h * DH, w, lane, lds, lds_base, [&](int i) {
        if (i == 1) X.first(a, lds_base, w, lane, 0);
        if (i == 3)
#pragma unroll
            for (int j = 0; j < 9; ++j) X.dma(a, lds_base, w, 1, 1, j);
    }, [&](auto ic, auto jc) {
        constexpr int i = decltype(ic)::value - 1, part = decltype(jc)::value;
        mm(k0, v0, 0, i, part); mm(k1, v1, 1, i, part);
        if (part < 2) return;
        if (i < 0)
#pragma unroll
            for (int j = 0; j < 4; ++j) { P[j] = (u32x4){SENT, SENT, SENT, SENT}; put(oo, 0, j); }
#pragma unroll
        for (int j = 0; j < 8; ++j) if (i > (j >> 2) * 2 && i < 17 - 5 * (j >> 2)) xb[j >> 2][j & 3] = *(LAS u32x4*)(lds + X.xa[j & 3] + (j >> 2) * XT_BYTES + ((i + 1) % 9) * 128);
        mm(k0, v0, 0, i, 3); mm(k1, v1, 1, i, 3);
#pragma unroll
        for (int j = 0; j < 8; ++j) if (i > (j >> 2) * 2) xa[j >> 2][j & 3] = xb[j >> 2][j & 3];
        if (i == 4) { tag = launch_tag(a); __builtin_amdgcn_raw_buffer_store_b64(tag, rw, lane == 0 ? F_INIT + (33 + 4 * h + w) * 128 : OOB, 0, 16); }
        if (i == 10) asm volatile("buffer_load_dwordx2 %0, %1, %2, 0 offen sc1" : "=v"(in) : "v"(init_at(0)), "s"(rw) : "memory");
        if (i == 11) { cvt(k0, v0, 0); put(oo, 0, 0); cvt(k0, v0, 1); put(oo, 0, 1); }
    });
#pragma unroll
    for (int i = 0; i < 6; ++i) frag(1, i);
    asm volatile("s_nop 7\n\ts_nop 3" : "+v"(v0) :: "memory");
    cvt(k0, v0, 2); put(oo, 0, 2); cvt(k0, v0, 3); put(oo, 0, 3);
    WG_BARRIER();
#pragma unroll
    for (int ks = 0; ks < 36; ++ks) {
        if (ks < 28) asm volatile("v_mfma_f32_32x32x16_bf16 %0, %2, %1, %0" : "+v"(v1) : "a"(W1.f[ks]), "v"(xf[ks % 6]) : "memory");
        else asm volatile("v_mfma_f32_32x32x16_bf16 %0, %2, %1, %0" : "+v"(v1) : "v"(W1.f[ks]), "v"(xf[ks % 6]) : "memory");
        if (ks < 18 && !(ks & 1)) X.dma(a, lds_base, w, 2, 0, ks >> 1);
        if (ks == 19 || ks == 22) { cvt(k1, v1, ks == 22); put(oo, 1, ks == 22); }
        if (ks == 30) ring_wait<2>();
        frag(ks < 30, (ks + 6) % 36);
        if (ks > 30) X.dma(a, lds_base, w, 3, 1, ks - 31);
    }
    asm volatile("s_nop 7\n\ts_nop 3" : "+v"(v1), "+v"(in) :: "memory");
    cvt(k1, v1, 2); put(oo, 1, 2); cvt(k1, v1, 3); put(oo, 1, 3);
    if (!init_ok(in, tag)) wait_init(rw, tag, budget);
    for (int s = 2; s < NSEG; s += 2) { seg(k0, v0, k1, v1, s, 0); seg(k1, v1, k0, v0, s + 1, 1); }
    now_(k1, v1, NSEG - 1);
    wait_prog(rw, F_S4 + lane * 4, true, 1, budget);
    VM_DRAIN();
    st32(NSEG, rw, fo, 0);
}

DI int prog_min(rsrc_t rw, int off) {
    const int v = (int)ld32(rw, off, 0);
    asm volatile("" ::: "memory");
    return -wave_max32(-v);
}
DI float red_max_g(float v) {
    auto a = __builtin_amdgcn_permlane16_swap(__float_as_uint(v), __float_as_uint(v), false, false);
    v = fmaxf(__uint_as_float(a[0]), __uint_as_float(a[1]));
    auto b = __builtin_amdgcn_permlane32_swap(__float_as_uint(v), __float_as_uint(v), false, false);
    return fmaxf(__uint_as_float(b[0]), __uint_as_float(b[1]));
}
#define M16 "v_mfma_f32_16x16x32_bf16 "
DI void role_qa(const Args& a, LAS char* lds, unsigned lds_base, int h0, int s0, int s1, rsrc_t rw, int& budget) {
    const int tid = threadIdx.x, lane = tid & 63, w = __builtin_amdgcn_readfirstlane(tid >> 6);
    const int n = lane & 15, g = lane >> 4, h = h0 + (w >> 1), tt = w & 1;
    WSlice W0, W1;
    XTile X;
    load_wslices<0, 9>(W0, W1, a.Wq + h0 * DH, a.Wq + (h0 + 1) * DH, w, lane, lds, lds_base, [&](int i) { if (i == 3) X.first(a, lds_base, w, lane, s0); }, [](auto, auto) {});
    const int oo = OFF_OF + (2 * tt + (n >> 3)) * 65536 + h * 2048 + g * 128 + (n & 7) * 16;
#pragma unroll
    for (int t = 0; t < 4; ++t) st128((u32x4){SENT, SENT, SENT, SENT}, rw, oo + t * 512, s1 * 262144);
    VM_DRAIN();
    WG_BARRIER();
    constexpr int Q_Q = P_STG, Q_KV = Q_Q + 16384, Q_M = Q_KV + 32768;
    static_assert(Q_M + 16384 <= P_STG + P_RING * 16384, "");
    const int fk = F_KV + h * 512 + (lane & 3) * 128, fm = F_KVM + h * 256 + (lane & 1) * 128;
    int pm_, m_rdy = -1, kv_rdy = -1;
    auto look = [&]() { pm_ = (int)ld32(rw, lane & 4 ? fk : fm, 0); };
    auto seen = [&]() { auto rl = [&](int i) { return __builtin_amdgcn_readlane(pm_, i); }; m_rdy = min(rl(0), rl(1)); kv_rdy = min(min(rl(4), rl(5)), min(rl(6), rl(7))); };
    auto wait_m = [&](int thr, unsigned& t) {
        for (;;) {
            const u32x2 v = ld64(rw, fm, 0);
            asm volatile("" ::: "memory");
            t = now();
            const int p0 = __builtin_amdgcn_readlane(v.x, 0), p1 = __builtin_amdgcn_readlane(v.x, 1);
            const int d0 = (int)(__builtin_amdgcn_readlane(v.y, 0) - 8 * t) >> 3, d1 = (int)(__builtin_amdgcn_readlane(v.y, 1) - 8 * t) >> 3;
            if (min(p0, p1) >= thr || --budget < 0) return max(p0 == thr ? d0 : -1000, p1 == thr ? d1 : -1000);
            (void)wait_until(t + min(max(min(d0, d1) + (int)(a.xa >> 16 & 255), 10), 200));
        }
    };
    auto proj = [&](int s, auto gap) {
        f32x16 acc0, acc1;
        project(acc0, acc1, W0, W1, X, a, lds, lds_base, w, ((s - s0) >> 1) & 1, s + 2, [&](auto ic) { gap(ic); if (decltype(ic)::value == 55) look(); });
        if (s >= LATE) {
            const int o = OFF_OF + h0 * 8192 + w * 2048 + lane * 16;
            st128(piece_pi(acc0, 0), rw, o, s * 262144); st128(piece_pi(acc0, 1), rw, o + 1024, s * 262144);
            st128(piece_pi(acc1, 0), rw, o + 8192, s * 262144); st128(piece_pi(acc1, 1), rw, o + 8192 + 1024, s * 262144);
            return;
        }
        const int o = Q_Q + w * 2048 + lane * 16;
        *(LAS u32x4*)(lds + o) = piece_pi(acc0, 0); *(LAS u32x4*)(lds + o + 1024) = piece_pi(acc0, 1);
        *(LAS u32x4*)(lds + o + 8192) = piece_pi(acc1, 0); *(LAS u32x4*)(lds + o + 8192 + 1024) = piece_pi(acc1, 1);
    };
    const int fs = (tt ? OFF_VF : OFF_KF) + (g >> 1) * 1024 + ((g & 1) * 32 + 16 * (n >> 3) + (n & 7)) * 16;
    auto fetch = [&](int s) {
        const int so = (min(s, NSEG - 1) * NH + h) * 8192;
#pragma unroll
        for (int f = 0; f < 8; ++f) bdma(rw, fs, so + (f >> 1) * 2048 + (f & 1) * 128, lds_base + Q_KV + w * 8192 + f * 1024);
    };
    const int ms = OFF_KM + (lane & 7) * 256 + (lane >> 3) * 16;
    auto fetch_m = [&](int s) {
        const int so = (s * NH + h) * 2048;
        const unsigned d = lds_base + Q_M + w * 4096;
        bdma(rw, ms, so, d); bdma(rw, ms, so + 128, d + 1024); bdma(rw, lane * 16, OFF_VM + so, d + 2048); bdma(rw, lane * 16, OFF_VM + so + 1024, d + 3072);
    };
    const float c = 0.08838834764831845f * 1.4426950408889634f;
    u32x4 qf[4];
    f32x4 o[8]; float mx, l;
    auto xpart = [&]() {
        const int kb = Q_KV + (w >> 1) * 16384 + lane * 16;
        u32x4 kn[8], vn[8];
#pragma unroll
        for (int ks = 0; ks < 4; ++ks) qf[ks] = *(LAS u32x4*)(lds + Q_Q + (w >> 1) * 8192 + (2 * ks + (g >> 1)) * 1024 + ((g & 1) * 32 + 16 * tt + n) * 16);
#pragma unroll
        for (int f = 0; f < 8; ++f) kn[f] = *(LAS u32x4*)(lds + kb + f * 1024);
        FENCE();
#pragma unroll
        for (int f = 0; f < 8; ++f) vn[f] = *(LAS u32x4*)(lds + kb + 8192 + f * 1024);
        f32x4 s0, s1;
        asm volatile(M16 "%0, %2, %10, 0\n\t" M16 "%1, %3, %10, 0\n\t" M16 "%0, %4, %11, %0\n\t" M16 "%1, %5, %11, %1\n\t"
                     M16 "%0, %6, %12, %0\n\t" M16 "%1, %7, %12, %1\n\t" M16 "%0, %8, %13, %0\n\t" M16 "%1, %9, %13, %1\n\ts_nop 7"
                     : "=&v"(s0), "=&v"(s1) : "v"(kn[0]), "v"(kn[1]), "v"(kn[2]), "v"(kn[3]), "v"(kn[4]), "v"(kn[5]), "v"(kn[6]), "v"(kn[7]), "v"(qf[0]), "v"(qf[1]), "v"(qf[2]), "v"(qf[3]));
        float p[8];
        mx = -3.0e38f; l = 0.f;
#pragma unroll
        for (int r = 0; r < 8; ++r) { const int key = 16 * (g >> 1) + 4 * (g & 1) + (r & 3) + 8 * (r >> 2); p[r] = key <= 16 * tt + n ? (r < 4 ? s0[r & 3] : s1[r & 3]) : -3.0e38f; mx = fmaxf(mx, p[r]); }
        mx = red_max_g(mx);
#pragma unroll
        for (int r = 0; r < 8; ++r) { p[r] = __builtin_amdgcn_exp2f((p[r] - mx) * c); l += p[r]; }
        const u32x4 pf = {pack2(p[0], p[1]), pack2(p[2], p[3]), pack2(p[4], p[5]), pack2(p[6], p[7])};
        asm volatile("s_nop 1\n\t" M16 "%0, %8, %16, 0\n\t" M16 "%1, %9, %16, 0\n\t" M16 "%2, %10, %16, 0\n\t" M16 "%3, %11, %16, 0\n\t"
                     M16 "%4, %12, %16, 0\n\t" M16 "%5, %13, %16, 0\n\t" M16 "%6, %14, %16, 0\n\t" M16 "%7, %15, %16, 0\n\ts_nop 7"
                     : "=&v"(o[0]), "=&v"(o[1]), "=&v"(o[2]), "=&v"(o[3]), "=&v"(o[4]), "=&v"(o[5]), "=&v"(o[6]), "=&v"(o[7])
                     : "v"(vn[0]), "v"(vn[1]), "v"(vn[2]), "v"(vn[3]), "v"(vn[4]), "v"(vn[5]), "v"(vn[6]), "v"(vn[7]), "v"(pf));
    };
    const int fo = lane == 0 ? F_O + h * 128 + (2 * s0 + tt) * 4 : OOB;
    bool pre = false;
    auto both = [&](int s) {
        const int kb = Q_KV + (w >> 1) * 16384 + lane * 16;
        u32x4 kn[8], vn[8], pf;
        f32x4 sc[2];
        float p[8];
        int v0, v1, v2;
        asm volatile("buffer_load_dword %0, %1, %2, 0 offen sc1" : "=v"(pm_) : "v"(lane & 4 ? fk : fm), "s"(rw) : "memory");
#define PIN(...) asm volatile("" : __VA_ARGS__)
        proj(s + 4, [&](auto ic) {
            constexpr int i = decltype(ic)::value;
            if constexpr (i < 8) {
                if (!(i & 1)) qf[i >> 1] = *(LAS u32x4*)(lds + Q_Q + (w >> 1) * 8192 + (i + (g >> 1)) * 1024 + ((g & 1) * 32 + 16 * tt + n) * 16);
                kn[i] = *(LAS u32x4*)(lds + kb + i * 1024);
            }
            if constexpr (i >= 16 && i < 32 && !(i & 1)) vn[(i - 16) >> 1] = *(LAS u32x4*)(lds + kb + 8192 + (i - 16) * 512);
            if constexpr (i == 5 || i == 6) asm volatile(M16 "%0, %1, %2, 0" : "=&v"(sc[i - 5]) : "v"(kn[i - 5]), "v"(qf[0]));
            if constexpr (i > 6 && i < 13) asm volatile(M16 "%0, %1, %2, %0" : "+v"(sc[(i - 5) & 1]) : "v"(kn[i - 5]), "v"(qf[(i - 5) >> 1]));
            if constexpr (i == 15 || i == 16) {
                constexpr int r = 4 * (i - 15);
                PIN("+v"(sc[i - 15]));
#pragma unroll
                for (int j = 0; j < 4; ++j) p[r + j] = 16 * (g >> 1) + 4 * (g & 1) + j + 2 * r <= 16 * tt + n ? sc[i - 15][j] : -3.0e38f;
                PIN("+v"(p[r]), "+v"(p[r + 1]), "+v"(p[r + 2]), "+v"(p[r + 3]));
            }
            if constexpr (i == 17) { mx = fmaxf(fmaxf(fmaxf(fmaxf(p[0], p[1]), p[2]), fmaxf(fmaxf(p[3], p[4]), p[5])), fmaxf(p[6], p[7])); PIN("+v"(mx)); }
            if constexpr (i == 18) { auto x = __builtin_amdgcn_permlane16_swap(__float_as_uint(mx), __float_as_uint(mx), false, false); mx = fmaxf(__uint_as_float(x[0]), __uint_as_float(x[1])); PIN("+v"(mx)); }
            if constexpr (i == 19) { auto x = __builtin_amdgcn_permlane32_swap(__float_as_uint(mx), __float_as_uint(mx), false, false); mx = fmaxf(__uint_as_float(x[0]), __uint_as_float(x[1])); PIN("+v"(mx)); }
            if constexpr (i >= 20 && i < 24) {
                constexpr int r = 2 * (i - 20);
                PIN("+v"(p[r]), "+v"(p[r + 1]));
                p[r] = __builtin_amdgcn_exp2f((p[r] - mx) * c); p[r + 1] = __builtin_amdgcn_exp2f((p[r + 1] - mx) * c);
                PIN("+v"(p[r]), "+v"(p[r + 1]));
            }
            if constexpr (i == 24) { pf = (u32x4){pack2(p[0], p[1]), pack2(p[2], p[3]), pack2(p[4], p[5]), pack2(p[6], p[7])}; PIN("+v"(pf)); }
            if constexpr (i == 25) { l = ((p[0] + p[1]) + p[2]) + p[3]; PIN("+v"(l)); }
            if constexpr (i == 26) { l = (((l + p[4]) + p[5]) + p[6]) + p[7]; PIN("+v"(l)); }
            if constexpr (i == 27) { asm volatile("s_waitcnt vmcnt(9)" : "+v"(pm_) :: "memory"); st32((unsigned)s, rw, fo, 0); }
            if constexpr (i == 29) { seen(); pre = m_rdy >= s + 2 && kv_rdy >= s + 4; }
            if constexpr (i == 31) { v0 = pre ? ms : OOB; v1 = pre ? lane * 16 : OOB; v2 = pre ? fs : OOB; PIN("+v"(v0), "+v"(v1), "+v"(v2)); }
            if constexpr (i >= 28 && i < 36) asm volatile(M16 "%0, %1, %2, 0" : "=&v"(o[i - 28]) : "v"(vn[i - 28]), "v"(pf));
            if constexpr (i == 36) asm volatile("s_barrier" ::: "memory");
            if constexpr (i >= 38 && i < 42) bdma(rw, i < 40 ? v0 : v1, ((s + 2) * NH + h) * 2048 + (i < 40 ? (i & 1) * 128 : OFF_VM + (i & 1) * 1024), lds_base + Q_M + w * 4096 + (i - 38) * 1024);
            if constexpr (i >= 42 && i < 50) bdma(rw, v2, ((s + 4) * NH + h) * 8192 + ((i - 42) >> 1) * 2048 + (i & 1) * 128, lds_base + Q_KV + w * 8192 + (i - 42) * 1024);
        });
    };
    auto none = [](auto) {};
    proj(s0, none);
    if (s0 < FAST) wait_init(rw, launch_tag(a), budget);
    wait_prog(rw, fk, true, s0, budget);
    fetch(s0);
    VM_DRAIN();
    WG_BARRIER();
    xpart();
    WG_BARRIER();
    proj(s0 + 2, none);
    for (int s = s0; s <= s1; s += 2) {
        S4S(0)
        const int mb = Q_M + w * 4096;
        u32x4 kf[4]; u32x2 vm[4];
        auto frags = [&]() {
#pragma unroll
            for (int ks = 0; ks < 4; ++ks) kf[ks] = *(LAS u32x4*)(lds + mb + (4 * ks + g) * 128 + (2 * (n >> 2) + (n & 1)) * 16);
#pragma unroll
            for (int t = 0; t < 4; ++t) { const int vo = mb + 2048 + t * 512 + (n >> 3) * 256 + g * 64 + (n & 7) * 4; vm[t] = (u32x2){*(LAS unsigned*)(lds + vo), *(LAS unsigned*)(lds + vo + 32)}; }
        };
        if (s == s1) {
            TT(true, 3)
            unsigned t;
            const int d = min(wait_m(s - 1, t) + (int)(a.xa & 255), 500);
            st32((t + max(d, 0) + (a.xb >> 8 * ((s - FAST) >> 1) & 255)) & 0x7FFFFFFFu, rw, lane == 0 ? F_H + ((2 * (s - FAST) + tt) * NH + h) * 128 : OOB, 0);
            (void)wait_until(t + d);
            TT(true, 0)
            for (;;) {
                fetch_m(s);
                VM_DRAIN();
                frags();
                unsigned c = 0;
#pragma unroll
                for (int i = 0; i < 4; ++i) c = max(max(mx4(c, kf[i]), vm[i].x), vm[i].y);
                if (!__any(c == SENT) || --budget < 0) break;
            }
        } else {
            if (!pre) {
                seen();
                unsigned t;
                if (m_rdy < s) { const int d = wait_m(s, t); (void)wait_until(t + min(d + (int)(a.xa >> 24), 300)); }
                fetch_m(s);
                if (kv_rdy < s + 2) wait_prog(rw, fk, true, s + 2, budget);
                fetch(s + 2);
            }
            S4S(1)
            asm volatile("s_waitcnt vmcnt(8)" ::: "memory");
            frags();
        }
        TT(s == s1, 1)
        S4S(2)
        f32x4 sm;
        asm volatile(M16 "%0, %1, %5, 0\n\t" M16 "%0, %2, %6, %0\n\t" M16 "%0, %3, %7, %0\n\t" M16 "%0, %4, %8, %0\n\ts_nop 7"
                     : "=&v"(sm) : "v"(kf[0]), "v"(kf[1]), "v"(kf[2]), "v"(kf[3]), "v"(qf[0]), "v"(qf[1]), "v"(qf[2]), "v"(qf[3]));
        const float m2 = red_max_g(fmaxf(mx, fmaxf(sm[0], sm[1])));
        const float al = __builtin_amdgcn_exp2f((mx - m2) * c), p0 = __builtin_amdgcn_exp2f((sm[0] - m2) * c), p1 = __builtin_amdgcn_exp2f((sm[1] - m2) * c);
        const float inv = __builtin_amdgcn_rcpf(red_sum_g(l * al + p0 + p1)), ai = al * inv;
#pragma unroll
        for (int t = 0; t < 8; ++t) o[t] *= ai;
        const unsigned pm = pack2(p0 * inv, p1 * inv);
#define M1K "v_mfma_f32_16x16x16_bf16 "
        asm volatile("s_nop 1\n\t" M1K "%0, %8, %12, %0\n\t" M1K "%1, %8, %13, %1\n\t" M1K "%2, %9, %12, %2\n\t" M1K "%3, %9, %13, %3\n\t"
                     M1K "%4, %10, %12, %4\n\t" M1K "%5, %10, %13, %5\n\t" M1K "%6, %11, %12, %6\n\t" M1K "%7, %11, %13, %7\n\ts_nop 7"
                     : "+v"(o[0]), "+v"(o[1]), "+v"(o[2]), "+v"(o[3]), "+v"(o[4]), "+v"(o[5]), "+v"(o[6]), "+v"(o[7])
                     : "v"(vm[0]), "v"(vm[1]), "v"(vm[2]), "v"(vm[3]), "v"((u32x2){pm, 0u}), "v"((u32x2){0u, pm}));
#pragma unroll
        for (int b = 0; b < 2; ++b) {
            u32x4 x, y;
#pragma unroll
            for (int j = 0; j < 4; ++j) { x[j] = pack2(o[4 * b + (j >> 1)][2 * (j & 1)], o[4 * b + (j >> 1)][2 * (j & 1) + 1]); y[j] = pack2(o[4 * b + 2 + (j >> 1)][2 * (j & 1)], o[4 * b + 2 + (j >> 1)][2 * (j & 1) + 1]); }
            if (s == LATE - 1) { const u32x4 z = {x.x, x.z, y.x, y.z}; y = (u32x4){x.y, x.w, y.y, y.w}; x = z; }
            st128(x, rw, oo + b * 1024, s * 262144); st128(y, rw, oo + b * 1024 + 512, s * 262144);
        }
        TT(s == s1, 2)
        S4S(3)
        if (s == s1) break;
        asm volatile("s_waitcnt vmcnt(4)" ::: "memory");
        WG_BARRIER();
        S4S(4)
        if (s + 4 <= s1) { both(s); S4S(5) continue; }
        xpart();
        FENCE();
        VM_DRAIN();
        st32((unsigned)s, rw, fo, 0);
        TT(true, 4)
    }
}

#define STEP_ASM \
    "v_readfirstlane_b32 s36, v0\n" \
    "v_readfirstlane_b32 s37, v1\n" \
    "v_readfirstlane_b32 s38, v2\n" \
    "v_readfirstlane_b32 s39, v3\n" \
    "v_readfirstlane_b32 s40, v4\n" \
    "v_readfirstlane_b32 s41, v5\n" \
    "v_readfirstlane_b32 s42, v6\n" \
    "v_readfirstlane_b32 s43, v7\n" \
    "v_readfirstlane_b32 s28, v8\n" \
    "v_readfirstlane_b32 s29, v9\n" \
    "v_readfirstlane_b32 s30, v10\n" \
    "v_readfirstlane_b32 s31, v11\n" \
    "v_readfirstlane_b32 s32, v12\n" \
    "v_readfirstlane_b32 s33, v13\n" \
    "v_readfirstlane_b32 s34, v14\n" \
    "v_readfirstlane_b32 s35, v15\n" \
    "v_readfirstlane_b32 s24, v16\n" \
    "v_readfirstlane_b32 s25, v17\n" \
    "v_readfirstlane_b32 s26, v18\n" \
    "v_readfirstlane_b32 s27, v19\n" \
    "v_readfirstlane_b32 s60, v24\n" \
    "v_readfirstlane_b32 s61, v25\n" \
    "v_readfirstlane_b32 s62, v26\n" \
    "v_readfirstlane_b32 s63, v27\n" \
    "v_readfirstlane_b32 s64, v28\n" \
    "v_readfirstlane_b32 s65, v29\n" \
    "v_readfirstlane_b32 s66, v30\n" \
    "v_readfirstlane_b32 s67, v31\n" \
    "s_mov_b32 s68, m0\n" \
    "s_getpc_b64 s[96:97]\n" \
    "14:\n" \
    "s_add_u32 s96, s96, 2f-14b\n" \
    "s_addc_u32 s97, s97, 0\n" \
    "s_lshl_b32 s74, s60, 10\n" \
    "s_lshl_b32 s95, s74, 1\n" \
    "s_add_i32 s95, s95, 16384\n" \
    "s_mov_b32 s81, 0x3e0293ee\n" \
    "s_mov_b32 s82, 0x53800000\n" \
    "s_lshl_b32 s83, s60, 6\n" \
    "s_and_b32 s83, s83, 128\n" \
    "v_add_u32 v109, s83, v131\n" \
    "v_sub_u32 v111, v131, s83\n" \
    "v_xor_b32 v112, 0x2000, v133\n" \
    "v_and_b32 v113, 0x3c0, v127\n" \
    "v_mad_u32_u24 v113, v113, 7, v127\n" \
    "s_bfe_u32 s83, s61, 0x2000b\n" \
    "s_and_b32 s83, s83, 3\n" \
    "v_lshl_add_u32 v113, s83, 7, v113\n" \
    "v_lshlrev_b32 v114, 1, v127\n" \
    "v_add_u32 v106, s63, v133\n" \
    "s_bfe_u32 s83, s61, 0x40009\n" \
    "s_bfe_u32 s80, s61, 0x20004\n" \
    "s_lshl2_add_u32 s83, s83, s80\n" \
    "s_mov_b64 s[72:73], 0\n" \
    "s_cmp_eq_u32 s43, 4\n" \
    "s_cbranch_scc1 20f\n" \
    "0:\n" \
    "s_and_b32 s75, s36, 3\n" \
    "s_lshl_b32 s75, s75, 16\n" \
    "s_add_i32 s69, s75, s26\n" \
    "s_xor_b32 s70, s69, 2048\n" \
    "s_andn2_b32 s71, s69, 0x1fff\n" \
    "s_add_i32 s71, s71, 32768\n" \
    "ds_read_b128 v[72:75], v133 offset:0\n" \
    "ds_read_b128 v[76:79], v133 offset:1024\n" \
    "ds_read_b128 v[80:83], v133 offset:2048\n" \
    "ds_read_b128 v[84:87], v133 offset:3072\n" \
    "ds_read_b128 v[88:91], v133 offset:16384\n" \
    "ds_read_b128 v[92:95], v133 offset:17408\n" \
    "v_mov_b64 v[40:41], 0\n" \
    "v_mov_b64 v[42:43], 0\n" \
    "v_mov_b64 v[44:45], 0\n" \
    "v_mov_b64 v[46:47], 0\n" \
    "v_mov_b64 v[48:49], 0\n" \
    "v_mov_b64 v[50:51], 0\n" \
    "v_mov_b64 v[52:53], 0\n" \
    "v_mov_b64 v[54:55], 0\n" \
    "v_mov_b64 v[56:57], 0\n" \
    "v_mov_b64 v[58:59], 0\n" \
    "v_mov_b64 v[60:61], 0\n" \
    "v_mov_b64 v[62:63], 0\n" \
    "v_mov_b64 v[64:65], 0\n" \
    "v_mov_b64 v[66:67], 0\n" \
    "v_mov_b64 v[68:69], 0\n" \
    "v_mov_b64 v[70:71], 0\n" \
    "v_add_u32 v108, s74, v127\n" \
    "v_xor_b32 v24, 0x400, v108\n" \
    "v_xor_b32 v28, 0x800, v108\n" \
    "v_xor_b32 v32, 0xc00, v108\n" \
    "v_and_b32 v22, 0x200, v127\n" \
    "s_and_b32 s76, s26, 0x800\n" \
    "v_mad_u32_u24 v22, v22, 3, v127\n" \
    "v_xor_b32 v22, s76, v22\n" \
    "s_movk_i32 m0, 0x2000\n" \
    "s_cmp_eq_u32 s36, 0\n" \
    "s_cbranch_scc0 17f\n" \
    "v_and_b32 v36, 0x380, v127\n" \
    "v_add_u32 v36, v36, v127\n" \
    "v_and_b32 v37, 0x300, v127\n" \
    "v_add_u32 v37, v37, v127\n" \
    "s_add_i32 s93, s64, 0x1000000\n" \
    "v_add_u32 v21, s95, v127\n" \
    "v_xor_b32 v17, 32, v119\n" \
    "v_xor_b32 v18, 64, v119\n" \
    "v_xor_b32 v19, 96, v119\n" \
    "s_waitcnt vmcnt(8) lgkmcnt(0)\n" \
    "s_barrier\n" \
    "ds_read_b64_tr_b16 v[40:41], v119 offset:0\n" \
    "ds_read_b64_tr_b16 v[42:43], v18 offset:512\n" \
    "ds_read_b64_tr_b16 v[44:45], v119 offset:8192\n" \
    "ds_read_b64_tr_b16 v[46:47], v18 offset:8704\n" \
    "ds_read_b64_tr_b16 v[48:49], v17 offset:0\n" \
    "ds_read_b64_tr_b16 v[50:51], v19 offset:512\n" \
    "ds_read_b64_tr_b16 v[52:53], v17 offset:8192\n" \
    "ds_read_b64_tr_b16 v[54:55], v19 offset:8704\n" \
    "ds_read_b64_tr_b16 v[56:57], v18 offset:0\n" \
    "ds_read_b64_tr_b16 v[58:59], v119 offset:512\n" \
    "ds_read_b64_tr_b16 v[60:61], v18 offset:8192\n" \
    "ds_read_b64_tr_b16 v[62:63], v119 offset:8704\n" \
    "s_add_i32 m0, s95, 0\n" \
    "s_nop 0\n" \
    "buffer_load_dwordx4 v36, %[rw], s64 offen sc1 lds\n" \
    "s_add_i32 m0, s95, 896\n" \
    "s_nop 0\n" \
    "buffer_load_dwordx4 v36, %[rw], s64 offen offset:128 sc1 lds\n" \
    "s_add_i32 m0, s95, 8192\n" \
    "s_nop 0\n" \
    "buffer_load_dwordx4 v37, %[rw], s93 offen sc1 lds\n" \
    "s_add_i32 m0, s95, 8960\n" \
    "s_nop 0\n" \
    "buffer_load_dwordx4 v37, %[rw], s93 offen offset:256 sc1 lds\n" \
    "s_waitcnt lgkmcnt(8)\n" \
    "ds_read_b64_tr_b16 v[64:65], v19 offset:0\n" \
    "ds_read_b64_tr_b16 v[66:67], v17 offset:512\n" \
    "ds_read_b64_tr_b16 v[68:69], v19 offset:8192\n" \
    "ds_read_b64_tr_b16 v[70:71], v17 offset:8704\n" \
    "s_waitcnt vmcnt(4) lgkmcnt(0)\n" \
    "s_barrier\n" \
    "ds_read_b64_tr_b16 v[0:1], v119 offset:32768\n" \
    "ds_read_b64_tr_b16 v[2:3], v18 offset:33280\n" \
    "ds_read_b64_tr_b16 v[4:5], v119 offset:40960\n" \
    "ds_read_b64_tr_b16 v[6:7], v18 offset:41472\n" \
    "ds_write_b128 v106, v[40:43] offset:0\n" \
    "ds_write_b128 v106, v[44:47] offset:1024\n" \
    "ds_read_b64_tr_b16 v[8:9], v17 offset:32768\n" \
    "ds_read_b64_tr_b16 v[10:11], v19 offset:33280\n" \
    "ds_read_b64_tr_b16 v[12:13], v17 offset:40960\n" \
    "ds_read_b64_tr_b16 v[14:15], v19 offset:41472\n" \
    "ds_write_b128 v106, v[48:51] offset:2048\n" \
    "ds_write_b128 v106, v[52:55] offset:3072\n" \
    "s_waitcnt lgkmcnt(8)\n" \
    "ds_read_b64_tr_b16 v[40:41], v18 offset:32768\n" \
    "ds_read_b64_tr_b16 v[42:43], v119 offset:33280\n" \
    "ds_read_b64_tr_b16 v[44:45], v18 offset:40960\n" \
    "ds_read_b64_tr_b16 v[46:47], v119 offset:41472\n" \
    "s_waitcnt lgkmcnt(6)\n" \
    "ds_read_b64_tr_b16 v[48:49], v19 offset:32768\n" \
    "ds_read_b64_tr_b16 v[50:51], v17 offset:33280\n" \
    "ds_read_b64_tr_b16 v[52:53], v19 offset:40960\n" \
    "ds_read_b64_tr_b16 v[54:55], v17 offset:41472\n" \
    "ds_write_b128 v106, v[56:59] offset:4096\n" \
    "ds_write_b128 v106, v[60:63] offset:5120\n" \
    "ds_write_b128 v106, v[64:67] offset:6144\n" \
    "ds_write_b128 v106, v[68:71] offset:7168\n" \
    "s_waitcnt lgkmcnt(4)\n" \
    "s_barrier\n" \
    "ds_write_b128 v106, v[0:3] offset:32768\n" \
    "ds_write_b128 v106, v[4:7] offset:33792\n" \
    "ds_write_b128 v106, v[8:11] offset:34816\n" \
    "ds_write_b128 v106, v[12:15] offset:35840\n" \
    "ds_write_b128 v106, v[40:43] offset:36864\n" \
    "ds_write_b128 v106, v[44:47] offset:37888\n" \
    "ds_write_b128 v106, v[48:51] offset:38912\n" \
    "ds_write_b128 v106, v[52:55] offset:39936\n" \
    "v_mov_b64 v[56:57], 0\n" \
    "v_mov_b64 v[58:59], 0\n" \
    "v_mov_b64 v[60:61], 0\n" \
    "v_mov_b64 v[62:63], 0\n" \
    "v_mov_b64 v[64:65], 0\n" \
    "v_mov_b64 v[66:67], 0\n" \
    "v_mov_b64 v[68:69], 0\n" \
    "v_mov_b64 v[70:71], 0\n" \
    "s_waitcnt lgkmcnt(6)\n" \
    "ds_read_b128 v[0:3], v108 offset:0\n" \
    "ds_read_b128 v[16:19], v108 offset:4096\n" \
    "s_waitcnt lgkmcnt(2)\n" \
    "v_mov_b64 v[40:41], 0\n" \
    "v_mov_b64 v[42:43], 0\n" \
    "v_mov_b64 v[44:45], 0\n" \
    "v_mov_b64 v[46:47], 0\n" \
    "v_mov_b64 v[48:49], 0\n" \
    "v_mov_b64 v[50:51], 0\n" \
    "v_mov_b64 v[52:53], 0\n" \
    "v_mov_b64 v[54:55], 0\n" \
    "73:\n" \
    "s_waitcnt vmcnt(0) lgkmcnt(0)\n" \
    "ds_read_b128 v[4:7], v21 offset:0\n" \
    "ds_read_b128 v[8:11], v21 offset:1024\n" \
    "ds_read_b128 v[12:15], v21 offset:8192\n" \
    "ds_read_b128 v[102:105], v21 offset:9216\n" \
    "v_mov_b32 v107, 0\n" \
    "s_waitcnt lgkmcnt(3)\n" \
    "v_max3_u32 v107, v107, v4, v5\n" \
    "v_max3_u32 v107, v107, v6, v7\n" \
    "s_waitcnt lgkmcnt(2)\n" \
    "v_max3_u32 v107, v107, v8, v9\n" \
    "v_max3_u32 v107, v107, v10, v11\n" \
    "s_waitcnt lgkmcnt(1)\n" \
    "v_max3_u32 v107, v107, v12, v13\n" \
    "v_max3_u32 v107, v107, v14, v15\n" \
    "s_waitcnt lgkmcnt(0)\n" \
    "v_max3_u32 v107, v107, v102, v103\n" \
    "v_max3_u32 v107, v107, v104, v105\n" \
    "v_cmp_eq_u32 vcc, -1, v107\n" \
    "s_add_i32 s39, s39, -1\n" \
    "s_cmp_lt_i32 s39, 0\n" \
    "s_nop 2\n" \
    "s_cbranch_vccnz 75f\n" \
    "74:\n" \
    "s_barrier\n" \
    "s_memrealtime s[88:89]\n" \
    "s_pack_ll_b32_b16 s80, 1, s42\n" \
    "v_mov_b32 v5, s80\n" \
    "s_cmp_eq_u32 s60, 0\n" \
    "s_cselect_b32 s75, 0x1010101, 0\n" \
    "s_waitcnt lgkmcnt(0)\n" \
    "s_lshl3_add_u32 s80, s88, s37\n" \
    "v_mov_b32 v4, s80\n" \
    "s_mov_b32 exec_lo, s75\n" \
    "s_mov_b32 exec_hi, 0\n" \
    "buffer_store_dwordx2 v[4:5], v127, %[rs], s61 offen sc1\n" \
    "s_mov_b64 exec, -1\n" \
    "s_branch 3f\n" \
    "75:\n" \
    "s_cbranch_scc1 74b\n" \
    "s_add_i32 m0, s95, 0\n" \
    "s_nop 0\n" \
    "buffer_load_dwordx4 v36, %[rw], s64 offen sc1 lds\n" \
    "s_add_i32 m0, s95, 896\n" \
    "s_nop 0\n" \
    "buffer_load_dwordx4 v36, %[rw], s64 offen offset:128 sc1 lds\n" \
    "s_add_i32 m0, s95, 8192\n" \
    "s_nop 0\n" \
    "buffer_load_dwordx4 v37, %[rw], s93 offen sc1 lds\n" \
    "s_add_i32 m0, s95, 8960\n" \
    "s_nop 0\n" \
    "buffer_load_dwordx4 v37, %[rw], s93 offen offset:256 sc1 lds\n" \
    "s_branch 73b\n" \
    "17:\n" \
    "s_sub_i32 s37, s37, s29\n" \
    "12:\n" \
    "1:\n" \
    "s_memtime vcc\n" \
    "s_mul_i32 s90, s37, s25\n" \
    "s_ashr_i32 s90, s90, 12\n" \
    "s_add_i32 s90, s90, s24\n" \
    "s_add_i32 s90, s90, -116\n" \
    "s_waitcnt lgkmcnt(0)\n" \
    "s_sub_i32 s75, s90, vcc_lo\n" \
    "s_cmp_gt_i32 s75, 112\n" \
    "s_cbranch_scc1 1b\n" \
    "s_max_i32 s75, s75, 0\n" \
    "s_and_b32 s75, s75, -4\n" \
    "s_sub_u32 vcc_lo, s96, s75\n" \
    "s_subb_u32 vcc_hi, s97, 0\n" \
    "s_setpc_b64 vcc\n" \
    "s_nop 0\n" \
    "s_nop 0\n" \
    "s_nop 0\n" \
    "s_nop 0\n" \
    "s_nop 0\n" \
    "s_nop 0\n" \
    "s_nop 0\n" \
    "s_nop 0\n" \
    "s_nop 0\n" \
    "s_nop 0\n" \
    "s_nop 0\n" \
    "s_nop 0\n" \
    "s_nop 0\n" \
    "s_nop 0\n" \
    "s_nop 0\n" \
    "s_nop 0\n" \
    "s_nop 0\n" \
    "s_nop 0\n" \
    "s_nop 0\n" \
    "s_nop 0\n" \
    "s_nop 0\n" \
    "s_nop 0\n" \
    "s_nop 0\n" \
    "s_nop 0\n" \
    "s_nop 0\n" \
    "s_nop 0\n" \
    "s_nop 0\n" \
    "s_nop 0\n" \
    "2:\n" \
    "buffer_load_dwordx4 v[0:3], v125, %[rs], s69 offen sc1\n" \
    "s_nop 3\n" \
    "s_nop 3\n" \
    "buffer_load_dwordx4 v[16:19], v125, %[rs], s70 offen sc1\n" \
    "s_mov_b32 exec_lo, s27\n" \
    "s_mov_b32 exec_hi, s27\n" \
    "buffer_load_dwordx4 v22, %[rs], s71 offen sc1 lds\n" \
    "s_mov_b64 exec, -1\n" \
    "v_bfe_u32 v20, v127, 8, 2\n" \
    "v_subrev_u32 v20, s60, v20\n" \
    "v_mul_i32_i24 v20, -1088, v20\n" \
    "v_add_u32 v20, v20, v129\n" \
    "v_lshrrev_b32 v21, 2, v127\n" \
    "s_mov_b32 exec_lo, s66\n" \
    "s_mov_b32 exec_hi, s66\n" \
    "ds_read2_b32 v[96:97], v20 offset0:0 offset1:144\n" \
    "s_mov_b64 exec, -1\n" \
    "s_lshl_b32 s80, s36, 16\n" \
    "s_add_i32 s80, s80, s67\n" \
    "s_sub_i32 s75, s37, s79\n" \
    "s_cmp_gt_i32 s75, 24\n" \
    "s_cselect_b32 s76, 2, 0\n" \
    "s_cmp_lt_i32 s75, 10\n" \
    "s_cselect_b32 s76, -6, s76\n" \
    "s_add_i32 s28, s28, s76\n" \
    "s_max_i32 s28, s28, 0\n" \
    "s_min_i32 s28, s28, 20\n" \
    "s_cmp_eq_u32 s60, 0\n" \
    "s_cselect_b32 s72, 0x1010101, 0\n" \
    "s_waitcnt lgkmcnt(0)\n" \
    "buffer_store_dword v96, v21, %[rw], s80 offen sc1\n" \
    "buffer_store_dword v97, v21, %[rw], s80 offen offset:256 sc1\n" \
    "3:\n" \
    "s_mov_b32 m0, s95\n" \
    "s_add_i32 s91, s36, 1\n" \
    "s_and_b32 s78, s36, 3\n" \
    "s_lshl_b32 s78, s78, 14\n" \
    "s_add_i32 s77, s78, s61\n" \
    "s_bitcmp1_b32 s36, 0\n" \
    "s_cselect_b32 s79, 0x8000, 0\n" \
    "s_lshl_b32 s92, s91, 18\n" \
    "s_add_i32 s92, s92, s64\n" \
    "s_add_i32 s78, s78, 0x1a0000\n" \
    "s_add_i32 s93, s92, 0x1000000\n" \
    "s_sub_i32 s76, s33, s42\n" \
    "s_ashr_i32 s76, s76, s32\n" \
    "s_add_i32 s42, s42, s76\n" \
    "s_lshr_b32 s76, s32, 8\n" \
    "s_sub_i32 s76, s76, s40\n" \
    "s_ashr_i32 s76, s76, 2\n" \
    "s_add_i32 s40, s40, s76\n" \
    "s_sub_i32 s76, s40, s24\n" \
    "s_min_i32 s94, s91, s38\n" \
    "s_lshl_b32 s94, s94, 18\n" \
    "s_andn2_b32 s80, 0x1000000, s66\n" \
    "s_add_i32 s94, s94, s64\n" \
    "s_add_i32 s94, s94, s80\n" \
    "s_add_i32 s75, s36, 2\n" \
    "s_cmp_lt_i32 s38, s75\n" \
    "s_cselect_b32 s75, s62, 0x1b0040\n" \
    "v_mov_b64 v[98:99], 0\n" \
    "v_mov_b32 v101, 0\n" \
    "s_pack_ll_b32_b16 s80, s91, s42\n" \
    "v_mov_b32 v103, s80\n" \
    "s_waitcnt vmcnt(4)\n" \
    "v_smfmac_f32_16x16x64_bf16 v[40:43], v[0:3], a[0:7], v124\n" \
    "ds_write_b128 v108, v[0:3] offset:0\n" \
    "v_smfmac_f32_16x16x64_bf16 v[44:47], v[0:3], a[8:15], v124\n" \
    "v_smfmac_f32_16x16x64_bf16 v[48:51], v[0:3], a[128:135], v124\n" \
    "v_smfmac_f32_16x16x64_bf16 v[52:55], v[0:3], a[136:143], v124\n" \
    "s_waitcnt vmcnt(3)\n" \
    "v_smfmac_f32_16x16x64_bf16 v[40:43], v[16:19], a[64:71], v124\n" \
    "ds_write_b128 v108, v[16:19] offset:4096\n" \
    "v_smfmac_f32_16x16x64_bf16 v[44:47], v[16:19], a[72:79], v124\n" \
    "s_waitcnt lgkmcnt(1)\n" \
    "s_barrier\n" \
    "v_smfmac_f32_16x16x64_bf16 v[48:51], v[16:19], a[192:199], v124\n" \
    "ds_read_b128 v[4:7], v24 offset:0\n" \
    "v_smfmac_f32_16x16x64_bf16 v[52:55], v[16:19], a[200:207], v124\n" \
    "ds_read_b128 v[8:11], v28 offset:0\n" \
    "v_smfmac_f32_16x16x64_bf16 v[56:59], v[0:3], v[72:79], v124\n" \
    "ds_read_b128 v[12:15], v32 offset:0\n" \
    "v_smfmac_f32_16x16x64_bf16 v[60:63], v[0:3], v[80:87], v124\n" \
    "s_waitcnt vmcnt(2) lgkmcnt(3)\n" \
    "s_barrier\n" \
    "s_memtime s[86:87]\n" \
    "v_smfmac_f32_16x16x64_bf16 v[56:59], v[16:19], v[168:175], v124\n" \
    "ds_read_b128 v[24:27], v24 offset:4096\n" \
    "v_smfmac_f32_16x16x64_bf16 v[60:63], v[16:19], v[176:183], v124\n" \
    "ds_read_b128 v[28:31], v28 offset:4096\n" \
    "s_waitcnt lgkmcnt(3)\n" \
    "v_smfmac_f32_16x16x64_bf16 v[40:43], v[4:7], a[16:23], v124\n" \
    "ds_read_b128 v[32:35], v32 offset:4096\n" \
    "v_smfmac_f32_16x16x64_bf16 v[40:43], v[8:11], a[32:39], v124\n" \
    "ds_read_b128 v[36:39], v126 offset:0\n" \
    "s_waitcnt lgkmcnt(4)\n" \
    "v_smfmac_f32_16x16x64_bf16 v[40:43], v[12:15], a[48:55], v124\n" \
    "buffer_load_dword v110, v114, %[rw], s94 offen sc1\n" \
    "v_smfmac_f32_16x16x64_bf16 v[44:47], v[4:7], a[24:31], v124\n" \
    "s_waitcnt lgkmcnt(2)\n" \
    "v_smfmac_f32_16x16x64_bf16 v[40:43], v[24:27], a[80:87], v124\n" \
    "v_smfmac_f32_16x16x64_bf16 v[40:43], v[28:31], a[96:103], v124\n" \
    "s_waitcnt lgkmcnt(0)\n" \
    "v_smfmac_f32_16x16x64_bf16 v[40:43], v[32:35], a[112:119], v124\n" \
    "ds_read_b128 v[72:75], v133 offset:18432\n" \
    "s_add_i32 s80, s86, s76\n" \
    "v_smfmac_f32_16x16x64_bf16 v[40:43], v[36:39], v[136:143], v124\n" \
    "ds_read_b128 v[76:79], v133 offset:19456\n" \
    "s_mul_hi_u32 s80, s80, s41\n" \
    "v_smfmac_f32_16x16x64_bf16 v[44:47], v[8:11], a[40:47], v124\n" \
    "ds_read_b128 v[80:83], v133 offset:32768\n" \
    "s_add_i32 s80, s80, s29\n" \
    "v_mov_b32 v102, s80\n" \
    "v_smfmac_f32_16x16x64_bf16 v[44:47], v[12:15], a[56:63], v124\n" \
    "ds_read_b128 v[84:87], v133 offset:33792\n" \
    "v_add_f32 v40, v40, v42\n" \
    "v_add_f32 v41, v41, v43\n" \
    "v_smfmac_f32_16x16x64_bf16 v[44:47], v[24:27], a[88:95], v124\n" \
    "ds_read_b128 v[20:23], v127 offset:19456\n" \
    "v_cmp_u_f32 vcc, v40, v41\n" \
    "v_smfmac_f32_16x16x64_bf16 v[44:47], v[28:31], a[104:111], v124\n" \
    "v_mul_f32 v40, s81, v40\n" \
    "v_smfmac_f32_16x16x64_bf16 v[44:47], v[32:35], a[120:127], v124\n" \
    "s_cbranch_vccnz 5f\n" \
    "6:\n" \
    "s_mov_b64 exec, s[72:73]\n" \
    "v_smfmac_f32_16x16x64_bf16 v[44:47], v[36:39], v[144:151], v124\n" \
    "buffer_store_dwordx2 v[102:103], v127, %[rs], s77 offen sc1\n" \
    "v_smfmac_f32_16x16x64_bf16 v[56:59], v[4:7], v[88:95], v124\n" \
    "s_mov_b64 exec, -1\n" \
    "s_waitcnt lgkmcnt(1)\n" \
    "v_mul_f32 v41, s81, v41\n" \
    "v_smfmac_f32_16x16x64_bf16 v[60:63], v[4:7], v[72:79], v124\n" \
    "ds_read_b128 v[88:91], v133 offset:34816\n" \
    "v_add_f32 v44, v44, v46\n" \
    "v_smfmac_f32_16x16x64_bf16 v[56:59], v[8:11], v[80:87], v124\n" \
    "ds_read_b128 v[92:95], v133 offset:35840\n" \
    "v_add_f32 v45, v45, v47\n" \
    "v_smfmac_f32_16x16x64_bf16 v[48:51], v[4:7], a[144:151], v124\n" \
    "ds_read_b128 v[72:75], v133 offset:49152\n" \
    "v_mul_f32 v44, s81, v44\n" \
    "v_smfmac_f32_16x16x64_bf16 v[52:55], v[4:7], a[152:159], v124\n" \
    "ds_read_b128 v[76:79], v133 offset:50176\n" \
    "v_mul_f32 v45, s81, v45\n" \
    "v_smfmac_f32_16x16x64_bf16 v[48:51], v[8:11], a[160:167], v124\n" \
    "ds_read_b128 v[0:3], v127 offset:16384\n" \
    "v_cvt_pk_bf16_f32 v107, v40, v44\n" \
    "v_smfmac_f32_16x16x64_bf16 v[52:55], v[8:11], a[168:175], v124\n" \
    "ds_read_b128 v[4:7], v127 offset:18432\n" \
    "s_waitcnt lgkmcnt(4)\n" \
    "v_cvt_pk_bf16_f32 v108, v41, v45\n" \
    "v_smfmac_f32_16x16x64_bf16 v[60:63], v[8:11], v[88:95], v124\n" \
    "ds_write2_b32 v128, v107, v108 offset0:0 offset1:80\n" \
    "v_smfmac_f32_16x16x64_bf16 v[60:63], v[12:15], v[248:255], v124\n" \
    "ds_read_b128 v[16:19], v127 offset:17408\n" \
    "s_waitcnt lgkmcnt(4)\n" \
    "v_smfmac_f32_16x16x64_bf16 v[56:59], v[12:15], v[72:79], v124\n" \
    "ds_read_b128 v[8:11], v127 offset:20480\n" \
    "v_smfmac_f32_16x16x64_bf16 v[48:51], v[12:15], a[176:183], v124\n" \
    "ds_read_b128 v[40:43], v127 offset:21504\n" \
    "v_smfmac_f32_16x16x64_bf16 v[52:55], v[12:15], a[184:191], v124\n" \
    "ds_read_b128 v[44:47], v127 offset:23552\n" \
    "v_smfmac_f32_16x16x64_bf16 v[48:51], v[24:27], a[208:215], v124\n" \
    "s_waitcnt lgkmcnt(4)\n" \
    "s_barrier\n" \
    "v_smfmac_f32_16x16x64_bf16 v[52:55], v[24:27], a[216:223], v124\n" \
    "ds_read_b128 v[72:75], v130 offset:0\n" \
    "v_smfmac_f32_16x16x64_bf16 v[48:51], v[28:31], a[224:231], v124\n" \
    "ds_read_b128 v[76:79], v130 offset:64\n" \
    "v_smfmac_f32_16x16x64_bf16 v[48:51], v[32:35], a[240:247], v124\n" \
    "ds_read_b128 v[80:83], v130 offset:128\n" \
    "v_smfmac_f32_16x16x64_bf16 v[48:51], v[36:39], v[152:159], v124\n" \
    "ds_read_b128 v[84:87], v130 offset:192\n" \
    "v_smfmac_f32_16x16x64_bf16 v[52:55], v[28:31], a[232:239], v124\n" \
    "ds_read_b128 v[12:15], v127 offset:22528\n" \
    "v_smfmac_f32_16x16x64_bf16 v[52:55], v[32:35], a[248:255], v124\n" \
    "v_add_f32 v48, v48, v50\n" \
    "v_smfmac_f32_16x16x64_bf16 v[52:55], v[36:39], v[160:167], v124\n" \
    "s_waitcnt lgkmcnt(3)\n" \
    "v_add_f32 v49, v49, v51\n" \
    "v_mfma_f32_16x16x32_bf16 v[88:91], v[0:3], v[72:75], 0\n" \
    "ds_read_b64 v[0:1], v132 offset:0\n" \
    "ds_read_b64 v[2:3], v132 offset:1024\n" \
    "v_mfma_f32_16x16x32_bf16 v[88:91], v[4:7], v[76:79], v[88:91]\n" \
    "s_waitcnt lgkmcnt(2)\n" \
    "v_add_f32 v52, v52, v54\n" \
    "v_add_f32 v53, v53, v55\n" \
    "v_mfma_f32_16x16x32_bf16 v[88:91], v[8:11], v[80:83], v[88:91]\n" \
    "v_cvt_pk_bf16_f32 v107, v48, v52\n" \
    "v_cvt_pk_bf16_f32 v108, v49, v53\n" \
    "v_mfma_f32_16x16x32_bf16 v[88:91], v[12:15], v[84:87], v[88:91]\n" \
    "ds_write2_b32 v129, v107, v108 offset0:0 offset1:144\n" \
    "v_mfma_f32_16x16x32_bf16 v[92:95], v[16:19], v[72:75], 0\n" \
    "ds_read_b64 v[4:5], v132 offset:512\n" \
    "ds_read_b64 v[6:7], v132 offset:1536\n" \
    "v_mfma_f32_16x16x32_bf16 v[92:95], v[20:23], v[76:79], v[92:95]\n" \
    "ds_read_b128 v[8:11], v106 offset:0\n" \
    "v_exp_f32 v88, v88\n" \
    "v_mfma_f32_16x16x32_bf16 v[92:95], v[40:43], v[80:83], v[92:95]\n" \
    "ds_read_b128 v[12:15], v106 offset:1024\n" \
    "v_exp_f32 v89, v89\n" \
    "v_mfma_f32_16x16x32_bf16 v[92:95], v[44:47], v[84:87], v[92:95]\n" \
    "s_waitcnt lgkmcnt(4)\n" \
    "s_barrier\n" \
    "v_smfmac_f32_16x16x64_bf16 v[56:59], v[24:27], v[184:191], v124\n" \
    "ds_read_b128 v[48:51], v131 offset:0\n" \
    "ds_read_b128 v[52:55], v131 offset:128\n" \
    "v_cvt_pk_bf16_f32 v88, v88, v89\n" \
    "v_smfmac_f32_16x16x64_bf16 v[56:59], v[28:31], v[200:207], v124\n" \
    "ds_read_b128 v[16:19], v106 offset:32768\n" \
    "v_exp_f32 v90, v90\n" \
    "v_smfmac_f32_16x16x64_bf16 v[56:59], v[32:35], v[216:223], v124\n" \
    "ds_read_b128 v[20:23], v106 offset:33792\n" \
    "v_exp_f32 v91, v91\n" \
    "v_smfmac_f32_16x16x64_bf16 v[56:59], v[36:39], v[232:239], v124\n" \
    "ds_read_b128 v[40:43], v106 offset:2048\n" \
    "s_waitcnt lgkmcnt(3)\n" \
    "v_exp_f32 v92, v92\n" \
    "v_cvt_pk_bf16_f32 v89, v90, v91\n" \
    "v_smfmac_f32_16x16x64_bf16 v[64:67], v[48:51], v[72:79], v124\n" \
    "ds_read_b128 v[44:47], v106 offset:3072\n" \
    "v_exp_f32 v93, v93\n" \
    "v_smfmac_f32_16x16x64_bf16 v[64:67], v[52:55], v[80:87], v124\n" \
    "ds_read_b128 v[72:75], v106 offset:34816\n" \
    "v_exp_f32 v94, v94\n" \
    "v_cvt_pk_bf16_f32 v90, v92, v93\n" \
    "v_smfmac_f32_16x16x64_bf16 v[60:63], v[24:27], v[192:199], v124\n" \
    "ds_read_b128 v[76:79], v106 offset:35840\n" \
    "v_exp_f32 v95, v95\n" \
    "v_smfmac_f32_16x16x64_bf16 v[60:63], v[28:31], v[208:215], v124\n" \
    "ds_read_b128 v[80:83], v106 offset:4096\n" \
    "s_cmp_eq_u32 s36, 62\n" \
    "v_add_f32 v64, v64, v66\n" \
    "v_add_f32 v65, v65, v67\n" \
    "v_smfmac_f32_16x16x64_bf16 v[60:63], v[32:35], v[224:231], v124\n" \
    "ds_read_b128 v[84:87], v106 offset:5120\n" \
    "v_exp_f32 v64, v64\n" \
    "v_cvt_pk_bf16_f32 v91, v94, v95\n" \
    "v_smfmac_f32_16x16x64_bf16 v[60:63], v[36:39], v[240:247], v124\n" \
    "ds_read_b128 v[48:51], v106 offset:36864\n" \
    "v_exp_f32 v65, v65\n" \
    "v_swap_b32 v1, v2\n" \
    "v_swap_b32 v5, v6\n" \
    "v_mfma_f32_16x16x32_bf16 v[102:105], v[88:91], v[120:123], 0\n" \
    "ds_read_b128 v[52:55], v106 offset:37888\n" \
    "v_cvt_pk_bf16_f32 v100, v64, v65\n" \
    "v_add_f32 v56, v56, v58\n" \
    "v_add_f32 v57, v57, v59\n" \
    "v_smfmac_f32_16x16x64_bf16 v[68:71], v[88:91], v[0:7], v124\n" \
    "ds_read_b128 v[24:27], v106 offset:6144\n" \
    "v_add_f32 v60, v60, v62\n" \
    "v_add_f32 v61, v61, v63\n" \
    "v_mfma_f32_16x16x16_bf16 v[102:105], v[100:101], v[120:121], v[102:105]\n" \
    "ds_read_b128 v[28:31], v106 offset:7168\n" \
    "v_cvt_pk_bf16_f32 v96, v56, v57\n" \
    "v_cvt_pk_bf16_f32 v97, v60, v61\n" \
    "s_nop 1\n" \
    "v_smfmac_f32_16x16x32_bf16 v[68:71], v[100:101], v[96:99], v124\n" \
    "ds_read_b128 v[32:35], v106 offset:38912\n" \
    "v_mov_b64 v[56:57], 0\n" \
    "v_mov_b64 v[58:59], 0\n" \
    "v_rcp_f32 v104, v102\n" \
    "v_rcp_f32 v105, v103\n" \
    "v_mul_f32 v68, v68, v104\n" \
    "v_mul_f32 v70, v70, v104\n" \
    "v_mul_f32 v69, v69, v105\n" \
    "v_mul_f32 v71, v71, v105\n" \
    "v_cvt_pk_bf16_f32 v107, v68, v70\n" \
    "v_cvt_pk_bf16_f32 v108, v69, v71\n" \
    "ds_write2_b32 v129, v107, v108 offset0:64 offset1:208\n" \
    "v_max3_f32 v102, v102, v103, v104\n" \
    "v_max_f32 v102, v102, v105\n" \
    "v_cmp_nge_f32 vcc, s82, v102\n" \
    "v_mov_b64 v[60:61], 0\n" \
    "v_mov_b64 v[62:63], 0\n" \
    "v_mov_b64 v[88:89], 0\n" \
    "v_mov_b64 v[90:91], 0\n" \
    "v_mov_b64 v[92:93], 0\n" \
    "v_mov_b64 v[94:95], 0\n" \
    "s_waitcnt vmcnt(2) lgkmcnt(0)\n" \
    "s_barrier\n" \
    "ds_read_b128 v[68:71], v109 offset:256\n" \
    "ds_read_b128 v[102:105], v111 offset:384\n" \
    "s_cbranch_vccnz 7f\n" \
    "buffer_load_dwordx2 v[98:99], v113, %[rs], s78 offen sc1\n" \
    "buffer_load_dword v100, off, %[rw], s75 sc1\n" \
    "v_mov_b64 v[64:65], 0\n" \
    "v_mov_b64 v[66:67], 0\n" \
    "s_waitcnt lgkmcnt(1)\n" \
    "v_smfmac_f32_16x16x64_bf16 v[56:59], v[68:71], v[8:15], v124\n" \
    "s_waitcnt lgkmcnt(0)\n" \
    "ds_read_b128 v[36:39], v106 offset:39936\n" \
    "v_smfmac_f32_16x16x64_bf16 v[56:59], v[102:105], v[16:23], v124\n" \
    "ds_read_b128 v[0:3], v133 offset:51200\n" \
    "v_smfmac_f32_16x16x64_bf16 v[60:63], v[68:71], v[40:47], v124\n" \
    "ds_read_b128 v[4:7], v133 offset:52224\n" \
    "v_smfmac_f32_16x16x64_bf16 v[60:63], v[102:105], v[72:79], v124\n" \
    "ds_read_b128 v[16:19], v112 offset:51200\n" \
    "v_add_f32 v56, v56, v58\n" \
    "v_smfmac_f32_16x16x64_bf16 v[88:91], v[68:71], v[80:87], v124\n" \
    "ds_read_b128 v[20:23], v112 offset:52224\n" \
    "v_add_f32 v57, v57, v59\n" \
    "v_smfmac_f32_16x16x64_bf16 v[88:91], v[102:105], v[48:55], v124\n" \
    "v_add_f32 v60, v60, v62\n" \
    "v_smfmac_f32_16x16x64_bf16 v[92:95], v[68:71], v[24:31], v124\n" \
    "s_waitcnt lgkmcnt(4)\n" \
    "v_add_f32 v61, v61, v63\n" \
    "v_smfmac_f32_16x16x64_bf16 v[92:95], v[102:105], v[32:39], v124\n" \
    "s_waitcnt lgkmcnt(2)\n" \
    "v_add_f32 v88, v88, v90\n" \
    "v_smfmac_f32_16x16x64_bf16 v[64:67], v[68:71], v[0:7], v124\n" \
    "s_waitcnt lgkmcnt(0)\n" \
    "v_add_f32 v89, v89, v91\n" \
    "v_smfmac_f32_16x16x64_bf16 v[64:67], v[102:105], v[16:23], v124\n" \
    "v_add_f32 v92, v92, v94\n" \
    "v_add_f32 v93, v93, v95\n" \
    "v_cvt_pk_bf16_f32 v8, v56, v60\n" \
    "v_cvt_pk_bf16_f32 v10, v57, v61\n" \
    "v_cvt_pk_bf16_f32 v9, v88, v92\n" \
    "v_cvt_pk_bf16_f32 v11, v89, v93\n" \
    "buffer_store_dwordx4 v[8:11], v134, %[rs], s79 offen sc1\n" \
    "v_add_f32 v64, v64, v66\n" \
    "v_add_f32 v65, v65, v67\n" \
    "v_cvt_pk_bf16_f32 v64, v64, v65\n" \
    "buffer_store_dword v64, v135, %[rs], s79 offen sc1\n" \
    "s_cbranch_scc1 16f\n" \
    "s_memtime s[88:89]\n" \
    "s_memrealtime s[44:45]\n" \
    "s_and_b32 s75, s91, 3\n" \
    "s_lshl_b32 s75, s75, 16\n" \
    "s_memrealtime s[46:47]\n" \
    "s_add_i32 s69, s75, s26\n" \
    "s_xor_b32 s70, s69, 2048\n" \
    "s_memrealtime s[48:49]\n" \
    "s_andn2_b32 s71, s69, 0x1fff\n" \
    "s_add_i32 s71, s71, 32768\n" \
    "s_memrealtime s[50:51]\n" \
    "s_mov_b32 s94, 0\n" \
    "s_mov_b32 s43, 0\n" \
    "s_memrealtime s[52:53]\n" \
    "s_cmp_lt_i32 s38, s91\n" \
    "s_nop 0\n" \
    "s_memrealtime s[54:55]\n" \
    "s_nop 1\n" \
    "s_memrealtime s[56:57]\n" \
    "s_nop 1\n" \
    "s_memrealtime vcc\n" \
    "v_and_b32 v19, 0x380, v127\n" \
    "v_add_u32 v19, v19, v127\n" \
    "v_and_b32 v18, 0x300, v127\n" \
    "v_add_u32 v18, v18, v127\n" \
    "s_cbranch_scc1 40f\n" \
    "41:\n" \
    "buffer_load_dwordx4 v19, %[rw], s92 offen sc1 lds\n" \
    "s_add_i32 m0, s95, 896\n" \
    "s_waitcnt lgkmcnt(0)\n" \
    "s_add_i32 s44, s44, s46\n" \
    "s_add_i32 s48, s48, s50\n" \
    "s_add_i32 s52, s52, s54\n" \
    "s_add_i32 s44, s44, s48\n" \
    "s_add_i32 s52, s52, s56\n" \
    "s_add_i32 s44, s44, s52\n" \
    "s_add_i32 s80, s44, vcc_lo\n" \
    "s_sub_i32 s45, s80, s29\n" \
    "s_mov_b32 s29, s80\n" \
    "s_sub_i32 s47, s88, s24\n" \
    "s_mov_b32 s24, s88\n" \
    "buffer_load_dwordx4 v19, %[rw], s92 offen offset:128 sc1 lds\n" \
    "s_add_i32 m0, s95, 8192\n" \
    "s_add_i32 s76, s29, s42\n" \
    "v_add_u32 v16, -1, s36\n" \
    "v_mov_b32 v17, s76\n" \
    "s_mov_b64 exec, s[72:73]\n" \
    "buffer_store_dwordx2 v[16:17], off, %[rw], s65 sc1\n" \
    "s_mov_b64 exec, -1\n" \
    "ds_read_b128 v[72:75], v133 offset:0\n" \
    "ds_read_b128 v[76:79], v133 offset:1024\n" \
    "ds_read_b128 v[80:83], v133 offset:2048\n" \
    "ds_read_b128 v[84:87], v133 offset:3072\n" \
    "ds_read_b128 v[88:91], v133 offset:16384\n" \
    "ds_read_b128 v[92:95], v133 offset:17408\n" \
    "buffer_load_dwordx4 v18, %[rw], s93 offen sc1 lds\n" \
    "s_add_i32 m0, s95, 8960\n" \
    "v_mov_b64 v[40:41], 0\n" \
    "v_mov_b64 v[42:43], 0\n" \
    "v_mov_b64 v[44:45], 0\n" \
    "v_mov_b64 v[46:47], 0\n" \
    "v_mov_b64 v[48:49], 0\n" \
    "v_mov_b64 v[50:51], 0\n" \
    "v_mov_b64 v[52:53], 0\n" \
    "v_mov_b64 v[54:55], 0\n" \
    "buffer_load_dwordx4 v18, %[rw], s93 offen offset:256 sc1 lds\n" \
    "s_movk_i32 m0, 0x2000\n" \
    "v_mov_b64 v[56:57], 0\n" \
    "v_mov_b64 v[58:59], 0\n" \
    "v_mov_b64 v[60:61], 0\n" \
    "v_mov_b64 v[62:63], 0\n" \
    "v_mov_b64 v[64:65], 0\n" \
    "v_mov_b64 v[66:67], 0\n" \
    "v_mov_b64 v[68:69], 0\n" \
    "v_mov_b64 v[70:71], 0\n" \
    "v_add_u32 v108, s74, v127\n" \
    "v_xor_b32 v24, 0x400, v108\n" \
    "v_xor_b32 v28, 0x800, v108\n" \
    "v_xor_b32 v32, 0xc00, v108\n" \
    "v_and_b32 v22, 0x200, v127\n" \
    "s_and_b32 s76, s26, 0x800\n" \
    "v_mad_u32_u24 v22, v22, 3, v127\n" \
    "v_xor_b32 v22, s76, v22\n" \
    "s_waitcnt vmcnt(7)\n" \
    "v_cmp_ne_u16 vcc, s91, v99\n" \
    "v_readfirstlane_b32 s80, v100\n" \
    "s_max_i32 s38, s38, s80\n" \
    "s_cbranch_vccnz 15f\n" \
    "10:\n" \
    "v_subrev_u32 v20, s29, v98\n" \
    "s_bitcmp1_b32 s30, 15\n" \
    "v_min_i32 v20, 0x4000, v20\n" \
    "v_max_i32 v20, 0xffffc000, v20\n" \
    "s_cbranch_scc1 18f\n" \
    "19:\n" \
    "v_readlane_b32 s79, v20, s83\n" \
    "v_max_i32_dpp v20, v20, v20 row_shr:1 row_mask:0xf bank_mask:0xf\n" \
    "s_mul_i32 s49, s45, s25\n" \
    "s_lshr_b32 s49, s49, 12\n" \
    "v_max_i32_dpp v20, v20, v20 row_shr:2 row_mask:0xf bank_mask:0xf\n" \
    "s_sub_i32 s49, s47, s49\n" \
    "s_mul_hi_u32 s47, s47, s41\n" \
    "v_max_i32_dpp v20, v20, v20 row_shr:4 row_mask:0xf bank_mask:0xf\n" \
    "s_sub_i32 s47, s45, s47\n" \
    "s_lshl_b32 s47, s47, 18\n" \
    "v_max_i32_dpp v20, v20, v20 row_shr:8 row_mask:0xf bank_mask:0xf\n" \
    "s_cmp_lt_u32 s45, 3000\n" \
    "s_cselect_b32 s49, s49, 0\n" \
    "v_max_i32_dpp v20, v20, v20 row_bcast:15 row_mask:0xa bank_mask:0xf\n" \
    "s_cselect_b32 s47, s47, 0\n" \
    "s_add_i32 s25, s25, s49\n" \
    "v_max_i32_dpp v20, v20, v20 row_bcast:31 row_mask:0xc bank_mask:0xf\n" \
    "s_add_i32 s41, s41, s47\n" \
    "s_add_i32 s76, s42, s28\n" \
    "s_add_i32 s79, s79, s76\n" \
    "v_readlane_b32 s37, v20, 63\n" \
    "s_add_i32 s37, s37, s76\n" \
    "s_cmp_lg_u32 s94, 0\n" \
    "s_cselect_b32 s36, s36, s91\n" \
    "s_cbranch_scc0 12b\n" \
    "s_branch 9f\n" \
    "5:\n" \
    "v_mov_b32 v107, 0\n" \
    "v_max3_u32 v107, v107, v0, v1\n" \
    "v_max3_u32 v107, v107, v2, v3\n" \
    "v_max3_u32 v107, v107, v4, v5\n" \
    "v_max3_u32 v107, v107, v6, v7\n" \
    "v_max3_u32 v107, v107, v8, v9\n" \
    "v_max3_u32 v107, v107, v10, v11\n" \
    "v_max3_u32 v107, v107, v12, v13\n" \
    "v_max3_u32 v107, v107, v14, v15\n" \
    "v_max3_u32 v107, v107, v16, v17\n" \
    "v_max3_u32 v107, v107, v18, v19\n" \
    "v_max3_u32 v107, v107, v24, v25\n" \
    "v_max3_u32 v107, v107, v26, v27\n" \
    "v_max3_u32 v107, v107, v28, v29\n" \
    "v_max3_u32 v107, v107, v30, v31\n" \
    "v_max3_u32 v107, v107, v32, v33\n" \
    "v_max3_u32 v107, v107, v34, v35\n" \
    "v_max3_u32 v107, v107, v36, v37\n" \
    "v_max3_u32 v107, v107, v38, v39\n" \
    "v_cmp_eq_u32 vcc, -1, v107\n" \
    "s_nop 4\n" \
    "s_cbranch_vccz 6b\n" \
    "s_cmp_eq_u32 s36, 0\n" \
    "s_cbranch_scc1 6b\n" \
    "s_add_i32 s39, s39, -1\n" \
    "s_cmp_lt_i32 s39, 0\n" \
    "s_cbranch_scc1 6b\n" \
    "s_add_i32 s30, s30, 1\n" \
    "s_bitset1_b32 s30, 15\n" \
    "s_and_b32 s80, s31, 0xff\n" \
    "s_add_i32 s42, s42, s80\n" \
    "s_bfe_u32 s80, s31, 0x80008\n" \
    "s_add_i32 s40, s40, s80\n" \
    "s_waitcnt lgkmcnt(0)\n" \
    "s_mov_b32 s37, s29\n" \
    "s_nop 7\n" \
    "s_barrier\n" \
    "s_branch 0b\n" \
    "15:\n" \
    "s_add_i32 s30, s30, 0x10000\n" \
    "s_add_i32 s39, s39, -1\n" \
    "s_cmp_lt_i32 s39, 0\n" \
    "s_cbranch_scc1 10b\n" \
    "s_sleep 9\n" \
    "buffer_load_dwordx2 v[98:99], v113, %[rs], s78 offen sc1\n" \
    "s_waitcnt vmcnt(0)\n" \
    "v_cmp_ne_u16 vcc, s91, v99\n" \
    "s_nop 1\n" \
    "s_cbranch_vccnz 15b\n" \
    "s_memtime s[88:89]\n" \
    "s_memrealtime s[44:45]\n" \
    "s_nop 1\n" \
    "s_memrealtime s[46:47]\n" \
    "s_nop 1\n" \
    "s_memrealtime s[48:49]\n" \
    "s_nop 1\n" \
    "s_memrealtime s[50:51]\n" \
    "s_nop 1\n" \
    "s_memrealtime s[52:53]\n" \
    "s_nop 1\n" \
    "s_memrealtime s[54:55]\n" \
    "s_nop 1\n" \
    "s_memrealtime s[56:57]\n" \
    "s_nop 1\n" \
    "s_memrealtime vcc\n" \
    "s_waitcnt lgkmcnt(0)\n" \
    "s_add_i32 s44, s44, s46\n" \
    "s_add_i32 s48, s48, s50\n" \
    "s_add_i32 s52, s52, s54\n" \
    "s_add_i32 s44, s44, s48\n" \
    "s_add_i32 s52, s52, s56\n" \
    "s_add_i32 s44, s44, s52\n" \
    "s_add_i32 s29, s44, vcc_lo\n" \
    "s_mov_b32 s24, s88\n" \
    "s_mov_b32 s45, -1\n" \
    "s_branch 10b\n" \
    "18:\n" \
    "v_cmp_gt_i32 vcc, -320, v20\n" \
    "s_bitset0_b32 s30, 15\n" \
    "s_lshr_b32 s80, s31, 16\n" \
    "s_cmp_lt_u32 s36, 4\n" \
    "s_cselect_b32 s80, 0, s80\n" \
    "s_min_u32 s76, s80, 1\n" \
    "s_lshl_b32 s76, s76, 16\n" \
    "s_sub_i32 s31, s31, s76\n" \
    "s_mul_i32 s76, s80, 768\n" \
    "s_cmp_eq_u64 vcc, 0\n" \
    "s_cselect_b32 s80, 0, s80\n" \
    "s_cselect_b32 s76, s76, 0\n" \
    "s_add_i32 s33, s33, s80\n" \
    "s_add_i32 s32, s32, s76\n" \
    "s_branch 19b\n" \
    "40:\n" \
    "s_waitcnt vmcnt(2)\n" \
    "v_readfirstlane_b32 s80, v100\n" \
    "s_max_i32 s38, s38, s80\n" \
    "s_cmp_lt_i32 s38, s91\n" \
    "s_cbranch_scc0 41b\n" \
    "s_mov_b32 s94, 8\n" \
    "v_mov_b32 v18, 0x7ffffff0\n" \
    "v_mov_b32 v19, 0x7ffffff0\n" \
    "s_branch 41b\n" \
    "20:\n" \
    "s_mov_b32 s91, 63\n" \
    "s_mov_b32 s78, 0x1a8000\n" \
    "buffer_load_dwordx2 v[98:99], v113, %[rs], s78 offen sc1\n" \
    "buffer_load_dword v100, off, %[rw], s62 sc1\n" \
    "s_waitcnt vmcnt(0)\n" \
    "16:\n" \
    "v_and_b32 v112, 0x300, v127\n" \
    "v_add_u32 v112, v112, v127\n" \
    "s_add_i32 s44, %[qb], 0\n" \
    "s_add_i32 s45, s44, 4096\n" \
    "buffer_load_dwordx4 v[136:139], v112, %[rw], s44 offen offset:0 sc1\n" \
    "v_lshrrev_b32 v103, 1, v127\n" \
    "s_lshr_b32 s76, s74, 1\n" \
    "v_add_u32 v101, s76, v103\n" \
    "ds_write_b64 v101, v[96:97] offset:9216\n" \
    "ds_read_b128 v[0:3], v127 offset:16384\n" \
    "v_lshrrev_b32 v42, 8, v127\n" \
    "v_bfe_u32 v40, v127, 4, 4\n" \
    "ds_read_b128 v[4:7], v127 offset:17408\n" \
    "v_and_b32 v41, 2, v42\n" \
    "v_add_u32 v43, v41, v42\n" \
    "ds_read_b128 v[8:11], v127 offset:18432\n" \
    "s_lshl_b32 s76, s60, 4\n" \
    "s_and_b32 s76, s76, 16\n" \
    "v_add_u32 v40, s76, v40\n" \
    "v_mad_i32_i24 v40, v43, -4, v40\n" \
    "buffer_load_dwordx4 v[140:143], v112, %[rw], s44 offen offset:2048 sc1\n" \
    "ds_read_b128 v[12:15], v127 offset:19456\n" \
    "v_mov_b32 v43, 0xff61b1e6\n" \
    "v_cmp_gt_i32 vcc, 0, v40\n" \
    "ds_read_b128 v[16:19], v127 offset:20480\n" \
    "v_cndmask_b32 v88, 0, v43, vcc\n" \
    "v_cmp_gt_i32 vcc, 1, v40\n" \
    "ds_read_b128 v[20:23], v127 offset:21504\n" \
    "v_cndmask_b32 v89, 0, v43, vcc\n" \
    "v_cmp_gt_i32 vcc, 2, v40\n" \
    "ds_read_b128 v[24:27], v127 offset:22528\n" \
    "v_cndmask_b32 v90, 0, v43, vcc\n" \
    "v_cmp_gt_i32 vcc, 3, v40\n" \
    "buffer_load_dwordx4 v[144:147], v112, %[rw], s45 offen offset:0 sc1\n" \
    "ds_read_b128 v[28:31], v127 offset:23552\n" \
    "v_cndmask_b32 v91, 0, v43, vcc\n" \
    "v_cmp_gt_i32 vcc, 8, v40\n" \
    "ds_read_b128 v[32:35], v131 offset:0\n" \
    "v_cndmask_b32 v92, 0, v43, vcc\n" \
    "v_cmp_gt_i32 vcc, 9, v40\n" \
    "ds_read_b128 v[36:39], v131 offset:128\n" \
    "v_cndmask_b32 v93, 0, v43, vcc\n" \
    "v_cmp_gt_i32 vcc, 10, v40\n" \
    "ds_read_b128 v[56:59], v127 offset:24576\n" \
    "v_cndmask_b32 v94, 0, v43, vcc\n" \
    "v_cmp_gt_i32 vcc, 11, v40\n" \
    "buffer_load_dwordx4 v[148:151], v112, %[rw], s45 offen offset:2048 sc1\n" \
    "ds_read_b128 v[60:63], v127 offset:25600\n" \
    "v_cndmask_b32 v95, 0, v43, vcc\n" \
    "v_mad_u32_u24 v44, v41, 3, v42\n" \
    "ds_read_b128 v[64:67], v127 offset:26624\n" \
    "v_bfe_u32 v45, v127, 6, 1\n" \
    "v_bfe_u32 v46, v127, 4, 2\n" \
    "s_waitcnt lgkmcnt(8)\n" \
    "ds_read_b128 v[68:71], v127 offset:27648\n" \
    "v_lshl_add_u32 v44, v46, 1, v44\n" \
    "v_bfe_u32 v46, v127, 7, 1\n" \
    "ds_read_b128 a[120:123], v127 offset:28672\n" \
    "v_lshl_add_u32 v44, v44, 1, v46\n" \
    "v_mul_u32_u24 v44, 4352, v44\n" \
    "ds_read_b128 a[124:127], v127 offset:29696\n" \
    "v_lshl_add_u32 v44, v45, 1, v44\n" \
    "v_lshlrev_b32 v45, 2, v45\n" \
    "ds_read_b128 v[80:83], v127 offset:30720\n" \
    "s_lshr_b32 s79, %[u], 4\n" \
    "s_mul_i32 s79, s79, 32 * 4352\n" \
    "s_and_b32 s94, %[u], 15\n" \
    "s_lshl2_add_u32 s79, s94, s79\n" \
    "s_add_i32 s79, s79, 0x3e00fc0\n" \
    "s_lshl3_add_u32 s94, s94, 0x4065f80\n" \
    "s_lshr_b32 s80, s60, 1\n" \
    "s_mul_i32 s76, s80, 49152\n" \
    "s_add_i32 s76, s76, 98304\n" \
    "v_add_u32 v128, s76, v127\n" \
    "v_xor_b32 v130, 0x38000, v128\n" \
    "ds_read_b128 v[84:87], v127 offset:31744\n" \
    "s_lshl_b32 s76, s80, 12\n" \
    "s_add_i32 s76, s76, 83968\n" \
    "v_add_u32 v132, s76, v127\n" \
    "s_add_i32 s92, s64, 16515072\n" \
    "s_add_i32 s93, s92, 0x1000000\n" \
    "s_mov_b32 m0, s95\n" \
    "v_and_b32 v97, 0x380, v127\n" \
    "v_add_u32 v97, v97, v127\n" \
    "s_waitcnt vmcnt(6)\n" \
    "v_readfirstlane_b32 s80, v100\n" \
    "v_cmp_ne_u16 vcc, s91, v99\n" \
    "s_max_i32 s38, s38, s80\n" \
    "s_cmp_lt_i32 s38, s91\n" \
    "s_cbranch_scc1 21f\n" \
    "22:\n" \
    "s_cbranch_vccnz 23f\n" \
    "24:\n" \
    "buffer_load_ushort v113, v44, %[rw], s79 offen sc1\n" \
    "buffer_load_dword v114, v45, %[rw], s94 offen sc1\n" \
    "v_subrev_u32 v96, s29, v98\n" \
    "v_min_i32 v96, 0x4000, v96\n" \
    "v_max_i32 v96, 0, v96\n" \
    "s_waitcnt lgkmcnt(0)\n" \
    "s_barrier\n" \
    "v_max_i32_dpp v96, v96, v96 row_shr:1 row_mask:0xf bank_mask:0xf\n" \
    "ds_read_b64 v[52:53], v103 offset:9216\n" \
    "s_and_b32 s75, s91, 3\n" \
    "v_max_i32_dpp v96, v96, v96 row_shr:2 row_mask:0xf bank_mask:0xf\n" \
    "ds_read_b64 v[54:55], v103 offset:9728\n" \
    "s_lshl_b32 s75, s75, 16\n" \
    "v_max_i32_dpp v96, v96, v96 row_shr:4 row_mask:0xf bank_mask:0xf\n" \
    "ds_read_b64 v[100:101], v103 offset:10240\n" \
    "s_add_i32 s69, s75, s26\n" \
    "v_max_i32_dpp v96, v96, v96 row_shr:8 row_mask:0xf bank_mask:0xf\n" \
    "ds_read_b64 v[102:103], v103 offset:10752\n" \
    "s_xor_b32 s70, s69, 2048\n" \
    "v_max_i32_dpp v96, v96, v96 row_bcast:15 row_mask:0xa bank_mask:0xf\n" \
    "ds_read_b128 a[0:3], v128 offset:0\n" \
    "s_andn2_b32 s71, s69, 0x1fff\n" \
    "v_max_i32_dpp v96, v96, v96 row_bcast:31 row_mask:0xc bank_mask:0xf\n" \
    "ds_read_b128 a[16:19], v128 offset:2048\n" \
    "s_add_i32 s71, s71, 32768\n" \
    "v_readlane_b32 s90, v96, 63\n" \
    "s_add_i32 s90, s90, s42\n" \
    "s_mul_i32 s90, s90, s25\n" \
    "s_ashr_i32 s90, s90, 12\n" \
    "s_add_i32 s90, s90, s24\n" \
    "buffer_load_dwordx4 v97, %[rw], s92 offen sc1 lds\n" \
    "s_add_i32 m0, s95, 896\n" \
    "v_mov_b32 v99, 0\n" \
    "buffer_load_dwordx4 v97, %[rw], s92 offen offset:128 sc1 lds\n" \
    "s_add_i32 m0, s95, 8192\n" \
    "v_mov_b32 v96, 0\n" \
    "25:\n" \
    "v_mov_b64 v[48:49], 0\n" \
    "v_mov_b64 v[50:51], 0\n" \
    "s_waitcnt vmcnt(7)\n" \
    "v_mfma_f32_16x16x32_bf16 v[40:43], v[0:3], v[136:139], v[88:91]\n" \
    "ds_read_b128 a[4:7], v128 offset:1024\n" \
    "v_max3_u32 v98, v136, v137, v136\n" \
    "v_mfma_f32_16x16x32_bf16 v[44:47], v[4:7], v[136:139], v[92:95]\n" \
    "ds_read_b128 a[20:23], v128 offset:3072\n" \
    "v_max3_u32 v98, v138, v139, v98\n" \
    "s_waitcnt vmcnt(6)\n" \
    "v_mfma_f32_16x16x32_bf16 v[40:43], v[8:11], v[140:143], v[40:43]\n" \
    "ds_read_b128 a[8:11], v130 offset:0\n" \
    "v_max3_u32 v98, v140, v141, v98\n" \
    "v_mfma_f32_16x16x32_bf16 v[44:47], v[12:15], v[140:143], v[44:47]\n" \
    "ds_read_b128 a[24:27], v130 offset:2048\n" \
    "v_max3_u32 v98, v142, v143, v98\n" \
    "v_smfmac_f32_16x16x64_bf16 v[48:51], v[32:35], v[136:143], v124\n" \
    "ds_read_b128 a[12:15], v130 offset:1024\n" \
    "s_waitcnt vmcnt(5)\n" \
    "v_mfma_f32_16x16x32_bf16 v[40:43], v[16:19], v[144:147], v[40:43]\n" \
    "ds_read_b128 a[28:31], v130 offset:3072\n" \
    "v_max3_u32 v98, v144, v145, v98\n" \
    "v_mfma_f32_16x16x32_bf16 v[44:47], v[20:23], v[144:147], v[44:47]\n" \
    "ds_read_b128 a[32:35], v128 offset:4096\n" \
    "v_max3_u32 v98, v146, v147, v98\n" \
    "s_waitcnt vmcnt(4)\n" \
    "v_smfmac_f32_16x16x64_bf16 v[48:51], v[36:39], v[144:151], v124\n" \
    "ds_read_b128 a[48:51], v128 offset:6144\n" \
    "v_mfma_f32_16x16x32_bf16 v[40:43], v[24:27], v[148:151], v[40:43]\n" \
    "ds_read_b128 a[36:39], v128 offset:5120\n" \
    "v_max3_u32 v98, v148, v149, v98\n" \
    "v_max3_u32 v98, v150, v151, v98\n" \
    "v_mfma_f32_16x16x32_bf16 v[44:47], v[28:31], v[148:151], v[44:47]\n" \
    "v_cmp_eq_u32 vcc, -1, v98\n" \
    "s_add_i32 s44, %[qb], 262144\n" \
    "s_add_i32 s45, s44, 4096\n" \
    "s_waitcnt lgkmcnt(3)\n" \
    "v_add_f32 v48, v48, v50\n" \
    "v_add_f32 v49, v49, v51\n" \
    "s_nop 0\n" \
    "s_cbranch_vccnz 26f\n" \
    "27:\n" \
    "v_max3_f32 v0, v40, v41, v42\n" \
    "v_max3_f32 v1, v43, v44, v45\n" \
    "v_max3_f32 v0, v0, v46, v47\n" \
    "v_max3_f32 v1, v1, v48, v49\n" \
    "v_max_f32 v0, v0, v1\n" \
    "v_max_f32 v1, v0, v1\n" \
    "buffer_load_dwordx4 v112, %[rw], s93 offen sc1 lds\n" \
    "s_add_i32 m0, s95, 8960\n" \
    "s_nop 0\n" \
    "v_permlane16_swap_b32 v0, v1\n" \
    "v_max_f32 v0, v0, v1\n" \
    "v_max_f32 v1, v0, v1\n" \
    "buffer_load_dwordx4 v112, %[rw], s93 offen offset:256 sc1 lds\n" \
    "s_nop 0\n" \
    "v_permlane32_swap_b32 v0, v1\n" \
    "v_max_f32 v0, v0, v1\n" \
    "v_mul_f32 v0, s81, v0\n" \
    "v_fma_f32 v40, v40, s81, -v0\n" \
    "v_fma_f32 v41, v41, s81, -v0\n" \
    "ds_read_b128 v[136:139], v132 offset:0\n" \
    "v_exp_f32 v40, v40\n" \
    "v_exp_f32 v41, v41\n" \
    "v_fma_f32 v42, v42, s81, -v0\n" \
    "ds_read_b128 v[140:143], v132 offset:1024\n" \
    "v_fma_f32 v43, v43, s81, -v0\n" \
    "v_exp_f32 v42, v42\n" \
    "v_exp_f32 v43, v43\n" \
    "ds_read_b128 v[144:147], v132 offset:8192\n" \
    "v_cvt_pk_bf16_f32 v32, v40, v41\n" \
    "v_fma_f32 v44, v44, s81, -v0\n" \
    "v_fma_f32 v45, v45, s81, -v0\n" \
    "ds_read_b128 v[148:151], v132 offset:9216\n" \
    "v_exp_f32 v44, v44\n" \
    "v_exp_f32 v45, v45\n" \
    "v_cvt_pk_bf16_f32 v33, v42, v43\n" \
    "ds_read_b128 a[52:55], v128 offset:7168\n" \
    "v_fma_f32 v46, v46, s81, -v0\n" \
    "v_fma_f32 v47, v47, s81, -v0\n" \
    "v_exp_f32 v46, v46\n" \
    "s_memtime vcc\n" \
    "v_exp_f32 v47, v47\n" \
    "v_cvt_pk_bf16_f32 v34, v44, v45\n" \
    "v_fma_f32 v48, v48, s81, -v0\n" \
    "v_fma_f32 v49, v49, s81, -v0\n" \
    "v_exp_f32 v48, v48\n" \
    "v_exp_f32 v49, v49\n" \
    "v_cvt_pk_bf16_f32 v35, v46, v47\n" \
    "v_cvt_pk_bf16_f32 v98, v48, v49\n" \
    "v_cvt_pk_bf16_f32 v97, v48, v49\n" \
    "s_waitcnt lgkmcnt(0)\n" \
    "v_mfma_f32_16x16x32_bf16 v[36:39], v[120:123], v[32:35], 0\n" \
    "s_sub_i32 s75, s90, vcc_lo\n" \
    "s_cmp_gt_i32 s75, 100\n" \
    "v_mfma_f32_16x16x32_bf16 v[0:3], v[56:59], v[32:35], 0\n" \
    "s_cbranch_scc1 30f\n" \
    "31:\n" \
    "ds_read_b128 a[40:43], v130 offset:4096\n" \
    "v_and_b32 v44, 0x200, v127\n" \
    "s_and_b32 s80, s26, 0x800\n" \
    "v_mad_u32_u24 v44, v44, 3, v127\n" \
    "v_mfma_f32_16x16x32_bf16 v[4:7], v[60:63], v[32:35], 0\n" \
    "s_mov_b32 m0, s74\n" \
    "s_add_i32 s76, s74, 4096\n" \
    "buffer_load_dwordx4 v125, %[rs], s69 offen sc1 lds\n" \
    "s_mov_b32 m0, s76\n" \
    "v_mfma_f32_16x16x32_bf16 v[8:11], v[64:67], v[32:35], 0\n" \
    "ds_read_b128 a[56:59], v130 offset:6144\n" \
    "v_xor_b32 v44, s80, v44\n" \
    "v_bfe_u32 v40, v127, 6, 2\n" \
    "v_mfma_f32_16x16x32_bf16 v[12:15], v[68:71], v[32:35], 0\n" \
    "buffer_load_dwordx4 v125, %[rs], s70 offen sc1 lds\n" \
    "s_movk_i32 m0, 0x2000\n" \
    "v_mfma_f32_16x16x32_bf16 v[16:19], a[120:123], v[32:35], 0\n" \
    "ds_read_b128 a[44:47], v130 offset:5120\n" \
    "v_bfe_u32 v41, v127, 5, 1\n" \
    "v_bfe_u32 v42, v127, 4, 1\n" \
    "v_mfma_f32_16x16x32_bf16 v[20:23], a[124:127], v[32:35], 0\n" \
    "s_mov_b32 exec_lo, s27\n" \
    "s_mov_b32 exec_hi, s27\n" \
    "buffer_load_dwordx4 v44, %[rs], s71 offen sc1 lds\n" \
    "s_mov_b64 exec, -1\n" \
    "v_mfma_f32_16x16x32_bf16 v[24:27], v[80:83], v[32:35], 0\n" \
    "ds_read_b128 a[60:63], v130 offset:7168\n" \
    "v_lshrrev_b32 v43, 8, v127\n" \
    "v_lshlrev_b32 v134, 14, v40\n" \
    "v_mfma_f32_16x16x32_bf16 v[28:31], v[84:87], v[32:35], 0\n" \
    "ds_read_b128 a[64:67], v128 offset:8192\n" \
    "v_lshl_add_u32 v134, v41, 12, v134\n" \
    "v_lshl_add_u32 v134, v42, 6, v134\n" \
    "v_mfma_f32_16x16x16_bf16 v[36:39], v[120:121], v[98:99], v[36:39]\n" \
    "ds_read_b128 a[80:83], v128 offset:10240\n" \
    "v_lshl_add_u32 v134, v43, 4, v134\n" \
    "v_and_b32 v41, 2, v40\n" \
    "v_mfma_f32_16x16x16_bf16 v[0:3], v[52:53], v[98:99], v[0:3]\n" \
    "buffer_load_dwordx4 v[72:75], v112, %[rw], s44 offen offset:0 sc1\n" \
    "v_mfma_f32_16x16x16_bf16 v[4:7], v[52:53], v[96:97], v[4:7]\n" \
    "ds_read_b128 a[68:71], v128 offset:9216\n" \
    "v_add_lshl_u32 v135, v40, v41, 12\n" \
    "v_bfe_u32 v42, v127, 4, 2\n" \
    "v_mfma_f32_16x16x16_bf16 v[8:11], v[54:55], v[98:99], v[8:11]\n" \
    "buffer_load_dwordx4 v[76:79], v112, %[rw], s44 offen offset:2048 sc1\n" \
    "v_mfma_f32_16x16x16_bf16 v[12:15], v[54:55], v[96:97], v[12:15]\n" \
    "ds_read_b128 a[84:87], v128 offset:11264\n" \
    "v_lshl_add_u32 v135, v42, 5, v135\n" \
    "v_lshl_add_u32 v135, v43, 3, v135\n" \
    "v_mfma_f32_16x16x16_bf16 v[16:19], v[100:101], v[98:99], v[16:19]\n" \
    "buffer_load_dwordx4 v[104:107], v112, %[rw], s45 offen offset:0 sc1\n" \
    "v_mfma_f32_16x16x16_bf16 v[20:23], v[100:101], v[96:97], v[20:23]\n" \
    "ds_read_b128 a[72:75], v130 offset:8192\n" \
    "v_mfma_f32_16x16x16_bf16 v[24:27], v[102:103], v[98:99], v[24:27]\n" \
    "buffer_load_dwordx4 v[108:111], v112, %[rw], s45 offen offset:2048 sc1\n" \
    "v_mfma_f32_16x16x16_bf16 v[28:31], v[102:103], v[96:97], v[28:31]\n" \
    "ds_read_b128 a[88:91], v130 offset:10240\n" \
    "v_cvt_pk_bf16_f32 v40, v0, v4\n" \
    "v_cvt_pk_bf16_f32 v41, v1, v5\n" \
    "v_cvt_pk_bf16_f32 v42, v2, v6\n" \
    "v_cvt_pk_bf16_f32 v43, v3, v7\n" \
    "v_cvt_pk_bf16_f32 v44, v8, v12\n" \
    "v_cvt_pk_bf16_f32 v45, v9, v13\n" \
    "v_mfma_f32_16x16x32_bf16 v[64:67], v[136:139], v[40:43], 0\n" \
    "s_mov_b32 s46, %[tp]\n" \
    "ds_read_b128 a[76:79], v130 offset:9216\n" \
    "v_cvt_pk_bf16_f32 v46, v10, v14\n" \
    "v_cvt_pk_bf16_f32 v47, v11, v15\n" \
    "v_mfma_f32_16x16x32_bf16 v[56:59], a[0:3], v[40:43], 0\n" \
    "s_mov_b32 s47, %[tx]\n" \
    "ds_read_b128 a[92:95], v130 offset:11264\n" \
    "v_cvt_pk_bf16_f32 v48, v16, v20\n" \
    "v_cvt_pk_bf16_f32 v49, v17, v21\n" \
    "v_mfma_f32_16x16x32_bf16 v[64:67], v[140:143], v[44:47], v[64:67]\n" \
    "ds_read_b128 a[96:99], v128 offset:12288\n" \
    "v_cvt_pk_bf16_f32 v50, v18, v22\n" \
    "v_cvt_pk_bf16_f32 v51, v19, v23\n" \
    "v_mfma_f32_16x16x32_bf16 v[60:63], a[16:19], v[40:43], 0\n" \
    "s_waitcnt lgkmcnt(5)\n" \
    "ds_read_b128 a[112:115], v128 offset:14336\n" \
    "v_cvt_pk_bf16_f32 v52, v24, v28\n" \
    "v_cvt_pk_bf16_f32 v53, v25, v29\n" \
    "v_mfma_f32_16x16x32_bf16 v[64:67], v[144:147], v[48:51], v[64:67]\n" \
    "ds_read_b128 a[100:103], v128 offset:13312\n" \
    "v_cvt_pk_bf16_f32 v54, v26, v30\n" \
    "v_cvt_pk_bf16_f32 v55, v27, v31\n" \
    "v_mfma_f32_16x16x32_bf16 v[56:59], a[4:7], v[44:47], v[56:59]\n" \
    "ds_read_b128 a[116:119], v128 offset:15360\n" \
    "v_rcp_f32 v36, v36\n" \
    "v_mfma_f32_16x16x32_bf16 v[64:67], v[148:151], v[52:55], v[64:67]\n" \
    "ds_read_b128 a[104:107], v130 offset:12288\n" \
    "v_add_u32 v37, s74, v127\n" \
    "v_xor_b32 v38, 0x400, v37\n" \
    "v_mfma_f32_16x16x32_bf16 v[60:63], a[20:23], v[44:47], v[60:63]\n" \
    "ds_read_b128 a[120:123], v130 offset:14336\n" \
    "v_xor_b32 v39, 0x800, v37\n" \
    "v_xor_b32 v132, 0xc00, v37\n" \
    "v_mfma_f32_16x16x32_bf16 v[56:59], a[8:11], v[48:51], v[56:59]\n" \
    "ds_read_b128 a[108:111], v130 offset:13312\n" \
    "v_mul_f32 v64, v64, v36\n" \
    "v_mul_f32 v65, v65, v36\n" \
    "v_mfma_f32_16x16x32_bf16 v[60:63], a[24:27], v[48:51], v[60:63]\n" \
    "ds_read_b128 a[124:127], v130 offset:15360\n" \
    "v_mul_f32 v66, v66, v36\n" \
    "v_mul_f32 v67, v67, v36\n" \
    "v_mfma_f32_16x16x32_bf16 v[56:59], a[12:15], v[52:55], v[56:59]\n" \
    "v_cvt_pk_bf16_f32 v100, v64, v65\n" \
    "v_cvt_pk_bf16_f32 v101, v66, v67\n" \
    "v_mfma_f32_16x16x32_bf16 v[60:63], a[28:31], v[52:55], v[60:63]\n" \
    "buffer_store_dwordx2 v[100:101], v135, %[rw], s47 offen sc1\n" \
    "v_mfma_f32_16x16x32_bf16 v[64:67], a[32:35], v[40:43], 0\n" \
    "v_mul_f32 v56, v56, v36\n" \
    "v_mul_f32 v57, v57, v36\n" \
    "v_mfma_f32_16x16x32_bf16 v[68:71], a[48:51], v[40:43], 0\n" \
    "v_mul_f32 v58, v58, v36\n" \
    "v_mul_f32 v59, v59, v36\n" \
    "v_mfma_f32_16x16x32_bf16 v[64:67], a[36:39], v[44:47], v[64:67]\n" \
    "v_mul_f32 v60, v60, v36\n" \
    "v_mul_f32 v61, v61, v36\n" \
    "v_mfma_f32_16x16x32_bf16 v[68:71], a[52:55], v[44:47], v[68:71]\n" \
    "v_mul_f32 v62, v62, v36\n" \
    "v_mul_f32 v63, v63, v36\n" \
    "v_mfma_f32_16x16x32_bf16 v[64:67], a[40:43], v[48:51], v[64:67]\n" \
    "v_cvt_pk_bf16_f32 v96, v56, v57\n" \
    "v_cvt_pk_bf16_f32 v97, v58, v59\n" \
    "v_mfma_f32_16x16x32_bf16 v[68:71], a[56:59], v[48:51], v[68:71]\n" \
    "v_cvt_pk_bf16_f32 v98, v60, v61\n" \
    "v_cvt_pk_bf16_f32 v99, v62, v63\n" \
    "v_mfma_f32_16x16x32_bf16 v[64:67], a[44:47], v[52:55], v[64:67]\n" \
    "buffer_store_dwordx4 v[96:99], v134, %[rw], s46 offen sc1\n" \
    "s_add_i32 s46, s46, 0x20000\n" \
    "v_mfma_f32_16x16x32_bf16 v[68:71], a[60:63], v[52:55], v[68:71]\n" \
    "v_mov_b64 v[80:81], 0\n" \
    "v_mov_b64 v[82:83], 0\n" \
    "v_mfma_f32_16x16x32_bf16 v[56:59], a[64:67], v[40:43], 0\n" \
    "v_mul_f32 v64, v64, v36\n" \
    "v_mul_f32 v65, v65, v36\n" \
    "v_mfma_f32_16x16x32_bf16 v[60:63], a[80:83], v[40:43], 0\n" \
    "v_mul_f32 v66, v66, v36\n" \
    "v_mul_f32 v67, v67, v36\n" \
    "v_mfma_f32_16x16x32_bf16 v[56:59], a[68:71], v[44:47], v[56:59]\n" \
    "v_mul_f32 v68, v68, v36\n" \
    "v_mul_f32 v69, v69, v36\n" \
    "v_mfma_f32_16x16x32_bf16 v[60:63], a[84:87], v[44:47], v[60:63]\n" \
    "v_mul_f32 v70, v70, v36\n" \
    "v_mul_f32 v71, v71, v36\n" \
    "s_waitcnt lgkmcnt(11)\n" \
    "v_mfma_f32_16x16x32_bf16 v[56:59], a[72:75], v[48:51], v[56:59]\n" \
    "v_cvt_pk_bf16_f32 v100, v64, v65\n" \
    "v_cvt_pk_bf16_f32 v101, v66, v67\n" \
    "s_waitcnt lgkmcnt(10)\n" \
    "v_mfma_f32_16x16x32_bf16 v[60:63], a[88:91], v[48:51], v[60:63]\n" \
    "s_waitcnt vmcnt(6) lgkmcnt(0)\n" \
    "s_barrier\n" \
    "v_cvt_pk_bf16_f32 v102, v68, v69\n" \
    "v_cvt_pk_bf16_f32 v103, v70, v71\n" \
    "v_mfma_f32_16x16x32_bf16 v[56:59], a[76:79], v[52:55], v[56:59]\n" \
    "ds_read_b128 v[0:3], v37 offset:0\n" \
    "buffer_store_dwordx4 v[100:103], v134, %[rw], s46 offen sc1\n" \
    "s_add_i32 s46, s46, 0x20000\n" \
    "v_mfma_f32_16x16x32_bf16 v[60:63], a[92:95], v[52:55], v[60:63]\n" \
    "ds_read_b128 v[4:7], v38 offset:0\n" \
    "v_mov_b64 v[84:85], 0\n" \
    "v_mov_b64 v[86:87], 0\n" \
    "v_mfma_f32_16x16x32_bf16 v[64:67], a[96:99], v[40:43], 0\n" \
    "ds_read_b128 v[8:11], v39 offset:0\n" \
    "v_mul_f32 v56, v56, v36\n" \
    "v_mul_f32 v57, v57, v36\n" \
    "v_mfma_f32_16x16x32_bf16 v[68:71], a[112:115], v[40:43], 0\n" \
    "ds_read_b128 v[12:15], v132 offset:0\n" \
    "v_mul_f32 v58, v58, v36\n" \
    "v_mul_f32 v59, v59, v36\n" \
    "v_mfma_f32_16x16x32_bf16 v[64:67], a[100:103], v[44:47], v[64:67]\n" \
    "ds_read_b128 v[16:19], v37 offset:4096\n" \
    "v_mul_f32 v60, v60, v36\n" \
    "v_mul_f32 v61, v61, v36\n" \
    "v_mfma_f32_16x16x32_bf16 v[68:71], a[116:119], v[44:47], v[68:71]\n" \
    "ds_read_b128 v[20:23], v38 offset:4096\n" \
    "v_mul_f32 v62, v62, v36\n" \
    "v_mul_f32 v63, v63, v36\n" \
    "v_mfma_f32_16x16x32_bf16 v[64:67], a[104:107], v[48:51], v[64:67]\n" \
    "ds_read_b128 v[24:27], v39 offset:4096\n" \
    "v_cvt_pk_bf16_f32 v96, v56, v57\n" \
    "v_cvt_pk_bf16_f32 v97, v58, v59\n" \
    "v_mfma_f32_16x16x32_bf16 v[68:71], a[120:123], v[48:51], v[68:71]\n" \
    "ds_read_b128 v[28:31], v132 offset:4096\n" \
    "v_cvt_pk_bf16_f32 v98, v60, v61\n" \
    "v_cvt_pk_bf16_f32 v99, v62, v63\n" \
    "v_mfma_f32_16x16x32_bf16 v[64:67], a[108:111], v[52:55], v[64:67]\n" \
    "ds_read_b128 v[32:35], v126 offset:0\n" \
    "buffer_store_dwordx4 v[96:99], v134, %[rw], s46 offen sc1\n" \
    "s_add_i32 s46, s46, 0x20000\n" \
    "v_mfma_f32_16x16x32_bf16 v[68:71], a[124:127], v[52:55], v[68:71]\n" \
    "s_waitcnt lgkmcnt(3)\n" \
    "33:\n" \
    "v_smfmac_f32_16x16x64_bf16 v[80:83], v[0:3], a[128:135], v124\n" \
    "v_mul_f32 v64, v64, v36\n" \
    "v_smfmac_f32_16x16x64_bf16 v[80:83], v[4:7], a[144:151], v124\n" \
    "v_mul_f32 v65, v65, v36\n" \
    "v_smfmac_f32_16x16x64_bf16 v[80:83], v[8:11], a[160:167], v124\n" \
    "s_waitcnt lgkmcnt(0)\n" \
    "v_mul_f32 v66, v66, v36\n" \
    "v_smfmac_f32_16x16x64_bf16 v[80:83], v[12:15], a[176:183], v124\n" \
    "ds_read_b128 v[40:43], v127 offset:16384\n" \
    "v_mul_f32 v67, v67, v36\n" \
    "v_smfmac_f32_16x16x64_bf16 v[80:83], v[16:19], a[192:199], v124\n" \
    "ds_read_b128 v[44:47], v127 offset:17408\n" \
    "v_mul_f32 v68, v68, v36\n" \
    "v_smfmac_f32_16x16x64_bf16 v[80:83], v[20:23], a[208:215], v124\n" \
    "ds_read_b128 v[48:51], v127 offset:18432\n" \
    "v_mul_f32 v69, v69, v36\n" \
    "v_smfmac_f32_16x16x64_bf16 v[80:83], v[24:27], a[224:231], v124\n" \
    "ds_read_b128 v[52:55], v127 offset:19456\n" \
    "v_mul_f32 v70, v70, v36\n" \
    "v_mul_f32 v71, v71, v36\n" \
    "v_smfmac_f32_16x16x64_bf16 v[80:83], v[28:31], a[240:247], v124\n" \
    "ds_read_b128 v[56:59], v127 offset:20480\n" \
    "v_cvt_pk_bf16_f32 v100, v64, v65\n" \
    "v_cvt_pk_bf16_f32 v101, v66, v67\n" \
    "v_smfmac_f32_16x16x64_bf16 v[80:83], v[32:35], v[152:159], v124\n" \
    "ds_read_b128 v[60:63], v127 offset:21504\n" \
    "v_cvt_pk_bf16_f32 v102, v68, v69\n" \
    "v_cvt_pk_bf16_f32 v103, v70, v71\n" \
    "v_smfmac_f32_16x16x64_bf16 v[84:87], v[0:3], a[136:143], v124\n" \
    "ds_read_b128 v[64:67], v127 offset:22528\n" \
    "buffer_store_dwordx4 v[100:103], v134, %[rw], s46 offen sc1\n" \
    "v_smfmac_f32_16x16x64_bf16 v[84:87], v[4:7], a[152:159], v124\n" \
    "ds_read_b128 v[68:71], v127 offset:23552\n" \
    "v_add_f32 v80, v80, v82\n" \
    "v_add_f32 v81, v81, v83\n" \
    "v_smfmac_f32_16x16x64_bf16 v[84:87], v[8:11], a[168:175], v124\n" \
    "s_waitcnt vmcnt(5)\n" \
    "v_cmp_u_f32 vcc, v80, v81\n" \
    "v_max3_u32 v128, v72, v73, v72\n" \
    "v_smfmac_f32_16x16x64_bf16 v[84:87], v[12:15], a[184:191], v124\n" \
    "v_max3_u32 v128, v74, v75, v128\n" \
    "v_max3_u32 v128, v76, v77, v128\n" \
    "v_smfmac_f32_16x16x64_bf16 v[84:87], v[16:19], a[200:207], v124\n" \
    "s_cbranch_vccnz 32f\n" \
    "34:\n" \
    "v_max3_u32 v128, v78, v79, v128\n" \
    "v_max3_u32 v128, v104, v105, v128\n" \
    "v_smfmac_f32_16x16x64_bf16 v[84:87], v[20:23], a[216:223], v124\n" \
    "v_max3_u32 v128, v106, v107, v128\n" \
    "v_max3_u32 v128, v108, v109, v128\n" \
    "v_smfmac_f32_16x16x64_bf16 v[84:87], v[24:27], a[232:239], v124\n" \
    "v_max3_u32 v128, v110, v111, v128\n" \
    "v_cmp_eq_u32 vcc, -1, v128\n" \
    "v_smfmac_f32_16x16x64_bf16 v[84:87], v[28:31], a[248:255], v124\n" \
    "s_waitcnt lgkmcnt(0)\n" \
    "v_smfmac_f32_16x16x64_bf16 v[84:87], v[32:35], v[160:167], v124\n" \
    "s_cbranch_vccnz 28f\n" \
    "29:\n" \
    "v_mfma_f32_16x16x32_bf16 v[152:155], v[40:43], v[72:75], v[88:91]\n" \
    "ds_read_b128 a[128:131], v133 offset:0\n" \
    "v_mov_b64 v[96:97], 0\n" \
    "v_mov_b64 v[98:99], 0\n" \
    "v_mfma_f32_16x16x32_bf16 v[156:159], v[44:47], v[72:75], v[92:95]\n" \
    "ds_read_b128 a[132:135], v133 offset:1024\n" \
    "v_add_f32 v84, v84, v86\n" \
    "v_add_f32 v85, v85, v87\n" \
    "v_mfma_f32_16x16x32_bf16 v[152:155], v[48:51], v[76:79], v[152:155]\n" \
    "ds_read_b128 a[136:139], v133 offset:2048\n" \
    "v_cvt_pk_bf16_f32 v80, v80, v84\n" \
    "v_cvt_pk_bf16_f32 v81, v81, v85\n" \
    "v_mfma_f32_16x16x32_bf16 v[156:159], v[52:55], v[76:79], v[156:159]\n" \
    "ds_write2_b32 v129, v80, v81 offset0:0 offset1:144\n" \
    "v_mfma_f32_16x16x32_bf16 v[152:155], v[56:59], v[104:107], v[152:155]\n" \
    "ds_read_b128 a[140:143], v133 offset:3072\n" \
    "v_mov_b64 v[160:161], 0\n" \
    "v_mov_b64 v[162:163], 0\n" \
    "v_mfma_f32_16x16x32_bf16 v[156:159], v[60:63], v[104:107], v[156:159]\n" \
    "ds_read_b128 a[144:147], v133 offset:16384\n" \
    "v_mov_b64 v[100:101], 0\n" \
    "v_mov_b64 v[102:103], 0\n" \
    "v_mfma_f32_16x16x32_bf16 v[152:155], v[64:67], v[108:111], v[152:155]\n" \
    "ds_read_b128 a[148:151], v133 offset:17408\n" \
    "v_lshrrev_b32 v42, 1, v37\n" \
    "v_lshrrev_b32 v43, 1, v127\n" \
    "v_mfma_f32_16x16x32_bf16 v[156:159], v[68:71], v[108:111], v[156:159]\n" \
    "s_waitcnt lgkmcnt(3)\n" \
    "s_barrier\n" \
    "ds_read_b128 v[80:83], v131 offset:0\n" \
    "ds_read_b128 v[84:87], v131 offset:128\n" \
    "v_smfmac_f32_16x16x64_bf16 v[96:99], v[16:19], v[168:175], v124\n" \
    "ds_read_b128 a[152:155], v133 offset:18432\n" \
    "v_mov_b32 v61, 0\n" \
    "v_smfmac_f32_16x16x64_bf16 v[96:99], v[20:23], v[184:191], v124\n" \
    "ds_read_b128 a[156:159], v133 offset:19456\n" \
    "v_max3_f32 v40, v152, v153, v154\n" \
    "v_smfmac_f32_16x16x64_bf16 v[96:99], v[24:27], v[200:207], v124\n" \
    "ds_read_b128 a[160:163], v133 offset:32768\n" \
    "s_waitcnt lgkmcnt(3)\n" \
    "v_max3_f32 v41, v155, v156, v157\n" \
    "v_smfmac_f32_16x16x64_bf16 v[160:163], v[80:83], v[72:79], v124\n" \
    "ds_read_b128 a[164:167], v133 offset:33792\n" \
    "v_max3_f32 v40, v40, v158, v159\n" \
    "v_smfmac_f32_16x16x64_bf16 v[160:163], v[84:87], v[104:111], v124\n" \
    "ds_read_b128 a[168:171], v133 offset:34816\n" \
    "v_mov_b32 v62, 0\n" \
    "v_smfmac_f32_16x16x64_bf16 v[96:99], v[28:31], v[216:223], v124\n" \
    "ds_read_b128 a[172:175], v133 offset:35840\n" \
    "v_smfmac_f32_16x16x64_bf16 v[96:99], v[32:35], v[232:239], v124\n" \
    "ds_read_b128 a[176:179], v133 offset:49152\n" \
    "v_add_f32 v160, v160, v162\n" \
    "v_add_f32 v161, v161, v163\n" \
    "v_max3_f32 v41, v41, v160, v161\n" \
    "v_smfmac_f32_16x16x64_bf16 v[100:103], v[16:19], v[176:183], v124\n" \
    "ds_read_b128 a[180:183], v133 offset:50176\n" \
    "v_max_f32 v40, v40, v41\n" \
    "v_max_f32 v41, v40, v41\n" \
    "v_smfmac_f32_16x16x64_bf16 v[100:103], v[20:23], v[192:199], v124\n" \
    "ds_read_b128 a[192:195], v127 offset:24576\n" \
    "v_permlane16_swap_b32 v40, v41\n" \
    "v_max_f32 v40, v40, v41\n" \
    "v_max_f32 v41, v40, v41\n" \
    "v_smfmac_f32_16x16x64_bf16 v[96:99], v[0:3], a[128:135], v124\n" \
    "ds_read_b128 a[196:199], v127 offset:25600\n" \
    "v_permlane32_swap_b32 v40, v41\n" \
    "v_max_f32 v40, v40, v41\n" \
    "v_mul_f32 v40, s81, v40\n" \
    "v_smfmac_f32_16x16x64_bf16 v[96:99], v[4:7], a[144:151], v124\n" \
    "ds_read_b128 a[200:203], v127 offset:26624\n" \
    "s_waitcnt lgkmcnt(3)\n" \
    "v_fma_f32 v152, v152, s81, -v40\n" \
    "v_fma_f32 v153, v153, s81, -v40\n" \
    "v_exp_f32 v152, v152\n" \
    "v_smfmac_f32_16x16x64_bf16 v[96:99], v[8:11], a[160:167], v124\n" \
    "ds_read_b128 a[204:207], v127 offset:27648\n" \
    "v_exp_f32 v153, v153\n" \
    "v_fma_f32 v154, v154, s81, -v40\n" \
    "v_fma_f32 v155, v155, s81, -v40\n" \
    "v_smfmac_f32_16x16x64_bf16 v[96:99], v[12:15], a[176:183], v124\n" \
    "ds_read_b128 a[208:211], v127 offset:28672\n" \
    "v_exp_f32 v154, v154\n" \
    "v_exp_f32 v155, v155\n" \
    "v_cvt_pk_bf16_f32 v56, v152, v153\n" \
    "v_smfmac_f32_16x16x64_bf16 v[100:103], v[24:27], v[208:215], v124\n" \
    "ds_read_b128 a[212:215], v127 offset:29696\n" \
    "v_fma_f32 v156, v156, s81, -v40\n" \
    "v_fma_f32 v157, v157, s81, -v40\n" \
    "v_exp_f32 v156, v156\n" \
    "v_smfmac_f32_16x16x64_bf16 v[100:103], v[28:31], v[224:231], v124\n" \
    "ds_read_b128 a[216:219], v127 offset:30720\n" \
    "v_exp_f32 v157, v157\n" \
    "v_cvt_pk_bf16_f32 v57, v154, v155\n" \
    "v_fma_f32 v158, v158, s81, -v40\n" \
    "v_smfmac_f32_16x16x64_bf16 v[100:103], v[32:35], v[240:247], v124\n" \
    "ds_read_b128 a[220:223], v127 offset:31744\n" \
    "v_fma_f32 v159, v159, s81, -v40\n" \
    "v_exp_f32 v158, v158\n" \
    "v_exp_f32 v159, v159\n" \
    "v_smfmac_f32_16x16x64_bf16 v[100:103], v[12:15], v[248:255], v124\n" \
    "v_cvt_pk_bf16_f32 v58, v156, v157\n" \
    "v_fma_f32 v160, v160, s81, -v40\n" \
    "v_fma_f32 v161, v161, s81, -v40\n" \
    "v_smfmac_f32_16x16x64_bf16 v[100:103], v[0:3], a[136:143], v124\n" \
    "v_exp_f32 v160, v160\n" \
    "v_exp_f32 v161, v161\n" \
    "v_cvt_pk_bf16_f32 v59, v158, v159\n" \
    "v_smfmac_f32_16x16x64_bf16 v[100:103], v[4:7], a[152:159], v124\n" \
    "v_cvt_pk_bf16_f32 v60, v160, v161\n" \
    "v_cvt_pk_bf16_f32 v63, v160, v161\n" \
    "v_smfmac_f32_16x16x64_bf16 v[100:103], v[8:11], a[168:175], v124\n" \
    "s_waitcnt lgkmcnt(0)\n" \
    "v_mfma_f32_16x16x32_bf16 v[36:39], v[120:123], v[56:59], 0\n" \
    "v_add_f32 v96, v96, v98\n" \
    "v_add_f32 v97, v97, v99\n" \
    "v_mfma_f32_16x16x32_bf16 v[0:3], a[192:195], v[56:59], 0\n" \
    "v_add_f32 v100, v100, v102\n" \
    "v_add_f32 v101, v101, v103\n" \
    "v_mfma_f32_16x16x32_bf16 v[4:7], a[196:199], v[56:59], 0\n" \
    "v_cvt_pk_bf16_f32 v96, v96, v97\n" \
    "v_cvt_pk_bf16_f32 v97, v100, v101\n" \
    "v_mfma_f32_16x16x32_bf16 v[8:11], a[200:203], v[56:59], 0\n" \
    "ds_write_b64 v42, v[96:97] offset:9216\n" \
    "v_mfma_f32_16x16x32_bf16 v[12:15], a[204:207], v[56:59], 0\n" \
    "v_lshrrev_b32 v169, 8, v127\n" \
    "v_and_b32 v170, 2, v169\n" \
    "v_mfma_f32_16x16x32_bf16 v[16:19], a[208:211], v[56:59], 0\n" \
    "v_mad_u32_u24 v169, v170, 3, v169\n" \
    "v_bfe_u32 v168, v127, 6, 1\n" \
    "v_mfma_f32_16x16x32_bf16 v[20:23], a[212:215], v[56:59], 0\n" \
    "v_bfe_u32 v170, v127, 4, 2\n" \
    "v_lshl_add_u32 v169, v170, 1, v169\n" \
    "v_mfma_f32_16x16x32_bf16 v[24:27], a[216:219], v[56:59], 0\n" \
    "s_waitcnt lgkmcnt(0)\n" \
    "s_barrier\n" \
    "ds_read_b64 v[64:65], v43 offset:9216\n" \
    "ds_read_b64 v[66:67], v43 offset:9728\n" \
    "ds_read_b64 v[68:69], v43 offset:10240\n" \
    "ds_read_b64 v[70:71], v43 offset:10752\n" \
    "v_mfma_f32_16x16x32_bf16 v[28:31], a[220:223], v[56:59], 0\n" \
    "v_mul_u32_u24 v168, 1152, v168\n" \
    "v_lshl_add_u32 v168, v169, 2, v168\n" \
    "v_mfma_f32_16x16x16_bf16 v[36:39], v[120:121], v[60:61], v[36:39]\n" \
    "s_waitcnt lgkmcnt(2)\n" \
    "v_mfma_f32_16x16x16_bf16 v[0:3], v[64:65], v[60:61], v[0:3]\n" \
    "v_sub_f32 v169, 1.0, v114\n" \
    "v_lshlrev_b32 v172, 16, v113\n" \
    "v_mfma_f32_16x16x16_bf16 v[4:7], v[64:65], v[62:63], v[4:7]\n" \
    "s_waitcnt lgkmcnt(0)\n" \
    "v_mul_f32 v172, v172, v169\n" \
    "v_and_b32 v170, 16, v127\n" \
    "v_mfma_f32_16x16x16_bf16 v[8:11], v[66:67], v[60:61], v[8:11]\n" \
    "v_mov_b32 v169, 0x3f80\n" \
    "v_lshlrev_b32 v170, v170, v169\n" \
    "v_mfma_f32_16x16x16_bf16 v[12:15], v[66:67], v[62:63], v[12:15]\n" \
    "v_mov_b32_dpp v173, v172 row_ror:8 row_mask:0xf bank_mask:0xf\n" \
    "v_mov_b64 v[176:177], 0\n" \
    "v_mfma_f32_16x16x16_bf16 v[16:19], v[68:69], v[60:61], v[16:19]\n" \
    "v_mov_b64 v[178:179], 0\n" \
    "v_mov_b32_dpp v176, v170 quad_perm:[0,1,2,3] row_mask:0xf bank_mask:0x1\n" \
    "v_mfma_f32_16x16x16_bf16 v[20:23], v[68:69], v[62:63], v[20:23]\n" \
    "v_mov_b32_dpp v177, v170 quad_perm:[0,1,2,3] row_mask:0xf bank_mask:0x2\n" \
    "v_mov_b32_dpp v178, v170 quad_perm:[0,1,2,3] row_mask:0xf bank_mask:0x4\n" \
    "v_mfma_f32_16x16x16_bf16 v[24:27], v[70:71], v[60:61], v[24:27]\n" \
    "v_mov_b32_dpp v179, v170 quad_perm:[0,1,2,3] row_mask:0xf bank_mask:0x8\n" \
    "v_mfma_f32_16x16x16_bf16 v[28:31], v[70:71], v[62:63], v[28:31]\n" \
    "v_cvt_pk_bf16_f32 v40, v0, v4\n" \
    "v_cvt_pk_bf16_f32 v41, v1, v5\n" \
    "v_cvt_pk_bf16_f32 v42, v2, v6\n" \
    "v_cvt_pk_bf16_f32 v43, v3, v7\n" \
    "v_cvt_pk_bf16_f32 v44, v8, v12\n" \
    "v_cvt_pk_bf16_f32 v45, v9, v13\n" \
    "v_mfma_f32_16x16x32_bf16 v[80:83], v[136:139], v[40:43], 0\n" \
    "s_mov_b32 s46, %[tq]\n" \
    "v_cvt_pk_bf16_f32 v46, v10, v14\n" \
    "v_cvt_pk_bf16_f32 v47, v11, v15\n" \
    "v_mfma_f32_16x16x32_bf16 v[72:75], a[0:3], v[40:43], 0\n" \
    "s_add_i32 s47, %[tx], 0x40000\n" \
    "v_cvt_pk_bf16_f32 v48, v16, v20\n" \
    "v_cvt_pk_bf16_f32 v49, v17, v21\n" \
    "v_mfma_f32_16x16x32_bf16 v[80:83], v[140:143], v[44:47], v[80:83]\n" \
    "v_cvt_pk_bf16_f32 v50, v18, v22\n" \
    "v_cvt_pk_bf16_f32 v51, v19, v23\n" \
    "v_mfma_f32_16x16x32_bf16 v[76:79], a[16:19], v[40:43], 0\n" \
    "v_cvt_pk_bf16_f32 v52, v24, v28\n" \
    "v_cvt_pk_bf16_f32 v53, v25, v29\n" \
    "v_mfma_f32_16x16x32_bf16 v[80:83], v[144:147], v[48:51], v[80:83]\n" \
    "v_cvt_pk_bf16_f32 v54, v26, v30\n" \
    "v_cvt_pk_bf16_f32 v55, v27, v31\n" \
    "v_mfma_f32_16x16x32_bf16 v[72:75], a[4:7], v[44:47], v[72:75]\n" \
    "v_rcp_f32 v36, v36\n" \
    "v_mfma_f32_16x16x32_bf16 v[80:83], v[148:151], v[52:55], v[80:83]\n" \
    "s_and_b32 s94, %[u], 15\n" \
    "s_mul_i32 s94, s94, 2304\n" \
    "v_mfma_f32_16x16x32_bf16 v[76:79], a[20:23], v[44:47], v[76:79]\n" \
    "s_andn2_b32 s77, %[u], 15\n" \
    "s_lshl2_add_u32 s94, s77, s94\n" \
    "v_mfma_f32_16x16x32_bf16 v[72:75], a[8:11], v[48:51], v[72:75]\n" \
    "v_mul_f32 v80, v80, v36\n" \
    "v_mul_f32 v81, v81, v36\n" \
    "v_mfma_f32_16x16x32_bf16 v[76:79], a[24:27], v[48:51], v[76:79]\n" \
    "v_mul_f32 v82, v82, v36\n" \
    "v_mul_f32 v83, v83, v36\n" \
    "v_mfma_f32_16x16x32_bf16 v[72:75], a[12:15], v[52:55], v[72:75]\n" \
    "v_cvt_pk_bf16_f32 v100, v80, v81\n" \
    "v_cvt_pk_bf16_f32 v101, v82, v83\n" \
    "v_mfma_f32_16x16x32_bf16 v[76:79], a[28:31], v[52:55], v[76:79]\n" \
    "buffer_store_dwordx2 v[100:101], v135, %[rw], s47 offen sc1\n" \
    "v_mfma_f32_16x16x32_bf16 v[80:83], a[32:35], v[40:43], 0\n" \
    "v_mul_f32 v72, v72, v36\n" \
    "v_mul_f32 v73, v73, v36\n" \
    "v_mfma_f32_16x16x32_bf16 v[84:87], a[48:51], v[40:43], 0\n" \
    "v_mul_f32 v74, v74, v36\n" \
    "v_mul_f32 v75, v75, v36\n" \
    "v_mfma_f32_16x16x32_bf16 v[80:83], a[36:39], v[44:47], v[80:83]\n" \
    "v_mul_f32 v76, v76, v36\n" \
    "v_mul_f32 v77, v77, v36\n" \
    "v_mfma_f32_16x16x32_bf16 v[84:87], a[52:55], v[44:47], v[84:87]\n" \
    "v_mul_f32 v78, v78, v36\n" \
    "v_mul_f32 v79, v79, v36\n" \
    "v_mfma_f32_16x16x32_bf16 v[80:83], a[40:43], v[48:51], v[80:83]\n" \
    "v_cvt_pk_bf16_f32 v96, v72, v73\n" \
    "v_cvt_pk_bf16_f32 v97, v74, v75\n" \
    "v_mfma_f32_16x16x32_bf16 v[84:87], a[56:59], v[48:51], v[84:87]\n" \
    "v_cvt_pk_bf16_f32 v98, v76, v77\n" \
    "v_cvt_pk_bf16_f32 v99, v78, v79\n" \
    "v_mfma_f32_16x16x32_bf16 v[80:83], a[44:47], v[52:55], v[80:83]\n" \
    "buffer_store_dwordx4 v[96:99], v134, %[rw], s46 offen sc1\n" \
    "s_add_i32 s46, s46, 0x20000\n" \
    "v_mfma_f32_16x16x32_bf16 v[84:87], a[60:63], v[52:55], v[84:87]\n" \
    "s_add_i32 s94, s94, 2322432\n" \
    "v_mfma_f32_16x16x32_bf16 v[72:75], a[64:67], v[40:43], 0\n" \
    "v_mul_f32 v80, v80, v36\n" \
    "v_mul_f32 v81, v81, v36\n" \
    "v_mfma_f32_16x16x32_bf16 v[76:79], a[80:83], v[40:43], 0\n" \
    "v_mul_f32 v82, v82, v36\n" \
    "v_mul_f32 v83, v83, v36\n" \
    "v_mfma_f32_16x16x32_bf16 v[72:75], a[68:71], v[44:47], v[72:75]\n" \
    "v_mul_f32 v84, v84, v36\n" \
    "v_mul_f32 v85, v85, v36\n" \
    "v_mfma_f32_16x16x32_bf16 v[76:79], a[84:87], v[44:47], v[76:79]\n" \
    "v_mul_f32 v86, v86, v36\n" \
    "v_mul_f32 v87, v87, v36\n" \
    "v_mfma_f32_16x16x32_bf16 v[72:75], a[72:75], v[48:51], v[72:75]\n" \
    "v_cvt_pk_bf16_f32 v100, v80, v81\n" \
    "v_cvt_pk_bf16_f32 v101, v82, v83\n" \
    "v_mfma_f32_16x16x32_bf16 v[76:79], a[88:91], v[48:51], v[76:79]\n" \
    "v_cvt_pk_bf16_f32 v102, v84, v85\n" \
    "v_cvt_pk_bf16_f32 v103, v86, v87\n" \
    "v_mfma_f32_16x16x32_bf16 v[72:75], a[76:79], v[52:55], v[72:75]\n" \
    "buffer_store_dwordx4 v[100:103], v134, %[rw], s46 offen sc1\n" \
    "s_add_i32 s46, s46, 0x20000\n" \
    "v_mfma_f32_16x16x32_bf16 v[76:79], a[92:95], v[52:55], v[76:79]\n" \
    "v_mfma_f32_16x16x32_bf16 v[80:83], a[96:99], v[40:43], 0\n" \
    "v_mul_f32 v72, v72, v36\n" \
    "v_mul_f32 v73, v73, v36\n" \
    "v_mfma_f32_16x16x32_bf16 v[84:87], a[112:115], v[40:43], 0\n" \
    "v_mul_f32 v74, v74, v36\n" \
    "v_mul_f32 v75, v75, v36\n" \
    "v_mfma_f32_16x16x32_bf16 v[80:83], a[100:103], v[44:47], v[80:83]\n" \
    "v_mul_f32 v76, v76, v36\n" \
    "v_mul_f32 v77, v77, v36\n" \
    "v_mfma_f32_16x16x32_bf16 v[84:87], a[116:119], v[44:47], v[84:87]\n" \
    "v_mul_f32 v78, v78, v36\n" \
    "v_mul_f32 v79, v79, v36\n" \
    "v_mfma_f32_16x16x32_bf16 v[80:83], a[104:107], v[48:51], v[80:83]\n" \
    "v_cvt_pk_bf16_f32 v96, v72, v73\n" \
    "v_cvt_pk_bf16_f32 v97, v74, v75\n" \
    "v_mfma_f32_16x16x32_bf16 v[84:87], a[120:123], v[48:51], v[84:87]\n" \
    "v_cvt_pk_bf16_f32 v98, v76, v77\n" \
    "v_cvt_pk_bf16_f32 v99, v78, v79\n" \
    "v_mfma_f32_16x16x32_bf16 v[80:83], a[108:111], v[52:55], v[80:83]\n" \
    "buffer_store_dwordx4 v[96:99], v134, %[rw], s46 offen sc1\n" \
    "s_add_i32 s46, s46, 0x20000\n" \
    "v_mfma_f32_16x16x32_bf16 v[84:87], a[124:127], v[52:55], v[84:87]\n" \
    "s_memtime vcc\n" \
    "s_nop 2\n" \
    "v_mul_f32 v80, v80, v36\n" \
    "v_mul_f32 v81, v81, v36\n" \
    "v_mul_f32 v82, v82, v36\n" \
    "v_mul_f32 v83, v83, v36\n" \
    "v_mul_f32 v84, v84, v36\n" \
    "v_mul_f32 v85, v85, v36\n" \
    "v_mul_f32 v86, v86, v36\n" \
    "v_mul_f32 v87, v87, v36\n" \
    "v_cvt_pk_bf16_f32 v100, v80, v81\n" \
    "v_cvt_pk_bf16_f32 v101, v82, v83\n" \
    "v_cvt_pk_bf16_f32 v102, v84, v85\n" \
    "v_cvt_pk_bf16_f32 v103, v86, v87\n" \
    "buffer_store_dwordx4 v[100:103], v134, %[rw], s46 offen sc1\n" \
    "s_waitcnt lgkmcnt(0)\n" \
    "s_add_i32 s90, vcc_lo, %[fw]\n" \
    "35:\n" \
    "s_memtime vcc\n" \
    "s_waitcnt lgkmcnt(0)\n" \
    "s_sub_i32 s75, s90, vcc_lo\n" \
    "s_cmp_gt_i32 s75, 0\n" \
    "s_cbranch_scc1 35b\n" \
    "36:\n" \
    "buffer_load_dwordx4 v[0:3], v127, %[rw], %[ua] offen offset:0 sc1\n" \
    "buffer_load_dwordx4 v[4:7], v127, %[rw], %[ua] offen offset:1024 sc1\n" \
    "buffer_load_dwordx4 v[8:11], v127, %[rw], %[ua] offen offset:2048 sc1\n" \
    "buffer_load_dwordx4 v[12:15], v127, %[rw], %[ua] offen offset:3072 sc1\n" \
    "s_waitcnt vmcnt(3)\n" \
    "v_mfma_f32_16x16x32_bf16 v[32:35], v[176:179], v[0:3], 0\n" \
    "v_max3_u32 v40, v0, v1, v0\n" \
    "v_max3_u32 v40, v2, v3, v40\n" \
    "s_waitcnt vmcnt(2)\n" \
    "v_mfma_f32_16x16x32_bf16 v[32:35], v[176:179], v[4:7], v[32:35]\n" \
    "v_max3_u32 v40, v4, v5, v40\n" \
    "v_max3_u32 v40, v6, v7, v40\n" \
    "s_waitcnt vmcnt(1)\n" \
    "v_mfma_f32_16x16x32_bf16 v[32:35], v[176:179], v[8:11], v[32:35]\n" \
    "v_max3_u32 v40, v8, v9, v40\n" \
    "v_max3_u32 v40, v10, v11, v40\n" \
    "s_waitcnt vmcnt(0)\n" \
    "v_mfma_f32_16x16x32_bf16 v[32:35], v[176:179], v[12:15], v[32:35]\n" \
    "v_max3_u32 v40, v12, v13, v40\n" \
    "v_max3_u32 v40, v14, v15, v40\n" \
    "v_cmp_eq_u32 vcc, -1, v40\n" \
    "s_nop 3\n" \
    "s_cbranch_vccnz 38f\n" \
    "39:\n" \
    "v_add_f32_dpp v32, v32, v32 row_ror:8 row_mask:0xf bank_mask:0xf\n" \
    "v_add_f32_dpp v33, v33, v33 row_ror:8 row_mask:0xf bank_mask:0xf\n" \
    "s_mov_b32 exec_lo, 0x00ff00ff\n" \
    "s_mov_b32 exec_hi, 0x00ff00ff\n" \
    "v_fma_f32 v32, v32, v114, v172\n" \
    "v_fma_f32 v33, v33, v114, v173\n" \
    "v_cvt_pk_bf16_f32 v32, v32, v33\n" \
    "buffer_store_dword v32, v168, %[ru], s94 offen sc1\n" \
    "s_mov_b64 exec, -1\n" \
    "s_mov_b32 s36, 63\n" \
    "s_branch 9f\n" \
    "38:\n" \
    "s_add_i32 s39, s39, -1\n" \
    "s_cmp_lt_i32 s39, 0\n" \
    "s_cbranch_scc1 39b\n" \
    "s_add_i32 s30, s30, 0x10000\n" \
    "s_sleep 2\n" \
    "s_branch 36b\n" \
    "21:\n" \
    "s_add_i32 s39, s39, -1\n" \
    "s_cmp_lt_i32 s39, 0\n" \
    "s_cbranch_scc1 22b\n" \
    "buffer_load_dword v100, off, %[rw], s62 sc1\n" \
    "s_waitcnt vmcnt(0)\n" \
    "v_readfirstlane_b32 s38, v100\n" \
    "s_cmp_lt_i32 s38, s91\n" \
    "s_cbranch_scc1 21b\n" \
    "s_branch 22b\n" \
    "23:\n" \
    "s_add_i32 s39, s39, -1\n" \
    "s_cmp_lt_i32 s39, 0\n" \
    "s_cbranch_scc1 24b\n" \
    "s_sleep 4\n" \
    "buffer_load_dwordx2 v[98:99], v113, %[rs], s78 offen sc1\n" \
    "s_waitcnt vmcnt(0)\n" \
    "v_cmp_ne_u16 vcc, s91, v99\n" \
    "s_nop 1\n" \
    "s_cbranch_vccnz 23b\n" \
    "s_branch 24b\n" \
    "26:\n" \
    "s_add_i32 s39, s39, -1\n" \
    "s_cmp_lt_i32 s39, 0\n" \
    "s_cbranch_scc1 27b\n" \
    "s_sleep 4\n" \
    "s_add_i32 s44, %[qb], 0\n" \
    "s_add_i32 s45, s44, 4096\n" \
    "buffer_load_dwordx4 v[136:139], v112, %[rw], s44 offen offset:0 sc1\n" \
    "buffer_load_dwordx4 v[140:143], v112, %[rw], s44 offen offset:2048 sc1\n" \
    "buffer_load_dwordx4 v[144:147], v112, %[rw], s45 offen offset:0 sc1\n" \
    "buffer_load_dwordx4 v[148:151], v112, %[rw], s45 offen offset:2048 sc1\n" \
    "s_waitcnt vmcnt(0)\n" \
    "s_branch 25b\n" \
    "28:\n" \
    "s_add_i32 s39, s39, -1\n" \
    "s_cmp_lt_i32 s39, 0\n" \
    "s_cbranch_scc1 29b\n" \
    "s_sleep 4\n" \
    "s_add_i32 s44, %[qb], 262144\n" \
    "s_add_i32 s45, s44, 4096\n" \
    "buffer_load_dwordx4 v[72:75], v112, %[rw], s44 offen offset:0 sc1\n" \
    "buffer_load_dwordx4 v[76:79], v112, %[rw], s44 offen offset:2048 sc1\n" \
    "buffer_load_dwordx4 v[104:107], v112, %[rw], s45 offen offset:0 sc1\n" \
    "buffer_load_dwordx4 v[108:111], v112, %[rw], s45 offen offset:2048 sc1\n" \
    "s_waitcnt vmcnt(0)\n" \
    "v_max3_u32 v128, v72, v73, v72\n" \
    "v_max3_u32 v128, v74, v75, v128\n" \
    "v_max3_u32 v128, v76, v77, v128\n" \
    "v_max3_u32 v128, v78, v79, v128\n" \
    "v_max3_u32 v128, v104, v105, v128\n" \
    "v_max3_u32 v128, v106, v107, v128\n" \
    "v_max3_u32 v128, v108, v109, v128\n" \
    "v_max3_u32 v128, v110, v111, v128\n" \
    "v_cmp_eq_u32 vcc, -1, v128\n" \
    "s_nop 1\n" \
    "s_cbranch_vccnz 28b\n" \
    "s_branch 29b\n" \
    "30:\n" \
    "s_memtime vcc\n" \
    "s_waitcnt lgkmcnt(0)\n" \
    "s_sub_i32 s75, s90, vcc_lo\n" \
    "s_cmp_gt_i32 s75, 100\n" \
    "s_cbranch_scc1 30b\n" \
    "s_branch 31b\n" \
    "32:\n" \
    "v_max3_u32 v82, v0, v1, v0\n" \
    "v_max3_u32 v82, v2, v3, v82\n" \
    "v_max3_u32 v82, v4, v5, v82\n" \
    "v_max3_u32 v82, v6, v7, v82\n" \
    "v_max3_u32 v82, v8, v9, v82\n" \
    "v_max3_u32 v82, v10, v11, v82\n" \
    "v_max3_u32 v82, v12, v13, v82\n" \
    "v_max3_u32 v82, v14, v15, v82\n" \
    "v_max3_u32 v82, v16, v17, v82\n" \
    "v_max3_u32 v82, v18, v19, v82\n" \
    "v_max3_u32 v82, v20, v21, v82\n" \
    "v_max3_u32 v82, v22, v23, v82\n" \
    "v_max3_u32 v82, v24, v25, v82\n" \
    "v_max3_u32 v82, v26, v27, v82\n" \
    "v_max3_u32 v82, v28, v29, v82\n" \
    "v_max3_u32 v82, v30, v31, v82\n" \
    "v_max3_u32 v82, v32, v33, v82\n" \
    "v_max3_u32 v82, v34, v35, v82\n" \
    "v_cmp_eq_u32 vcc, -1, v82\n" \
    "s_nop 4\n" \
    "s_cbranch_vccz 34b\n" \
    "s_add_i32 s39, s39, -1\n" \
    "s_cmp_lt_i32 s39, 0\n" \
    "s_cbranch_scc1 34b\n" \
    "s_add_i32 s30, s30, 1\n" \
    "s_waitcnt lgkmcnt(0)\n" \
    "s_barrier\n" \
    "v_and_b32 v96, 0x200, v127\n" \
    "s_and_b32 s80, s26, 0x800\n" \
    "v_mad_u32_u24 v96, v96, 3, v127\n" \
    "v_xor_b32 v96, s80, v96\n" \
    "s_mov_b32 m0, s74\n" \
    "s_add_i32 s76, s74, 4096\n" \
    "buffer_load_dwordx4 v125, %[rs], s69 offen sc1 lds\n" \
    "s_mov_b32 m0, s76\n" \
    "v_mov_b64 v[80:81], 0\n" \
    "v_mov_b64 v[82:83], 0\n" \
    "buffer_load_dwordx4 v125, %[rs], s70 offen sc1 lds\n" \
    "s_movk_i32 m0, 0x2000\n" \
    "s_mov_b32 exec_lo, s27\n" \
    "s_mov_b32 exec_hi, s27\n" \
    "buffer_load_dwordx4 v96, %[rs], s71 offen sc1 lds\n" \
    "s_mov_b64 exec, -1\n" \
    "v_mov_b64 v[84:85], 0\n" \
    "v_mov_b64 v[86:87], 0\n" \
    "s_waitcnt vmcnt(0)\n" \
    "s_barrier\n" \
    "ds_read_b128 v[0:3], v37 offset:0\n" \
    "ds_read_b128 v[4:7], v38 offset:0\n" \
    "ds_read_b128 v[8:11], v39 offset:0\n" \
    "ds_read_b128 v[12:15], v132 offset:0\n" \
    "ds_read_b128 v[16:19], v37 offset:4096\n" \
    "ds_read_b128 v[20:23], v38 offset:4096\n" \
    "ds_read_b128 v[24:27], v39 offset:4096\n" \
    "ds_read_b128 v[28:31], v132 offset:4096\n" \
    "ds_read_b128 v[32:35], v126 offset:0\n" \
    "s_waitcnt lgkmcnt(8)\n" \
    "v_smfmac_f32_16x16x64_bf16 v[80:83], v[0:3], a[128:135], v124\n" \
    "s_waitcnt lgkmcnt(7)\n" \
    "v_smfmac_f32_16x16x64_bf16 v[80:83], v[4:7], a[144:151], v124\n" \
    "s_waitcnt lgkmcnt(6)\n" \
    "v_smfmac_f32_16x16x64_bf16 v[80:83], v[8:11], a[160:167], v124\n" \
    "s_waitcnt lgkmcnt(5)\n" \
    "v_smfmac_f32_16x16x64_bf16 v[80:83], v[12:15], a[176:183], v124\n" \
    "s_waitcnt lgkmcnt(4)\n" \
    "v_smfmac_f32_16x16x64_bf16 v[80:83], v[16:19], a[192:199], v124\n" \
    "s_waitcnt lgkmcnt(3)\n" \
    "v_smfmac_f32_16x16x64_bf16 v[80:83], v[20:23], a[208:215], v124\n" \
    "s_waitcnt lgkmcnt(2)\n" \
    "v_smfmac_f32_16x16x64_bf16 v[80:83], v[24:27], a[224:231], v124\n" \
    "s_waitcnt lgkmcnt(1)\n" \
    "v_smfmac_f32_16x16x64_bf16 v[80:83], v[28:31], a[240:247], v124\n" \
    "s_waitcnt lgkmcnt(0)\n" \
    "v_smfmac_f32_16x16x64_bf16 v[80:83], v[32:35], v[152:159], v124\n" \
    "v_smfmac_f32_16x16x64_bf16 v[84:87], v[0:3], a[136:143], v124\n" \
    "v_smfmac_f32_16x16x64_bf16 v[84:87], v[4:7], a[152:159], v124\n" \
    "v_smfmac_f32_16x16x64_bf16 v[84:87], v[8:11], a[168:175], v124\n" \
    "v_smfmac_f32_16x16x64_bf16 v[84:87], v[12:15], a[184:191], v124\n" \
    "v_smfmac_f32_16x16x64_bf16 v[84:87], v[16:19], a[200:207], v124\n" \
    "v_add_f32 v80, v80, v82\n" \
    "v_add_f32 v81, v81, v83\n" \
    "s_branch 32b\n" \
    "7:\n" \
    "s_mov_b32 s43, 2\n" \
    "s_nop 7\n" \
    "s_waitcnt vmcnt(0)\n" \
    "9:\n" \
    "s_mov_b32 m0, s68\n" \
    "s_add_i32 s37, s37, s29\n" \
    "s_waitcnt lgkmcnt(0)\n" \
    "v_mov_b32 v0, s36\n" \
    "v_mov_b32 v1, s37\n" \
    "v_mov_b32 v2, s38\n" \
    "v_mov_b32 v3, s39\n" \
    "v_mov_b32 v4, s40\n" \
    "v_mov_b32 v5, s41\n" \
    "v_mov_b32 v6, s42\n" \
    "v_mov_b32 v7, s43\n" \
    "v_mov_b32 v8, s28\n" \
    "v_mov_b32 v9, s29\n" \
    "v_mov_b32 v10, s30\n" \
    "v_mov_b32 v11, s31\n" \
    "v_mov_b32 v12, s32\n" \
    "v_mov_b32 v13, s33\n" \
    "v_mov_b32 v14, s34\n" \
    "v_mov_b32 v15, s35\n" \
    "v_mov_b32 v16, s24\n" \
    "v_mov_b32 v17, s25\n" \
    "v_mov_b32 v18, s26\n" \
    "v_mov_b32 v19, s27\n" \
    "v_mov_b32 v20, s84\n" \
    "v_mov_b32 v21, s86\n" \
    "v_mov_b32 v22, s88\n" \
    "v_mov_b32 v23, s84\n" \
    "v_mov_b32 v24, s60\n" \
    "v_mov_b32 v25, s61\n" \
    "v_mov_b32 v26, s62\n" \
    "v_mov_b32 v27, s63\n" \
    "v_mov_b32 v28, s64\n" \
    "v_mov_b32 v29, s65\n" \
    "v_mov_b32 v30, s66\n" \
    "v_mov_b32 v31, s67\n" \
    ".Lstep_end:\n" \
    ""
static_assert(2 * PART_STEP == 32768 && 2 * OFF_CAN == 1703936 && F_ERR == 2 * OFF_CAN + 65536 && MEMB_STRIDE == 32768 && OFF_VF - OFF_KF == 2048 * 4096 * 2 && OFF_BO == 65011712 && OFF_GT == 67518464 && BO_ROW == 4352, "constants of the step block");
#define STEP_CLOBBER "v32", "v33", "v34", "v35", "v36", "v37", "v38", "v39", "v40", "v41", "v42", "v43", "v44", "v45", "v46", "v47", "v48", "v49", "v50", "v51", "v52", "v53", "v54", "v55", "v56", "v57", "v58", "v59", "v60", "v61", "v62", "v63", "v64", "v65", "v66", "v67", "v68", "v69", "v70", "v71", "v72", "v73", "v74", "v75", "v76", "v77", "v78", "v79", "v80", "v81", "v82", "v83", "v84", "v85", "v86", "v87", "v88", "v89", "v90", "v91", "v92", "v93", "v94", "v95", "v98", "v99", "v100", "v101", "v102", "v103", "v104", "v105", "v106", "v107", "v108", "v109", "v110", "v111", "v112", "v113", "v114", "s24", "s25", "s26", "s27", "s28", "s29", "s30", "s31", "s32", "s33", "s34", "s35", "s36", "s37", "s38", "s39", "s40", "s41", "s42", "s43", "s44", "s45", "s46", "s47", "s48", "s49", "s50", "s51", "s52", "s53", "s54", "s55", "s56", "s57", "s58", "s59", "s60", "s61", "s62", "s63", "s64", "s65", "s66", "s67", "s68", "s69", "s70", "s71", "s72", "s73", "s74", "s75", "s76", "s77", "s78", "s79", "s80", "s81", "s82", "s83", "s84", "s85", "s86", "s87", "s88", "s89", "s90", "s91", "s92", "s93", "s94", "s95", "s96", "s97", "vcc", "scc", "memory"
#define OUT16_ASM \
    "s_mov_b32 exec_lo, %[tm]\n" \
    "s_mov_b32 exec_hi, 0\n" \
    "buffer_load_dword v79, %[tv], %[rw], %[so] offen sc1\n" \
    "s_mov_b64 exec, -1\n" \
    "s_add_i32 s44, %[so], 4096\n" \
    "s_add_i32 s45, %[so], 8192\n" \
    "s_add_i32 s46, %[so], 12288\n" \
    "buffer_load_dwordx4 v[16:19], %[vo], %[rw], %[so] offen offset:0 sc1\n" \
    "ds_read_b128 v[80:83], %[wl] offset:0\n" \
    "ds_read_b128 v[84:87], %[wl] offset:1024\n" \
    "s_nop 7\n" \
    "v_mov_b32 v104, 0\n" \
    "buffer_load_dwordx4 v[20:23], %[vo], %[rw], %[so] offen offset:256 sc1\n" \
    "s_nop 7\n" \
    "s_nop 3\n" \
    "buffer_load_dwordx4 v[24:27], %[vo], %[rw], %[so] offen offset:1024 sc1\n" \
    "s_nop 7\n" \
    "s_nop 3\n" \
    "buffer_load_dwordx4 v[28:31], %[vo], %[rw], %[so] offen offset:1280 sc1\n" \
    "s_nop 7\n" \
    "s_nop 3\n" \
    "buffer_load_dwordx4 v[32:35], %[vo], %[rw], %[so] offen offset:2048 sc1\n" \
    "s_nop 7\n" \
    "s_nop 3\n" \
    "buffer_load_dwordx4 v[36:39], %[vo], %[rw], %[so] offen offset:2304 sc1\n" \
    "s_nop 7\n" \
    "s_nop 3\n" \
    "buffer_load_dwordx4 v[40:43], %[vo], %[rw], %[so] offen offset:3072 sc1\n" \
    "s_nop 7\n" \
    "s_nop 3\n" \
    "buffer_load_dwordx4 v[44:47], %[vo], %[rw], %[so] offen offset:3328 sc1\n" \
    "s_nop 7\n" \
    "s_nop 3\n" \
    "buffer_load_dwordx4 v[48:51], %[vo], %[rw], s44 offen offset:0 sc1\n" \
    "s_nop 7\n" \
    "s_nop 3\n" \
    "buffer_load_dwordx4 v[52:55], %[vo], %[rw], s44 offen offset:256 sc1\n" \
    "s_nop 7\n" \
    "s_nop 3\n" \
    "buffer_load_dwordx4 v[56:59], %[vo], %[rw], s44 offen offset:1024 sc1\n" \
    "s_nop 7\n" \
    "s_nop 3\n" \
    "buffer_load_dwordx4 v[60:63], %[vo], %[rw], s44 offen offset:1280 sc1\n" \
    "ds_read_b128 v[88:91], %[wl] offset:2048\n" \
    "s_waitcnt vmcnt(11)\n" \
    "v_mfma_f32_16x16x32_bf16 v[0:3], a[0:3], v[16:19], 0\n" \
    "v_max3_u32 v104, v104, v16, v17\n" \
    "v_mfma_f32_16x16x32_bf16 v[4:7], a[128:131], v[16:19], 0\n" \
    "v_max3_u32 v104, v104, v18, v19\n" \
    "v_mfma_f32_16x16x32_bf16 v[8:11], v[128:131], v[16:19], 0\n" \
    "s_waitcnt lgkmcnt(2)\n" \
    "v_mfma_f32_16x16x32_bf16 v[12:15], v[80:83], v[16:19], 0\n" \
    "buffer_load_dwordx4 v[64:67], %[vo], %[rw], s44 offen offset:2048 sc1\n" \
    "ds_read_b128 v[80:83], %[wl] offset:3072\n" \
    "s_waitcnt vmcnt(11)\n" \
    "v_mfma_f32_16x16x32_bf16 v[0:3], a[4:7], v[20:23], v[0:3]\n" \
    "v_max3_u32 v104, v104, v20, v21\n" \
    "v_mfma_f32_16x16x32_bf16 v[4:7], a[132:135], v[20:23], v[4:7]\n" \
    "v_max3_u32 v104, v104, v22, v23\n" \
    "v_mfma_f32_16x16x32_bf16 v[8:11], v[132:135], v[20:23], v[8:11]\n" \
    "s_waitcnt lgkmcnt(2)\n" \
    "v_mfma_f32_16x16x32_bf16 v[12:15], v[84:87], v[20:23], v[12:15]\n" \
    "buffer_load_dwordx4 v[68:71], %[vo], %[rw], s44 offen offset:2304 sc1\n" \
    "ds_read_b128 v[84:87], %[wl] offset:4096\n" \
    "s_waitcnt vmcnt(11)\n" \
    "v_mfma_f32_16x16x32_bf16 v[0:3], a[8:11], v[24:27], v[0:3]\n" \
    "v_max3_u32 v104, v104, v24, v25\n" \
    "v_mfma_f32_16x16x32_bf16 v[4:7], a[136:139], v[24:27], v[4:7]\n" \
    "v_max3_u32 v104, v104, v26, v27\n" \
    "v_mfma_f32_16x16x32_bf16 v[8:11], v[136:139], v[24:27], v[8:11]\n" \
    "s_waitcnt lgkmcnt(2)\n" \
    "v_mfma_f32_16x16x32_bf16 v[12:15], v[88:91], v[24:27], v[12:15]\n" \
    "buffer_load_dwordx4 v[72:75], %[vo], %[rw], s44 offen offset:3072 sc1\n" \
    "ds_read_b128 v[88:91], %[wl] offset:5120\n" \
    "s_waitcnt vmcnt(11)\n" \
    "v_mfma_f32_16x16x32_bf16 v[0:3], a[12:15], v[28:31], v[0:3]\n" \
    "v_max3_u32 v104, v104, v28, v29\n" \
    "v_mfma_f32_16x16x32_bf16 v[4:7], a[140:143], v[28:31], v[4:7]\n" \
    "v_max3_u32 v104, v104, v30, v31\n" \
    "v_mfma_f32_16x16x32_bf16 v[8:11], v[140:143], v[28:31], v[8:11]\n" \
    "s_waitcnt lgkmcnt(2)\n" \
    "v_mfma_f32_16x16x32_bf16 v[12:15], v[80:83], v[28:31], v[12:15]\n" \
    "buffer_load_dwordx4 v[76:79], %[vo], %[rw], s44 offen offset:3328 sc1\n" \
    "ds_read_b128 v[80:83], %[wl] offset:6144\n" \
    "s_waitcnt vmcnt(11)\n" \
    "v_mfma_f32_16x16x32_bf16 v[0:3], a[16:19], v[32:35], v[0:3]\n" \
    "v_max3_u32 v104, v104, v32, v33\n" \
    "v_mfma_f32_16x16x32_bf16 v[4:7], a[144:147], v[32:35], v[4:7]\n" \
    "v_max3_u32 v104, v104, v34, v35\n" \
    "v_mfma_f32_16x16x32_bf16 v[8:11], v[144:147], v[32:35], v[8:11]\n" \
    "s_waitcnt lgkmcnt(2)\n" \
    "v_mfma_f32_16x16x32_bf16 v[12:15], v[84:87], v[32:35], v[12:15]\n" \
    "buffer_load_dwordx4 v[16:19], %[vo], %[rw], s45 offen offset:0 sc1\n" \
    "ds_read_b128 v[84:87], %[wl] offset:7168\n" \
    "s_waitcnt vmcnt(11)\n" \
    "v_mfma_f32_16x16x32_bf16 v[0:3], a[20:23], v[36:39], v[0:3]\n" \
    "v_max3_u32 v104, v104, v36, v37\n" \
    "v_mfma_f32_16x16x32_bf16 v[4:7], a[148:151], v[36:39], v[4:7]\n" \
    "v_max3_u32 v104, v104, v38, v39\n" \
    "v_mfma_f32_16x16x32_bf16 v[8:11], v[148:151], v[36:39], v[8:11]\n" \
    "s_waitcnt lgkmcnt(2)\n" \
    "v_mfma_f32_16x16x32_bf16 v[12:15], v[88:91], v[36:39], v[12:15]\n" \
    "buffer_load_dwordx4 v[20:23], %[vo], %[rw], s45 offen offset:256 sc1\n" \
    "ds_read_b128 v[88:91], %[wl] offset:8192\n" \
    "s_waitcnt vmcnt(11)\n" \
    "v_mfma_f32_16x16x32_bf16 v[0:3], a[24:27], v[40:43], v[0:3]\n" \
    "v_max3_u32 v104, v104, v40, v41\n" \
    "v_mfma_f32_16x16x32_bf16 v[4:7], a[152:155], v[40:43], v[4:7]\n" \
    "v_max3_u32 v104, v104, v42, v43\n" \
    "v_mfma_f32_16x16x32_bf16 v[8:11], v[152:155], v[40:43], v[8:11]\n" \
    "s_waitcnt lgkmcnt(2)\n" \
    "v_mfma_f32_16x16x32_bf16 v[12:15], v[80:83], v[40:43], v[12:15]\n" \
    "buffer_load_dwordx4 v[24:27], %[vo], %[rw], s45 offen offset:1024 sc1\n" \
    "ds_read_b128 v[80:83], %[wl] offset:9216\n" \
    "s_waitcnt vmcnt(11)\n" \
    "v_mfma_f32_16x16x32_bf16 v[0:3], a[28:31], v[44:47], v[0:3]\n" \
    "v_max3_u32 v104, v104, v44, v45\n" \
    "v_mfma_f32_16x16x32_bf16 v[4:7], a[156:159], v[44:47], v[4:7]\n" \
    "v_max3_u32 v104, v104, v46, v47\n" \
    "v_mfma_f32_16x16x32_bf16 v[8:11], v[156:159], v[44:47], v[8:11]\n" \
    "s_waitcnt lgkmcnt(2)\n" \
    "v_mfma_f32_16x16x32_bf16 v[12:15], v[84:87], v[44:47], v[12:15]\n" \
    "buffer_load_dwordx4 v[28:31], %[vo], %[rw], s45 offen offset:1280 sc1\n" \
    "ds_read_b128 v[84:87], %[wl] offset:10240\n" \
    "s_waitcnt vmcnt(11)\n" \
    "v_mfma_f32_16x16x32_bf16 v[0:3], a[32:35], v[48:51], v[0:3]\n" \
    "v_max3_u32 v104, v104, v48, v49\n" \
    "v_mfma_f32_16x16x32_bf16 v[4:7], a[160:163], v[48:51], v[4:7]\n" \
    "v_max3_u32 v104, v104, v50, v51\n" \
    "v_mfma_f32_16x16x32_bf16 v[8:11], v[160:163], v[48:51], v[8:11]\n" \
    "s_waitcnt lgkmcnt(2)\n" \
    "v_mfma_f32_16x16x32_bf16 v[12:15], v[88:91], v[48:51], v[12:15]\n" \
    "buffer_load_dwordx4 v[32:35], %[vo], %[rw], s45 offen offset:2048 sc1\n" \
    "ds_read_b128 v[88:91], %[wl] offset:11264\n" \
    "s_waitcnt vmcnt(11)\n" \
    "v_mfma_f32_16x16x32_bf16 v[0:3], a[36:39], v[52:55], v[0:3]\n" \
    "v_max3_u32 v104, v104, v52, v53\n" \
    "v_mfma_f32_16x16x32_bf16 v[4:7], a[164:167], v[52:55], v[4:7]\n" \
    "v_max3_u32 v104, v104, v54, v55\n" \
    "v_mfma_f32_16x16x32_bf16 v[8:11], v[164:167], v[52:55], v[8:11]\n" \
    "s_waitcnt lgkmcnt(2)\n" \
    "v_mfma_f32_16x16x32_bf16 v[12:15], v[80:83], v[52:55], v[12:15]\n" \
    "buffer_load_dwordx4 v[36:39], %[vo], %[rw], s45 offen offset:2304 sc1\n" \
    "ds_read_b128 v[80:83], %[wl] offset:12288\n" \
    "s_waitcnt vmcnt(11)\n" \
    "v_mfma_f32_16x16x32_bf16 v[0:3], a[40:43], v[56:59], v[0:3]\n" \
    "v_max3_u32 v104, v104, v56, v57\n" \
    "v_mfma_f32_16x16x32_bf16 v[4:7], a[168:171], v[56:59], v[4:7]\n" \
    "v_max3_u32 v104, v104, v58, v59\n" \
    "v_mfma_f32_16x16x32_bf16 v[8:11], v[168:171], v[56:59], v[8:11]\n" \
    "s_waitcnt lgkmcnt(2)\n" \
    "v_mfma_f32_16x16x32_bf16 v[12:15], v[84:87], v[56:59], v[12:15]\n" \
    "buffer_load_dwordx4 v[40:43], %[vo], %[rw], s45 offen offset:3072 sc1\n" \
    "ds_read_b128 v[84:87], %[wl] offset:13312\n" \
    "s_waitcnt vmcnt(11)\n" \
    "v_mfma_f32_16x16x32_bf16 v[0:3], a[44:47], v[60:63], v[0:3]\n" \
    "v_max3_u32 v104, v104, v60, v61\n" \
    "v_mfma_f32_16x16x32_bf16 v[4:7], a[172:175], v[60:63], v[4:7]\n" \
    "v_max3_u32 v104, v104, v62, v63\n" \
    "v_mfma_f32_16x16x32_bf16 v[8:11], v[172:175], v[60:63], v[8:11]\n" \
    "s_waitcnt lgkmcnt(2)\n" \
    "v_mfma_f32_16x16x32_bf16 v[12:15], v[88:91], v[60:63], v[12:15]\n" \
    "buffer_load_dwordx4 v[44:47], %[vo], %[rw], s45 offen offset:3328 sc1\n" \
    "ds_read_b128 v[88:91], %[wl] offset:14336\n" \
    "s_waitcnt vmcnt(11)\n" \
    "v_mfma_f32_16x16x32_bf16 v[0:3], a[48:51], v[64:67], v[0:3]\n" \
    "v_max3_u32 v104, v104, v64, v65\n" \
    "v_mfma_f32_16x16x32_bf16 v[4:7], a[176:179], v[64:67], v[4:7]\n" \
    "v_max3_u32 v104, v104, v66, v67\n" \
    "v_mfma_f32_16x16x32_bf16 v[8:11], v[176:179], v[64:67], v[8:11]\n" \
    "s_waitcnt lgkmcnt(2)\n" \
    "v_mfma_f32_16x16x32_bf16 v[12:15], v[80:83], v[64:67], v[12:15]\n" \
    "buffer_load_dwordx4 v[48:51], %[vo], %[rw], s46 offen offset:0 sc1\n" \
    "ds_read_b128 v[80:83], %[wl] offset:15360\n" \
    "s_waitcnt vmcnt(11)\n" \
    "v_mfma_f32_16x16x32_bf16 v[0:3], a[52:55], v[68:71], v[0:3]\n" \
    "v_max3_u32 v104, v104, v68, v69\n" \
    "v_mfma_f32_16x16x32_bf16 v[4:7], a[180:183], v[68:71], v[4:7]\n" \
    "v_max3_u32 v104, v104, v70, v71\n" \
    "v_mfma_f32_16x16x32_bf16 v[8:11], v[180:183], v[68:71], v[8:11]\n" \
    "s_waitcnt lgkmcnt(2)\n" \
    "v_mfma_f32_16x16x32_bf16 v[12:15], v[84:87], v[68:71], v[12:15]\n" \
    "buffer_load_dwordx4 v[52:55], %[vo], %[rw], s46 offen offset:256 sc1\n" \
    "ds_read_b128 v[84:87], %[wl] offset:16384\n" \
    "s_waitcnt vmcnt(11)\n" \
    "v_mfma_f32_16x16x32_bf16 v[0:3], a[56:59], v[72:75], v[0:3]\n" \
    "v_max3_u32 v104, v104, v72, v73\n" \
    "v_mfma_f32_16x16x32_bf16 v[4:7], a[184:187], v[72:75], v[4:7]\n" \
    "v_max3_u32 v104, v104, v74, v75\n" \
    "v_mfma_f32_16x16x32_bf16 v[8:11], v[184:187], v[72:75], v[8:11]\n" \
    "s_waitcnt lgkmcnt(2)\n" \
    "v_mfma_f32_16x16x32_bf16 v[12:15], v[88:91], v[72:75], v[12:15]\n" \
    "buffer_load_dwordx4 v[56:59], %[vo], %[rw], s46 offen offset:1024 sc1\n" \
    "ds_read_b128 v[88:91], %[wl] offset:17408\n" \
    "s_waitcnt vmcnt(11)\n" \
    "v_mfma_f32_16x16x32_bf16 v[0:3], a[60:63], v[76:79], v[0:3]\n" \
    "v_max3_u32 v104, v104, v76, v77\n" \
    "v_mfma_f32_16x16x32_bf16 v[4:7], a[188:191], v[76:79], v[4:7]\n" \
    "v_max3_u32 v104, v104, v78, v79\n" \
    "v_mfma_f32_16x16x32_bf16 v[8:11], v[188:191], v[76:79], v[8:11]\n" \
    "s_waitcnt lgkmcnt(2)\n" \
    "v_mfma_f32_16x16x32_bf16 v[12:15], v[80:83], v[76:79], v[12:15]\n" \
    "buffer_load_dwordx4 v[60:63], %[vo], %[rw], s46 offen offset:1280 sc1\n" \
    "ds_read_b128 v[80:83], %[wl] offset:18432\n" \
    "s_waitcnt vmcnt(11)\n" \
    "v_mfma_f32_16x16x32_bf16 v[0:3], a[64:67], v[16:19], v[0:3]\n" \
    "v_max3_u32 v104, v104, v16, v17\n" \
    "v_mfma_f32_16x16x32_bf16 v[4:7], a[192:195], v[16:19], v[4:7]\n" \
    "v_max3_u32 v104, v104, v18, v19\n" \
    "v_mfma_f32_16x16x32_bf16 v[8:11], v[192:195], v[16:19], v[8:11]\n" \
    "s_waitcnt lgkmcnt(2)\n" \
    "v_mfma_f32_16x16x32_bf16 v[12:15], v[84:87], v[16:19], v[12:15]\n" \
    "buffer_load_dwordx4 v[64:67], %[vo], %[rw], s46 offen offset:2048 sc1\n" \
    "ds_read_b128 v[84:87], %[wl] offset:19456\n" \
    "s_waitcnt vmcnt(11)\n" \
    "v_mfma_f32_16x16x32_bf16 v[0:3], a[68:71], v[20:23], v[0:3]\n" \
    "v_max3_u32 v104, v104, v20, v21\n" \
    "v_mfma_f32_16x16x32_bf16 v[4:7], a[196:199], v[20:23], v[4:7]\n" \
    "v_max3_u32 v104, v104, v22, v23\n" \
    "v_mfma_f32_16x16x32_bf16 v[8:11], v[196:199], v[20:23], v[8:11]\n" \
    "s_waitcnt lgkmcnt(2)\n" \
    "v_mfma_f32_16x16x32_bf16 v[12:15], v[88:91], v[20:23], v[12:15]\n" \
    "buffer_load_dwordx4 v[68:71], %[vo], %[rw], s46 offen offset:2304 sc1\n" \
    "ds_read_b128 v[88:91], %[wl] offset:20480\n" \
    "s_waitcnt vmcnt(11)\n" \
    "v_mfma_f32_16x16x32_bf16 v[0:3], a[72:75], v[24:27], v[0:3]\n" \
    "v_max3_u32 v104, v104, v24, v25\n" \
    "v_mfma_f32_16x16x32_bf16 v[4:7], a[200:203], v[24:27], v[4:7]\n" \
    "v_max3_u32 v104, v104, v26, v27\n" \
    "v_mfma_f32_16x16x32_bf16 v[8:11], v[200:203], v[24:27], v[8:11]\n" \
    "s_waitcnt lgkmcnt(2)\n" \
    "v_mfma_f32_16x16x32_bf16 v[12:15], v[80:83], v[24:27], v[12:15]\n" \
    "buffer_load_dwordx4 v[72:75], %[vo], %[rw], s46 offen offset:3072 sc1\n" \
    "ds_read_b128 v[80:83], %[wl] offset:21504\n" \
    "s_waitcnt vmcnt(11)\n" \
    "v_mfma_f32_16x16x32_bf16 v[0:3], a[76:79], v[28:31], v[0:3]\n" \
    "v_max3_u32 v104, v104, v28, v29\n" \
    "v_mfma_f32_16x16x32_bf16 v[4:7], a[204:207], v[28:31], v[4:7]\n" \
    "v_max3_u32 v104, v104, v30, v31\n" \
    "v_mfma_f32_16x16x32_bf16 v[8:11], v[204:207], v[28:31], v[8:11]\n" \
    "s_waitcnt lgkmcnt(2)\n" \
    "v_mfma_f32_16x16x32_bf16 v[12:15], v[84:87], v[28:31], v[12:15]\n" \
    "buffer_load_dwordx4 v[76:79], %[vo], %[rw], s46 offen offset:3328 sc1\n" \
    "ds_read_b128 v[84:87], %[wl] offset:22528\n" \
    "s_waitcnt vmcnt(11)\n" \
    "v_mfma_f32_16x16x32_bf16 v[0:3], a[80:83], v[32:35], v[0:3]\n" \
    "v_max3_u32 v104, v104, v32, v33\n" \
    "v_mfma_f32_16x16x32_bf16 v[4:7], a[208:211], v[32:35], v[4:7]\n" \
    "v_max3_u32 v104, v104, v34, v35\n" \
    "v_mfma_f32_16x16x32_bf16 v[8:11], v[208:211], v[32:35], v[8:11]\n" \
    "s_waitcnt lgkmcnt(2)\n" \
    "v_mfma_f32_16x16x32_bf16 v[12:15], v[88:91], v[32:35], v[12:15]\n" \
    "buffer_load_dwordx2 v[106:107], %[bv], %[rw], %[bs] offen sc1\n" \
    "ds_read_b128 v[88:91], %[wl] offset:23552\n" \
    "s_waitcnt vmcnt(11)\n" \
    "v_mfma_f32_16x16x32_bf16 v[0:3], a[84:87], v[36:39], v[0:3]\n" \
    "v_max3_u32 v104, v104, v36, v37\n" \
    "v_mfma_f32_16x16x32_bf16 v[4:7], a[212:215], v[36:39], v[4:7]\n" \
    "v_max3_u32 v104, v104, v38, v39\n" \
    "v_mfma_f32_16x16x32_bf16 v[8:11], v[212:215], v[36:39], v[8:11]\n" \
    "s_waitcnt lgkmcnt(2)\n" \
    "v_mfma_f32_16x16x32_bf16 v[12:15], v[80:83], v[36:39], v[12:15]\n" \
    "buffer_load_dword v108, %[gv], %[rw], %[gs] offen sc1\n" \
    "ds_read_b128 v[80:83], %[wl] offset:24576\n" \
    "s_waitcnt vmcnt(11)\n" \
    "v_mfma_f32_16x16x32_bf16 v[0:3], a[88:91], v[40:43], v[0:3]\n" \
    "v_max3_u32 v104, v104, v40, v41\n" \
    "v_mfma_f32_16x16x32_bf16 v[4:7], a[216:219], v[40:43], v[4:7]\n" \
    "v_max3_u32 v104, v104, v42, v43\n" \
    "v_mfma_f32_16x16x32_bf16 v[8:11], v[216:219], v[40:43], v[8:11]\n" \
    "s_waitcnt lgkmcnt(2)\n" \
    "v_mfma_f32_16x16x32_bf16 v[12:15], v[84:87], v[40:43], v[12:15]\n" \
    "buffer_load_dword v105, %[sv], %[rw], %[ss] offen sc1\n" \
    "ds_read_b128 v[84:87], %[wl] offset:25600\n" \
    "s_waitcnt vmcnt(11)\n" \
    "v_mfma_f32_16x16x32_bf16 v[0:3], a[92:95], v[44:47], v[0:3]\n" \
    "v_max3_u32 v104, v104, v44, v45\n" \
    "v_mfma_f32_16x16x32_bf16 v[4:7], a[220:223], v[44:47], v[4:7]\n" \
    "v_max3_u32 v104, v104, v46, v47\n" \
    "v_mfma_f32_16x16x32_bf16 v[8:11], v[220:223], v[44:47], v[8:11]\n" \
    "s_waitcnt lgkmcnt(2)\n" \
    "v_mfma_f32_16x16x32_bf16 v[12:15], v[88:91], v[44:47], v[12:15]\n" \
    "ds_read_b128 v[88:91], %[wl] offset:26624\n" \
    "s_waitcnt vmcnt(10)\n" \
    "v_mfma_f32_16x16x32_bf16 v[0:3], a[96:99], v[48:51], v[0:3]\n" \
    "v_max3_u32 v104, v104, v48, v49\n" \
    "v_mfma_f32_16x16x32_bf16 v[4:7], a[224:227], v[48:51], v[4:7]\n" \
    "v_max3_u32 v104, v104, v50, v51\n" \
    "v_mfma_f32_16x16x32_bf16 v[8:11], v[224:227], v[48:51], v[8:11]\n" \
    "s_waitcnt lgkmcnt(2)\n" \
    "v_mfma_f32_16x16x32_bf16 v[12:15], v[80:83], v[48:51], v[12:15]\n" \
    "ds_read_b128 v[80:83], %[wl] offset:27648\n" \
    "s_waitcnt vmcnt(9)\n" \
    "v_mfma_f32_16x16x32_bf16 v[0:3], a[100:103], v[52:55], v[0:3]\n" \
    "v_max3_u32 v104, v104, v52, v53\n" \
    "v_mfma_f32_16x16x32_bf16 v[4:7], a[228:231], v[52:55], v[4:7]\n" \
    "v_max3_u32 v104, v104, v54, v55\n" \
    "v_mfma_f32_16x16x32_bf16 v[8:11], v[228:231], v[52:55], v[8:11]\n" \
    "s_waitcnt lgkmcnt(2)\n" \
    "v_mfma_f32_16x16x32_bf16 v[12:15], v[84:87], v[52:55], v[12:15]\n" \
    "ds_read_b128 v[84:87], %[wl] offset:28672\n" \
    "s_waitcnt vmcnt(8)\n" \
    "v_mfma_f32_16x16x32_bf16 v[0:3], a[104:107], v[56:59], v[0:3]\n" \
    "v_max3_u32 v104, v104, v56, v57\n" \
    "v_mfma_f32_16x16x32_bf16 v[4:7], a[232:235], v[56:59], v[4:7]\n" \
    "v_max3_u32 v104, v104, v58, v59\n" \
    "v_mfma_f32_16x16x32_bf16 v[8:11], v[232:235], v[56:59], v[8:11]\n" \
    "s_waitcnt lgkmcnt(2)\n" \
    "v_mfma_f32_16x16x32_bf16 v[12:15], v[88:91], v[56:59], v[12:15]\n" \
    "ds_read_b128 v[88:91], %[wl] offset:29696\n" \
    "s_waitcnt vmcnt(7)\n" \
    "v_mfma_f32_16x16x32_bf16 v[0:3], a[108:111], v[60:63], v[0:3]\n" \
    "v_max3_u32 v104, v104, v60, v61\n" \
    "v_mfma_f32_16x16x32_bf16 v[4:7], a[236:239], v[60:63], v[4:7]\n" \
    "v_max3_u32 v104, v104, v62, v63\n" \
    "v_mfma_f32_16x16x32_bf16 v[8:11], v[236:239], v[60:63], v[8:11]\n" \
    "s_waitcnt lgkmcnt(2)\n" \
    "v_mfma_f32_16x16x32_bf16 v[12:15], v[80:83], v[60:63], v[12:15]\n" \
    "ds_read_b128 v[80:83], %[wl] offset:30720\n" \
    "s_waitcnt vmcnt(6)\n" \
    "v_mfma_f32_16x16x32_bf16 v[0:3], a[112:115], v[64:67], v[0:3]\n" \
    "v_max3_u32 v104, v104, v64, v65\n" \
    "v_mfma_f32_16x16x32_bf16 v[4:7], a[240:243], v[64:67], v[4:7]\n" \
    "v_max3_u32 v104, v104, v66, v67\n" \
    "v_mfma_f32_16x16x32_bf16 v[8:11], v[240:243], v[64:67], v[8:11]\n" \
    "s_waitcnt lgkmcnt(2)\n" \
    "v_mfma_f32_16x16x32_bf16 v[12:15], v[84:87], v[64:67], v[12:15]\n" \
    "ds_read_b128 v[84:87], %[wl] offset:31744\n" \
    "s_waitcnt vmcnt(5)\n" \
    "v_mfma_f32_16x16x32_bf16 v[0:3], a[116:119], v[68:71], v[0:3]\n" \
    "v_max3_u32 v104, v104, v68, v69\n" \
    "v_mfma_f32_16x16x32_bf16 v[4:7], a[244:247], v[68:71], v[4:7]\n" \
    "v_max3_u32 v104, v104, v70, v71\n" \
    "v_mfma_f32_16x16x32_bf16 v[8:11], v[244:247], v[68:71], v[8:11]\n" \
    "s_waitcnt lgkmcnt(2)\n" \
    "v_mfma_f32_16x16x32_bf16 v[12:15], v[88:91], v[68:71], v[12:15]\n" \
    "s_waitcnt vmcnt(4)\n" \
    "v_mfma_f32_16x16x32_bf16 v[0:3], a[120:123], v[72:75], v[0:3]\n" \
    "v_max3_u32 v104, v104, v72, v73\n" \
    "v_mfma_f32_16x16x32_bf16 v[4:7], a[248:251], v[72:75], v[4:7]\n" \
    "v_max3_u32 v104, v104, v74, v75\n" \
    "v_mfma_f32_16x16x32_bf16 v[8:11], v[248:251], v[72:75], v[8:11]\n" \
    "s_waitcnt lgkmcnt(1)\n" \
    "v_mfma_f32_16x16x32_bf16 v[12:15], v[80:83], v[72:75], v[12:15]\n" \
    "s_waitcnt vmcnt(3)\n" \
    "v_mfma_f32_16x16x32_bf16 v[0:3], a[124:127], v[76:79], v[0:3]\n" \
    "v_max3_u32 v104, v104, v76, v77\n" \
    "v_mfma_f32_16x16x32_bf16 v[4:7], a[252:255], v[76:79], v[4:7]\n" \
    "v_max3_u32 v104, v104, v78, v79\n" \
    "v_mfma_f32_16x16x32_bf16 v[8:11], v[252:255], v[76:79], v[8:11]\n" \
    "s_waitcnt lgkmcnt(0)\n" \
    "v_mfma_f32_16x16x32_bf16 v[12:15], v[84:87], v[76:79], v[12:15]\n" \
    "s_waitcnt vmcnt(0)\n" \
    "s_nop 7\n" \
    ""
#define OUT8_ASM \
    "s_mov_b32 exec_lo, %[tm]\n" \
    "s_mov_b32 exec_hi, 0\n" \
    "buffer_load_dword v79, %[tv], %[rw], %[so] offen sc1\n" \
    "s_mov_b64 exec, -1\n" \
    "s_add_i32 s44, %[so], 4096\n" \
    "s_add_i32 s45, %[so], 8192\n" \
    "s_add_i32 s46, %[so], 12288\n" \
    "buffer_load_dwordx4 v[16:19], %[vo], %[rw], %[so] offen offset:0 sc1\n" \
    "ds_read_b128 v[80:83], %[wl] offset:0\n" \
    "ds_read_b128 v[84:87], %[wl] offset:1024\n" \
    "ds_read_b128 v[88:91], %[wl] offset:2048\n" \
    "ds_read_b128 v[92:95], %[wl] offset:3072\n" \
    "v_mov_b64 v[0:1], 0\n" \
    "v_mov_b64 v[2:3], 0\n" \
    "v_mov_b64 v[4:5], 0\n" \
    "v_mov_b64 v[6:7], 0\n" \
    "v_mov_b64 v[8:9], 0\n" \
    "v_mov_b64 v[10:11], 0\n" \
    "v_mov_b64 v[12:13], 0\n" \
    "v_mov_b64 v[14:15], 0\n" \
    "v_mov_b32 v104, 0\n" \
    "buffer_load_dwordx4 v[20:23], %[vo], %[rw], %[so] offen offset:1024 sc1\n" \
    "s_nop 7\n" \
    "s_nop 3\n" \
    "buffer_load_dwordx4 v[24:27], %[vo], %[rw], %[so] offen offset:2048 sc1\n" \
    "s_nop 7\n" \
    "s_nop 3\n" \
    "buffer_load_dwordx4 v[28:31], %[vo], %[rw], %[so] offen offset:3072 sc1\n" \
    "s_nop 7\n" \
    "s_nop 3\n" \
    "buffer_load_dwordx4 v[32:35], %[vo], %[rw], s44 offen offset:0 sc1\n" \
    "s_nop 7\n" \
    "s_nop 3\n" \
    "buffer_load_dwordx4 v[36:39], %[vo], %[rw], s44 offen offset:1024 sc1\n" \
    "s_nop 7\n" \
    "s_nop 3\n" \
    "buffer_load_dwordx4 v[40:43], %[vo], %[rw], s44 offen offset:2048 sc1\n" \
    "s_nop 7\n" \
    "s_nop 3\n" \
    "buffer_load_dwordx4 v[44:47], %[vo], %[rw], s44 offen offset:3072 sc1\n" \
    "s_nop 7\n" \
    "s_nop 3\n" \
    "buffer_load_dwordx4 v[48:51], %[vo], %[rw], s45 offen offset:0 sc1\n" \
    "s_nop 7\n" \
    "s_nop 3\n" \
    "buffer_load_dwordx4 v[52:55], %[vo], %[rw], s45 offen offset:1024 sc1\n" \
    "ds_read_b128 v[96:99], %[wl] offset:4096\n" \
    "ds_read_b128 v[100:103], %[wl] offset:5120\n" \
    "s_waitcnt vmcnt(9)\n" \
    "v_smfmac_f32_16x16x64_bf16 v[0:3], v[16:19], a[0:7], %[idx]\n" \
    "v_max3_u32 v104, v104, v16, v17\n" \
    "v_smfmac_f32_16x16x64_bf16 v[4:7], v[16:19], a[128:135], %[idx]\n" \
    "v_max3_u32 v104, v104, v18, v19\n" \
    "v_smfmac_f32_16x16x64_bf16 v[8:11], v[16:19], v[128:135], %[idx]\n" \
    "s_waitcnt lgkmcnt(4)\n" \
    "v_smfmac_f32_16x16x64_bf16 v[12:15], v[16:19], v[80:87], %[idx]\n" \
    "buffer_load_dwordx4 v[56:59], %[vo], %[rw], s45 offen offset:2048 sc1\n" \
    "ds_read_b128 v[80:83], %[wl] offset:6144\n" \
    "ds_read_b128 v[84:87], %[wl] offset:7168\n" \
    "s_waitcnt vmcnt(9)\n" \
    "v_smfmac_f32_16x16x64_bf16 v[0:3], v[20:23], a[8:15], %[idx]\n" \
    "v_max3_u32 v104, v104, v20, v21\n" \
    "v_smfmac_f32_16x16x64_bf16 v[4:7], v[20:23], a[136:143], %[idx]\n" \
    "v_max3_u32 v104, v104, v22, v23\n" \
    "v_smfmac_f32_16x16x64_bf16 v[8:11], v[20:23], v[136:143], %[idx]\n" \
    "s_waitcnt lgkmcnt(4)\n" \
    "v_smfmac_f32_16x16x64_bf16 v[12:15], v[20:23], v[88:95], %[idx]\n" \
    "buffer_load_dwordx4 v[60:63], %[vo], %[rw], s45 offen offset:3072 sc1\n" \
    "ds_read_b128 v[88:91], %[wl] offset:8192\n" \
    "ds_read_b128 v[92:95], %[wl] offset:9216\n" \
    "s_waitcnt vmcnt(9)\n" \
    "v_smfmac_f32_16x16x64_bf16 v[0:3], v[24:27], a[16:23], %[idx]\n" \
    "v_max3_u32 v104, v104, v24, v25\n" \
    "v_smfmac_f32_16x16x64_bf16 v[4:7], v[24:27], a[144:151], %[idx]\n" \
    "v_max3_u32 v104, v104, v26, v27\n" \
    "v_smfmac_f32_16x16x64_bf16 v[8:11], v[24:27], v[144:151], %[idx]\n" \
    "s_waitcnt lgkmcnt(4)\n" \
    "v_smfmac_f32_16x16x64_bf16 v[12:15], v[24:27], v[96:103], %[idx]\n" \
    "buffer_load_dwordx4 v[64:67], %[vo], %[rw], s46 offen offset:0 sc1\n" \
    "ds_read_b128 v[96:99], %[wl] offset:10240\n" \
    "ds_read_b128 v[100:103], %[wl] offset:11264\n" \
    "s_waitcnt vmcnt(9)\n" \
    "v_smfmac_f32_16x16x64_bf16 v[0:3], v[28:31], a[24:31], %[idx]\n" \
    "v_max3_u32 v104, v104, v28, v29\n" \
    "v_smfmac_f32_16x16x64_bf16 v[4:7], v[28:31], a[152:159], %[idx]\n" \
    "v_max3_u32 v104, v104, v30, v31\n" \
    "v_smfmac_f32_16x16x64_bf16 v[8:11], v[28:31], v[152:159], %[idx]\n" \
    "s_waitcnt lgkmcnt(4)\n" \
    "v_smfmac_f32_16x16x64_bf16 v[12:15], v[28:31], v[80:87], %[idx]\n" \
    "buffer_load_dwordx4 v[68:71], %[vo], %[rw], s46 offen offset:1024 sc1\n" \
    "ds_read_b128 v[80:83], %[wl] offset:12288\n" \
    "ds_read_b128 v[84:87], %[wl] offset:13312\n" \
    "s_waitcnt vmcnt(9)\n" \
    "v_smfmac_f32_16x16x64_bf16 v[0:3], v[32:35], a[32:39], %[idx]\n" \
    "v_max3_u32 v104, v104, v32, v33\n" \
    "v_smfmac_f32_16x16x64_bf16 v[4:7], v[32:35], a[160:167], %[idx]\n" \
    "v_max3_u32 v104, v104, v34, v35\n" \
    "v_smfmac_f32_16x16x64_bf16 v[8:11], v[32:35], v[160:167], %[idx]\n" \
    "s_waitcnt lgkmcnt(4)\n" \
    "v_smfmac_f32_16x16x64_bf16 v[12:15], v[32:35], v[88:95], %[idx]\n" \
    "buffer_load_dwordx4 v[72:75], %[vo], %[rw], s46 offen offset:2048 sc1\n" \
    "ds_read_b128 v[88:91], %[wl] offset:14336\n" \
    "ds_read_b128 v[92:95], %[wl] offset:15360\n" \
    "s_waitcnt vmcnt(9)\n" \
    "v_smfmac_f32_16x16x64_bf16 v[0:3], v[36:39], a[40:47], %[idx]\n" \
    "v_max3_u32 v104, v104, v36, v37\n" \
    "v_smfmac_f32_16x16x64_bf16 v[4:7], v[36:39], a[168:175], %[idx]\n" \
    "v_max3_u32 v104, v104, v38, v39\n" \
    "v_smfmac_f32_16x16x64_bf16 v[8:11], v[36:39], v[168:175], %[idx]\n" \
    "s_waitcnt lgkmcnt(4)\n" \
    "v_smfmac_f32_16x16x64_bf16 v[12:15], v[36:39], v[96:103], %[idx]\n" \
    "buffer_load_dwordx4 v[76:79], %[vo], %[rw], s46 offen offset:3072 sc1\n" \
    "ds_read_b128 v[96:99], %[wl] offset:16384\n" \
    "ds_read_b128 v[100:103], %[wl] offset:17408\n" \
    "s_waitcnt vmcnt(9)\n" \
    "v_smfmac_f32_16x16x64_bf16 v[0:3], v[40:43], a[48:55], %[idx]\n" \
    "v_max3_u32 v104, v104, v40, v41\n" \
    "v_smfmac_f32_16x16x64_bf16 v[4:7], v[40:43], a[176:183], %[idx]\n" \
    "v_max3_u32 v104, v104, v42, v43\n" \
    "v_smfmac_f32_16x16x64_bf16 v[8:11], v[40:43], v[176:183], %[idx]\n" \
    "s_waitcnt lgkmcnt(4)\n" \
    "v_smfmac_f32_16x16x64_bf16 v[12:15], v[40:43], v[80:87], %[idx]\n" \
    "buffer_load_dwordx2 v[106:107], %[bv], %[rw], %[bs] offen sc1\n" \
    "ds_read_b128 v[80:83], %[wl] offset:18432\n" \
    "ds_read_b128 v[84:87], %[wl] offset:19456\n" \
    "s_waitcnt vmcnt(9)\n" \
    "v_smfmac_f32_16x16x64_bf16 v[0:3], v[44:47], a[56:63], %[idx]\n" \
    "v_max3_u32 v104, v104, v44, v45\n" \
    "v_smfmac_f32_16x16x64_bf16 v[4:7], v[44:47], a[184:191], %[idx]\n" \
    "v_max3_u32 v104, v104, v46, v47\n" \
    "v_smfmac_f32_16x16x64_bf16 v[8:11], v[44:47], v[184:191], %[idx]\n" \
    "s_waitcnt lgkmcnt(4)\n" \
    "v_smfmac_f32_16x16x64_bf16 v[12:15], v[44:47], v[88:95], %[idx]\n" \
    "buffer_load_dword v108, %[gv], %[rw], %[gs] offen sc1\n" \
    "ds_read_b128 v[88:91], %[wl] offset:20480\n" \
    "ds_read_b128 v[92:95], %[wl] offset:21504\n" \
    "s_waitcnt vmcnt(9)\n" \
    "v_smfmac_f32_16x16x64_bf16 v[0:3], v[48:51], a[64:71], %[idx]\n" \
    "v_max3_u32 v104, v104, v48, v49\n" \
    "v_smfmac_f32_16x16x64_bf16 v[4:7], v[48:51], a[192:199], %[idx]\n" \
    "v_max3_u32 v104, v104, v50, v51\n" \
    "v_smfmac_f32_16x16x64_bf16 v[8:11], v[48:51], v[192:199], %[idx]\n" \
    "s_waitcnt lgkmcnt(4)\n" \
    "v_smfmac_f32_16x16x64_bf16 v[12:15], v[48:51], v[96:103], %[idx]\n" \
    "buffer_load_dword v105, %[sv], %[rw], %[ss] offen sc1\n" \
    "ds_read_b128 v[96:99], %[wl] offset:22528\n" \
    "ds_read_b128 v[100:103], %[wl] offset:23552\n" \
    "s_waitcnt vmcnt(9)\n" \
    "v_smfmac_f32_16x16x64_bf16 v[0:3], v[52:55], a[72:79], %[idx]\n" \
    "v_max3_u32 v104, v104, v52, v53\n" \
    "v_smfmac_f32_16x16x64_bf16 v[4:7], v[52:55], a[200:207], %[idx]\n" \
    "v_max3_u32 v104, v104, v54, v55\n" \
    "v_smfmac_f32_16x16x64_bf16 v[8:11], v[52:55], v[200:207], %[idx]\n" \
    "s_waitcnt lgkmcnt(4)\n" \
    "v_smfmac_f32_16x16x64_bf16 v[12:15], v[52:55], v[80:87], %[idx]\n" \
    "ds_read_b128 v[80:83], %[wl] offset:24576\n" \
    "ds_read_b128 v[84:87], %[wl] offset:25600\n" \
    "s_waitcnt vmcnt(8)\n" \
    "v_smfmac_f32_16x16x64_bf16 v[0:3], v[56:59], a[80:87], %[idx]\n" \
    "v_max3_u32 v104, v104, v56, v57\n" \
    "v_smfmac_f32_16x16x64_bf16 v[4:7], v[56:59], a[208:215], %[idx]\n" \
    "v_max3_u32 v104, v104, v58, v59\n" \
    "v_smfmac_f32_16x16x64_bf16 v[8:11], v[56:59], v[208:215], %[idx]\n" \
    "s_waitcnt lgkmcnt(4)\n" \
    "v_smfmac_f32_16x16x64_bf16 v[12:15], v[56:59], v[88:95], %[idx]\n" \
    "ds_read_b128 v[88:91], %[wl] offset:26624\n" \
    "ds_read_b128 v[92:95], %[wl] offset:27648\n" \
    "s_waitcnt vmcnt(7)\n" \
    "v_smfmac_f32_16x16x64_bf16 v[0:3], v[60:63], a[88:95], %[idx]\n" \
    "v_max3_u32 v104, v104, v60, v61\n" \
    "v_smfmac_f32_16x16x64_bf16 v[4:7], v[60:63], a[216:223], %[idx]\n" \
    "v_max3_u32 v104, v104, v62, v63\n" \
    "v_smfmac_f32_16x16x64_bf16 v[8:11], v[60:63], v[216:223], %[idx]\n" \
    "s_waitcnt lgkmcnt(4)\n" \
    "v_smfmac_f32_16x16x64_bf16 v[12:15], v[60:63], v[96:103], %[idx]\n" \
    "ds_read_b128 v[96:99], %[wl] offset:28672\n" \
    "ds_read_b128 v[100:103], %[wl] offset:29696\n" \
    "s_waitcnt vmcnt(6)\n" \
    "v_smfmac_f32_16x16x64_bf16 v[0:3], v[64:67], a[96:103], %[idx]\n" \
    "v_max3_u32 v104, v104, v64, v65\n" \
    "v_smfmac_f32_16x16x64_bf16 v[4:7], v[64:67], a[224:231], %[idx]\n" \
    "v_max3_u32 v104, v104, v66, v67\n" \
    "v_smfmac_f32_16x16x64_bf16 v[8:11], v[64:67], v[224:231], %[idx]\n" \
    "s_waitcnt lgkmcnt(4)\n" \
    "v_smfmac_f32_16x16x64_bf16 v[12:15], v[64:67], v[80:87], %[idx]\n" \
    "ds_read_b128 v[80:83], %[wl] offset:30720\n" \
    "ds_read_b128 v[84:87], %[wl] offset:31744\n" \
    "s_waitcnt vmcnt(5)\n" \
    "v_smfmac_f32_16x16x64_bf16 v[0:3], v[68:71], a[104:111], %[idx]\n" \
    "v_max3_u32 v104, v104, v68, v69\n" \
    "v_smfmac_f32_16x16x64_bf16 v[4:7], v[68:71], a[232:239], %[idx]\n" \
    "v_max3_u32 v104, v104, v70, v71\n" \
    "v_smfmac_f32_16x16x64_bf16 v[8:11], v[68:71], v[232:239], %[idx]\n" \
    "s_waitcnt lgkmcnt(4)\n" \
    "v_smfmac_f32_16x16x64_bf16 v[12:15], v[68:71], v[88:95], %[idx]\n" \
    "s_waitcnt vmcnt(4)\n" \
    "v_smfmac_f32_16x16x64_bf16 v[0:3], v[72:75], a[112:119], %[idx]\n" \
    "v_max3_u32 v104, v104, v72, v73\n" \
    "v_smfmac_f32_16x16x64_bf16 v[4:7], v[72:75], a[240:247], %[idx]\n" \
    "v_max3_u32 v104, v104, v74, v75\n" \
    "v_smfmac_f32_16x16x64_bf16 v[8:11], v[72:75], v[240:247], %[idx]\n" \
    "s_waitcnt lgkmcnt(2)\n" \
    "v_smfmac_f32_16x16x64_bf16 v[12:15], v[72:75], v[96:103], %[idx]\n" \
    "s_waitcnt vmcnt(3)\n" \
    "v_smfmac_f32_16x16x64_bf16 v[0:3], v[76:79], a[120:127], %[idx]\n" \
    "v_max3_u32 v104, v104, v76, v77\n" \
    "v_smfmac_f32_16x16x64_bf16 v[4:7], v[76:79], a[248:255], %[idx]\n" \
    "v_max3_u32 v104, v104, v78, v79\n" \
    "v_smfmac_f32_16x16x64_bf16 v[8:11], v[76:79], v[248:255], %[idx]\n" \
    "s_waitcnt lgkmcnt(0)\n" \
    "v_smfmac_f32_16x16x64_bf16 v[12:15], v[76:79], v[80:87], %[idx]\n" \
    "s_waitcnt vmcnt(0)\n" \
    "s_nop 5\n" \
    "v_permlane32_swap_b32 v0, v8\n" \
    "v_add_f32 v0, v0, v8\n" \
    "v_permlane32_swap_b32 v1, v9\n" \
    "v_add_f32 v1, v1, v9\n" \
    "v_permlane32_swap_b32 v2, v10\n" \
    "v_add_f32 v2, v2, v10\n" \
    "v_permlane32_swap_b32 v3, v11\n" \
    "v_add_f32 v3, v3, v11\n" \
    "v_permlane32_swap_b32 v4, v12\n" \
    "v_add_f32 v4, v4, v12\n" \
    "v_permlane32_swap_b32 v5, v13\n" \
    "v_add_f32 v5, v5, v13\n" \
    "v_permlane32_swap_b32 v6, v14\n" \
    "v_add_f32 v6, v6, v14\n" \
    "v_permlane32_swap_b32 v7, v15\n" \
    "v_add_f32 v7, v7, v15\n" \
    ""
#define OUT_CLOBBER "v16", "v17", "v18", "v19", "v20", "v21", "v22", "v23", "v24", "v25", "v26", "v27", "v28", "v29", "v30", "v31", "v32", "v33", "v34", "v35", "v36", "v37", "v38", "v39", "v40", "v41", "v42", "v43", "v44", "v45", "v46", "v47", "v48", "v49", "v50", "v51", "v52", "v53", "v54", "v55", "v56", "v57", "v58", "v59", "v60", "v61", "v62", "v63", "v64", "v65", "v66", "v67", "v68", "v69", "v70", "v71", "v72", "v73", "v74", "v75", "v76", "v77", "v78", "v79", "v80", "v81", "v82", "v83", "v84", "v85", "v86", "v87", "v88", "v89", "v90", "v91", "v92", "v93", "v94", "v95", "v96", "v97", "v98", "v99", "v100", "v101", "v102", "v103", "s44", "s45", "s46", "scc", "memory"
constexpr int NREP = 2, NCP = 4;
#define OWNER_ASM \
    "s_getpc_b64 s[96:97]\n" \
    "14:\n" \
    "s_add_u32 s96, s96, 2f-14b\n" \
    "s_addc_u32 s97, s97, 0\n" \
    "s_sub_u32 s68, s96, 2f-11f\n" \
    "s_subb_u32 s69, s97, 0\n" \
    "s_mov_b32 %[np], 0\n" \
    "s_add_i32 s72, %[pb], 0x10000\n" \
    "13:\n" \
    "s_memtime vcc\n" \
    "s_waitcnt lgkmcnt(0)\n" \
    "s_sub_i32 s70, %[lk], vcc_lo\n" \
    "s_cmp_gt_i32 s70, %[rd]\n" \
    "s_cbranch_scc1 13b\n" \
    "s_add_i32 s70, %[pq], 0x10000\n" \
    "buffer_store_dwordx4 %[sv], %[ro], %[rs], %[pq] offen offset:0 sc1\n" \
    "buffer_store_dwordx4 %[sv], %[ro], %[rs], %[pq] offen offset:1024 sc1\n" \
    "buffer_store_dwordx4 %[sv], %[ro], %[rs], s70 offen offset:0 sc1\n" \
    "buffer_store_dwordx4 %[sv], %[ro], %[rs], s70 offen offset:1024 sc1\n" \
    "v_mov_b32 v2, -1\n" \
    "buffer_store_dword v2, %[rm], %[rs], %[mq] offen sc1\n" \
    "buffer_store_dwordx4 %[sv], %[rt], %[rw], %[tq] offen sc1\n" \
    "s_mov_b32 s71, 4\n" \
    "10:\n" \
    "s_memtime vcc\n" \
    "s_waitcnt lgkmcnt(0)\n" \
    "s_sub_i32 s70, %[lk], vcc_lo\n" \
    "s_cmp_gt_i32 s70, 112\n" \
    "s_cbranch_scc1 10b\n" \
    "s_max_i32 s70, s70, 0\n" \
    "s_memtime s[58:59]\n" \
    "s_memrealtime s[44:45]\n" \
    "s_nop 1\n" \
    "s_memrealtime s[46:47]\n" \
    "s_nop 1\n" \
    "s_memrealtime s[48:49]\n" \
    "s_nop 1\n" \
    "s_memrealtime s[50:51]\n" \
    "s_nop 1\n" \
    "s_memrealtime s[52:53]\n" \
    "s_nop 1\n" \
    "s_memrealtime s[54:55]\n" \
    "s_nop 1\n" \
    "s_memrealtime s[56:57]\n" \
    "s_nop 1\n" \
    "s_memrealtime vcc\n" \
    "s_waitcnt lgkmcnt(0)\n" \
    "s_add_i32 s44, s44, s46\n" \
    "s_add_i32 s48, s48, s50\n" \
    "s_add_i32 s52, s52, s54\n" \
    "s_add_i32 s44, s44, s48\n" \
    "s_add_i32 s52, s52, s56\n" \
    "s_add_i32 s44, s44, s52\n" \
    "s_add_i32 s59, s44, vcc_lo\n" \
    "s_sub_i32 s44, s59, %[oT]\n" \
    "s_mov_b32 %[oT], s59\n" \
    "s_sub_i32 s45, s58, %[oC]\n" \
    "s_mov_b32 %[oC], s58\n" \
    "s_mul_i32 s46, s44, %[oR]\n" \
    "s_lshr_b32 s46, s46, 12\n" \
    "s_sub_i32 s46, s45, s46\n" \
    "s_sub_i32 s44, s44, 1000\n" \
    "s_cmp_lt_u32 s44, 2000\n" \
    "s_cselect_b32 s46, s46, 0\n" \
    "s_add_i32 %[oR], %[oR], s46\n" \
    "s_sub_i32 s45, %[oT], %[c8]\n" \
    "s_and_b32 s70, s70, -4\n" \
    "s_sub_u32 vcc_lo, s68, s70\n" \
    "s_subb_u32 vcc_hi, s69, 0\n" \
    "s_setpc_b64 vcc\n" \
    "s_nop 0\n" \
    "s_nop 0\n" \
    "s_nop 0\n" \
    "s_nop 0\n" \
    "s_nop 0\n" \
    "s_nop 0\n" \
    "s_nop 0\n" \
    "s_nop 0\n" \
    "s_nop 0\n" \
    "s_nop 0\n" \
    "s_nop 0\n" \
    "s_nop 0\n" \
    "s_nop 0\n" \
    "s_nop 0\n" \
    "s_nop 0\n" \
    "s_nop 0\n" \
    "s_nop 0\n" \
    "s_nop 0\n" \
    "s_nop 0\n" \
    "s_nop 0\n" \
    "s_nop 0\n" \
    "s_nop 0\n" \
    "s_nop 0\n" \
    "s_nop 0\n" \
    "s_nop 0\n" \
    "s_nop 0\n" \
    "s_nop 0\n" \
    "s_nop 0\n" \
    "11:\n" \
    "buffer_load_dwordx2 v[0:1], %[ca], %[rs], %[cs] offen sc1\n" \
    "s_waitcnt vmcnt(0)\n" \
    "6:\n" \
    "s_memtime s[46:47]\n" \
    "v_subrev_u32 v0, s45, v0\n" \
    "v_cmp_ne_u16 vcc, %[tag], v1\n" \
    "s_add_i32 s48, %[oC], -136\n" \
    "v_max_i32_dpp v0, v0, v0 row_shr:1 row_mask:0xf bank_mask:0xf\n" \
    "v_max_u32_dpp v1, v1, v1 row_shr:1 row_mask:0xf bank_mask:0xf\n" \
    "s_cbranch_vccnz 7f\n" \
    "8:\n" \
    "v_max_i32_dpp v0, v0, v0 row_shr:2 row_mask:0xf bank_mask:0xf\n" \
    "v_max_u32_dpp v1, v1, v1 row_shr:2 row_mask:0xf bank_mask:0xf\n" \
    "s_nop 0\n" \
    "v_max_i32_dpp v0, v0, v0 row_shr:4 row_mask:0xf bank_mask:0xf\n" \
    "v_max_u32_dpp v1, v1, v1 row_shr:4 row_mask:0xf bank_mask:0xf\n" \
    "s_nop 0\n" \
    "v_max_i32_dpp v0, v0, v0 row_shr:8 row_mask:0xf bank_mask:0xf\n" \
    "v_max_u32_dpp v1, v1, v1 row_shr:8 row_mask:0xf bank_mask:0xf\n" \
    "s_waitcnt lgkmcnt(0)\n" \
    "v_max_i32_dpp v0, v0, v0 row_bcast:15 row_mask:0xa bank_mask:0xf\n" \
    "v_max_u32_dpp v1, v1, v1 row_bcast:15 row_mask:0xa bank_mask:0xf\n" \
    "s_add_i32 s46, s46, 44\n" \
    "v_max_i32_dpp v0, v0, v0 row_bcast:31 row_mask:0xc bank_mask:0xf\n" \
    "v_max_u32_dpp v1, v1, v1 row_bcast:31 row_mask:0xc bank_mask:0xf\n" \
    "v_readlane_b32 s44, v0, 63\n" \
    "v_readlane_b32 s49, v1, 63\n" \
    "s_mul_i32 s47, s44, %[oR]\n" \
    "s_ashr_i32 s47, s47, 12\n" \
    "s_add_i32 s47, s47, s48\n" \
    "s_sub_i32 s58, s47, s46\n" \
    "s_cmp_gt_i32 s58, 112\n" \
    "s_cbranch_scc1 1f\n" \
    "12:\n" \
    "s_max_i32 s58, s58, 0\n" \
    "s_and_b32 s58, s58, -4\n" \
    "s_sub_u32 vcc_lo, s96, s58\n" \
    "s_subb_u32 vcc_hi, s97, 0\n" \
    "s_setpc_b64 vcc\n" \
    "s_nop 0\n" \
    "s_nop 0\n" \
    "s_nop 0\n" \
    "s_nop 0\n" \
    "s_nop 0\n" \
    "s_nop 0\n" \
    "s_nop 0\n" \
    "s_nop 0\n" \
    "s_nop 0\n" \
    "s_nop 0\n" \
    "s_nop 0\n" \
    "s_nop 0\n" \
    "s_nop 0\n" \
    "s_nop 0\n" \
    "s_nop 0\n" \
    "s_nop 0\n" \
    "s_nop 0\n" \
    "s_nop 0\n" \
    "s_nop 0\n" \
    "s_nop 0\n" \
    "s_nop 0\n" \
    "s_nop 0\n" \
    "s_nop 0\n" \
    "s_nop 0\n" \
    "s_nop 0\n" \
    "s_nop 0\n" \
    "s_nop 0\n" \
    "s_nop 0\n" \
    "2:\n" \
    "buffer_load_dwordx4 v[0:3], %[o], %[rs], %[pb] offen offset:0 sc1\n" \
    "buffer_load_dwordx4 v[4:7], %[o], %[rs], %[pb] offen offset:1024 sc1\n" \
    "buffer_load_dwordx4 v[8:11], %[o], %[rs], s72 offen offset:0 sc1\n" \
    "buffer_load_dwordx4 v[12:15], %[o], %[rs], s72 offen offset:1024 sc1\n" \
    "s_add_i32 %[np], %[np], 1\n" \
    "s_add_i32 %[tm], s44, s45\n" \
    "s_lshr_b32 s50, s49, 16\n" \
    "s_add_i32 s50, s50, s44\n" \
    "s_add_i32 s50, s50, %[cw]\n" \
    "s_mul_i32 s50, s50, %[oR]\n" \
    "s_ashr_i32 s50, s50, 12\n" \
    "s_add_i32 %[lk], s50, %[oC]\n" \
    "s_waitcnt vmcnt(3)\n" \
    "v_mfma_f32_16x16x32_bf16 v[16:19], %[sel], v[0:3], 0\n" \
    "v_max3_u32 v20, v0, v1, v1\n" \
    "v_max3_u32 v20, v2, v3, v20\n" \
    "s_waitcnt vmcnt(2)\n" \
    "v_mfma_f32_16x16x32_bf16 v[16:19], %[sel], v[4:7], v[16:19]\n" \
    "v_max3_u32 v20, v4, v5, v20\n" \
    "v_max3_u32 v20, v6, v7, v20\n" \
    "s_waitcnt vmcnt(1)\n" \
    "v_mfma_f32_16x16x32_bf16 v[16:19], %[sel], v[8:11], v[16:19]\n" \
    "v_max3_u32 v20, v8, v9, v20\n" \
    "v_max3_u32 v20, v10, v11, v20\n" \
    "s_waitcnt vmcnt(0)\n" \
    "v_mfma_f32_16x16x32_bf16 v[16:19], %[sel], v[12:15], v[16:19]\n" \
    "v_max3_u32 v20, v12, v13, v20\n" \
    "v_max3_u32 v20, v14, v15, v20\n" \
    "v_cmp_eq_u32 vcc, -1, v20\n" \
    "s_nop 3\n" \
    "s_cbranch_vccnz 4f\n" \
    "3:\n" \
    "v_add_f32_dpp v16, v16, v16 row_ror:8 row_mask:0xf bank_mask:0xf\n" \
    "v_add_f32_dpp v17, v17, v17 row_ror:8 row_mask:0xf bank_mask:0xf\n" \
    "v_cvt_pk_bf16_f32 v16, v16, v17\n" \
    "buffer_store_dword v16, %[so], %[rs], %[mb0] offen sc1\n" \
    "s_branch 5f\n" \
    "1:\n" \
    "s_memtime vcc\n" \
    "s_waitcnt lgkmcnt(0)\n" \
    "s_sub_i32 s58, s47, vcc_lo\n" \
    "s_cmp_gt_i32 s58, 112\n" \
    "s_cbranch_scc1 1b\n" \
    "s_branch 12b\n" \
    "4:\n" \
    "s_add_i32 %[bud], %[bud], -1\n" \
    "s_cmp_lt_i32 %[bud], 0\n" \
    "s_cbranch_scc0 2b\n" \
    "s_branch 3b\n" \
    "7:\n" \
    "s_bcnt1_i32_b64 s44, vcc\n" \
    "s_cmp_le_u32 s44, s71\n" \
    "s_mov_b32 s71, 0\n" \
    "s_cbranch_scc1 15f\n" \
    "s_cmp_eq_u32 %[tag], 1\n" \
    "s_cbranch_scc1 9f\n" \
    "s_sleep 15\n" \
    "9:\n" \
    "s_sleep 6\n" \
    "15:\n" \
    "s_add_i32 %[bud], %[bud], -1\n" \
    "s_cmp_lt_i32 %[bud], 0\n" \
    "s_cbranch_scc1 8b\n" \
    "buffer_load_dwordx2 v[0:1], %[ca], %[rs], %[cs] offen sc1\n" \
    "s_memtime s[58:59]\n" \
    "s_memrealtime s[44:45]\n" \
    "s_nop 1\n" \
    "s_memrealtime s[46:47]\n" \
    "s_nop 1\n" \
    "s_memrealtime s[48:49]\n" \
    "s_nop 1\n" \
    "s_memrealtime s[50:51]\n" \
    "s_nop 1\n" \
    "s_memrealtime s[52:53]\n" \
    "s_nop 1\n" \
    "s_memrealtime s[54:55]\n" \
    "s_nop 1\n" \
    "s_memrealtime s[56:57]\n" \
    "s_nop 1\n" \
    "s_memrealtime vcc\n" \
    "s_waitcnt lgkmcnt(0)\n" \
    "s_add_i32 s44, s44, s46\n" \
    "s_add_i32 s48, s48, s50\n" \
    "s_add_i32 s52, s52, s54\n" \
    "s_add_i32 s44, s44, s48\n" \
    "s_add_i32 s52, s52, s56\n" \
    "s_add_i32 s44, s44, s52\n" \
    "s_add_i32 s59, s44, vcc_lo\n" \
    "s_sub_i32 s44, s59, %[oT]\n" \
    "s_mov_b32 %[oT], s59\n" \
    "s_sub_i32 s45, s58, %[oC]\n" \
    "s_mov_b32 %[oC], s58\n" \
    "s_mul_i32 s46, s44, %[oR]\n" \
    "s_lshr_b32 s46, s46, 12\n" \
    "s_sub_i32 s46, s45, s46\n" \
    "s_sub_i32 s44, s44, 1000\n" \
    "s_cmp_lt_u32 s44, 2000\n" \
    "s_cselect_b32 s46, s46, 0\n" \
    "s_add_i32 %[oR], %[oR], s46\n" \
    "s_sub_i32 s45, %[oT], %[c8]\n" \
    "s_waitcnt vmcnt(0)\n" \
    "s_branch 6b\n" \
    "5:\n" \
    ""
#define OWNER_CLOBBER "v0", "v1", "v2", "v3", "v4", "v5", "v6", "v7", "v8", "v9", "v10", "v11", "v12", "v13", "v14", "v15", "v16", "v17", "v18", "v19", "v20", "s44", "s45", "s46", "s47", "s48", "s49", "s50", "s51", "s52", "s53", "s54", "s55", "s56", "s57", "s58", "s59", "s68", "s69", "s70", "s71", "s72", "s96", "s97", "vcc", "scc", "memory"
constexpr int O_W = 0;
constexpr int O_STG = 131072;
constexpr int O_B = O_STG + 20480;
constexpr int OUT_REP = 7;
#define OUT_AUX 16
template <int O, class V> DI void put4(V& d, u32x4 f) {
#pragma unroll
    for (int j = 0; j < 4; ++j) d[O + j] = f[j];
}
DI void role_outp(const Args& a, LAS char* lds, unsigned lds_base, int nb, int rep, rsrc_t rw, int& budget) {
    const int tid = threadIdx.x, lane = tid & 63, w = __builtin_amdgcn_readfirstlane(tid >> 6);
    WG_BARRIER();
    const int n = lane & 15, g = lane >> 4;
    const int wl = O_W + w * 32768 + lane * 16;
    u32x32 A[8], V[4];
    {
        const int stg = O_STG + w * 5120;
        const rsrc_t ro = __builtin_amdgcn_make_buffer_rsrc((void*)a.Wo, 0, HD * DM * 2, 0x00020000);
        const int so = (1024 * w + (lane >> 3)) * (DM * 2) + nb * 128 + (lane & 7) * 16, sw = stg + (lane >> 3) * 160 + (lane & 7) * 16;
        const int ra = stg + (16 * (g & 1) + 4 * (g >> 1) + (n >> 2)) * 160 + (n & 3) * 8;
        constexpr int PB = 4;
        u32x4 b[PB][4];
        auto fetch = [&](int ks) {
#pragma unroll
            for (int i = 0; i < 4; ++i) b[ks % PB][i] = __builtin_amdgcn_raw_buffer_load_b128(ro, so, (64 * (ks >> 1) + 16 * (ks & 1) + 8 * (i & 1) + 32 * (i >> 1)) * (DM * 2), 0);
        };
        unsigned tp = now();
        const int pc = a.d1 >> 16;
#pragma unroll
        for (int ks = 0; ks < PB; ++ks) { fetch(ks); FENCE(); tp += pc; while ((int)(now() - tp) < 0) {} }
        FENCE();
        static_for<32>([&](auto ic) {
            constexpr int ks = decltype(ic)::value;
#pragma unroll
            for (int i = 0; i < 4; ++i) *(LAS u32x4*)(lds + sw + i * 1280) = b[ks % PB][i];
            if (ks + PB < 32) { fetch(ks + PB); FENCE(); tp += pc; }
            u32x4 f[4];
#pragma unroll
            for (int t = 0; t < 4; ++t) { const u32x2 lo = tr_read(lds, ra + t * 32), hi = tr_read(lds, ra + t * 32 + 1280); f[t] = (u32x4){lo.x, lo.y, hi.x, hi.y}; }
            put4<4 * (ks & 7)>(A[ks >> 3], f[0]); put4<4 * (ks & 7)>(A[4 + (ks >> 3)], f[1]); put4<4 * (ks & 7)>(V[ks >> 3], f[2]);
            *(LAS u32x4*)(lds + wl + ks * 1024) = f[3];
            if constexpr (ks == 7) asm volatile("" : "+{a[0:31]}"(A[0]), "+{a[128:159]}"(A[4]), "+{v[128:159]}"(V[0]));
            if constexpr (ks == 15) asm volatile("" : "+{a[32:63]}"(A[1]), "+{a[160:191]}"(A[5]), "+{v[160:191]}"(V[1]));
            if constexpr (ks == 23) asm volatile("" : "+{a[64:95]}"(A[2]), "+{a[192:223]}"(A[6]), "+{v[192:223]}"(V[2]));
            if constexpr (ks == 31) asm volatile("" : "+{a[96:127]}"(A[3]), "+{a[224:255]}"(A[7]), "+{v[224:255]}"(V[3]));
            FENCE();
            if (ks + PB < 31) while ((int)(now() - tp) < 0) {}
        });
    }
    wait_prog(rw, F_S4 + lane * 4, true, 1, budget);
    const rsrc_t ru = __builtin_amdgcn_make_buffer_rsrc(a.out, 0, SEQ * DM * 2, 0x00020000);
    constexpr int SP = 2 * LATE - 2;
    auto first = [&](int u) { return u >= SP ? 8 * u + 8 * SP : 16 * u; };
    auto st_s = [&](int u) { return (u >= 2 * FAST ? F_H + ((first(u) >> 4) - 2 * FAST) * 4096 : F_O + (u & 3) * 4) + w * 1024; };
    auto bn_s = [&](int u) { return OFF_BO + (64 * nb + 16 * w) * BO_ROW + first(u) * 2; };
    auto gn_s = [&](int u) { return OFF_GT + (first(u) + 4 * w) * 4; };
    int st = -1;
    u32x2 bn = ld64(rw, (lane >> 2) * BO_ROW + (lane & 3) * 8, bn_s(rep));
    float gn = __uint_as_float(ld32(rw, g * 4, gn_s(rep)));
    __builtin_amdgcn_s_waitcnt(0);
#define OUT_OPS "=&{v[0:15]}"(acc), "=&{v104}"(top), "=&{v105}"(st), "=&{v[106:107]}"(bn), "=&{v108}"(gn) \
        : "{a[0:31]}"(A[0]), "{a[32:63]}"(A[1]), "{a[64:95]}"(A[2]), "{a[96:127]}"(A[3]), "{a[128:159]}"(A[4]), "{a[160:191]}"(A[5]), "{a[192:223]}"(A[6]), "{a[224:255]}"(A[7]), \
          "{v[128:159]}"(V[0]), "{v[160:191]}"(V[1]), "{v[192:223]}"(V[2]), "{v[224:255]}"(V[3]), [rw] "s"(rw), [so] "s"(t0 * 8192), [wl] "v"(lds_base + O_W + w * 32768 + lane * 16), \
          [tv] "v"(tv), [tm] "s"(tm), [vo] "v"(vo), [sv] "v"(sv), [bv] "v"((lane >> 2) * BO_ROW + (lane & 3) * 8), [gv] "v"(g * 4), \
          [ss] "s"(st_s(u + OUT_REP)), [bs] "s"(bn_s(u + OUT_REP)), [gs] "s"(gn_s(u + OUT_REP))
    for (int u = rep; u < SP + 4; u += OUT_REP) {
        const bool fast = u >= 2 * FAST, sp = u >= SP;
        const int t0 = first(u);
        WG_BARRIER();
        int lane = opaque_lane(), n = lane & 15, g = lane >> 4;
        if (u == 3 * OUT_REP && nb == 0) __builtin_amdgcn_raw_buffer_store_b64(launch_tag(a), rw, w + lane == 0 ? F_EPOCH : OOB, 0, 16);
        S4S(0)
        TT(u >= 119, 0)
        *(LAS u32x2*)(lds + O_B + (16 * w + (lane >> 2)) * 40 + (lane & 3) * 8) = bn;
        const float gt = gn;
        const int tv = OFF_OF + w * 16384 + nb * 2048 + g * 65536 + n * 128, tm = __builtin_amdgcn_readfirstlane(nb < 8 ? sp ? 0xFFFF : -1 : 0);
        const int vo = OFF_OF + w * 16384 + (n & 7) * 16 + (sp ? (n >> 3) * 512 + g * 128 : (n >> 3) * 65536 + (g & 1) * 512 + (g >> 1) * 128);
        const int thr = fast ? 0 : u >> 1, sv = (lane & 7) * 128;
        if (!__all(st >= thr)) do { __builtin_amdgcn_s_sleep(1); st = (int)ld32(rw, sv, st_s(u)); asm volatile("" ::: "memory"); } while (!__all(st >= thr) && --budget >= 0);
        if (fast) { const unsigned t = now(); (void)wait_until(t + wave_max32(min((int)(2 * (st - t)) >> 1, 500))); }
        S4S(1)
        TT(u >= 119, 1)
        f32x16 acc; unsigned top;
        if (sp) {
            do asm volatile(OUT8_ASM : OUT_OPS, [idx] "v"(n & 8 ? 0xEEEE : 0x4444) : OUT_CLOBBER); while (__any(top == SENT) && --budget >= 0);
            TT(u >= 119, 2)
            lane = opaque_lane(); n = lane & 15; g = lane >> 4;
#pragma unroll
            for (int i = 0; i < 8; ++i) *(LAS float*)(lds + O_STG + (w * 16 + 4 * (g & 1) + (i & 3)) * 272 + (g >> 1) * 128 + (i >> 2) * 64 + n * 4) = acc[i];
        } else {
            do asm volatile(OUT16_ASM : OUT_OPS : OUT_CLOBBER); while (__any(top == SENT) && --budget >= 0);
            S4S(2)
            TT(u >= 119, 2)
            lane = opaque_lane(); n = lane & 15; g = lane >> 4;
#pragma unroll
            for (int t = 0; t < 4; ++t) *(LAS f32x4*)(lds + O_STG + (w * 16 + n) * 272 + t * 64 + g * 16) = (f32x4){acc[4 * t], acc[4 * t + 1], acc[4 * t + 2], acc[4 * t + 3]};
        }
        WG_BARRIER();
        S4S(3)
        const int tok = 4 * w + g, c4 = 4 * n;
        f32x4 r[4]; u16 bv[4];
#pragma unroll
        for (int ww = 0; ww < 4; ++ww) { r[ww] = *(LAS f32x4*)(lds + O_STG + (ww * 16 + tok) * 272 + c4 * 4); bv[ww] = *(LAS u16*)(lds + O_B + (c4 + ww) * 40 + tok * 2); }
        FENCE();
        const f32x4 v = gt * ((r[0] + r[1]) + (r[2] + r[3])) + (1.0f - gt) * (f32x4){bf2f(bv[0]), bf2f(bv[1]), bf2f(bv[2]), bf2f(bv[3])};
        __builtin_amdgcn_raw_buffer_store_b64((u32x2){pack2(v[0], v[1]), pack2(v[2], v[3])}, ru, sp && tok >= 8 ? OOB : (tok * DM + 64 * nb + c4) * 2, t0 * (DM * 2), OUT_AUX);
        S4S(4)
        TT(u >= 119, 3)
        TT(u >= 105 && u < 112, 4)
    }
}

constexpr int L_MEMS = 0;
constexpr int L_Q = 9216;
constexpr int L_KO = L_Q + 2560;
constexpr int L_KX = 16384;
constexpr int L_W = 32768;
constexpr int C_RING = 8;
static_assert(S_TOTAL <= L_SINK && L_W + C_RING * 16384 <= L_TOTAL && L_KO + 8 * 576 <= L_KX, "LDS budget");
static_assert(P_STG + P_RING * 16384 <= L_TOTAL, "LDS budget");

DI int pos2col(int p) { return p < 512 ? (p & ~63) + 16 * (p & 3) + ((p & 63) >> 2) : p; }
DI void unit_rc(int u, int& i, int& p0) {
    if (u < 512) { i = 2 * ((u >> 2) & 3) + (u & 1); p0 = (u & ~15) + 4 * (u & 2); }
    else { const int v = u - 512; i = (v >> 1) & 7; p0 = 512 + 16 * (v >> 4) + 8 * (v & 1); }
}
DI void mem0_arrange(LAS char* lds, int raw, int tid) {
#pragma unroll
    for (int k = 0; k < 2; ++k) {
        int i, p0; unit_rc(tid + 256 * k, i, p0);
        unsigned r[4];
#pragma unroll
        for (int j = 0; j < 4; ++j) r[j] = *(LAS unsigned*)(lds + raw + (i * DM + pos2col(p0)) * 2 + 32 * j);
        *(LAS u32x4*)(lds + L_MEMS + (tid + 256 * k) * 16) = (u32x4){__builtin_amdgcn_perm(r[1], r[0], 0x05040100), __builtin_amdgcn_perm(r[3], r[2], 0x05040100),
                                                                     __builtin_amdgcn_perm(r[1], r[0], 0x07060302), __builtin_amdgcn_perm(r[3], r[2], 0x07060302)};
    }
    int i, p0; unit_rc(512 + (tid & 63), i, p0);
    *(LAS u32x4*)(lds + L_MEMS + 8192 + (tid & 63) * 16) = *(LAS u32x4*)(lds + raw + (i * DM + p0) * 2);
}
DI unsigned read_canaries(int s, rsrc_t rs, int lane, int& budget) {
    const int o = 2 * OFF_CAN + (s & 3) * 16384 + (lane >> 2) * 512 + (lane & 3) * 16;
    u32x2 v;
    for (;;) {
        v = ld64(rs, o, 0);
        asm volatile("" ::: "memory");
        if (__all((v.y & 0xffff) == (unsigned)(s + 1))) break;
        if (--budget < 0) break;
    }
    const unsigned t = 8 * now();
    return t + (unsigned)wave_max32(min(max((int)(v.x - t), -0x4000), 0x4000));
}
DI u32x8 sm_frag(LAS char* lds, int sl, int r0, int sa, int sH, int cb, int lo8) {
    auto rd = [&](int r) { return tr_read(lds, sl + r * 256 + ((cb ^ (2 * swz(r))) * 16) + lo8); };
    const u32x2 a = rd(r0), b = rd(r0 + sa), c = rd(r0 + sH), d = rd(r0 + sa + sH);
    return (u32x8){a.x, a.y, b.x, b.y, c.x, c.y, d.x, d.y};
}
#define GS(slot, i)
#define GV(slot, i, v)
DI int tp_unit(int U, int sel) { return OFF_TP + cmap((U & 511) * 4096) + ((sel >> (4 + (U >> 8 & 1)) ^ U >> 9) & 1) * 8192; }
template <int N> DI void finish(const Args& a, rsrc_t rw, int U, int sel, unsigned t, int& budget) {
    const int lane = opaque_lane(), n = lane & 7, g = lane >> 4;
    const rsrc_t ru = __builtin_amdgcn_make_buffer_rsrc(a.out, 0, SEQ * DM * 2, 0x00020000);
    constexpr int M = (N + 1) / 2;
    int at[M]; f32x2 bv[M]; float gt[M];
#pragma unroll
    for (int i = 0; i < M; ++i) {
        const int u = U + 2 * i + (lane >> 3 & 1 & (N - 1));
        const int tok = LATE * SEG + (U < 512 ? 2 * (u & 15) + (n >> 2) : (u & 32) + 4 * (u & 7) + (n >> 1));
        const int col = U < 512 ? 32 * (u >> 4) + 16 * (g >> 1) + 4 * (n & 3) + 2 * (g & 1) : 512 + 2 * (u & 24) + 8 * (n & 1) + 2 * g;
#pragma unroll
        for (int r = 0; r < 2; ++r) bv[i][r] = bf2f(__builtin_amdgcn_raw_buffer_load_b16(rw, col * BO_ROW + tok * 2, OFF_BO + r * BO_ROW, 16));
        gt[i] = __uint_as_float(ld32(rw, tok * 4, OFF_GT));
        at[i] = (tok * DM + col) * 2;
    }
    u32x4 one;
#pragma unroll
    for (int j = 0; j < 4; ++j) one[j] = (lane >> 2 & 3) == j ? 0x3F80u << 16 * (lane & 1) : 0u;
#pragma unroll
    for (int i = 0; i < M; ++i) { bv[i] *= 1.0f - gt[i]; asm volatile("" : "+v"(bv[i]), "+v"(one)); }
    (void)wait_until(t);
    f32x4 v[N];
    for (;;) {
        u32x4 d[4 * N];
#pragma unroll
        for (int k = 0; k < 4 * N; ++k) d[k] = ld128(rw, lane * 16 + (k & 3) * 1024, tp_unit(U + (k >> 2), sel));
        unsigned top = 0;
#pragma unroll
        for (int i = 0; i < N; ++i) {
            asm volatile(M16 "%0, %1, %2, 0\n\t" M16 "%0, %1, %3, %0\n\t" M16 "%0, %1, %4, %0\n\t" M16 "%0, %1, %5, %0\n\ts_nop 7" : "=&v"(v[i]) : "v"(one), "v"(d[4 * i]), "v"(d[4 * i + 1]), "v"(d[4 * i + 2]), "v"(d[4 * i + 3]));
            top = mx4(mx4(mx4(mx4(top, d[4 * i]), d[4 * i + 1]), d[4 * i + 2]), d[4 * i + 3]);
        }
        if (!__any(top == SENT) || --budget < 0) break;
        __builtin_amdgcn_s_sleep(4);
    }
#pragma unroll
    for (int i = 0; i < M; ++i) {
        f32x2 x;
#pragma unroll
        for (int r = 0; r < 2; ++r) {
            auto both = [](float y) { return y + __int_as_float(__builtin_amdgcn_update_dpp(0, __float_as_int(y), 0x128, 0xf, 0xf, true)); };
            const float p = both(v[2 * i][r]), q = both(v[(2 * i + 1) & (N - 1)][r]);
            x[r] = gt[i] * (lane & 8 ? q : p) + bv[i][r];
        }
        __builtin_amdgcn_raw_buffer_store_b32(pack2(x[0], x[1]), ru, at[i], 0, OUT_AUX);
    }
}
DI void owner_loop(const Args& a, int G, rsrc_t rs, rsrc_t rw, int sel, unsigned tbase, unsigned cbase, int& budget) {
    const int lane = threadIdx.x & 63, n = lane & 15, g = lane >> 4;
    const bool ex = G >= 64;
    const u32x4 sent4 = {SENT, SENT, SENT, SENT};
    const int js = (n & 2) ? 8 : ex ? (n & 4) + 2 * (n & 1) + (n >> 3) : 2 * (n >> 2) + (n & 1);
    u32x4 selu;
#pragma unroll
    for (int j = 0; j < 4; ++j) selu[j] = (js == 2 * j ? 0x3F80u : 0u) | (js == 2 * j + 1 ? 0x3F800000u : 0u);
    const int o = (part_unit(G, 0) ^ (sel & 8) * 256) + lane * 16;
    const int so = (n >> 3) * 16384 + ((ex ? 32768 + (G & 4) * 512 + (G & 3) * 128 : (G & 16) * 256 + (G & 32) * 64 + (G & 15) * 128) ^ (~sel & 8) * 256) + (g & 1) * 4 +
                   (ex ? (n & 4) * 16 + (g >> 1) * 32 + (n & 3) * 8 : (n & 6) * 16 + (g >> 1) * 16 + (n & 1) * 8);
    unsigned fails = 0;
    const int c8 = G >= 64 ? a.c1 >> 16 & 255 : (G & 28) == 0 ? a.c1 >> 8 & 255 : a.c1 & 255;
    const bool gv_on = true; (void)gv_on;
    unsigned oT = now(), oC = (unsigned)__builtin_readcyclecounter(), oR;
    { const unsigned r = (unsigned)(512.0f * (float)(oC - cbase) * __builtin_amdgcn_rcpf((float)(oT - tbase))); asm volatile("s_nop 0\n\tv_readfirstlane_b32 %0, %1" : "=s"(oR) : "v"(r)); }
    oT = 8 * oT + 16;
    unsigned lk = oC;
    budget = __builtin_amdgcn_readfirstlane(budget);
    const int ca = 2 * OFF_CAN + (lane >> 2) * 512 + (blockIdx.x & (NCP - 1)) * 128 + (lane & 3) * 16;
    unsigned tc;
    const unsigned oR0 = oR;
    oR = 0;
#pragma unroll 1
    for (int s = -1; s < NSEG - 1; ++s) {
        asm volatile("" : "+s"(s));
        const int pb = (s & 1) * 2 * PART_STEP;
        int np;
        asm volatile(OWNER_ASM : [np] "=&s"(np), [tm] "=&s"(tc), [bud] "+s"(budget), [oT] "+s"(oT), [oC] "+s"(oC), [oR] "+s"(oR), [lk] "+s"(lk)
            : [c8] "s"(c8), [ca] "v"(s < 0 ? OOB : ca), [cs] "s"((s & 3) * 16384), [tag] "s"(s + 1), [o] "v"(s < 0 ? OOB : o), [so] "v"(s < 0 ? OOB : so), [sel] "v"(selu), [rs] "s"(rs), [pb] "s"(pb), [mb0] "s"(2 * (OFF_MEMB + ((s + 1) & 3) * MEMB_STRIDE)),
              [cw] "s"(a.cw - c8), [rd] "s"(a.rd), [sv] "v"(sent4), [ro] "v"(s < 0 ? OOB : o), [pq] "s"(2 * PART_STEP - pb), [rm] "v"(s < 0 ? OOB : so), [mq] "s"(2 * (OFF_MEMB + ((s + 2) & 3) * MEMB_STRIDE)),
              [rw] "s"(rw), [rt] "v"(s >= 24 && s < 56 ? lane * 16 : OOB), [tq] "s"(tp_unit(8 * G + (s >> 2 & 7), sel) + (s & 3) * 1024) : OWNER_CLOBBER);
        if (s < 0) { oR = oR0; continue; }
        fails += np > 1;
    }
    wait_prog(rw, F_S4 + lane * 4, true, 1, budget);
    const unsigned t = now(), t0 = t + min((int)(tc - 8 * t) >> 3, 100);
    finish<4>(a, rw, 4 * G + 4 * (G & 64), sel, t0 + (a.xa >> 8 & 255), budget);
    if (G >= 32 && G < 64) finish<1>(a, rw, 512 + G, sel, t0 + (a.xb >> 24), budget);
}
#define CORNER 5
DI void role_owner(const Args& a, unsigned lds_base, int ow, rsrc_t rw, int& budget) {
    const int tid = threadIdx.x, lane = tid & 63, w = __builtin_amdgcn_readfirstlane(tid >> 6);
    const unsigned tbase = now(), cbase = (unsigned)__builtin_readcyclecounter();
    if (ow < 56 && w) { prefetch(a, lds_base, ow & 3, (ow >> 2) * 3 + w - 1, 42); VM_DRAIN(); }
    if (w >= 2 || (w == 1 && (ow & ~11))) return;
    const u32x2 tag = launch_tag(a);
    tell_side(rw, tag);
    if (ow >= 56) {
        auto rtt = [&](int o) {
            unsigned long long t0, t1;
            asm volatile("s_memtime %0\n\ts_waitcnt lgkmcnt(0)" : "=s"(t0) :: "memory");
            st32(SENT, rw, lane == 0 ? 0 : OOB, o);
            asm volatile("s_waitcnt vmcnt(0)\n\ts_memtime %0\n\ts_waitcnt lgkmcnt(0)" : "=s"(t1) :: "memory");
            return (unsigned)(t1 - t0);
        };
        const int p = ow - 56, o = p == 0 ? 0 : p == 1 ? 32768 : p < 4 ? OFF_TP + (p - 2) * (2 << 20) : (p & 2) * 4096 + 1024 + (p & 1) * 1024;
        const int d = p == 1 ? 2048 : p < 4 ? 8192 : p & 1 ? 16384 : 4096;
        unsigned t0 = ~0u, t1 = ~0u;
        for (int r = 0; r < CORNER; ++r) { t0 = min(t0, rtt(o + r * 128)); t1 = min(t1, rtt(o + d + r * 128)); }
        const unsigned near = (t1 < t0) ^ (p == 1 ? die_side() : p > 4);
        __builtin_amdgcn_raw_buffer_store_b64((u32x2){(tag.x & ~7u) | near, tag.y}, rw, lane == 0 ? F_INIT + (105 + ow) * 128 : OOB, 0, 16);
    }
    const int sel = wait_init(rw, tag, budget, 161) ^ a.c1 >> 24;
    const rsrc_t rs = __builtin_amdgcn_make_buffer_rsrc(a.ws + (sel & 1) * 8192, 0, COMM_BYTES - 8192, 0x00020000);
    const int side = agree_side(rw, tag, ~tag, budget), cc = sel >> 1;
    const int G = (w ? 64 + 4 * side : 32 * side + (ow >> 1 & 24) + (ow & 4)) + ((cc ^ ow >> 1 ^ w) & 1) + ((cc ^ 2 * ow ^ ow >> 4) & 2);
    owner_loop(a, G, rs, rw, sel, tbase, cbase, budget);
}

template <int O, class V> DI void put8(V& d, u32x8 f) {
#pragma unroll
    for (int j = 0; j < 8; ++j) d[O + j] = f[j];
}
DI f32x4 SMF64(u32x4 a, u32x8 b, f32x4 c, int i) { asm volatile("s_nop 1\n\tv_smfmac_f32_16x16x64_bf16 %0, %1, %2, %3\n\ts_nop 7" : "+v"(c) : "v"(a), "v"(b), "v"(i)); return c; }
DI f32x4 SMF32(u32x2 a, u32x4 b, f32x4 c, int i) { asm volatile("s_nop 1\n\tv_smfmac_f32_16x16x32_bf16 %0, %1, %2, %3\n\ts_nop 7" : "+v"(c) : "v"(a), "v"(b), "v"(i)); return c; }
DI f32x4 MFV(u32x4 a, u32x4 b, f32x4 c) { asm volatile("s_nop 1\n\tv_mfma_f32_16x16x32_bf16 %0, %1, %2, %0\n\ts_nop 7" : "+v"(c) : "v"(a), "v"(b)); return c; }

DI void role_head(const Args& a, LAS char* lds, unsigned lds_base, int h, rsrc_t rw, int& budget) {
    const int tid = threadIdx.x, w = __builtin_amdgcn_readfirstlane(tid >> 6);
    char* ws = a.ws;
    const unsigned tbase = now(), cbase = (unsigned)__builtin_readcyclecounter();
    int tw = 0;
    u32x32 A0, A1, A2, A3, A4, A5, A6, A7, R0, R1, R2, R3; u32x8 R4;
    auto dma = [&](int voff, int soff, unsigned dst) { bdma(rw, voff, soff, dst); };
    auto fetch_kv = [&](int s, int lane) {
        const int n = lane & 15, g = lane >> 4;
        const int kxs = (g >> 1) * 1024 + ((g & 1) * 32 + 16 * (n >> 3) + (n & 7)) * 16;
        const int vxs = (g >> 1) * 1024 + ((g & 1) * 32 + n) * 16;
        const int so_ = (s * 32 + h) * 8192 + w * 2048;
        const unsigned xdst = lds_base + L_KX + w * 2048;
        dma(kxs, OFF_KF + so_, xdst); dma(kxs, OFF_KF + so_ + 128, xdst + 1024);
        dma(vxs, OFF_VF + so_, xdst + 8192); dma(vxs, OFF_VF + so_ + 256, xdst + 8192 + 1024);
    };
    u32x2 tag, in;
    unsigned wo_at;
    u32x8 S1, S2, S3, scon;
    int sel, kv_rdy = 0, tp, tq_, tx, U1, ua;
    auto setup = [&]() {
        sel = (init_ok(in, tag) ? init_sel(in) : wait_init(rw, tag, budget, 33 + 4 * h + w)) ^ a.c1 >> 24;
        R0[14] ^= (sel & 8) * 256; R0[15] ^= (sel & 8) * 256;
        scon[0] = w; scon[1] = 2 * OFF_CAN + (h >> 1) * 512 + (2 * (h & 1) + tw) * 16; scon[2] = F_KV + h * 512 + w * 128; scon[3] = 65536 + w * 4096;
        scon[4] = OFF_KF + h * 8192 + w * 2048; scon[5] = F_KVM + h * 256 + tw * 128; scon[6] = tw - 1; scon[7] = (tw ? OFF_VM : OFF_KM) + (h - NH) * 2048 + w * 512;
        const unsigned t = now(), c = (unsigned)__builtin_readcyclecounter();
        const float r = (float)(c - cbase) * __builtin_amdgcn_rcpf(8.0f * (float)(t - tbase));
        S1 = (u32x8){0u, (unsigned)a.e0, 0u, (unsigned)budget, (unsigned)a.d0, (unsigned)(4294967296.0f * __builtin_amdgcn_rcpf(r)), (unsigned)(a.c2 + a.b0), 0u};
        S2 = (u32x8){0u, 8 * t + 16, 0u, (unsigned)a.up, (unsigned)a.dn, (unsigned)a.c2, 0u, 1u};
        S3 = (u32x8){c, (unsigned)(4096.0f * r), 2u * OFF_MEMB + ((h >> 3 ^ sel >> 2) & 1) * 16384 + (w & 2) * 2048 + (~sel & 8) * 256 + (w & 1) * 1024, w == 3 ? ~0u : 0u};
        const int U0 = (2 * tw + (w >> 1)) * 64 + 8 * (w & 1);
        U1 = 4 * (2 * h + tw) + w;
        tp = tp_unit(U0, sel) + h * 128; tq_ = tp_unit(256 + U0, sel) + h * 128; tx = tp_unit(512 + (U0 >> 3) + 3 * (w & 1), sel) + h * 128; ua = tp_unit(256 + U1, sel);
        asm volatile("" : "+{v[0:7]}"(S1), "+{v[8:15]}"(S2), "+{v[16:23]}"(S3), "+{v[24:31]}"(scon), "+{v[120:151]}"(R0), "+s"(tp), "+s"(tq_), "+s"(tx), "+s"(U1), "+s"(ua), "+s"(sel));
    };
    {
        const int lane = tid & 63, n = lane & 15, g = lane >> 4;
#pragma unroll
        for (int j = 0; j < 3; ++j) glds16(a.mem0, min(4 * j + w, 8) * 1024 + lane * 16, lds_base + L_KX + min(4 * j + w, 8) * 1024);
        const int tq = n >> 2, tc = (n & 3) >> 1, lo8 = (n & 1) * 8;
        const int r_nat = 16 * (g >> 1) + 4 * (g & 1) + (tq & 1) + 8 * (tq >> 1);
        const int r_sig = 16 * (tq & 1) + 4 * (g >> 1) + (g & 1) + 2 * (tq >> 1);
        const int r_o = 16 * (tq & 1) + 8 * (g >> 1) + 4 * (g & 1) + (tq >> 1);
#define WFRAG(sl, kb, t) ((kb) < 8 ? sm_frag(lds, sl, r_sig, 32, 8, 4 * w + 2 * (t) + tc, lo8) : sm_frag(lds, sl, r_nat, 2, 32, 4 * w + 2 * (t) + tc, lo8))
        const u32x4 ro = ring_offs(w, lane, HD);
        auto issue = [&](int i) {
            const unsigned slot = lds_base + L_W + (i % C_RING) * 16384 + w * 4096;
            if (i == 23) ring_issue(a.Wo + (size_t)h * DH * DM + 512, ring_offs(w, lane, [](int r, int c) { return (r + 64 * (c >> 3)) * (DM * 2) + (c & 7) * 16; }), slot);
            else if (i < 28) ring_issue((i < 8 || i == 20 ? a.Wq : i < 16 || i == 21 ? a.Wk : a.Wv) + (size_t)(64 * (i < 16 ? i & 7 : i < 20 ? i - 12 : i < 24 ? 8 : i - 24)) * HD + h * DH, ro, slot);
            else ring_issue(a.Wo + (size_t)(h * DH + 64 * ((i & 1) ^ ((i - 28) >> 1))) * DM + 256 * tw + 128 * (i & 1), ring_offs(w, lane, DM), slot);
        };
        auto keep = [&](int at, u32x8 f) {
            *(LAS u32x4*)(lds + at + lane * 16) = (u32x4){f[0], f[1], f[2], f[3]};
            *(LAS u32x4*)(lds + at + 1024 + lane * 16) = (u32x4){f[4], f[5], f[6], f[7]};
        };
        u32x8 hold[8] = {};
#pragma unroll
        for (int i = 0; i < C_RING; ++i) issue(i);
        tag = launch_tag(a);
        tell_side(rw, tag);
        const int ai = 2 * (n >> 2) + (n & 1), ahf = (n >> 1) & 1;
        R0[0] = R0[1] = R0[2] = R0[3] = 0x3F803F80u;
        R0[4] = ahf ? 0xEEEE : 0x4444;
        R0[5] = (g >> 1) * 512 + (n >> 2) * 128 + (g & 1) * 64 + (n & 3) * 16;
        R0[6] = lds_base + L_MEMS + 8192 + g * 256 + ai * 32 + ahf * 16;
        R0[7] = lane * 16;
        R0[8] = lds_base + L_Q + 2 * g * 320 + (16 * w + n) * 4;
        R0[9] = lds_base + L_KO + 2 * g * 576 + (16 * w + (n & 8) + 4 * (n & 1) + ((n >> 1) & 3)) * 4;
        R0[10] = lds_base + L_Q + ai * 320 + g * 16;
        R0[11] = lds_base + L_KO + ai * 576 + g * 32 + ahf * 16;
        R0[12] = lds_base + L_KX + 8192 + w * 2048 + (g >> 1) * 256 + n * 16 + (g & 1) * 8;
        R0[13] = lds_base + L_W + w * 4096 + lane * 16;
        wo_at = lds_base + L_W + (4 + (w >> 1)) * 16384 + r_o * 256 + ((8 * (w & 1) + tc) ^ (2 * swz(r_o))) * 16 + lo8;
        asm volatile("" : "+{v[120:151]}"(R0), "+{v119}"(wo_at));
        u32x2 other;
        ring_wait<4 * C_RING + 1>();
        mem0_arrange(lds, L_KX, tid);
        static_for<7>([&](auto ic) {
            constexpr int G = decltype(ic)::value;
            ring_wait<(G == 2 || G == 5 || G == 6 ? 16 : 17)>();
            if constexpr (G == 5) { asm volatile("" : "+v"(other)); tw = agree_side(rw, tag, other, budget); }
            const int sl = L_W + (G & 1) * 65536;
            auto fr = [&](int j, int t) { return WFRAG(sl + ((j ^ w) << 14), 0, t); };
            if constexpr (G < 4) {
                u32x32& A = G == 0 ? A0 : G == 1 ? A2 : G == 2 ? A4 : A6;
                u32x32& B = G == 0 ? A1 : G == 1 ? A3 : G == 2 ? A5 : A7;
                put8<0>(A, fr(0, 0)); put8<8>(A, fr(0, 1)); put8<16>(A, fr(1, 0)); put8<24>(A, fr(1, 1));
                put8<0>(B, fr(2, 0)); put8<8>(B, fr(2, 1)); put8<16>(B, fr(3, 0)); put8<24>(B, fr(3, 1));
                if constexpr (G == 0) asm volatile("" : "+{a[0:31]}"(A0), "+{a[32:63]}"(A1));
                if constexpr (G == 1) asm volatile("" : "+{a[64:95]}"(A2), "+{a[96:127]}"(A3));
                if constexpr (G == 2) asm volatile("" : "+{a[128:159]}"(A4), "+{a[160:191]}"(A5));
                if constexpr (G == 3) asm volatile("" : "+{a[192:223]}"(A6), "+{a[224:255]}"(A7));
            } else if constexpr (G == 4) {
                put8<16>(R1, fr(0, 0)); put8<24>(R1, fr(0, 1)); put8<0>(R2, fr(1, 0)); put8<8>(R2, fr(1, 1));
                put8<16>(R2, fr(2, 0)); put8<24>(R2, fr(2, 1)); put8<0>(R3, fr(3, 0)); put8<8>(R3, fr(3, 1));
            } else if constexpr (G == 5) {
                put8<16>(R0, WFRAG(sl, 8, 0)); put8<24>(R0, WFRAG(sl, 8, 1)); put8<0>(R1, WFRAG(sl + 16384, 8, 0)); put8<8>(R1, WFRAG(sl + 16384, 8, 1));
                put8<16>(R3, WFRAG(sl + 32768, 8, 0)); put8<24>(R3, WFRAG(sl + 32768, 8, 1));
                hold[7] = sm_frag(lds, sl + 49152, r_o, 2, 32, 8 * (w >> 1) + 4 * tw + 2 * (w & 1) + tc, lo8);
                R0[14] = part_unit((2 * (4 * tw + w) + (n >> 3)) * 4 + g, h) + (n & 7) * 16;
                R0[15] = (g >> 1) == (w >> 1) ? part_unit(64 + 2 * (2 * tw + (w & 1)) + (g >> 1), h) + (g & 1) * 64 + n * 4 : OOB;
                asm volatile("" : "+{v[120:151]}"(R0));
            } else if constexpr (G == 6) {
#pragma unroll
                for (int j = 0; j < 7; ++j) hold[j] = fr(j >> 1, j & 1);
                R4 = fr(3, 1);
            }
            WG_BARRIER();
            if constexpr (G == 4) asm volatile("buffer_load_dwordx2 %0, %1, %2, 0 offen sc1" : "=v"(in) : "v"(init_at(33 + 4 * h + w)), "s"(rw) : "memory");
            if constexpr (G < 6)
#pragma unroll
                for (int i = 0; i < 4; ++i) issue(4 * G + 8 + i);
            if constexpr (G == 2) asm volatile("buffer_load_dwordx2 %0, off, %1, %2 sc1" : "=v"(other) : "s"(rw), "s"(side_at(16)) : "memory");
            if constexpr (G == 5) { asm volatile("" : "+v"(in)); setup(); }
            if constexpr (G == 6)
#pragma unroll
                for (int j = 0; j < 8; ++j) keep(L_W + (j >> 1) * 16384 + w * 4096 + (j & 1) * 2048, hold[j]);
        });
        asm volatile("" : "+{v[120:151]}"(R0), "+{v[152:183]}"(R1), "+{v[184:215]}"(R2), "+{v[216:247]}"(R3), "+{v[248:255]}"(R4));
    }
    const bool gv_on = tw == 0; (void)gv_on;
    const rsrc_t rs = __builtin_amdgcn_make_buffer_rsrc(ws + (sel & 1) * 8192, 0, COMM_BYTES - 8192, 0x00020000);
    const int fo = F_KVM + h * 256 + tw * 128;
    u32x2 vm = {};
    for (;;) {
        asm volatile(STEP_ASM
            : "+{v[96:97]}"(vm), "+{v[0:7]}"(S1), "+{v[8:15]}"(S2), "+{v[16:23]}"(S3), "+{v[24:31]}"(scon)
              , "+{a[0:31]}"(A0), "+{a[32:63]}"(A1), "+{a[64:95]}"(A2), "+{a[96:127]}"(A3), "+{a[128:159]}"(A4), "+{a[160:191]}"(A5), "+{a[192:223]}"(A6), "+{v[120:151]}"(R0), "+{v[152:183]}"(R1)
            : "{a[224:255]}"(A7), "{v[184:215]}"(R2), "{v[216:247]}"(R3), "{v[248:255]}"(R4), "{v119}"(wo_at),
              [rs] "s"(rs), [rw] "s"(rw), [qb] "s"(OFF_OF + LATE * 262144 + h * 8192 + (w & 1) * 256),
              [tp] "s"(tp), [tq] "s"(tq_), [tx] "s"(tx), [u] "s"(U1), [ua] "s"(ua), [fw] "s"(8 * (a.xb >> 16 & 255)), [ru] "s"(__builtin_amdgcn_make_buffer_rsrc(a.out, 0, SEQ * DM * 2, 0x00020000))
            : STEP_CLOBBER);
        const int s = __builtin_amdgcn_readfirstlane(S1[0]);
        const unsigned st = __builtin_amdgcn_readfirstlane(S1[7]);
        kv_rdy = __builtin_amdgcn_readfirstlane(S1[2]); budget = __builtin_amdgcn_readfirstlane(S1[3]);
        if (s == NSEG - 1) break;
        if (__builtin_expect(st & 2, 0)) {
            const int lane = opaque_lane();
            LAS char* z = (LAS char*)0;
            const f32x4 zero = {0, 0, 0, 0};
            const u32x4 ones = __builtin_shufflevector(R0, R0, 0, 1, 2, 3);
            const unsigned zr = lane >> 6;
            const int idx = R0[4];
            u32x4 qf[4];
            f32x4 s1 = zero, s2 = zero;
#pragma unroll
            for (int ks = 0; ks < 4; ++ks) {
                qf[ks] = *(LAS u32x4*)(z + R0[10] + ks * 64);
                s1 = MFV(*(LAS u32x4*)(lds + L_KX + lane * 16 + ks * 2048), qf[ks], s1);
                s2 = MFV(*(LAS u32x4*)(lds + L_KX + lane * 16 + ks * 2048 + 1024), qf[ks], s2);
            }
            f32x4 s0 = SMF64(*(LAS u32x4*)(z + R0[11]), __builtin_shufflevector(qf[0], qf[1], 0, 1, 2, 3, 4, 5, 6, 7), zero, idx);
            s0 = SMF64(*(LAS u32x4*)(z + R0[11] + 128), __builtin_shufflevector(qf[2], qf[3], 0, 1, 2, 3, 4, 5, 6, 7), s0, idx);
            const float sa = s0[0] + s0[2], sb = s0[1] + s0[3];
            float mx = fmaxf(fmaxf(fmaxf(fmaxf(s1[0], s1[1]), fmaxf(s1[2], s1[3])), fmaxf(fmaxf(s2[0], s2[1]), fmaxf(s2[2], s2[3]))), fmaxf(sa, sb));
            mx = fmaxf(mx, __shfl_xor(mx, 16)); mx = fmaxf(mx, __shfl_xor(mx, 32));
            auto ex2 = [&](float x, float y) { return pack2(__builtin_amdgcn_exp2f(x - mx), __builtin_amdgcn_exp2f(y - mx)); };
            const u32x4 px = {ex2(s1[0], s1[1]), ex2(s1[2], s1[3]), ex2(s2[0], s2[1]), ex2(s2[2], s2[3])};
            const unsigned pm = ex2(sa, sb);
            const f32x4 l = MFV(((u32x4){pm, zr, zr, zr}), ones, MFV(px, ones, zero));
            const u32x2 x0 = *(LAS u32x2*)(z + R0[12]), y0 = *(LAS u32x2*)(z + R0[12] + 1024), x1 = *(LAS u32x2*)(z + R0[12] + 512), y1 = *(LAS u32x2*)(z + R0[12] + 1536);
            const f32x4 o = SMF64(px, ((u32x8){x0.x, y0.x, x0.y, y0.y, x1.x, y1.x, x1.y, y1.y}), zero, idx) + SMF32(((u32x2){pm, zr}), ((u32x4){vm.x, vm.y, zr, zr}), zero, idx);
            const float i0 = __builtin_amdgcn_rcpf(l[0]), i1 = __builtin_amdgcn_rcpf(l[1]);
            *(LAS unsigned*)(z + R0[9] + 256) = pack2(o[0] * i0, o[2] * i0);
            *(LAS unsigned*)(z + R0[9] + 256 + 576) = pack2(o[1] * i1, o[3] * i1);
            WG_BARRIER();
            const u32x4 oa = *(LAS u32x4*)(z + R0[11] + 256 + (w >> 1) * 128), ob = *(LAS u32x4*)(z + R0[11] + 384 - (w >> 1) * 128);
            auto frag = [&](int at) { return __builtin_shufflevector(*(LAS u32x4*)(z + at), *(LAS u32x4*)(z + at + 1024), 0, 1, 2, 3, 4, 5, 6, 7); };
            f32x2 f[4];
#pragma unroll
            for (int T = 0; T < 4; ++T) f[T] = fold(SMF64(ob, frag(R0[13] + scon[3] + 32768 + T * 2048), SMF64(oa, frag(R0[13] + scon[3] + T * 2048), zero, idx), idx));
            const f32x2 fx = fold(SMF64(ob, frag((R0[13] ^ 8192) + 49152 + 2048), SMF64(oa, frag(R0[13] + 49152 + 2048), zero, idx), idx));
            const int pb = (s & 1) * 2 * PART_STEP;
            st128((u32x4){pack2(f[0].x, f[1].x), pack2(f[2].x, f[3].x), pack2(f[0].y, f[1].y), pack2(f[2].y, f[3].y)}, rs, R0[14], pb);
            st32(pack2(fx.x, fx.y), rs, R0[15], pb);
            if (s == LATE) { S1[7] = 4; continue; }
        }
        if (__builtin_expect((st & 2) || kv_rdy <= s, 0)) {
            while (kv_rdy <= s) { kv_rdy = prog_min(rw, F_KV + h * 512 + w * 128); if (--budget < 0) break; }
            fetch_kv(s + 1, opaque_lane());
        }
        if (st & 2) { S1[1] = read_canaries(s, rs, opaque_lane(), budget) + S1[6]; S2[2] += 0x10000; }
        if ((int)(S1[1] - S2[1]) > 0x8000) { S2[1] = 8 * now() + 16; S3[0] = (unsigned)__builtin_readcyclecounter(); }
        S1[0] = s + 1; S1[2] = kv_rdy; S1[3] = budget;
    }
    TT(true, 0)
    st32((unsigned)(NSEG - 2), rw, opaque_lane() == 0 && w == 0 ? fo : OOB, 0);
    TT(true, 2)
}

__global__ void __launch_bounds__(256, 1) fused_kernel(Args a) {
    extern __shared__ __attribute__((aligned(16))) char smem[];
    LAS char* lds = (LAS char*)smem;
    const unsigned lds_base = (unsigned)(uintptr_t)lds;
    const int w = __builtin_amdgcn_readfirstlane(threadIdx.x >> 6);
    const int b = blockIdx.x, bx = b & 7, bi = b >> 3;
    if (bi >= 28) {
        const void* pc; unsigned len;
        asm volatile("s_getpc_b64 %0\n1:\ts_mov_b32 %1, .Lstep_end-1b" : "=s"(pc), "=s"(len));
        touch(pc, min((bi - 28) * 32768 + threadIdx.x * 128, len), lds_base);
    }
    asm volatile("" :: "s"(a.Wq), "s"(a.cw));
    const rsrc_t rw = __builtin_amdgcn_make_buffer_rsrc(a.ws, 0, WS_USED, 0x00020000);
    int budget = SPIN_MAX;
#define RT(i)
    if (bx < 4) {
        if (bi < 16) role_head(a, lds, lds_base, 8 * bx + (bi >> 2) * 2 + (bi & 1), rw, budget);
        else role_owner(a, lds_base, 4 * (bi - 16) + bx, rw, budget);
    } else {
        const int fx = bx - 4, no = fx < 3 ? 14 : 22, j = 14 * fx + bi;
        if (bi >= no || (j & 2)) {
            const unsigned t = now() + (a.d1 & 0xffff);
            if (bi >= no) { if (w) prefetch(a, lds_base, bx, (bi - no) * 3 + w - 1, 3 * (32 - no)); }
            else {
                const int sh = (j >> 2) * 2 + (j & 1);
#pragma unroll
                for (int i = 0; i < 6; ++i) {
                    const int u = 6 * sh + i, c = u >> 3;
                    st128((u32x4){SENT, SENT, SENT, SENT}, rw, threadIdx.x * 16, (c < 18 ? c * 4 * PART_STEP : (c < 20 ? 2 * (OFF_MEMB + MEMB_STRIDE) - 18 * 32768 : 2 * OFF_CAN - 20 * 32768) + c * 32768) + (u & 7) * 4096);
                }
                const u32x2 tag = launch_tag(a);
                VM_DRAIN();
                WG_BARRIER();
                __builtin_amdgcn_raw_buffer_store_b64(tag, rw, threadIdx.x == 0 ? F_INIT + sh * 128 : OOB, 0, 16);
                RT(3)
            }
            (void)wait_until(t);
        }
        if (bi >= no) {
            const int t = 18 * fx + bi - no;
            role_s4d(a, lds, lds_base, t, rw, budget);
            RT(1)
            TT(true, 6)
            if (t < 9 * OUT_REP) role_outp(a, lds, lds_base, t % 9, t / 9, rw, budget);
        }
        else {
            if (!(j & 2)) role_kv(a, lds, lds_base, (j >> 2) * 2 + (j & 1), rw, budget);
            RT(1)
            TT(true, 6)
            role_qa(a, lds, lds_base, (j >> 2) * 2, (j & 2 ? 0 : FAST + 2) + (j & 1), (j & 2 ? FAST : FAST + 2) + (j & 1), rw, budget);
        }
    }
    RT(2)
    TT(true, 5)
    if (budget < 0 && threadIdx.x == 0) st32(0x1000u + b, rw, F_ERR, 0);
}

static int g_c1 = 34, g_c1m = 30, g_c1x = 46, g_c2 = 316, g_b0 = 32, g_up = 8, g_fl = 8, g_dn = 2, g_d0 = 1680, g_da = 100, g_ua = 48, g_d1 = 300, g_rd = 34, g_cw = 69, g_e0 = 107, g_x1 = 40, g_x2 = 140, g_x3 = 150, g_x4 = 75, g_x5 = 135, g_x6 = 157, g_x7 = 35, g_x8 = 210, g_pc = 45, g_cc = 0;
extern "C" void kernel_launch(void* const* d_in, const int* in_sizes, int n_in, void* d_out, int out_size, void* d_ws, size_t ws_size, hipStream_t stream) {
    Args a;
    a.x = (const u16*)d_in[0]; a.mem0 = (const u16*)d_in[1];
    a.Wq = (const u16*)d_in[2]; a.Wk = (const u16*)d_in[3]; a.Wv = (const u16*)d_in[4]; a.Wo = (const u16*)d_in[5];
    a.lAr = (const u16*)d_in[8]; a.Aim = (const u16*)d_in[9]; a.Cr = (const u16*)d_in[10]; a.Ci = (const u16*)d_in[11];
    a.ldt = (const u16*)d_in[12]; a.Dsk = (const u16*)d_in[13]; a.gw = (const u16*)d_in[14];
    a.out = (u16*)d_out;
    a.ws = (char*)(((uintptr_t)d_ws + ((2u << 20) - 1)) & ~(uintptr_t)((2u << 20) - 1));
    a.c1 = g_c1 | g_c1m << 8 | g_c1x << 16 | g_cc << 24; a.c2 = g_c2; a.d0 = g_d0 + g_da; a.b0 = g_b0; a.d1 = g_d1 | g_pc << 16; a.up = g_up | g_ua << 8 | g_fl << 16; a.dn = g_dn | g_d0 << 8; a.rd = 23 * g_rd - 300; a.cw = 8 * g_cw - 104; a.e0 = 8 * g_e0;
    a.xa = g_x1 | g_x2 << 8 | g_x5 << 16 | g_x7 << 24; a.xb = g_x6 | g_x3 << 8 | g_x4 << 16 | g_x8 << 24;
    (void)hipFuncSetAttribute((const void*)fused_kernel, hipFuncAttributeMaxDynamicSharedMemorySize, L_TOTAL);
    fused_kernel<<<256, 256, L_TOTAL, stream>>>(a);
}
```

```cpp
#include <hip/hip_runtime.h>
#include <hip/hip_bf16.h>
#include <stdint.h>

typedef unsigned short u16;
typedef __bf16 bf16x8 __attribute__((ext_vector_type(8)));
typedef __bf16 bf16x16 __attribute__((ext_vector_type(16)));
typedef _Float16 f16x2 __attribute__((ext_vector_type(2)));
typedef _Float16 f16x8 __attribute__((ext_vector_type(8)));
typedef short s16x4 __attribute__((ext_vector_type(4)));
typedef float f32x2 __attribute__((ext_vector_type(2)));
typedef float f32x4 __attribute__((ext_vector_type(4)));
typedef float f32x16 __attribute__((ext_vector_type(16)));
typedef unsigned u32x2 __attribute__((ext_vector_type(2)));
typedef unsigned u32x4 __attribute__((ext_vector_type(4)));
typedef unsigned u32x8 __attribute__((ext_vector_type(8)));
typedef unsigned u32x16 __attribute__((ext_vector_type(16)));
typedef unsigned u32x32 __attribute__((ext_vector_type(32)));
typedef short s16x8 __attribute__((ext_vector_type(8)));
#define LAS __attribute__((address_space(3)))
#define DI __device__ __forceinline__
typedef __amdgpu_buffer_rsrc_t rsrc_t;

constexpr int SEQ = 2048, DM = 576, NH = 32, DH = 128, HD = NH * DH, NM = 8, SEG = 32, NSEG = 64, NS = 128;
constexpr unsigned SENT = 0xFFFFFFFFu;
constexpr unsigned OOB = 0x7FFFFFF0u;
constexpr int SPIN_MAX = 1 << 20;

constexpr int PART_STEP = 16384;
constexpr int OFF_MEMB = 40 * PART_STEP;
constexpr int MEMB_STRIDE = 32768;
constexpr int OFF_CAN = 52 * PART_STEP;
constexpr int COMM_LOGICAL = OFF_CAN + 32768;
constexpr int COMM_BYTES = 2 * COMM_LOGICAL;
constexpr int F_ERR = COMM_BYTES;
constexpr int F_KV = F_ERR + 128;
constexpr int F_KVM = F_KV + 16384;
constexpr int F_O = F_KVM + 8192;
constexpr int F_H = F_O + 4096;
constexpr int F_S4 = F_H + 32768;
constexpr int F_DONE = F_S4 + 512;
constexpr int INIT_BYTES = 2 * OFF_CAN + 131072;
constexpr int F_INIT = INIT_BYTES;
constexpr int F_CL = F_INIT + 24576;
constexpr int F_EPOCH = F_CL + 16384;
static_assert(F_DONE + 128 <= INIT_BYTES, "");
static_assert(F_EPOCH + 128 <= (2 << 20), "hand-off region");
constexpr int OFF_TP = 2 << 20;
constexpr int OFF_KF = 6 << 20;
constexpr int OFF_VF = OFF_KF + SEQ * HD * 2;
constexpr int OFF_OF = OFF_VF + SEQ * HD * 2;
constexpr int OFF_KM = OFF_OF + SEQ * HD * 2;
constexpr int OFF_VM = OFF_KM + NSEG * NH * NM * DH * 2;
constexpr int FAST = NSEG - 6;
constexpr int LATE = NSEG - 2;
constexpr int OFF_BO = OFF_VM + NSEG * NH * NM * DH * 2;
constexpr int BO_ROW = 4352;
constexpr int OFF_GT = OFF_BO + DM * BO_ROW;
constexpr int OFF_DBG = OFF_GT + SEQ * 4;
constexpr int WS_USED = OFF_DBG + (4 << 20);

struct Args {
    const u16 *x, *mem0, *Wq, *Wk, *Wv, *Wo, *lAr, *Aim, *Cr, *Ci, *ldt, *Dsk, *gw;
    u16* out; char* ws;
    int c1, c2, d0, b0, d1, up, dn, rd, cw, e0;
    unsigned xa, xb;
};

DI float bf2f(u16 b) { return __uint_as_float((unsigned)b << 16); }
DI float bflo(unsigned w) { return __uint_as_float(w << 16); }
DI float bfhi(unsigned w) { return __uint_as_float(w & 0xFFFF0000u); }
DI u16 f2bf(float f) { return __builtin_bit_cast(u16, (__bf16)f); }
DI unsigned pack2(float lo, float hi) {
    typedef __bf16 bf16x2 __attribute__((ext_vector_type(2)));
    bf16x2 v = {(__bf16)lo, (__bf16)hi};
    return __builtin_bit_cast(unsigned, v);
}
#define WG_BARRIER() asm volatile("s_waitcnt lgkmcnt(0)\n\ts_barrier" ::: "memory")
#define VM_DRAIN() asm volatile("s_waitcnt vmcnt(0)" ::: "memory")
DI int opaque_lane() { int l; asm volatile("v_mbcnt_lo_u32_b32 %0, -1, 0\n\tv_mbcnt_hi_u32_b32 %0, -1, %0" : "=v"(l)); return l; }
DI int cmap(int o) { return (o & 0x1FFF) | ((o >> 13) << 14); }
DI int part_unit(int G, int h) { const int p = G < 64 ? G & 31 : 32 + (G & 3); return (p >> 2) * 131072 + cmap((p & 3) * 4096) + ((G >> (G < 64 ? 5 : 2) ^ p >> 3) & 1) * 2048 + (h >> 4) * 65536 + (h & 15) * 128; }
DI unsigned mx4(unsigned a, u32x4 v) { a = max(max(a, v.x), v.y); return max(max(a, v.z), v.w); }
DI unsigned now() { return (unsigned)__builtin_amdgcn_s_memrealtime(); }
DI unsigned wait_until(unsigned t) {
    unsigned v; int guard = 8192;
    do { v = now(); } while ((int)(t - v) >= 4 && --guard > 0);
    const int r = __builtin_amdgcn_readfirstlane((int)(t - v));
    asm volatile("s_cmp_lt_i32 %0, 1\n\ts_cbranch_scc1 1f\n\ts_nop 3\n\ts_cmp_lt_i32 %0, 2\n\ts_cbranch_scc1 1f\n\ts_nop 3\n\ts_cmp_lt_i32 %0, 3\n\ts_cbranch_scc1 1f\n\ts_nop 7\n1:" :: "s"(r) : "scc");
    return r > 0 ? t : v + 2;
}
DI void st128(u32x4 v, rsrc_t rs, int off, int soff) {
    __builtin_amdgcn_raw_buffer_store_b128(v, rs, off, soff, 16);
    asm volatile("s_nop 1" :: "v"(v) : "memory");
}
DI void st32(unsigned v, rsrc_t rs, int off, int soff) { __builtin_amdgcn_raw_buffer_store_b32(v, rs, off, soff, 16); }
DI u32x4 ld128(rsrc_t rs, int off, int soff) { return __builtin_amdgcn_raw_buffer_load_b128(rs, off, soff, 16); }
DI u32x2 ld64(rsrc_t rs, int off, int soff) { return __builtin_amdgcn_raw_buffer_load_b64(rs, off, soff, 16); }
DI unsigned ld32(rsrc_t rs, int off, int soff) { return __builtin_amdgcn_raw_buffer_load_b32(rs, off, soff, 16); }
DI f32x2 fold(f32x4 a) { return (f32x2){a[0], a[1]} + (f32x2){a[2], a[3]}; }
DI float dpp_xor1(float v) { return __int_as_float(__builtin_amdgcn_update_dpp(0, __float_as_int(v), 0xB1, 0xf, 0xf, true)); }
DI float red_sum_g(float v) {
    auto a = __builtin_amdgcn_permlane16_swap(__float_as_uint(v), __float_as_uint(v), false, false);
    v = __uint_as_float(a[0]) + __uint_as_float(a[1]);
    auto b = __builtin_amdgcn_permlane32_swap(__float_as_uint(v), __float_as_uint(v), false, false);
    return __uint_as_float(b[0]) + __uint_as_float(b[1]);
}
DI int wave_max32(int d) {
    d = max(d, __builtin_amdgcn_update_dpp(d, d, 0x121, 0xf, 0xf, false));
    d = max(d, __builtin_amdgcn_update_dpp(d, d, 0x122, 0xf, 0xf, false));
    d = max(d, __builtin_amdgcn_update_dpp(d, d, 0x124, 0xf, 0xf, false));
    d = max(d, __builtin_amdgcn_update_dpp(d, d, 0x128, 0xf, 0xf, false));
    return __builtin_amdgcn_readfirstlane(max(max(__builtin_amdgcn_readlane(d, 0), __builtin_amdgcn_readlane(d, 16)), max(__builtin_amdgcn_readlane(d, 32), __builtin_amdgcn_readlane(d, 48))));
}
DI u32x2 tr_read(LAS char* lds, int addr) { return __builtin_bit_cast(u32x2, __builtin_amdgcn_ds_read_tr16_b64_v4i16((LAS s16x4*)(lds + addr))); }
DI void wait_prog(rsrc_t rw, int off, bool act, int thr, int& budget) {
    for (;;) {
        const int v = (int)ld32(rw, off, 0);
        asm volatile("" ::: "memory");
        if (__all(!act || v >= thr)) break;
        if (--budget < 0) break;
        __builtin_amdgcn_s_sleep(1);
    }
}
DI u32x2 launch_tag(const Args& a) {
    unsigned long long z;
    asm volatile("s_load_dwordx2 %0, %1, 0x0\n\ts_waitcnt lgkmcnt(0)" : "=s"(z) : "s"(a.ws + F_EPOCH) : "memory");
    z += 0x9E3779B97F4A7C15ull;
    z = (z ^ (z >> 30)) * 0xBF58476D1CE4E5B9ull; z = (z ^ (z >> 27)) * 0x94D049BB133111EBull; z ^= z >> 31;
    return (u32x2){(unsigned)z, (unsigned)(z >> 32)};
}
DI int init_at(int more) { const int l = threadIdx.x & 63; return F_INIT + (l < 32 ? l : l < 56 || !more ? more : l + 105) * 128; }
DI bool init_ok(u32x2 v, u32x2 tag) { return __all(((v.x ^ tag.x) >> 3) == 0 && v.y == tag.y); }
DI int init_sel(u32x2 v) { const int m = __ballot(v.x & 1) >> 56; return (m & 1) + 2 * (m >> (4 + 2 * (m & 1)) & 3) + 4 * (m & 14); }
DI int wait_init(rsrc_t rw, u32x2 tag, int& budget, int more = 0) {
    u32x2 v;
    for (;;) {
        v = ld64(rw, init_at(more), 0);
        asm volatile("" ::: "memory");
        if (init_ok(v, tag) || --budget < 0) break;
        __builtin_amdgcn_s_sleep(2);
    }
    return init_sel(v);
}
DI unsigned die_side() { return (__builtin_amdgcn_s_getreg(4 | 14 << 6) ^ blockIdx.x >> 1) & 1; }
DI int side_at(int x) { const int b = blockIdx.x ^ x; return F_CL + ((b >> 3) * 4 + (b & 3)) * 128; }
DI void tell_side(rsrc_t rw, u32x2 tag) { __builtin_amdgcn_raw_buffer_store_b64((u32x2){(tag.x & ~1u) | die_side(), tag.y}, rw, threadIdx.x == 0 ? 0 : OOB, side_at(0), 16); }
DI int agree_side(rsrc_t rw, u32x2 tag, u32x2 v, int& budget) {
    while (!__all(((v.x ^ tag.x) >> 1) == 0 && v.y == tag.y) && --budget >= 0) { v = ld64(rw, 0, side_at(16)); asm volatile("" ::: "memory"); }
    const unsigned me = die_side();
    return (__builtin_amdgcn_readfirstlane(v.x) ^ me) & 1 ? me : blockIdx.x >> 4 & 1;
}
DI void glds16(const void* base, int voff, unsigned lds_dst) {
    unsigned keep;
    asm volatile("s_mov_b32 %0, m0\n\ts_mov_b32 m0, %3\n\ts_nop 0\n\tglobal_load_lds_dwordx4 %1, %2\n\ts_mov_b32 m0, %0" : "=&s"(keep) : "v"(voff), "s"(base), "s"(lds_dst) : "memory");
}
DI void bdma(rsrc_t rs, int voff, int soff, unsigned lds_dst) {
    unsigned keep;
    asm volatile("s_mov_b32 %0, m0\n\ts_mov_b32 m0, %4\n\ts_nop 0\n\tbuffer_load_dwordx4 %1, %2, %3 offen sc1 lds\n\ts_mov_b32 m0, %0" : "=&s"(keep) : "v"(voff), "s"(rs), "s"(soff), "s"(lds_dst) : "memory");
}
DI int swz(int r) { return ((r & 3) ^ (((r >> 2) & 3) << 1) ^ (((r >> 4) & 1) << 2)) & 7; }
template <class F> DI u32x4 ring_offs(int w, int lane, F at) {
    u32x4 o;
#pragma unroll
    for (int j = 0; j < 4; ++j) { const int r = 16 * w + 4 * j + (lane >> 4); o[j] = at(r, (lane & 15) ^ (2 * swz(r))) - 1024 * j; }
    return o;
}
DI u32x4 ring_offs(int w, int lane, int ld) { return ring_offs(w, lane, [&](int r, int c) { return r * ld * 2 + c * 16; }); }
DI void ring_issue(const u16* src, u32x4 o, unsigned slot) {
    unsigned keep;
    asm volatile("s_mov_b32 %0, m0\n\ts_mov_b32 m0, %6\n\ts_nop 0\n\tglobal_load_lds_dwordx4 %1, %5\n\tglobal_load_lds_dwordx4 %2, %5 offset:1024\n\t"
                 "global_load_lds_dwordx4 %3, %5 offset:2048\n\tglobal_load_lds_dwordx4 %4, %5 offset:3072\n\ts_mov_b32 m0, %0"
                 : "=&s"(keep) : "v"(o.x), "v"(o.y), "v"(o.z), "v"(o.w), "s"(src), "s"(slot) : "memory");
}
#define PF_FIRST 2
constexpr int L_TOTAL = 163840, L_SINK = L_TOTAL - 256;
DI void touch(const void* base, int voff, unsigned lds_base) {
    unsigned keep;
    asm volatile("s_mov_b32 %0, m0\n\ts_mov_b32 m0, %3\n\ts_nop 0\n\tglobal_load_lds_dword %1, %2\n\ts_mov_b32 m0, %0" : "=&s"(keep) : "v"(voff), "s"(base), "s"(lds_base + L_SINK) : "memory");
}
DI void prefetch(const Args& a, unsigned lds_base, int x, int id, int n) {
    const int lane = threadIdx.x & 63;
    const int h0 = x < 4 ? 8 * x : (0x160E0800 >> (8 * (x - 4))) & 255, lpr = x == 5 ? 12 : x == 7 ? 20 : 16, per = 9 * lpr;
    const int first = PF_FIRST * lpr, end = (x < 4 ? 4 : 2) * per;
    const float rl = __builtin_amdgcn_rcpf((float)lpr);
    if (x >= 4 && id < 18) touch(a.x, (id * 64 + lane) * 128, lds_base);
    for (int i = first + id; i < end; i += n) {
        int m = (i >= per) + (i >= 2 * per) + (i >= 3 * per), j = i - m * per;
        if (x < 4 && m < 3) { const int b = i >> 4; m = b < 20 ? b >> 3 : min(b - 20, 2); j = (b < 16 ? b & 7 : b < 20 ? b - 12 : b < 23 ? 8 : b - 23) * 16 + (i & 15); }
        const int mm = m + (x >= 4);
        const int L = j * 64 + lane, row = (int)((L + 0.5f) * rl);
        touch(mm == 0 ? a.Wq : mm == 1 ? a.Wk : mm == 2 ? a.Wv : a.Wo, m == 3 ? h0 * (DH * DM * 2) + L * 128 : row * (HD * 2) + (L - row * lpr) * 128 + h0 * 256, lds_base);
    }
}
template <int N> DI void ring_wait() { asm volatile("s_waitcnt vmcnt(%0) lgkmcnt(0)\n\ts_barrier" :: "i"(N) : "memory"); }
template <int I> struct ic_t { static constexpr int value = I; };
template <int N, int I = 0, class F> DI void static_for(F&& f) { if constexpr (I < N) { f(ic_t<I>{}); static_for<N, I + 1>(f); } }
#define MFMA32(a, b, c) __builtin_amdgcn_mfma_f32_32x32x16_bf16(__builtin_bit_cast(bf16x8, a), __builtin_bit_cast(bf16x8, b), c, 0, 0, 0)
#define MFMA16(a, b, c) __builtin_amdgcn_mfma_f32_16x16x32_bf16(__builtin_bit_cast(bf16x8, a), __builtin_bit_cast(bf16x8, b), c, 0, 0, 0)

constexpr int S_CH = 9;
constexpr int S_XRS = 4096 + 32;
constexpr int S_XR = 0;
constexpr int S_RC = S_CH * S_XRS;
constexpr int S_RCS = 4192;
constexpr int S_KD = S_RC + 4 * S_RCS;
constexpr int S_K = S_KD + 48 * 128;
constexpr int S_KROW = 96;
constexpr int S_TAB = S_K + 79 * S_KROW;
constexpr int S_TROW = 544;
constexpr int S_RED = S_TAB + 57 * S_TROW;
constexpr int S_TOTAL = S_RED + 4 * 64 * 144;
static_assert(S_RC % 16 == 0 && S_TAB % 16 == 0 && S_RED % 16 == 0, "");
DI f32x2 crot(f32x2 z) { return (f32x2){-z.y, z.x}; }
DI f32x2 cmul(f32x2 a, f32x2 z) { return __builtin_elementwise_fma(a.yy, crot(z), a.xx * z); }
DI f32x2 cexp_p(float ar, float ai, float p) {
    const float x = ai * p;
    const float hi = x * 0.15915494309189535f;
    const float lo = __builtin_fmaf(x, 0.15915494309189535f, -hi) + x * 6.420638316725915e-9f;
    const float rev = (hi - __builtin_rintf(hi)) + lo;
    const float m = __builtin_amdgcn_exp2f(ar * p * 1.4426950408889634f);
    return (f32x2){m * __builtin_amdgcn_cosf(rev), m * __builtin_amdgcn_sinf(rev)};
}
#define FENCE() __builtin_amdgcn_sched_barrier(0)
#define TT(c, i)
DI void role_s4d(const Args& a, LAS char* lds, unsigned lds_base, int task, rsrc_t rw, int& budget) {
    const int tid = threadIdx.x, lane = tid & 63, w = __builtin_amdgcn_readfirstlane(tid >> 6);
    const int d0 = task * S_CH;
    touch(w == 0 ? a.lAr : w == 1 ? a.Aim : w == 2 ? a.Cr : a.Ci, min(d0 * (NS * 2) + min(lane, 2 * S_CH + 1) * 128, DM * NS * 2 - 4), lds_base);
    touch(w & 1 ? a.ldt : a.Dsk, min(lane * 128, DM * 2 - 4), lds_base);
#define S4S(i)
    {
        const rsrc_t rx = __builtin_amdgcn_make_buffer_rsrc((void*)a.x, 0, SEQ * DM * 2, 0x00020000);
        const unsigned sh = (d0 & 1) * 16;
        u32x4 v[8]; unsigned e[8];
#pragma unroll
        for (int i = 0; i < 8; ++i) {
            const int o = (tid + 256 * i) * (DM * 2) + (d0 & ~1) * 2;
            v[i] = __builtin_amdgcn_raw_buffer_load_b128(rx, o, 0, 0); e[i] = __builtin_amdgcn_raw_buffer_load_b32(rx, o, 16, 0);
        }
#pragma unroll
        for (int p = 0; p < 2; ++p) {
            const int tok = 32 * task + 8 * w + 4 * p + (lane >> 4);
            float acc = 0.f;
#pragma unroll
            for (int i = 0; i < 5; ++i) {
                const int ch = (lane & 15) + 16 * i;
                const u32x4 gv = ch < 72 ? *(const u32x4*)(a.gw + ch * 8) : (u32x4){0u, 0u, 0u, 0u};
                const u32x4 xv = *(const u32x4*)(a.x + (size_t)tok * DM + min(ch, 71) * 8);
#pragma unroll
                for (int j = 0; j < 4; ++j) acc += bflo(xv[j]) * bflo(gv[j]) + bfhi(xv[j]) * bfhi(gv[j]);
            }
            acc += __shfl_xor(acc, 1); acc += __shfl_xor(acc, 2); acc += __shfl_xor(acc, 4); acc += __shfl_xor(acc, 8);
            st32(__float_as_uint(1.0f / (1.0f + __expf(-acc))), rw, (lane & 15) == 0 ? OFF_GT + tok * 4 : OOB, 0);
        }
#pragma unroll
        for (int i = 0; i < 8; ++i) {
            const unsigned x5[5] = {__builtin_amdgcn_alignbit(v[i].y, v[i].x, sh), __builtin_amdgcn_alignbit(v[i].z, v[i].y, sh), __builtin_amdgcn_alignbit(v[i].w, v[i].z, sh),
                                    __builtin_amdgcn_alignbit(e[i], v[i].w, sh), e[i] >> sh};
#pragma unroll
            for (int c = 0; c < S_CH; ++c) *(LAS u16*)(lds + S_XR + c * S_XRS + (2047 - tid - 256 * i) * 2) = (u16)(x5[c >> 1] >> (16 * (c & 1)));
        }
    }
    if (tid < 8 * S_CH) *(LAS unsigned*)(lds + S_XR + (tid >> 3) * S_XRS + 4096 + (tid & 7) * 4) = 0;
    if (tid < 64) *(LAS unsigned*)(lds + S_RC + (tid >> 4) * S_RCS + 4096 + (tid & 15) * 4) = 0;
    for (int i = tid; i < 15 * S_KROW / 4; i += 256) *(LAS unsigned*)(lds + S_K + i * 4) = 0;
    int kd[8];
#pragma unroll
    for (int j = 0; j < 8; ++j) {
        const int p = (int)bf2f(f2bf((float)(8 * tid + j))), k = p >> 7, r = p & 7;
        const int row = p < 256 ? 8 * k + r : (p < 512 ? 8 + 4 * k + (r >> 1) : (p < 1024 ? 16 + 2 * k + (r >> 2) : 24 + k));
        kd[j] = S_KD + (row * 16 + ((p >> 3) & 15)) * 8;
    }
    const int n = tid & 127, nn = lane & 15, g = lane >> 4;
    u16 pA = a.lAr[d0 * NS + n], pI = a.Aim[d0 * NS + n], pCr = a.Cr[d0 * NS + n], pCi = a.Ci[d0 * NS + n], pT = a.ldt[d0], pD = a.Dsk[d0];
    WG_BARRIER();
    for (int ch = 0; ch < S_CH; ++ch) {
        S4S(0)
        const float dsk = bf2f(pD);
        {
            const u32x4 v = *(LAS u32x4*)(lds + S_XR + ch * S_XRS + tid * 16);
            const u32x2 e = *(LAS u32x2*)(lds + S_XR + ch * S_XRS + tid * 16 + 16);
            const unsigned s01 = __builtin_amdgcn_alignbit(v.y, v.x, 16), s12 = __builtin_amdgcn_alignbit(v.z, v.y, 16), s23 = __builtin_amdgcn_alignbit(v.w, v.z, 16);
            const unsigned s34 = __builtin_amdgcn_alignbit(e.x, v.w, 16), s45 = __builtin_amdgcn_alignbit(e.y, e.x, 16);
            *(LAS u32x4*)(lds + S_RC + tid * 16) = v;
            *(LAS u32x4*)(lds + S_RC + S_RCS + tid * 16) = (u32x4){s01, s12, s23, s34};
            *(LAS u32x4*)(lds + S_RC + 2 * S_RCS + tid * 16) = (u32x4){v.y, v.z, v.w, e.x};
            *(LAS u32x4*)(lds + S_RC + 3 * S_RCS + tid * 16) = (u32x4){s12, s23, s34, s45};
        }
        S4S(1)
        const float dt = bf2f(f2bf(expf(bf2f(pT))));
        const int sc = 140 - (wave_max32(max(pCr & 0x7FFF, pCi & 0x7FFF)) >> 7);
        {
            const float ar = -bf2f(f2bf(expf(bf2f(pA)))) * dt, ai = bf2f(pI) * dt;
            const f32x2 C = {__builtin_amdgcn_ldexpf(bf2f(pCr), sc), __builtin_amdgcn_ldexpf(bf2f(pCi), sc)};
            const int dn = min(d0 + ch + 1, DM - 1);
            pA = a.lAr[dn * NS + n]; pI = a.Aim[dn * NS + n]; pCr = a.Cr[dn * NS + n]; pCi = a.Ci[dn * NS + n]; pT = a.ldt[dn]; pD = a.Dsk[dn];
            const int to = S_TAB + n * 4;
            auto put = [&](int row, f32x2 v) { *(LAS f16x2*)(lds + to + row * S_TROW) = __builtin_convertvector(v, f16x2); };
            const f32x2 z128 = cexp_p(ar, ai, 128.f);
            if (w < 2) {
                f32x2 E[8], G[4];
                E[1] = cexp_p(ar, ai, 1.f); G[0] = C;
#pragma unroll
                for (int r = 2; r < 8; ++r) E[r] = cmul(E[r >> 1], E[r - (r >> 1)]);
#pragma unroll
                for (int k = 1; k < 4; ++k) G[k] = cmul(G[k - 1], z128);
#pragma unroll
                for (int k = 0; k < 4; ++k)
#pragma unroll
                    for (int r = 0; r < 8; r += 1 + (k >> 1)) put(k < 2 ? 8 * k + r : 8 + 4 * k + (r >> 1), r ? cmul(E[r], G[k]) : G[k]);
            } else {
                const f32x2 z4 = cexp_p(ar, ai, 4.f), z8 = cexp_p(ar, -ai, 8.f);
                f32x2 G = cmul(C, cexp_p(ar, ai, 512.f)), B = z8;
                put(41, (f32x2){1.f, 0.f});
#pragma unroll
                for (int k = 1; k < 16; ++k) {
                    put(41 + k, B); B = cmul(B, z8);
                    if (k < 5) { put(22 + 2 * k, G); put(23 + 2 * k, cmul(G, z4)); }
                    else if (k < 14) put(27 + k, G);
                    G = cmul(G, z128);
                }
            }
        }
        WG_BARRIER();
        S4S(2)
        {
            const int ko = (w & 1) * 256 + g * 16, t0 = w & 2;
            u32x4 av[2][4], bv[4];
#pragma unroll
            for (int j = 0; j < 4; ++j) {
                bv[j] = *(LAS u32x4*)(lds + S_TAB + (41 + nn) * S_TROW + ko + j * 64);
                av[0][j] = *(LAS u32x4*)(lds + S_TAB + min(16 * t0 + nn, 40) * S_TROW + ko + j * 64);
                av[1][j] = *(LAS u32x4*)(lds + S_TAB + (16 + nn) * S_TROW + ko + j * 64);
            }
            f32x4 acc[2] = {};
#pragma unroll
            for (int j = 0; j < 8; ++j) acc[j & 1] = __builtin_amdgcn_mfma_f32_16x16x32_f16(__builtin_bit_cast(f16x8, av[j & 1][j >> 1]), __builtin_bit_cast(f16x8, bv[j >> 1]), acc[j & 1], 0, 0, 0);
#pragma unroll
            for (int r = 0; r < 8; ++r) *(LAS float*)(lds + S_KD + (((r < 4 ? 16 * t0 : 16) + 4 * g + (r & 3)) * 16 + nn) * 8 + (w & 1) * 4) = __builtin_amdgcn_ldexpf(acc[r >> 2][r & 3] * dt, 1 - sc);
        }
        WG_BARRIER();
        S4S(3)
        {
            float kv[8];
#pragma unroll
            for (int j = 0; j < 8; ++j) { const f32x2 t = *(LAS f32x2*)(lds + kd[j]); kv[j] = t.x + t.y; }
            *(LAS u32x4*)(lds + S_K + (15 + (tid >> 2)) * S_KROW + (tid & 3) * 16) = (u32x4){pack2(kv[0], kv[1]), pack2(kv[2], kv[3]), pack2(kv[4], kv[5]), pack2(kv[6], kv[7])};
        }
        WG_BARRIER();
        S4S(4)
        {
            const int st0 = 2047 - nn + 8 * g - 32 * w, cc = st0 & 3;
            const int ub = S_RC + cc * S_RCS + (st0 - cc) * 2 - 3872, kb = S_K + (3 + nn - w) * S_KROW + g * 16;
            f32x4 y[4][2] = {};
            u32x2 U[2][16]; u32x4 F[2][4];
            auto rd = [&](int i, int f) {
                if (f < 4) F[i & 1][f] = *(LAS u32x4*)(lds + kb + (12 - 4 * i + 16 * f) * S_KROW);
                else U[i & 1][f - 4] = *(LAS u32x2*)(lds + ub + 3872 - 256 * i - 1024 * ((f - 4) >> 2) - 32 * ((f >> 1) & 1) + 8 * (f & 1));
            };
#pragma unroll
            for (int f = 0; f < 20; ++f) { rd(0, f); FENCE(); }
#pragma unroll
            for (int i = 0; i < 4; ++i) {
                int c = 0;
#pragma unroll
                for (int q = 0; q < 4; ++q)
#pragma unroll
                    for (int T = q; T < 4; ++T)
#pragma unroll
                        for (int bt = 0; bt < 2; ++bt) {
                            y[T][bt] = MFMA16(__builtin_shufflevector(U[i & 1][4 * q + 2 * bt], U[i & 1][4 * q + 2 * bt + 1], 0, 1, 2, 3), F[i & 1][T - q], y[T][bt]);
                            if (i < 3) rd(i + 1, c);
                            ++c;
                            FENCE();
                        }
            }
#pragma unroll
            for (int T = 0; T < 4; ++T)
#pragma unroll
                for (int bt = 0; bt < 2; ++bt) *(LAS f32x4*)(lds + S_RED + (w * 64 + 16 * T + nn) * 144 + bt * 64 + g * 16) = y[T][bt];
        }
        WG_BARRIER();
        S4S(5)
        {
            f32x4 y0 = {0, 0, 0, 0}, y1 = y0;
#pragma unroll
            for (int ww = 0; ww < 4; ++ww) {
                const int o = S_RED + (ww * 64 + (tid >> 2)) * 144 + (tid & 3) * 32;
                y0 += *(LAS f32x4*)(lds + o); y1 += *(LAS f32x4*)(lds + o + 16);
            }
            const u32x4 u = *(LAS u32x4*)(lds + S_XR + ch * S_XRS + (255 - tid) * 16);
            st128((u32x4){pack2(y0[0] + bfhi(u.w) * dsk, y0[1] + bflo(u.w) * dsk), pack2(y0[2] + bfhi(u.z) * dsk, y0[3] + bflo(u.z) * dsk),
                          pack2(y1[0] + bfhi(u.y) * dsk, y1[1] + bflo(u.y) * dsk), pack2(y1[2] + bfhi(u.x) * dsk, y1[3] + bflo(u.x) * dsk)}, rw, tid * 16, OFF_BO + (d0 + ch) * BO_ROW);
        }
        S4S(6)
    }
    wait_init(rw, launch_tag(a), budget);
    VM_DRAIN();
    WG_BARRIER();
    if (tid == 0) st32(1u, rw, F_S4 + task * 4, 0);
}

constexpr int XT_BYTES = 32 * 1152;
constexpr int P_XT = 0;
constexpr int P_STG = 2 * XT_BYTES;
struct WSlice { u32x4 f[36]; };
constexpr int P_RING = 5;
template <class E, class F> DI void load_wslices(WSlice& W0, WSlice& W1, const u16* s0, const u16* s1, int w, int lane, LAS char* lds, unsigned lds_base, E also, F got) {
    constexpr int NB = 18;
    const int r0 = 8 * (lane >> 5) + ((lane & 15) >> 2);
    const int cb = (4 * w + 2 * ((lane >> 4) & 1) + ((lane & 3) >> 1)), lo8 = (lane & 1) * 8;
    auto src = [&](int i) { return (i < 9 ? s0 : s1) + (size_t)(64 * (i % 9)) * HD; };
    const u32x4 ro = ring_offs(w, lane, HD);
#pragma unroll
    for (int i = 0; i < P_RING - 1; ++i) { ring_issue(src(i), ro, lds_base + P_STG + i * 16384 + w * 4096); also(i); }
    static_for<NB>([&](auto ic) {
        constexpr int i = decltype(ic)::value, rem = NB - 1 - i < P_RING - 2 ? NB - 1 - i : P_RING - 2;
        ring_wait<4 * rem + (i < 2) * 9 + (i < 4) * 9 + (i > 0 && i < 5) * 4>();
        got(ic, ic_t<0>{});
        if constexpr (i + P_RING - 1 < NB) ring_issue(src(i + P_RING - 1), ro, lds_base + P_STG + ((i + P_RING - 1) % P_RING) * 16384 + w * 4096);
        got(ic, ic_t<1>{});
        const int sl = P_STG + (i % P_RING) * 16384;
#pragma unroll
        for (int kk = 0; kk < 4; ++kk) {
            const int ra = r0 + 16 * kk, rb = ra + 4;
            const u32x2 lo = tr_read(lds, sl + ra * 256 + ((cb ^ (2 * swz(ra))) * 16) + lo8), hi = tr_read(lds, sl + rb * 256 + ((cb ^ (2 * swz(rb))) * 16) + lo8);
            (i < 9 ? W0 : W1).f[(i % 9) * 4 + kk] = (u32x4){lo.x, lo.y, hi.x, hi.y};
        }
        got(ic, ic_t<2>{});
        u32x4* f = (i < 9 ? W0 : W1).f + (i % 9) * 4;
        if constexpr (i < 16) asm volatile("" : "+a"(f[0]), "+a"(f[1]), "+a"(f[2]), "+a"(f[3]));
    });
    static_for<3>([&](auto jc) { got(ic_t<NB>{}, jc); });
}
DI u32x4 piece_pi(const f32x16& a, int qq) {
    return (u32x4){pack2(a[4 * qq], a[4 * qq + 8]), pack2(a[4 * qq + 1], a[4 * qq + 9]), pack2(a[4 * qq + 2], a[4 * qq + 10]), pack2(a[4 * qq + 3], a[4 * qq + 11])};
}
DI u32x4 piece_seq(const f32x16& a, int s2) {
    return (u32x4){pack2(a[8 * s2], a[8 * s2 + 1]), pack2(a[8 * s2 + 2], a[8 * s2 + 3]), pack2(a[8 * s2 + 4], a[8 * s2 + 5]), pack2(a[8 * s2 + 6], a[8 * s2 + 7])};
}
struct XTile {
    int xs[9], xa[4];
    DI void init(int w, int lane) {
#pragma unroll
        for (int i = 0; i < 9; ++i) { const int L = (i * 4 + w) * 64 + lane, r = L / 72, cs = L - 72 * r; xs[i] = r * 1152 + (cs ^ ((r >> 1) & 7)) * 16; }
        const int c32 = lane & 31, hf = lane >> 5;
#pragma unroll
        for (int j = 0; j < 4; ++j) xa[j] = P_XT + c32 * 1152 + (((2 * j + hf) ^ ((c32 >> 1) & 7)) * 16);
    }
    DI void dma(const Args& a, unsigned lds_base, int w, int s, int buf, int i) const {
        glds16((const char*)a.x + (size_t)min(s, NSEG - 1) * XT_BYTES, xs[i], lds_base + P_XT + buf * XT_BYTES + (i * 4 + w) * 1024);
    }
    DI void first(const Args& a, unsigned lds_base, int w, int lane, int s) {
        init(w, lane);
#pragma unroll
        for (int i = 0; i < 9; ++i) dma(a, lds_base, w, s, 0, i);
    }
};
template <class F> DI void project(f32x16& acc0, f32x16& acc1, const WSlice& W0, const WSlice& W1, const XTile& X, const Args& a, LAS char* lds, unsigned lds_base, int w, int buf, int sn, F gap) {
    const int bo = buf * XT_BYTES;
    constexpr int PD = 5;
    u32x4 xf[PD];
#pragma unroll
    for (int i = 0; i < PD; ++i) xf[i] = *(LAS u32x4*)(lds + X.xa[i & 3] + bo + (i >> 2) * 128);
    static_for<36>([&](auto ic) {
        constexpr int ks = decltype(ic)::value;
        if (ks == 0) asm volatile("v_mfma_f32_32x32x16_bf16 %0, %1, %2, 0" : "=&v"(acc0) : "a"(W0.f[0]), "v"(xf[0]) : "memory");
        else asm volatile("v_mfma_f32_32x32x16_bf16 %0, %1, %2, %0" : "+v"(acc0) : "a"(W0.f[ks]), "v"(xf[ks % PD]) : "memory");
        if (ks < 9) X.dma(a, lds_base, w, sn, buf ^ 1, ks);
        gap(ic_t<2 * ks>{});
        if (ks == 0) asm volatile("v_mfma_f32_32x32x16_bf16 %0, %1, %2, 0" : "=&v"(acc1) : "a"(W1.f[0]), "v"(xf[0]) : "memory");
        else if (ks < 28) asm volatile("v_mfma_f32_32x32x16_bf16 %0, %1, %2, %0" : "+v"(acc1) : "a"(W1.f[ks]), "v"(xf[ks % PD]) : "memory");
        else asm volatile("v_mfma_f32_32x32x16_bf16 %0, %1, %2, %0" : "+v"(acc1) : "v"(W1.f[ks]), "v"(xf[ks % PD]) : "memory");
        if (ks + PD < 36) xf[ks % PD] = *(LAS u32x4*)(lds + X.xa[(ks + PD) & 3] + bo + ((ks + PD) >> 2) * 128);
        gap(ic_t<2 * ks + 1>{});
    });
    asm volatile("s_nop 7\n\ts_nop 3" : "+v"(acc0), "+v"(acc1) :: "memory");
}
#define KV_T 12
#define KV_F 22
DI void role_kv(const Args& a, LAS char* lds, unsigned lds_base, int h, rsrc_t rw, int& budget) {
    const int tid = threadIdx.x, lane = tid & 63, w = __builtin_amdgcn_readfirstlane(tid >> 6);
    WSlice W0, W1;
    XTile X;
    const int oo = h * 8192 + w * 2048 + lane * 16;
    const int fo = lane == 0 ? F_KV + h * 512 + w * 128 : OOB;
    u32x4 xf[6], P[4] = {};
    auto frag = [&](int buf, int ks) { xf[ks % 6] = *(LAS u32x4*)(lds + X.xa[ks & 3] + buf * XT_BYTES + (ks >> 2) * 128); };
    auto cvt = [&](const f32x16& k, const f32x16& v, int i) {
#pragma unroll
        for (int j = 0; j < 4; ++j) asm volatile("v_cvt_pk_bf16_f32 %0, %1, %2" : "=v"(P[i][j]) : "v"(i < 2 ? k[4 * i + j] : v[8 * i - 16 + 2 * j]), "v"(i < 2 ? k[4 * i + j + 8] : v[8 * i - 15 + 2 * j]));
    };
    auto put = [&](int o, int s, int i) { st128(P[i], rw, o + (i & 1) * 1024, (i < 2 ? OFF_KF : OFF_VF) + s * (NH * 8192)); };
    auto now_ = [&](f32x16& k, f32x16& v, int s) {
        asm volatile("s_nop 7\n\ts_nop 3" : "+v"(k), "+v"(v) :: "memory");
#pragma unroll
        for (int i = 0; i < 4; ++i) { cvt(k, v, i); put(oo, s, i); }
    };
    auto seg = [&](f32x16& k, f32x16& v, const f32x16& pk, const f32x16& pv, int s, int buf) {
        const int o = s > 2 ? oo : OOB;
#pragma unroll
        for (int ks = 0; ks < 36; ++ks) {
            if (ks == 0) asm volatile("v_mfma_f32_32x32x16_bf16 %0, %1, %2, 0" : "=&v"(k) : "a"(W0.f[0]), "v"(xf[0]) : "memory");
            else asm volatile("v_mfma_f32_32x32x16_bf16 %0, %1, %2, %0" : "+v"(k) : "a"(W0.f[ks]), "v"(xf[ks % 6]) : "memory");
            if (ks >= 1 && ks < 5) cvt(pk, pv, ks - 1);
            if (ks >= 5 && ks < 13 && (ks & 1)) put(o, s - 1, (ks - 5) >> 1);
            if (ks == KV_T && !buf && s == 2) { asm volatile("s_waitcnt vmcnt(8)" ::: "memory"); st32(1u, rw, fo, 0); }
            if (ks == 14 && (unsigned)(s - 4) < 8) {
                const int i = s - 4, t = opaque_lane() + 64 * w;
                st128((u32x4){SENT, SENT, SENT, SENT}, rw, i < 4 ? (t >> 7) * 65536 + (t & 127) * 16 : t * 16,
                      i < 4 ? (i & 2 ? OFF_VM : OFF_KM) + (FAST + 2 * (i & 1)) * 65536 + h * 2048 : OFF_OF + (LATE - 2 + (i >> 1)) * 262144 + h * 8192 + (i & 1) * 4096);
            }
            if (ks == KV_F) { VM_DRAIN(); st32((unsigned)(s - 1), rw, fo, 0); }
            if (ks == KV_F + 2 && (unsigned)(s - 52) < 10) {
                const int l = opaque_lane();
                if (s < 61) touch(a.Wq + (h & ~1) * DH, ((s - 52) * 64 + w * 16 + (l >> 2)) * (HD * 2) + (l & 3) * 128, lds_base);
                else bdma(rw, (l & 15) * 16, F_S4, lds_base + P_STG + w * 4096);
            }
            if (ks == 0) asm volatile("v_mfma_f32_32x32x16_bf16 %0, %2, %1, 0" : "=&v"(v) : "a"(W1.f[0]), "v"(xf[0]) : "memory");
            else if (ks < 28) asm volatile("v_mfma_f32_32x32x16_bf16 %0, %2, %1, %0" : "+v"(v) : "a"(W1.f[ks]), "v"(xf[ks % 6]) : "memory");
            else asm volatile("v_mfma_f32_32x32x16_bf16 %0, %2, %1, %0" : "+v"(v) : "v"(W1.f[ks]), "v"(xf[ks % 6]) : "memory");
            if (ks == 30) WG_BARRIER();
            frag(buf ^ (ks >= 30), (ks + 6) % 36);
            if (ks < 4) X.dma(a, lds_base, w, s + 1, buf ^ 1, ks + 5);
            if (ks > 30) X.dma(a, lds_base, w, s + 2, buf, ks - 31);
        }
    };
    f32x16 k0 = {}, v0 = {}, k1 = {}, v1 = {};
    u32x2 tag, in;
    u32x4 xa[2][4], xb[2][4];
    auto mm = [&](f32x16& k, f32x16& v, int t, int i, int j) {
        if (t) {}
        else if (i >= 16) asm volatile("s_nop 1\n\tv_mfma_f32_32x32x16_bf16 %0, %2, %1, %0" : "+v"(v) : "v"(W1.f[4 * i % 36 + j]), "v"(xa[t][j]) : "memory");
        else if (i >= 9) asm volatile("s_nop 1\n\tv_mfma_f32_32x32x16_bf16 %0, %2, %1, %0" : "+v"(v) : "a"(W1.f[4 * i % 36 + j]), "v"(xa[t][j]) : "memory");
        if (i > 1 + 2 * t && i < 11 + 2 * t) asm volatile("s_nop 1\n\tv_mfma_f32_32x32x16_bf16 %0, %1, %2, %0" : "+v"(k) : "a"(W0.f[4 * i % 36 + j]), "v"(xa[t][j]) : "memory");
    };
    load_wslices(W0, W1, a.Wk + h * DH, a.Wv + h * DH, w, lane, lds, lds_base, [&](int i) {
        if (i == 1) X.first(a, lds_base, w, lane, 0);
        if (i == 3)
#pragma unroll
            for (int j = 0; j < 9; ++j) X.dma(a, lds_base, w, 1, 1, j);
    }, [&](auto ic, auto jc) {
        constexpr int i = decltype(ic)::value - 1, part = decltype(jc)::value;
        mm(k0, v0, 0, i, part); mm(k1, v1, 1, i, part);
        if (part < 2) return;
        if (i < 0)
#pragma unroll
            for (int j = 0; j < 4; ++j) { P[j] = (u32x4){SENT, SENT, SENT, SENT}; put(oo, 0, j); }
#pragma unroll
        for (int j = 0; j < 8; ++j) if (i > (j >> 2) * 2 && i < 17 - 5 * (j >> 2)) xb[j >> 2][j & 3] = *(LAS u32x4*)(lds + X.xa[j & 3] + (j >> 2) * XT_BYTES + ((i + 1) % 9) * 128);
        mm(k0, v0, 0, i, 3); mm(k1, v1, 1, i, 3);
#pragma unroll
        for (int j = 0; j < 8; ++j) if (i > (j >> 2) * 2) xa[j >> 2][j & 3] = xb[j >> 2][j & 3];
        if (i == 4) { tag = launch_tag(a); __builtin_amdgcn_raw_buffer_store_b64(tag, rw, lane == 0 ? F_INIT + (33 + 4 * h + w) * 128 : OOB, 0, 16); }
        if (i == 10) asm volatile("buffer_load_dwordx2 %0, %1, %2, 0 offen sc1" : "=v"(in) : "v"(init_at(0)), "s"(rw) : "memory");
        if (i == 11) { cvt(k0, v0, 0); put(oo, 0, 0); cvt(k0, v0, 1); put(oo, 0, 1); }
    });
#pragma unroll
    for (int i = 0; i < 6; ++i) frag(1, i);
    asm volatile("s_nop 7\n\ts_nop 3" : "+v"(v0) :: "memory");
    cvt(k0, v0, 2); put(oo, 0, 2); cvt(k0, v0, 3); put(oo, 0, 3);
    WG_BARRIER();
#pragma unroll
    for (int ks = 0; ks < 36; ++ks) {
        if (ks == 0) asm volatile("v_mfma_f32_32x32x16_bf16 %0, %2, %1, 0" : "=&v"(v1) : "a"(W1.f[0]), "v"(xf[0]) : "memory");
        else if (ks < 28) asm volatile("v_mfma_f32_32x32x16_bf16 %0, %2, %1, %0" : "+v"(v1) : "a"(W1.f[ks]), "v"(xf[ks % 6]) : "memory");
        else asm volatile("v_mfma_f32_32x32x16_bf16 %0, %2, %1, %0" : "+v"(v1) : "v"(W1.f[ks]), "v"(xf[ks % 6]) : "memory");
        if (ks < 18 && !(ks & 1)) X.dma(a, lds_base, w, 2, 0, ks >> 1);
        if (ks == 19 || ks == 22) { cvt(k1, v1, ks == 22); put(oo, 1, ks == 22); }
        if (ks == 30) ring_wait<2>();
        frag(ks < 30, (ks + 6) % 36);
        if (ks > 30) X.dma(a, lds_base, w, 3, 1, ks - 31);
    }
    asm volatile("s_nop 7\n\ts_nop 3" : "+v"(v1), "+v"(in) :: "memory");
    cvt(k1, v1, 2); put(oo, 1, 2); cvt(k1, v1, 3); put(oo, 1, 3);
    if (!init_ok(in, tag)) wait_init(rw, tag, budget);
    for (int s = 2; s < NSEG; s += 2) { seg(k0, v0, k1, v1, s, 0); seg(k1, v1, k0, v0, s + 1, 1); }
    now_(k1, v1, NSEG - 1);
    if (!__all(*(volatile LAS int*)(lds + P_STG + w * 4096 + lane * 4) >= 1)) wait_prog(rw, F_S4 + lane * 4, true, 1, budget);
}

DI int prog_min(rsrc_t rw, int off) {
    const int v = (int)ld32(rw, off, 0);
    asm volatile("" ::: "memory");
    return -wave_max32(-v);
}
DI float red_max_g(float v) {
    auto a = __builtin_amdgcn_permlane16_swap(__float_as_uint(v), __float_as_uint(v), false, false);
    v = fmaxf(__uint_as_float(a[0]), __uint_as_float(a[1]));
    auto b = __builtin_amdgcn_permlane32_swap(__float_as_uint(v), __float_as_uint(v), false, false);
    return fmaxf(__uint_as_float(b[0]), __uint_as_float(b[1]));
}
#define M16 "v_mfma_f32_16x16x32_bf16 "
DI void role_qa(const Args& a, LAS char* lds, unsigned lds_base, int h0, int s0, int s1, rsrc_t rw, int& budget) {
    const int tid = threadIdx.x, lane = tid & 63, w = __builtin_amdgcn_readfirstlane(tid >> 6);
    const int n = lane & 15, g = lane >> 4, h = h0 + (w >> 1), tt = w & 1;
    WSlice W0, W1;
    XTile X;
    const int oo = OFF_OF + (2 * tt + (n >> 3)) * 65536 + h * 2048 + g * 128 + (n & 7) * 16;
    const int fk = F_KV + h * 512 + (lane & 3) * 128, fm = F_KVM + h * 256 + (lane & 1) * 128;
    int pm_, m_rdy = -1, kv_rdy = -1;
    auto look = [&]() { pm_ = (int)ld32(rw, lane & 4 ? fk : fm, 0); };
    auto seen = [&]() { auto rl = [&](int i) { return __builtin_amdgcn_readlane(pm_, i); }; m_rdy = min(rl(0), rl(1)); kv_rdy = min(min(rl(4), rl(5)), min(rl(6), rl(7))); };
    f32x16 q0 = {}, q1 = {};
    u32x4 xa[4], xb[4];
    auto mm = [&](int i, int j) {
        if (i >= 16) asm volatile("s_nop 1\n\tv_mfma_f32_32x32x16_bf16 %0, %1, %2, %0" : "+v"(q1) : "v"(W1.f[4 * i % 36 + j]), "v"(xa[j]) : "memory");
        else if (i >= 9) asm volatile("s_nop 1\n\tv_mfma_f32_32x32x16_bf16 %0, %1, %2, %0" : "+v"(q1) : "a"(W1.f[4 * i % 36 + j]), "v"(xa[j]) : "memory");
        if (i > 1 && i < 11) asm volatile("s_nop 1\n\tv_mfma_f32_32x32x16_bf16 %0, %1, %2, %0" : "+v"(q0) : "a"(W0.f[4 * i % 36 + j]), "v"(xa[j]) : "memory");
    };
    load_wslices(W0, W1, a.Wq + h0 * DH, a.Wq + (h0 + 1) * DH, w, lane, lds, lds_base, [&](int i) {
        if (i == 1) X.first(a, lds_base, w, lane, s0);
        if (i == 3)
#pragma unroll
            for (int j = 0; j < 9; ++j) X.dma(a, lds_base, w, s0 + 2, 1, j);
    }, [&](auto ic, auto jc) {
        constexpr int i = decltype(ic)::value - 1, part = decltype(jc)::value;
        mm(i, part);
        if (i < 0 && part == 0) st32(NSEG, rw, lane == 0 && s0 > FAST ? F_KV + (h0 + (s0 & 1)) * 512 + w * 128 : OOB, 0);
        if (part < 2) return;
#pragma unroll
        for (int j = 0; j < 4; ++j) {
            if (i < 0) st128((u32x4){SENT, SENT, SENT, SENT}, rw, oo + j * 512, s1 * 262144);
            if (i > 0 && i < 17) xb[j] = *(LAS u32x4*)(lds + X.xa[j] + ((i + 1) % 9) * 128);
        }
        mm(i, 3);
        if (i == 13) look();
#pragma unroll
        for (int j = 0; j < 4; ++j) if (i > 0) xa[j] = xb[j];
    });
    asm volatile("s_nop 7\n\ts_nop 3\n\ts_barrier" : "+v"(q0), "+v"(q1) :: "memory");
    constexpr int Q_Q = P_STG, Q_KV = Q_Q + 16384, Q_M = Q_KV + 32768;
    static_assert(Q_M + 16384 <= P_STG + P_RING * 16384, "");
    auto wait_m = [&](int thr, unsigned& t) {
        for (;;) {
            const u32x2 v = ld64(rw, fm, 0);
            asm volatile("" ::: "memory");
            t = now();
            const int p0 = __builtin_amdgcn_readlane(v.x, 0), p1 = __builtin_amdgcn_readlane(v.x, 1);
            const int d0 = (int)(__builtin_amdgcn_readlane(v.y, 0) - 8 * t) >> 3, d1 = (int)(__builtin_amdgcn_readlane(v.y, 1) - 8 * t) >> 3;
            if (min(p0, p1) >= thr || --budget < 0) return max(p0 == thr ? d0 : -1000, p1 == thr ? d1 : -1000);
            (void)wait_until(t + min(max(min(d0, d1) + (int)(a.xa >> 16 & 255), 10), 200));
        }
    };
    auto keep_q = [&](const f32x16& acc0, const f32x16& acc1) {
        const int o = Q_Q + w * 2048 + lane * 16;
        *(LAS u32x4*)(lds + o) = piece_pi(acc0, 0); *(LAS u32x4*)(lds + o + 1024) = piece_pi(acc0, 1);
        *(LAS u32x4*)(lds + o + 8192) = piece_pi(acc1, 0); *(LAS u32x4*)(lds + o + 8192 + 1024) = piece_pi(acc1, 1);
    };
    auto proj = [&](int s, auto gap) {
        f32x16 acc0, acc1;
        project(acc0, acc1, W0, W1, X, a, lds, lds_base, w, ((s - s0) >> 1) & 1, s + 2, [&](auto ic) { gap(ic); if (decltype(ic)::value == 55) look(); });
        if (s >= LATE) {
            const int o = OFF_OF + h0 * 8192 + w * 2048 + lane * 16;
            acc0 *= __uint_as_float(0x3e0293eeu); acc1 *= __uint_as_float(0x3e0293eeu);
            st128(piece_pi(acc0, 0), rw, o, s * 262144); st128(piece_pi(acc0, 1), rw, o + 1024, s * 262144);
            st128(piece_pi(acc1, 0), rw, o + 8192, s * 262144); st128(piece_pi(acc1, 1), rw, o + 8192 + 1024, s * 262144);
            return;
        }
        keep_q(acc0, acc1);
    };
    const int fs = (tt ? OFF_VF : OFF_KF) + (g >> 1) * 1024 + ((g & 1) * 32 + 16 * (n >> 3) + (n & 7)) * 16;
    auto fetch = [&](int s) {
        const int so = (min(s, NSEG - 1) * NH + h) * 8192;
#pragma unroll
        for (int f = 0; f < 8; ++f) bdma(rw, fs, so + (f >> 1) * 2048 + (f & 1) * 128, lds_base + Q_KV + w * 8192 + f * 1024);
    };
    const int ms = OFF_KM + (lane & 7) * 256 + (lane >> 3) * 16;
    auto fetch_m = [&](int s) {
        const int so = (s * NH + h) * 2048;
        const unsigned d = lds_base + Q_M + w * 4096;
        bdma(rw, ms, so, d); bdma(rw, ms, so + 128, d + 1024); bdma(rw, lane * 16, OFF_VM + so, d + 2048); bdma(rw, lane * 16, OFF_VM + so + 1024, d + 3072);
    };
    const float c = 0.08838834764831845f * 1.4426950408889634f;
    u32x4 qf[4];
    f32x4 o[8]; float mx, l;
    auto xpart = [&]() {
        const int kb = Q_KV + (w >> 1) * 16384 + lane * 16;
        u32x4 kn[8], vn[8];
#pragma unroll
        for (int ks = 0; ks < 4; ++ks) qf[ks] = *(LAS u32x4*)(lds + Q_Q + (w >> 1) * 8192 + (2 * ks + (g >> 1)) * 1024 + ((g & 1) * 32 + 16 * tt + n) * 16);
#pragma unroll
        for (int f = 0; f < 8; ++f) kn[f] = *(LAS u32x4*)(lds + kb + f * 1024);
        FENCE();
#pragma unroll
        for (int f = 0; f < 8; ++f) vn[f] = *(LAS u32x4*)(lds + kb + 8192 + f * 1024);
        f32x4 s0, s1;
        asm volatile(M16 "%0, %2, %10, 0\n\t" M16 "%1, %3, %10, 0\n\t" M16 "%0, %4, %11, %0\n\t" M16 "%1, %5, %11, %1\n\t"
                     M16 "%0, %6, %12, %0\n\t" M16 "%1, %7, %12, %1\n\t" M16 "%0, %8, %13, %0\n\t" M16 "%1, %9, %13, %1\n\ts_nop 7"
                     : "=&v"(s0), "=&v"(s1) : "v"(kn[0]), "v"(kn[1]), "v"(kn[2]), "v"(kn[3]), "v"(kn[4]), "v"(kn[5]), "v"(kn[6]), "v"(kn[7]), "v"(qf[0]), "v"(qf[1]), "v"(qf[2]), "v"(qf[3]));
        float p[8];
        mx = -3.0e38f; l = 0.f;
#pragma unroll
        for (int r = 0; r < 8; ++r) { const int key = 16 * (g >> 1) + 4 * (g & 1) + (r & 3) + 8 * (r >> 2); p[r] = key <= 16 * tt + n ? (r < 4 ? s0[r & 3] : s1[r & 3]) : -3.0e38f; mx = fmaxf(mx, p[r]); }
        mx = red_max_g(mx);
#pragma unroll
        for (int r = 0; r < 8; ++r) { p[r] = __builtin_amdgcn_exp2f((p[r] - mx) * c); l += p[r]; }
        const u32x4 pf = {pack2(p[0], p[1]), pack2(p[2], p[3]), pack2(p[4], p[5]), pack2(p[6], p[7])};
        asm volatile("s_nop 1\n\t" M16 "%0, %8, %16, 0\n\t" M16 "%1, %9, %16, 0\n\t" M16 "%2, %10, %16, 0\n\t" M16 "%3, %11, %16, 0\n\t"
                     M16 "%4, %12, %16, 0\n\t" M16 "%5, %13, %16, 0\n\t" M16 "%6, %14, %16, 0\n\t" M16 "%7, %15, %16, 0\n\ts_nop 7"
                     : "=&v"(o[0]), "=&v"(o[1]), "=&v"(o[2]), "=&v"(o[3]), "=&v"(o[4]), "=&v"(o[5]), "=&v"(o[6]), "=&v"(o[7])
                     : "v"(vn[0]), "v"(vn[1]), "v"(vn[2]), "v"(vn[3]), "v"(vn[4]), "v"(vn[5]), "v"(vn[6]), "v"(vn[7]), "v"(pf));
    };
    const int fo = lane == 0 ? F_O + h * 128 + (2 * s0 + tt) * 4 : OOB;
    bool pre = false;
    auto both = [&](int s) {
        const int kb = Q_KV + (w >> 1) * 16384 + lane * 16;
        u32x4 kn[8], vn[8], pf;
        f32x4 sc[2];
        float p[8];
        int v0, v1, v2;
        asm volatile("buffer_load_dword %0, %1, %2, 0 offen sc1" : "=v"(pm_) : "v"(lane & 4 ? fk : fm), "s"(rw) : "memory");
#define PIN(...) asm volatile("" : __VA_ARGS__)
        proj(s + 4, [&](auto ic) {
            constexpr int i = decltype(ic)::value;
            if constexpr (i < 8) {
                if (!(i & 1)) qf[i >> 1] = *(LAS u32x4*)(lds + Q_Q + (w >> 1) * 8192 + (i + (g >> 1)) * 1024 + ((g & 1) * 32 + 16 * tt + n) * 16);
                kn[i] = *(LAS u32x4*)(lds + kb + i * 1024);
            }
            if constexpr (i >= 16 && i < 32 && !(i & 1)) vn[(i - 16) >> 1] = *(LAS u32x4*)(lds + kb + 8192 + (i - 16) * 512);
            if constexpr (i == 5 || i == 6) asm volatile(M16 "%0, %1, %2, 0" : "=&v"(sc[i - 5]) : "v"(kn[i - 5]), "v"(qf[0]));
            if constexpr (i > 6 && i < 13) asm volatile(M16 "%0, %1, %2, %0" : "+v"(sc[(i - 5) & 1]) : "v"(kn[i - 5]), "v"(qf[(i - 5) >> 1]));
            if constexpr (i == 15 || i == 16) {
                constexpr int r = 4 * (i - 15);
                PIN("+v"(sc[i - 15]));
#pragma unroll
                for (int j = 0; j < 4; ++j) p[r + j] = 16 * (g >> 1) + 4 * (g & 1) + j + 2 * r <= 16 * tt + n ? sc[i - 15][j] : -3.0e38f;
                PIN("+v"(p[r]), "+v"(p[r + 1]), "+v"(p[r + 2]), "+v"(p[r + 3]));
            }
            if constexpr (i == 17) { mx = fmaxf(fmaxf(fmaxf(fmaxf(p[0], p[1]), p[2]), fmaxf(fmaxf(p[3], p[4]), p[5])), fmaxf(p[6], p[7])); PIN("+v"(mx)); }
            if constexpr (i == 18) { auto x = __builtin_amdgcn_permlane16_swap(__float_as_uint(mx), __float_as_uint(mx), false, false); mx = fmaxf(__uint_as_float(x[0]), __uint_as_float(x[1])); PIN("+v"(mx)); }
            if constexpr (i == 19) { auto x = __builtin_amdgcn_permlane32_swap(__float_as_uint(mx), __float_as_uint(mx), false, false); mx = fmaxf(__uint_as_float(x[0]), __uint_as_float(x[1])); PIN("+v"(mx)); }
            if constexpr (i >= 20 && i < 24) {
                constexpr int r = 2 * (i - 20);
                PIN("+v"(p[r]), "+v"(p[r + 1]));
                p[r] = __builtin_amdgcn_exp2f((p[r] - mx) * c); p[r + 1] = __builtin_amdgcn_exp2f((p[r + 1] - mx) * c);
                PIN("+v"(p[r]), "+v"(p[r + 1]));
            }
            if constexpr (i == 24) { pf = (u32x4){pack2(p[0], p[1]), pack2(p[2], p[3]), pack2(p[4], p[5]), pack2(p[6], p[7])}; PIN("+v"(pf)); }
            if constexpr (i == 25) { l = ((p[0] + p[1]) + p[2]) + p[3]; PIN("+v"(l)); }
            if constexpr (i == 26) { l = (((l + p[4]) + p[5]) + p[6]) + p[7]; PIN("+v"(l)); }
            if constexpr (i == 27) { asm volatile("s_waitcnt vmcnt(9)" : "+v"(pm_) :: "memory"); st32((unsigned)s, rw, fo, 0); }
            if constexpr (i == 29) { seen(); pre = m_rdy >= s + 2 && kv_rdy >= s + 4; }
            if constexpr (i == 31) { v0 = pre ? ms : OOB; v1 = pre ? lane * 16 : OOB; v2 = pre ? fs : OOB; PIN("+v"(v0), "+v"(v1), "+v"(v2)); }
            if constexpr (i >= 28 && i < 36) asm volatile(M16 "%0, %1, %2, 0" : "=&v"(o[i - 28]) : "v"(vn[i - 28]), "v"(pf));
            if constexpr (i == 36) asm volatile("s_barrier" ::: "memory");
            if constexpr (i >= 38 && i < 42) bdma(rw, i < 40 ? v0 : v1, ((s + 2) * NH + h) * 2048 + (i < 40 ? (i & 1) * 128 : OFF_VM + (i & 1) * 1024), lds_base + Q_M + w * 4096 + (i - 38) * 1024);
            if constexpr (i >= 42 && i < 50) bdma(rw, v2, ((s + 4) * NH + h) * 8192 + ((i - 42) >> 1) * 2048 + (i & 1) * 128, lds_base + Q_KV + w * 8192 + (i - 42) * 1024);
        });
    };
    auto none = [](auto) {};
    keep_q(q0, q1);
    if (s0 < FAST) wait_init(rw, launch_tag(a), budget); else seen();
    if (kv_rdy < s0) wait_prog(rw, fk, true, s0, budget);
    fetch(s0);
#pragma unroll 1
    for (int p = 0; p < 2; ++p) {
        const bool x = p == (s0 > FAST);
        if (x) VM_DRAIN();
        WG_BARRIER();
        if (x) xpart(); else proj(s0 + 2, none);
    }
    for (int s = s0; s <= s1; s += 2) {
        S4S(0)
        const int mb = Q_M + w * 4096;
        u32x4 kf[4]; u32x2 vm[4];
        auto frags = [&]() {
#pragma unroll
            for (int ks = 0; ks < 4; ++ks) kf[ks] = *(LAS u32x4*)(lds + mb + (4 * ks + g) * 128 + (2 * (n >> 2) + (n & 1)) * 16);
#pragma unroll
            for (int t = 0; t < 4; ++t) { const int vo = mb + 2048 + t * 512 + (n >> 3) * 256 + g * 64 + (n & 7) * 4; vm[t] = (u32x2){*(LAS unsigned*)(lds + vo), *(LAS unsigned*)(lds + vo + 32)}; }
        };
        if (s == s1) {
            TT(true, 3)
            unsigned t;
            const int d = min(wait_m(s - 1, t) + (int)(a.xa & 255), 500);
            st32((t + max(d, 0) + (a.xb >> 8 * ((s - FAST) >> 1) & 255)) & 0x7FFFFFFFu, rw, lane == 0 ? F_H + ((2 * (s - FAST) + tt) * NH + h) * 128 : OOB, 0);
            (void)wait_until(t + d);
            TT(true, 0)
            for (;;) {
                fetch_m(s);
                VM_DRAIN();
                frags();
                unsigned c = 0;
#pragma unroll
                for (int i = 0; i < 4; ++i) c = max(max(mx4(c, kf[i]), vm[i].x), vm[i].y);
                if (!__any(c == SENT) || --budget < 0) break;
            }
        } else {
            if (!pre) {
                seen();
                unsigned t;
                if (m_rdy < s) { const int d = wait_m(s, t); (void)wait_until(t + min(d + (int)(a.xa >> 24), 300)); }
                fetch_m(s);
                if (kv_rdy < s + 2) wait_prog(rw, fk, true, s + 2, budget);
                fetch(s + 2);
            }
            S4S(1)
            asm volatile("s_waitcnt vmcnt(8)" ::: "memory");
            frags();
        }
        TT(s == s1, 1)
        S4S(2)
        f32x4 sm;
        asm volatile(M16 "%0, %1, %5, 0\n\t" M16 "%0, %2, %6, %0\n\t" M16 "%0, %3, %7, %0\n\t" M16 "%0, %4, %8, %0\n\ts_nop 7"
                     : "=&v"(sm) : "v"(kf[0]), "v"(kf[1]), "v"(kf[2]), "v"(kf[3]), "v"(qf[0]), "v"(qf[1]), "v"(qf[2]), "v"(qf[3]));
        const float m2 = red_max_g(fmaxf(mx, fmaxf(sm[0], sm[1])));
        const float al = __builtin_amdgcn_exp2f((mx - m2) * c), p0 = __builtin_amdgcn_exp2f((sm[0] - m2) * c), p1 = __builtin_amdgcn_exp2f((sm[1] - m2) * c);
        const float inv = __builtin_amdgcn_rcpf(red_sum_g(l * al + p0 + p1)), ai = al * inv;
#pragma unroll
        for (int t = 0; t < 8; ++t) o[t] *= ai;
        const unsigned pm = pack2(p0 * inv, p1 * inv);
#define M1K "v_mfma_f32_16x16x16_bf16 "
        asm volatile("s_nop 1\n\t" M1K "%0, %8, %12, %0\n\t" M1K "%1, %8, %13, %1\n\t" M1K "%2, %9, %12, %2\n\t" M1K "%3, %9, %13, %3\n\t"
                     M1K "%4, %10, %12, %4\n\t" M1K "%5, %10, %13, %5\n\t" M1K "%6, %11, %12, %6\n\t" M1K "%7, %11, %13, %7\n\ts_nop 7"
                     : "+v"(o[0]), "+v"(o[1]), "+v"(o[2]), "+v"(o[3]), "+v"(o[4]), "+v"(o[5]), "+v"(o[6]), "+v"(o[7])
                     : "v"(vm[0]), "v"(vm[1]), "v"(vm[2]), "v"(vm[3]), "v"((u32x2){pm, 0u}), "v"((u32x2){0u, pm}));
#pragma unroll
        for (int b = 0; b < 2; ++b) {
            u32x4 x, y;
#pragma unroll
            for (int j = 0; j < 4; ++j) { x[j] = pack2(o[4 * b + (j >> 1)][2 * (j & 1)], o[4 * b + (j >> 1)][2 * (j & 1) + 1]); y[j] = pack2(o[4 * b + 2 + (j >> 1)][2 * (j & 1)], o[4 * b + 2 + (j >> 1)][2 * (j & 1) + 1]); }
            if (s == LATE - 1) { const u32x4 z = {x.x, x.z, y.x, y.z}; y = (u32x4){x.y, x.w, y.y, y.w}; x = z; }
            st128(x, rw, oo + b * 1024, s * 262144); st128(y, rw, oo + b * 1024 + 512, s * 262144);
        }
        TT(s == s1, 2)
        S4S(3)
        if (s == s1) break;
        asm volatile("s_waitcnt vmcnt(4)" ::: "memory");
        WG_BARRIER();
        S4S(4)
        if (s + 4 <= s1) { both(s); S4S(5) continue; }
        xpart();
        FENCE();
        VM_DRAIN();
        st32((unsigned)s, rw, fo, 0);
        TT(true, 4)
    }
}

#define STEP_ASM \
    "v_readfirstlane_b32 s36, v0\n" \
    "v_readfirstlane_b32 s37, v1\n" \
    "v_readfirstlane_b32 s38, v2\n" \
    "v_readfirstlane_b32 s39, v3\n" \
    "v_readfirstlane_b32 s40, v4\n" \
    "v_readfirstlane_b32 s41, v5\n" \
    "v_readfirstlane_b32 s42, v6\n" \
    "v_readfirstlane_b32 s43, v7\n" \
    "v_readfirstlane_b32 s28, v8\n" \
    "v_readfirstlane_b32 s29, v9\n" \
    "v_readfirstlane_b32 s30, v10\n" \
    "v_readfirstlane_b32 s31, v11\n" \
    "v_readfirstlane_b32 s32, v12\n" \
    "v_readfirstlane_b32 s33, v13\n" \
    "v_readfirstlane_b32 s34, v14\n" \
    "v_readfirstlane_b32 s35, v15\n" \
    "v_readfirstlane_b32 s24, v16\n" \
    "v_readfirstlane_b32 s25, v17\n" \
    "v_readfirstlane_b32 s26, v18\n" \
    "v_readfirstlane_b32 s27, v19\n" \
    "v_readfirstlane_b32 s60, v24\n" \
    "v_readfirstlane_b32 s61, v25\n" \
    "v_readfirstlane_b32 s62, v26\n" \
    "v_readfirstlane_b32 s63, v27\n" \
    "v_readfirstlane_b32 s64, v28\n" \
    "v_readfirstlane_b32 s65, v29\n" \
    "v_readfirstlane_b32 s66, v30\n" \
    "v_readfirstlane_b32 s67, v31\n" \
    "s_mov_b32 s68, m0\n" \
    "s_getpc_b64 s[96:97]\n" \
    "14:\n" \
    "s_add_u32 s96, s96, 2f-14b\n" \
    "s_addc_u32 s97, s97, 0\n" \
    "s_lshl_b32 s74, s60, 10\n" \
    "s_lshl_b32 s95, s74, 1\n" \
    "s_add_i32 s95, s95, 16384\n" \
    "s_mov_b32 s81, 0x3e0293ee\n" \
    "s_mov_b32 s82, 0x53800000\n" \
    "s_lshl_b32 s83, s60, 6\n" \
    "s_and_b32 s83, s83, 128\n" \
    "v_add_u32 v109, s83, v131\n" \
    "v_sub_u32 v111, v131, s83\n" \
    "v_xor_b32 v112, 0x2000, v133\n" \
    "v_and_b32 v113, 0x3c0, v127\n" \
    "v_mad_u32_u24 v113, v113, 7, v127\n" \
    "s_bfe_u32 s83, s61, 0x2000b\n" \
    "s_and_b32 s83, s83, 3\n" \
    "v_lshl_add_u32 v113, s83, 7, v113\n" \
    "v_lshlrev_b32 v114, 1, v127\n" \
    "v_add_u32 v106, s63, v133\n" \
    "s_bfe_u32 s83, s61, 0x40009\n" \
    "s_bfe_u32 s80, s61, 0x20004\n" \
    "s_lshl2_add_u32 s83, s83, s80\n" \
    "s_mov_b64 s[72:73], 0\n" \
    "s_cmp_eq_u32 s43, 4\n" \
    "s_cbranch_scc1 20f\n" \
    "0:\n" \
    "s_and_b32 s75, s36, 3\n" \
    "s_lshl_b32 s75, s75, 16\n" \
    "s_add_i32 s69, s75, s26\n" \
    "s_xor_b32 s70, s69, 2048\n" \
    "s_andn2_b32 s71, s69, 0x1fff\n" \
    "s_add_i32 s71, s71, 32768\n" \
    "ds_read_b128 v[72:75], v133 offset:0\n" \
    "ds_read_b128 v[76:79], v133 offset:1024\n" \
    "ds_read_b128 v[80:83], v133 offset:2048\n" \
    "ds_read_b128 v[84:87], v133 offset:3072\n" \
    "ds_read_b128 v[88:91], v133 offset:16384\n" \
    "ds_read_b128 v[92:95], v133 offset:17408\n" \
    "v_mov_b64 v[40:41], 0\n" \
    "v_mov_b64 v[42:43], 0\n" \
    "v_mov_b64 v[44:45], 0\n" \
    "v_mov_b64 v[46:47], 0\n" \
    "v_mov_b64 v[48:49], 0\n" \
    "v_mov_b64 v[50:51], 0\n" \
    "v_mov_b64 v[52:53], 0\n" \
    "v_mov_b64 v[54:55], 0\n" \
    "v_mov_b64 v[56:57], 0\n" \
    "v_mov_b64 v[58:59], 0\n" \
    "v_mov_b64 v[60:61], 0\n" \
    "v_mov_b64 v[62:63], 0\n" \
    "v_mov_b64 v[64:65], 0\n" \
    "v_mov_b64 v[66:67], 0\n" \
    "v_mov_b64 v[68:69], 0\n" \
    "v_mov_b64 v[70:71], 0\n" \
    "v_add_u32 v108, s74, v127\n" \
    "v_xor_b32 v24, 0x400, v108\n" \
    "v_xor_b32 v28, 0x800, v108\n" \
    "v_xor_b32 v32, 0xc00, v108\n" \
    "v_and_b32 v22, 0x200, v127\n" \
    "s_and_b32 s76, s26, 0x800\n" \
    "v_mad_u32_u24 v22, v22, 3, v127\n" \
    "v_xor_b32 v22, s76, v22\n" \
    "s_movk_i32 m0, 0x2000\n" \
    "s_cmp_eq_u32 s36, 0\n" \
    "s_cbranch_scc0 17f\n" \
    "v_and_b32 v36, 0x380, v127\n" \
    "v_add_u32 v36, v36, v127\n" \
    "v_and_b32 v37, 0x300, v127\n" \
    "v_add_u32 v37, v37, v127\n" \
    "s_add_i32 s93, s64, 0x1000000\n" \
    "v_add_u32 v21, s95, v127\n" \
    "v_xor_b32 v17, 32, v119\n" \
    "v_xor_b32 v18, 64, v119\n" \
    "v_xor_b32 v19, 96, v119\n" \
    "s_waitcnt vmcnt(8) lgkmcnt(0)\n" \
    "s_barrier\n" \
    "ds_read_b64_tr_b16 v[40:41], v119 offset:0\n" \
    "ds_read_b64_tr_b16 v[42:43], v18 offset:512\n" \
    "ds_read_b64_tr_b16 v[44:45], v119 offset:8192\n" \
    "ds_read_b64_tr_b16 v[46:47], v18 offset:8704\n" \
    "ds_read_b64_tr_b16 v[48:49], v17 offset:0\n" \
    "ds_read_b64_tr_b16 v[50:51], v19 offset:512\n" \
    "ds_read_b64_tr_b16 v[52:53], v17 offset:8192\n" \
    "ds_read_b64_tr_b16 v[54:55], v19 offset:8704\n" \
    "ds_read_b64_tr_b16 v[56:57], v18 offset:0\n" \
    "ds_read_b64_tr_b16 v[58:59], v119 offset:512\n" \
    "ds_read_b64_tr_b16 v[60:61], v18 offset:8192\n" \
    "ds_read_b64_tr_b16 v[62:63], v119 offset:8704\n" \
    "s_add_i32 m0, s95, 0\n" \
    "s_nop 0\n" \
    "buffer_load_dwordx4 v36, %[rw], s64 offen sc1 lds\n" \
    "s_add_i32 m0, s95, 896\n" \
    "s_nop 0\n" \
    "buffer_load_dwordx4 v36, %[rw], s64 offen offset:128 sc1 lds\n" \
    "s_add_i32 m0, s95, 8192\n" \
    "s_nop 0\n" \
    "buffer_load_dwordx4 v37, %[rw], s93 offen sc1 lds\n" \
    "s_add_i32 m0, s95, 8960\n" \
    "s_nop 0\n" \
    "buffer_load_dwordx4 v37, %[rw], s93 offen offset:256 sc1 lds\n" \
    "s_waitcnt lgkmcnt(8)\n" \
    "ds_read_b64_tr_b16 v[64:65], v19 offset:0\n" \
    "ds_read_b64_tr_b16 v[66:67], v17 offset:512\n" \
    "ds_read_b64_tr_b16 v[68:69], v19 offset:8192\n" \
    "ds_read_b64_tr_b16 v[70:71], v17 offset:8704\n" \
    "s_waitcnt vmcnt(4) lgkmcnt(0)\n" \
    "s_barrier\n" \
    "ds_read_b64_tr_b16 v[0:1], v119 offset:32768\n" \
    "ds_read_b64_tr_b16 v[2:3], v18 offset:33280\n" \
    "ds_read_b64_tr_b16 v[4:5], v119 offset:40960\n" \
    "ds_read_b64_tr_b16 v[6:7], v18 offset:41472\n" \
    "ds_write_b128 v106, v[40:43] offset:0\n" \
    "ds_write_b128 v106, v[44:47] offset:1024\n" \
    "ds_read_b64_tr_b16 v[8:9], v17 offset:32768\n" \
    "ds_read_b64_tr_b16 v[10:11], v19 offset:33280\n" \
    "ds_read_b64_tr_b16 v[12:13], v17 offset:40960\n" \
    "ds_read_b64_tr_b16 v[14:15], v19 offset:41472\n" \
    "ds_write_b128 v106, v[48:51] offset:2048\n" \
    "ds_write_b128 v106, v[52:55] offset:3072\n" \
    "s_waitcnt lgkmcnt(8)\n" \
    "ds_read_b64_tr_b16 v[40:41], v18 offset:32768\n" \
    "ds_read_b64_tr_b16 v[42:43], v119 offset:33280\n" \
    "ds_read_b64_tr_b16 v[44:45], v18 offset:40960\n" \
    "ds_read_b64_tr_b16 v[46:47], v119 offset:41472\n" \
    "s_waitcnt lgkmcnt(6)\n" \
    "ds_read_b64_tr_b16 v[48:49], v19 offset:32768\n" \
    "ds_read_b64_tr_b16 v[50:51], v17 offset:33280\n" \
    "ds_read_b64_tr_b16 v[52:53], v19 offset:40960\n" \
    "ds_read_b64_tr_b16 v[54:55], v17 offset:41472\n" \
    "ds_write_b128 v106, v[56:59] offset:4096\n" \
    "ds_write_b128 v106, v[60:63] offset:5120\n" \
    "ds_write_b128 v106, v[64:67] offset:6144\n" \
    "ds_write_b128 v106, v[68:71] offset:7168\n" \
    "s_waitcnt lgkmcnt(4)\n" \
    "s_barrier\n" \
    "ds_write_b128 v106, v[0:3] offset:32768\n" \
    "ds_write_b128 v106, v[4:7] offset:33792\n" \
    "ds_write_b128 v106, v[8:11] offset:34816\n" \
    "ds_write_b128 v106, v[12:15] offset:35840\n" \
    "ds_write_b128 v106, v[40:43] offset:36864\n" \
    "ds_write_b128 v106, v[44:47] offset:37888\n" \
    "ds_write_b128 v106, v[48:51] offset:38912\n" \
    "ds_write_b128 v106, v[52:55] offset:39936\n" \
    "v_mov_b64 v[56:57], 0\n" \
    "v_mov_b64 v[58:59], 0\n" \
    "v_mov_b64 v[60:61], 0\n" \
    "v_mov_b64 v[62:63], 0\n" \
    "v_mov_b64 v[64:65], 0\n" \
    "v_mov_b64 v[66:67], 0\n" \
    "v_mov_b64 v[68:69], 0\n" \
    "v_mov_b64 v[70:71], 0\n" \
    "s_waitcnt lgkmcnt(6)\n" \
    "ds_read_b128 v[0:3], v108 offset:0\n" \
    "ds_read_b128 v[16:19], v108 offset:4096\n" \
    "s_waitcnt lgkmcnt(2)\n" \
    "v_mov_b64 v[40:41], 0\n" \
    "v_mov_b64 v[42:43], 0\n" \
    "v_mov_b64 v[44:45], 0\n" \
    "v_mov_b64 v[46:47], 0\n" \
    "v_mov_b64 v[48:49], 0\n" \
    "v_mov_b64 v[50:51], 0\n" \
    "v_mov_b64 v[52:53], 0\n" \
    "v_mov_b64 v[54:55], 0\n" \
    "73:\n" \
    "s_waitcnt vmcnt(0) lgkmcnt(0)\n" \
    "ds_read_b128 v[4:7], v21 offset:0\n" \
    "ds_read_b128 v[8:11], v21 offset:1024\n" \
    "ds_read_b128 v[12:15], v21 offset:8192\n" \
    "ds_read_b128 v[102:105], v21 offset:9216\n" \
    "v_mov_b32 v107, 0\n" \
    "s_waitcnt lgkmcnt(3)\n" \
    "v_max3_u32 v107, v107, v4, v5\n" \
    "v_max3_u32 v107, v107, v6, v7\n" \
    "s_waitcnt lgkmcnt(2)\n" \
    "v_max3_u32 v107, v107, v8, v9\n" \
    "v_max3_u32 v107, v107, v10, v11\n" \
    "s_waitcnt lgkmcnt(1)\n" \
    "v_max3_u32 v107, v107, v12, v13\n" \
    "v_max3_u32 v107, v107, v14, v15\n" \
    "s_waitcnt lgkmcnt(0)\n" \
    "v_max3_u32 v107, v107, v102, v103\n" \
    "v_max3_u32 v107, v107, v104, v105\n" \
    "v_cmp_eq_u32 vcc, -1, v107\n" \
    "s_add_i32 s39, s39, -1\n" \
    "s_cmp_lt_i32 s39, 0\n" \
    "s_nop 2\n" \
    "s_cbranch_vccnz 75f\n" \
    "74:\n" \
    "s_barrier\n" \
    "s_memrealtime s[88:89]\n" \
    "s_pack_ll_b32_b16 s80, 1, s42\n" \
    "v_mov_b32 v5, s80\n" \
    "s_cmp_eq_u32 s60, 0\n" \
    "s_cselect_b32 s75, 0x1010101, 0\n" \
    "s_waitcnt lgkmcnt(0)\n" \
    "s_lshl3_add_u32 s80, s88, s37\n" \
    "v_mov_b32 v4, s80\n" \
    "s_mov_b32 exec_lo, s75\n" \
    "s_mov_b32 exec_hi, 0\n" \
    "buffer_store_dwordx2 v[4:5], v127, %[rs], s61 offen sc1\n" \
    "s_mov_b64 exec, -1\n" \
    "s_branch 3f\n" \
    "75:\n" \
    "s_cbranch_scc1 74b\n" \
    "s_add_i32 m0, s95, 0\n" \
    "s_nop 0\n" \
    "buffer_load_dwordx4 v36, %[rw], s64 offen sc1 lds\n" \
    "s_add_i32 m0, s95, 896\n" \
    "s_nop 0\n" \
    "buffer_load_dwordx4 v36, %[rw], s64 offen offset:128 sc1 lds\n" \
    "s_add_i32 m0, s95, 8192\n" \
    "s_nop 0\n" \
    "buffer_load_dwordx4 v37, %[rw], s93 offen sc1 lds\n" \
    "s_add_i32 m0, s95, 8960\n" \
    "s_nop 0\n" \
    "buffer_load_dwordx4 v37, %[rw], s93 offen offset:256 sc1 lds\n" \
    "s_branch 73b\n" \
    "17:\n" \
    "s_sub_i32 s37, s37, s29\n" \
    "12:\n" \
    "1:\n" \
    "s_memtime vcc\n" \
    "s_mul_i32 s90, s37, s25\n" \
    "s_ashr_i32 s90, s90, 12\n" \
    "s_add_i32 s90, s90, s24\n" \
    "s_add_i32 s90, s90, -116\n" \
    "s_waitcnt lgkmcnt(0)\n" \
    "s_sub_i32 s75, s90, vcc_lo\n" \
    "s_cmp_gt_i32 s75, 112\n" \
    "s_cbranch_scc1 1b\n" \
    "s_max_i32 s75, s75, 0\n" \
    "s_and_b32 s75, s75, -4\n" \
    "s_sub_u32 vcc_lo, s96, s75\n" \
    "s_subb_u32 vcc_hi, s97, 0\n" \
    "s_setpc_b64 vcc\n" \
    "s_nop 0\n" \
    "s_nop 0\n" \
    "s_nop 0\n" \
    "s_nop 0\n" \
    "s_nop 0\n" \
    "s_nop 0\n" \
    "s_nop 0\n" \
    "s_nop 0\n" \
    "s_nop 0\n" \
    "s_nop 0\n" \
    "s_nop 0\n" \
    "s_nop 0\n" \
    "s_nop 0\n" \
    "s_nop 0\n" \
    "s_nop 0\n" \
    "s_nop 0\n" \
    "s_nop 0\n" \
    "s_nop 0\n" \
    "s_nop 0\n" \
    "s_nop 0\n" \
    "s_nop 0\n" \
    "s_nop 0\n" \
    "s_nop 0\n" \
    "s_nop 0\n" \
    "s_nop 0\n" \
    "s_nop 0\n" \
    "s_nop 0\n" \
    "s_nop 0\n" \
    "2:\n" \
    "buffer_load_dwordx4 v[0:3], v125, %[rs], s69 offen sc1\n" \
    "s_nop 3\n" \
    "s_nop 3\n" \
    "buffer_load_dwordx4 v[16:19], v125, %[rs], s70 offen sc1\n" \
    "s_mov_b32 exec_lo, s27\n" \
    "s_mov_b32 exec_hi, s27\n" \
    "buffer_load_dwordx4 v22, %[rs], s71 offen sc1 lds\n" \
    "s_mov_b64 exec, -1\n" \
    "v_bfe_u32 v20, v127, 8, 2\n" \
    "v_subrev_u32 v20, s60, v20\n" \
    "v_mul_i32_i24 v20, -1088, v20\n" \
    "v_add_u32 v20, v20, v129\n" \
    "v_lshrrev_b32 v21, 2, v127\n" \
    "s_mov_b32 exec_lo, s66\n" \
    "s_mov_b32 exec_hi, s66\n" \
    "ds_read2_b32 v[96:97], v20 offset0:0 offset1:144\n" \
    "s_mov_b64 exec, -1\n" \
    "s_lshl_b32 s80, s36, 16\n" \
    "s_add_i32 s80, s80, s67\n" \
    "s_sub_i32 s75, s37, s79\n" \
    "s_cmp_gt_i32 s75, 24\n" \
    "s_cselect_b32 s76, 2, 0\n" \
    "s_cmp_lt_i32 s75, 10\n" \
    "s_cselect_b32 s76, -6, s76\n" \
    "s_add_i32 s28, s28, s76\n" \
    "s_max_i32 s28, s28, 0\n" \
    "s_min_i32 s28, s28, 20\n" \
    "s_cmp_eq_u32 s60, 0\n" \
    "s_cselect_b32 s72, 0x1010101, 0\n" \
    "s_waitcnt lgkmcnt(0)\n" \
    "buffer_store_dword v96, v21, %[rw], s80 offen sc1\n" \
    "buffer_store_dword v97, v21, %[rw], s80 offen offset:256 sc1\n" \
    "3:\n" \
    "s_mov_b32 m0, s95\n" \
    "s_add_i32 s91, s36, 1\n" \
    "s_and_b32 s78, s36, 3\n" \
    "s_lshl_b32 s78, s78, 14\n" \
    "s_add_i32 s77, s78, s61\n" \
    "s_bitcmp1_b32 s36, 0\n" \
    "s_cselect_b32 s79, 0x8000, 0\n" \
    "s_lshl_b32 s92, s91, 18\n" \
    "s_add_i32 s92, s92, s64\n" \
    "s_add_i32 s78, s78, 0x1a0000\n" \
    "s_add_i32 s93, s92, 0x1000000\n" \
    "s_sub_i32 s76, s33, s42\n" \
    "s_ashr_i32 s76, s76, s32\n" \
    "s_add_i32 s42, s42, s76\n" \
    "s_lshr_b32 s76, s32, 8\n" \
    "s_sub_i32 s76, s76, s40\n" \
    "s_ashr_i32 s76, s76, 2\n" \
    "s_add_i32 s40, s40, s76\n" \
    "s_sub_i32 s76, s40, s24\n" \
    "s_min_i32 s94, s91, s38\n" \
    "s_lshl_b32 s94, s94, 18\n" \
    "s_andn2_b32 s80, 0x1000000, s66\n" \
    "s_add_i32 s94, s94, s64\n" \
    "s_add_i32 s94, s94, s80\n" \
    "s_add_i32 s75, s36, 2\n" \
    "s_cmp_lt_i32 s38, s75\n" \
    "s_cselect_b32 s75, s62, 0x1b0040\n" \
    "v_mov_b64 v[98:99], 0\n" \
    "v_mov_b32 v101, 0\n" \
    "s_pack_ll_b32_b16 s80, s91, s42\n" \
    "v_mov_b32 v103, s80\n" \
    "s_waitcnt vmcnt(4)\n" \
    "v_smfmac_f32_16x16x64_bf16 v[40:43], v[0:3], a[0:7], v124\n" \
    "ds_write_b128 v108, v[0:3] offset:0\n" \
    "v_smfmac_f32_16x16x64_bf16 v[44:47], v[0:3], a[8:15], v124\n" \
    "v_smfmac_f32_16x16x64_bf16 v[48:51], v[0:3], a[128:135], v124\n" \
    "v_smfmac_f32_16x16x64_bf16 v[52:55], v[0:3], a[136:143], v124\n" \
    "s_waitcnt vmcnt(3)\n" \
    "v_smfmac_f32_16x16x64_bf16 v[40:43], v[16:19], a[64:71], v124\n" \
    "ds_write_b128 v108, v[16:19] offset:4096\n" \
    "v_smfmac_f32_16x16x64_bf16 v[44:47], v[16:19], a[72:79], v124\n" \
    "s_waitcnt lgkmcnt(1)\n" \
    "s_barrier\n" \
    "v_smfmac_f32_16x16x64_bf16 v[48:51], v[16:19], a[192:199], v124\n" \
    "ds_read_b128 v[4:7], v24 offset:0\n" \
    "v_smfmac_f32_16x16x64_bf16 v[52:55], v[16:19], a[200:207], v124\n" \
    "ds_read_b128 v[8:11], v28 offset:0\n" \
    "v_smfmac_f32_16x16x64_bf16 v[56:59], v[0:3], v[72:79], v124\n" \
    "ds_read_b128 v[12:15], v32 offset:0\n" \
    "v_smfmac_f32_16x16x64_bf16 v[60:63], v[0:3], v[80:87], v124\n" \
    "s_waitcnt vmcnt(2) lgkmcnt(3)\n" \
    "s_barrier\n" \
    "s_memtime s[86:87]\n" \
    "v_smfmac_f32_16x16x64_bf16 v[56:59], v[16:19], v[168:175], v124\n" \
    "ds_read_b128 v[24:27], v24 offset:4096\n" \
    "v_smfmac_f32_16x16x64_bf16 v[60:63], v[16:19], v[176:183], v124\n" \
    "ds_read_b128 v[28:31], v28 offset:4096\n" \
    "s_waitcnt lgkmcnt(3)\n" \
    "v_smfmac_f32_16x16x64_bf16 v[40:43], v[4:7], a[16:23], v124\n" \
    "ds_read_b128 v[32:35], v32 offset:4096\n" \
    "v_smfmac_f32_16x16x64_bf16 v[40:43], v[8:11], a[32:39], v124\n" \
    "ds_read_b128 v[36:39], v126 offset:0\n" \
    "s_waitcnt lgkmcnt(4)\n" \
    "v_smfmac_f32_16x16x64_bf16 v[40:43], v[12:15], a[48:55], v124\n" \
    "buffer_load_dword v110, v114, %[rw], s94 offen sc1\n" \
    "v_smfmac_f32_16x16x64_bf16 v[44:47], v[4:7], a[24:31], v124\n" \
    "s_waitcnt lgkmcnt(2)\n" \
    "v_smfmac_f32_16x16x64_bf16 v[40:43], v[24:27], a[80:87], v124\n" \
    "v_smfmac_f32_16x16x64_bf16 v[40:43], v[28:31], a[96:103], v124\n" \
    "s_waitcnt lgkmcnt(0)\n" \
    "v_smfmac_f32_16x16x64_bf16 v[40:43], v[32:35], a[112:119], v124\n" \
    "ds_read_b128 v[72:75], v133 offset:18432\n" \
    "s_add_i32 s80, s86, s76\n" \
    "v_smfmac_f32_16x16x64_bf16 v[40:43], v[36:39], v[136:143], v124\n" \
    "ds_read_b128 v[76:79], v133 offset:19456\n" \
    "s_mul_hi_u32 s80, s80, s41\n" \
    "v_smfmac_f32_16x16x64_bf16 v[44:47], v[8:11], a[40:47], v124\n" \
    "ds_read_b128 v[80:83], v133 offset:32768\n" \
    "s_add_i32 s80, s80, s29\n" \
    "v_mov_b32 v102, s80\n" \
    "v_smfmac_f32_16x16x64_bf16 v[44:47], v[12:15], a[56:63], v124\n" \
    "ds_read_b128 v[84:87], v133 offset:33792\n" \
    "v_add_f32 v40, v40, v42\n" \
    "v_add_f32 v41, v41, v43\n" \
    "v_smfmac_f32_16x16x64_bf16 v[44:47], v[24:27], a[88:95], v124\n" \
    "ds_read_b128 v[20:23], v127 offset:19456\n" \
    "v_cmp_u_f32 vcc, v40, v41\n" \
    "v_smfmac_f32_16x16x64_bf16 v[44:47], v[28:31], a[104:111], v124\n" \
    "v_mul_f32 v40, s81, v40\n" \
    "v_smfmac_f32_16x16x64_bf16 v[44:47], v[32:35], a[120:127], v124\n" \
    "s_cbranch_vccnz 5f\n" \
    "6:\n" \
    "s_mov_b64 exec, s[72:73]\n" \
    "v_smfmac_f32_16x16x64_bf16 v[44:47], v[36:39], v[144:151], v124\n" \
    "buffer_store_dwordx2 v[102:103], v127, %[rs], s77 offen sc1\n" \
    "v_smfmac_f32_16x16x64_bf16 v[56:59], v[4:7], v[88:95], v124\n" \
    "s_mov_b64 exec, -1\n" \
    "s_waitcnt lgkmcnt(1)\n" \
    "v_mul_f32 v41, s81, v41\n" \
    "v_smfmac_f32_16x16x64_bf16 v[60:63], v[4:7], v[72:79], v124\n" \
    "ds_read_b128 v[88:91], v133 offset:34816\n" \
    "v_add_f32 v44, v44, v46\n" \
    "v_smfmac_f32_16x16x64_bf16 v[56:59], v[8:11], v[80:87], v124\n" \
    "ds_read_b128 v[92:95], v133 offset:35840\n" \
    "v_add_f32 v45, v45, v47\n" \
    "v_smfmac_f32_16x16x64_bf16 v[48:51], v[4:7], a[144:151], v124\n" \
    "ds_read_b128 v[72:75], v133 offset:49152\n" \
    "v_mul_f32 v44, s81, v44\n" \
    "v_smfmac_f32_16x16x64_bf16 v[52:55], v[4:7], a[152:159], v124\n" \
    "ds_read_b128 v[76:79], v133 offset:50176\n" \
    "v_mul_f32 v45, s81, v45\n" \
    "v_smfmac_f32_16x16x64_bf16 v[48:51], v[8:11], a[160:167], v124\n" \
    "ds_read_b128 v[0:3], v127 offset:16384\n" \
    "v_cvt_pk_bf16_f32 v107, v40, v44\n" \
    "v_smfmac_f32_16x16x64_bf16 v[52:55], v[8:11], a[168:175], v124\n" \
    "ds_read_b128 v[4:7], v127 offset:18432\n" \
    "s_waitcnt lgkmcnt(4)\n" \
    "v_cvt_pk_bf16_f32 v108, v41, v45\n" \
    "v_smfmac_f32_16x16x64_bf16 v[60:63], v[8:11], v[88:95], v124\n" \
    "ds_write2_b32 v128, v107, v108 offset0:0 offset1:80\n" \
    "v_smfmac_f32_16x16x64_bf16 v[60:63], v[12:15], v[248:255], v124\n" \
    "ds_read_b128 v[16:19], v127 offset:17408\n" \
    "s_waitcnt lgkmcnt(4)\n" \
    "v_smfmac_f32_16x16x64_bf16 v[56:59], v[12:15], v[72:79], v124\n" \
    "ds_read_b128 v[8:11], v127 offset:20480\n" \
    "v_smfmac_f32_16x16x64_bf16 v[48:51], v[12:15], a[176:183], v124\n" \
    "ds_read_b128 v[40:43], v127 offset:21504\n" \
    "v_smfmac_f32_16x16x64_bf16 v[52:55], v[12:15], a[184:191], v124\n" \
    "ds_read_b128 v[44:47], v127 offset:23552\n" \
    "v_smfmac_f32_16x16x64_bf16 v[48:51], v[24:27], a[208:215], v124\n" \
    "s_waitcnt lgkmcnt(4)\n" \
    "s_barrier\n" \
    "v_smfmac_f32_16x16x64_bf16 v[52:55], v[24:27], a[216:223], v124\n" \
    "ds_read_b128 v[72:75], v130 offset:0\n" \
    "v_smfmac_f32_16x16x64_bf16 v[48:51], v[28:31], a[224:231], v124\n" \
    "ds_read_b128 v[76:79], v130 offset:64\n" \
    "v_smfmac_f32_16x16x64_bf16 v[48:51], v[32:35], a[240:247], v124\n" \
    "ds_read_b128 v[80:83], v130 offset:128\n" \
    "v_smfmac_f32_16x16x64_bf16 v[48:51], v[36:39], v[152:159], v124\n" \
    "ds_read_b128 v[84:87], v130 offset:192\n" \
    "v_smfmac_f32_16x16x64_bf16 v[52:55], v[28:31], a[232:239], v124\n" \
    "ds_read_b128 v[12:15], v127 offset:22528\n" \
    "v_smfmac_f32_16x16x64_bf16 v[52:55], v[32:35], a[248:255], v124\n" \
    "v_add_f32 v48, v48, v50\n" \
    "v_smfmac_f32_16x16x64_bf16 v[52:55], v[36:39], v[160:167], v124\n" \
    "s_waitcnt lgkmcnt(3)\n" \
    "v_add_f32 v49, v49, v51\n" \
    "v_mfma_f32_16x16x32_bf16 v[88:91], v[0:3], v[72:75], 0\n" \
    "ds_read_b64 v[0:1], v132 offset:0\n" \
    "ds_read_b64 v[2:3], v132 offset:1024\n" \
    "v_mfma_f32_16x16x32_bf16 v[88:91], v[4:7], v[76:79], v[88:91]\n" \
    "s_waitcnt lgkmcnt(2)\n" \
    "v_add_f32 v52, v52, v54\n" \
    "v_add_f32 v53, v53, v55\n" \
    "v_mfma_f32_16x16x32_bf16 v[88:91], v[8:11], v[80:83], v[88:91]\n" \
    "v_cvt_pk_bf16_f32 v107, v48, v52\n" \
    "v_cvt_pk_bf16_f32 v108, v49, v53\n" \
    "v_mfma_f32_16x16x32_bf16 v[88:91], v[12:15], v[84:87], v[88:91]\n" \
    "ds_write2_b32 v129, v107, v108 offset0:0 offset1:144\n" \
    "v_mfma_f32_16x16x32_bf16 v[92:95], v[16:19], v[72:75], 0\n" \
    "ds_read_b64 v[4:5], v132 offset:512\n" \
    "ds_read_b64 v[6:7], v132 offset:1536\n" \
    "v_mfma_f32_16x16x32_bf16 v[92:95], v[20:23], v[76:79], v[92:95]\n" \
    "ds_read_b128 v[8:11], v106 offset:0\n" \
    "v_exp_f32 v88, v88\n" \
    "v_mfma_f32_16x16x32_bf16 v[92:95], v[40:43], v[80:83], v[92:95]\n" \
    "ds_read_b128 v[12:15], v106 offset:1024\n" \
    "v_exp_f32 v89, v89\n" \
    "v_mfma_f32_16x16x32_bf16 v[92:95], v[44:47], v[84:87], v[92:95]\n" \
    "s_waitcnt lgkmcnt(4)\n" \
    "s_barrier\n" \
    "v_smfmac_f32_16x16x64_bf16 v[56:59], v[24:27], v[184:191], v124\n" \
    "ds_read_b128 v[48:51], v131 offset:0\n" \
    "ds_read_b128 v[52:55], v131 offset:128\n" \
    "v_cvt_pk_bf16_f32 v88, v88, v89\n" \
    "v_smfmac_f32_16x16x64_bf16 v[56:59], v[28:31], v[200:207], v124\n" \
    "ds_read_b128 v[16:19], v106 offset:32768\n" \
    "v_exp_f32 v90, v90\n" \
    "v_smfmac_f32_16x16x64_bf16 v[56:59], v[32:35], v[216:223], v124\n" \
    "ds_read_b128 v[20:23], v106 offset:33792\n" \
    "v_exp_f32 v91, v91\n" \
    "v_smfmac_f32_16x16x64_bf16 v[56:59], v[36:39], v[232:239], v124\n" \
    "ds_read_b128 v[40:43], v106 offset:2048\n" \
    "s_waitcnt lgkmcnt(3)\n" \
    "v_exp_f32 v92, v92\n" \
    "v_cvt_pk_bf16_f32 v89, v90, v91\n" \
    "v_smfmac_f32_16x16x64_bf16 v[64:67], v[48:51], v[72:79], v124\n" \
    "ds_read_b128 v[44:47], v106 offset:3072\n" \
    "v_exp_f32 v93, v93\n" \
    "v_smfmac_f32_16x16x64_bf16 v[64:67], v[52:55], v[80:87], v124\n" \
    "ds_read_b128 v[72:75], v106 offset:34816\n" \
    "v_exp_f32 v94, v94\n" \
    "v_cvt_pk_bf16_f32 v90, v92, v93\n" \
    "v_smfmac_f32_16x16x64_bf16 v[60:63], v[24:27], v[192:199], v124\n" \
    "ds_read_b128 v[76:79], v106 offset:35840\n" \
    "v_exp_f32 v95, v95\n" \
    "v_smfmac_f32_16x16x64_bf16 v[60:63], v[28:31], v[208:215], v124\n" \
    "ds_read_b128 v[80:83], v106 offset:4096\n" \
    "s_cmp_eq_u32 s36, 62\n" \
    "v_add_f32 v64, v64, v66\n" \
    "v_add_f32 v65, v65, v67\n" \
    "v_smfmac_f32_16x16x64_bf16 v[60:63], v[32:35], v[224:231], v124\n" \
    "ds_read_b128 v[84:87], v106 offset:5120\n" \
    "v_exp_f32 v64, v64\n" \
    "v_cvt_pk_bf16_f32 v91, v94, v95\n" \
    "v_smfmac_f32_16x16x64_bf16 v[60:63], v[36:39], v[240:247], v124\n" \
    "ds_read_b128 v[48:51], v106 offset:36864\n" \
    "v_exp_f32 v65, v65\n" \
    "v_swap_b32 v1, v2\n" \
    "v_swap_b32 v5, v6\n" \
    "v_mfma_f32_16x16x32_bf16 v[102:105], v[88:91], v[120:123], 0\n" \
    "ds_read_b128 v[52:55], v106 offset:37888\n" \
    "v_cvt_pk_bf16_f32 v100, v64, v65\n" \
    "v_add_f32 v56, v56, v58\n" \
    "v_add_f32 v57, v57, v59\n" \
    "v_smfmac_f32_16x16x64_bf16 v[68:71], v[88:91], v[0:7], v124\n" \
    "ds_read_b128 v[24:27], v106 offset:6144\n" \
    "v_add_f32 v60, v60, v62\n" \
    "v_add_f32 v61, v61, v63\n" \
    "v_mfma_f32_16x16x16_bf16 v[102:105], v[100:101], v[120:121], v[102:105]\n" \
    "ds_read_b128 v[28:31], v106 offset:7168\n" \
    "v_cvt_pk_bf16_f32 v96, v56, v57\n" \
    "v_cvt_pk_bf16_f32 v97, v60, v61\n" \
    "s_nop 1\n" \
    "v_smfmac_f32_16x16x32_bf16 v[68:71], v[100:101], v[96:99], v124\n" \
    "ds_read_b128 v[32:35], v106 offset:38912\n" \
    "v_mov_b64 v[56:57], 0\n" \
    "v_mov_b64 v[58:59], 0\n" \
    "v_rcp_f32 v104, v102\n" \
    "v_rcp_f32 v105, v103\n" \
    "v_mul_f32 v68, v68, v104\n" \
    "v_mul_f32 v70, v70, v104\n" \
    "v_mul_f32 v69, v69, v105\n" \
    "v_mul_f32 v71, v71, v105\n" \
    "v_cvt_pk_bf16_f32 v107, v68, v70\n" \
    "v_cvt_pk_bf16_f32 v108, v69, v71\n" \
    "ds_write2_b32 v129, v107, v108 offset0:64 offset1:208\n" \
    "v_max3_f32 v102, v102, v103, v104\n" \
    "v_max_f32 v102, v102, v105\n" \
    "v_cmp_nge_f32 vcc, s82, v102\n" \
    "v_mov_b64 v[60:61], 0\n" \
    "v_mov_b64 v[62:63], 0\n" \
    "v_mov_b64 v[88:89], 0\n" \
    "v_mov_b64 v[90:91], 0\n" \
    "v_mov_b64 v[92:93], 0\n" \
    "v_mov_b64 v[94:95], 0\n" \
    "s_waitcnt vmcnt(2) lgkmcnt(0)\n" \
    "s_barrier\n" \
    "ds_read_b128 v[68:71], v109 offset:256\n" \
    "ds_read_b128 v[102:105], v111 offset:384\n" \
    "s_cbranch_vccnz 7f\n" \
    "buffer_load_dwordx2 v[98:99], v113, %[rs], s78 offen sc1\n" \
    "buffer_load_dword v100, off, %[rw], s75 sc1\n" \
    "v_mov_b64 v[64:65], 0\n" \
    "v_mov_b64 v[66:67], 0\n" \
    "s_waitcnt lgkmcnt(1)\n" \
    "v_smfmac_f32_16x16x64_bf16 v[56:59], v[68:71], v[8:15], v124\n" \
    "s_waitcnt lgkmcnt(0)\n" \
    "ds_read_b128 v[36:39], v106 offset:39936\n" \
    "v_smfmac_f32_16x16x64_bf16 v[56:59], v[102:105], v[16:23], v124\n" \
    "ds_read_b128 v[0:3], v133 offset:51200\n" \
    "v_smfmac_f32_16x16x64_bf16 v[60:63], v[68:71], v[40:47], v124\n" \
    "ds_read_b128 v[4:7], v133 offset:52224\n" \
    "v_smfmac_f32_16x16x64_bf16 v[60:63], v[102:105], v[72:79], v124\n" \
    "ds_read_b128 v[16:19], v112 offset:51200\n" \
    "v_add_f32 v56, v56, v58\n" \
    "v_smfmac_f32_16x16x64_bf16 v[88:91], v[68:71], v[80:87], v124\n" \
    "ds_read_b128 v[20:23], v112 offset:52224\n" \
    "v_add_f32 v57, v57, v59\n" \
    "v_smfmac_f32_16x16x64_bf16 v[88:91], v[102:105], v[48:55], v124\n" \
    "v_add_f32 v60, v60, v62\n" \
    "v_smfmac_f32_16x16x64_bf16 v[92:95], v[68:71], v[24:31], v124\n" \
    "s_waitcnt lgkmcnt(4)\n" \
    "v_add_f32 v61, v61, v63\n" \
    "v_smfmac_f32_16x16x64_bf16 v[92:95], v[102:105], v[32:39], v124\n" \
    "s_waitcnt lgkmcnt(2)\n" \
    "v_add_f32 v88, v88, v90\n" \
    "v_smfmac_f32_16x16x64_bf16 v[64:67], v[68:71], v[0:7], v124\n" \
    "s_waitcnt lgkmcnt(0)\n" \
    "v_add_f32 v89, v89, v91\n" \
    "v_smfmac_f32_16x16x64_bf16 v[64:67], v[102:105], v[16:23], v124\n" \
    "v_add_f32 v92, v92, v94\n" \
    "v_add_f32 v93, v93, v95\n" \
    "v_cvt_pk_bf16_f32 v8, v56, v60\n" \
    "v_cvt_pk_bf16_f32 v10, v57, v61\n" \
    "v_cvt_pk_bf16_f32 v9, v88, v92\n" \
    "v_cvt_pk_bf16_f32 v11, v89, v93\n" \
    "buffer_store_dwordx4 v[8:11], v134, %[rs], s79 offen sc1\n" \
    "v_add_f32 v64, v64, v66\n" \
    "v_add_f32 v65, v65, v67\n" \
    "v_cvt_pk_bf16_f32 v64, v64, v65\n" \
    "buffer_store_dword v64, v135, %[rs], s79 offen sc1\n" \
    "s_cbranch_scc1 16f\n" \
    "s_memtime s[88:89]\n" \
    "s_memrealtime s[44:45]\n" \
    "s_and_b32 s75, s91, 3\n" \
    "s_lshl_b32 s75, s75, 16\n" \
    "s_memrealtime s[46:47]\n" \
    "s_add_i32 s69, s75, s26\n" \
    "s_xor_b32 s70, s69, 2048\n" \
    "s_memrealtime s[48:49]\n" \
    "s_andn2_b32 s71, s69, 0x1fff\n" \
    "s_add_i32 s71, s71, 32768\n" \
    "s_memrealtime s[50:51]\n" \
    "s_mov_b32 s94, 0\n" \
    "s_mov_b32 s43, 0\n" \
    "s_memrealtime s[52:53]\n" \
    "s_cmp_lt_i32 s38, s91\n" \
    "s_nop 0\n" \
    "s_memrealtime s[54:55]\n" \
    "s_nop 1\n" \
    "s_memrealtime s[56:57]\n" \
    "s_nop 1\n" \
    "s_memrealtime vcc\n" \
    "v_and_b32 v19, 0x380, v127\n" \
    "v_add_u32 v19, v19, v127\n" \
    "v_and_b32 v18, 0x300, v127\n" \
    "v_add_u32 v18, v18, v127\n" \
    "s_cbranch_scc1 40f\n" \
    "41:\n" \
    "buffer_load_dwordx4 v19, %[rw], s92 offen sc1 lds\n" \
    "s_add_i32 m0, s95, 896\n" \
    "s_waitcnt lgkmcnt(0)\n" \
    "s_add_i32 s44, s44, s46\n" \
    "s_add_i32 s48, s48, s50\n" \
    "s_add_i32 s52, s52, s54\n" \
    "s_add_i32 s44, s44, s48\n" \
    "s_add_i32 s52, s52, s56\n" \
    "s_add_i32 s44, s44, s52\n" \
    "s_add_i32 s80, s44, vcc_lo\n" \
    "s_sub_i32 s45, s80, s29\n" \
    "s_mov_b32 s29, s80\n" \
    "s_sub_i32 s47, s88, s24\n" \
    "s_mov_b32 s24, s88\n" \
    "buffer_load_dwordx4 v19, %[rw], s92 offen offset:128 sc1 lds\n" \
    "s_add_i32 m0, s95, 8192\n" \
    "s_add_i32 s76, s29, s42\n" \
    "v_add_u32 v16, -1, s36\n" \
    "v_mov_b32 v17, s76\n" \
    "s_mov_b64 exec, s[72:73]\n" \
    "buffer_store_dwordx2 v[16:17], off, %[rw], s65 sc1\n" \
    "s_mov_b64 exec, -1\n" \
    "ds_read_b128 v[72:75], v133 offset:0\n" \
    "ds_read_b128 v[76:79], v133 offset:1024\n" \
    "ds_read_b128 v[80:83], v133 offset:2048\n" \
    "ds_read_b128 v[84:87], v133 offset:3072\n" \
    "ds_read_b128 v[88:91], v133 offset:16384\n" \
    "ds_read_b128 v[92:95], v133 offset:17408\n" \
    "buffer_load_dwordx4 v18, %[rw], s93 offen sc1 lds\n" \
    "s_add_i32 m0, s95, 8960\n" \
    "v_mov_b64 v[40:41], 0\n" \
    "v_mov_b64 v[42:43], 0\n" \
    "v_mov_b64 v[44:45], 0\n" \
    "v_mov_b64 v[46:47], 0\n" \
    "v_mov_b64 v[48:49], 0\n" \
    "v_mov_b64 v[50:51], 0\n" \
    "v_mov_b64 v[52:53], 0\n" \
    "v_mov_b64 v[54:55], 0\n" \
    "buffer_load_dwordx4 v18, %[rw], s93 offen offset:256 sc1 lds\n" \
    "s_movk_i32 m0, 0x2000\n" \
    "v_mov_b64 v[56:57], 0\n" \
    "v_mov_b64 v[58:59], 0\n" \
    "v_mov_b64 v[60:61], 0\n" \
    "v_mov_b64 v[62:63], 0\n" \
    "v_mov_b64 v[64:65], 0\n" \
    "v_mov_b64 v[66:67], 0\n" \
    "v_mov_b64 v[68:69], 0\n" \
    "v_mov_b64 v[70:71], 0\n" \
    "v_add_u32 v108, s74, v127\n" \
    "v_xor_b32 v24, 0x400, v108\n" \
    "v_xor_b32 v28, 0x800, v108\n" \
    "v_xor_b32 v32, 0xc00, v108\n" \
    "v_and_b32 v22, 0x200, v127\n" \
    "s_and_b32 s76, s26, 0x800\n" \
    "v_mad_u32_u24 v22, v22, 3, v127\n" \
    "v_xor_b32 v22, s76, v22\n" \
    "s_waitcnt vmcnt(7)\n" \
    "v_cmp_ne_u16 vcc, s91, v99\n" \
    "v_readfirstlane_b32 s80, v100\n" \
    "s_max_i32 s38, s38, s80\n" \
    "s_cbranch_vccnz 15f\n" \
    "10:\n" \
    "v_subrev_u32 v20, s29, v98\n" \
    "s_bitcmp1_b32 s30, 15\n" \
    "v_min_i32 v20, 0x4000, v20\n" \
    "v_max_i32 v20, 0xffffc000, v20\n" \
    "s_cbranch_scc1 18f\n" \
    "19:\n" \
    "v_readlane_b32 s79, v20, s83\n" \
    "v_max_i32_dpp v20, v20, v20 row_shr:1 row_mask:0xf bank_mask:0xf\n" \
    "s_mul_i32 s49, s45, s25\n" \
    "s_lshr_b32 s49, s49, 12\n" \
    "v_max_i32_dpp v20, v20, v20 row_shr:2 row_mask:0xf bank_mask:0xf\n" \
    "s_sub_i32 s49, s47, s49\n" \
    "s_mul_hi_u32 s47, s47, s41\n" \
    "v_max_i32_dpp v20, v20, v20 row_shr:4 row_mask:0xf bank_mask:0xf\n" \
    "s_sub_i32 s47, s45, s47\n" \
    "s_lshl_b32 s47, s47, 19\n" \
    "v_max_i32_dpp v20, v20, v20 row_shr:8 row_mask:0xf bank_mask:0xf\n" \
    "s_cmp_lt_u32 s45, 3000\n" \
    "s_cselect_b32 s49, s49, 0\n" \
    "v_max_i32_dpp v20, v20, v20 row_bcast:15 row_mask:0xa bank_mask:0xf\n" \
    "s_cselect_b32 s47, s47, 0\n" \
    "s_lshl1_add_u32 s25, s49, s25\n" \
    "v_max_i32_dpp v20, v20, v20 row_bcast:31 row_mask:0xc bank_mask:0xf\n" \
    "s_add_i32 s41, s41, s47\n" \
    "s_add_i32 s76, s42, s28\n" \
    "s_add_i32 s79, s79, s76\n" \
    "v_readlane_b32 s37, v20, 63\n" \
    "s_add_i32 s37, s37, s76\n" \
    "s_cmp_lg_u32 s94, 0\n" \
    "s_cselect_b32 s36, s36, s91\n" \
    "s_cbranch_scc0 12b\n" \
    "s_branch 9f\n" \
    ".Lcold:\n" \
    "5:\n" \
    "v_mov_b32 v107, 0\n" \
    "v_max3_u32 v107, v107, v0, v1\n" \
    "v_max3_u32 v107, v107, v2, v3\n" \
    "v_max3_u32 v107, v107, v4, v5\n" \
    "v_max3_u32 v107, v107, v6, v7\n" \
    "v_max3_u32 v107, v107, v8, v9\n" \
    "v_max3_u32 v107, v107, v10, v11\n" \
    "v_max3_u32 v107, v107, v12, v13\n" \
    "v_max3_u32 v107, v107, v14, v15\n" \
    "v_max3_u32 v107, v107, v16, v17\n" \
    "v_max3_u32 v107, v107, v18, v19\n" \
    "v_max3_u32 v107, v107, v24, v25\n" \
    "v_max3_u32 v107, v107, v26, v27\n" \
    "v_max3_u32 v107, v107, v28, v29\n" \
    "v_max3_u32 v107, v107, v30, v31\n" \
    "v_max3_u32 v107, v107, v32, v33\n" \
    "v_max3_u32 v107, v107, v34, v35\n" \
    "v_max3_u32 v107, v107, v36, v37\n" \
    "v_max3_u32 v107, v107, v38, v39\n" \
    "v_cmp_eq_u32 vcc, -1, v107\n" \
    "s_nop 4\n" \
    "s_cbranch_vccz 6b\n" \
    "s_cmp_eq_u32 s36, 0\n" \
    "s_cbranch_scc1 6b\n" \
    "s_add_i32 s39, s39, -1\n" \
    "s_cmp_lt_i32 s39, 0\n" \
    "s_cbranch_scc1 6b\n" \
    "s_add_i32 s30, s30, 1\n" \
    "s_bitset1_b32 s30, 15\n" \
    "s_and_b32 s80, s31, 0xff\n" \
    "s_add_i32 s42, s42, s80\n" \
    "s_bfe_u32 s80, s31, 0x80008\n" \
    "s_add_i32 s40, s40, s80\n" \
    "s_waitcnt lgkmcnt(0)\n" \
    "s_mov_b32 s37, s29\n" \
    "s_nop 7\n" \
    "s_barrier\n" \
    "s_branch 0b\n" \
    "15:\n" \
    "s_add_i32 s30, s30, 0x10000\n" \
    "s_add_i32 s39, s39, -1\n" \
    "s_cmp_lt_i32 s39, 0\n" \
    "s_cbranch_scc1 10b\n" \
    "s_sleep 9\n" \
    "buffer_load_dwordx2 v[98:99], v113, %[rs], s78 offen sc1\n" \
    "s_waitcnt vmcnt(0)\n" \
    "v_cmp_ne_u16 vcc, s91, v99\n" \
    "s_nop 1\n" \
    "s_cbranch_vccnz 15b\n" \
    "s_memtime s[88:89]\n" \
    "s_memrealtime s[44:45]\n" \
    "s_nop 1\n" \
    "s_memrealtime s[46:47]\n" \
    "s_nop 1\n" \
    "s_memrealtime s[48:49]\n" \
    "s_nop 1\n" \
    "s_memrealtime s[50:51]\n" \
    "s_nop 1\n" \
    "s_memrealtime s[52:53]\n" \
    "s_nop 1\n" \
    "s_memrealtime s[54:55]\n" \
    "s_nop 1\n" \
    "s_memrealtime s[56:57]\n" \
    "s_nop 1\n" \
    "s_memrealtime vcc\n" \
    "s_waitcnt lgkmcnt(0)\n" \
    "s_add_i32 s44, s44, s46\n" \
    "s_add_i32 s48, s48, s50\n" \
    "s_add_i32 s52, s52, s54\n" \
    "s_add_i32 s44, s44, s48\n" \
    "s_add_i32 s52, s52, s56\n" \
    "s_add_i32 s44, s44, s52\n" \
    "s_add_i32 s29, s44, vcc_lo\n" \
    "s_mov_b32 s24, s88\n" \
    "s_mov_b32 s45, -1\n" \
    "s_branch 10b\n" \
    "18:\n" \
    "v_cmp_gt_i32 vcc, -320, v20\n" \
    "s_bitset0_b32 s30, 15\n" \
    "s_lshr_b32 s80, s31, 16\n" \
    "s_cmp_lt_u32 s36, 4\n" \
    "s_cselect_b32 s80, 0, s80\n" \
    "s_min_u32 s76, s80, 1\n" \
    "s_lshl_b32 s76, s76, 16\n" \
    "s_sub_i32 s31, s31, s76\n" \
    "s_mul_i32 s76, s80, 768\n" \
    "s_cmp_eq_u64 vcc, 0\n" \
    "s_cselect_b32 s80, 0, s80\n" \
    "s_cselect_b32 s76, s76, 0\n" \
    "s_add_i32 s33, s33, s80\n" \
    "s_add_i32 s32, s32, s76\n" \
    "s_branch 19b\n" \
    "40:\n" \
    "s_waitcnt vmcnt(2)\n" \
    "v_readfirstlane_b32 s80, v100\n" \
    "s_max_i32 s38, s38, s80\n" \
    "s_cmp_lt_i32 s38, s91\n" \
    "s_cbranch_scc0 41b\n" \
    "s_mov_b32 s94, 8\n" \
    "v_mov_b32 v18, 0x7ffffff0\n" \
    "v_mov_b32 v19, 0x7ffffff0\n" \
    "s_branch 41b\n" \
    "20:\n" \
    "s_mov_b32 s91, 63\n" \
    "s_mov_b32 s78, 0x1a8000\n" \
    "buffer_load_dwordx2 v[98:99], v113, %[rs], s78 offen sc1\n" \
    "buffer_load_dword v100, off, %[rw], s62 sc1\n" \
    "s_waitcnt vmcnt(0)\n" \
    "16:\n" \
    "v_and_b32 v112, 0x300, v127\n" \
    "v_add_u32 v112, v112, v127\n" \
    "s_add_i32 s44, %[qb], 0\n" \
    "s_add_i32 s45, s44, 4096\n" \
    "buffer_load_dwordx4 v[136:139], v112, %[rw], s44 offen offset:0 sc1\n" \
    "v_lshrrev_b32 v103, 1, v127\n" \
    "s_lshr_b32 s76, s74, 1\n" \
    "v_add_u32 v101, s76, v103\n" \
    "ds_write_b64 v101, v[96:97] offset:9216\n" \
    "ds_read_b128 v[0:3], v127 offset:16384\n" \
    "v_lshrrev_b32 v42, 8, v127\n" \
    "v_bfe_u32 v40, v127, 4, 4\n" \
    "ds_read_b128 v[4:7], v127 offset:17408\n" \
    "v_and_b32 v41, 2, v42\n" \
    "v_add_u32 v43, v41, v42\n" \
    "ds_read_b128 v[8:11], v127 offset:18432\n" \
    "s_lshl_b32 s76, s60, 4\n" \
    "s_and_b32 s76, s76, 16\n" \
    "v_add_u32 v40, s76, v40\n" \
    "v_mad_i32_i24 v40, v43, -4, v40\n" \
    "buffer_load_dwordx4 v[140:143], v112, %[rw], s44 offen offset:2048 sc1\n" \
    "ds_read_b128 v[12:15], v127 offset:19456\n" \
    "v_mov_b32 v43, 0xff61b1e6\n" \
    "v_cmp_gt_i32 vcc, 0, v40\n" \
    "ds_read_b128 v[16:19], v127 offset:20480\n" \
    "v_cndmask_b32 v88, 0, v43, vcc\n" \
    "v_cmp_gt_i32 vcc, 1, v40\n" \
    "ds_read_b128 v[20:23], v127 offset:21504\n" \
    "v_cndmask_b32 v89, 0, v43, vcc\n" \
    "v_cmp_gt_i32 vcc, 2, v40\n" \
    "ds_read_b128 v[24:27], v127 offset:22528\n" \
    "v_cndmask_b32 v90, 0, v43, vcc\n" \
    "v_cmp_gt_i32 vcc, 3, v40\n" \
    "buffer_load_dwordx4 v[144:147], v112, %[rw], s45 offen offset:0 sc1\n" \
    "ds_read_b128 v[28:31], v127 offset:23552\n" \
    "v_cndmask_b32 v91, 0, v43, vcc\n" \
    "v_cmp_gt_i32 vcc, 8, v40\n" \
    "ds_read_b128 v[32:35], v131 offset:0\n" \
    "v_cndmask_b32 v92, 0, v43, vcc\n" \
    "v_cmp_gt_i32 vcc, 9, v40\n" \
    "ds_read_b128 v[36:39], v131 offset:128\n" \
    "v_cndmask_b32 v93, 0, v43, vcc\n" \
    "v_cmp_gt_i32 vcc, 10, v40\n" \
    "ds_read_b128 v[56:59], v127 offset:24576\n" \
    "v_cndmask_b32 v94, 0, v43, vcc\n" \
    "v_cmp_gt_i32 vcc, 11, v40\n" \
    "buffer_load_dwordx4 v[148:151], v112, %[rw], s45 offen offset:2048 sc1\n" \
    "ds_read_b128 v[60:63], v127 offset:25600\n" \
    "v_cndmask_b32 v95, 0, v43, vcc\n" \
    "v_mad_u32_u24 v44, v41, 3, v42\n" \
    "ds_read_b128 v[64:67], v127 offset:26624\n" \
    "v_bfe_u32 v45, v127, 6, 1\n" \
    "v_bfe_u32 v46, v127, 4, 2\n" \
    "s_waitcnt lgkmcnt(8)\n" \
    "ds_read_b128 v[68:71], v127 offset:27648\n" \
    "v_lshl_add_u32 v44, v46, 1, v44\n" \
    "v_bfe_u32 v46, v127, 7, 1\n" \
    "ds_read_b128 a[120:123], v127 offset:28672\n" \
    "v_lshl_add_u32 v44, v44, 1, v46\n" \
    "v_mul_u32_u24 v44, 4352, v44\n" \
    "ds_read_b128 a[124:127], v127 offset:29696\n" \
    "v_lshl_add_u32 v44, v45, 1, v44\n" \
    "v_lshlrev_b32 v45, 2, v45\n" \
    "ds_read_b128 v[80:83], v127 offset:30720\n" \
    "s_lshr_b32 s79, %[u], 4\n" \
    "s_mul_i32 s79, s79, 32 * 4352\n" \
    "s_and_b32 s94, %[u], 15\n" \
    "s_lshl2_add_u32 s79, s94, s79\n" \
    "s_add_i32 s79, s79, 0x3e00fc0\n" \
    "s_lshl3_add_u32 s94, s94, 0x4065f80\n" \
    "s_lshr_b32 s80, s60, 1\n" \
    "s_mul_i32 s76, s80, 49152\n" \
    "s_add_i32 s76, s76, 98304\n" \
    "v_add_u32 v128, s76, v127\n" \
    "v_xor_b32 v130, 0x38000, v128\n" \
    "ds_read_b128 v[84:87], v127 offset:31744\n" \
    "s_lshl_b32 s76, s80, 12\n" \
    "s_add_i32 s76, s76, 83968\n" \
    "v_add_u32 v132, s76, v127\n" \
    "s_add_i32 s92, s64, 16515072\n" \
    "s_add_i32 s93, s92, 0x1000000\n" \
    "s_mov_b32 m0, s95\n" \
    "v_and_b32 v97, 0x380, v127\n" \
    "v_add_u32 v97, v97, v127\n" \
    "s_waitcnt vmcnt(6)\n" \
    "v_readfirstlane_b32 s80, v100\n" \
    "v_cmp_ne_u16 vcc, s91, v99\n" \
    "s_max_i32 s38, s38, s80\n" \
    "s_cmp_lt_i32 s38, s91\n" \
    "s_cbranch_scc1 21f\n" \
    "22:\n" \
    "s_cbranch_vccnz 23f\n" \
    "24:\n" \
    "buffer_load_ushort v113, v44, %[rw], s79 offen sc1\n" \
    "buffer_load_dword v114, v45, %[rw], s94 offen sc1\n" \
    "v_subrev_u32 v96, s29, v98\n" \
    "v_min_i32 v96, 0x4000, v96\n" \
    "v_max_i32 v96, 0, v96\n" \
    "s_waitcnt lgkmcnt(0)\n" \
    "s_barrier\n" \
    "v_max_i32_dpp v96, v96, v96 row_shr:1 row_mask:0xf bank_mask:0xf\n" \
    "ds_read_b64 v[52:53], v103 offset:9216\n" \
    "s_and_b32 s75, s91, 3\n" \
    "v_max_i32_dpp v96, v96, v96 row_shr:2 row_mask:0xf bank_mask:0xf\n" \
    "ds_read_b64 v[54:55], v103 offset:9728\n" \
    "s_lshl_b32 s75, s75, 16\n" \
    "v_max_i32_dpp v96, v96, v96 row_shr:4 row_mask:0xf bank_mask:0xf\n" \
    "ds_read_b64 v[100:101], v103 offset:10240\n" \
    "s_add_i32 s69, s75, s26\n" \
    "v_max_i32_dpp v96, v96, v96 row_shr:8 row_mask:0xf bank_mask:0xf\n" \
    "ds_read_b64 v[102:103], v103 offset:10752\n" \
    "s_xor_b32 s70, s69, 2048\n" \
    "v_max_i32_dpp v96, v96, v96 row_bcast:15 row_mask:0xa bank_mask:0xf\n" \
    "ds_read_b128 a[0:3], v128 offset:0\n" \
    "s_andn2_b32 s71, s69, 0x1fff\n" \
    "v_max_i32_dpp v96, v96, v96 row_bcast:31 row_mask:0xc bank_mask:0xf\n" \
    "ds_read_b128 a[16:19], v128 offset:2048\n" \
    "s_add_i32 s71, s71, 32768\n" \
    "v_readlane_b32 s90, v96, 63\n" \
    "s_add_i32 s90, s90, s42\n" \
    "s_mul_i32 s90, s90, s25\n" \
    "s_ashr_i32 s90, s90, 12\n" \
    "s_add_i32 s90, s90, s24\n" \
    "buffer_load_dwordx4 v97, %[rw], s92 offen sc1 lds\n" \
    "s_add_i32 m0, s95, 896\n" \
    "v_mov_b32 v99, 0\n" \
    "buffer_load_dwordx4 v97, %[rw], s92 offen offset:128 sc1 lds\n" \
    "s_add_i32 m0, s95, 8192\n" \
    "v_mov_b32 v96, 0\n" \
    "25:\n" \
    "v_mov_b64 v[48:49], 0\n" \
    "v_mov_b64 v[50:51], 0\n" \
    "s_waitcnt vmcnt(7)\n" \
    "v_mfma_f32_16x16x32_bf16 v[40:43], v[0:3], v[136:139], v[88:91]\n" \
    "ds_read_b128 a[4:7], v128 offset:1024\n" \
    "v_max3_u32 v98, v136, v137, v136\n" \
    "v_mfma_f32_16x16x32_bf16 v[44:47], v[4:7], v[136:139], v[92:95]\n" \
    "ds_read_b128 a[20:23], v128 offset:3072\n" \
    "v_max3_u32 v98, v138, v139, v98\n" \
    "s_waitcnt vmcnt(6)\n" \
    "v_mfma_f32_16x16x32_bf16 v[40:43], v[8:11], v[140:143], v[40:43]\n" \
    "ds_read_b128 a[8:11], v130 offset:0\n" \
    "v_max3_u32 v98, v140, v141, v98\n" \
    "v_mfma_f32_16x16x32_bf16 v[44:47], v[12:15], v[140:143], v[44:47]\n" \
    "ds_read_b128 a[24:27], v130 offset:2048\n" \
    "v_max3_u32 v98, v142, v143, v98\n" \
    "v_smfmac_f32_16x16x64_bf16 v[48:51], v[32:35], v[136:143], v124\n" \
    "ds_read_b128 a[12:15], v130 offset:1024\n" \
    "s_waitcnt vmcnt(5)\n" \
    "v_mfma_f32_16x16x32_bf16 v[40:43], v[16:19], v[144:147], v[40:43]\n" \
    "ds_read_b128 a[28:31], v130 offset:3072\n" \
    "v_max3_u32 v98, v144, v145, v98\n" \
    "v_mfma_f32_16x16x32_bf16 v[44:47], v[20:23], v[144:147], v[44:47]\n" \
    "ds_read_b128 a[32:35], v128 offset:4096\n" \
    "v_max3_u32 v98, v146, v147, v98\n" \
    "s_waitcnt vmcnt(4)\n" \
    "v_smfmac_f32_16x16x64_bf16 v[48:51], v[36:39], v[144:151], v124\n" \
    "ds_read_b128 a[48:51], v128 offset:6144\n" \
    "v_mfma_f32_16x16x32_bf16 v[40:43], v[24:27], v[148:151], v[40:43]\n" \
    "ds_read_b128 a[36:39], v128 offset:5120\n" \
    "v_max3_u32 v98, v148, v149, v98\n" \
    "v_max3_u32 v98, v150, v151, v98\n" \
    "v_mfma_f32_16x16x32_bf16 v[44:47], v[28:31], v[148:151], v[44:47]\n" \
    "v_cmp_eq_u32 vcc, -1, v98\n" \
    "s_add_i32 s44, %[qb], 262144\n" \
    "s_add_i32 s45, s44, 4096\n" \
    "s_waitcnt lgkmcnt(3)\n" \
    "v_add_f32 v48, v48, v50\n" \
    "v_add_f32 v49, v49, v51\n" \
    "s_nop 0\n" \
    "s_cbranch_vccnz 26f\n" \
    "27:\n" \
    "v_max3_f32 v0, v40, v41, v42\n" \
    "v_max3_f32 v1, v43, v44, v45\n" \
    "v_max3_f32 v0, v0, v46, v47\n" \
    "v_max3_f32 v1, v1, v48, v49\n" \
    "v_max_f32 v0, v0, v1\n" \
    "v_max_f32 v1, v0, v1\n" \
    "buffer_load_dwordx4 v112, %[rw], s93 offen sc1 lds\n" \
    "s_add_i32 m0, s95, 8960\n" \
    "s_nop 0\n" \
    "v_permlane16_swap_b32 v0, v1\n" \
    "v_max_f32 v0, v0, v1\n" \
    "v_max_f32 v1, v0, v1\n" \
    "buffer_load_dwordx4 v112, %[rw], s93 offen offset:256 sc1 lds\n" \
    "s_nop 0\n" \
    "v_permlane32_swap_b32 v0, v1\n" \
    "v_max_f32 v0, v0, v1\n" \
    "s_nop 0\n" \
    "v_sub_f32 v40, v40, v0\n" \
    "v_sub_f32 v41, v41, v0\n" \
    "ds_read_b128 v[136:139], v132 offset:0\n" \
    "v_exp_f32 v40, v40\n" \
    "v_exp_f32 v41, v41\n" \
    "v_sub_f32 v42, v42, v0\n" \
    "ds_read_b128 v[140:143], v132 offset:1024\n" \
    "v_sub_f32 v43, v43, v0\n" \
    "v_exp_f32 v42, v42\n" \
    "v_exp_f32 v43, v43\n" \
    "ds_read_b128 v[144:147], v132 offset:8192\n" \
    "v_cvt_pk_bf16_f32 v32, v40, v41\n" \
    "v_sub_f32 v44, v44, v0\n" \
    "v_sub_f32 v45, v45, v0\n" \
    "ds_read_b128 v[148:151], v132 offset:9216\n" \
    "v_exp_f32 v44, v44\n" \
    "v_exp_f32 v45, v45\n" \
    "v_cvt_pk_bf16_f32 v33, v42, v43\n" \
    "ds_read_b128 a[52:55], v128 offset:7168\n" \
    "v_sub_f32 v46, v46, v0\n" \
    "v_sub_f32 v47, v47, v0\n" \
    "v_exp_f32 v46, v46\n" \
    "s_memtime vcc\n" \
    "v_exp_f32 v47, v47\n" \
    "v_cvt_pk_bf16_f32 v34, v44, v45\n" \
    "v_sub_f32 v48, v48, v0\n" \
    "v_sub_f32 v49, v49, v0\n" \
    "v_exp_f32 v48, v48\n" \
    "v_exp_f32 v49, v49\n" \
    "v_cvt_pk_bf16_f32 v35, v46, v47\n" \
    "v_cvt_pk_bf16_f32 v98, v48, v49\n" \
    "v_cvt_pk_bf16_f32 v97, v48, v49\n" \
    "s_waitcnt lgkmcnt(0)\n" \
    "v_mfma_f32_16x16x32_bf16 v[36:39], v[120:123], v[32:35], 0\n" \
    "s_sub_i32 s75, s90, vcc_lo\n" \
    "s_cmp_gt_i32 s75, 100\n" \
    "v_mfma_f32_16x16x32_bf16 v[0:3], v[56:59], v[32:35], 0\n" \
    "s_cbranch_scc1 30f\n" \
    "31:\n" \
    "ds_read_b128 a[40:43], v130 offset:4096\n" \
    "v_and_b32 v44, 0x200, v127\n" \
    "s_and_b32 s80, s26, 0x800\n" \
    "v_mad_u32_u24 v44, v44, 3, v127\n" \
    "v_mfma_f32_16x16x32_bf16 v[4:7], v[60:63], v[32:35], 0\n" \
    "s_mov_b32 m0, s74\n" \
    "s_add_i32 s76, s74, 4096\n" \
    "buffer_load_dwordx4 v125, %[rs], s69 offen sc1 lds\n" \
    "s_mov_b32 m0, s76\n" \
    "v_mfma_f32_16x16x32_bf16 v[8:11], v[64:67], v[32:35], 0\n" \
    "ds_read_b128 a[56:59], v130 offset:6144\n" \
    "v_xor_b32 v44, s80, v44\n" \
    "v_bfe_u32 v40, v127, 6, 2\n" \
    "v_mfma_f32_16x16x32_bf16 v[12:15], v[68:71], v[32:35], 0\n" \
    "buffer_load_dwordx4 v125, %[rs], s70 offen sc1 lds\n" \
    "s_movk_i32 m0, 0x2000\n" \
    "v_mfma_f32_16x16x32_bf16 v[16:19], a[120:123], v[32:35], 0\n" \
    "ds_read_b128 a[44:47], v130 offset:5120\n" \
    "v_bfe_u32 v41, v127, 5, 1\n" \
    "v_bfe_u32 v42, v127, 4, 1\n" \
    "v_mfma_f32_16x16x32_bf16 v[20:23], a[124:127], v[32:35], 0\n" \
    "s_mov_b32 exec_lo, s27\n" \
    "s_mov_b32 exec_hi, s27\n" \
    "buffer_load_dwordx4 v44, %[rs], s71 offen sc1 lds\n" \
    "s_mov_b64 exec, -1\n" \
    "v_mfma_f32_16x16x32_bf16 v[24:27], v[80:83], v[32:35], 0\n" \
    "ds_read_b128 a[60:63], v130 offset:7168\n" \
    "v_lshrrev_b32 v43, 8, v127\n" \
    "v_lshlrev_b32 v134, 14, v40\n" \
    "v_mfma_f32_16x16x32_bf16 v[28:31], v[84:87], v[32:35], 0\n" \
    "ds_read_b128 a[64:67], v128 offset:8192\n" \
    "v_lshl_add_u32 v134, v41, 12, v134\n" \
    "v_lshl_add_u32 v134, v42, 6, v134\n" \
    "v_mfma_f32_16x16x16_bf16 v[36:39], v[120:121], v[98:99], v[36:39]\n" \
    "ds_read_b128 a[80:83], v128 offset:10240\n" \
    "v_lshl_add_u32 v134, v43, 4, v134\n" \
    "v_and_b32 v41, 2, v40\n" \
    "v_mfma_f32_16x16x16_bf16 v[0:3], v[52:53], v[98:99], v[0:3]\n" \
    "buffer_load_dwordx4 v[72:75], v112, %[rw], s44 offen offset:0 sc1\n" \
    "v_mfma_f32_16x16x16_bf16 v[4:7], v[52:53], v[96:97], v[4:7]\n" \
    "ds_read_b128 a[68:71], v128 offset:9216\n" \
    "v_add_lshl_u32 v135, v40, v41, 12\n" \
    "v_bfe_u32 v42, v127, 4, 2\n" \
    "v_mfma_f32_16x16x16_bf16 v[8:11], v[54:55], v[98:99], v[8:11]\n" \
    "buffer_load_dwordx4 v[76:79], v112, %[rw], s44 offen offset:2048 sc1\n" \
    "v_mfma_f32_16x16x16_bf16 v[12:15], v[54:55], v[96:97], v[12:15]\n" \
    "ds_read_b128 a[84:87], v128 offset:11264\n" \
    "v_lshl_add_u32 v135, v42, 5, v135\n" \
    "v_lshl_add_u32 v135, v43, 3, v135\n" \
    "v_mfma_f32_16x16x16_bf16 v[16:19], v[100:101], v[98:99], v[16:19]\n" \
    "buffer_load_dwordx4 v[104:107], v112, %[rw], s45 offen offset:0 sc1\n" \
    "v_mfma_f32_16x16x16_bf16 v[20:23], v[100:101], v[96:97], v[20:23]\n" \
    "ds_read_b128 a[72:75], v130 offset:8192\n" \
    "v_mfma_f32_16x16x16_bf16 v[24:27], v[102:103], v[98:99], v[24:27]\n" \
    "buffer_load_dwordx4 v[108:111], v112, %[rw], s45 offen offset:2048 sc1\n" \
    "v_mfma_f32_16x16x16_bf16 v[28:31], v[102:103], v[96:97], v[28:31]\n" \
    "ds_read_b128 a[88:91], v130 offset:10240\n" \
    "v_cvt_pk_bf16_f32 v40, v0, v4\n" \
    "v_cvt_pk_bf16_f32 v41, v1, v5\n" \
    "v_cvt_pk_bf16_f32 v42, v2, v6\n" \
    "v_cvt_pk_bf16_f32 v43, v3, v7\n" \
    "v_cvt_pk_bf16_f32 v44, v8, v12\n" \
    "v_cvt_pk_bf16_f32 v45, v9, v13\n" \
    "v_mfma_f32_16x16x32_bf16 v[64:67], v[136:139], v[40:43], 0\n" \
    "s_mov_b32 s46, %[tp]\n" \
    "ds_read_b128 a[76:79], v130 offset:9216\n" \
    "v_cvt_pk_bf16_f32 v46, v10, v14\n" \
    "v_cvt_pk_bf16_f32 v47, v11, v15\n" \
    "v_mfma_f32_16x16x32_bf16 v[56:59], a[0:3], v[40:43], 0\n" \
    "s_mov_b32 s47, %[tx]\n" \
    "ds_read_b128 a[92:95], v130 offset:11264\n" \
    "v_cvt_pk_bf16_f32 v48, v16, v20\n" \
    "v_cvt_pk_bf16_f32 v49, v17, v21\n" \
    "v_mfma_f32_16x16x32_bf16 v[64:67], v[140:143], v[44:47], v[64:67]\n" \
    "ds_read_b128 a[96:99], v128 offset:12288\n" \
    "v_cvt_pk_bf16_f32 v50, v18, v22\n" \
    "v_cvt_pk_bf16_f32 v51, v19, v23\n" \
    "v_mfma_f32_16x16x32_bf16 v[60:63], a[16:19], v[40:43], 0\n" \
    "s_waitcnt lgkmcnt(5)\n" \
    "ds_read_b128 a[112:115], v128 offset:14336\n" \
    "v_cvt_pk_bf16_f32 v52, v24, v28\n" \
    "v_cvt_pk_bf16_f32 v53, v25, v29\n" \
    "v_mfma_f32_16x16x32_bf16 v[64:67], v[144:147], v[48:51], v[64:67]\n" \
    "ds_read_b128 a[100:103], v128 offset:13312\n" \
    "v_cvt_pk_bf16_f32 v54, v26, v30\n" \
    "v_cvt_pk_bf16_f32 v55, v27, v31\n" \
    "v_mfma_f32_16x16x32_bf16 v[56:59], a[4:7], v[44:47], v[56:59]\n" \
    "ds_read_b128 a[116:119], v128 offset:15360\n" \
    "v_rcp_f32 v36, v36\n" \
    "v_mfma_f32_16x16x32_bf16 v[64:67], v[148:151], v[52:55], v[64:67]\n" \
    "ds_read_b128 a[104:107], v130 offset:12288\n" \
    "v_add_u32 v37, s74, v127\n" \
    "v_xor_b32 v38, 0x400, v37\n" \
    "v_mfma_f32_16x16x32_bf16 v[60:63], a[20:23], v[44:47], v[60:63]\n" \
    "ds_read_b128 a[120:123], v130 offset:14336\n" \
    "v_xor_b32 v39, 0x800, v37\n" \
    "v_xor_b32 v132, 0xc00, v37\n" \
    "v_mfma_f32_16x16x32_bf16 v[56:59], a[8:11], v[48:51], v[56:59]\n" \
    "ds_read_b128 a[108:111], v130 offset:13312\n" \
    "v_mul_f32 v64, v64, v36\n" \
    "v_mul_f32 v65, v65, v36\n" \
    "v_mfma_f32_16x16x32_bf16 v[60:63], a[24:27], v[48:51], v[60:63]\n" \
    "ds_read_b128 a[124:127], v130 offset:15360\n" \
    "v_mul_f32 v66, v66, v36\n" \
    "v_mul_f32 v67, v67, v36\n" \
    "v_mfma_f32_16x16x32_bf16 v[56:59], a[12:15], v[52:55], v[56:59]\n" \
    "v_cvt_pk_bf16_f32 v100, v64, v65\n" \
    "v_cvt_pk_bf16_f32 v101, v66, v67\n" \
    "v_mfma_f32_16x16x32_bf16 v[60:63], a[28:31], v[52:55], v[60:63]\n" \
    "buffer_store_dwordx2 v[100:101], v135, %[rw], s47 offen sc1\n" \
    "v_mfma_f32_16x16x32_bf16 v[64:67], a[32:35], v[40:43], 0\n" \
    "v_mul_f32 v56, v56, v36\n" \
    "v_mul_f32 v57, v57, v36\n" \
    "v_mfma_f32_16x16x32_bf16 v[68:71], a[48:51], v[40:43], 0\n" \
    "v_mul_f32 v58, v58, v36\n" \
    "v_mul_f32 v59, v59, v36\n" \
    "v_mfma_f32_16x16x32_bf16 v[64:67], a[36:39], v[44:47], v[64:67]\n" \
    "v_mul_f32 v60, v60, v36\n" \
    "v_mul_f32 v61, v61, v36\n" \
    "v_mfma_f32_16x16x32_bf16 v[68:71], a[52:55], v[44:47], v[68:71]\n" \
    "v_mul_f32 v62, v62, v36\n" \
    "v_mul_f32 v63, v63, v36\n" \
    "v_mfma_f32_16x16x32_bf16 v[64:67], a[40:43], v[48:51], v[64:67]\n" \
    "v_cvt_pk_bf16_f32 v96, v56, v57\n" \
    "v_cvt_pk_bf16_f32 v97, v58, v59\n" \
    "v_mfma_f32_16x16x32_bf16 v[68:71], a[56:59], v[48:51], v[68:71]\n" \
    "v_cvt_pk_bf16_f32 v98, v60, v61\n" \
    "v_cvt_pk_bf16_f32 v99, v62, v63\n" \
    "v_mfma_f32_16x16x32_bf16 v[64:67], a[44:47], v[52:55], v[64:67]\n" \
    "buffer_store_dwordx4 v[96:99], v134, %[rw], s46 offen sc1\n" \
    "s_add_i32 s46, s46, 0x20000\n" \
    "v_mfma_f32_16x16x32_bf16 v[68:71], a[60:63], v[52:55], v[68:71]\n" \
    "v_mov_b64 v[80:81], 0\n" \
    "v_mov_b64 v[82:83], 0\n" \
    "v_mfma_f32_16x16x32_bf16 v[56:59], a[64:67], v[40:43], 0\n" \
    "v_mul_f32 v64, v64, v36\n" \
    "v_mul_f32 v65, v65, v36\n" \
    "v_mfma_f32_16x16x32_bf16 v[60:63], a[80:83], v[40:43], 0\n" \
    "v_mul_f32 v66, v66, v36\n" \
    "v_mul_f32 v67, v67, v36\n" \
    "v_mfma_f32_16x16x32_bf16 v[56:59], a[68:71], v[44:47], v[56:59]\n" \
    "v_mul_f32 v68, v68, v36\n" \
    "v_mul_f32 v69, v69, v36\n" \
    "v_mfma_f32_16x16x32_bf16 v[60:63], a[84:87], v[44:47], v[60:63]\n" \
    "v_mul_f32 v70, v70, v36\n" \
    "v_mul_f32 v71, v71, v36\n" \
    "s_waitcnt lgkmcnt(11)\n" \
    "v_mfma_f32_16x16x32_bf16 v[56:59], a[72:75], v[48:51], v[56:59]\n" \
    "v_cvt_pk_bf16_f32 v100, v64, v65\n" \
    "v_cvt_pk_bf16_f32 v101, v66, v67\n" \
    "s_waitcnt lgkmcnt(10)\n" \
    "v_mfma_f32_16x16x32_bf16 v[60:63], a[88:91], v[48:51], v[60:63]\n" \
    "s_waitcnt vmcnt(6) lgkmcnt(0)\n" \
    "s_barrier\n" \
    "v_cvt_pk_bf16_f32 v102, v68, v69\n" \
    "v_cvt_pk_bf16_f32 v103, v70, v71\n" \
    "v_mfma_f32_16x16x32_bf16 v[56:59], a[76:79], v[52:55], v[56:59]\n" \
    "ds_read_b128 v[0:3], v37 offset:0\n" \
    "buffer_store_dwordx4 v[100:103], v134, %[rw], s46 offen sc1\n" \
    "s_add_i32 s46, s46, 0x20000\n" \
    "v_mfma_f32_16x16x32_bf16 v[60:63], a[92:95], v[52:55], v[60:63]\n" \
    "ds_read_b128 v[4:7], v38 offset:0\n" \
    "v_mov_b64 v[84:85], 0\n" \
    "v_mov_b64 v[86:87], 0\n" \
    "v_mfma_f32_16x16x32_bf16 v[64:67], a[96:99], v[40:43], 0\n" \
    "ds_read_b128 v[8:11], v39 offset:0\n" \
    "v_mul_f32 v56, v56, v36\n" \
    "v_mul_f32 v57, v57, v36\n" \
    "v_mfma_f32_16x16x32_bf16 v[68:71], a[112:115], v[40:43], 0\n" \
    "ds_read_b128 v[12:15], v132 offset:0\n" \
    "v_mul_f32 v58, v58, v36\n" \
    "v_mul_f32 v59, v59, v36\n" \
    "v_mfma_f32_16x16x32_bf16 v[64:67], a[100:103], v[44:47], v[64:67]\n" \
    "ds_read_b128 v[16:19], v37 offset:4096\n" \
    "v_mul_f32 v60, v60, v36\n" \
    "v_mul_f32 v61, v61, v36\n" \
    "v_mfma_f32_16x16x32_bf16 v[68:71], a[116:119], v[44:47], v[68:71]\n" \
    "ds_read_b128 v[20:23], v38 offset:4096\n" \
    "v_mul_f32 v62, v62, v36\n" \
    "v_mul_f32 v63, v63, v36\n" \
    "v_mfma_f32_16x16x32_bf16 v[64:67], a[104:107], v[48:51], v[64:67]\n" \
    "ds_read_b128 v[24:27], v39 offset:4096\n" \
    "v_cvt_pk_bf16_f32 v96, v56, v57\n" \
    "v_cvt_pk_bf16_f32 v97, v58, v59\n" \
    "v_mfma_f32_16x16x32_bf16 v[68:71], a[120:123], v[48:51], v[68:71]\n" \
    "ds_read_b128 v[28:31], v132 offset:4096\n" \
    "v_cvt_pk_bf16_f32 v98, v60, v61\n" \
    "v_cvt_pk_bf16_f32 v99, v62, v63\n" \
    "v_mfma_f32_16x16x32_bf16 v[64:67], a[108:111], v[52:55], v[64:67]\n" \
    "ds_read_b128 v[32:35], v126 offset:0\n" \
    "buffer_store_dwordx4 v[96:99], v134, %[rw], s46 offen sc1\n" \
    "s_add_i32 s46, s46, 0x20000\n" \
    "v_mfma_f32_16x16x32_bf16 v[68:71], a[124:127], v[52:55], v[68:71]\n" \
    "s_waitcnt lgkmcnt(3)\n" \
    "33:\n" \
    "v_smfmac_f32_16x16x64_bf16 v[80:83], v[0:3], a[128:135], v124\n" \
    "v_mul_f32 v64, v64, v36\n" \
    "v_smfmac_f32_16x16x64_bf16 v[80:83], v[4:7], a[144:151], v124\n" \
    "v_mul_f32 v65, v65, v36\n" \
    "v_smfmac_f32_16x16x64_bf16 v[80:83], v[8:11], a[160:167], v124\n" \
    "s_waitcnt lgkmcnt(0)\n" \
    "v_mul_f32 v66, v66, v36\n" \
    "v_smfmac_f32_16x16x64_bf16 v[80:83], v[12:15], a[176:183], v124\n" \
    "ds_read_b128 v[40:43], v127 offset:16384\n" \
    "v_mul_f32 v67, v67, v36\n" \
    "v_smfmac_f32_16x16x64_bf16 v[80:83], v[16:19], a[192:199], v124\n" \
    "ds_read_b128 v[44:47], v127 offset:17408\n" \
    "v_mul_f32 v68, v68, v36\n" \
    "v_smfmac_f32_16x16x64_bf16 v[80:83], v[20:23], a[208:215], v124\n" \
    "ds_read_b128 v[48:51], v127 offset:18432\n" \
    "v_mul_f32 v69, v69, v36\n" \
    "v_smfmac_f32_16x16x64_bf16 v[80:83], v[24:27], a[224:231], v124\n" \
    "ds_read_b128 v[52:55], v127 offset:19456\n" \
    "v_mul_f32 v70, v70, v36\n" \
    "v_mul_f32 v71, v71, v36\n" \
    "v_smfmac_f32_16x16x64_bf16 v[80:83], v[28:31], a[240:247], v124\n" \
    "ds_read_b128 v[56:59], v127 offset:20480\n" \
    "v_cvt_pk_bf16_f32 v100, v64, v65\n" \
    "v_cvt_pk_bf16_f32 v101, v66, v67\n" \
    "v_smfmac_f32_16x16x64_bf16 v[80:83], v[32:35], v[152:159], v124\n" \
    "ds_read_b128 v[60:63], v127 offset:21504\n" \
    "v_cvt_pk_bf16_f32 v102, v68, v69\n" \
    "v_cvt_pk_bf16_f32 v103, v70, v71\n" \
    "v_smfmac_f32_16x16x64_bf16 v[84:87], v[0:3], a[136:143], v124\n" \
    "ds_read_b128 v[64:67], v127 offset:22528\n" \
    "buffer_store_dwordx4 v[100:103], v134, %[rw], s46 offen sc1\n" \
    "v_smfmac_f32_16x16x64_bf16 v[84:87], v[4:7], a[152:159], v124\n" \
    "ds_read_b128 v[68:71], v127 offset:23552\n" \
    "v_add_f32 v80, v80, v82\n" \
    "v_add_f32 v81, v81, v83\n" \
    "v_smfmac_f32_16x16x64_bf16 v[84:87], v[8:11], a[168:175], v124\n" \
    "s_waitcnt vmcnt(5)\n" \
    "v_cmp_u_f32 vcc, v80, v81\n" \
    "v_max3_u32 v128, v72, v73, v72\n" \
    "v_smfmac_f32_16x16x64_bf16 v[84:87], v[12:15], a[184:191], v124\n" \
    "v_max3_u32 v128, v74, v75, v128\n" \
    "v_max3_u32 v128, v76, v77, v128\n" \
    "v_smfmac_f32_16x16x64_bf16 v[84:87], v[16:19], a[200:207], v124\n" \
    "s_cbranch_vccnz 32f\n" \
    "34:\n" \
    "v_max3_u32 v128, v78, v79, v128\n" \
    "v_max3_u32 v128, v104, v105, v128\n" \
    "v_smfmac_f32_16x16x64_bf16 v[84:87], v[20:23], a[216:223], v124\n" \
    "v_max3_u32 v128, v106, v107, v128\n" \
    "v_max3_u32 v128, v108, v109, v128\n" \
    "v_smfmac_f32_16x16x64_bf16 v[84:87], v[24:27], a[232:239], v124\n" \
    "v_max3_u32 v128, v110, v111, v128\n" \
    "v_cmp_eq_u32 vcc, -1, v128\n" \
    "v_smfmac_f32_16x16x64_bf16 v[84:87], v[28:31], a[248:255], v124\n" \
    "s_waitcnt lgkmcnt(0)\n" \
    "v_smfmac_f32_16x16x64_bf16 v[84:87], v[32:35], v[160:167], v124\n" \
    "s_cbranch_vccnz 28f\n" \
    "29:\n" \
    "v_mfma_f32_16x16x32_bf16 v[152:155], v[40:43], v[72:75], v[88:91]\n" \
    "ds_read_b128 a[128:131], v133 offset:0\n" \
    "v_mov_b64 v[96:97], 0\n" \
    "v_mov_b64 v[98:99], 0\n" \
    "v_mfma_f32_16x16x32_bf16 v[156:159], v[44:47], v[72:75], v[92:95]\n" \
    "ds_read_b128 a[132:135], v133 offset:1024\n" \
    "v_add_f32 v84, v84, v86\n" \
    "v_add_f32 v85, v85, v87\n" \
    "v_mfma_f32_16x16x32_bf16 v[152:155], v[48:51], v[76:79], v[152:155]\n" \
    "ds_read_b128 a[136:139], v133 offset:2048\n" \
    "v_cvt_pk_bf16_f32 v80, v80, v84\n" \
    "v_cvt_pk_bf16_f32 v81, v81, v85\n" \
    "v_mfma_f32_16x16x32_bf16 v[156:159], v[52:55], v[76:79], v[156:159]\n" \
    "ds_write2_b32 v129, v80, v81 offset0:0 offset1:144\n" \
    "v_mfma_f32_16x16x32_bf16 v[152:155], v[56:59], v[104:107], v[152:155]\n" \
    "ds_read_b128 a[140:143], v133 offset:3072\n" \
    "v_mov_b64 v[160:161], 0\n" \
    "v_mov_b64 v[162:163], 0\n" \
    "v_mfma_f32_16x16x32_bf16 v[156:159], v[60:63], v[104:107], v[156:159]\n" \
    "ds_read_b128 a[144:147], v133 offset:16384\n" \
    "v_mov_b64 v[100:101], 0\n" \
    "v_mov_b64 v[102:103], 0\n" \
    "v_mfma_f32_16x16x32_bf16 v[152:155], v[64:67], v[108:111], v[152:155]\n" \
    "ds_read_b128 a[148:151], v133 offset:17408\n" \
    "v_lshrrev_b32 v42, 1, v37\n" \
    "v_lshrrev_b32 v43, 1, v127\n" \
    "v_mfma_f32_16x16x32_bf16 v[156:159], v[68:71], v[108:111], v[156:159]\n" \
    "s_waitcnt lgkmcnt(3)\n" \
    "s_barrier\n" \
    "ds_read_b128 v[80:83], v131 offset:0\n" \
    "ds_read_b128 v[84:87], v131 offset:128\n" \
    "v_smfmac_f32_16x16x64_bf16 v[96:99], v[16:19], v[168:175], v124\n" \
    "ds_read_b128 a[152:155], v133 offset:18432\n" \
    "v_mov_b32 v61, 0\n" \
    "v_smfmac_f32_16x16x64_bf16 v[96:99], v[20:23], v[184:191], v124\n" \
    "ds_read_b128 a[156:159], v133 offset:19456\n" \
    "v_max3_f32 v40, v152, v153, v154\n" \
    "v_smfmac_f32_16x16x64_bf16 v[96:99], v[24:27], v[200:207], v124\n" \
    "ds_read_b128 a[160:163], v133 offset:32768\n" \
    "s_waitcnt lgkmcnt(3)\n" \
    "v_max3_f32 v41, v155, v156, v157\n" \
    "v_smfmac_f32_16x16x64_bf16 v[160:163], v[80:83], v[72:79], v124\n" \
    "ds_read_b128 a[164:167], v133 offset:33792\n" \
    "v_max3_f32 v40, v40, v158, v159\n" \
    "v_smfmac_f32_16x16x64_bf16 v[160:163], v[84:87], v[104:111], v124\n" \
    "ds_read_b128 a[168:171], v133 offset:34816\n" \
    "v_mov_b32 v62, 0\n" \
    "v_smfmac_f32_16x16x64_bf16 v[96:99], v[28:31], v[216:223], v124\n" \
    "ds_read_b128 a[172:175], v133 offset:35840\n" \
    "v_smfmac_f32_16x16x64_bf16 v[96:99], v[32:35], v[232:239], v124\n" \
    "ds_read_b128 a[176:179], v133 offset:49152\n" \
    "v_add_f32 v160, v160, v162\n" \
    "v_add_f32 v161, v161, v163\n" \
    "v_max3_f32 v41, v41, v160, v161\n" \
    "v_smfmac_f32_16x16x64_bf16 v[100:103], v[16:19], v[176:183], v124\n" \
    "ds_read_b128 a[180:183], v133 offset:50176\n" \
    "v_max_f32 v40, v40, v41\n" \
    "v_max_f32 v41, v40, v41\n" \
    "v_smfmac_f32_16x16x64_bf16 v[100:103], v[20:23], v[192:199], v124\n" \
    "ds_read_b128 a[192:195], v127 offset:24576\n" \
    "v_permlane16_swap_b32 v40, v41\n" \
    "v_max_f32 v40, v40, v41\n" \
    "v_max_f32 v41, v40, v41\n" \
    "v_smfmac_f32_16x16x64_bf16 v[96:99], v[0:3], a[128:135], v124\n" \
    "ds_read_b128 a[196:199], v127 offset:25600\n" \
    "v_permlane32_swap_b32 v40, v41\n" \
    "v_max_f32 v40, v40, v41\n" \
    "s_nop 0\n" \
    "v_smfmac_f32_16x16x64_bf16 v[96:99], v[4:7], a[144:151], v124\n" \
    "ds_read_b128 a[200:203], v127 offset:26624\n" \
    "s_waitcnt lgkmcnt(3)\n" \
    "v_sub_f32 v152, v152, v40\n" \
    "v_sub_f32 v153, v153, v40\n" \
    "v_exp_f32 v152, v152\n" \
    "v_smfmac_f32_16x16x64_bf16 v[96:99], v[8:11], a[160:167], v124\n" \
    "ds_read_b128 a[204:207], v127 offset:27648\n" \
    "v_exp_f32 v153, v153\n" \
    "v_sub_f32 v154, v154, v40\n" \
    "v_sub_f32 v155, v155, v40\n" \
    "v_smfmac_f32_16x16x64_bf16 v[96:99], v[12:15], a[176:183], v124\n" \
    "ds_read_b128 a[208:211], v127 offset:28672\n" \
    "v_exp_f32 v154, v154\n" \
    "v_exp_f32 v155, v155\n" \
    "v_cvt_pk_bf16_f32 v56, v152, v153\n" \
    "v_smfmac_f32_16x16x64_bf16 v[100:103], v[24:27], v[208:215], v124\n" \
    "ds_read_b128 a[212:215], v127 offset:29696\n" \
    "v_sub_f32 v156, v156, v40\n" \
    "v_sub_f32 v157, v157, v40\n" \
    "v_exp_f32 v156, v156\n" \
    "v_smfmac_f32_16x16x64_bf16 v[100:103], v[28:31], v[224:231], v124\n" \
    "ds_read_b128 a[216:219], v127 offset:30720\n" \
    "v_exp_f32 v157, v157\n" \
    "v_cvt_pk_bf16_f32 v57, v154, v155\n" \
    "v_sub_f32 v158, v158, v40\n" \
    "v_smfmac_f32_16x16x64_bf16 v[100:103], v[32:35], v[240:247], v124\n" \
    "ds_read_b128 a[220:223], v127 offset:31744\n" \
    "v_sub_f32 v159, v159, v40\n" \
    "v_exp_f32 v158, v158\n" \
    "v_exp_f32 v159, v159\n" \
    "v_smfmac_f32_16x16x64_bf16 v[100:103], v[12:15], v[248:255], v124\n" \
    "v_cvt_pk_bf16_f32 v58, v156, v157\n" \
    "v_sub_f32 v160, v160, v40\n" \
    "v_sub_f32 v161, v161, v40\n" \
    "v_smfmac_f32_16x16x64_bf16 v[100:103], v[0:3], a[136:143], v124\n" \
    "v_exp_f32 v160, v160\n" \
    "v_exp_f32 v161, v161\n" \
    "v_cvt_pk_bf16_f32 v59, v158, v159\n" \
    "v_smfmac_f32_16x16x64_bf16 v[100:103], v[4:7], a[152:159], v124\n" \
    "v_cvt_pk_bf16_f32 v60, v160, v161\n" \
    "v_cvt_pk_bf16_f32 v63, v160, v161\n" \
    "v_smfmac_f32_16x16x64_bf16 v[100:103], v[8:11], a[168:175], v124\n" \
    "s_waitcnt lgkmcnt(0)\n" \
    "v_mfma_f32_16x16x32_bf16 v[36:39], v[120:123], v[56:59], 0\n" \
    "v_add_f32 v96, v96, v98\n" \
    "v_add_f32 v97, v97, v99\n" \
    "v_mfma_f32_16x16x32_bf16 v[0:3], a[192:195], v[56:59], 0\n" \
    "v_add_f32 v100, v100, v102\n" \
    "v_add_f32 v101, v101, v103\n" \
    "v_mfma_f32_16x16x32_bf16 v[4:7], a[196:199], v[56:59], 0\n" \
    "v_cvt_pk_bf16_f32 v96, v96, v97\n" \
    "v_cvt_pk_bf16_f32 v97, v100, v101\n" \
    "v_mfma_f32_16x16x32_bf16 v[8:11], a[200:203], v[56:59], 0\n" \
    "ds_write_b64 v42, v[96:97] offset:9216\n" \
    "v_mfma_f32_16x16x32_bf16 v[12:15], a[204:207], v[56:59], 0\n" \
    "v_lshrrev_b32 v169, 8, v127\n" \
    "v_and_b32 v170, 2, v169\n" \
    "v_mfma_f32_16x16x32_bf16 v[16:19], a[208:211], v[56:59], 0\n" \
    "v_mad_u32_u24 v169, v170, 3, v169\n" \
    "v_bfe_u32 v168, v127, 6, 1\n" \
    "v_mfma_f32_16x16x32_bf16 v[20:23], a[212:215], v[56:59], 0\n" \
    "v_bfe_u32 v170, v127, 4, 2\n" \
    "v_lshl_add_u32 v169, v170, 1, v169\n" \
    "v_mfma_f32_16x16x32_bf16 v[24:27], a[216:219], v[56:59], 0\n" \
    "s_waitcnt lgkmcnt(0)\n" \
    "s_barrier\n" \
    "ds_read_b64 v[64:65], v43 offset:9216\n" \
    "ds_read_b64 v[66:67], v43 offset:9728\n" \
    "ds_read_b64 v[68:69], v43 offset:10240\n" \
    "ds_read_b64 v[70:71], v43 offset:10752\n" \
    "v_mfma_f32_16x16x32_bf16 v[28:31], a[220:223], v[56:59], 0\n" \
    "v_mul_u32_u24 v168, 1152, v168\n" \
    "v_lshl_add_u32 v168, v169, 2, v168\n" \
    "v_mfma_f32_16x16x16_bf16 v[36:39], v[120:121], v[60:61], v[36:39]\n" \
    "s_waitcnt lgkmcnt(2)\n" \
    "v_mfma_f32_16x16x16_bf16 v[0:3], v[64:65], v[60:61], v[0:3]\n" \
    "v_sub_f32 v169, 1.0, v114\n" \
    "v_lshlrev_b32 v172, 16, v113\n" \
    "v_mfma_f32_16x16x16_bf16 v[4:7], v[64:65], v[62:63], v[4:7]\n" \
    "s_waitcnt lgkmcnt(0)\n" \
    "v_mul_f32 v172, v172, v169\n" \
    "v_and_b32 v170, 16, v127\n" \
    "v_mfma_f32_16x16x16_bf16 v[8:11], v[66:67], v[60:61], v[8:11]\n" \
    "v_mov_b32 v169, 0x3f80\n" \
    "v_lshlrev_b32 v170, v170, v169\n" \
    "v_mfma_f32_16x16x16_bf16 v[12:15], v[66:67], v[62:63], v[12:15]\n" \
    "v_mov_b32_dpp v173, v172 row_ror:8 row_mask:0xf bank_mask:0xf\n" \
    "v_mov_b64 v[176:177], 0\n" \
    "v_mfma_f32_16x16x16_bf16 v[16:19], v[68:69], v[60:61], v[16:19]\n" \
    "v_mov_b64 v[178:179], 0\n" \
    "v_mov_b32_dpp v176, v170 quad_perm:[0,1,2,3] row_mask:0xf bank_mask:0x1\n" \
    "v_mfma_f32_16x16x16_bf16 v[20:23], v[68:69], v[62:63], v[20:23]\n" \
    "v_mov_b32_dpp v177, v170 quad_perm:[0,1,2,3] row_mask:0xf bank_mask:0x2\n" \
    "v_mov_b32_dpp v178, v170 quad_perm:[0,1,2,3] row_mask:0xf bank_mask:0x4\n" \
    "v_mfma_f32_16x16x16_bf16 v[24:27], v[70:71], v[60:61], v[24:27]\n" \
    "v_mov_b32_dpp v179, v170 quad_perm:[0,1,2,3] row_mask:0xf bank_mask:0x8\n" \
    "v_mfma_f32_16x16x16_bf16 v[28:31], v[70:71], v[62:63], v[28:31]\n" \
    "v_cvt_pk_bf16_f32 v40, v0, v4\n" \
    "v_cvt_pk_bf16_f32 v41, v1, v5\n" \
    "v_cvt_pk_bf16_f32 v42, v2, v6\n" \
    "v_cvt_pk_bf16_f32 v43, v3, v7\n" \
    "v_cvt_pk_bf16_f32 v44, v8, v12\n" \
    "v_cvt_pk_bf16_f32 v45, v9, v13\n" \
    "v_mfma_f32_16x16x32_bf16 v[80:83], v[136:139], v[40:43], 0\n" \
    "s_mov_b32 s46, %[tq]\n" \
    "v_cvt_pk_bf16_f32 v46, v10, v14\n" \
    "v_cvt_pk_bf16_f32 v47, v11, v15\n" \
    "v_mfma_f32_16x16x32_bf16 v[72:75], a[0:3], v[40:43], 0\n" \
    "s_add_i32 s47, %[tx], 0x40000\n" \
    "v_cvt_pk_bf16_f32 v48, v16, v20\n" \
    "v_cvt_pk_bf16_f32 v49, v17, v21\n" \
    "v_mfma_f32_16x16x32_bf16 v[80:83], v[140:143], v[44:47], v[80:83]\n" \
    "v_cvt_pk_bf16_f32 v50, v18, v22\n" \
    "v_cvt_pk_bf16_f32 v51, v19, v23\n" \
    "v_mfma_f32_16x16x32_bf16 v[76:79], a[16:19], v[40:43], 0\n" \
    "v_cvt_pk_bf16_f32 v52, v24, v28\n" \
    "v_cvt_pk_bf16_f32 v53, v25, v29\n" \
    "v_mfma_f32_16x16x32_bf16 v[80:83], v[144:147], v[48:51], v[80:83]\n" \
    "v_cvt_pk_bf16_f32 v54, v26, v30\n" \
    "v_cvt_pk_bf16_f32 v55, v27, v31\n" \
    "v_mfma_f32_16x16x32_bf16 v[72:75], a[4:7], v[44:47], v[72:75]\n" \
    "v_rcp_f32 v36, v36\n" \
    "v_mfma_f32_16x16x32_bf16 v[80:83], v[148:151], v[52:55], v[80:83]\n" \
    "s_and_b32 s94, %[u], 15\n" \
    "s_mul_i32 s94, s94, 2304\n" \
    "v_mfma_f32_16x16x32_bf16 v[76:79], a[20:23], v[44:47], v[76:79]\n" \
    "s_andn2_b32 s77, %[u], 15\n" \
    "s_lshl2_add_u32 s94, s77, s94\n" \
    "v_mfma_f32_16x16x32_bf16 v[72:75], a[8:11], v[48:51], v[72:75]\n" \
    "v_mul_f32 v80, v80, v36\n" \
    "v_mul_f32 v81, v81, v36\n" \
    "v_mfma_f32_16x16x32_bf16 v[76:79], a[24:27], v[48:51], v[76:79]\n" \
    "v_mul_f32 v82, v82, v36\n" \
    "v_mul_f32 v83, v83, v36\n" \
    "v_mfma_f32_16x16x32_bf16 v[72:75], a[12:15], v[52:55], v[72:75]\n" \
    "v_cvt_pk_bf16_f32 v100, v80, v81\n" \
    "v_cvt_pk_bf16_f32 v101, v82, v83\n" \
    "v_mfma_f32_16x16x32_bf16 v[76:79], a[28:31], v[52:55], v[76:79]\n" \
    "buffer_store_dwordx2 v[100:101], v135, %[rw], s47 offen sc1\n" \
    "v_mfma_f32_16x16x32_bf16 v[80:83], a[32:35], v[40:43], 0\n" \
    "v_mul_f32 v72, v72, v36\n" \
    "v_mul_f32 v73, v73, v36\n" \
    "v_mfma_f32_16x16x32_bf16 v[84:87], a[48:51], v[40:43], 0\n" \
    "v_mul_f32 v74, v74, v36\n" \
    "v_mul_f32 v75, v75, v36\n" \
    "v_mfma_f32_16x16x32_bf16 v[80:83], a[36:39], v[44:47], v[80:83]\n" \
    "v_mul_f32 v76, v76, v36\n" \
    "v_mul_f32 v77, v77, v36\n" \
    "v_mfma_f32_16x16x32_bf16 v[84:87], a[52:55], v[44:47], v[84:87]\n" \
    "v_mul_f32 v78, v78, v36\n" \
    "v_mul_f32 v79, v79, v36\n" \
    "v_mfma_f32_16x16x32_bf16 v[80:83], a[40:43], v[48:51], v[80:83]\n" \
    "v_cvt_pk_bf16_f32 v96, v72, v73\n" \
    "v_cvt_pk_bf16_f32 v97, v74, v75\n" \
    "v_mfma_f32_16x16x32_bf16 v[84:87], a[56:59], v[48:51], v[84:87]\n" \
    "v_cvt_pk_bf16_f32 v98, v76, v77\n" \
    "v_cvt_pk_bf16_f32 v99, v78, v79\n" \
    "v_mfma_f32_16x16x32_bf16 v[80:83], a[44:47], v[52:55], v[80:83]\n" \
    "buffer_store_dwordx4 v[96:99], v134, %[rw], s46 offen sc1\n" \
    "s_add_i32 s46, s46, 0x20000\n" \
    "v_mfma_f32_16x16x32_bf16 v[84:87], a[60:63], v[52:55], v[84:87]\n" \
    "s_add_i32 s94, s94, 2322432\n" \
    "v_mfma_f32_16x16x32_bf16 v[72:75], a[64:67], v[40:43], 0\n" \
    "v_mul_f32 v80, v80, v36\n" \
    "v_mul_f32 v81, v81, v36\n" \
    "v_mfma_f32_16x16x32_bf16 v[76:79], a[80:83], v[40:43], 0\n" \
    "v_mul_f32 v82, v82, v36\n" \
    "v_mul_f32 v83, v83, v36\n" \
    "v_mfma_f32_16x16x32_bf16 v[72:75], a[68:71], v[44:47], v[72:75]\n" \
    "v_mul_f32 v84, v84, v36\n" \
    "v_mul_f32 v85, v85, v36\n" \
    "v_mfma_f32_16x16x32_bf16 v[76:79], a[84:87], v[44:47], v[76:79]\n" \
    "v_mul_f32 v86, v86, v36\n" \
    "v_mul_f32 v87, v87, v36\n" \
    "v_mfma_f32_16x16x32_bf16 v[72:75], a[72:75], v[48:51], v[72:75]\n" \
    "v_cvt_pk_bf16_f32 v100, v80, v81\n" \
    "v_cvt_pk_bf16_f32 v101, v82, v83\n" \
    "v_mfma_f32_16x16x32_bf16 v[76:79], a[88:91], v[48:51], v[76:79]\n" \
    "v_cvt_pk_bf16_f32 v102, v84, v85\n" \
    "v_cvt_pk_bf16_f32 v103, v86, v87\n" \
    "v_mfma_f32_16x16x32_bf16 v[72:75], a[76:79], v[52:55], v[72:75]\n" \
    "buffer_store_dwordx4 v[100:103], v134, %[rw], s46 offen sc1\n" \
    "s_add_i32 s46, s46, 0x20000\n" \
    "v_mfma_f32_16x16x32_bf16 v[76:79], a[92:95], v[52:55], v[76:79]\n" \
    "v_mfma_f32_16x16x32_bf16 v[80:83], a[96:99], v[40:43], 0\n" \
    "v_mul_f32 v72, v72, v36\n" \
    "v_mul_f32 v73, v73, v36\n" \
    "v_mfma_f32_16x16x32_bf16 v[84:87], a[112:115], v[40:43], 0\n" \
    "v_mul_f32 v74, v74, v36\n" \
    "v_mul_f32 v75, v75, v36\n" \
    "v_mfma_f32_16x16x32_bf16 v[80:83], a[100:103], v[44:47], v[80:83]\n" \
    "v_mul_f32 v76, v76, v36\n" \
    "v_mul_f32 v77, v77, v36\n" \
    "v_mfma_f32_16x16x32_bf16 v[84:87], a[116:119], v[44:47], v[84:87]\n" \
    "v_mul_f32 v78, v78, v36\n" \
    "v_mul_f32 v79, v79, v36\n" \
    "v_mfma_f32_16x16x32_bf16 v[80:83], a[104:107], v[48:51], v[80:83]\n" \
    "v_cvt_pk_bf16_f32 v96, v72, v73\n" \
    "v_cvt_pk_bf16_f32 v97, v74, v75\n" \
    "v_mfma_f32_16x16x32_bf16 v[84:87], a[120:123], v[48:51], v[84:87]\n" \
    "v_cvt_pk_bf16_f32 v98, v76, v77\n" \
    "v_cvt_pk_bf16_f32 v99, v78, v79\n" \
    "v_mfma_f32_16x16x32_bf16 v[80:83], a[108:111], v[52:55], v[80:83]\n" \
    "buffer_store_dwordx4 v[96:99], v134, %[rw], s46 offen sc1\n" \
    "s_add_i32 s46, s46, 0x20000\n" \
    "v_mfma_f32_16x16x32_bf16 v[84:87], a[124:127], v[52:55], v[84:87]\n" \
    "s_memtime vcc\n" \
    "s_nop 2\n" \
    "v_mul_f32 v80, v80, v36\n" \
    "v_mul_f32 v81, v81, v36\n" \
    "v_mul_f32 v82, v82, v36\n" \
    "v_mul_f32 v83, v83, v36\n" \
    "v_mul_f32 v84, v84, v36\n" \
    "v_mul_f32 v85, v85, v36\n" \
    "v_mul_f32 v86, v86, v36\n" \
    "v_mul_f32 v87, v87, v36\n" \
    "v_cvt_pk_bf16_f32 v100, v80, v81\n" \
    "v_cvt_pk_bf16_f32 v101, v82, v83\n" \
    "v_cvt_pk_bf16_f32 v102, v84, v85\n" \
    "v_cvt_pk_bf16_f32 v103, v86, v87\n" \
    "buffer_store_dwordx4 v[100:103], v134, %[rw], s46 offen sc1\n" \
    "s_waitcnt lgkmcnt(0)\n" \
    "s_add_i32 s90, vcc_lo, %[fw]\n" \
    "35:\n" \
    "s_memtime vcc\n" \
    "s_waitcnt lgkmcnt(0)\n" \
    "s_sub_i32 s75, s90, vcc_lo\n" \
    "s_cmp_gt_i32 s75, 0\n" \
    "s_cbranch_scc1 35b\n" \
    "36:\n" \
    "buffer_load_dwordx4 v[0:3], v127, %[rw], %[ua] offen offset:0 sc1\n" \
    "buffer_load_dwordx4 v[4:7], v127, %[rw], %[ua] offen offset:1024 sc1\n" \
    "buffer_load_dwordx4 v[8:11], v127, %[rw], %[ua] offen offset:2048 sc1\n" \
    "buffer_load_dwordx4 v[12:15], v127, %[rw], %[ua] offen offset:3072 sc1\n" \
    "s_waitcnt vmcnt(3)\n" \
    "v_mfma_f32_16x16x32_bf16 v[32:35], v[176:179], v[0:3], 0\n" \
    "v_max3_u32 v40, v0, v1, v0\n" \
    "v_max3_u32 v40, v2, v3, v40\n" \
    "s_waitcnt vmcnt(2)\n" \
    "v_mfma_f32_16x16x32_bf16 v[32:35], v[176:179], v[4:7], v[32:35]\n" \
    "v_max3_u32 v40, v4, v5, v40\n" \
    "v_max3_u32 v40, v6, v7, v40\n" \
    "s_waitcnt vmcnt(1)\n" \
    "v_mfma_f32_16x16x32_bf16 v[32:35], v[176:179], v[8:11], v[32:35]\n" \
    "v_max3_u32 v40, v8, v9, v40\n" \
    "v_max3_u32 v40, v10, v11, v40\n" \
    "s_waitcnt vmcnt(0)\n" \
    "v_mfma_f32_16x16x32_bf16 v[32:35], v[176:179], v[12:15], v[32:35]\n" \
    "v_max3_u32 v40, v12, v13, v40\n" \
    "v_max3_u32 v40, v14, v15, v40\n" \
    "v_cmp_eq_u32 vcc, -1, v40\n" \
    "s_nop 3\n" \
    "s_cbranch_vccnz 38f\n" \
    "39:\n" \
    "v_add_f32_dpp v32, v32, v32 row_ror:8 row_mask:0xf bank_mask:0xf\n" \
    "v_add_f32_dpp v33, v33, v33 row_ror:8 row_mask:0xf bank_mask:0xf\n" \
    "s_mov_b32 exec_lo, 0x00ff00ff\n" \
    "s_mov_b32 exec_hi, 0x00ff00ff\n" \
    "v_fma_f32 v32, v32, v114, v172\n" \
    "v_fma_f32 v33, v33, v114, v173\n" \
    "v_cvt_pk_bf16_f32 v32, v32, v33\n" \
    "buffer_store_dword v32, v168, %[ru], s94 offen sc1\n" \
    "s_mov_b64 exec, -1\n" \
    "s_mov_b32 s36, 63\n" \
    "s_branch 9f\n" \
    "38:\n" \
    "s_add_i32 s39, s39, -1\n" \
    "s_cmp_lt_i32 s39, 0\n" \
    "s_cbranch_scc1 39b\n" \
    "s_add_i32 s30, s30, 0x10000\n" \
    "s_sleep 2\n" \
    "s_branch 36b\n" \
    "21:\n" \
    "s_add_i32 s39, s39, -1\n" \
    "s_cmp_lt_i32 s39, 0\n" \
    "s_cbranch_scc1 22b\n" \
    "buffer_load_dword v100, off, %[rw], s62 sc1\n" \
    "s_waitcnt vmcnt(0)\n" \
    "v_readfirstlane_b32 s38, v100\n" \
    "s_cmp_lt_i32 s38, s91\n" \
    "s_cbranch_scc1 21b\n" \
    "s_branch 22b\n" \
    "23:\n" \
    "s_add_i32 s39, s39, -1\n" \
    "s_cmp_lt_i32 s39, 0\n" \
    "s_cbranch_scc1 24b\n" \
    "s_sleep 4\n" \
    "buffer_load_dwordx2 v[98:99], v113, %[rs], s78 offen sc1\n" \
    "s_waitcnt vmcnt(0)\n" \
    "v_cmp_ne_u16 vcc, s91, v99\n" \
    "s_nop 1\n" \
    "s_cbranch_vccnz 23b\n" \
    "s_branch 24b\n" \
    "26:\n" \
    "s_add_i32 s39, s39, -1\n" \
    "s_cmp_lt_i32 s39, 0\n" \
    "s_cbranch_scc1 27b\n" \
    "s_sleep 4\n" \
    "s_add_i32 s44, %[qb], 0\n" \
    "s_add_i32 s45, s44, 4096\n" \
    "buffer_load_dwordx4 v[136:139], v112, %[rw], s44 offen offset:0 sc1\n" \
    "buffer_load_dwordx4 v[140:143], v112, %[rw], s44 offen offset:2048 sc1\n" \
    "buffer_load_dwordx4 v[144:147], v112, %[rw], s45 offen offset:0 sc1\n" \
    "buffer_load_dwordx4 v[148:151], v112, %[rw], s45 offen offset:2048 sc1\n" \
    "s_waitcnt vmcnt(0)\n" \
    "s_branch 25b\n" \
    "28:\n" \
    "s_add_i32 s39, s39, -1\n" \
    "s_cmp_lt_i32 s39, 0\n" \
    "s_cbranch_scc1 29b\n" \
    "s_sleep 4\n" \
    "s_add_i32 s44, %[qb], 262144\n" \
    "s_add_i32 s45, s44, 4096\n" \
    "buffer_load_dwordx4 v[72:75], v112, %[rw], s44 offen offset:0 sc1\n" \
    "buffer_load_dwordx4 v[76:79], v112, %[rw], s44 offen offset:2048 sc1\n" \
    "buffer_load_dwordx4 v[104:107], v112, %[rw], s45 offen offset:0 sc1\n" \
    "buffer_load_dwordx4 v[108:111], v112, %[rw], s45 offen offset:2048 sc1\n" \
    "s_waitcnt vmcnt(0)\n" \
    "v_max3_u32 v128, v72, v73, v72\n" \
    "v_max3_u32 v128, v74, v75, v128\n" \
    "v_max3_u32 v128, v76, v77, v128\n" \
    "v_max3_u32 v128, v78, v79, v128\n" \
    "v_max3_u32 v128, v104, v105, v128\n" \
    "v_max3_u32 v128, v106, v107, v128\n" \
    "v_max3_u32 v128, v108, v109, v128\n" \
    "v_max3_u32 v128, v110, v111, v128\n" \
    "v_cmp_eq_u32 vcc, -1, v128\n" \
    "s_nop 1\n" \
    "s_cbranch_vccnz 28b\n" \
    "s_branch 29b\n" \
    "30:\n" \
    "s_memtime vcc\n" \
    "s_waitcnt lgkmcnt(0)\n" \
    "s_sub_i32 s75, s90, vcc_lo\n" \
    "s_cmp_gt_i32 s75, 100\n" \
    "s_cbranch_scc1 30b\n" \
    "s_branch 31b\n" \
    "32:\n" \
    "v_max3_u32 v82, v0, v1, v0\n" \
    "v_max3_u32 v82, v2, v3, v82\n" \
    "v_max3_u32 v82, v4, v5, v82\n" \
    "v_max3_u32 v82, v6, v7, v82\n" \
    "v_max3_u32 v82, v8, v9, v82\n" \
    "v_max3_u32 v82, v10, v11, v82\n" \
    "v_max3_u32 v82, v12, v13, v82\n" \
    "v_max3_u32 v82, v14, v15, v82\n" \
    "v_max3_u32 v82, v16, v17, v82\n" \
    "v_max3_u32 v82, v18, v19, v82\n" \
    "v_max3_u32 v82, v20, v21, v82\n" \
    "v_max3_u32 v82, v22, v23, v82\n" \
    "v_max3_u32 v82, v24, v25, v82\n" \
    "v_max3_u32 v82, v26, v27, v82\n" \
    "v_max3_u32 v82, v28, v29, v82\n" \
    "v_max3_u32 v82, v30, v31, v82\n" \
    "v_max3_u32 v82, v32, v33, v82\n" \
    "v_max3_u32 v82, v34, v35, v82\n" \
    "v_cmp_eq_u32 vcc, -1, v82\n" \
    "s_nop 4\n" \
    "s_cbranch_vccz 34b\n" \
    "s_add_i32 s39, s39, -1\n" \
    "s_cmp_lt_i32 s39, 0\n" \
    "s_cbranch_scc1 34b\n" \
    "s_add_i32 s30, s30, 1\n" \
    "s_waitcnt lgkmcnt(0)\n" \
    "s_barrier\n" \
    "v_and_b32 v96, 0x200, v127\n" \
    "s_and_b32 s80, s26, 0x800\n" \
    "v_mad_u32_u24 v96, v96, 3, v127\n" \
    "v_xor_b32 v96, s80, v96\n" \
    "s_mov_b32 m0, s74\n" \
    "s_add_i32 s76, s74, 4096\n" \
    "buffer_load_dwordx4 v125, %[rs], s69 offen sc1 lds\n" \
    "s_mov_b32 m0, s76\n" \
    "v_mov_b64 v[80:81], 0\n" \
    "v_mov_b64 v[82:83], 0\n" \
    "buffer_load_dwordx4 v125, %[rs], s70 offen sc1 lds\n" \
    "s_movk_i32 m0, 0x2000\n" \
    "s_mov_b32 exec_lo, s27\n" \
    "s_mov_b32 exec_hi, s27\n" \
    "buffer_load_dwordx4 v96, %[rs], s71 offen sc1 lds\n" \
    "s_mov_b64 exec, -1\n" \
    "v_mov_b64 v[84:85], 0\n" \
    "v_mov_b64 v[86:87], 0\n" \
    "s_waitcnt vmcnt(0)\n" \
    "s_barrier\n" \
    "ds_read_b128 v[0:3], v37 offset:0\n" \
    "ds_read_b128 v[4:7], v38 offset:0\n" \
    "ds_read_b128 v[8:11], v39 offset:0\n" \
    "ds_read_b128 v[12:15], v132 offset:0\n" \
    "ds_read_b128 v[16:19], v37 offset:4096\n" \
    "ds_read_b128 v[20:23], v38 offset:4096\n" \
    "ds_read_b128 v[24:27], v39 offset:4096\n" \
    "ds_read_b128 v[28:31], v132 offset:4096\n" \
    "ds_read_b128 v[32:35], v126 offset:0\n" \
    "s_waitcnt lgkmcnt(8)\n" \
    "v_smfmac_f32_16x16x64_bf16 v[80:83], v[0:3], a[128:135], v124\n" \
    "s_waitcnt lgkmcnt(7)\n" \
    "v_smfmac_f32_16x16x64_bf16 v[80:83], v[4:7], a[144:151], v124\n" \
    "s_waitcnt lgkmcnt(6)\n" \
    "v_smfmac_f32_16x16x64_bf16 v[80:83], v[8:11], a[160:167], v124\n" \
    "s_waitcnt lgkmcnt(5)\n" \
    "v_smfmac_f32_16x16x64_bf16 v[80:83], v[12:15], a[176:183], v124\n" \
    "s_waitcnt lgkmcnt(4)\n" \
    "v_smfmac_f32_16x16x64_bf16 v[80:83], v[16:19], a[192:199], v124\n" \
    "s_waitcnt lgkmcnt(3)\n" \
    "v_smfmac_f32_16x16x64_bf16 v[80:83], v[20:23], a[208:215], v124\n" \
    "s_waitcnt lgkmcnt(2)\n" \
    "v_smfmac_f32_16x16x64_bf16 v[80:83], v[24:27], a[224:231], v124\n" \
    "s_waitcnt lgkmcnt(1)\n" \
    "v_smfmac_f32_16x16x64_bf16 v[80:83], v[28:31], a[240:247], v124\n" \
    "s_waitcnt lgkmcnt(0)\n" \
    "v_smfmac_f32_16x16x64_bf16 v[80:83], v[32:35], v[152:159], v124\n" \
    "v_smfmac_f32_16x16x64_bf16 v[84:87], v[0:3], a[136:143], v124\n" \
    "v_smfmac_f32_16x16x64_bf16 v[84:87], v[4:7], a[152:159], v124\n" \
    "v_smfmac_f32_16x16x64_bf16 v[84:87], v[8:11], a[168:175], v124\n" \
    "v_smfmac_f32_16x16x64_bf16 v[84:87], v[12:15], a[184:191], v124\n" \
    "v_smfmac_f32_16x16x64_bf16 v[84:87], v[16:19], a[200:207], v124\n" \
    "v_add_f32 v80, v80, v82\n" \
    "v_add_f32 v81, v81, v83\n" \
    "s_branch 32b\n" \
    "7:\n" \
    "s_mov_b32 s43, 2\n" \
    "s_nop 7\n" \
    "s_waitcnt vmcnt(0)\n" \
    "9:\n" \
    "s_mov_b32 m0, s68\n" \
    "s_add_i32 s37, s37, s29\n" \
    "s_waitcnt lgkmcnt(0)\n" \
    "v_mov_b32 v0, s36\n" \
    "v_mov_b32 v1, s37\n" \
    "v_mov_b32 v2, s38\n" \
    "v_mov_b32 v3, s39\n" \
    "v_mov_b32 v4, s40\n" \
    "v_mov_b32 v5, s41\n" \
    "v_mov_b32 v6, s42\n" \
    "v_mov_b32 v7, s43\n" \
    "v_mov_b32 v8, s28\n" \
    "v_mov_b32 v9, s29\n" \
    "v_mov_b32 v10, s30\n" \
    "v_mov_b32 v11, s31\n" \
    "v_mov_b32 v12, s32\n" \
    "v_mov_b32 v13, s33\n" \
    "v_mov_b32 v14, s34\n" \
    "v_mov_b32 v15, s35\n" \
    "v_mov_b32 v16, s24\n" \
    "v_mov_b32 v17, s25\n" \
    "v_mov_b32 v18, s26\n" \
    "v_mov_b32 v19, s27\n" \
    "v_mov_b32 v20, s84\n" \
    "v_mov_b32 v21, s86\n" \
    "v_mov_b32 v22, s88\n" \
    "v_mov_b32 v23, s84\n" \
    "v_mov_b32 v24, s60\n" \
    "v_mov_b32 v25, s61\n" \
    "v_mov_b32 v26, s62\n" \
    "v_mov_b32 v27, s63\n" \
    "v_mov_b32 v28, s64\n" \
    "v_mov_b32 v29, s65\n" \
    "v_mov_b32 v30, s66\n" \
    "v_mov_b32 v31, s67\n" \
    ".Lstep_end:\n" \
    ""
static_assert(2 * PART_STEP == 32768 && 2 * OFF_CAN == 1703936 && F_ERR == 2 * OFF_CAN + 65536 && MEMB_STRIDE == 32768 && OFF_VF - OFF_KF == 2048 * 4096 * 2 && OFF_BO == 65011712 && OFF_GT == 67518464 && BO_ROW == 4352, "constants of the step block");
#define STEP_CLOBBER "v32", "v33", "v34", "v35", "v36", "v37", "v38", "v39", "v40", "v41", "v42", "v43", "v44", "v45", "v46", "v47", "v48", "v49", "v50", "v51", "v52", "v53", "v54", "v55", "v56", "v57", "v58", "v59", "v60", "v61", "v62", "v63", "v64", "v65", "v66", "v67", "v68", "v69", "v70", "v71", "v72", "v73", "v74", "v75", "v76", "v77", "v78", "v79", "v80", "v81", "v82", "v83", "v84", "v85", "v86", "v87", "v88", "v89", "v90", "v91", "v92", "v93", "v94", "v95", "v98", "v99", "v100", "v101", "v102", "v103", "v104", "v105", "v106", "v107", "v108", "v109", "v110", "v111", "v112", "v113", "v114", "s24", "s25", "s26", "s27", "s28", "s29", "s30", "s31", "s32", "s33", "s34", "s35", "s36", "s37", "s38", "s39", "s40", "s41", "s42", "s43", "s44", "s45", "s46", "s47", "s48", "s49", "s50", "s51", "s52", "s53", "s54", "s55", "s56", "s57", "s58", "s59", "s60", "s61", "s62", "s63", "s64", "s65", "s66", "s67", "s68", "s69", "s70", "s71", "s72", "s73", "s74", "s75", "s76", "s77", "s78", "s79", "s80", "s81", "s82", "s83", "s84", "s85", "s86", "s87", "s88", "s89", "s90", "s91", "s92", "s93", "s94", "s95", "s96", "s97", "vcc", "scc", "memory"
#define OUT16_ASM \
    "s_mov_b32 exec_lo, %[tm]\n" \
    "s_mov_b32 exec_hi, 0\n" \
    "buffer_load_dword v79, %[tv], %[rw], %[so] offen sc1\n" \
    "s_mov_b64 exec, -1\n" \
    "s_add_i32 s44, %[so], 4096\n" \
    "s_add_i32 s45, %[so], 8192\n" \
    "s_add_i32 s46, %[so], 12288\n" \
    "buffer_load_dwordx4 v[16:19], %[vo], %[rw], %[so] offen offset:0 sc1\n" \
    "ds_read_b128 v[80:83], %[wl] offset:0\n" \
    "ds_read_b128 v[84:87], %[wl] offset:1024\n" \
    "s_nop 7\n" \
    "v_mov_b32 v104, 0\n" \
    "buffer_load_dwordx4 v[20:23], %[vo], %[rw], %[so] offen offset:256 sc1\n" \
    "s_nop 7\n" \
    "s_nop 3\n" \
    "buffer_load_dwordx4 v[24:27], %[vo], %[rw], %[so] offen offset:1024 sc1\n" \
    "s_nop 7\n" \
    "s_nop 3\n" \
    "buffer_load_dwordx4 v[28:31], %[vo], %[rw], %[so] offen offset:1280 sc1\n" \
    "s_nop 7\n" \
    "s_nop 3\n" \
    "buffer_load_dwordx4 v[32:35], %[vo], %[rw], %[so] offen offset:2048 sc1\n" \
    "s_nop 7\n" \
    "s_nop 3\n" \
    "buffer_load_dwordx4 v[36:39], %[vo], %[rw], %[so] offen offset:2304 sc1\n" \
    "s_nop 7\n" \
    "s_nop 3\n" \
    "buffer_load_dwordx4 v[40:43], %[vo], %[rw], %[so] offen offset:3072 sc1\n" \
    "s_nop 7\n" \
    "s_nop 3\n" \
    "buffer_load_dwordx4 v[44:47], %[vo], %[rw], %[so] offen offset:3328 sc1\n" \
    "s_nop 7\n" \
    "s_nop 3\n" \
    "buffer_load_dwordx4 v[48:51], %[vo], %[rw], s44 offen offset:0 sc1\n" \
    "s_nop 7\n" \
    "s_nop 3\n" \
    "buffer_load_dwordx4 v[52:55], %[vo], %[rw], s44 offen offset:256 sc1\n" \
    "s_nop 7\n" \
    "s_nop 3\n" \
    "buffer_load_dwordx4 v[56:59], %[vo], %[rw], s44 offen offset:1024 sc1\n" \
    "s_nop 7\n" \
    "s_nop 3\n" \
    "buffer_load_dwordx4 v[60:63], %[vo], %[rw], s44 offen offset:1280 sc1\n" \
    "ds_read_b128 v[88:91], %[wl] offset:2048\n" \
    "s_waitcnt vmcnt(11)\n" \
    "v_mfma_f32_16x16x32_bf16 v[0:3], a[0:3], v[16:19], 0\n" \
    "v_max3_u32 v104, v104, v16, v17\n" \
    "v_mfma_f32_16x16x32_bf16 v[4:7], a[128:131], v[16:19], 0\n" \
    "v_max3_u32 v104, v104, v18, v19\n" \
    "v_mfma_f32_16x16x32_bf16 v[8:11], v[128:131], v[16:19], 0\n" \
    "s_waitcnt lgkmcnt(2)\n" \
    "v_mfma_f32_16x16x32_bf16 v[12:15], v[80:83], v[16:19], 0\n" \
    "buffer_load_dwordx4 v[64:67], %[vo], %[rw], s44 offen offset:2048 sc1\n" \
    "ds_read_b128 v[80:83], %[wl] offset:3072\n" \
    "s_waitcnt vmcnt(11)\n" \
    "v_mfma_f32_16x16x32_bf16 v[0:3], a[4:7], v[20:23], v[0:3]\n" \
    "v_max3_u32 v104, v104, v20, v21\n" \
    "v_mfma_f32_16x16x32_bf16 v[4:7], a[132:135], v[20:23], v[4:7]\n" \
    "v_max3_u32 v104, v104, v22, v23\n" \
    "v_mfma_f32_16x16x32_bf16 v[8:11], v[132:135], v[20:23], v[8:11]\n" \
    "s_waitcnt lgkmcnt(2)\n" \
    "v_mfma_f32_16x16x32_bf16 v[12:15], v[84:87], v[20:23], v[12:15]\n" \
    "buffer_load_dwordx4 v[68:71], %[vo], %[rw], s44 offen offset:2304 sc1\n" \
    "ds_read_b128 v[84:87], %[wl] offset:4096\n" \
    "s_waitcnt vmcnt(11)\n" \
    "v_mfma_f32_16x16x32_bf16 v[0:3], a[8:11], v[24:27], v[0:3]\n" \
    "v_max3_u32 v104, v104, v24, v25\n" \
    "v_mfma_f32_16x16x32_bf16 v[4:7], a[136:139], v[24:27], v[4:7]\n" \
    "v_max3_u32 v104, v104, v26, v27\n" \
    "v_mfma_f32_16x16x32_bf16 v[8:11], v[136:139], v[24:27], v[8:11]\n" \
    "s_waitcnt lgkmcnt(2)\n" \
    "v_mfma_f32_16x16x32_bf16 v[12:15], v[88:91], v[24:27], v[12:15]\n" \
    "buffer_load_dwordx4 v[72:75], %[vo], %[rw], s44 offen offset:3072 sc1\n" \
    "ds_read_b128 v[88:91], %[wl] offset:5120\n" \
    "s_waitcnt vmcnt(11)\n" \
    "v_mfma_f32_16x16x32_bf16 v[0:3], a[12:15], v[28:31], v[0:3]\n" \
    "v_max3_u32 v104, v104, v28, v29\n" \
    "v_mfma_f32_16x16x32_bf16 v[4:7], a[140:143], v[28:31], v[4:7]\n" \
    "v_max3_u32 v104, v104, v30, v31\n" \
    "v_mfma_f32_16x16x32_bf16 v[8:11], v[140:143], v[28:31], v[8:11]\n" \
    "s_waitcnt lgkmcnt(2)\n" \
    "v_mfma_f32_16x16x32_bf16 v[12:15], v[80:83], v[28:31], v[12:15]\n" \
    "buffer_load_dwordx4 v[76:79], %[vo], %[rw], s44 offen offset:3328 sc1\n" \
    "ds_read_b128 v[80:83], %[wl] offset:6144\n" \
    "s_waitcnt vmcnt(11)\n" \
    "v_mfma_f32_16x16x32_bf16 v[0:3], a[16:19], v[32:35], v[0:3]\n" \
    "v_max3_u32 v104, v104, v32, v33\n" \
    "v_mfma_f32_16x16x32_bf16 v[4:7], a[144:147], v[32:35], v[4:7]\n" \
    "v_max3_u32 v104, v104, v34, v35\n" \
    "v_mfma_f32_16x16x32_bf16 v[8:11], v[144:147], v[32:35], v[8:11]\n" \
    "s_waitcnt lgkmcnt(2)\n" \
    "v_mfma_f32_16x16x32_bf16 v[12:15], v[84:87], v[32:35], v[12:15]\n" \
    "buffer_load_dwordx4 v[16:19], %[vo], %[rw], s45 offen offset:0 sc1\n" \
    "ds_read_b128 v[84:87], %[wl] offset:7168\n" \
    "s_waitcnt vmcnt(11)\n" \
    "v_mfma_f32_16x16x32_bf16 v[0:3], a[20:23], v[36:39], v[0:3]\n" \
    "v_max3_u32 v104, v104, v36, v37\n" \
    "v_mfma_f32_16x16x32_bf16 v[4:7], a[148:151], v[36:39], v[4:7]\n" \
    "v_max3_u32 v104, v104, v38, v39\n" \
    "v_mfma_f32_16x16x32_bf16 v[8:11], v[148:151], v[36:39], v[8:11]\n" \
    "s_waitcnt lgkmcnt(2)\n" \
    "v_mfma_f32_16x16x32_bf16 v[12:15], v[88:91], v[36:39], v[12:15]\n" \
    "buffer_load_dwordx4 v[20:23], %[vo], %[rw], s45 offen offset:256 sc1\n" \
    "ds_read_b128 v[88:91], %[wl] offset:8192\n" \
    "s_waitcnt vmcnt(11)\n" \
    "v_mfma_f32_16x16x32_bf16 v[0:3], a[24:27], v[40:43], v[0:3]\n" \
    "v_max3_u32 v104, v104, v40, v41\n" \
    "v_mfma_f32_16x16x32_bf16 v[4:7], a[152:155], v[40:43], v[4:7]\n" \
    "v_max3_u32 v104, v104, v42, v43\n" \
    "v_mfma_f32_16x16x32_bf16 v[8:11], v[152:155], v[40:43], v[8:11]\n" \
    "s_waitcnt lgkmcnt(2)\n" \
    "v_mfma_f32_16x16x32_bf16 v[12:15], v[80:83], v[40:43], v[12:15]\n" \
    "buffer_load_dwordx4 v[24:27], %[vo], %[rw], s45 offen offset:1024 sc1\n" \
    "ds_read_b128 v[80:83], %[wl] offset:9216\n" \
    "s_waitcnt vmcnt(11)\n" \
    "v_mfma_f32_16x16x32_bf16 v[0:3], a[28:31], v[44:47], v[0:3]\n" \
    "v_max3_u32 v104, v104, v44, v45\n" \
    "v_mfma_f32_16x16x32_bf16 v[4:7], a[156:159], v[44:47], v[4:7]\n" \
    "v_max3_u32 v104, v104, v46, v47\n" \
    "v_mfma_f32_16x16x32_bf16 v[8:11], v[156:159], v[44:47], v[8:11]\n" \
    "s_waitcnt lgkmcnt(2)\n" \
    "v_mfma_f32_16x16x32_bf16 v[12:15], v[84:87], v[44:47], v[12:15]\n" \
    "buffer_load_dwordx4 v[28:31], %[vo], %[rw], s45 offen offset:1280 sc1\n" \
    "ds_read_b128 v[84:87], %[wl] offset:10240\n" \
    "s_waitcnt vmcnt(11)\n" \
    "v_mfma_f32_16x16x32_bf16 v[0:3], a[32:35], v[48:51], v[0:3]\n" \
    "v_max3_u32 v104, v104, v48, v49\n" \
    "v_mfma_f32_16x16x32_bf16 v[4:7], a[160:163], v[48:51], v[4:7]\n" \
    "v_max3_u32 v104, v104, v50, v51\n" \
    "v_mfma_f32_16x16x32_bf16 v[8:11], v[160:163], v[48:51], v[8:11]\n" \
    "s_waitcnt lgkmcnt(2)\n" \
    "v_mfma_f32_16x16x32_bf16 v[12:15], v[88:91], v[48:51], v[12:15]\n" \
    "buffer_load_dwordx4 v[32:35], %[vo], %[rw], s45 offen offset:2048 sc1\n" \
    "ds_read_b128 v[88:91], %[wl] offset:11264\n" \
    "s_waitcnt vmcnt(11)\n" \
    "v_mfma_f32_16x16x32_bf16 v[0:3], a[36:39], v[52:55], v[0:3]\n" \
    "v_max3_u32 v104, v104, v52, v53\n" \
    "v_mfma_f32_16x16x32_bf16 v[4:7], a[164:167], v[52:55], v[4:7]\n" \
    "v_max3_u32 v104, v104, v54, v55\n" \
    "v_mfma_f32_16x16x32_bf16 v[8:11], v[164:167], v[52:55], v[8:11]\n" \
    "s_waitcnt lgkmcnt(2)\n" \
    "v_mfma_f32_16x16x32_bf16 v[12:15], v[80:83], v[52:55], v[12:15]\n" \
    "buffer_load_dwordx4 v[36:39], %[vo], %[rw], s45 offen offset:2304 sc1\n" \
    "ds_read_b128 v[80:83], %[wl] offset:12288\n" \
    "s_waitcnt vmcnt(11)\n" \
    "v_mfma_f32_16x16x32_bf16 v[0:3], a[40:43], v[56:59], v[0:3]\n" \
    "v_max3_u32 v104, v104, v56, v57\n" \
    "v_mfma_f32_16x16x32_bf16 v[4:7], a[168:171], v[56:59], v[4:7]\n" \
    "v_max3_u32 v104, v104, v58, v59\n" \
    "v_mfma_f32_16x16x32_bf16 v[8:11], v[168:171], v[56:59], v[8:11]\n" \
    "s_waitcnt lgkmcnt(2)\n" \
    "v_mfma_f32_16x16x32_bf16 v[12:15], v[84:87], v[56:59], v[12:15]\n" \
    "buffer_load_dwordx4 v[40:43], %[vo], %[rw], s45 offen offset:3072 sc1\n" \
    "ds_read_b128 v[84:87], %[wl] offset:13312\n" \
    "s_waitcnt vmcnt(11)\n" \
    "v_mfma_f32_16x16x32_bf16 v[0:3], a[44:47], v[60:63], v[0:3]\n" \
    "v_max3_u32 v104, v104, v60, v61\n" \
    "v_mfma_f32_16x16x32_bf16 v[4:7], a[172:175], v[60:63], v[4:7]\n" \
    "v_max3_u32 v104, v104, v62, v63\n" \
    "v_mfma_f32_16x16x32_bf16 v[8:11], v[172:175], v[60:63], v[8:11]\n" \
    "s_waitcnt lgkmcnt(2)\n" \
    "v_mfma_f32_16x16x32_bf16 v[12:15], v[88:91], v[60:63], v[12:15]\n" \
    "buffer_load_dwordx4 v[44:47], %[vo], %[rw], s45 offen offset:3328 sc1\n" \
    "ds_read_b128 v[88:91], %[wl] offset:14336\n" \
    "s_waitcnt vmcnt(11)\n" \
    "v_mfma_f32_16x16x32_bf16 v[0:3], a[48:51], v[64:67], v[0:3]\n" \
    "v_max3_u32 v104, v104, v64, v65\n" \
    "v_mfma_f32_16x16x32_bf16 v[4:7], a[176:179], v[64:67], v[4:7]\n" \
    "v_max3_u32 v104, v104, v66, v67\n" \
    "v_mfma_f32_16x16x32_bf16 v[8:11], v[176:179], v[64:67], v[8:11]\n" \
    "s_waitcnt lgkmcnt(2)\n" \
    "v_mfma_f32_16x16x32_bf16 v[12:15], v[80:83], v[64:67], v[12:15]\n" \
    "buffer_load_dwordx4 v[48:51], %[vo], %[rw], s46 offen offset:0 sc1\n" \
    "ds_read_b128 v[80:83], %[wl] offset:15360\n" \
    "s_waitcnt vmcnt(11)\n" \
    "v_mfma_f32_16x16x32_bf16 v[0:3], a[52:55], v[68:71], v[0:3]\n" \
    "v_max3_u32 v104, v104, v68, v69\n" \
    "v_mfma_f32_16x16x32_bf16 v[4:7], a[180:183], v[68:71], v[4:7]\n" \
    "v_max3_u32 v104, v104, v70, v71\n" \
    "v_mfma_f32_16x16x32_bf16 v[8:11], v[180:183], v[68:71], v[8:11]\n" \
    "s_waitcnt lgkmcnt(2)\n" \
    "v_mfma_f32_16x16x32_bf16 v[12:15], v[84:87], v[68:71], v[12:15]\n" \
    "buffer_load_dwordx4 v[52:55], %[vo], %[rw], s46 offen offset:256 sc1\n" \
    "ds_read_b128 v[84:87], %[wl] offset:16384\n" \
    "s_waitcnt vmcnt(11)\n" \
    "v_mfma_f32_16x16x32_bf16 v[0:3], a[56:59], v[72:75], v[0:3]\n" \
    "v_max3_u32 v104, v104, v72, v73\n" \
    "v_mfma_f32_16x16x32_bf16 v[4:7], a[184:187], v[72:75], v[4:7]\n" \
    "v_max3_u32 v104, v104, v74, v75\n" \
    "v_mfma_f32_16x16x32_bf16 v[8:11], v[184:187], v[72:75], v[8:11]\n" \
    "s_waitcnt lgkmcnt(2)\n" \
    "v_mfma_f32_16x16x32_bf16 v[12:15], v[88:91], v[72:75], v[12:15]\n" \
    "buffer_load_dwordx4 v[56:59], %[vo], %[rw], s46 offen offset:1024 sc1\n" \
    "ds_read_b128 v[88:91], %[wl] offset:17408\n" \
    "s_waitcnt vmcnt(11)\n" \
    "v_mfma_f32_16x16x32_bf16 v[0:3], a[60:63], v[76:79], v[0:3]\n" \
    "v_max3_u32 v104, v104, v76, v77\n" \
    "v_mfma_f32_16x16x32_bf16 v[4:7], a[188:191], v[76:79], v[4:7]\n" \
    "v_max3_u32 v104, v104, v78, v79\n" \
    "v_mfma_f32_16x16x32_bf16 v[8:11], v[188:191], v[76:79], v[8:11]\n" \
    "s_waitcnt lgkmcnt(2)\n" \
    "v_mfma_f32_16x16x32_bf16 v[12:15], v[80:83], v[76:79], v[12:15]\n" \
    "buffer_load_dwordx4 v[60:63], %[vo], %[rw], s46 offen offset:1280 sc1\n" \
    "ds_read_b128 v[80:83], %[wl] offset:18432\n" \
    "s_waitcnt vmcnt(11)\n" \
    "v_mfma_f32_16x16x32_bf16 v[0:3], a[64:67], v[16:19], v[0:3]\n" \
    "v_max3_u32 v104, v104, v16, v17\n" \
    "v_mfma_f32_16x16x32_bf16 v[4:7], a[192:195], v[16:19], v[4:7]\n" \
    "v_max3_u32 v104, v104, v18, v19\n" \
    "v_mfma_f32_16x16x32_bf16 v[8:11], v[192:195], v[16:19], v[8:11]\n" \
    "s_waitcnt lgkmcnt(2)\n" \
    "v_mfma_f32_16x16x32_bf16 v[12:15], v[84:87], v[16:19], v[12:15]\n" \
    "buffer_load_dwordx4 v[64:67], %[vo], %[rw], s46 offen offset:2048 sc1\n" \
    "ds_read_b128 v[84:87], %[wl] offset:19456\n" \
    "s_waitcnt vmcnt(11)\n" \
    "v_mfma_f32_16x16x32_bf16 v[0:3], a[68:71], v[20:23], v[0:3]\n" \
    "v_max3_u32 v104, v104, v20, v21\n" \
    "v_mfma_f32_16x16x32_bf16 v[4:7], a[196:199], v[20:23], v[4:7]\n" \
    "v_max3_u32 v104, v104, v22, v23\n" \
    "v_mfma_f32_16x16x32_bf16 v[8:11], v[196:199], v[20:23], v[8:11]\n" \
    "s_waitcnt lgkmcnt(2)\n" \
    "v_mfma_f32_16x16x32_bf16 v[12:15], v[88:91], v[20:23], v[12:15]\n" \
    "buffer_load_dwordx4 v[68:71], %[vo], %[rw], s46 offen offset:2304 sc1\n" \
    "ds_read_b128 v[88:91], %[wl] offset:20480\n" \
    "s_waitcnt vmcnt(11)\n" \
    "v_mfma_f32_16x16x32_bf16 v[0:3], a[72:75], v[24:27], v[0:3]\n" \
    "v_max3_u32 v104, v104, v24, v25\n" \
    "v_mfma_f32_16x16x32_bf16 v[4:7], a[200:203], v[24:27], v[4:7]\n" \
    "v_max3_u32 v104, v104, v26, v27\n" \
    "v_mfma_f32_16x16x32_bf16 v[8:11], v[200:203], v[24:27], v[8:11]\n" \
    "s_waitcnt lgkmcnt(2)\n" \
    "v_mfma_f32_16x16x32_bf16 v[12:15], v[80:83], v[24:27], v[12:15]\n" \
    "buffer_load_dwordx4 v[72:75], %[vo], %[rw], s46 offen offset:3072 sc1\n" \
    "ds_read_b128 v[80:83], %[wl] offset:21504\n" \
    "s_waitcnt vmcnt(11)\n" \
    "v_mfma_f32_16x16x32_bf16 v[0:3], a[76:79], v[28:31], v[0:3]\n" \
    "v_max3_u32 v104, v104, v28, v29\n" \
    "v_mfma_f32_16x16x32_bf16 v[4:7], a[204:207], v[28:31], v[4:7]\n" \
    "v_max3_u32 v104, v104, v30, v31\n" \
    "v_mfma_f32_16x16x32_bf16 v[8:11], v[204:207], v[28:31], v[8:11]\n" \
    "s_waitcnt lgkmcnt(2)\n" \
    "v_mfma_f32_16x16x32_bf16 v[12:15], v[84:87], v[28:31], v[12:15]\n" \
    "buffer_load_dwordx4 v[76:79], %[vo], %[rw], s46 offen offset:3328 sc1\n" \
    "ds_read_b128 v[84:87], %[wl] offset:22528\n" \
    "s_waitcnt vmcnt(11)\n" \
    "v_mfma_f32_16x16x32_bf16 v[0:3], a[80:83], v[32:35], v[0:3]\n" \
    "v_max3_u32 v104, v104, v32, v33\n" \
    "v_mfma_f32_16x16x32_bf16 v[4:7], a[208:211], v[32:35], v[4:7]\n" \
    "v_max3_u32 v104, v104, v34, v35\n" \
    "v_mfma_f32_16x16x32_bf16 v[8:11], v[208:211], v[32:35], v[8:11]\n" \
    "s_waitcnt lgkmcnt(2)\n" \
    "v_mfma_f32_16x16x32_bf16 v[12:15], v[88:91], v[32:35], v[12:15]\n" \
    "buffer_load_dwordx2 v[106:107], %[bv], %[rw], %[bs] offen sc1\n" \
    "ds_read_b128 v[88:91], %[wl] offset:23552\n" \
    "s_waitcnt vmcnt(11)\n" \
    "v_mfma_f32_16x16x32_bf16 v[0:3], a[84:87], v[36:39], v[0:3]\n" \
    "v_max3_u32 v104, v104, v36, v37\n" \
    "v_mfma_f32_16x16x32_bf16 v[4:7], a[212:215], v[36:39], v[4:7]\n" \
    "v_max3_u32 v104, v104, v38, v39\n" \
    "v_mfma_f32_16x16x32_bf16 v[8:11], v[212:215], v[36:39], v[8:11]\n" \
    "s_waitcnt lgkmcnt(2)\n" \
    "v_mfma_f32_16x16x32_bf16 v[12:15], v[80:83], v[36:39], v[12:15]\n" \
    "buffer_load_dword v108, %[gv], %[rw], %[gs] offen sc1\n" \
    "ds_read_b128 v[80:83], %[wl] offset:24576\n" \
    "s_waitcnt vmcnt(11)\n" \
    "v_mfma_f32_16x16x32_bf16 v[0:3], a[88:91], v[40:43], v[0:3]\n" \
    "v_max3_u32 v104, v104, v40, v41\n" \
    "v_mfma_f32_16x16x32_bf16 v[4:7], a[216:219], v[40:43], v[4:7]\n" \
    "v_max3_u32 v104, v104, v42, v43\n" \
    "v_mfma_f32_16x16x32_bf16 v[8:11], v[216:219], v[40:43], v[8:11]\n" \
    "s_waitcnt lgkmcnt(2)\n" \
    "v_mfma_f32_16x16x32_bf16 v[12:15], v[84:87], v[40:43], v[12:15]\n" \
    "buffer_load_dword v105, %[sv], %[rw], %[ss] offen sc1\n" \
    "ds_read_b128 v[84:87], %[wl] offset:25600\n" \
    "s_waitcnt vmcnt(11)\n" \
    "v_mfma_f32_16x16x32_bf16 v[0:3], a[92:95], v[44:47], v[0:3]\n" \
    "v_max3_u32 v104, v104, v44, v45\n" \
    "v_mfma_f32_16x16x32_bf16 v[4:7], a[220:223], v[44:47], v[4:7]\n" \
    "v_max3_u32 v104, v104, v46, v47\n" \
    "v_mfma_f32_16x16x32_bf16 v[8:11], v[220:223], v[44:47], v[8:11]\n" \
    "s_waitcnt lgkmcnt(2)\n" \
    "v_mfma_f32_16x16x32_bf16 v[12:15], v[88:91], v[44:47], v[12:15]\n" \
    "ds_read_b128 v[88:91], %[wl] offset:26624\n" \
    "s_waitcnt vmcnt(10)\n" \
    "v_mfma_f32_16x16x32_bf16 v[0:3], a[96:99], v[48:51], v[0:3]\n" \
    "v_max3_u32 v104, v104, v48, v49\n" \
    "v_mfma_f32_16x16x32_bf16 v[4:7], a[224:227], v[48:51], v[4:7]\n" \
    "v_max3_u32 v104, v104, v50, v51\n" \
    "v_mfma_f32_16x16x32_bf16 v[8:11], v[224:227], v[48:51], v[8:11]\n" \
    "s_waitcnt lgkmcnt(2)\n" \
    "v_mfma_f32_16x16x32_bf16 v[12:15], v[80:83], v[48:51], v[12:15]\n" \
    "ds_read_b128 v[80:83], %[wl] offset:27648\n" \
    "s_waitcnt vmcnt(9)\n" \
    "v_mfma_f32_16x16x32_bf16 v[0:3], a[100:103], v[52:55], v[0:3]\n" \
    "v_max3_u32 v104, v104, v52, v53\n" \
    "v_mfma_f32_16x16x32_bf16 v[4:7], a[228:231], v[52:55], v[4:7]\n" \
    "v_max3_u32 v104, v104, v54, v55\n" \
    "v_mfma_f32_16x16x32_bf16 v[8:11], v[228:231], v[52:55], v[8:11]\n" \
    "s_waitcnt lgkmcnt(2)\n" \
    "v_mfma_f32_16x16x32_bf16 v[12:15], v[84:87], v[52:55], v[12:15]\n" \
    "ds_read_b128 v[84:87], %[wl] offset:28672\n" \
    "s_waitcnt vmcnt(8)\n" \
    "v_mfma_f32_16x16x32_bf16 v[0:3], a[104:107], v[56:59], v[0:3]\n" \
    "v_max3_u32 v104, v104, v56, v57\n" \
    "v_mfma_f32_16x16x32_bf16 v[4:7], a[232:235], v[56:59], v[4:7]\n" \
    "v_max3_u32 v104, v104, v58, v59\n" \
    "v_mfma_f32_16x16x32_bf16 v[8:11], v[232:235], v[56:59], v[8:11]\n" \
    "s_waitcnt lgkmcnt(2)\n" \
    "v_mfma_f32_16x16x32_bf16 v[12:15], v[88:91], v[56:59], v[12:15]\n" \
    "ds_read_b128 v[88:91], %[wl] offset:29696\n" \
    "s_waitcnt vmcnt(7)\n" \
    "v_mfma_f32_16x16x32_bf16 v[0:3], a[108:111], v[60:63], v[0:3]\n" \
    "v_max3_u32 v104, v104, v60, v61\n" \
    "v_mfma_f32_16x16x32_bf16 v[4:7], a[236:239], v[60:63], v[4:7]\n" \
    "v_max3_u32 v104, v104, v62, v63\n" \
    "v_mfma_f32_16x16x32_bf16 v[8:11], v[236:239], v[60:63], v[8:11]\n" \
    "s_waitcnt lgkmcnt(2)\n" \
    "v_mfma_f32_16x16x32_bf16 v[12:15], v[80:83], v[60:63], v[12:15]\n" \
    "ds_read_b128 v[80:83], %[wl] offset:30720\n" \
    "s_waitcnt vmcnt(6)\n" \
    "v_mfma_f32_16x16x32_bf16 v[0:3], a[112:115], v[64:67], v[0:3]\n" \
    "v_max3_u32 v104, v104, v64, v65\n" \
    "v_mfma_f32_16x16x32_bf16 v[4:7], a[240:243], v[64:67], v[4:7]\n" \
    "v_max3_u32 v104, v104, v66, v67\n" \
    "v_mfma_f32_16x16x32_bf16 v[8:11], v[240:243], v[64:67], v[8:11]\n" \
    "s_waitcnt lgkmcnt(2)\n" \
    "v_mfma_f32_16x16x32_bf16 v[12:15], v[84:87], v[64:67], v[12:15]\n" \
    "ds_read_b128 v[84:87], %[wl] offset:31744\n" \
    "s_waitcnt vmcnt(5)\n" \
    "v_mfma_f32_16x16x32_bf16 v[0:3], a[116:119], v[68:71], v[0:3]\n" \
    "v_max3_u32 v104, v104, v68, v69\n" \
    "v_mfma_f32_16x16x32_bf16 v[4:7], a[244:247], v[68:71], v[4:7]\n" \
    "v_max3_u32 v104, v104, v70, v71\n" \
    "v_mfma_f32_16x16x32_bf16 v[8:11], v[244:247], v[68:71], v[8:11]\n" \
    "s_waitcnt lgkmcnt(2)\n" \
    "v_mfma_f32_16x16x32_bf16 v[12:15], v[88:91], v[68:71], v[12:15]\n" \
    "s_waitcnt vmcnt(4)\n" \
    "v_mfma_f32_16x16x32_bf16 v[0:3], a[120:123], v[72:75], v[0:3]\n" \
    "v_max3_u32 v104, v104, v72, v73\n" \
    "v_mfma_f32_16x16x32_bf16 v[4:7], a[248:251], v[72:75], v[4:7]\n" \
    "v_max3_u32 v104, v104, v74, v75\n" \
    "v_mfma_f32_16x16x32_bf16 v[8:11], v[248:251], v[72:75], v[8:11]\n" \
    "s_waitcnt lgkmcnt(1)\n" \
    "v_mfma_f32_16x16x32_bf16 v[12:15], v[80:83], v[72:75], v[12:15]\n" \
    "s_waitcnt vmcnt(3)\n" \
    "v_mfma_f32_16x16x32_bf16 v[0:3], a[124:127], v[76:79], v[0:3]\n" \
    "v_max3_u32 v104, v104, v76, v77\n" \
    "v_mfma_f32_16x16x32_bf16 v[4:7], a[252:255], v[76:79], v[4:7]\n" \
    "v_max3_u32 v104, v104, v78, v79\n" \
    "v_mfma_f32_16x16x32_bf16 v[8:11], v[252:255], v[76:79], v[8:11]\n" \
    "s_waitcnt lgkmcnt(0)\n" \
    "v_mfma_f32_16x16x32_bf16 v[12:15], v[84:87], v[76:79], v[12:15]\n" \
    "s_waitcnt vmcnt(0)\n" \
    "s_nop 7\n" \
    ""
#define OUT8_ASM \
    "s_mov_b32 exec_lo, %[tm]\n" \
    "s_mov_b32 exec_hi, 0\n" \
    "buffer_load_dword v79, %[tv], %[rw], %[so] offen sc1\n" \
    "s_mov_b64 exec, -1\n" \
    "s_add_i32 s44, %[so], 4096\n" \
    "s_add_i32 s45, %[so], 8192\n" \
    "s_add_i32 s46, %[so], 12288\n" \
    "buffer_load_dwordx4 v[16:19], %[vo], %[rw], %[so] offen offset:0 sc1\n" \
    "ds_read_b128 v[80:83], %[wl] offset:0\n" \
    "ds_read_b128 v[84:87], %[wl] offset:1024\n" \
    "ds_read_b128 v[88:91], %[wl] offset:2048\n" \
    "ds_read_b128 v[92:95], %[wl] offset:3072\n" \
    "v_mov_b64 v[0:1], 0\n" \
    "v_mov_b64 v[2:3], 0\n" \
    "v_mov_b64 v[4:5], 0\n" \
    "v_mov_b64 v[6:7], 0\n" \
    "v_mov_b64 v[8:9], 0\n" \
    "v_mov_b64 v[10:11], 0\n" \
    "v_mov_b64 v[12:13], 0\n" \
    "v_mov_b64 v[14:15], 0\n" \
    "v_mov_b32 v104, 0\n" \
    "buffer_load_dwordx4 v[20:23], %[vo], %[rw], %[so] offen offset:1024 sc1\n" \
    "s_nop 7\n" \
    "s_nop 3\n" \
    "buffer_load_dwordx4 v[24:27], %[vo], %[rw], %[so] offen offset:2048 sc1\n" \
    "s_nop 7\n" \
    "s_nop 3\n" \
    "buffer_load_dwordx4 v[28:31], %[vo], %[rw], %[so] offen offset:3072 sc1\n" \
    "s_nop 7\n" \
    "s_nop 3\n" \
    "buffer_load_dwordx4 v[32:35], %[vo], %[rw], s44 offen offset:0 sc1\n" \
    "s_nop 7\n" \
    "s_nop 3\n" \
    "buffer_load_dwordx4 v[36:39], %[vo], %[rw], s44 offen offset:1024 sc1\n" \
    "s_nop 7\n" \
    "s_nop 3\n" \
    "buffer_load_dwordx4 v[40:43], %[vo], %[rw], s44 offen offset:2048 sc1\n" \
    "s_nop 7\n" \
    "s_nop 3\n" \
    "buffer_load_dwordx4 v[44:47], %[vo], %[rw], s44 offen offset:3072 sc1\n" \
    "s_nop 7\n" \
    "s_nop 3\n" \
    "buffer_load_dwordx4 v[48:51], %[vo], %[rw], s45 offen offset:0 sc1\n" \
    "s_nop 7\n" \
    "s_nop 3\n" \
    "buffer_load_dwordx4 v[52:55], %[vo], %[rw], s45 offen offset:1024 sc1\n" \
    "ds_read_b128 v[96:99], %[wl] offset:4096\n" \
    "ds_read_b128 v[100:103], %[wl] offset:5120\n" \
    "s_waitcnt vmcnt(9)\n" \
    "v_smfmac_f32_16x16x64_bf16 v[0:3], v[16:19], a[0:7], %[idx]\n" \
    "v_max3_u32 v104, v104, v16, v17\n" \
    "v_smfmac_f32_16x16x64_bf16 v[4:7], v[16:19], a[128:135], %[idx]\n" \
    "v_max3_u32 v104, v104, v18, v19\n" \
    "v_smfmac_f32_16x16x64_bf16 v[8:11], v[16:19], v[128:135], %[idx]\n" \
    "s_waitcnt lgkmcnt(4)\n" \
    "v_smfmac_f32_16x16x64_bf16 v[12:15], v[16:19], v[80:87], %[idx]\n" \
    "buffer_load_dwordx4 v[56:59], %[vo], %[rw], s45 offen offset:2048 sc1\n" \
    "ds_read_b128 v[80:83], %[wl] offset:6144\n" \
    "ds_read_b128 v[84:87], %[wl] offset:7168\n" \
    "s_waitcnt vmcnt(9)\n" \
    "v_smfmac_f32_16x16x64_bf16 v[0:3], v[20:23], a[8:15], %[idx]\n" \
    "v_max3_u32 v104, v104, v20, v21\n" \
    "v_smfmac_f32_16x16x64_bf16 v[4:7], v[20:23], a[136:143], %[idx]\n" \
    "v_max3_u32 v104, v104, v22, v23\n" \
    "v_smfmac_f32_16x16x64_bf16 v[8:11], v[20:23], v[136:143], %[idx]\n" \
    "s_waitcnt lgkmcnt(4)\n" \
    "v_smfmac_f32_16x16x64_bf16 v[12:15], v[20:23], v[88:95], %[idx]\n" \
    "buffer_load_dwordx4 v[60:63], %[vo], %[rw], s45 offen offset:3072 sc1\n" \
    "ds_read_b128 v[88:91], %[wl] offset:8192\n" \
    "ds_read_b128 v[92:95], %[wl] offset:9216\n" \
    "s_waitcnt vmcnt(9)\n" \
    "v_smfmac_f32_16x16x64_bf16 v[0:3], v[24:27], a[16:23], %[idx]\n" \
    "v_max3_u32 v104, v104, v24, v25\n" \
    "v_smfmac_f32_16x16x64_bf16 v[4:7], v[24:27], a[144:151], %[idx]\n" \
    "v_max3_u32 v104, v104, v26, v27\n" \
    "v_smfmac_f32_16x16x64_bf16 v[8:11], v[24:27], v[144:151], %[idx]\n" \
    "s_waitcnt lgkmcnt(4)\n" \
    "v_smfmac_f32_16x16x64_bf16 v[12:15], v[24:27], v[96:103], %[idx]\n" \
    "buffer_load_dwordx4 v[64:67], %[vo], %[rw], s46 offen offset:0 sc1\n" \
    "ds_read_b128 v[96:99], %[wl] offset:10240\n" \
    "ds_read_b128 v[100:103], %[wl] offset:11264\n" \
    "s_waitcnt vmcnt(9)\n" \
    "v_smfmac_f32_16x16x64_bf16 v[0:3], v[28:31], a[24:31], %[idx]\n" \
    "v_max3_u32 v104, v104, v28, v29\n" \
    "v_smfmac_f32_16x16x64_bf16 v[4:7], v[28:31], a[152:159], %[idx]\n" \
    "v_max3_u32 v104, v104, v30, v31\n" \
    "v_smfmac_f32_16x16x64_bf16 v[8:11], v[28:31], v[152:159], %[idx]\n" \
    "s_waitcnt lgkmcnt(4)\n" \
    "v_smfmac_f32_16x16x64_bf16 v[12:15], v[28:31], v[80:87], %[idx]\n" \
    "buffer_load_dwordx4 v[68:71], %[vo], %[rw], s46 offen offset:1024 sc1\n" \
    "ds_read_b128 v[80:83], %[wl] offset:12288\n" \
    "ds_read_b128 v[84:87], %[wl] offset:13312\n" \
    "s_waitcnt vmcnt(9)\n" \
    "v_smfmac_f32_16x16x64_bf16 v[0:3], v[32:35], a[32:39], %[idx]\n" \
    "v_max3_u32 v104, v104, v32, v33\n" \
    "v_smfmac_f32_16x16x64_bf16 v[4:7], v[32:35], a[160:167], %[idx]\n" \
    "v_max3_u32 v104, v104, v34, v35\n" \
    "v_smfmac_f32_16x16x64_bf16 v[8:11], v[32:35], v[160:167], %[idx]\n" \
    "s_waitcnt lgkmcnt(4)\n" \
    "v_smfmac_f32_16x16x64_bf16 v[12:15], v[32:35], v[88:95], %[idx]\n" \
    "buffer_load_dwordx4 v[72:75], %[vo], %[rw], s46 offen offset:2048 sc1\n" \
    "ds_read_b128 v[88:91], %[wl] offset:14336\n" \
    "ds_read_b128 v[92:95], %[wl] offset:15360\n" \
    "s_waitcnt vmcnt(9)\n" \
    "v_smfmac_f32_16x16x64_bf16 v[0:3], v[36:39], a[40:47], %[idx]\n" \
    "v_max3_u32 v104, v104, v36, v37\n" \
    "v_smfmac_f32_16x16x64_bf16 v[4:7], v[36:39], a[168:175], %[idx]\n" \
    "v_max3_u32 v104, v104, v38, v39\n" \
    "v_smfmac_f32_16x16x64_bf16 v[8:11], v[36:39], v[168:175], %[idx]\n" \
    "s_waitcnt lgkmcnt(4)\n" \
    "v_smfmac_f32_16x16x64_bf16 v[12:15], v[36:39], v[96:103], %[idx]\n" \
    "buffer_load_dwordx4 v[76:79], %[vo], %[rw], s46 offen offset:3072 sc1\n" \
    "ds_read_b128 v[96:99], %[wl] offset:16384\n" \
    "ds_read_b128 v[100:103], %[wl] offset:17408\n" \
    "s_waitcnt vmcnt(9)\n" \
    "v_smfmac_f32_16x16x64_bf16 v[0:3], v[40:43], a[48:55], %[idx]\n" \
    "v_max3_u32 v104, v104, v40, v41\n" \
    "v_smfmac_f32_16x16x64_bf16 v[4:7], v[40:43], a[176:183], %[idx]\n" \
    "v_max3_u32 v104, v104, v42, v43\n" \
    "v_smfmac_f32_16x16x64_bf16 v[8:11], v[40:43], v[176:183], %[idx]\n" \
    "s_waitcnt lgkmcnt(4)\n" \
    "v_smfmac_f32_16x16x64_bf16 v[12:15], v[40:43], v[80:87], %[idx]\n" \
    "buffer_load_dwordx2 v[106:107], %[bv], %[rw], %[bs] offen sc1\n" \
    "ds_read_b128 v[80:83], %[wl] offset:18432\n" \
    "ds_read_b128 v[84:87], %[wl] offset:19456\n" \
    "s_waitcnt vmcnt(9)\n" \
    "v_smfmac_f32_16x16x64_bf16 v[0:3], v[44:47], a[56:63], %[idx]\n" \
    "v_max3_u32 v104, v104, v44, v45\n" \
    "v_smfmac_f32_16x16x64_bf16 v[4:7], v[44:47], a[184:191], %[idx]\n" \
    "v_max3_u32 v104, v104, v46, v47\n" \
    "v_smfmac_f32_16x16x64_bf16 v[8:11], v[44:47], v[184:191], %[idx]\n" \
    "s_waitcnt lgkmcnt(4)\n" \
    "v_smfmac_f32_16x16x64_bf16 v[12:15], v[44:47], v[88:95], %[idx]\n" \
    "buffer_load_dword v108, %[gv], %[rw], %[gs] offen sc1\n" \
    "ds_read_b128 v[88:91], %[wl] offset:20480\n" \
    "ds_read_b128 v[92:95], %[wl] offset:21504\n" \
    "s_waitcnt vmcnt(9)\n" \
    "v_smfmac_f32_16x16x64_bf16 v[0:3], v[48:51], a[64:71], %[idx]\n" \
    "v_max3_u32 v104, v104, v48, v49\n" \
    "v_smfmac_f32_16x16x64_bf16 v[4:7], v[48:51], a[192:199], %[idx]\n" \
    "v_max3_u32 v104, v104, v50, v51\n" \
    "v_smfmac_f32_16x16x64_bf16 v[8:11], v[48:51], v[192:199], %[idx]\n" \
    "s_waitcnt lgkmcnt(4)\n" \
    "v_smfmac_f32_16x16x64_bf16 v[12:15], v[48:51], v[96:103], %[idx]\n" \
    "buffer_load_dword v105, %[sv], %[rw], %[ss] offen sc1\n" \
    "ds_read_b128 v[96:99], %[wl] offset:22528\n" \
    "ds_read_b128 v[100:103], %[wl] offset:23552\n" \
    "s_waitcnt vmcnt(9)\n" \
    "v_smfmac_f32_16x16x64_bf16 v[0:3], v[52:55], a[72:79], %[idx]\n" \
    "v_max3_u32 v104, v104, v52, v53\n" \
    "v_smfmac_f32_16x16x64_bf16 v[4:7], v[52:55], a[200:207], %[idx]\n" \
    "v_max3_u32 v104, v104, v54, v55\n" \
    "v_smfmac_f32_16x16x64_bf16 v[8:11], v[52:55], v[200:207], %[idx]\n" \
    "s_waitcnt lgkmcnt(4)\n" \
    "v_smfmac_f32_16x16x64_bf16 v[12:15], v[52:55], v[80:87], %[idx]\n" \
    "ds_read_b128 v[80:83], %[wl] offset:24576\n" \
    "ds_read_b128 v[84:87], %[wl] offset:25600\n" \
    "s_waitcnt vmcnt(8)\n" \
    "v_smfmac_f32_16x16x64_bf16 v[0:3], v[56:59], a[80:87], %[idx]\n" \
    "v_max3_u32 v104, v104, v56, v57\n" \
    "v_smfmac_f32_16x16x64_bf16 v[4:7], v[56:59], a[208:215], %[idx]\n" \
    "v_max3_u32 v104, v104, v58, v59\n" \
    "v_smfmac_f32_16x16x64_bf16 v[8:11], v[56:59], v[208:215], %[idx]\n" \
    "s_waitcnt lgkmcnt(4)\n" \
    "v_smfmac_f32_16x16x64_bf16 v[12:15], v[56:59], v[88:95], %[idx]\n" \
    "ds_read_b128 v[88:91], %[wl] offset:26624\n" \
    "ds_read_b128 v[92:95], %[wl] offset:27648\n" \
    "s_waitcnt vmcnt(7)\n" \
    "v_smfmac_f32_16x16x64_bf16 v[0:3], v[60:63], a[88:95], %[idx]\n" \
    "v_max3_u32 v104, v104, v60, v61\n" \
    "v_smfmac_f32_16x16x64_bf16 v[4:7], v[60:63], a[216:223], %[idx]\n" \
    "v_max3_u32 v104, v104, v62, v63\n" \
    "v_smfmac_f32_16x16x64_bf16 v[8:11], v[60:63], v[216:223], %[idx]\n" \
    "s_waitcnt lgkmcnt(4)\n" \
    "v_smfmac_f32_16x16x64_bf16 v[12:15], v[60:63], v[96:103], %[idx]\n" \
    "ds_read_b128 v[96:99], %[wl] offset:28672\n" \
    "ds_read_b128 v[100:103], %[wl] offset:29696\n" \
    "s_waitcnt vmcnt(6)\n" \
    "v_smfmac_f32_16x16x64_bf16 v[0:3], v[64:67], a[96:103], %[idx]\n" \
    "v_max3_u32 v104, v104, v64, v65\n" \
    "v_smfmac_f32_16x16x64_bf16 v[4:7], v[64:67], a[224:231], %[idx]\n" \
    "v_max3_u32 v104, v104, v66, v67\n" \
    "v_smfmac_f32_16x16x64_bf16 v[8:11], v[64:67], v[224:231], %[idx]\n" \
    "s_waitcnt lgkmcnt(4)\n" \
    "v_smfmac_f32_16x16x64_bf16 v[12:15], v[64:67], v[80:87], %[idx]\n" \
    "ds_read_b128 v[80:83], %[wl] offset:30720\n" \
    "ds_read_b128 v[84:87], %[wl] offset:31744\n" \
    "s_waitcnt vmcnt(5)\n" \
    "v_smfmac_f32_16x16x64_bf16 v[0:3], v[68:71], a[104:111], %[idx]\n" \
    "v_max3_u32 v104, v104, v68, v69\n" \
    "v_smfmac_f32_16x16x64_bf16 v[4:7], v[68:71], a[232:239], %[idx]\n" \
    "v_max3_u32 v104, v104, v70, v71\n" \
    "v_smfmac_f32_16x16x64_bf16 v[8:11], v[68:71], v[232:239], %[idx]\n" \
    "s_waitcnt lgkmcnt(4)\n" \
    "v_smfmac_f32_16x16x64_bf16 v[12:15], v[68:71], v[88:95], %[idx]\n" \
    "s_waitcnt vmcnt(4)\n" \
    "v_smfmac_f32_16x16x64_bf16 v[0:3], v[72:75], a[112:119], %[idx]\n" \
    "v_max3_u32 v104, v104, v72, v73\n" \
    "v_smfmac_f32_16x16x64_bf16 v[4:7], v[72:75], a[240:247], %[idx]\n" \
    "v_max3_u32 v104, v104, v74, v75\n" \
    "v_smfmac_f32_16x16x64_bf16 v[8:11], v[72:75], v[240:247], %[idx]\n" \
    "s_waitcnt lgkmcnt(2)\n" \
    "v_smfmac_f32_16x16x64_bf16 v[12:15], v[72:75], v[96:103], %[idx]\n" \
    "s_waitcnt vmcnt(3)\n" \
    "v_smfmac_f32_16x16x64_bf16 v[0:3], v[76:79], a[120:127], %[idx]\n" \
    "v_max3_u32 v104, v104, v76, v77\n" \
    "v_smfmac_f32_16x16x64_bf16 v[4:7], v[76:79], a[248:255], %[idx]\n" \
    "v_max3_u32 v104, v104, v78, v79\n" \
    "v_smfmac_f32_16x16x64_bf16 v[8:11], v[76:79], v[248:255], %[idx]\n" \
    "s_waitcnt lgkmcnt(0)\n" \
    "v_smfmac_f32_16x16x64_bf16 v[12:15], v[76:79], v[80:87], %[idx]\n" \
    "s_waitcnt vmcnt(0)\n" \
    "s_nop 5\n" \
    "v_permlane32_swap_b32 v0, v8\n" \
    "v_add_f32 v0, v0, v8\n" \
    "v_permlane32_swap_b32 v1, v9\n" \
    "v_add_f32 v1, v1, v9\n" \
    "v_permlane32_swap_b32 v2, v10\n" \
    "v_add_f32 v2, v2, v10\n" \
    "v_permlane32_swap_b32 v3, v11\n" \
    "v_add_f32 v3, v3, v11\n" \
    "v_permlane32_swap_b32 v4, v12\n" \
    "v_add_f32 v4, v4, v12\n" \
    "v_permlane32_swap_b32 v5, v13\n" \
    "v_add_f32 v5, v5, v13\n" \
    "v_permlane32_swap_b32 v6, v14\n" \
    "v_add_f32 v6, v6, v14\n" \
    "v_permlane32_swap_b32 v7, v15\n" \
    "v_add_f32 v7, v7, v15\n" \
    ""
#define OUT_CLOBBER "v16", "v17", "v18", "v19", "v20", "v21", "v22", "v23", "v24", "v25", "v26", "v27", "v28", "v29", "v30", "v31", "v32", "v33", "v34", "v35", "v36", "v37", "v38", "v39", "v40", "v41", "v42", "v43", "v44", "v45", "v46", "v47", "v48", "v49", "v50", "v51", "v52", "v53", "v54", "v55", "v56", "v57", "v58", "v59", "v60", "v61", "v62", "v63", "v64", "v65", "v66", "v67", "v68", "v69", "v70", "v71", "v72", "v73", "v74", "v75", "v76", "v77", "v78", "v79", "v80", "v81", "v82", "v83", "v84", "v85", "v86", "v87", "v88", "v89", "v90", "v91", "v92", "v93", "v94", "v95", "v96", "v97", "v98", "v99", "v100", "v101", "v102", "v103", "s44", "s45", "s46", "scc", "memory"
constexpr int NREP = 2, NCP = 4;
#define OWNER_ASM \
    "s_getpc_b64 s[96:97]\n" \
    "14:\n" \
    "s_add_u32 s96, s96, 2f-14b\n" \
    "s_addc_u32 s97, s97, 0\n" \
    "s_sub_u32 s68, s96, 2f-11f\n" \
    "s_subb_u32 s69, s97, 0\n" \
    "s_mov_b32 %[np], 0\n" \
    "s_add_i32 s72, %[pb], 0x10000\n" \
    "13:\n" \
    "s_memtime vcc\n" \
    "s_waitcnt lgkmcnt(0)\n" \
    "s_sub_i32 s70, %[lk], vcc_lo\n" \
    "s_cmp_gt_i32 s70, %[rd]\n" \
    "s_cbranch_scc1 13b\n" \
    "s_add_i32 s70, %[pq], 0x10000\n" \
    "buffer_store_dwordx4 %[sv], %[ro], %[rs], %[pq] offen offset:0 sc1\n" \
    "buffer_store_dwordx4 %[sv], %[ro], %[rs], %[pq] offen offset:1024 sc1\n" \
    "buffer_store_dwordx4 %[sv], %[ro], %[rs], s70 offen offset:0 sc1\n" \
    "buffer_store_dwordx4 %[sv], %[ro], %[rs], s70 offen offset:1024 sc1\n" \
    "v_mov_b32 v2, -1\n" \
    "buffer_store_dword v2, %[rm], %[rs], %[mq] offen sc1\n" \
    "buffer_store_dwordx4 %[sv], %[rt], %[rw], %[tq] offen sc1\n" \
    "s_mov_b32 s71, 4\n" \
    "10:\n" \
    "s_memtime vcc\n" \
    "s_waitcnt lgkmcnt(0)\n" \
    "s_sub_i32 s70, %[lk], vcc_lo\n" \
    "s_cmp_gt_i32 s70, 112\n" \
    "s_cbranch_scc1 10b\n" \
    "s_max_i32 s70, s70, 0\n" \
    "s_memtime s[58:59]\n" \
    "s_memrealtime s[44:45]\n" \
    "s_nop 1\n" \
    "s_memrealtime s[46:47]\n" \
    "s_nop 1\n" \
    "s_memrealtime s[48:49]\n" \
    "s_nop 1\n" \
    "s_memrealtime s[50:51]\n" \
    "s_nop 1\n" \
    "s_memrealtime s[52:53]\n" \
    "s_nop 1\n" \
    "s_memrealtime s[54:55]\n" \
    "s_nop 1\n" \
    "s_memrealtime s[56:57]\n" \
    "s_nop 1\n" \
    "s_memrealtime vcc\n" \
    "s_waitcnt lgkmcnt(0)\n" \
    "s_add_i32 s44, s44, s46\n" \
    "s_add_i32 s48, s48, s50\n" \
    "s_add_i32 s52, s52, s54\n" \
    "s_add_i32 s44, s44, s48\n" \
    "s_add_i32 s52, s52, s56\n" \
    "s_add_i32 s44, s44, s52\n" \
    "s_add_i32 s59, s44, vcc_lo\n" \
    "s_sub_i32 s44, s59, %[oT]\n" \
    "s_mov_b32 %[oT], s59\n" \
    "s_sub_i32 s45, s58, %[oC]\n" \
    "s_mov_b32 %[oC], s58\n" \
    "s_mul_i32 s46, s44, %[oR]\n" \
    "s_lshr_b32 s46, s46, 12\n" \
    "s_sub_i32 s46, s45, s46\n" \
    "s_sub_i32 s44, s44, 1000\n" \
    "s_cmp_lt_u32 s44, 2000\n" \
    "s_cselect_b32 s46, s46, 0\n" \
    "s_add_i32 %[oR], %[oR], s46\n" \
    "s_sub_i32 s45, %[oT], %[c8]\n" \
    "s_and_b32 s70, s70, -4\n" \
    "s_sub_u32 vcc_lo, s68, s70\n" \
    "s_subb_u32 vcc_hi, s69, 0\n" \
    "s_setpc_b64 vcc\n" \
    "s_nop 0\n" \
    "s_nop 0\n" \
    "s_nop 0\n" \
    "s_nop 0\n" \
    "s_nop 0\n" \
    "s_nop 0\n" \
    "s_nop 0\n" \
    "s_nop 0\n" \
    "s_nop 0\n" \
    "s_nop 0\n" \
    "s_nop 0\n" \
    "s_nop 0\n" \
    "s_nop 0\n" \
    "s_nop 0\n" \
    "s_nop 0\n" \
    "s_nop 0\n" \
    "s_nop 0\n" \
    "s_nop 0\n" \
    "s_nop 0\n" \
    "s_nop 0\n" \
    "s_nop 0\n" \
    "s_nop 0\n" \
    "s_nop 0\n" \
    "s_nop 0\n" \
    "s_nop 0\n" \
    "s_nop 0\n" \
    "s_nop 0\n" \
    "s_nop 0\n" \
    "11:\n" \
    "buffer_load_dwordx2 v[0:1], %[ca], %[rs], %[cs] offen sc1\n" \
    "s_waitcnt vmcnt(0)\n" \
    "6:\n" \
    "s_memtime s[46:47]\n" \
    "v_subrev_u32 v0, s45, v0\n" \
    "v_cmp_ne_u16 vcc, %[tag], v1\n" \
    "s_add_i32 s48, %[oC], -136\n" \
    "v_max_i32_dpp v0, v0, v0 row_shr:1 row_mask:0xf bank_mask:0xf\n" \
    "v_max_u32_dpp v1, v1, v1 row_shr:1 row_mask:0xf bank_mask:0xf\n" \
    "s_cbranch_vccnz 7f\n" \
    "8:\n" \
    "v_max_i32_dpp v0, v0, v0 row_shr:2 row_mask:0xf bank_mask:0xf\n" \
    "v_max_u32_dpp v1, v1, v1 row_shr:2 row_mask:0xf bank_mask:0xf\n" \
    "s_nop 0\n" \
    "v_max_i32_dpp v0, v0, v0 row_shr:4 row_mask:0xf bank_mask:0xf\n" \
    "v_max_u32_dpp v1, v1, v1 row_shr:4 row_mask:0xf bank_mask:0xf\n" \
    "s_nop 0\n" \
    "v_max_i32_dpp v0, v0, v0 row_shr:8 row_mask:0xf bank_mask:0xf\n" \
    "v_max_u32_dpp v1, v1, v1 row_shr:8 row_mask:0xf bank_mask:0xf\n" \
    "s_waitcnt lgkmcnt(0)\n" \
    "v_max_i32_dpp v0, v0, v0 row_bcast:15 row_mask:0xa bank_mask:0xf\n" \
    "v_max_u32_dpp v1, v1, v1 row_bcast:15 row_mask:0xa bank_mask:0xf\n" \
    "s_add_i32 s46, s46, 44\n" \
    "v_max_i32_dpp v0, v0, v0 row_bcast:31 row_mask:0xc bank_mask:0xf\n" \
    "v_max_u32_dpp v1, v1, v1 row_bcast:31 row_mask:0xc bank_mask:0xf\n" \
    "v_readlane_b32 s44, v0, 63\n" \
    "v_readlane_b32 s49, v1, 63\n" \
    "s_mul_i32 s47, s44, %[oR]\n" \
    "s_ashr_i32 s47, s47, 12\n" \
    "s_add_i32 s47, s47, s48\n" \
    "s_sub_i32 s58, s47, s46\n" \
    "s_cmp_gt_i32 s58, 112\n" \
    "s_cbranch_scc1 1f\n" \
    "12:\n" \
    "s_max_i32 s58, s58, 0\n" \
    "s_and_b32 s58, s58, -4\n" \
    "s_sub_u32 vcc_lo, s96, s58\n" \
    "s_subb_u32 vcc_hi, s97, 0\n" \
    "s_setpc_b64 vcc\n" \
    "s_nop 0\n" \
    "s_nop 0\n" \
    "s_nop 0\n" \
    "s_nop 0\n" \
    "s_nop 0\n" \
    "s_nop 0\n" \
    "s_nop 0\n" \
    "s_nop 0\n" \
    "s_nop 0\n" \
    "s_nop 0\n" \
    "s_nop 0\n" \
    "s_nop 0\n" \
    "s_nop 0\n" \
    "s_nop 0\n" \
    "s_nop 0\n" \
    "s_nop 0\n" \
    "s_nop 0\n" \
    "s_nop 0\n" \
    "s_nop 0\n" \
    "s_nop 0\n" \
    "s_nop 0\n" \
    "s_nop 0\n" \
    "s_nop 0\n" \
    "s_nop 0\n" \
    "s_nop 0\n" \
    "s_nop 0\n" \
    "s_nop 0\n" \
    "s_nop 0\n" \
    "2:\n" \
    "buffer_load_dwordx4 v[0:3], %[o], %[rs], %[pb] offen offset:0 sc1\n" \
    "buffer_load_dwordx4 v[4:7], %[o], %[rs], %[pb] offen offset:1024 sc1\n" \
    "buffer_load_dwordx4 v[8:11], %[o], %[rs], s72 offen offset:0 sc1\n" \
    "buffer_load_dwordx4 v[12:15], %[o], %[rs], s72 offen offset:1024 sc1\n" \
    "s_add_i32 %[np], %[np], 1\n" \
    "s_add_i32 %[tm], s44, s45\n" \
    "s_lshr_b32 s50, s49, 16\n" \
    "s_add_i32 s50, s50, s44\n" \
    "s_add_i32 s50, s50, %[cw]\n" \
    "s_mul_i32 s50, s50, %[oR]\n" \
    "s_ashr_i32 s50, s50, 12\n" \
    "s_add_i32 %[lk], s50, %[oC]\n" \
    "s_waitcnt vmcnt(3)\n" \
    "v_mfma_f32_16x16x32_bf16 v[16:19], %[sel], v[0:3], 0\n" \
    "v_max3_u32 v20, v0, v1, v1\n" \
    "v_max3_u32 v20, v2, v3, v20\n" \
    "s_waitcnt vmcnt(2)\n" \
    "v_mfma_f32_16x16x32_bf16 v[16:19], %[sel], v[4:7], v[16:19]\n" \
    "v_max3_u32 v20, v4, v5, v20\n" \
    "v_max3_u32 v20, v6, v7, v20\n" \
    "s_waitcnt vmcnt(1)\n" \
    "v_mfma_f32_16x16x32_bf16 v[16:19], %[sel], v[8:11], v[16:19]\n" \
    "v_max3_u32 v20, v8, v9, v20\n" \
    "v_max3_u32 v20, v10, v11, v20\n" \
    "s_waitcnt vmcnt(0)\n" \
    "v_mfma_f32_16x16x32_bf16 v[16:19], %[sel], v[12:15], v[16:19]\n" \
    "v_max3_u32 v20, v12, v13, v20\n" \
    "v_max3_u32 v20, v14, v15, v20\n" \
    "v_cmp_eq_u32 vcc, -1, v20\n" \
    "s_nop 3\n" \
    "s_cbranch_vccnz 4f\n" \
    "3:\n" \
    "v_add_f32_dpp v16, v16, v16 row_ror:8 row_mask:0xf bank_mask:0xf\n" \
    "v_add_f32_dpp v17, v17, v17 row_ror:8 row_mask:0xf bank_mask:0xf\n" \
    "v_cvt_pk_bf16_f32 v16, v16, v17\n" \
    "buffer_store_dword v16, %[so], %[rs], %[mb0] offen sc1\n" \
    "s_branch 5f\n" \
    "1:\n" \
    "s_memtime vcc\n" \
    "s_waitcnt lgkmcnt(0)\n" \
    "s_sub_i32 s58, s47, vcc_lo\n" \
    "s_cmp_gt_i32 s58, 112\n" \
    "s_cbranch_scc1 1b\n" \
    "s_branch 12b\n" \
    "4:\n" \
    "s_add_i32 %[bud], %[bud], -1\n" \
    "s_cmp_lt_i32 %[bud], 0\n" \
    "s_cbranch_scc0 2b\n" \
    "s_branch 3b\n" \
    "7:\n" \
    "s_bcnt1_i32_b64 s44, vcc\n" \
    "s_cmp_le_u32 s44, s71\n" \
    "s_mov_b32 s71, 0\n" \
    "s_cbranch_scc1 15f\n" \
    "s_cmp_eq_u32 %[tag], 1\n" \
    "s_cbranch_scc1 9f\n" \
    "s_sleep 15\n" \
    "9:\n" \
    "s_sleep 6\n" \
    "15:\n" \
    "s_add_i32 %[bud], %[bud], -1\n" \
    "s_cmp_lt_i32 %[bud], 0\n" \
    "s_cbranch_scc1 8b\n" \
    "buffer_load_dwordx2 v[0:1], %[ca], %[rs], %[cs] offen sc1\n" \
    "s_memtime s[58:59]\n" \
    "s_memrealtime s[44:45]\n" \
    "s_nop 1\n" \
    "s_memrealtime s[46:47]\n" \
    "s_nop 1\n" \
    "s_memrealtime s[48:49]\n" \
    "s_nop 1\n" \
    "s_memrealtime s[50:51]\n" \
    "s_nop 1\n" \
    "s_memrealtime s[52:53]\n" \
    "s_nop 1\n" \
    "s_memrealtime s[54:55]\n" \
    "s_nop 1\n" \
    "s_memrealtime s[56:57]\n" \
    "s_nop 1\n" \
    "s_memrealtime vcc\n" \
    "s_waitcnt lgkmcnt(0)\n" \
    "s_add_i32 s44, s44, s46\n" \
    "s_add_i32 s48, s48, s50\n" \
    "s_add_i32 s52, s52, s54\n" \
    "s_add_i32 s44, s44, s48\n" \
    "s_add_i32 s52, s52, s56\n" \
    "s_add_i32 s44, s44, s52\n" \
    "s_add_i32 s59, s44, vcc_lo\n" \
    "s_sub_i32 s44, s59, %[oT]\n" \
    "s_mov_b32 %[oT], s59\n" \
    "s_sub_i32 s45, s58, %[oC]\n" \
    "s_mov_b32 %[oC], s58\n" \
    "s_mul_i32 s46, s44, %[oR]\n" \
    "s_lshr_b32 s46, s46, 12\n" \
    "s_sub_i32 s46, s45, s46\n" \
    "s_sub_i32 s44, s44, 1000\n" \
    "s_cmp_lt_u32 s44, 2000\n" \
    "s_cselect_b32 s46, s46, 0\n" \
    "s_add_i32 %[oR], %[oR], s46\n" \
    "s_sub_i32 s45, %[oT], %[c8]\n" \
    "s_waitcnt vmcnt(0)\n" \
    "s_branch 6b\n" \
    "5:\n" \
    ""
#define OWNER_CLOBBER "v0", "v1", "v2", "v3", "v4", "v5", "v6", "v7", "v8", "v9", "v10", "v11", "v12", "v13", "v14", "v15", "v16", "v17", "v18", "v19", "v20", "s44", "s45", "s46", "s47", "s48", "s49", "s50", "s51", "s52", "s53", "s54", "s55", "s56", "s57", "s58", "s59", "s68", "s69", "s70", "s71", "s72", "s96", "s97", "vcc", "scc", "memory"
constexpr int O_W = 0;
constexpr int O_STG = 131072;
constexpr int O_B = O_STG + 20480;
constexpr int OUT_REP = 7;
#define OUT_AUX 16
template <int O, class V> DI void put4(V& d, u32x4 f) {
#pragma unroll
    for (int j = 0; j < 4; ++j) d[O + j] = f[j];
}
DI void role_outp(const Args& a, LAS char* lds, unsigned lds_base, int nb, int rep, rsrc_t rw, int& budget) {
    const int tid = threadIdx.x, lane = tid & 63, w = __builtin_amdgcn_readfirstlane(tid >> 6);
    WG_BARRIER();
    const int n = lane & 15, g = lane >> 4;
    const int wl = O_W + w * 32768 + lane * 16;
    u32x32 A[8], V[4];
    {
        const int stg = O_STG + w * 5120;
        const rsrc_t ro = __builtin_amdgcn_make_buffer_rsrc((void*)a.Wo, 0, HD * DM * 2, 0x00020000);
        const int so = (1024 * w + (lane >> 3)) * (DM * 2) + nb * 128 + (lane & 7) * 16, sw = stg + (lane >> 3) * 160 + (lane & 7) * 16;
        const int ra = stg + (16 * (g & 1) + 4 * (g >> 1) + (n >> 2)) * 160 + (n & 3) * 8;
        constexpr int PB = 4;
        u32x4 b[PB][4];
        auto fetch = [&](int ks) {
#pragma unroll
            for (int i = 0; i < 4; ++i) b[ks % PB][i] = __builtin_amdgcn_raw_buffer_load_b128(ro, so, (64 * (ks >> 1) + 16 * (ks & 1) + 8 * (i & 1) + 32 * (i >> 1)) * (DM * 2), 0);
        };
        unsigned tp = now();
        const int pc = a.d1 >> 16;
#pragma unroll
        for (int ks = 0; ks < PB; ++ks) { fetch(ks); FENCE(); tp += pc; while ((int)(now() - tp) < 0) {} }
        FENCE();
        static_for<32>([&](auto ic) {
            constexpr int ks = decltype(ic)::value;
#pragma unroll
            for (int i = 0; i < 4; ++i) *(LAS u32x4*)(lds + sw + i * 1280) = b[ks % PB][i];
            if (ks + PB < 32) { fetch(ks + PB); FENCE(); tp += pc; }
            u32x4 f[4];
#pragma unroll
            for (int t = 0; t < 4; ++t) { const u32x2 lo = tr_read(lds, ra + t * 32), hi = tr_read(lds, ra + t * 32 + 1280); f[t] = (u32x4){lo.x, lo.y, hi.x, hi.y}; }
            put4<4 * (ks & 7)>(A[ks >> 3], f[0]); put4<4 * (ks & 7)>(A[4 + (ks >> 3)], f[1]); put4<4 * (ks & 7)>(V[ks >> 3], f[2]);
            *(LAS u32x4*)(lds + wl + ks * 1024) = f[3];
            if constexpr (ks == 7) asm volatile("" : "+{a[0:31]}"(A[0]), "+{a[128:159]}"(A[4]), "+{v[128:159]}"(V[0]));
            if constexpr (ks == 15) asm volatile("" : "+{a[32:63]}"(A[1]), "+{a[160:191]}"(A[5]), "+{v[160:191]}"(V[1]));
            if constexpr (ks == 23) asm volatile("" : "+{a[64:95]}"(A[2]), "+{a[192:223]}"(A[6]), "+{v[192:223]}"(V[2]));
            if constexpr (ks == 31) asm volatile("" : "+{a[96:127]}"(A[3]), "+{a[224:255]}"(A[7]), "+{v[224:255]}"(V[3]));
            FENCE();
            if (ks + PB < 31) while ((int)(now() - tp) < 0) {}
        });
    }
    wait_prog(rw, F_S4 + lane * 4, true, 1, budget);
    const rsrc_t ru = __builtin_amdgcn_make_buffer_rsrc(a.out, 0, SEQ * DM * 2, 0x00020000);
    constexpr int SP = 2 * LATE - 2;
    auto first = [&](int u) { return u >= SP ? 8 * u + 8 * SP : 16 * u; };
    auto st_s = [&](int u) { return (u >= 2 * FAST ? F_H + ((first(u) >> 4) - 2 * FAST) * 4096 : F_O + (u & 3) * 4) + w * 1024; };
    auto bn_s = [&](int u) { return OFF_BO + (64 * nb + 16 * w) * BO_ROW + first(u) * 2; };
    auto gn_s = [&](int u) { return OFF_GT + (first(u) + 4 * w) * 4; };
    int st = -1;
    u32x2 bn = ld64(rw, (lane >> 2) * BO_ROW + (lane & 3) * 8, bn_s(rep));
    float gn = __uint_as_float(ld32(rw, g * 4, gn_s(rep)));
    __builtin_amdgcn_s_waitcnt(0);
#define OUT_OPS "=&{v[0:15]}"(acc), "=&{v104}"(top), "=&{v105}"(st), "=&{v[106:107]}"(bn), "=&{v108}"(gn) \
        : "{a[0:31]}"(A[0]), "{a[32:63]}"(A[1]), "{a[64:95]}"(A[2]), "{a[96:127]}"(A[3]), "{a[128:159]}"(A[4]), "{a[160:191]}"(A[5]), "{a[192:223]}"(A[6]), "{a[224:255]}"(A[7]), \
          "{v[128:159]}"(V[0]), "{v[160:191]}"(V[1]), "{v[192:223]}"(V[2]), "{v[224:255]}"(V[3]), [rw] "s"(rw), [so] "s"(t0 * 8192), [wl] "v"(lds_base + O_W + w * 32768 + lane * 16), \
          [tv] "v"(tv), [tm] "s"(tm), [vo] "v"(vo), [sv] "v"(sv), [bv] "v"((lane >> 2) * BO_ROW + (lane & 3) * 8), [gv] "v"(g * 4), \
          [ss] "s"(st_s(u + OUT_REP)), [bs] "s"(bn_s(u + OUT_REP)), [gs] "s"(gn_s(u + OUT_REP))
    for (int u = rep; u < SP + 4; u += OUT_REP) {
        const bool fast = u >= 2 * FAST, sp = u >= SP;
        const int t0 = first(u);
        WG_BARRIER();
        int lane = opaque_lane(), n = lane & 15, g = lane >> 4;
        if (u == 3 * OUT_REP && nb == 0) __builtin_amdgcn_raw_buffer_store_b64(launch_tag(a), rw, w + lane == 0 ? F_EPOCH : OOB, 0, 16);
        S4S(0)
        TT(u >= 119, 0)
        *(LAS u32x2*)(lds + O_B + (16 * w + (lane >> 2)) * 40 + (lane & 3) * 8) = bn;
        const float gt = gn;
        const int tv = OFF_OF + w * 16384 + nb * 2048 + g * 65536 + n * 128, tm = __builtin_amdgcn_readfirstlane(nb < 8 ? sp ? 0xFFFF : -1 : 0);
        const int vo = OFF_OF + w * 16384 + (n & 7) * 16 + (sp ? (n >> 3) * 512 + g * 128 : (n >> 3) * 65536 + (g & 1) * 512 + (g >> 1) * 128);
        const int thr = fast ? 0 : u >> 1, sv = (lane & 7) * 128;
        if (!__all(st >= thr)) do { __builtin_amdgcn_s_sleep(1); st = (int)ld32(rw, sv, st_s(u)); asm volatile("" ::: "memory"); } while (!__all(st >= thr) && --budget >= 0);
        if (fast) { const unsigned t = now(); (void)wait_until(t + wave_max32(min((int)(2 * (st - t)) >> 1, 500))); }
        S4S(1)
        TT(u >= 119, 1)
        f32x16 acc; unsigned top;
        if (sp) {
            do asm volatile(OUT8_ASM : OUT_OPS, [idx] "v"(n & 8 ? 0xEEEE : 0x4444) : OUT_CLOBBER); while (__any(top == SENT) && --budget >= 0);
            TT(u >= 119, 2)
            lane = opaque_lane(); n = lane & 15; g = lane >> 4;
#pragma unroll
            for (int i = 0; i < 8; ++i) *(LAS float*)(lds + O_STG + (w * 16 + 4 * (g & 1) + (i & 3)) * 272 + (g >> 1) * 128 + (i >> 2) * 64 + n * 4) = acc[i];
        } else {
            do asm volatile(OUT16_ASM : OUT_OPS : OUT_CLOBBER); while (__any(top == SENT) && --budget >= 0);
            S4S(2)
            TT(u >= 119, 2)
            lane = opaque_lane(); n = lane & 15; g = lane >> 4;
#pragma unroll
            for (int t = 0; t < 4; ++t) *(LAS f32x4*)(lds + O_STG + (w * 16 + n) * 272 + t * 64 + g * 16) = (f32x4){acc[4 * t], acc[4 * t + 1], acc[4 * t + 2], acc[4 * t + 3]};
        }
        WG_BARRIER();
        S4S(3)
        const int tok = 4 * w + g, c4 = 4 * n;
        f32x4 r[4]; u16 bv[4];
#pragma unroll
        for (int ww = 0; ww < 4; ++ww) { r[ww] = *(LAS f32x4*)(lds + O_STG + (ww * 16 + tok) * 272 + c4 * 4); bv[ww] = *(LAS u16*)(lds + O_B + (c4 + ww) * 40 + tok * 2); }
        FENCE();
        const f32x4 v = gt * ((r[0] + r[1]) + (r[2] + r[3])) + (1.0f - gt) * (f32x4){bf2f(bv[0]), bf2f(bv[1]), bf2f(bv[2]), bf2f(bv[3])};
        __builtin_amdgcn_raw_buffer_store_b64((u32x2){pack2(v[0], v[1]), pack2(v[2], v[3])}, ru, sp && tok >= 8 ? OOB : (tok * DM + 64 * nb + c4) * 2, t0 * (DM * 2), OUT_AUX);
        S4S(4)
        TT(u >= 119, 3)
        TT(u >= 105 && u < 112, 4)
    }
}

constexpr int L_MEMS = 0;
constexpr int L_Q = 9216;
constexpr int L_KO = L_Q + 2560;
constexpr int L_KX = 16384;
constexpr int L_W = 32768;
constexpr int C_RING = 8;
static_assert(S_TOTAL <= L_SINK && L_W + C_RING * 16384 <= L_TOTAL && L_KO + 8 * 576 <= L_KX, "LDS budget");
static_assert(P_STG + P_RING * 16384 <= L_TOTAL, "LDS budget");

DI int pos2col(int p) { return p < 512 ? (p & ~63) + 16 * (p & 3) + ((p & 63) >> 2) : p; }
DI void unit_rc(int u, int& i, int& p0) {
    if (u < 512) { i = 2 * ((u >> 2) & 3) + (u & 1); p0 = (u & ~15) + 4 * (u & 2); }
    else { const int v = u - 512; i = (v >> 1) & 7; p0 = 512 + 16 * (v >> 4) + 8 * (v & 1); }
}
DI void mem0_arrange(LAS char* lds, int raw, int tid) {
#pragma unroll
    for (int k = 0; k < 2; ++k) {
        int i, p0; unit_rc(tid + 256 * k, i, p0);
        unsigned r[4];
#pragma unroll
        for (int j = 0; j < 4; ++j) r[j] = *(LAS unsigned*)(lds + raw + (i * DM + pos2col(p0)) * 2 + 32 * j);
        *(LAS u32x4*)(lds + L_MEMS + (tid + 256 * k) * 16) = (u32x4){__builtin_amdgcn_perm(r[1], r[0], 0x05040100), __builtin_amdgcn_perm(r[3], r[2], 0x05040100),
                                                                     __builtin_amdgcn_perm(r[1], r[0], 0x07060302), __builtin_amdgcn_perm(r[3], r[2], 0x07060302)};
    }
    int i, p0; unit_rc(512 + (tid & 63), i, p0);
    *(LAS u32x4*)(lds + L_MEMS + 8192 + (tid & 63) * 16) = *(LAS u32x4*)(lds + raw + (i * DM + p0) * 2);
}
DI unsigned read_canaries(int s, rsrc_t rs, int lane, int& budget) {
    const int o = 2 * OFF_CAN + (s & 3) * 16384 + (lane >> 2) * 512 + (lane & 3) * 16;
    u32x2 v;
    for (;;) {
        v = ld64(rs, o, 0);
        asm volatile("" ::: "memory");
        if (__all((v.y & 0xffff) == (unsigned)(s + 1))) break;
        if (--budget < 0) break;
    }
    const unsigned t = 8 * now();
    return t + (unsigned)wave_max32(min(max((int)(v.x - t), -0x4000), 0x4000));
}
DI u32x8 sm_frag(LAS char* lds, int sl, int r0, int sa, int sH, int cb, int lo8) {
    auto rd = [&](int r) { return tr_read(lds, sl + r * 256 + ((cb ^ (2 * swz(r))) * 16) + lo8); };
    const u32x2 a = rd(r0), b = rd(r0 + sa), c = rd(r0 + sH), d = rd(r0 + sa + sH);
    return (u32x8){a.x, a.y, b.x, b.y, c.x, c.y, d.x, d.y};
}
#define GS(slot, i)
#define GV(slot, i, v)
DI int tp_unit(int U, int sel) { return OFF_TP + cmap((U & 511) * 4096) + ((sel >> (4 + (U >> 8 & 1)) ^ U >> 9) & 1) * 8192; }
template <int N> DI void finish(const Args& a, rsrc_t rw, int U, int sel, unsigned t, int& budget) {
    const int lane = opaque_lane(), n = lane & 7, g = lane >> 4;
    const rsrc_t ru = __builtin_amdgcn_make_buffer_rsrc(a.out, 0, SEQ * DM * 2, 0x00020000);
    constexpr int M = (N + 1) / 2;
    int at[M]; f32x2 bv[M]; float gt[M];
#pragma unroll
    for (int i = 0; i < M; ++i) {
        const int u = U + 2 * i + (lane >> 3 & 1 & (N - 1));
        const int tok = LATE * SEG + (U < 512 ? 2 * (u & 15) + (n >> 2) : (u & 32) + 4 * (u & 7) + (n >> 1));
        const int col = U < 512 ? 32 * (u >> 4) + 16 * (g >> 1) + 4 * (n & 3) + 2 * (g & 1) : 512 + 2 * (u & 24) + 8 * (n & 1) + 2 * g;
#pragma unroll
        for (int r = 0; r < 2; ++r) bv[i][r] = bf2f(__builtin_amdgcn_raw_buffer_load_b16(rw, col * BO_ROW + tok * 2, OFF_BO + r * BO_ROW, 16));
        gt[i] = __uint_as_float(ld32(rw, tok * 4, OFF_GT));
        at[i] = (tok * DM + col) * 2;
    }
    u32x4 one;
#pragma unroll
    for (int j = 0; j < 4; ++j) one[j] = (lane >> 2 & 3) == j ? 0x3F80u << 16 * (lane & 1) : 0u;
#pragma unroll
    for (int i = 0; i < M; ++i) { bv[i] *= 1.0f - gt[i]; asm volatile("" : "+v"(bv[i]), "+v"(one)); }
    (void)wait_until(t);
    f32x4 v[N];
    for (;;) {
        u32x4 d[4 * N];
#pragma unroll
        for (int k = 0; k < 4 * N; ++k) d[k] = ld128(rw, lane * 16 + (k & 3) * 1024, tp_unit(U + (k >> 2), sel));
        unsigned top = 0;
#pragma unroll
        for (int i = 0; i < N; ++i) {
            asm volatile(M16 "%0, %1, %2, 0\n\t" M16 "%0, %1, %3, %0\n\t" M16 "%0, %1, %4, %0\n\t" M16 "%0, %1, %5, %0\n\ts_nop 7" : "=&v"(v[i]) : "v"(one), "v"(d[4 * i]), "v"(d[4 * i + 1]), "v"(d[4 * i + 2]), "v"(d[4 * i + 3]));
            top = mx4(mx4(mx4(mx4(top, d[4 * i]), d[4 * i + 1]), d[4 * i + 2]), d[4 * i + 3]);
        }
        if (!__any(top == SENT) || --budget < 0) break;
        __builtin_amdgcn_s_sleep(4);
    }
#pragma unroll
    for (int i = 0; i < M; ++i) {
        f32x2 x;
#pragma unroll
        for (int r = 0; r < 2; ++r) {
            auto both = [](float y) { return y + __int_as_float(__builtin_amdgcn_update_dpp(0, __float_as_int(y), 0x128, 0xf, 0xf, true)); };
            const float p = both(v[2 * i][r]), q = both(v[(2 * i + 1) & (N - 1)][r]);
            x[r] = gt[i] * (lane & 8 ? q : p) + bv[i][r];
        }
        __builtin_amdgcn_raw_buffer_store_b32(pack2(x[0], x[1]), ru, at[i], 0, OUT_AUX);
    }
}
#define TOUCH_M 15
DI void owner_loop(const Args& a, unsigned lds_base, int G, rsrc_t rs, rsrc_t rw, int sel, unsigned tbase, unsigned cbase, int& budget) {
    const int lane = threadIdx.x & 63, n = lane & 15, g = lane >> 4;
    const bool ex = G >= 64;
    const u32x4 sent4 = {SENT, SENT, SENT, SENT};
    const int js = (n & 2) ? 8 : ex ? (n & 4) + 2 * (n & 1) + (n >> 3) : 2 * (n >> 2) + (n & 1);
    u32x4 selu;
#pragma unroll
    for (int j = 0; j < 4; ++j) selu[j] = (js == 2 * j ? 0x3F80u : 0u) | (js == 2 * j + 1 ? 0x3F800000u : 0u);
    const int o = (part_unit(G, 0) ^ (sel & 8) * 256) + lane * 16;
    const int so = (n >> 3) * 16384 + ((ex ? 32768 + (G & 4) * 512 + (G & 3) * 128 : (G & 16) * 256 + (G & 32) * 64 + (G & 15) * 128) ^ (~sel & 8) * 256) + (g & 1) * 4 +
                   (ex ? (n & 4) * 16 + (g >> 1) * 32 + (n & 3) * 8 : (n & 6) * 16 + (g >> 1) * 16 + (n & 1) * 8);
    unsigned fails = 0;
    const int c8 = G >= 64 ? a.c1 >> 16 & 255 : (G & 28) == 0 ? a.c1 >> 8 & 255 : a.c1 & 255;
    const bool gv_on = true; (void)gv_on;
    unsigned oT = now(), oC = (unsigned)__builtin_readcyclecounter(), oR;
    { const unsigned r = (unsigned)(512.0f * (float)(oC - cbase) * __builtin_amdgcn_rcpf((float)(oT - tbase))); asm volatile("s_nop 0\n\tv_readfirstlane_b32 %0, %1" : "=s"(oR) : "v"(r)); }
    oT = 8 * oT + 16;
    unsigned lk = oC;
    budget = __builtin_amdgcn_readfirstlane(budget);
    const int ca = 2 * OFF_CAN + (lane >> 2) * 512 + (blockIdx.x & (NCP - 1)) * 128 + (lane & 3) * 16;
    unsigned tc;
    const unsigned oR0 = oR;
    oR = 0;
#pragma unroll 1
    for (int s = -1; s < NSEG - 1; ++s) {
        asm volatile("" : "+s"(s));
        const int pb = (s & 1) * 2 * PART_STEP;
        int np;
        asm volatile(OWNER_ASM : [np] "=&s"(np), [tm] "=&s"(tc), [bud] "+s"(budget), [oT] "+s"(oT), [oC] "+s"(oC), [oR] "+s"(oR), [lk] "+s"(lk)
            : [c8] "s"(c8), [ca] "v"(s < 0 ? OOB : ca), [cs] "s"((s & 3) * 16384), [tag] "s"(s + 1), [o] "v"(s < 0 ? OOB : o), [so] "v"(s < 0 ? OOB : so), [sel] "v"(selu), [rs] "s"(rs), [pb] "s"(pb), [mb0] "s"(2 * (OFF_MEMB + ((s + 1) & 3) * MEMB_STRIDE)),
              [cw] "s"(a.cw - c8), [rd] "s"(a.rd), [sv] "v"(sent4), [ro] "v"(s < 0 ? OOB : o), [pq] "s"(2 * PART_STEP - pb), [rm] "v"(s < 0 ? OOB : so), [mq] "s"(2 * (OFF_MEMB + ((s + 2) & 3) * MEMB_STRIDE)),
              [rw] "s"(rw), [rt] "v"(s >= 24 && s < 56 ? lane * 16 : OOB), [tq] "s"(tp_unit(8 * G + (s >> 2 & 7), sel) + (s & 3) * 1024) : OWNER_CLOBBER);
        if (s < 0) { oR = oR0; continue; }
        if ((s & TOUCH_M) == 8) {
            const char* pc; int d, len;
            asm volatile("s_getpc_b64 %0\n1:\ts_mov_b32 %1, .Lcold-1b+2048\n\ts_mov_b32 %2, .Lk_end-1b+2048" : "=s"(pc), "=s"(d), "=s"(len));
            touch(pc - 2048, max(min((G & 4 ? d + (G & 8) * 1024 : 0) + lane * 128, len), 0), lds_base);
        }
        fails += np > 1;
    }
    wait_prog(rw, F_S4 + lane * 4, true, 1, budget);
    const unsigned t = now(), t0 = t + min((int)(tc - 8 * t) >> 3, 100);
    finish<4>(a, rw, 4 * G + 4 * (G & 64), sel, t0 + (a.xa >> 8 & 255), budget);
    if (G >= 32 && G < 64) finish<1>(a, rw, 512 + G, sel, t0 + (a.xb >> 24), budget);
}
#define CORNER 5
DI void role_owner(const Args& a, unsigned lds_base, int ow, rsrc_t rw, int& budget) {
    const int tid = threadIdx.x, lane = tid & 63, w = __builtin_amdgcn_readfirstlane(tid >> 6);
    const unsigned tbase = now(), cbase = (unsigned)__builtin_readcyclecounter();
    if (ow < 56 && w) { prefetch(a, lds_base, ow & 3, (ow >> 2) * 3 + w - 1, 42); VM_DRAIN(); }
    if (w >= 2 || (w == 1 && (ow & ~11))) return;
    const u32x2 tag = launch_tag(a);
    tell_side(rw, tag);
    if (ow >= 56) {
        auto rtt = [&](int o) {
            unsigned long long t0, t1;
            asm volatile("s_memtime %0\n\ts_waitcnt lgkmcnt(0)" : "=s"(t0) :: "memory");
            st32(SENT, rw, lane == 0 ? 0 : OOB, o);
            asm volatile("s_waitcnt vmcnt(0)\n\ts_memtime %0\n\ts_waitcnt lgkmcnt(0)" : "=s"(t1) :: "memory");
            return (unsigned)(t1 - t0);
        };
        const int p = ow - 56, o = p == 0 ? 0 : p == 1 ? 32768 : p < 4 ? OFF_TP + (p - 2) * (2 << 20) : (p & 2) * 4096 + 1024 + (p & 1) * 1024;
        const int d = p == 1 ? 2048 : p < 4 ? 8192 : p & 1 ? 16384 : 4096;
        unsigned t0 = ~0u, t1 = ~0u;
        for (int r = 0; r < CORNER; ++r) { t0 = min(t0, rtt(o + r * 128)); t1 = min(t1, rtt(o + d + r * 128)); }
        const unsigned near = (t1 < t0) ^ (p == 1 ? die_side() : p > 4);
        __builtin_amdgcn_raw_buffer_store_b64((u32x2){(tag.x & ~7u) | near, tag.y}, rw, lane == 0 ? F_INIT + (105 + ow) * 128 : OOB, 0, 16);
    }
    const int sel = wait_init(rw, tag, budget, 161) ^ a.c1 >> 24;
    const rsrc_t rs = __builtin_amdgcn_make_buffer_rsrc(a.ws + (sel & 1) * 8192, 0, COMM_BYTES - 8192, 0x00020000);
    const int side = agree_side(rw, tag, ~tag, budget), cc = sel >> 1;
    const int G = (w ? 64 + 4 * side : 32 * side + (ow >> 1 & 24) + (ow & 4)) + ((cc ^ ow >> 1 ^ w) & 1) + ((cc ^ 2 * ow ^ ow >> 4) & 2);
    owner_loop(a, lds_base, G, rs, rw, sel, tbase, cbase, budget);
}

template <int O, class V> DI void put8(V& d, u32x8 f) {
#pragma unroll
    for (int j = 0; j < 8; ++j) d[O + j] = f[j];
}
DI f32x4 SMF64(u32x4 a, u32x8 b, f32x4 c, int i) { asm volatile("s_nop 1\n\tv_smfmac_f32_16x16x64_bf16 %0, %1, %2, %3\n\ts_nop 7" : "+v"(c) : "v"(a), "v"(b), "v"(i)); return c; }
DI f32x4 SMF32(u32x2 a, u32x4 b, f32x4 c, int i) { asm volatile("s_nop 1\n\tv_smfmac_f32_16x16x32_bf16 %0, %1, %2, %3\n\ts_nop 7" : "+v"(c) : "v"(a), "v"(b), "v"(i)); return c; }
DI f32x4 MFV(u32x4 a, u32x4 b, f32x4 c) { asm volatile("s_nop 1\n\tv_mfma_f32_16x16x32_bf16 %0, %1, %2, %0\n\ts_nop 7" : "+v"(c) : "v"(a), "v"(b)); return c; }

DI void role_head(const Args& a, LAS char* lds, unsigned lds_base, int h, rsrc_t rw, int& budget) {
    const int tid = threadIdx.x, w = __builtin_amdgcn_readfirstlane(tid >> 6);
    char* ws = a.ws;
    const unsigned tbase = now(), cbase = (unsigned)__builtin_readcyclecounter();
    int tw = 0;
    u32x32 A0, A1, A2, A3, A4, A5, A6, A7, R0, R1, R2, R3; u32x8 R4;
    auto dma = [&](int voff, int soff, unsigned dst) { bdma(rw, voff, soff, dst); };
    auto fetch_kv = [&](int s, int lane) {
        const int n = lane & 15, g = lane >> 4;
        const int kxs = (g >> 1) * 1024 + ((g & 1) * 32 + 16 * (n >> 3) + (n & 7)) * 16;
        const int vxs = (g >> 1) * 1024 + ((g & 1) * 32 + n) * 16;
        const int so_ = (s * 32 + h) * 8192 + w * 2048;
        const unsigned xdst = lds_base + L_KX + w * 2048;
        dma(kxs, OFF_KF + so_, xdst); dma(kxs, OFF_KF + so_ + 128, xdst + 1024);
        dma(vxs, OFF_VF + so_, xdst + 8192); dma(vxs, OFF_VF + so_ + 256, xdst + 8192 + 1024);
    };
    u32x2 tag, in;
    unsigned wo_at;
    u32x8 S1, S2, S3, scon;
    int sel, kv_rdy = 0, tp, tq_, tx, U1, ua;
    auto setup = [&]() {
        sel = (init_ok(in, tag) ? init_sel(in) : wait_init(rw, tag, budget, 33 + 4 * h + w)) ^ a.c1 >> 24;
        R0[14] ^= (sel & 8) * 256; R0[15] ^= (sel & 8) * 256;
        scon[0] = w; scon[1] = 2 * OFF_CAN + (h >> 1) * 512 + (2 * (h & 1) + tw) * 16; scon[2] = F_KV + h * 512 + w * 128; scon[3] = 65536 + w * 4096;
        scon[4] = OFF_KF + h * 8192 + w * 2048; scon[5] = F_KVM + h * 256 + tw * 128; scon[6] = tw - 1; scon[7] = (tw ? OFF_VM : OFF_KM) + (h - NH) * 2048 + w * 512;
        const unsigned t = now(), c = (unsigned)__builtin_readcyclecounter();
        const float r = (float)(c - cbase) * __builtin_amdgcn_rcpf(8.0f * (float)(t - tbase));
        S1 = (u32x8){0u, (unsigned)a.e0, 0u, (unsigned)budget, (unsigned)a.d0, (unsigned)(4294967296.0f * __builtin_amdgcn_rcpf(r)), (unsigned)(a.c2 + a.b0), 0u};
        S2 = (u32x8){0u, 8 * t + 16, 0u, (unsigned)a.up, (unsigned)a.dn, (unsigned)a.c2, 0u, 1u};
        S3 = (u32x8){c, (unsigned)(4096.0f * r), 2u * OFF_MEMB + ((h >> 3 ^ sel >> 2) & 1) * 16384 + (w & 2) * 2048 + (~sel & 8) * 256 + (w & 1) * 1024, w == 3 ? ~0u : 0u};
        const int U0 = (2 * tw + (w >> 1)) * 64 + 8 * (w & 1);
        U1 = 4 * (2 * h + tw) + w;
        tp = tp_unit(U0, sel) + h * 128; tq_ = tp_unit(256 + U0, sel) + h * 128; tx = tp_unit(512 + (U0 >> 3) + 3 * (w & 1), sel) + h * 128; ua = tp_unit(256 + U1, sel);
        asm volatile("" : "+{v[0:7]}"(S1), "+{v[8:15]}"(S2), "+{v[16:23]}"(S3), "+{v[24:31]}"(scon), "+{v[120:151]}"(R0), "+s"(tp), "+s"(tq_), "+s"(tx), "+s"(U1), "+s"(ua), "+s"(sel));
    };
    {
        const int lane = tid & 63, n = lane & 15, g = lane >> 4;
#pragma unroll
        for (int j = 0; j < 3; ++j) glds16(a.mem0, min(4 * j + w, 8) * 1024 + lane * 16, lds_base + L_KX + min(4 * j + w, 8) * 1024);
        const int tq = n >> 2, tc = (n & 3) >> 1, lo8 = (n & 1) * 8;
        const int r_nat = 16 * (g >> 1) + 4 * (g & 1) + (tq & 1) + 8 * (tq >> 1);
        const int r_sig = 16 * (tq & 1) + 4 * (g >> 1) + (g & 1) + 2 * (tq >> 1);
        const int r_o = 16 * (tq & 1) + 8 * (g >> 1) + 4 * (g & 1) + (tq >> 1);
#define WFRAG(sl, kb, t) ((kb) < 8 ? sm_frag(lds, sl, r_sig, 32, 8, 4 * w + 2 * (t) + tc, lo8) : sm_frag(lds, sl, r_nat, 2, 32, 4 * w + 2 * (t) + tc, lo8))
        const u32x4 ro = ring_offs(w, lane, HD);
        auto issue = [&](int i) {
            const unsigned slot = lds_base + L_W + (i % C_RING) * 16384 + w * 4096;
            if (i == 23) ring_issue(a.Wo + (size_t)h * DH * DM + 512, ring_offs(w, lane, [](int r, int c) { return (r + 64 * (c >> 3)) * (DM * 2) + (c & 7) * 16; }), slot);
            else if (i < 28) ring_issue((i < 8 || i == 20 ? a.Wq : i < 16 || i == 21 ? a.Wk : a.Wv) + (size_t)(64 * (i < 16 ? i & 7 : i < 20 ? i - 12 : i < 24 ? 8 : i - 24)) * HD + h * DH, ro, slot);
            else ring_issue(a.Wo + (size_t)(h * DH + 64 * ((i & 1) ^ ((i - 28) >> 1))) * DM + 256 * tw + 128 * (i & 1), ring_offs(w, lane, DM), slot);
        };
        auto keep = [&](int at, u32x8 f) {
            *(LAS u32x4*)(lds + at + lane * 16) = (u32x4){f[0], f[1], f[2], f[3]};
            *(LAS u32x4*)(lds + at + 1024 + lane * 16) = (u32x4){f[4], f[5], f[6], f[7]};
        };
        u32x8 hold[8] = {};
#pragma unroll
        for (int i = 0; i < C_RING; ++i) issue(i);
        tag = launch_tag(a);
        tell_side(rw, tag);
        const int ai = 2 * (n >> 2) + (n & 1), ahf = (n >> 1) & 1;
        R0[0] = R0[1] = R0[2] = R0[3] = 0x3F803F80u;
        R0[4] = ahf ? 0xEEEE : 0x4444;
        R0[5] = (g >> 1) * 512 + (n >> 2) * 128 + (g & 1) * 64 + (n & 3) * 16;
        R0[6] = lds_base + L_MEMS + 8192 + g * 256 + ai * 32 + ahf * 16;
        R0[7] = lane * 16;
        R0[8] = lds_base + L_Q + 2 * g * 320 + (16 * w + n) * 4;
        R0[9] = lds_base + L_KO + 2 * g * 576 + (16 * w + (n & 8) + 4 * (n & 1) + ((n >> 1) & 3)) * 4;
        R0[10] = lds_base + L_Q + ai * 320 + g * 16;
        R0[11] = lds_base + L_KO + ai * 576 + g * 32 + ahf * 16;
        R0[12] = lds_base + L_KX + 8192 + w * 2048 + (g >> 1) * 256 + n * 16 + (g & 1) * 8;
        R0[13] = lds_base + L_W + w * 4096 + lane * 16;
        wo_at = lds_base + L_W + (4 + (w >> 1)) * 16384 + r_o * 256 + ((8 * (w & 1) + tc) ^ (2 * swz(r_o))) * 16 + lo8;
        asm volatile("" : "+{v[120:151]}"(R0), "+{v119}"(wo_at));
        u32x2 other;
        ring_wait<4 * C_RING + 1>();
        mem0_arrange(lds, L_KX, tid);
        static_for<7>([&](auto ic) {
            constexpr int G = decltype(ic)::value;
            ring_wait<(G == 2 || G == 5 || G == 6 ? 16 : 17)>();
            if constexpr (G == 5) { asm volatile("" : "+v"(other)); tw = agree_side(rw, tag, other, budget); }
            const int sl = L_W + (G & 1) * 65536;
            auto fr = [&](int j, int t) { return WFRAG(sl + ((j ^ w) << 14), 0, t); };
            if constexpr (G < 4) {
                u32x32& A = G == 0 ? A0 : G == 1 ? A2 : G == 2 ? A4 : A6;
                u32x32& B = G == 0 ? A1 : G == 1 ? A3 : G == 2 ? A5 : A7;
                put8<0>(A, fr(0, 0)); put8<8>(A, fr(0, 1)); put8<16>(A, fr(1, 0)); put8<24>(A, fr(1, 1));
                put8<0>(B, fr(2, 0)); put8<8>(B, fr(2, 1)); put8<16>(B, fr(3, 0)); put8<24>(B, fr(3, 1));
                if constexpr (G == 0) asm volatile("" : "+{a[0:31]}"(A0), "+{a[32:63]}"(A1));
                if constexpr (G == 1) asm volatile("" : "+{a[64:95]}"(A2), "+{a[96:127]}"(A3));
                if constexpr (G == 2) asm volatile("" : "+{a[128:159]}"(A4), "+{a[160:191]}"(A5));
                if constexpr (G == 3) asm volatile("" : "+{a[192:223]}"(A6), "+{a[224:255]}"(A7));
            } else if constexpr (G == 4) {
                put8<16>(R1, fr(0, 0)); put8<24>(R1, fr(0, 1)); put8<0>(R2, fr(1, 0)); put8<8>(R2, fr(1, 1));
                put8<16>(R2, fr(2, 0)); put8<24>(R2, fr(2, 1)); put8<0>(R3, fr(3, 0)); put8<8>(R3, fr(3, 1));
            } else if constexpr (G == 5) {
                put8<16>(R0, WFRAG(sl, 8, 0)); put8<24>(R0, WFRAG(sl, 8, 1)); put8<0>(R1, WFRAG(sl + 16384, 8, 0)); put8<8>(R1, WFRAG(sl + 16384, 8, 1));
                put8<16>(R3, WFRAG(sl + 32768, 8, 0)); put8<24>(R3, WFRAG(sl + 32768, 8, 1));
                hold[7] = sm_frag(lds, sl + 49152, r_o, 2, 32, 8 * (w >> 1) + 4 * tw + 2 * (w & 1) + tc, lo8);
                R0[14] = part_unit((2 * (4 * tw + w) + (n >> 3)) * 4 + g, h) + (n & 7) * 16;
                R0[15] = (g >> 1) == (w >> 1) ? part_unit(64 + 2 * (2 * tw + (w & 1)) + (g >> 1), h) + (g & 1) * 64 + n * 4 : OOB;
                asm volatile("" : "+{v[120:151]}"(R0));
            } else if constexpr (G == 6) {
#pragma unroll
                for (int j = 0; j < 7; ++j) hold[j] = fr(j >> 1, j & 1);
                R4 = fr(3, 1);
            }
            WG_BARRIER();
            if constexpr (G == 4) asm volatile("buffer_load_dwordx2 %0, %1, %2, 0 offen sc1" : "=v"(in) : "v"(init_at(33 + 4 * h + w)), "s"(rw) : "memory");
            if constexpr (G < 6)
#pragma unroll
                for (int i = 0; i < 4; ++i) issue(4 * G + 8 + i);
            if constexpr (G == 2) asm volatile("buffer_load_dwordx2 %0, off, %1, %2 sc1" : "=v"(other) : "s"(rw), "s"(side_at(16)) : "memory");
            if constexpr (G == 5) { asm volatile("" : "+v"(in)); setup(); }
            if constexpr (G == 6)
#pragma unroll
                for (int j = 0; j < 8; ++j) keep(L_W + (j >> 1) * 16384 + w * 4096 + (j & 1) * 2048, hold[j]);
        });
        asm volatile("" : "+{v[120:151]}"(R0), "+{v[152:183]}"(R1), "+{v[184:215]}"(R2), "+{v[216:247]}"(R3), "+{v[248:255]}"(R4));
    }
    const bool gv_on = tw == 0; (void)gv_on;
    const rsrc_t rs = __builtin_amdgcn_make_buffer_rsrc(ws + (sel & 1) * 8192, 0, COMM_BYTES - 8192, 0x00020000);
    const int fo = F_KVM + h * 256 + tw * 128;
    u32x2 vm = {};
    for (;;) {
        asm volatile(STEP_ASM
            : "+{v[96:97]}"(vm), "+{v[0:7]}"(S1), "+{v[8:15]}"(S2), "+{v[16:23]}"(S3), "+{v[24:31]}"(scon)
              , "+{a[0:31]}"(A0), "+{a[32:63]}"(A1), "+{a[64:95]}"(A2), "+{a[96:127]}"(A3), "+{a[128:159]}"(A4), "+{a[160:191]}"(A5), "+{a[192:223]}"(A6), "+{v[120:151]}"(R0), "+{v[152:183]}"(R1)
            : "{a[224:255]}"(A7), "{v[184:215]}"(R2), "{v[216:247]}"(R3), "{v[248:255]}"(R4), "{v119}"(wo_at),
              [rs] "s"(rs), [rw] "s"(rw), [qb] "s"(OFF_OF + LATE * 262144 + h * 8192 + (w & 1) * 256),
              [tp] "s"(tp), [tq] "s"(tq_), [tx] "s"(tx), [u] "s"(U1), [ua] "s"(ua), [fw] "s"(8 * (a.xb >> 16 & 255)), [ru] "s"(__builtin_amdgcn_make_buffer_rsrc(a.out, 0, SEQ * DM * 2, 0x00020000))
            : STEP_CLOBBER);
        const int s = __builtin_amdgcn_readfirstlane(S1[0]);
        const unsigned st = __builtin_amdgcn_readfirstlane(S1[7]);
        kv_rdy = __builtin_amdgcn_readfirstlane(S1[2]); budget = __builtin_amdgcn_readfirstlane(S1[3]);
        if (s == NSEG - 1) break;
        if (__builtin_expect(st & 2, 0)) {
            const int lane = opaque_lane();
            LAS char* z = (LAS char*)0;
            const f32x4 zero = {0, 0, 0, 0};
            const u32x4 ones = __builtin_shufflevector(R0, R0, 0, 1, 2, 3);
            const unsigned zr = lane >> 6;
            const int idx = R0[4];
            u32x4 qf[4];
            f32x4 s1 = zero, s2 = zero;
#pragma unroll
            for (int ks = 0; ks < 4; ++ks) {
                qf[ks] = *(LAS u32x4*)(z + R0[10] + ks * 64);
                s1 = MFV(*(LAS u32x4*)(lds + L_KX + lane * 16 + ks * 2048), qf[ks], s1);
                s2 = MFV(*(LAS u32x4*)(lds + L_KX + lane * 16 + ks * 2048 + 1024), qf[ks], s2);
            }
            f32x4 s0 = SMF64(*(LAS u32x4*)(z + R0[11]), __builtin_shufflevector(qf[0], qf[1], 0, 1, 2, 3, 4, 5, 6, 7), zero, idx);
            s0 = SMF64(*(LAS u32x4*)(z + R0[11] + 128), __builtin_shufflevector(qf[2], qf[3], 0, 1, 2, 3, 4, 5, 6, 7), s0, idx);
            const float sa = s0[0] + s0[2], sb = s0[1] + s0[3];
            float mx = fmaxf(fmaxf(fmaxf(fmaxf(s1[0], s1[1]), fmaxf(s1[2], s1[3])), fmaxf(fmaxf(s2[0], s2[1]), fmaxf(s2[2], s2[3]))), fmaxf(sa, sb));
            mx = fmaxf(mx, __shfl_xor(mx, 16)); mx = fmaxf(mx, __shfl_xor(mx, 32));
            auto ex2 = [&](float x, float y) { return pack2(__builtin_amdgcn_exp2f(x - mx), __builtin_amdgcn_exp2f(y - mx)); };
            const u32x4 px = {ex2(s1[0], s1[1]), ex2(s1[2], s1[3]), ex2(s2[0], s2[1]), ex2(s2[2], s2[3])};
            const unsigned pm = ex2(sa, sb);
            const f32x4 l = MFV(((u32x4){pm, zr, zr, zr}), ones, MFV(px, ones, zero));
            const u32x2 x0 = *(LAS u32x2*)(z + R0[12]), y0 = *(LAS u32x2*)(z + R0[12] + 1024), x1 = *(LAS u32x2*)(z + R0[12] + 512), y1 = *(LAS u32x2*)(z + R0[12] + 1536);
            const f32x4 o = SMF64(px, ((u32x8){x0.x, y0.x, x0.y, y0.y, x1.x, y1.x, x1.y, y1.y}), zero, idx) + SMF32(((u32x2){pm, zr}), ((u32x4){vm.x, vm.y, zr, zr}), zero, idx);
            const float i0 = __builtin_amdgcn_rcpf(l[0]), i1 = __builtin_amdgcn_rcpf(l[1]);
            *(LAS unsigned*)(z + R0[9] + 256) = pack2(o[0] * i0, o[2] * i0);
            *(LAS unsigned*)(z + R0[9] + 256 + 576) = pack2(o[1] * i1, o[3] * i1);
            WG_BARRIER();
            const u32x4 oa = *(LAS u32x4*)(z + R0[11] + 256 + (w >> 1) * 128), ob = *(LAS u32x4*)(z + R0[11] + 384 - (w >> 1) * 128);
            auto frag = [&](int at) { return __builtin_shufflevector(*(LAS u32x4*)(z + at), *(LAS u32x4*)(z + at + 1024), 0, 1, 2, 3, 4, 5, 6, 7); };
            f32x2 f[4];
#pragma unroll
            for (int T = 0; T < 4; ++T) f[T] = fold(SMF64(ob, frag(R0[13] + scon[3] + 32768 + T * 2048), SMF64(oa, frag(R0[13] + scon[3] + T * 2048), zero, idx), idx));
            const f32x2 fx = fold(SMF64(ob, frag((R0[13] ^ 8192) + 49152 + 2048), SMF64(oa, frag(R0[13] + 49152 + 2048), zero, idx), idx));
            const int pb = (s & 1) * 2 * PART_STEP;
            st128((u32x4){pack2(f[0].x, f[1].x), pack2(f[2].x, f[3].x), pack2(f[0].y, f[1].y), pack2(f[2].y, f[3].y)}, rs, R0[14], pb);
            st32(pack2(fx.x, fx.y), rs, R0[15], pb);
            if (s == LATE) { S1[7] = 4; continue; }
        }
        if (__builtin_expect((st & 2) || kv_rdy <= s, 0)) {
            while (kv_rdy <= s) { kv_rdy = prog_min(rw, F_KV + h * 512 + w * 128); if (--budget < 0) break; }
            fetch_kv(s + 1, opaque_lane());
        }
        if (st & 2) { S1[1] = read_canaries(s, rs, opaque_lane(), budget) + S1[6]; S2[2] += 0x10000; }
        if ((int)(S1[1] - S2[1]) > 0x8000) { S2[1] = 8 * now() + 16; S3[0] = (unsigned)__builtin_readcyclecounter(); }
        S1[0] = s + 1; S1[2] = kv_rdy; S1[3] = budget;
    }
    TT(true, 0)
    st32((unsigned)(NSEG - 2), rw, opaque_lane() == 0 && w == 0 ? fo : OOB, 0);
    TT(true, 2)
}

__global__ void __launch_bounds__(256, 1) fused_kernel(Args a) {
    extern __shared__ __attribute__((aligned(16))) char smem[];
    LAS char* lds = (LAS char*)smem;
    const unsigned lds_base = (unsigned)(uintptr_t)lds;
    const int w = __builtin_amdgcn_readfirstlane(threadIdx.x >> 6);
    const int b = blockIdx.x, bx = b & 7, bi = b >> 3;
    if (bi >= 28) {
        const void* pc; unsigned len;
        asm volatile("s_getpc_b64 %0\n1:\ts_mov_b32 %1, .Lk_end-1b" : "=s"(pc), "=s"(len));
        touch(pc, min((bi - 28) * 32768 + threadIdx.x * 128, len), lds_base);
    }
    asm volatile("" :: "s"(a.Wq), "s"(a.cw));
    const rsrc_t rw = __builtin_amdgcn_make_buffer_rsrc(a.ws, 0, WS_USED, 0x00020000);
    int budget = SPIN_MAX;
#define RT(i)
    if (bx < 4) {
        if (bi < 16) role_head(a, lds, lds_base, 8 * bx + (bi >> 2) * 2 + (bi & 1), rw, budget);
        else role_owner(a, lds_base, 4 * (bi - 16) + bx, rw, budget);
    } else {
        const int fx = bx - 4, no = fx < 3 ? 14 : 22, j = 14 * fx + bi;
        if (bi >= no || (j & 2)) {
            const unsigned t = now() + (a.d1 & 0xffff);
            if (bi >= no) { if (w) prefetch(a, lds_base, bx, (bi - no) * 3 + w - 1, 3 * (32 - no)); }
            else {
                const int sh = (j >> 2) * 2 + (j & 1);
#pragma unroll
                for (int i = 0; i < 6; ++i) {
                    const int u = 6 * sh + i, c = u >> 3;
                    st128((u32x4){SENT, SENT, SENT, SENT}, rw, threadIdx.x * 16, (c < 18 ? c * 4 * PART_STEP : (c < 20 ? 2 * (OFF_MEMB + MEMB_STRIDE) - 18 * 32768 : 2 * OFF_CAN - 20 * 32768) + c * 32768) + (u & 7) * 4096);
                }
                const u32x2 tag = launch_tag(a);
                VM_DRAIN();
                WG_BARRIER();
                __builtin_amdgcn_raw_buffer_store_b64(tag, rw, threadIdx.x == 0 ? F_INIT + sh * 128 : OOB, 0, 16);
                RT(3)
            }
            (void)wait_until(t);
        }
        if (bi >= no) {
            const int t = 18 * fx + bi - no;
            role_s4d(a, lds, lds_base, t, rw, budget);
            RT(1)
            TT(true, 6)
            if (t < 9 * OUT_REP) role_outp(a, lds, lds_base, t % 9, t / 9, rw, budget);
        }
        else {
            if (!(j & 2)) role_kv(a, lds, lds_base, (j >> 2) * 2 + (j & 1), rw, budget);
            RT(1)
            TT(true, 6)
            role_qa(a, lds, lds_base, (j >> 2) * 2, (j & 2 ? 0 : FAST + 2) + (j & 1), (j & 2 ? FAST : FAST + 2) + (j & 1), rw, budget);
        }
    }
    RT(2)
    TT(true, 5)
    if (budget < 0 && threadIdx.x == 0) st32(0x1000u + b, rw, F_ERR, 0);
    asm volatile(".Lk_end:");
}

static int g_c1 = 34, g_c1m = 30, g_c1x = 46, g_c2 = 316, g_b0 = 32, g_up = 8, g_fl = 8, g_dn = 2, g_d0 = 1680, g_da = 200, g_ua = 48, g_d1 = 300, g_rd = 34, g_cw = 69, g_e0 = 112, g_x1 = 40, g_x2 = 140, g_x3 = 150, g_x4 = 75, g_x5 = 135, g_x6 = 157, g_x7 = 35, g_x8 = 210, g_pc = 45, g_cc = 0;
extern "C" void kernel_launch(void* const* d_in, const int* in_sizes, int n_in, void* d_out, int out_size, void* d_ws, size_t ws_size, hipStream_t stream) {
    Args a;
    a.x = (const u16*)d_in[0]; a.mem0 = (const u16*)d_in[1];
    a.Wq = (const u16*)d_in[2]; a.Wk = (const u16*)d_in[3]; a.Wv = (const u16*)d_in[4]; a.Wo = (const u16*)d_in[5];
    a.lAr = (const u16*)d_in[8]; a.Aim = (const u16*)d_in[9]; a.Cr = (const u16*)d_in[10]; a.Ci = (const u16*)d_in[11];
    a.ldt = (const u16*)d_in[12]; a.Dsk = (const u16*)d_in[13]; a.gw = (const u16*)d_in[14];
    a.out = (u16*)d_out;
    a.ws = (char*)(((uintptr_t)d_ws + ((2u << 20) - 1)) & ~(uintptr_t)((2u << 20) - 1));
    a.c1 = g_c1 | g_c1m << 8 | g_c1x << 16 | g_cc << 24; a.c2 = g_c2; a.d0 = g_d0 + g_da; a.b0 = g_b0; a.d1 = g_d1 | g_pc << 16; a.up = g_up | g_ua << 8 | g_fl << 16; a.dn = g_dn | g_d0 << 8; a.rd = 23 * g_rd - 300; a.cw = 8 * g_cw - 104; a.e0 = 8 * g_e0;
    a.xa = g_x1 | g_x2 << 8 | g_x5 << 16 | g_x7 << 24; a.xb = g_x6 | g_x3 << 8 | g_x4 << 16 | g_x8 << 24;
    (void)hipFuncSetAttribute((const void*)fused_kernel, hipFuncAttributeMaxDynamicSharedMemorySize, L_TOTAL);
    fused_kernel<<<256, 256, L_TOTAL, stream>>>(a);
}
```

```cpp
#include <hip/hip_runtime.h>
#include <hip/hip_bf16.h>
#include <stdint.h>

typedef unsigned short u16;
typedef __bf16 bf16x8 __attribute__((ext_vector_type(8)));
typedef __bf16 bf16x16 __attribute__((ext_vector_type(16)));
typedef _Float16 f16x2 __attribute__((ext_vector_type(2)));
typedef _Float16 f16x8 __attribute__((ext_vector_type(8)));
typedef short s16x4 __attribute__((ext_vector_type(4)));
typedef float f32x2 __attribute__((ext_vector_type(2)));
typedef float f32x4 __attribute__((ext_vector_type(4)));
typedef float f32x16 __attribute__((ext_vector_type(16)));
typedef unsigned u32x2 __attribute__((ext_vector_type(2)));
typedef unsigned u32x4 __attribute__((ext_vector_type(4)));
typedef unsigned u32x8 __attribute__((ext_vector_type(8)));
typedef unsigned u32x16 __attribute__((ext_vector_type(16)));
typedef unsigned u32x32 __attribute__((ext_vector_type(32)));
typedef short s16x8 __attribute__((ext_vector_type(8)));
#define LAS __attribute__((address_space(3)))
#define DI __device__ __forceinline__
typedef __amdgpu_buffer_rsrc_t rsrc_t;

constexpr int SEQ = 2048, DM = 576, NH = 32, DH = 128, HD = NH * DH, NM = 8, SEG = 32, NSEG = 64, NS = 128;
constexpr unsigned SENT = 0xFFFFFFFFu;
constexpr unsigned OOB = 0x7FFFFFF0u;
constexpr int SPIN_MAX = 1 << 20;

constexpr int PART_STEP = 16384;
constexpr int OFF_MEMB = 40 * PART_STEP;
constexpr int MEMB_STRIDE = 32768;
constexpr int OFF_CAN = 52 * PART_STEP;
constexpr int COMM_LOGICAL = OFF_CAN + 32768;
constexpr int COMM_BYTES = 2 * COMM_LOGICAL;
constexpr int F_ERR = COMM_BYTES;
constexpr int F_KV = F_ERR + 128;
constexpr int F_KVM = F_KV + 16384;
constexpr int F_O = F_KVM + 8192;
constexpr int F_H = F_O + 4096;
constexpr int F_S4 = F_H + 32768;
constexpr int F_DONE = F_S4 + 512;
constexpr int INIT_BYTES = 2 * OFF_CAN + 131072;
constexpr int F_INIT = INIT_BYTES;
constexpr int F_CL = F_INIT + 24576;
constexpr int F_EPOCH = F_CL + 16384;
static_assert(F_DONE + 128 <= INIT_BYTES, "");
static_assert(F_EPOCH + 128 <= (2 << 20), "hand-off region");
constexpr int OFF_TP = 2 << 20;
constexpr int OFF_KF = 6 << 20;
constexpr int OFF_VF = OFF_KF + SEQ * HD * 2;
constexpr int OFF_OF = OFF_VF + SEQ * HD * 2;
constexpr int OFF_KM = OFF_OF + SEQ * HD * 2;
constexpr int OFF_VM = OFF_KM + NSEG * NH * NM * DH * 2;
constexpr int FAST = NSEG - 6;
constexpr int LATE = NSEG - 2;
constexpr int OFF_BO = OFF_VM + NSEG * NH * NM * DH * 2;
constexpr int BO_ROW = 4352;
constexpr int OFF_GT = OFF_BO + DM * BO_ROW;
constexpr int OFF_DBG = OFF_GT + SEQ * 4;
constexpr int WS_USED = OFF_DBG + (4 << 20);

struct Args {
    const u16 *x, *mem0, *Wq, *Wk, *Wv, *Wo, *lAr, *Aim, *Cr, *Ci, *ldt, *Dsk, *gw;
    u16* out; char* ws;
    int c1, c2, d0, b0, d1, up, dn, rd, cw, e0;
    unsigned xa, xb;
};

DI float bf2f(u16 b) { return __uint_as_float((unsigned)b << 16); }
DI float bflo(unsigned w) { return __uint_as_float(w << 16); }
DI float bfhi(unsigned w) { return __uint_as_float(w & 0xFFFF0000u); }
DI u16 f2bf(float f) { return __builtin_bit_cast(u16, (__bf16)f); }
DI unsigned pack2(float lo, float hi) {
    typedef __bf16 bf16x2 __attribute__((ext_vector_type(2)));
    bf16x2 v = {(__bf16)lo, (__bf16)hi};
    return __builtin_bit_cast(unsigned, v);
}
#define WG_BARRIER() asm volatile("s_waitcnt lgkmcnt(0)\n\ts_barrier" ::: "memory")
#define VM_DRAIN() asm volatile("s_waitcnt vmcnt(0)" ::: "memory")
DI int opaque_lane() { int l; asm volatile("v_mbcnt_lo_u32_b32 %0, -1, 0\n\tv_mbcnt_hi_u32_b32 %0, -1, %0" : "=v"(l)); return l; }
DI int cmap(int o) { return (o & 0x1FFF) | ((o >> 13) << 14); }
DI int part_unit(int G, int h) { const int p = G < 64 ? G & 31 : 32 + (G & 3); return (p >> 2) * 131072 + cmap((p & 3) * 4096) + ((G >> (G < 64 ? 5 : 2) ^ p >> 3) & 1) * 2048 + (h >> 4) * 65536 + (h & 15) * 128; }
DI unsigned mx4(unsigned a, u32x4 v) { a = max(max(a, v.x), v.y); return max(max(a, v.z), v.w); }
DI unsigned now() { return (unsigned)__builtin_amdgcn_s_memrealtime(); }
DI unsigned wait_until(unsigned t) {
    unsigned v; int guard = 8192;
    do { v = now(); } while ((int)(t - v) >= 4 && --guard > 0);
    const int r = __builtin_amdgcn_readfirstlane((int)(t - v));
    asm volatile("s_cmp_lt_i32 %0, 1\n\ts_cbranch_scc1 1f\n\ts_nop 3\n\ts_cmp_lt_i32 %0, 2\n\ts_cbranch_scc1 1f\n\ts_nop 3\n\ts_cmp_lt_i32 %0, 3\n\ts_cbranch_scc1 1f\n\ts_nop 7\n1:" :: "s"(r) : "scc");
    return r > 0 ? t : v + 2;
}
DI void st128(u32x4 v, rsrc_t rs, int off, int soff) {
    __builtin_amdgcn_raw_buffer_store_b128(v, rs, off, soff, 16);
    asm volatile("s_nop 1" :: "v"(v) : "memory");
}
DI void st32(unsigned v, rsrc_t rs, int off, int soff) { __builtin_amdgcn_raw_buffer_store_b32(v, rs, off, soff, 16); }
DI u32x4 ld128(rsrc_t rs, int off, int soff) { return __builtin_amdgcn_raw_buffer_load_b128(rs, off, soff, 16); }
DI u32x2 ld64(rsrc_t rs, int off, int soff) { return __builtin_amdgcn_raw_buffer_load_b64(rs, off, soff, 16); }
DI unsigned ld32(rsrc_t rs, int off, int soff) { return __builtin_amdgcn_raw_buffer_load_b32(rs, off, soff, 16); }
DI f32x2 fold(f32x4 a) { return (f32x2){a[0], a[1]} + (f32x2){a[2], a[3]}; }
DI float dpp_xor1(float v) { return __int_as_float(__builtin_amdgcn_update_dpp(0, __float_as_int(v), 0xB1, 0xf, 0xf, true)); }
DI float red_sum_g(float v) {
    auto a = __builtin_amdgcn_permlane16_swap(__float_as_uint(v), __float_as_uint(v), false, false);
    v = __uint_as_float(a[0]) + __uint_as_float(a[1]);
    auto b = __builtin_amdgcn_permlane32_swap(__float_as_uint(v), __float_as_uint(v), false, false);
    return __uint_as_float(b[0]) + __uint_as_float(b[1]);
}
DI int wave_max32(int d) {
    d = max(d, __builtin_amdgcn_update_dpp(d, d, 0x121, 0xf, 0xf, false));
    d = max(d, __builtin_amdgcn_update_dpp(d, d, 0x122, 0xf, 0xf, false));
    d = max(d, __builtin_amdgcn_update_dpp(d, d, 0x124, 0xf, 0xf, false));
    d = max(d, __builtin_amdgcn_update_dpp(d, d, 0x128, 0xf, 0xf, false));
    return __builtin_amdgcn_readfirstlane(max(max(__builtin_amdgcn_readlane(d, 0), __builtin_amdgcn_readlane(d, 16)), max(__builtin_amdgcn_readlane(d, 32), __builtin_amdgcn_readlane(d, 48))));
}
DI u32x2 tr_read(LAS char* lds, int addr) { return __builtin_bit_cast(u32x2, __builtin_amdgcn_ds_read_tr16_b64_v4i16((LAS s16x4*)(lds + addr))); }
DI void wait_prog(rsrc_t rw, int off, bool act, int thr, int& budget) {
    for (;;) {
        const int v = (int)ld32(rw, off, 0);
        asm volatile("" ::: "memory");
        if (__all(!act || v >= thr)) break;
        if (--budget < 0) break;
        __builtin_amdgcn_s_sleep(1);
    }
}
DI u32x2 launch_tag(const Args& a) {
    unsigned long long z;
    asm volatile("s_load_dwordx2 %0, %1, 0x0\n\ts_waitcnt lgkmcnt(0)" : "=s"(z) : "s"(a.ws + F_EPOCH) : "memory");
    z += 0x9E3779B97F4A7C15ull;
    z = (z ^ (z >> 30)) * 0xBF58476D1CE4E5B9ull; z = (z ^ (z >> 27)) * 0x94D049BB133111EBull; z ^= z >> 31;
    return (u32x2){(unsigned)z, (unsigned)(z >> 32)};
}
DI int init_at(int more) { const int l = threadIdx.x & 63; return F_INIT + (l < 32 ? l : l < 56 || !more ? more : l + 105) * 128; }
DI bool init_ok(u32x2 v, u32x2 tag) { return __all(((v.x ^ tag.x) >> 3) == 0 && v.y == tag.y); }
DI int init_sel(u32x2 v) { const int m = __ballot(v.x & 1) >> 56; return (m & 1) + 2 * (m >> (4 + 2 * (m & 1)) & 3) + 4 * (m & 14); }
DI int wait_init(rsrc_t rw, u32x2 tag, int& budget, int more = 0) {
    u32x2 v;
    for (;;) {
        v = ld64(rw, init_at(more), 0);
        asm volatile("" ::: "memory");
        if (init_ok(v, tag) || --budget < 0) break;
        __builtin_amdgcn_s_sleep(2);
    }
    return init_sel(v);
}
DI unsigned die_side() { return (__builtin_amdgcn_s_getreg(4 | 14 << 6) ^ blockIdx.x >> 1) & 1; }
DI int side_at(int x) { const int b = blockIdx.x ^ x; return F_CL + ((b >> 3) * 4 + (b & 3)) * 128; }
DI void tell_side(rsrc_t rw, u32x2 tag) { __builtin_amdgcn_raw_buffer_store_b64((u32x2){(tag.x & ~1u) | die_side(), tag.y}, rw, threadIdx.x == 0 ? 0 : OOB, side_at(0), 16); }
DI int agree_side(rsrc_t rw, u32x2 tag, u32x2 v, int& budget) {
    while (!__all(((v.x ^ tag.x) >> 1) == 0 && v.y == tag.y) && --budget >= 0) { v = ld64(rw, 0, side_at(16)); asm volatile("" ::: "memory"); }
    const unsigned me = die_side();
    return (__builtin_amdgcn_readfirstlane(v.x) ^ me) & 1 ? me : blockIdx.x >> 4 & 1;
}
DI void glds16(const void* base, int voff, unsigned lds_dst) {
    unsigned keep;
    asm volatile("s_mov_b32 %0, m0\n\ts_mov_b32 m0, %3\n\ts_nop 0\n\tglobal_load_lds_dwordx4 %1, %2\n\ts_mov_b32 m0, %0" : "=&s"(keep) : "v"(voff), "s"(base), "s"(lds_dst) : "memory");
}
DI void bdma(rsrc_t rs, int voff, int soff, unsigned lds_dst) {
    unsigned keep;
    asm volatile("s_mov_b32 %0, m0\n\ts_mov_b32 m0, %4\n\ts_nop 0\n\tbuffer_load_dwordx4 %1, %2, %3 offen sc1 lds\n\ts_mov_b32 m0, %0" : "=&s"(keep) : "v"(voff), "s"(rs), "s"(soff), "s"(lds_dst) : "memory");
}
DI int swz(int r) { return ((r & 3) ^ (((r >> 2) & 3) << 1) ^ (((r >> 4) & 1) << 2)) & 7; }
template <class F> DI u32x4 ring_offs(int w, int lane, F at) {
    u32x4 o;
#pragma unroll
    for (int j = 0; j < 4; ++j) { const int r = 16 * w + 4 * j + (lane >> 4); o[j] = at(r, (lane & 15) ^ (2 * swz(r))) - 1024 * j; }
    return o;
}
DI u32x4 ring_offs(int w, int lane, int ld) { return ring_offs(w, lane, [&](int r, int c) { return r * ld * 2 + c * 16; }); }
DI void ring_issue(const u16* src, u32x4 o, unsigned slot) {
    unsigned keep;
    asm volatile("s_mov_b32 %0, m0\n\ts_mov_b32 m0, %6\n\ts_nop 0\n\tglobal_load_lds_dwordx4 %1, %5\n\tglobal_load_lds_dwordx4 %2, %5 offset:1024\n\t"
                 "global_load_lds_dwordx4 %3, %5 offset:2048\n\tglobal_load_lds_dwordx4 %4, %5 offset:3072\n\ts_mov_b32 m0, %0"
                 : "=&s"(keep) : "v"(o.x), "v"(o.y), "v"(o.z), "v"(o.w), "s"(src), "s"(slot) : "memory");
}
#define PF_FIRST 2
constexpr int L_TOTAL = 163840, L_SINK = L_TOTAL - 256;
DI void touch(const void* base, int voff, unsigned lds_base) {
    unsigned keep;
    asm volatile("s_mov_b32 %0, m0\n\ts_mov_b32 m0, %3\n\ts_nop 0\n\tglobal_load_lds_dword %1, %2\n\ts_mov_b32 m0, %0" : "=&s"(keep) : "v"(voff), "s"(base), "s"(lds_base + L_SINK) : "memory");
}
DI void prefetch(const Args& a, unsigned lds_base, int x, int id, int n) {
    const int lane = threadIdx.x & 63;
    const int h0 = x < 4 ? 8 * x : (0x160E0800 >> (8 * (x - 4))) & 255, lpr = x == 5 ? 12 : x == 7 ? 20 : 16, per = 9 * lpr;
    const int first = PF_FIRST * lpr, end = (x < 4 ? 4 : 2) * per;
    const float rl = __builtin_amdgcn_rcpf((float)lpr);
    if (x >= 4 && id < 18) touch(a.x, (id * 64 + lane) * 128, lds_base);
    for (int i = first + id; i < end; i += n) {
        int m = (i >= per) + (i >= 2 * per) + (i >= 3 * per), j = i - m * per;
        if (x < 4 && m < 3) { const int b = i >> 4; m = b < 20 ? b >> 3 : min(b - 20, 2); j = (b < 16 ? b & 7 : b < 20 ? b - 12 : b < 23 ? 8 : b - 23) * 16 + (i & 15); }
        const int mm = m + (x >= 4);
        const int L = j * 64 + lane, row = (int)((L + 0.5f) * rl);
        touch(mm == 0 ? a.Wq : mm == 1 ? a.Wk : mm == 2 ? a.Wv : a.Wo, m == 3 ? h0 * (DH * DM * 2) + L * 128 : row * (HD * 2) + (L - row * lpr) * 128 + h0 * 256, lds_base);
    }
}
template <int N> DI void ring_wait() { asm volatile("s_waitcnt vmcnt(%0) lgkmcnt(0)\n\ts_barrier" :: "i"(N) : "memory"); }
template <int I> struct ic_t { static constexpr int value = I; };
template <int N, int I = 0, class F> DI void static_for(F&& f) { if constexpr (I < N) { f(ic_t<I>{}); static_for<N, I + 1>(f); } }
#define MFMA32(a, b, c) __builtin_amdgcn_mfma_f32_32x32x16_bf16(__builtin_bit_cast(bf16x8, a), __builtin_bit_cast(bf16x8, b), c, 0, 0, 0)
#define MFMA16(a, b, c) __builtin_amdgcn_mfma_f32_16x16x32_bf16(__builtin_bit_cast(bf16x8, a), __builtin_bit_cast(bf16x8, b), c, 0, 0, 0)

constexpr int S_CH = 9;
constexpr int S_XRS = 4096 + 32;
constexpr int S_XR = 0;
constexpr int S_RC = S_CH * S_XRS;
constexpr int S_RCS = 4192;
constexpr int S_KD = S_RC + 4 * S_RCS;
constexpr int S_K = S_KD + 48 * 128;
constexpr int S_KROW = 96;
constexpr int S_TAB = S_K + 79 * S_KROW;
constexpr int S_TROW = 544;
constexpr int S_RED = S_TAB + 57 * S_TROW;
constexpr int S_TOTAL = S_RED + 4 * 64 * 144;
static_assert(S_RC % 16 == 0 && S_TAB % 16 == 0 && S_RED % 16 == 0, "");
DI f32x2 crot(f32x2 z) { return (f32x2){-z.y, z.x}; }
DI f32x2 cmul(f32x2 a, f32x2 z) { return __builtin_elementwise_fma(a.yy, crot(z), a.xx * z); }
DI f32x2 cexp_p(float ar, float ai, float p) {
    const float x = ai * p;
    const float hi = x * 0.15915494309189535f;
    const float lo = __builtin_fmaf(x, 0.15915494309189535f, -hi) + x * 6.420638316725915e-9f;
    const float rev = (hi - __builtin_rintf(hi)) + lo;
    const float m = __builtin_amdgcn_exp2f(ar * p * 1.4426950408889634f);
    return (f32x2){m * __builtin_amdgcn_cosf(rev), m * __builtin_amdgcn_sinf(rev)};
}
#define FENCE() __builtin_amdgcn_sched_barrier(0)
#define TT(c, i)
DI void role_s4d(const Args& a, LAS char* lds, unsigned lds_base, int task, rsrc_t rw, int& budget) {
    const int tid = threadIdx.x, lane = tid & 63, w = __builtin_amdgcn_readfirstlane(tid >> 6);
    const int d0 = task * S_CH;
    touch(w == 0 ? a.lAr : w == 1 ? a.Aim : w == 2 ? a.Cr : a.Ci, min(d0 * (NS * 2) + min(lane, 2 * S_CH + 1) * 128, DM * NS * 2 - 4), lds_base);
    touch(w & 1 ? a.ldt : a.Dsk, min(lane * 128, DM * 2 - 4), lds_base);
#define S4S(i)
    {
        const rsrc_t rx = __builtin_amdgcn_make_buffer_rsrc((void*)a.x, 0, SEQ * DM * 2, 0x00020000);
        const unsigned sh = (d0 & 1) * 16;
        u32x4 v[8]; unsigned e[8];
#pragma unroll
        for (int i = 0; i < 8; ++i) {
            const int o = (tid + 256 * i) * (DM * 2) + (d0 & ~1) * 2;
            v[i] = __builtin_amdgcn_raw_buffer_load_b128(rx, o, 0, 0); e[i] = __builtin_amdgcn_raw_buffer_load_b32(rx, o, 16, 0);
        }
#pragma unroll
        for (int p = 0; p < 2; ++p) {
            const int tok = 32 * task + 8 * w + 4 * p + (lane >> 4);
            float acc = 0.f;
#pragma unroll
            for (int i = 0; i < 5; ++i) {
                const int ch = (lane & 15) + 16 * i;
                const u32x4 gv = ch < 72 ? *(const u32x4*)(a.gw + ch * 8) : (u32x4){0u, 0u, 0u, 0u};
                const u32x4 xv = *(const u32x4*)(a.x + (size_t)tok * DM + min(ch, 71) * 8);
#pragma unroll
                for (int j = 0; j < 4; ++j) acc += bflo(xv[j]) * bflo(gv[j]) + bfhi(xv[j]) * bfhi(gv[j]);
            }
            acc += __shfl_xor(acc, 1); acc += __shfl_xor(acc, 2); acc += __shfl_xor(acc, 4); acc += __shfl_xor(acc, 8);
            st32(__float_as_uint(1.0f / (1.0f + __expf(-acc))), rw, (lane & 15) == 0 ? OFF_GT + tok * 4 : OOB, 0);
        }
#pragma unroll
        for (int i = 0; i < 8; ++i) {
            const unsigned x5[5] = {__builtin_amdgcn_alignbit(v[i].y, v[i].x, sh), __builtin_amdgcn_alignbit(v[i].z, v[i].y, sh), __builtin_amdgcn_alignbit(v[i].w, v[i].z, sh),
                                    __builtin_amdgcn_alignbit(e[i], v[i].w, sh), e[i] >> sh};
#pragma unroll
            for (int c = 0; c < S_CH; ++c) *(LAS u16*)(lds + S_XR + c * S_XRS + (2047 - tid - 256 * i) * 2) = (u16)(x5[c >> 1] >> (16 * (c & 1)));
        }
    }
    if (tid < 8 * S_CH) *(LAS unsigned*)(lds + S_XR + (tid >> 3) * S_XRS + 4096 + (tid & 7) * 4) = 0;
    if (tid < 64) *(LAS unsigned*)(lds + S_RC + (tid >> 4) * S_RCS + 4096 + (tid & 15) * 4) = 0;
    for (int i = tid; i < 15 * S_KROW / 4; i += 256) *(LAS unsigned*)(lds + S_K + i * 4) = 0;
    int kd[8];
#pragma unroll
    for (int j = 0; j < 8; ++j) {
        const int p = (int)bf2f(f2bf((float)(8 * tid + j))), k = p >> 7, r = p & 7;
        const int row = p < 256 ? 8 * k + r : (p < 512 ? 8 + 4 * k + (r >> 1) : (p < 1024 ? 16 + 2 * k + (r >> 2) : 24 + k));
        kd[j] = S_KD + (row * 16 + ((p >> 3) & 15)) * 8;
    }
    const int n = tid & 127, nn = lane & 15, g = lane >> 4;
    u16 pA = a.lAr[d0 * NS + n], pI = a.Aim[d0 * NS + n], pCr = a.Cr[d0 * NS + n], pCi = a.Ci[d0 * NS + n], pT = a.ldt[d0], pD = a.Dsk[d0];
    WG_BARRIER();
    for (int ch = 0; ch < S_CH; ++ch) {
        S4S(0)
        const float dsk = bf2f(pD);
        {
            const u32x4 v = *(LAS u32x4*)(lds + S_XR + ch * S_XRS + tid * 16);
            const u32x2 e = *(LAS u32x2*)(lds + S_XR + ch * S_XRS + tid * 16 + 16);
            const unsigned s01 = __builtin_amdgcn_alignbit(v.y, v.x, 16), s12 = __builtin_amdgcn_alignbit(v.z, v.y, 16), s23 = __builtin_amdgcn_alignbit(v.w, v.z, 16);
            const unsigned s34 = __builtin_amdgcn_alignbit(e.x, v.w, 16), s45 = __builtin_amdgcn_alignbit(e.y, e.x, 16);
            *(LAS u32x4*)(lds + S_RC + tid * 16) = v;
            *(LAS u32x4*)(lds + S_RC + S_RCS + tid * 16) = (u32x4){s01, s12, s23, s34};
            *(LAS u32x4*)(lds + S_RC + 2 * S_RCS + tid * 16) = (u32x4){v.y, v.z, v.w, e.x};
            *(LAS u32x4*)(lds + S_RC + 3 * S_RCS + tid * 16) = (u32x4){s12, s23, s34, s45};
        }
        S4S(1)
        const float dt = bf2f(f2bf(expf(bf2f(pT))));
        const int sc = 140 - (wave_max32(max(pCr & 0x7FFF, pCi & 0x7FFF)) >> 7);
        {
            const float ar = -bf2f(f2bf(expf(bf2f(pA)))) * dt, ai = bf2f(pI) * dt;
            const f32x2 C = {__builtin_amdgcn_ldexpf(bf2f(pCr), sc), __builtin_amdgcn_ldexpf(bf2f(pCi), sc)};
            const int dn = min(d0 + ch + 1, DM - 1);
            pA = a.lAr[dn * NS + n]; pI = a.Aim[dn * NS + n]; pCr = a.Cr[dn * NS + n]; pCi = a.Ci[dn * NS + n]; pT = a.ldt[dn]; pD = a.Dsk[dn];
            const int to = S_TAB + n * 4;
            auto put = [&](int row, f32x2 v) { *(LAS f16x2*)(lds + to + row * S_TROW) = __builtin_convertvector(v, f16x2); };
            const f32x2 z128 = cexp_p(ar, ai, 128.f);
            if (w < 2) {
                f32x2 E[8], G[4];
                E[1] = cexp_p(ar, ai, 1.f); G[0] = C;
#pragma unroll
                for (int r = 2; r < 8; ++r) E[r] = cmul(E[r >> 1], E[r - (r >> 1)]);
#pragma unroll
                for (int k = 1; k < 4; ++k) G[k] = cmul(G[k - 1], z128);
#pragma unroll
                for (int k = 0; k < 4; ++k)
#pragma unroll
                    for (int r = 0; r < 8; r += 1 + (k >> 1)) put(k < 2 ? 8 * k + r : 8 + 4 * k + (r >> 1), r ? cmul(E[r], G[k]) : G[k]);
            } else {
                const f32x2 z4 = cexp_p(ar, ai, 4.f), z8 = cexp_p(ar, -ai, 8.f);
                f32x2 G = cmul(C, cexp_p(ar, ai, 512.f)), B = z8;
                put(41, (f32x2){1.f, 0.f});
#pragma unroll
                for (int k = 1; k < 16; ++k) {
                    put(41 + k, B); B = cmul(B, z8);
                    if (k < 5) { put(22 + 2 * k, G); put(23 + 2 * k, cmul(G, z4)); }
                    else if (k < 14) put(27 + k, G);
                    G = cmul(G, z128);
                }
            }
        }
        WG_BARRIER();
        S4S(2)
        {
            const int ko = (w & 1) * 256 + g * 16, t0 = w & 2;
            u32x4 av[2][4], bv[4];
#pragma unroll
            for (int j = 0; j < 4; ++j) {
                bv[j] = *(LAS u32x4*)(lds + S_TAB + (41 + nn) * S_TROW + ko + j * 64);
                av[0][j] = *(LAS u32x4*)(lds + S_TAB + min(16 * t0 + nn, 40) * S_TROW + ko + j * 64);
                av[1][j] = *(LAS u32x4*)(lds + S_TAB + (16 + nn) * S_TROW + ko + j * 64);
            }
            f32x4 acc[2] = {};
#pragma unroll
            for (int j = 0; j < 8; ++j) acc[j & 1] = __builtin_amdgcn_mfma_f32_16x16x32_f16(__builtin_bit_cast(f16x8, av[j & 1][j >> 1]), __builtin_bit_cast(f16x8, bv[j >> 1]), acc[j & 1], 0, 0, 0);
#pragma unroll
            for (int r = 0; r < 8; ++r) *(LAS float*)(lds + S_KD + (((r < 4 ? 16 * t0 : 16) + 4 * g + (r & 3)) * 16 + nn) * 8 + (w & 1) * 4) = __builtin_amdgcn_ldexpf(acc[r >> 2][r & 3] * dt, 1 - sc);
        }
        WG_BARRIER();
        S4S(3)
        {
            float kv[8];
#pragma unroll
            for (int j = 0; j < 8; ++j) { const f32x2 t = *(LAS f32x2*)(lds + kd[j]); kv[j] = t.x + t.y; }
            *(LAS u32x4*)(lds + S_K + (15 + (tid >> 2)) * S_KROW + (tid & 3) * 16) = (u32x4){pack2(kv[0], kv[1]), pack2(kv[2], kv[3]), pack2(kv[4], kv[5]), pack2(kv[6], kv[7])};
        }
        WG_BARRIER();
        S4S(4)
        {
            const int st0 = 2047 - nn + 8 * g - 32 * w, cc = st0 & 3;
            const int ub = S_RC + cc * S_RCS + (st0 - cc) * 2 - 3872, kb = S_K + (3 + nn - w) * S_KROW + g * 16;
            f32x4 y[4][2] = {};
            u32x2 U[2][16]; u32x4 F[2][4];
            auto rd = [&](int i, int f) {
                if (f < 4) F[i & 1][f] = *(LAS u32x4*)(lds + kb + (12 - 4 * i + 16 * f) * S_KROW);
                else U[i & 1][f - 4] = *(LAS u32x2*)(lds + ub + 3872 - 256 * i - 1024 * ((f - 4) >> 2) - 32 * ((f >> 1) & 1) + 8 * (f & 1));
            };
#pragma unroll
            for (int f = 0; f < 20; ++f) { rd(0, f); FENCE(); }
#pragma unroll
            for (int i = 0; i < 4; ++i) {
                int c = 0;
#pragma unroll
                for (int q = 0; q < 4; ++q)
#pragma unroll
                    for (int T = q; T < 4; ++T)
#pragma unroll
                        for (int bt = 0; bt < 2; ++bt) {
                            y[T][bt] = MFMA16(__builtin_shufflevector(U[i & 1][4 * q + 2 * bt], U[i & 1][4 * q + 2 * bt + 1], 0, 1, 2, 3), F[i & 1][T - q], y[T][bt]);
                            if (i < 3) rd(i + 1, c);
                            ++c;
                            FENCE();
                        }
            }
#pragma unroll
            for (int T = 0; T < 4; ++T)
#pragma unroll
                for (int bt = 0; bt < 2; ++bt) *(LAS f32x4*)(lds + S_RED + (w * 64 + 16 * T + nn) * 144 + bt * 64 + g * 16) = y[T][bt];
        }
        WG_BARRIER();
        S4S(5)
        {
            f32x4 y0 = {0, 0, 0, 0}, y1 = y0;
#pragma unroll
            for (int ww = 0; ww < 4; ++ww) {
                const int o = S_RED + (ww * 64 + (tid >> 2)) * 144 + (tid & 3) * 32;
                y0 += *(LAS f32x4*)(lds + o); y1 += *(LAS f32x4*)(lds + o + 16);
            }
            const u32x4 u = *(LAS u32x4*)(lds + S_XR + ch * S_XRS + (255 - tid) * 16);
            st128((u32x4){pack2(y0[0] + bfhi(u.w) * dsk, y0[1] + bflo(u.w) * dsk), pack2(y0[2] + bfhi(u.z) * dsk, y0[3] + bflo(u.z) * dsk),
                          pack2(y1[0] + bfhi(u.y) * dsk, y1[1] + bflo(u.y) * dsk), pack2(y1[2] + bfhi(u.x) * dsk, y1[3] + bflo(u.x) * dsk)}, rw, tid * 16, OFF_BO + (d0 + ch) * BO_ROW);
        }
        S4S(6)
    }
    wait_init(rw, launch_tag(a), budget);
    VM_DRAIN();
    WG_BARRIER();
    if (tid == 0) st32(1u, rw, F_S4 + task * 4, 0);
}

constexpr int XT_BYTES = 32 * 1152;
constexpr int P_XT = 0;
constexpr int P_STG = 2 * XT_BYTES;
struct WSlice { u32x4 f[36]; };
constexpr int P_RING = 5;
template <class E, class F> DI void load_wslices(WSlice& W0, WSlice& W1, const u16* s0, const u16* s1, int w, int lane, LAS char* lds, unsigned lds_base, E also, F got) {
    constexpr int NB = 18;
    const int r0 = 8 * (lane >> 5) + ((lane & 15) >> 2);
    const int cb = (4 * w + 2 * ((lane >> 4) & 1) + ((lane & 3) >> 1)), lo8 = (lane & 1) * 8;
    auto src = [&](int i) { return (i < 9 ? s0 : s1) + (size_t)(64 * (i % 9)) * HD; };
    const u32x4 ro = ring_offs(w, lane, HD);
#pragma unroll
    for (int i = 0; i < P_RING - 1; ++i) { ring_issue(src(i), ro, lds_base + P_STG + i * 16384 + w * 4096); also(i); }
    static_for<NB>([&](auto ic) {
        constexpr int i = decltype(ic)::value, rem = NB - 1 - i < P_RING - 2 ? NB - 1 - i : P_RING - 2;
        ring_wait<4 * rem + (i < 2) * 9 + (i < 4) * 9 + (i > 0 && i < 5) * 4>();
        got(ic, ic_t<0>{});
        if constexpr (i + P_RING - 1 < NB) ring_issue(src(i + P_RING - 1), ro, lds_base + P_STG + ((i + P_RING - 1) % P_RING) * 16384 + w * 4096);
        got(ic, ic_t<1>{});
        const int sl = P_STG + (i % P_RING) * 16384;
#pragma unroll
        for (int kk = 0; kk < 4; ++kk) {
            const int ra = r0 + 16 * kk, rb = ra + 4;
            const u32x2 lo = tr_read(lds, sl + ra * 256 + ((cb ^ (2 * swz(ra))) * 16) + lo8), hi = tr_read(lds, sl + rb * 256 + ((cb ^ (2 * swz(rb))) * 16) + lo8);
            (i < 9 ? W0 : W1).f[(i % 9) * 4 + kk] = (u32x4){lo.x, lo.y, hi.x, hi.y};
        }
        got(ic, ic_t<2>{});
        u32x4* f = (i < 9 ? W0 : W1).f + (i % 9) * 4;
        if constexpr (i < 16) asm volatile("" : "+a"(f[0]), "+a"(f[1]), "+a"(f[2]), "+a"(f[3]));
    });
    static_for<3>([&](auto jc) { got(ic_t<NB>{}, jc); });
}
DI u32x4 piece_pi(const f32x16& a, int qq) {
    return (u32x4){pack2(a[4 * qq], a[4 * qq + 8]), pack2(a[4 * qq + 1], a[4 * qq + 9]), pack2(a[4 * qq + 2], a[4 * qq + 10]), pack2(a[4 * qq + 3], a[4 * qq + 11])};
}
DI u32x4 piece_seq(const f32x16& a, int s2) {
    return (u32x4){pack2(a[8 * s2], a[8 * s2 + 1]), pack2(a[8 * s2 + 2], a[8 * s2 + 3]), pack2(a[8 * s2 + 4], a[8 * s2 + 5]), pack2(a[8 * s2 + 6], a[8 * s2 + 7])};
}
struct XTile {
    int xs[9], xa[4];
    DI void init(int w, int lane) {
#pragma unroll
        for (int i = 0; i < 9; ++i) { const int L = (i * 4 + w) * 64 + lane, r = L / 72, cs = L - 72 * r; xs[i] = r * 1152 + (cs ^ ((r >> 1) & 7)) * 16; }
        const int c32 = lane & 31, hf = lane >> 5;
#pragma unroll
        for (int j = 0; j < 4; ++j) xa[j] = P_XT + c32 * 1152 + (((2 * j + hf) ^ ((c32 >> 1) & 7)) * 16);
    }
    DI void dma(const Args& a, unsigned lds_base, int w, int s, int buf, int i) const {
        glds16((const char*)a.x + (size_t)min(s, NSEG - 1) * XT_BYTES, xs[i], lds_base + P_XT + buf * XT_BYTES + (i * 4 + w) * 1024);
    }
    DI void first(const Args& a, unsigned lds_base, int w, int lane, int s) {
        init(w, lane);
#pragma unroll
        for (int i = 0; i < 9; ++i) dma(a, lds_base, w, s, 0, i);
    }
};
template <class F> DI void project(f32x16& acc0, f32x16& acc1, const WSlice& W0, const WSlice& W1, const XTile& X, const Args& a, LAS char* lds, unsigned lds_base, int w, int buf, int sn, F gap) {
    const int bo = buf * XT_BYTES;
    constexpr int PD = 5;
    u32x4 xf[PD];
#pragma unroll
    for (int i = 0; i < PD; ++i) xf[i] = *(LAS u32x4*)(lds + X.xa[i & 3] + bo + (i >> 2) * 128);
    static_for<36>([&](auto ic) {
        constexpr int ks = decltype(ic)::value;
        if (ks == 0) asm volatile("v_mfma_f32_32x32x16_bf16 %0, %1, %2, 0" : "=&v"(acc0) : "a"(W0.f[0]), "v"(xf[0]) : "memory");
        else asm volatile("v_mfma_f32_32x32x16_bf16 %0, %1, %2, %0" : "+v"(acc0) : "a"(W0.f[ks]), "v"(xf[ks % PD]) : "memory");
        if (ks < 9) X.dma(a, lds_base, w, sn, buf ^ 1, ks);
        gap(ic_t<2 * ks>{});
        if (ks == 0) asm volatile("v_mfma_f32_32x32x16_bf16 %0, %1, %2, 0" : "=&v"(acc1) : "a"(W1.f[0]), "v"(xf[0]) : "memory");
        else if (ks < 28) asm volatile("v_mfma_f32_32x32x16_bf16 %0, %1, %2, %0" : "+v"(acc1) : "a"(W1.f[ks]), "v"(xf[ks % PD]) : "memory");
        else asm volatile("v_mfma_f32_32x32x16_bf16 %0, %1, %2, %0" : "+v"(acc1) : "v"(W1.f[ks]), "v"(xf[ks % PD]) : "memory");
        if (ks + PD < 36) xf[ks % PD] = *(LAS u32x4*)(lds + X.xa[(ks + PD) & 3] + bo + ((ks + PD) >> 2) * 128);
        gap(ic_t<2 * ks + 1>{});
    });
    asm volatile("s_nop 7\n\ts_nop 3" : "+v"(acc0), "+v"(acc1) :: "memory");
}
#define KV_T 12
#define KV_F 22
DI void role_kv(const Args& a, LAS char* lds, unsigned lds_base, int h, rsrc_t rw, int& budget) {
    const int tid = threadIdx.x, lane = tid & 63, w = __builtin_amdgcn_readfirstlane(tid >> 6);
    WSlice W0, W1;
    XTile X;
    const int oo = h * 8192 + w * 2048 + lane * 16;
    const int fo = lane == 0 ? F_KV + h * 512 + w * 128 : OOB;
    u32x4 xf[6], P[4] = {};
    auto frag = [&](int buf, int ks) { xf[ks % 6] = *(LAS u32x4*)(lds + X.xa[ks & 3] + buf * XT_BYTES + (ks >> 2) * 128); };
    auto cvt = [&](const f32x16& k, const f32x16& v, int i) {
#pragma unroll
        for (int j = 0; j < 4; ++j) asm volatile("v_cvt_pk_bf16_f32 %0, %1, %2" : "=v"(P[i][j]) : "v"(i < 2 ? k[4 * i + j] : v[8 * i - 16 + 2 * j]), "v"(i < 2 ? k[4 * i + j + 8] : v[8 * i - 15 + 2 * j]));
    };
    auto put = [&](int o, int s, int i) { st128(P[i], rw, o + (i & 1) * 1024, (i < 2 ? OFF_KF : OFF_VF) + s * (NH * 8192)); };
    auto now_ = [&](f32x16& k, f32x16& v, int s) {
        asm volatile("s_nop 7\n\ts_nop 3" : "+v"(k), "+v"(v) :: "memory");
#pragma unroll
        for (int i = 0; i < 4; ++i) { cvt(k, v, i); put(oo, s, i); }
    };
    auto seg = [&](f32x16& k, f32x16& v, const f32x16& pk, const f32x16& pv, int s, int buf) {
        const int o = s > 2 ? oo : OOB;
#pragma unroll
        for (int ks = 0; ks < 36; ++ks) {
            if (ks == 0) asm volatile("v_mfma_f32_32x32x16_bf16 %0, %1, %2, 0" : "=&v"(k) : "a"(W0.f[0]), "v"(xf[0]) : "memory");
            else asm volatile("v_mfma_f32_32x32x16_bf16 %0, %1, %2, %0" : "+v"(k) : "a"(W0.f[ks]), "v"(xf[ks % 6]) : "memory");
            if (ks >= 1 && ks < 5) cvt(pk, pv, ks - 1);
            if (ks >= 5 && ks < 13 && (ks & 1)) put(o, s - 1, (ks - 5) >> 1);
            if (ks == KV_T && !buf && s == 2) { asm volatile("s_waitcnt vmcnt(8)" ::: "memory"); st32(1u, rw, fo, 0); }
            if (ks == 14 && (unsigned)(s - 4) < 8) {
                const int i = s - 4, t = opaque_lane() + 64 * w;
                st128((u32x4){SENT, SENT, SENT, SENT}, rw, i < 4 ? (t >> 7) * 65536 + (t & 127) * 16 : t * 16,
                      i < 4 ? (i & 2 ? OFF_VM : OFF_KM) + (FAST + 2 * (i & 1)) * 65536 + h * 2048 : OFF_OF + (LATE - 2 + (i >> 1)) * 262144 + h * 8192 + (i & 1) * 4096);
            }
            if (ks == KV_F) { VM_DRAIN(); st32((unsigned)(s - 1), rw, fo, 0); }
            if (ks == KV_F + 2 && (unsigned)(s - 52) < 10) {
                const int l = opaque_lane();
                if (s < 61) touch(a.Wq + (h & ~1) * DH, ((s - 52) * 64 + w * 16 + (l >> 2)) * (HD * 2) + (l & 3) * 128, lds_base);
                else bdma(rw, (l & 15) * 16, F_S4, lds_base + P_STG + w * 4096);
            }
            if (ks == 0) asm volatile("v_mfma_f32_32x32x16_bf16 %0, %2, %1, 0" : "=&v"(v) : "a"(W1.f[0]), "v"(xf[0]) : "memory");
            else if (ks < 28) asm volatile("v_mfma_f32_32x32x16_bf16 %0, %2, %1, %0" : "+v"(v) : "a"(W1.f[ks]), "v"(xf[ks % 6]) : "memory");
            else asm volatile("v_mfma_f32_32x32x16_bf16 %0, %2, %1, %0" : "+v"(v) : "v"(W1.f[ks]), "v"(xf[ks % 6]) : "memory");
            if (ks == 30) WG_BARRIER();
            frag(buf ^ (ks >= 30), (ks + 6) % 36);
            if (ks < 4) X.dma(a, lds_base, w, s + 1, buf ^ 1, ks + 5);
            if (ks > 30) X.dma(a, lds_base, w, s + 2, buf, ks - 31);
        }
    };
    f32x16 k0 = {}, v0 = {}, k1 = {}, v1 = {};
    u32x2 tag, in;
    u32x4 xa[2][4], xb[2][4];
    auto mm = [&](f32x16& k, f32x16& v, int t, int i, int j) {
        if (t) {}
        else if (i >= 16) asm volatile("s_nop 1\n\tv_mfma_f32_32x32x16_bf16 %0, %2, %1, %0" : "+v"(v) : "v"(W1.f[4 * i % 36 + j]), "v"(xa[t][j]) : "memory");
        else if (i >= 9) asm volatile("s_nop 1\n\tv_mfma_f32_32x32x16_bf16 %0, %2, %1, %0" : "+v"(v) : "a"(W1.f[4 * i % 36 + j]), "v"(xa[t][j]) : "memory");
        if (i > 1 + 2 * t && i < 11 + 2 * t) asm volatile("s_nop 1\n\tv_mfma_f32_32x32x16_bf16 %0, %1, %2, %0" : "+v"(k) : "a"(W0.f[4 * i % 36 + j]), "v"(xa[t][j]) : "memory");
    };
    load_wslices(W0, W1, a.Wk + h * DH, a.Wv + h * DH, w, lane, lds, lds_base, [&](int i) {
        if (i == 1) X.first(a, lds_base, w, lane, 0);
        if (i == 3)
#pragma unroll
            for (int j = 0; j < 9; ++j) X.dma(a, lds_base, w, 1, 1, j);
    }, [&](auto ic, auto jc) {
        constexpr int i = decltype(ic)::value - 1, part = decltype(jc)::value;
        mm(k0, v0, 0, i, part); mm(k1, v1, 1, i, part);
        if (part < 2) return;
        if (i < 0)
#pragma unroll
            for (int j = 0; j < 4; ++j) { P[j] = (u32x4){SENT, SENT, SENT, SENT}; put(oo, 0, j); }
#pragma unroll
        for (int j = 0; j < 8; ++j) if (i > (j >> 2) * 2 && i < 17 - 5 * (j >> 2)) xb[j >> 2][j & 3] = *(LAS u32x4*)(lds + X.xa[j & 3] + (j >> 2) * XT_BYTES + ((i + 1) % 9) * 128);
        mm(k0, v0, 0, i, 3); mm(k1, v1, 1, i, 3);
#pragma unroll
        for (int j = 0; j < 8; ++j) if (i > (j >> 2) * 2) xa[j >> 2][j & 3] = xb[j >> 2][j & 3];
        if (i == 4) { tag = launch_tag(a); __builtin_amdgcn_raw_buffer_store_b64(tag, rw, lane == 0 ? F_INIT + (33 + 4 * h + w) * 128 : OOB, 0, 16); }
        if (i == 10) asm volatile("buffer_load_dwordx2 %0, %1, %2, 0 offen sc1" : "=v"(in) : "v"(init_at(0)), "s"(rw) : "memory");
        if (i == 11) { cvt(k0, v0, 0); put(oo, 0, 0); cvt(k0, v0, 1); put(oo, 0, 1); }
    });
#pragma unroll
    for (int i = 0; i < 6; ++i) frag(1, i);
    asm volatile("s_nop 7\n\ts_nop 3" : "+v"(v0) :: "memory");
    cvt(k0, v0, 2); put(oo, 0, 2); cvt(k0, v0, 3); put(oo, 0, 3);
    WG_BARRIER();
#pragma unroll
    for (int ks = 0; ks < 36; ++ks) {
        if (ks == 0) asm volatile("v_mfma_f32_32x32x16_bf16 %0, %2, %1, 0" : "=&v"(v1) : "a"(W1.f[0]), "v"(xf[0]) : "memory");
        else if (ks < 28) asm volatile("v_mfma_f32_32x32x16_bf16 %0, %2, %1, %0" : "+v"(v1) : "a"(W1.f[ks]), "v"(xf[ks % 6]) : "memory");
        else asm volatile("v_mfma_f32_32x32x16_bf16 %0, %2, %1, %0" : "+v"(v1) : "v"(W1.f[ks]), "v"(xf[ks % 6]) : "memory");
        if (ks < 18 && !(ks & 1)) X.dma(a, lds_base, w, 2, 0, ks >> 1);
        if (ks == 19 || ks == 22) { cvt(k1, v1, ks == 22); put(oo, 1, ks == 22); }
        if (ks == 30) ring_wait<2>();
        frag(ks < 30, (ks + 6) % 36);
        if (ks > 30) X.dma(a, lds_base, w, 3, 1, ks - 31);
    }
    asm volatile("s_nop 7\n\ts_nop 3" : "+v"(v1), "+v"(in) :: "memory");
    cvt(k1, v1, 2); put(oo, 1, 2); cvt(k1, v1, 3); put(oo, 1, 3);
    if (!init_ok(in, tag)) wait_init(rw, tag, budget);
    for (int s = 2; s < NSEG; s += 2) { seg(k0, v0, k1, v1, s, 0); seg(k1, v1, k0, v0, s + 1, 1); }
    now_(k1, v1, NSEG - 1);
    if (!__all(*(volatile LAS int*)(lds + P_STG + w * 4096 + lane * 4) >= 1)) wait_prog(rw, F_S4 + lane * 4, true, 1, budget);
}

DI int prog_min(rsrc_t rw, int off) {
    const int v = (int)ld32(rw, off, 0);
    asm volatile("" ::: "memory");
    return -wave_max32(-v);
}
DI float red_max_g(float v) {
    auto a = __builtin_amdgcn_permlane16_swap(__float_as_uint(v), __float_as_uint(v), false, false);
    v = fmaxf(__uint_as_float(a[0]), __uint_as_float(a[1]));
    auto b = __builtin_amdgcn_permlane32_swap(__float_as_uint(v), __float_as_uint(v), false, false);
    return fmaxf(__uint_as_float(b[0]), __uint_as_float(b[1]));
}
#define M16 "v_mfma_f32_16x16x32_bf16 "
DI void role_qa(const Args& a, LAS char* lds, unsigned lds_base, int h0, int s0, int s1, rsrc_t rw, int& budget) {
    const int tid = threadIdx.x, lane = tid & 63, w = __builtin_amdgcn_readfirstlane(tid >> 6);
    const int n = lane & 15, g = lane >> 4, h = h0 + (w >> 1), tt = w & 1;
    WSlice W0, W1;
    XTile X;
    const int oo = OFF_OF + (2 * tt + (n >> 3)) * 65536 + h * 2048 + g * 128 + (n & 7) * 16;
    const int fk = F_KV + h * 512 + (lane & 3) * 128, fm = F_KVM + h * 256 + (lane & 1) * 128;
    int pm_, m_rdy = -1, kv_rdy = -1;
    auto look = [&]() { pm_ = (int)ld32(rw, lane & 4 ? fk : fm, 0); };
    auto seen = [&]() { auto rl = [&](int i) { return __builtin_amdgcn_readlane(pm_, i); }; m_rdy = min(rl(0), rl(1)); kv_rdy = min(min(rl(4), rl(5)), min(rl(6), rl(7))); };
    f32x16 q0 = {}, q1 = {};
    u32x4 xa[4], xb[4];
    auto mm = [&](int i, int j) {
        if (i >= 16) asm volatile("s_nop 1\n\tv_mfma_f32_32x32x16_bf16 %0, %1, %2, %0" : "+v"(q1) : "v"(W1.f[4 * i % 36 + j]), "v"(xa[j]) : "memory");
        else if (i >= 9) asm volatile("s_nop 1\n\tv_mfma_f32_32x32x16_bf16 %0, %1, %2, %0" : "+v"(q1) : "a"(W1.f[4 * i % 36 + j]), "v"(xa[j]) : "memory");
        if (i > 1 && i < 11) asm volatile("s_nop 1\n\tv_mfma_f32_32x32x16_bf16 %0, %1, %2, %0" : "+v"(q0) : "a"(W0.f[4 * i % 36 + j]), "v"(xa[j]) : "memory");
    };
    load_wslices(W0, W1, a.Wq + h0 * DH, a.Wq + (h0 + 1) * DH, w, lane, lds, lds_base, [&](int i) {
        if (i == 1) X.first(a, lds_base, w, lane, s0);
        if (i == 3)
#pragma unroll
            for (int j = 0; j < 9; ++j) X.dma(a, lds_base, w, s0 + 2, 1, j);
    }, [&](auto ic, auto jc) {
        constexpr int i = decltype(ic)::value - 1, part = decltype(jc)::value;
        mm(i, part);
        if (i < 0 && part == 0) st32(NSEG, rw, lane == 0 && s0 > FAST ? F_KV + (h0 + (s0 & 1)) * 512 + w * 128 : OOB, 0);
        if (part < 2) return;
#pragma unroll
        for (int j = 0; j < 4; ++j) {
            if (i < 0) st128((u32x4){SENT, SENT, SENT, SENT}, rw, oo + j * 512, s1 * 262144);
            if (i > 0 && i < 17) xb[j] = *(LAS u32x4*)(lds + X.xa[j] + ((i + 1) % 9) * 128);
        }
        mm(i, 3);
        if (i == 13) look();
#pragma unroll
        for (int j = 0; j < 4; ++j) if (i > 0) xa[j] = xb[j];
    });
    asm volatile("s_nop 7\n\ts_nop 3\n\ts_barrier" : "+v"(q0), "+v"(q1) :: "memory");
    constexpr int Q_Q = P_STG, Q_KV = Q_Q + 16384, Q_M = Q_KV + 32768;
    static_assert(Q_M + 16384 <= P_STG + P_RING * 16384, "");
    auto wait_m = [&](int thr, unsigned& t) {
        for (;;) {
            const u32x2 v = ld64(rw, fm, 0);
            asm volatile("" ::: "memory");
            t = now();
            const int p0 = __builtin_amdgcn_readlane(v.x, 0), p1 = __builtin_amdgcn_readlane(v.x, 1);
            const int d0 = (int)(__builtin_amdgcn_readlane(v.y, 0) - 8 * t) >> 3, d1 = (int)(__builtin_amdgcn_readlane(v.y, 1) - 8 * t) >> 3;
            if (min(p0, p1) >= thr || --budget < 0) return max(p0 == thr ? d0 : -1000, p1 == thr ? d1 : -1000);
            (void)wait_until(t + min(max(min(d0, d1) + (int)(a.xa >> 16 & 255), 10), 200));
        }
    };
    auto keep_q = [&](const f32x16& acc0, const f32x16& acc1) {
        const int o = Q_Q + w * 2048 + lane * 16;
        *(LAS u32x4*)(lds + o) = piece_pi(acc0, 0); *(LAS u32x4*)(lds + o + 1024) = piece_pi(acc0, 1);
        *(LAS u32x4*)(lds + o + 8192) = piece_pi(acc1, 0); *(LAS u32x4*)(lds + o + 8192 + 1024) = piece_pi(acc1, 1);
    };
    auto proj = [&](int s, auto gap) {
        f32x16 acc0, acc1;
        project(acc0, acc1, W0, W1, X, a, lds, lds_base, w, ((s - s0) >> 1) & 1, s + 2, [&](auto ic) { gap(ic); if (decltype(ic)::value == 55) look(); });
        if (s >= LATE) {
            const int o = OFF_OF + h0 * 8192 + w * 2048 + lane * 16;
            acc0 *= __uint_as_float(0x3e0293eeu); acc1 *= __uint_as_float(0x3e0293eeu);
            st128(piece_pi(acc0, 0), rw, o, s * 262144); st128(piece_pi(acc0, 1), rw, o + 1024, s * 262144);
            st128(piece_pi(acc1, 0), rw, o + 8192, s * 262144); st128(piece_pi(acc1, 1), rw, o + 8192 + 1024, s * 262144);
            return;
        }
        keep_q(acc0, acc1);
    };
    const int fs = (tt ? OFF_VF : OFF_KF) + (g >> 1) * 1024 + ((g & 1) * 32 + 16 * (n >> 3) + (n & 7)) * 16;
    auto fetch = [&](int s) {
        const int so = (min(s, NSEG - 1) * NH + h) * 8192;
#pragma unroll
        for (int f = 0; f < 8; ++f) bdma(rw, fs, so + (f >> 1) * 2048 + (f & 1) * 128, lds_base + Q_KV + w * 8192 + f * 1024);
    };
    const int ms = OFF_KM + (lane & 7) * 256 + (lane >> 3) * 16;
    auto fetch_m = [&](int s) {
        const int so = (s * NH + h) * 2048;
        const unsigned d = lds_base + Q_M + w * 4096;
        bdma(rw, ms, so, d); bdma(rw, ms, so + 128, d + 1024); bdma(rw, lane * 16, OFF_VM + so, d + 2048); bdma(rw, lane * 16, OFF_VM + so + 1024, d + 3072);
    };
    const float c = 0.08838834764831845f * 1.4426950408889634f;
    u32x4 qf[4];
    f32x4 o[8]; float mx, l;
    auto xpart = [&]() {
        const int kb = Q_KV + (w >> 1) * 16384 + lane * 16;
        u32x4 kn[8], vn[8];
#pragma unroll
        for (int ks = 0; ks < 4; ++ks) qf[ks] = *(LAS u32x4*)(lds + Q_Q + (w >> 1) * 8192 + (2 * ks + (g >> 1)) * 1024 + ((g & 1) * 32 + 16 * tt + n) * 16);
#pragma unroll
        for (int f = 0; f < 8; ++f) kn[f] = *(LAS u32x4*)(lds + kb + f * 1024);
        FENCE();
#pragma unroll
        for (int f = 0; f < 8; ++f) vn[f] = *(LAS u32x4*)(lds + kb + 8192 + f * 1024);
        f32x4 s0, s1;
        asm volatile(M16 "%0, %2, %10, 0\n\t" M16 "%1, %3, %10, 0\n\t" M16 "%0, %4, %11, %0\n\t" M16 "%1, %5, %11, %1\n\t"
                     M16 "%0, %6, %12, %0\n\t" M16 "%1, %7, %12, %1\n\t" M16 "%0, %8, %13, %0\n\t" M16 "%1, %9, %13, %1\n\ts_nop 7"
                     : "=&v"(s0), "=&v"(s1) : "v"(kn[0]), "v"(kn[1]), "v"(kn[2]), "v"(kn[3]), "v"(kn[4]), "v"(kn[5]), "v"(kn[6]), "v"(kn[7]), "v"(qf[0]), "v"(qf[1]), "v"(qf[2]), "v"(qf[3]));
        float p[8];
        mx = -3.0e38f; l = 0.f;
#pragma unroll
        for (int r = 0; r < 8; ++r) { const int key = 16 * (g >> 1) + 4 * (g & 1) + (r & 3) + 8 * (r >> 2); p[r] = key <= 16 * tt + n ? (r < 4 ? s0[r & 3] : s1[r & 3]) : -3.0e38f; mx = fmaxf(mx, p[r]); }
        mx = red_max_g(mx);
#pragma unroll
        for (int r = 0; r < 8; ++r) { p[r] = __builtin_amdgcn_exp2f((p[r] - mx) * c); l += p[r]; }
        const u32x4 pf = {pack2(p[0], p[1]), pack2(p[2], p[3]), pack2(p[4], p[5]), pack2(p[6], p[7])};
        asm volatile("s_nop 1\n\t" M16 "%0, %8, %16, 0\n\t" M16 "%1, %9, %16, 0\n\t" M16 "%2, %10, %16, 0\n\t" M16 "%3, %11, %16, 0\n\t"
                     M16 "%4, %12, %16, 0\n\t" M16 "%5, %13, %16, 0\n\t" M16 "%6, %14, %16, 0\n\t" M16 "%7, %15, %16, 0\n\ts_nop 7"
                     : "=&v"(o[0]), "=&v"(o[1]), "=&v"(o[2]), "=&v"(o[3]), "=&v"(o[4]), "=&v"(o[5]), "=&v"(o[6]), "=&v"(o[7])
                     : "v"(vn[0]), "v"(vn[1]), "v"(vn[2]), "v"(vn[3]), "v"(vn[4]), "v"(vn[5]), "v"(vn[6]), "v"(vn[7]), "v"(pf));
    };
    const int fo = lane == 0 ? F_O + h * 128 + (2 * s0 + tt) * 4 : OOB;
    bool pre = false;
    auto both = [&](int s) {
        const int kb = Q_KV + (w >> 1) * 16384 + lane * 16;
        u32x4 kn[8], vn[8], pf;
        f32x4 sc[2];
        float p[8];
        int v0, v1, v2;
        asm volatile("buffer_load_dword %0, %1, %2, 0 offen sc1" : "=v"(pm_) : "v"(lane & 4 ? fk : fm), "s"(rw) : "memory");
#define PIN(...) asm volatile("" : __VA_ARGS__)
        proj(s + 4, [&](auto ic) {
            constexpr int i = decltype(ic)::value;
            if constexpr (i < 8) {
                if (!(i & 1)) qf[i >> 1] = *(LAS u32x4*)(lds + Q_Q + (w >> 1) * 8192 + (i + (g >> 1)) * 1024 + ((g & 1) * 32 + 16 * tt + n) * 16);
                kn[i] = *(LAS u32x4*)(lds + kb + i * 1024);
            }
            if constexpr (i >= 16 && i < 32 && !(i & 1)) vn[(i - 16) >> 1] = *(LAS u32x4*)(lds + kb + 8192 + (i - 16) * 512);
            if constexpr (i == 5 || i == 6) asm volatile(M16 "%0, %1, %2, 0" : "=&v"(sc[i - 5]) : "v"(kn[i - 5]), "v"(qf[0]));
            if constexpr (i > 6 && i < 13) asm volatile(M16 "%0, %1, %2, %0" : "+v"(sc[(i - 5) & 1]) : "v"(kn[i - 5]), "v"(qf[(i - 5) >> 1]));
            if constexpr (i == 15 || i == 16) {
                constexpr int r = 4 * (i - 15);
                PIN("+v"(sc[i - 15]));
#pragma unroll
                for (int j = 0; j < 4; ++j) p[r + j] = 16 * (g >> 1) + 4 * (g & 1) + j + 2 * r <= 16 * tt + n ? sc[i - 15][j] : -3.0e38f;
                PIN("+v"(p[r]), "+v"(p[r + 1]), "+v"(p[r + 2]), "+v"(p[r + 3]));
            }
            if constexpr (i == 17) { mx = fmaxf(fmaxf(fmaxf(fmaxf(p[0], p[1]), p[2]), fmaxf(fmaxf(p[3], p[4]), p[5])), fmaxf(p[6], p[7])); PIN("+v"(mx)); }
            if constexpr (i == 18) { auto x = __builtin_amdgcn_permlane16_swap(__float_as_uint(mx), __float_as_uint(mx), false, false); mx = fmaxf(__uint_as_float(x[0]), __uint_as_float(x[1])); PIN("+v"(mx)); }
            if constexpr (i == 19) { auto x = __builtin_amdgcn_permlane32_swap(__float_as_uint(mx), __float_as_uint(mx), false, false); mx = fmaxf(__uint_as_float(x[0]), __uint_as_float(x[1])); PIN("+v"(mx)); }
            if constexpr (i >= 20 && i < 24) {
                constexpr int r = 2 * (i - 20);
                PIN("+v"(p[r]), "+v"(p[r + 1]));
                p[r] = __builtin_amdgcn_exp2f((p[r] - mx) * c); p[r + 1] = __builtin_amdgcn_exp2f((p[r + 1] - mx) * c);
                PIN("+v"(p[r]), "+v"(p[r + 1]));
            }
            if constexpr (i == 24) { pf = (u32x4){pack2(p[0], p[1]), pack2(p[2], p[3]), pack2(p[4], p[5]), pack2(p[6], p[7])}; PIN("+v"(pf)); }
            if constexpr (i == 25) { l = ((p[0] + p[1]) + p[2]) + p[3]; PIN("+v"(l)); }
            if constexpr (i == 26) { l = (((l + p[4]) + p[5]) + p[6]) + p[7]; PIN("+v"(l)); }
            if constexpr (i == 27) { asm volatile("s_waitcnt vmcnt(9)" : "+v"(pm_) :: "memory"); st32((unsigned)s, rw, fo, 0); }
            if constexpr (i == 29) { seen(); pre = m_rdy >= s + 2 && kv_rdy >= s + 4; }
            if constexpr (i == 31) { v0 = pre ? ms : OOB; v1 = pre ? lane * 16 : OOB; v2 = pre ? fs : OOB; PIN("+v"(v0), "+v"(v1), "+v"(v2)); }
            if constexpr (i >= 28 && i < 36) asm volatile(M16 "%0, %1, %2, 0" : "=&v"(o[i - 28]) : "v"(vn[i - 28]), "v"(pf));
            if constexpr (i == 36) asm volatile("s_barrier" ::: "memory");
            if constexpr (i >= 38 && i < 42) bdma(rw, i < 40 ? v0 : v1, ((s + 2) * NH + h) * 2048 + (i < 40 ? (i & 1) * 128 : OFF_VM + (i & 1) * 1024), lds_base + Q_M + w * 4096 + (i - 38) * 1024);
            if constexpr (i >= 42 && i < 50) bdma(rw, v2, ((s + 4) * NH + h) * 8192 + ((i - 42) >> 1) * 2048 + (i & 1) * 128, lds_base + Q_KV + w * 8192 + (i - 42) * 1024);
        });
    };
    auto none = [](auto) {};
    keep_q(q0, q1);
    if (s0 < FAST) wait_init(rw, launch_tag(a), budget); else seen();
    if (kv_rdy < s0) wait_prog(rw, fk, true, s0, budget);
    fetch(s0);
#pragma unroll 1
    for (int p = 0; p < 2; ++p) {
        const bool x = p == (s0 > FAST);
        if (x) VM_DRAIN();
        WG_BARRIER();
        if (x) xpart(); else proj(s0 + 2, none);
    }
    for (int s = s0; s <= s1; s += 2) {
        S4S(0)
        const int mb = Q_M + w * 4096;
        u32x4 kf[4]; u32x2 vm[4];
        auto frags = [&]() {
#pragma unroll
            for (int ks = 0; ks < 4; ++ks) kf[ks] = *(LAS u32x4*)(lds + mb + (4 * ks + g) * 128 + (2 * (n >> 2) + (n & 1)) * 16);
#pragma unroll
            for (int t = 0; t < 4; ++t) { const int vo = mb + 2048 + t * 512 + (n >> 3) * 256 + g * 64 + (n & 7) * 4; vm[t] = (u32x2){*(LAS unsigned*)(lds + vo), *(LAS unsigned*)(lds + vo + 32)}; }
        };
        if (s == s1) {
            TT(true, 3)
            unsigned t;
            const int d = min(wait_m(s - 1, t) + (int)(a.xa & 255), 500);
            st32((t + max(d, 0) + (a.xb >> 8 * ((s - FAST) >> 1) & 255)) & 0x7FFFFFFFu, rw, lane == 0 ? F_H + ((2 * (s - FAST) + tt) * NH + h) * 128 : OOB, 0);
            (void)wait_until(t + d);
            TT(true, 0)
            for (;;) {
                fetch_m(s);
                VM_DRAIN();
                frags();
                unsigned c = 0;
#pragma unroll
                for (int i = 0; i < 4; ++i) c = max(max(mx4(c, kf[i]), vm[i].x), vm[i].y);
                if (!__any(c == SENT) || --budget < 0) break;
            }
        } else {
            if (!pre) {
                seen();
                unsigned t;
                if (m_rdy < s) { const int d = wait_m(s, t); (void)wait_until(t + min(d + (int)(a.xa >> 24), 300)); }
                fetch_m(s);
                if (kv_rdy < s + 2) wait_prog(rw, fk, true, s + 2, budget);
                fetch(s + 2);
            }
            S4S(1)
            asm volatile("s_waitcnt vmcnt(8)" ::: "memory");
            frags();
        }
        TT(s == s1, 1)
        S4S(2)
        f32x4 sm;
        asm volatile(M16 "%0, %1, %5, 0\n\t" M16 "%0, %2, %6, %0\n\t" M16 "%0, %3, %7, %0\n\t" M16 "%0, %4, %8, %0\n\ts_nop 7"
                     : "=&v"(sm) : "v"(kf[0]), "v"(kf[1]), "v"(kf[2]), "v"(kf[3]), "v"(qf[0]), "v"(qf[1]), "v"(qf[2]), "v"(qf[3]));
        const float m2 = red_max_g(fmaxf(mx, fmaxf(sm[0], sm[1])));
        const float al = __builtin_amdgcn_exp2f((mx - m2) * c), p0 = __builtin_amdgcn_exp2f((sm[0] - m2) * c), p1 = __builtin_amdgcn_exp2f((sm[1] - m2) * c);
        const float inv = __builtin_amdgcn_rcpf(red_sum_g(l * al + p0 + p1)), ai = al * inv;
#pragma unroll
        for (int t = 0; t < 8; ++t) o[t] *= ai;
        const unsigned pm = pack2(p0 * inv, p1 * inv);
#define M1K "v_mfma_f32_16x16x16_bf16 "
        asm volatile("s_nop 1\n\t" M1K "%0, %8, %12, %0\n\t" M1K "%1, %8, %13, %1\n\t" M1K "%2, %9, %12, %2\n\t" M1K "%3, %9, %13, %3\n\t"
                     M1K "%4, %10, %12, %4\n\t" M1K "%5, %10, %13, %5\n\t" M1K "%6, %11, %12, %6\n\t" M1K "%7, %11, %13, %7\n\ts_nop 7"
                     : "+v"(o[0]), "+v"(o[1]), "+v"(o[2]), "+v"(o[3]), "+v"(o[4]), "+v"(o[5]), "+v"(o[6]), "+v"(o[7])
                     : "v"(vm[0]), "v"(vm[1]), "v"(vm[2]), "v"(vm[3]), "v"((u32x2){pm, 0u}), "v"((u32x2){0u, pm}));
#pragma unroll
        for (int b = 0; b < 2; ++b) {
            u32x4 x, y;
#pragma unroll
            for (int j = 0; j < 4; ++j) { x[j] = pack2(o[4 * b + (j >> 1)][2 * (j & 1)], o[4 * b + (j >> 1)][2 * (j & 1) + 1]); y[j] = pack2(o[4 * b + 2 + (j >> 1)][2 * (j & 1)], o[4 * b + 2 + (j >> 1)][2 * (j & 1) + 1]); }
            if (s == LATE - 1) { const u32x4 z = {x.x, x.z, y.x, y.z}; y = (u32x4){x.y, x.w, y.y, y.w}; x = z; }
            st128(x, rw, oo + b * 1024, s * 262144); st128(y, rw, oo + b * 1024 + 512, s * 262144);
        }
        TT(s == s1, 2)
        S4S(3)
        if (s == s1) break;
        asm volatile("s_waitcnt vmcnt(4)" ::: "memory");
        WG_BARRIER();
        S4S(4)
        if (s + 4 <= s1) { both(s); S4S(5) continue; }
        xpart();
        FENCE();
        VM_DRAIN();
        st32((unsigned)s, rw, fo, 0);
        TT(true, 4)
    }
}

#define STEP_ASM \
    "v_readfirstlane_b32 s36, v0\n" \
    "v_readfirstlane_b32 s37, v1\n" \
    "v_readfirstlane_b32 s38, v2\n" \
    "v_readfirstlane_b32 s39, v3\n" \
    "v_readfirstlane_b32 s40, v4\n" \
    "v_readfirstlane_b32 s41, v5\n" \
    "v_readfirstlane_b32 s42, v6\n" \
    "v_readfirstlane_b32 s43, v7\n" \
    "v_readfirstlane_b32 s28, v8\n" \
    "v_readfirstlane_b32 s29, v9\n" \
    "v_readfirstlane_b32 s30, v10\n" \
    "v_readfirstlane_b32 s31, v11\n" \
    "v_readfirstlane_b32 s32, v12\n" \
    "v_readfirstlane_b32 s33, v13\n" \
    "v_readfirstlane_b32 s34, v14\n" \
    "v_readfirstlane_b32 s35, v15\n" \
    "v_readfirstlane_b32 s24, v16\n" \
    "v_readfirstlane_b32 s25, v17\n" \
    "v_readfirstlane_b32 s26, v18\n" \
    "v_readfirstlane_b32 s27, v19\n" \
    "v_readfirstlane_b32 s60, v24\n" \
    "v_readfirstlane_b32 s61, v25\n" \
    "v_readfirstlane_b32 s62, v26\n" \
    "v_readfirstlane_b32 s63, v27\n" \
    "v_readfirstlane_b32 s64, v28\n" \
    "v_readfirstlane_b32 s65, v29\n" \
    "v_readfirstlane_b32 s66, v30\n" \
    "v_readfirstlane_b32 s67, v31\n" \
    "s_mov_b32 s68, m0\n" \
    "s_getpc_b64 s[96:97]\n" \
    "14:\n" \
    "s_add_u32 s96, s96, 2f-14b\n" \
    "s_addc_u32 s97, s97, 0\n" \
    "s_lshl_b32 s74, s60, 10\n" \
    "s_lshl_b32 s95, s74, 1\n" \
    "s_add_i32 s95, s95, 16384\n" \
    "s_mov_b32 s81, 0x3e0293ee\n" \
    "s_mov_b32 s82, 0x53800000\n" \
    "s_lshl_b32 s83, s60, 6\n" \
    "s_and_b32 s83, s83, 128\n" \
    "v_add_u32 v109, s83, v131\n" \
    "v_sub_u32 v111, v131, s83\n" \
    "v_xor_b32 v112, 0x2000, v133\n" \
    "v_and_b32 v113, 0x3c0, v127\n" \
    "v_mad_u32_u24 v113, v113, 7, v127\n" \
    "s_bfe_u32 s83, s61, 0x2000b\n" \
    "s_and_b32 s83, s83, 3\n" \
    "v_lshl_add_u32 v113, s83, 7, v113\n" \
    "v_lshlrev_b32 v114, 1, v127\n" \
    "v_add_u32 v106, s63, v133\n" \
    "s_bfe_u32 s83, s61, 0x40009\n" \
    "s_bfe_u32 s80, s61, 0x20004\n" \
    "s_lshl2_add_u32 s83, s83, s80\n" \
    "s_mov_b64 s[72:73], 0\n" \
    "s_cmp_eq_u32 s43, 4\n" \
    "s_cbranch_scc1 20f\n" \
    "0:\n" \
    "s_and_b32 s75, s36, 3\n" \
    "s_lshl_b32 s75, s75, 16\n" \
    "s_add_i32 s69, s75, s26\n" \
    "s_xor_b32 s70, s69, 2048\n" \
    "s_andn2_b32 s71, s69, 0x1fff\n" \
    "s_add_i32 s71, s71, 32768\n" \
    "ds_read_b128 v[72:75], v133 offset:0\n" \
    "ds_read_b128 v[76:79], v133 offset:1024\n" \
    "ds_read_b128 v[80:83], v133 offset:2048\n" \
    "ds_read_b128 v[84:87], v133 offset:3072\n" \
    "ds_read_b128 v[88:91], v133 offset:16384\n" \
    "ds_read_b128 v[92:95], v133 offset:17408\n" \
    "v_mov_b64 v[40:41], 0\n" \
    "v_mov_b64 v[42:43], 0\n" \
    "v_mov_b64 v[44:45], 0\n" \
    "v_mov_b64 v[46:47], 0\n" \
    "v_mov_b64 v[48:49], 0\n" \
    "v_mov_b64 v[50:51], 0\n" \
    "v_mov_b64 v[52:53], 0\n" \
    "v_mov_b64 v[54:55], 0\n" \
    "v_mov_b64 v[56:57], 0\n" \
    "v_mov_b64 v[58:59], 0\n" \
    "v_mov_b64 v[60:61], 0\n" \
    "v_mov_b64 v[62:63], 0\n" \
    "v_mov_b64 v[64:65], 0\n" \
    "v_mov_b64 v[66:67], 0\n" \
    "v_mov_b64 v[68:69], 0\n" \
    "v_mov_b64 v[70:71], 0\n" \
    "v_add_u32 v108, s74, v127\n" \
    "v_xor_b32 v24, 0x400, v108\n" \
    "v_xor_b32 v28, 0x800, v108\n" \
    "v_xor_b32 v32, 0xc00, v108\n" \
    "v_and_b32 v22, 0x200, v127\n" \
    "s_and_b32 s76, s26, 0x800\n" \
    "v_mad_u32_u24 v22, v22, 3, v127\n" \
    "v_xor_b32 v22, s76, v22\n" \
    "s_movk_i32 m0, 0x2000\n" \
    "s_cmp_eq_u32 s36, 0\n" \
    "s_cbranch_scc0 17f\n" \
    "v_and_b32 v36, 0x380, v127\n" \
    "v_add_u32 v36, v36, v127\n" \
    "v_and_b32 v37, 0x300, v127\n" \
    "v_add_u32 v37, v37, v127\n" \
    "s_add_i32 s93, s64, 0x1000000\n" \
    "v_add_u32 v21, s95, v127\n" \
    "v_xor_b32 v17, 32, v119\n" \
    "v_xor_b32 v18, 64, v119\n" \
    "v_xor_b32 v19, 96, v119\n" \
    "v_xor_b32 v0, 0x0, v96\n" \
    "v_xor_b32 v1, 0x80, v96\n" \
    "v_xor_b32 v2, 0x20, v96\n" \
    "v_xor_b32 v3, 0xa0, v96\n" \
    "v_xor_b32 v4, 0x4000, v96\n" \
    "v_xor_b32 v5, 0x4080, v96\n" \
    "v_xor_b32 v6, 0x4020, v96\n" \
    "v_xor_b32 v7, 0x40a0, v96\n" \
    "v_xor_b32 v8, 0x8000, v96\n" \
    "v_xor_b32 v9, 0x8080, v96\n" \
    "v_xor_b32 v10, 0x8020, v96\n" \
    "v_xor_b32 v11, 0x80a0, v96\n" \
    "v_xor_b32 v12, 0xc000, v96\n" \
    "v_xor_b32 v13, 0xc080, v96\n" \
    "v_xor_b32 v14, 0xc020, v96\n" \
    "v_xor_b32 v15, 0xc0a0, v96\n" \
    "s_waitcnt vmcnt(16) lgkmcnt(0)\n" \
    "s_barrier\n" \
    "ds_read_b64_tr_b16 v[72:73], v0 offset:32768\n" \
    "ds_read_b64_tr_b16 v[74:75], v0 offset:40960\n" \
    "ds_read_b64_tr_b16 v[76:77], v1 offset:34816\n" \
    "ds_read_b64_tr_b16 v[78:79], v1 offset:43008\n" \
    "ds_read_b64_tr_b16 v[80:81], v2 offset:32768\n" \
    "ds_read_b64_tr_b16 v[82:83], v2 offset:40960\n" \
    "ds_read_b64_tr_b16 v[84:85], v3 offset:34816\n" \
    "ds_read_b64_tr_b16 v[86:87], v3 offset:43008\n" \
    "ds_read_b64_tr_b16 v[88:89], v4 offset:32768\n" \
    "ds_read_b64_tr_b16 v[90:91], v4 offset:40960\n" \
    "ds_read_b64_tr_b16 v[92:93], v5 offset:34816\n" \
    "ds_read_b64_tr_b16 v[94:95], v5 offset:43008\n" \
    "s_waitcnt lgkmcnt(8)\n" \
    "ds_read_b64_tr_b16 v[40:41], v6 offset:32768\n" \
    "ds_read_b64_tr_b16 v[42:43], v6 offset:40960\n" \
    "ds_read_b64_tr_b16 v[44:45], v7 offset:34816\n" \
    "ds_read_b64_tr_b16 v[46:47], v7 offset:43008\n" \
    "s_waitcnt lgkmcnt(8)\n" \
    "ds_read_b64_tr_b16 v[48:49], v8 offset:32768\n" \
    "ds_read_b64_tr_b16 v[50:51], v8 offset:40960\n" \
    "ds_read_b64_tr_b16 v[52:53], v9 offset:34816\n" \
    "ds_read_b64_tr_b16 v[54:55], v9 offset:43008\n" \
    "s_waitcnt lgkmcnt(8)\n" \
    "ds_read_b64_tr_b16 v[56:57], v10 offset:32768\n" \
    "ds_read_b64_tr_b16 v[58:59], v10 offset:40960\n" \
    "ds_read_b64_tr_b16 v[60:61], v11 offset:34816\n" \
    "ds_read_b64_tr_b16 v[62:63], v11 offset:43008\n" \
    "s_waitcnt lgkmcnt(8)\n" \
    "ds_read_b64_tr_b16 v[64:65], v12 offset:32768\n" \
    "ds_read_b64_tr_b16 v[66:67], v12 offset:40960\n" \
    "ds_read_b64_tr_b16 v[68:69], v13 offset:34816\n" \
    "ds_read_b64_tr_b16 v[70:71], v13 offset:43008\n" \
    "s_waitcnt lgkmcnt(8)\n" \
    "ds_read_b64_tr_b16 v[248:249], v14 offset:32768\n" \
    "ds_read_b64_tr_b16 v[250:251], v14 offset:40960\n" \
    "ds_read_b64_tr_b16 v[252:253], v15 offset:34816\n" \
    "ds_read_b64_tr_b16 v[254:255], v15 offset:43008\n" \
    "ds_read_b128 v[98:101], v21 offset:0\n" \
    "ds_read_b128 v[102:105], v21 offset:1024\n" \
    "s_waitcnt lgkmcnt(0)\n" \
    "s_barrier\n" \
    "ds_write_b128 v133, v[72:75] offset:0\n" \
    "ds_write_b128 v133, v[76:79] offset:1024\n" \
    "ds_write_b128 v133, v[80:83] offset:2048\n" \
    "ds_write_b128 v133, v[84:87] offset:3072\n" \
    "ds_write_b128 v133, v[88:91] offset:16384\n" \
    "ds_write_b128 v133, v[92:95] offset:17408\n" \
    "ds_write_b128 v133, v[40:43] offset:18432\n" \
    "ds_write_b128 v133, v[44:47] offset:19456\n" \
    "ds_write_b128 v133, v[48:51] offset:32768\n" \
    "ds_write_b128 v133, v[52:55] offset:33792\n" \
    "ds_write_b128 v133, v[56:59] offset:34816\n" \
    "ds_write_b128 v133, v[60:63] offset:35840\n" \
    "ds_write_b128 v133, v[64:67] offset:49152\n" \
    "ds_write_b128 v133, v[68:71] offset:50176\n" \
    "ds_write_b128 v133, v[98:101] offset:51200\n" \
    "s_waitcnt lgkmcnt(14)\n" \
    "ds_write_b128 v133, v[102:105] offset:52224\n" \
    "s_waitcnt vmcnt(8) lgkmcnt(0)\n" \
    "s_barrier\n" \
    "ds_read_b64_tr_b16 v[40:41], v119 offset:0\n" \
    "ds_read_b64_tr_b16 v[42:43], v18 offset:512\n" \
    "ds_read_b64_tr_b16 v[44:45], v119 offset:8192\n" \
    "ds_read_b64_tr_b16 v[46:47], v18 offset:8704\n" \
    "ds_read_b64_tr_b16 v[48:49], v17 offset:0\n" \
    "ds_read_b64_tr_b16 v[50:51], v19 offset:512\n" \
    "ds_read_b64_tr_b16 v[52:53], v17 offset:8192\n" \
    "ds_read_b64_tr_b16 v[54:55], v19 offset:8704\n" \
    "ds_read_b64_tr_b16 v[56:57], v18 offset:0\n" \
    "ds_read_b64_tr_b16 v[58:59], v119 offset:512\n" \
    "ds_read_b64_tr_b16 v[60:61], v18 offset:8192\n" \
    "ds_read_b64_tr_b16 v[62:63], v119 offset:8704\n" \
    "s_add_i32 m0, s95, 0\n" \
    "s_nop 0\n" \
    "buffer_load_dwordx4 v36, %[rw], s64 offen sc1 lds\n" \
    "s_add_i32 m0, s95, 896\n" \
    "s_nop 0\n" \
    "buffer_load_dwordx4 v36, %[rw], s64 offen offset:128 sc1 lds\n" \
    "s_add_i32 m0, s95, 8192\n" \
    "s_nop 0\n" \
    "buffer_load_dwordx4 v37, %[rw], s93 offen sc1 lds\n" \
    "s_add_i32 m0, s95, 8960\n" \
    "s_nop 0\n" \
    "buffer_load_dwordx4 v37, %[rw], s93 offen offset:256 sc1 lds\n" \
    "s_waitcnt lgkmcnt(8)\n" \
    "ds_read_b64_tr_b16 v[64:65], v19 offset:0\n" \
    "ds_read_b64_tr_b16 v[66:67], v17 offset:512\n" \
    "ds_read_b64_tr_b16 v[68:69], v19 offset:8192\n" \
    "ds_read_b64_tr_b16 v[70:71], v17 offset:8704\n" \
    "s_waitcnt vmcnt(4) lgkmcnt(0)\n" \
    "s_barrier\n" \
    "ds_read_b64_tr_b16 v[0:1], v119 offset:32768\n" \
    "ds_read_b64_tr_b16 v[2:3], v18 offset:33280\n" \
    "ds_read_b64_tr_b16 v[4:5], v119 offset:40960\n" \
    "ds_read_b64_tr_b16 v[6:7], v18 offset:41472\n" \
    "ds_write_b128 v106, v[40:43] offset:0\n" \
    "ds_write_b128 v106, v[44:47] offset:1024\n" \
    "ds_read_b64_tr_b16 v[8:9], v17 offset:32768\n" \
    "ds_read_b64_tr_b16 v[10:11], v19 offset:33280\n" \
    "ds_read_b64_tr_b16 v[12:13], v17 offset:40960\n" \
    "ds_read_b64_tr_b16 v[14:15], v19 offset:41472\n" \
    "ds_write_b128 v106, v[48:51] offset:2048\n" \
    "ds_write_b128 v106, v[52:55] offset:3072\n" \
    "s_waitcnt lgkmcnt(8)\n" \
    "ds_read_b64_tr_b16 v[40:41], v18 offset:32768\n" \
    "ds_read_b64_tr_b16 v[42:43], v119 offset:33280\n" \
    "ds_read_b64_tr_b16 v[44:45], v18 offset:40960\n" \
    "ds_read_b64_tr_b16 v[46:47], v119 offset:41472\n" \
    "s_waitcnt lgkmcnt(6)\n" \
    "ds_read_b64_tr_b16 v[48:49], v19 offset:32768\n" \
    "ds_read_b64_tr_b16 v[50:51], v17 offset:33280\n" \
    "ds_read_b64_tr_b16 v[52:53], v19 offset:40960\n" \
    "ds_read_b64_tr_b16 v[54:55], v17 offset:41472\n" \
    "ds_write_b128 v106, v[56:59] offset:4096\n" \
    "ds_write_b128 v106, v[60:63] offset:5120\n" \
    "ds_write_b128 v106, v[64:67] offset:6144\n" \
    "ds_write_b128 v106, v[68:71] offset:7168\n" \
    "s_waitcnt lgkmcnt(4)\n" \
    "s_barrier\n" \
    "ds_write_b128 v106, v[0:3] offset:32768\n" \
    "ds_write_b128 v106, v[4:7] offset:33792\n" \
    "ds_write_b128 v106, v[8:11] offset:34816\n" \
    "ds_write_b128 v106, v[12:15] offset:35840\n" \
    "ds_write_b128 v106, v[40:43] offset:36864\n" \
    "ds_write_b128 v106, v[44:47] offset:37888\n" \
    "ds_write_b128 v106, v[48:51] offset:38912\n" \
    "ds_write_b128 v106, v[52:55] offset:39936\n" \
    "v_mov_b64 v[56:57], 0\n" \
    "v_mov_b64 v[58:59], 0\n" \
    "v_mov_b64 v[60:61], 0\n" \
    "v_mov_b64 v[62:63], 0\n" \
    "v_mov_b64 v[64:65], 0\n" \
    "v_mov_b64 v[66:67], 0\n" \
    "v_mov_b64 v[68:69], 0\n" \
    "v_mov_b64 v[70:71], 0\n" \
    "s_waitcnt lgkmcnt(6)\n" \
    "ds_read_b128 v[0:3], v108 offset:0\n" \
    "ds_read_b128 v[16:19], v108 offset:4096\n" \
    "s_waitcnt lgkmcnt(2)\n" \
    "v_mov_b64 v[40:41], 0\n" \
    "v_mov_b64 v[42:43], 0\n" \
    "v_mov_b64 v[44:45], 0\n" \
    "v_mov_b64 v[46:47], 0\n" \
    "v_mov_b64 v[48:49], 0\n" \
    "v_mov_b64 v[50:51], 0\n" \
    "v_mov_b64 v[52:53], 0\n" \
    "v_mov_b64 v[54:55], 0\n" \
    "73:\n" \
    "s_waitcnt vmcnt(0) lgkmcnt(0)\n" \
    "ds_read_b128 v[4:7], v21 offset:0\n" \
    "ds_read_b128 v[8:11], v21 offset:1024\n" \
    "ds_read_b128 v[12:15], v21 offset:8192\n" \
    "ds_read_b128 v[102:105], v21 offset:9216\n" \
    "v_mov_b32 v107, 0\n" \
    "s_waitcnt lgkmcnt(3)\n" \
    "v_max3_u32 v107, v107, v4, v5\n" \
    "v_max3_u32 v107, v107, v6, v7\n" \
    "s_waitcnt lgkmcnt(2)\n" \
    "v_max3_u32 v107, v107, v8, v9\n" \
    "v_max3_u32 v107, v107, v10, v11\n" \
    "s_waitcnt lgkmcnt(1)\n" \
    "v_max3_u32 v107, v107, v12, v13\n" \
    "v_max3_u32 v107, v107, v14, v15\n" \
    "s_waitcnt lgkmcnt(0)\n" \
    "v_max3_u32 v107, v107, v102, v103\n" \
    "v_max3_u32 v107, v107, v104, v105\n" \
    "v_cmp_eq_u32 vcc, -1, v107\n" \
    "s_add_i32 s39, s39, -1\n" \
    "s_cmp_lt_i32 s39, 0\n" \
    "s_nop 2\n" \
    "s_cbranch_vccnz 75f\n" \
    "74:\n" \
    "s_barrier\n" \
    "s_memrealtime s[88:89]\n" \
    "s_pack_ll_b32_b16 s80, 1, s42\n" \
    "v_mov_b32 v5, s80\n" \
    "s_cmp_eq_u32 s60, 0\n" \
    "s_cselect_b32 s75, 0x1010101, 0\n" \
    "s_waitcnt lgkmcnt(0)\n" \
    "s_lshl3_add_u32 s80, s88, s37\n" \
    "v_mov_b32 v4, s80\n" \
    "s_mov_b32 exec_lo, s75\n" \
    "s_mov_b32 exec_hi, 0\n" \
    "buffer_store_dwordx2 v[4:5], v127, %[rs], s61 offen sc1\n" \
    "s_mov_b64 exec, -1\n" \
    "s_branch 3f\n" \
    "75:\n" \
    "s_cbranch_scc1 74b\n" \
    "s_add_i32 m0, s95, 0\n" \
    "s_nop 0\n" \
    "buffer_load_dwordx4 v36, %[rw], s64 offen sc1 lds\n" \
    "s_add_i32 m0, s95, 896\n" \
    "s_nop 0\n" \
    "buffer_load_dwordx4 v36, %[rw], s64 offen offset:128 sc1 lds\n" \
    "s_add_i32 m0, s95, 8192\n" \
    "s_nop 0\n" \
    "buffer_load_dwordx4 v37, %[rw], s93 offen sc1 lds\n" \
    "s_add_i32 m0, s95, 8960\n" \
    "s_nop 0\n" \
    "buffer_load_dwordx4 v37, %[rw], s93 offen offset:256 sc1 lds\n" \
    "s_branch 73b\n" \
    "17:\n" \
    "s_sub_i32 s37, s37, s29\n" \
    "12:\n" \
    "1:\n" \
    "s_memtime vcc\n" \
    "s_mul_i32 s90, s37, s25\n" \
    "s_ashr_i32 s90, s90, 12\n" \
    "s_add_i32 s90, s90, s24\n" \
    "s_add_i32 s90, s90, -116\n" \
    "s_waitcnt lgkmcnt(0)\n" \
    "s_sub_i32 s75, s90, vcc_lo\n" \
    "s_cmp_gt_i32 s75, 112\n" \
    "s_cbranch_scc1 1b\n" \
    "s_max_i32 s75, s75, 0\n" \
    "s_and_b32 s75, s75, -4\n" \
    "s_sub_u32 vcc_lo, s96, s75\n" \
    "s_subb_u32 vcc_hi, s97, 0\n" \
    "s_setpc_b64 vcc\n" \
    "s_nop 0\n" \
    "s_nop 0\n" \
    "s_nop 0\n" \
    "s_nop 0\n" \
    "s_nop 0\n" \
    "s_nop 0\n" \
    "s_nop 0\n" \
    "s_nop 0\n" \
    "s_nop 0\n" \
    "s_nop 0\n" \
    "s_nop 0\n" \
    "s_nop 0\n" \
    "s_nop 0\n" \
    "s_nop 0\n" \
    "s_nop 0\n" \
    "s_nop 0\n" \
    "s_nop 0\n" \
    "s_nop 0\n" \
    "s_nop 0\n" \
    "s_nop 0\n" \
    "s_nop 0\n" \
    "s_nop 0\n" \
    "s_nop 0\n" \
    "s_nop 0\n" \
    "s_nop 0\n" \
    "s_nop 0\n" \
    "s_nop 0\n" \
    "s_nop 0\n" \
    "2:\n" \
    "buffer_load_dwordx4 v[0:3], v125, %[rs], s69 offen sc1\n" \
    "s_nop 3\n" \
    "s_nop 3\n" \
    "buffer_load_dwordx4 v[16:19], v125, %[rs], s70 offen sc1\n" \
    "s_mov_b32 exec_lo, s27\n" \
    "s_mov_b32 exec_hi, s27\n" \
    "buffer_load_dwordx4 v22, %[rs], s71 offen sc1 lds\n" \
    "s_mov_b64 exec, -1\n" \
    "v_bfe_u32 v20, v127, 8, 2\n" \
    "v_subrev_u32 v20, s60, v20\n" \
    "v_mul_i32_i24 v20, -1088, v20\n" \
    "v_add_u32 v20, v20, v129\n" \
    "v_lshrrev_b32 v21, 2, v127\n" \
    "s_mov_b32 exec_lo, s66\n" \
    "s_mov_b32 exec_hi, s66\n" \
    "ds_read2_b32 v[96:97], v20 offset0:0 offset1:144\n" \
    "s_mov_b64 exec, -1\n" \
    "s_lshl_b32 s80, s36, 16\n" \
    "s_add_i32 s80, s80, s67\n" \
    "s_sub_i32 s75, s37, s79\n" \
    "s_cmp_gt_i32 s75, 24\n" \
    "s_cselect_b32 s76, 2, 0\n" \
    "s_cmp_lt_i32 s75, 10\n" \
    "s_cselect_b32 s76, -6, s76\n" \
    "s_add_i32 s28, s28, s76\n" \
    "s_max_i32 s28, s28, 0\n" \
    "s_min_i32 s28, s28, 20\n" \
    "s_cmp_eq_u32 s60, 0\n" \
    "s_cselect_b32 s72, 0x1010101, 0\n" \
    "s_waitcnt lgkmcnt(0)\n" \
    "buffer_store_dword v96, v21, %[rw], s80 offen sc1\n" \
    "buffer_store_dword v97, v21, %[rw], s80 offen offset:256 sc1\n" \
    "3:\n" \
    "s_mov_b32 m0, s95\n" \
    "s_add_i32 s91, s36, 1\n" \
    "s_and_b32 s78, s36, 3\n" \
    "s_lshl_b32 s78, s78, 14\n" \
    "s_add_i32 s77, s78, s61\n" \
    "s_bitcmp1_b32 s36, 0\n" \
    "s_cselect_b32 s79, 0x8000, 0\n" \
    "s_lshl_b32 s92, s91, 18\n" \
    "s_add_i32 s92, s92, s64\n" \
    "s_add_i32 s78, s78, 0x1a0000\n" \
    "s_add_i32 s93, s92, 0x1000000\n" \
    "s_sub_i32 s76, s33, s42\n" \
    "s_ashr_i32 s76, s76, s32\n" \
    "s_add_i32 s42, s42, s76\n" \
    "s_lshr_b32 s76, s32, 8\n" \
    "s_sub_i32 s76, s76, s40\n" \
    "s_ashr_i32 s76, s76, 2\n" \
    "s_add_i32 s40, s40, s76\n" \
    "s_sub_i32 s76, s40, s24\n" \
    "s_min_i32 s94, s91, s38\n" \
    "s_lshl_b32 s94, s94, 18\n" \
    "s_andn2_b32 s80, 0x1000000, s66\n" \
    "s_add_i32 s94, s94, s64\n" \
    "s_add_i32 s94, s94, s80\n" \
    "s_add_i32 s75, s36, 2\n" \
    "s_cmp_lt_i32 s38, s75\n" \
    "s_cselect_b32 s75, s62, 0x1b0040\n" \
    "v_mov_b64 v[98:99], 0\n" \
    "v_mov_b32 v101, 0\n" \
    "s_pack_ll_b32_b16 s80, s91, s42\n" \
    "v_mov_b32 v103, s80\n" \
    "s_waitcnt vmcnt(4)\n" \
    "v_smfmac_f32_16x16x64_bf16 v[40:43], v[0:3], a[0:7], v124\n" \
    "ds_write_b128 v108, v[0:3] offset:0\n" \
    "v_smfmac_f32_16x16x64_bf16 v[44:47], v[0:3], a[8:15], v124\n" \
    "v_smfmac_f32_16x16x64_bf16 v[48:51], v[0:3], a[128:135], v124\n" \
    "v_smfmac_f32_16x16x64_bf16 v[52:55], v[0:3], a[136:143], v124\n" \
    "s_waitcnt vmcnt(3)\n" \
    "v_smfmac_f32_16x16x64_bf16 v[40:43], v[16:19], a[64:71], v124\n" \
    "ds_write_b128 v108, v[16:19] offset:4096\n" \
    "v_smfmac_f32_16x16x64_bf16 v[44:47], v[16:19], a[72:79], v124\n" \
    "s_waitcnt lgkmcnt(1)\n" \
    "s_barrier\n" \
    "v_smfmac_f32_16x16x64_bf16 v[48:51], v[16:19], a[192:199], v124\n" \
    "ds_read_b128 v[4:7], v24 offset:0\n" \
    "v_smfmac_f32_16x16x64_bf16 v[52:55], v[16:19], a[200:207], v124\n" \
    "ds_read_b128 v[8:11], v28 offset:0\n" \
    "v_smfmac_f32_16x16x64_bf16 v[56:59], v[0:3], v[72:79], v124\n" \
    "ds_read_b128 v[12:15], v32 offset:0\n" \
    "v_smfmac_f32_16x16x64_bf16 v[60:63], v[0:3], v[80:87], v124\n" \
    "s_waitcnt vmcnt(2) lgkmcnt(3)\n" \
    "s_barrier\n" \
    "s_memtime s[86:87]\n" \
    "v_smfmac_f32_16x16x64_bf16 v[56:59], v[16:19], v[168:175], v124\n" \
    "ds_read_b128 v[24:27], v24 offset:4096\n" \
    "v_smfmac_f32_16x16x64_bf16 v[60:63], v[16:19], v[176:183], v124\n" \
    "ds_read_b128 v[28:31], v28 offset:4096\n" \
    "s_waitcnt lgkmcnt(3)\n" \
    "v_smfmac_f32_16x16x64_bf16 v[40:43], v[4:7], a[16:23], v124\n" \
    "ds_read_b128 v[32:35], v32 offset:4096\n" \
    "v_smfmac_f32_16x16x64_bf16 v[40:43], v[8:11], a[32:39], v124\n" \
    "ds_read_b128 v[36:39], v126 offset:0\n" \
    "s_waitcnt lgkmcnt(4)\n" \
    "v_smfmac_f32_16x16x64_bf16 v[40:43], v[12:15], a[48:55], v124\n" \
    "buffer_load_dword v110, v114, %[rw], s94 offen sc1\n" \
    "v_smfmac_f32_16x16x64_bf16 v[44:47], v[4:7], a[24:31], v124\n" \
    "s_waitcnt lgkmcnt(2)\n" \
    "v_smfmac_f32_16x16x64_bf16 v[40:43], v[24:27], a[80:87], v124\n" \
    "v_smfmac_f32_16x16x64_bf16 v[40:43], v[28:31], a[96:103], v124\n" \
    "s_waitcnt lgkmcnt(0)\n" \
    "v_smfmac_f32_16x16x64_bf16 v[40:43], v[32:35], a[112:119], v124\n" \
    "ds_read_b128 v[72:75], v133 offset:18432\n" \
    "s_add_i32 s80, s86, s76\n" \
    "v_smfmac_f32_16x16x64_bf16 v[40:43], v[36:39], v[136:143], v124\n" \
    "ds_read_b128 v[76:79], v133 offset:19456\n" \
    "s_mul_hi_u32 s80, s80, s41\n" \
    "v_smfmac_f32_16x16x64_bf16 v[44:47], v[8:11], a[40:47], v124\n" \
    "ds_read_b128 v[80:83], v133 offset:32768\n" \
    "s_add_i32 s80, s80, s29\n" \
    "v_mov_b32 v102, s80\n" \
    "v_smfmac_f32_16x16x64_bf16 v[44:47], v[12:15], a[56:63], v124\n" \
    "ds_read_b128 v[84:87], v133 offset:33792\n" \
    "v_add_f32 v40, v40, v42\n" \
    "v_add_f32 v41, v41, v43\n" \
    "v_smfmac_f32_16x16x64_bf16 v[44:47], v[24:27], a[88:95], v124\n" \
    "ds_read_b128 v[20:23], v127 offset:19456\n" \
    "v_cmp_u_f32 vcc, v40, v41\n" \
    "v_smfmac_f32_16x16x64_bf16 v[44:47], v[28:31], a[104:111], v124\n" \
    "v_mul_f32 v40, s81, v40\n" \
    "v_smfmac_f32_16x16x64_bf16 v[44:47], v[32:35], a[120:127], v124\n" \
    "s_cbranch_vccnz 5f\n" \
    "6:\n" \
    "s_mov_b64 exec, s[72:73]\n" \
    "v_smfmac_f32_16x16x64_bf16 v[44:47], v[36:39], v[144:151], v124\n" \
    "buffer_store_dwordx2 v[102:103], v127, %[rs], s77 offen sc1\n" \
    "v_smfmac_f32_16x16x64_bf16 v[56:59], v[4:7], v[88:95], v124\n" \
    "s_mov_b64 exec, -1\n" \
    "s_waitcnt lgkmcnt(1)\n" \
    "v_mul_f32 v41, s81, v41\n" \
    "v_smfmac_f32_16x16x64_bf16 v[60:63], v[4:7], v[72:79], v124\n" \
    "ds_read_b128 v[88:91], v133 offset:34816\n" \
    "v_add_f32 v44, v44, v46\n" \
    "v_smfmac_f32_16x16x64_bf16 v[56:59], v[8:11], v[80:87], v124\n" \
    "ds_read_b128 v[92:95], v133 offset:35840\n" \
    "v_add_f32 v45, v45, v47\n" \
    "v_smfmac_f32_16x16x64_bf16 v[48:51], v[4:7], a[144:151], v124\n" \
    "ds_read_b128 v[72:75], v133 offset:49152\n" \
    "v_mul_f32 v44, s81, v44\n" \
    "v_smfmac_f32_16x16x64_bf16 v[52:55], v[4:7], a[152:159], v124\n" \
    "ds_read_b128 v[76:79], v133 offset:50176\n" \
    "v_mul_f32 v45, s81, v45\n" \
    "v_smfmac_f32_16x16x64_bf16 v[48:51], v[8:11], a[160:167], v124\n" \
    "ds_read_b128 v[0:3], v127 offset:16384\n" \
    "v_cvt_pk_bf16_f32 v107, v40, v44\n" \
    "v_smfmac_f32_16x16x64_bf16 v[52:55], v[8:11], a[168:175], v124\n" \
    "ds_read_b128 v[4:7], v127 offset:18432\n" \
    "s_waitcnt lgkmcnt(4)\n" \
    "v_cvt_pk_bf16_f32 v108, v41, v45\n" \
    "v_smfmac_f32_16x16x64_bf16 v[60:63], v[8:11], v[88:95], v124\n" \
    "ds_write2_b32 v128, v107, v108 offset0:0 offset1:80\n" \
    "v_smfmac_f32_16x16x64_bf16 v[60:63], v[12:15], v[248:255], v124\n" \
    "ds_read_b128 v[16:19], v127 offset:17408\n" \
    "s_waitcnt lgkmcnt(4)\n" \
    "v_smfmac_f32_16x16x64_bf16 v[56:59], v[12:15], v[72:79], v124\n" \
    "ds_read_b128 v[8:11], v127 offset:20480\n" \
    "v_smfmac_f32_16x16x64_bf16 v[48:51], v[12:15], a[176:183], v124\n" \
    "ds_read_b128 v[40:43], v127 offset:21504\n" \
    "v_smfmac_f32_16x16x64_bf16 v[52:55], v[12:15], a[184:191], v124\n" \
    "ds_read_b128 v[44:47], v127 offset:23552\n" \
    "v_smfmac_f32_16x16x64_bf16 v[48:51], v[24:27], a[208:215], v124\n" \
    "s_waitcnt lgkmcnt(4)\n" \
    "s_barrier\n" \
    "v_smfmac_f32_16x16x64_bf16 v[52:55], v[24:27], a[216:223], v124\n" \
    "ds_read_b128 v[72:75], v130 offset:0\n" \
    "v_smfmac_f32_16x16x64_bf16 v[48:51], v[28:31], a[224:231], v124\n" \
    "ds_read_b128 v[76:79], v130 offset:64\n" \
    "v_smfmac_f32_16x16x64_bf16 v[48:51], v[32:35], a[240:247], v124\n" \
    "ds_read_b128 v[80:83], v130 offset:128\n" \
    "v_smfmac_f32_16x16x64_bf16 v[48:51], v[36:39], v[152:159], v124\n" \
    "ds_read_b128 v[84:87], v130 offset:192\n" \
    "v_smfmac_f32_16x16x64_bf16 v[52:55], v[28:31], a[232:239], v124\n" \
    "ds_read_b128 v[12:15], v127 offset:22528\n" \
    "v_smfmac_f32_16x16x64_bf16 v[52:55], v[32:35], a[248:255], v124\n" \
    "v_add_f32 v48, v48, v50\n" \
    "v_smfmac_f32_16x16x64_bf16 v[52:55], v[36:39], v[160:167], v124\n" \
    "s_waitcnt lgkmcnt(3)\n" \
    "v_add_f32 v49, v49, v51\n" \
    "v_mfma_f32_16x16x32_bf16 v[88:91], v[0:3], v[72:75], 0\n" \
    "ds_read_b64 v[0:1], v132 offset:0\n" \
    "ds_read_b64 v[2:3], v132 offset:1024\n" \
    "v_mfma_f32_16x16x32_bf16 v[88:91], v[4:7], v[76:79], v[88:91]\n" \
    "s_waitcnt lgkmcnt(2)\n" \
    "v_add_f32 v52, v52, v54\n" \
    "v_add_f32 v53, v53, v55\n" \
    "v_mfma_f32_16x16x32_bf16 v[88:91], v[8:11], v[80:83], v[88:91]\n" \
    "v_cvt_pk_bf16_f32 v107, v48, v52\n" \
    "v_cvt_pk_bf16_f32 v108, v49, v53\n" \
    "v_mfma_f32_16x16x32_bf16 v[88:91], v[12:15], v[84:87], v[88:91]\n" \
    "ds_write2_b32 v129, v107, v108 offset0:0 offset1:144\n" \
    "v_mfma_f32_16x16x32_bf16 v[92:95], v[16:19], v[72:75], 0\n" \
    "ds_read_b64 v[4:5], v132 offset:512\n" \
    "ds_read_b64 v[6:7], v132 offset:1536\n" \
    "v_mfma_f32_16x16x32_bf16 v[92:95], v[20:23], v[76:79], v[92:95]\n" \
    "ds_read_b128 v[8:11], v106 offset:0\n" \
    "v_exp_f32 v88, v88\n" \
    "v_mfma_f32_16x16x32_bf16 v[92:95], v[40:43], v[80:83], v[92:95]\n" \
    "ds_read_b128 v[12:15], v106 offset:1024\n" \
    "v_exp_f32 v89, v89\n" \
    "v_mfma_f32_16x16x32_bf16 v[92:95], v[44:47], v[84:87], v[92:95]\n" \
    "s_waitcnt lgkmcnt(4)\n" \
    "s_barrier\n" \
    "v_smfmac_f32_16x16x64_bf16 v[56:59], v[24:27], v[184:191], v124\n" \
    "ds_read_b128 v[48:51], v131 offset:0\n" \
    "ds_read_b128 v[52:55], v131 offset:128\n" \
    "v_cvt_pk_bf16_f32 v88, v88, v89\n" \
    "v_smfmac_f32_16x16x64_bf16 v[56:59], v[28:31], v[200:207], v124\n" \
    "ds_read_b128 v[16:19], v106 offset:32768\n" \
    "v_exp_f32 v90, v90\n" \
    "v_smfmac_f32_16x16x64_bf16 v[56:59], v[32:35], v[216:223], v124\n" \
    "ds_read_b128 v[20:23], v106 offset:33792\n" \
    "v_exp_f32 v91, v91\n" \
    "v_smfmac_f32_16x16x64_bf16 v[56:59], v[36:39], v[232:239], v124\n" \
    "ds_read_b128 v[40:43], v106 offset:2048\n" \
    "s_waitcnt lgkmcnt(3)\n" \
    "v_exp_f32 v92, v92\n" \
    "v_cvt_pk_bf16_f32 v89, v90, v91\n" \
    "v_smfmac_f32_16x16x64_bf16 v[64:67], v[48:51], v[72:79], v124\n" \
    "ds_read_b128 v[44:47], v106 offset:3072\n" \
    "v_exp_f32 v93, v93\n" \
    "v_smfmac_f32_16x16x64_bf16 v[64:67], v[52:55], v[80:87], v124\n" \
    "ds_read_b128 v[72:75], v106 offset:34816\n" \
    "v_exp_f32 v94, v94\n" \
    "v_cvt_pk_bf16_f32 v90, v92, v93\n" \
    "v_smfmac_f32_16x16x64_bf16 v[60:63], v[24:27], v[192:199], v124\n" \
    "ds_read_b128 v[76:79], v106 offset:35840\n" \
    "v_exp_f32 v95, v95\n" \
    "v_smfmac_f32_16x16x64_bf16 v[60:63], v[28:31], v[208:215], v124\n" \
    "ds_read_b128 v[80:83], v106 offset:4096\n" \
    "s_cmp_eq_u32 s36, 62\n" \
    "v_add_f32 v64, v64, v66\n" \
    "v_add_f32 v65, v65, v67\n" \
    "v_smfmac_f32_16x16x64_bf16 v[60:63], v[32:35], v[224:231], v124\n" \
    "ds_read_b128 v[84:87], v106 offset:5120\n" \
    "v_exp_f32 v64, v64\n" \
    "v_cvt_pk_bf16_f32 v91, v94, v95\n" \
    "v_smfmac_f32_16x16x64_bf16 v[60:63], v[36:39], v[240:247], v124\n" \
    "ds_read_b128 v[48:51], v106 offset:36864\n" \
    "v_exp_f32 v65, v65\n" \
    "v_swap_b32 v1, v2\n" \
    "v_swap_b32 v5, v6\n" \
    "v_mfma_f32_16x16x32_bf16 v[102:105], v[88:91], v[120:123], 0\n" \
    "ds_read_b128 v[52:55], v106 offset:37888\n" \
    "v_cvt_pk_bf16_f32 v100, v64, v65\n" \
    "v_add_f32 v56, v56, v58\n" \
    "v_add_f32 v57, v57, v59\n" \
    "v_smfmac_f32_16x16x64_bf16 v[68:71], v[88:91], v[0:7], v124\n" \
    "ds_read_b128 v[24:27], v106 offset:6144\n" \
    "v_add_f32 v60, v60, v62\n" \
    "v_add_f32 v61, v61, v63\n" \
    "v_mfma_f32_16x16x16_bf16 v[102:105], v[100:101], v[120:121], v[102:105]\n" \
    "ds_read_b128 v[28:31], v106 offset:7168\n" \
    "v_cvt_pk_bf16_f32 v96, v56, v57\n" \
    "v_cvt_pk_bf16_f32 v97, v60, v61\n" \
    "s_nop 1\n" \
    "v_smfmac_f32_16x16x32_bf16 v[68:71], v[100:101], v[96:99], v124\n" \
    "ds_read_b128 v[32:35], v106 offset:38912\n" \
    "v_mov_b64 v[56:57], 0\n" \
    "v_mov_b64 v[58:59], 0\n" \
    "v_rcp_f32 v104, v102\n" \
    "v_rcp_f32 v105, v103\n" \
    "v_mul_f32 v68, v68, v104\n" \
    "v_mul_f32 v70, v70, v104\n" \
    "v_mul_f32 v69, v69, v105\n" \
    "v_mul_f32 v71, v71, v105\n" \
    "v_cvt_pk_bf16_f32 v107, v68, v70\n" \
    "v_cvt_pk_bf16_f32 v108, v69, v71\n" \
    "ds_write2_b32 v129, v107, v108 offset0:64 offset1:208\n" \
    "v_max3_f32 v102, v102, v103, v104\n" \
    "v_max_f32 v102, v102, v105\n" \
    "v_cmp_nge_f32 vcc, s82, v102\n" \
    "v_mov_b64 v[60:61], 0\n" \
    "v_mov_b64 v[62:63], 0\n" \
    "v_mov_b64 v[88:89], 0\n" \
    "v_mov_b64 v[90:91], 0\n" \
    "v_mov_b64 v[92:93], 0\n" \
    "v_mov_b64 v[94:95], 0\n" \
    "s_waitcnt vmcnt(2) lgkmcnt(0)\n" \
    "s_barrier\n" \
    "ds_read_b128 v[68:71], v109 offset:256\n" \
    "ds_read_b128 v[102:105], v111 offset:384\n" \
    "s_cbranch_vccnz 7f\n" \
    "buffer_load_dwordx2 v[98:99], v113, %[rs], s78 offen sc1\n" \
    "buffer_load_dword v100, off, %[rw], s75 sc1\n" \
    "v_mov_b64 v[64:65], 0\n" \
    "v_mov_b64 v[66:67], 0\n" \
    "s_waitcnt lgkmcnt(1)\n" \
    "v_smfmac_f32_16x16x64_bf16 v[56:59], v[68:71], v[8:15], v124\n" \
    "s_waitcnt lgkmcnt(0)\n" \
    "ds_read_b128 v[36:39], v106 offset:39936\n" \
    "v_smfmac_f32_16x16x64_bf16 v[56:59], v[102:105], v[16:23], v124\n" \
    "ds_read_b128 v[0:3], v133 offset:51200\n" \
    "v_smfmac_f32_16x16x64_bf16 v[60:63], v[68:71], v[40:47], v124\n" \
    "ds_read_b128 v[4:7], v133 offset:52224\n" \
    "v_smfmac_f32_16x16x64_bf16 v[60:63], v[102:105], v[72:79], v124\n" \
    "ds_read_b128 v[16:19], v112 offset:51200\n" \
    "v_add_f32 v56, v56, v58\n" \
    "v_smfmac_f32_16x16x64_bf16 v[88:91], v[68:71], v[80:87], v124\n" \
    "ds_read_b128 v[20:23], v112 offset:52224\n" \
    "v_add_f32 v57, v57, v59\n" \
    "v_smfmac_f32_16x16x64_bf16 v[88:91], v[102:105], v[48:55], v124\n" \
    "v_add_f32 v60, v60, v62\n" \
    "v_smfmac_f32_16x16x64_bf16 v[92:95], v[68:71], v[24:31], v124\n" \
    "s_waitcnt lgkmcnt(4)\n" \
    "v_add_f32 v61, v61, v63\n" \
    "v_smfmac_f32_16x16x64_bf16 v[92:95], v[102:105], v[32:39], v124\n" \
    "s_waitcnt lgkmcnt(2)\n" \
    "v_add_f32 v88, v88, v90\n" \
    "v_smfmac_f32_16x16x64_bf16 v[64:67], v[68:71], v[0:7], v124\n" \
    "s_waitcnt lgkmcnt(0)\n" \
    "v_add_f32 v89, v89, v91\n" \
    "v_smfmac_f32_16x16x64_bf16 v[64:67], v[102:105], v[16:23], v124\n" \
    "v_add_f32 v92, v92, v94\n" \
    "v_add_f32 v93, v93, v95\n" \
    "v_cvt_pk_bf16_f32 v8, v56, v60\n" \
    "v_cvt_pk_bf16_f32 v10, v57, v61\n" \
    "v_cvt_pk_bf16_f32 v9, v88, v92\n" \
    "v_cvt_pk_bf16_f32 v11, v89, v93\n" \
    "buffer_store_dwordx4 v[8:11], v134, %[rs], s79 offen sc1\n" \
    "v_add_f32 v64, v64, v66\n" \
    "v_add_f32 v65, v65, v67\n" \
    "v_cvt_pk_bf16_f32 v64, v64, v65\n" \
    "buffer_store_dword v64, v135, %[rs], s79 offen sc1\n" \
    "s_cbranch_scc1 16f\n" \
    "s_memtime s[88:89]\n" \
    "s_memrealtime s[44:45]\n" \
    "s_and_b32 s75, s91, 3\n" \
    "s_lshl_b32 s75, s75, 16\n" \
    "s_memrealtime s[46:47]\n" \
    "s_add_i32 s69, s75, s26\n" \
    "s_xor_b32 s70, s69, 2048\n" \
    "s_memrealtime s[48:49]\n" \
    "s_andn2_b32 s71, s69, 0x1fff\n" \
    "s_add_i32 s71, s71, 32768\n" \
    "s_memrealtime s[50:51]\n" \
    "s_mov_b32 s94, 0\n" \
    "s_mov_b32 s43, 0\n" \
    "s_memrealtime s[52:53]\n" \
    "s_cmp_lt_i32 s38, s91\n" \
    "s_nop 0\n" \
    "s_memrealtime s[54:55]\n" \
    "s_nop 1\n" \
    "s_memrealtime s[56:57]\n" \
    "s_nop 1\n" \
    "s_memrealtime vcc\n" \
    "v_and_b32 v19, 0x380, v127\n" \
    "v_add_u32 v19, v19, v127\n" \
    "v_and_b32 v18, 0x300, v127\n" \
    "v_add_u32 v18, v18, v127\n" \
    "s_cbranch_scc1 40f\n" \
    "41:\n" \
    "buffer_load_dwordx4 v19, %[rw], s92 offen sc1 lds\n" \
    "s_add_i32 m0, s95, 896\n" \
    "s_waitcnt lgkmcnt(0)\n" \
    "s_add_i32 s44, s44, s46\n" \
    "s_add_i32 s48, s48, s50\n" \
    "s_add_i32 s52, s52, s54\n" \
    "s_add_i32 s44, s44, s48\n" \
    "s_add_i32 s52, s52, s56\n" \
    "s_add_i32 s44, s44, s52\n" \
    "s_add_i32 s80, s44, vcc_lo\n" \
    "s_sub_i32 s45, s80, s29\n" \
    "s_mov_b32 s29, s80\n" \
    "s_sub_i32 s47, s88, s24\n" \
    "s_mov_b32 s24, s88\n" \
    "buffer_load_dwordx4 v19, %[rw], s92 offen offset:128 sc1 lds\n" \
    "s_add_i32 m0, s95, 8192\n" \
    "s_add_i32 s76, s29, s42\n" \
    "v_add_u32 v16, -1, s36\n" \
    "v_mov_b32 v17, s76\n" \
    "s_mov_b64 exec, s[72:73]\n" \
    "buffer_store_dwordx2 v[16:17], off, %[rw], s65 sc1\n" \
    "s_mov_b64 exec, -1\n" \
    "ds_read_b128 v[72:75], v133 offset:0\n" \
    "ds_read_b128 v[76:79], v133 offset:1024\n" \
    "ds_read_b128 v[80:83], v133 offset:2048\n" \
    "ds_read_b128 v[84:87], v133 offset:3072\n" \
    "ds_read_b128 v[88:91], v133 offset:16384\n" \
    "ds_read_b128 v[92:95], v133 offset:17408\n" \
    "buffer_load_dwordx4 v18, %[rw], s93 offen sc1 lds\n" \
    "s_add_i32 m0, s95, 8960\n" \
    "v_mov_b64 v[40:41], 0\n" \
    "v_mov_b64 v[42:43], 0\n" \
    "v_mov_b64 v[44:45], 0\n" \
    "v_mov_b64 v[46:47], 0\n" \
    "v_mov_b64 v[48:49], 0\n" \
    "v_mov_b64 v[50:51], 0\n" \
    "v_mov_b64 v[52:53], 0\n" \
    "v_mov_b64 v[54:55], 0\n" \
    "buffer_load_dwordx4 v18, %[rw], s93 offen offset:256 sc1 lds\n" \
    "s_movk_i32 m0, 0x2000\n" \
    "v_mov_b64 v[56:57], 0\n" \
    "v_mov_b64 v[58:59], 0\n" \
    "v_mov_b64 v[60:61], 0\n" \
    "v_mov_b64 v[62:63], 0\n" \
    "v_mov_b64 v[64:65], 0\n" \
    "v_mov_b64 v[66:67], 0\n" \
    "v_mov_b64 v[68:69], 0\n" \
    "v_mov_b64 v[70:71], 0\n" \
    "v_add_u32 v108, s74, v127\n" \
    "v_xor_b32 v24, 0x400, v108\n" \
    "v_xor_b32 v28, 0x800, v108\n" \
    "v_xor_b32 v32, 0xc00, v108\n" \
    "v_and_b32 v22, 0x200, v127\n" \
    "s_and_b32 s76, s26, 0x800\n" \
    "v_mad_u32_u24 v22, v22, 3, v127\n" \
    "v_xor_b32 v22, s76, v22\n" \
    "s_waitcnt vmcnt(7)\n" \
    "v_cmp_ne_u16 vcc, s91, v99\n" \
    "v_readfirstlane_b32 s80, v100\n" \
    "s_max_i32 s38, s38, s80\n" \
    "s_cbranch_vccnz 15f\n" \
    "10:\n" \
    "v_subrev_u32 v20, s29, v98\n" \
    "s_bitcmp1_b32 s30, 15\n" \
    "v_min_i32 v20, 0x4000, v20\n" \
    "v_max_i32 v20, 0xffffc000, v20\n" \
    "s_cbranch_scc1 18f\n" \
    "19:\n" \
    "v_readlane_b32 s79, v20, s83\n" \
    "v_max_i32_dpp v20, v20, v20 row_shr:1 row_mask:0xf bank_mask:0xf\n" \
    "s_mul_i32 s49, s45, s25\n" \
    "s_lshr_b32 s49, s49, 12\n" \
    "v_max_i32_dpp v20, v20, v20 row_shr:2 row_mask:0xf bank_mask:0xf\n" \
    "s_sub_i32 s49, s47, s49\n" \
    "s_mul_hi_u32 s47, s47, s41\n" \
    "v_max_i32_dpp v20, v20, v20 row_shr:4 row_mask:0xf bank_mask:0xf\n" \
    "s_sub_i32 s47, s45, s47\n" \
    "s_lshl_b32 s47, s47, 19\n" \
    "v_max_i32_dpp v20, v20, v20 row_shr:8 row_mask:0xf bank_mask:0xf\n" \
    "s_cmp_lt_u32 s45, 3000\n" \
    "s_cselect_b32 s49, s49, 0\n" \
    "v_max_i32_dpp v20, v20, v20 row_bcast:15 row_mask:0xa bank_mask:0xf\n" \
    "s_cselect_b32 s47, s47, 0\n" \
    "s_lshl1_add_u32 s25, s49, s25\n" \
    "v_max_i32_dpp v20, v20, v20 row_bcast:31 row_mask:0xc bank_mask:0xf\n" \
    "s_add_i32 s41, s41, s47\n" \
    "s_add_i32 s76, s42, s28\n" \
    "s_add_i32 s79, s79, s76\n" \
    "v_readlane_b32 s37, v20, 63\n" \
    "s_add_i32 s37, s37, s76\n" \
    "s_cmp_lg_u32 s94, 0\n" \
    "s_cselect_b32 s36, s36, s91\n" \
    "s_cbranch_scc0 12b\n" \
    "s_branch 9f\n" \
    ".Lcold:\n" \
    "5:\n" \
    "v_mov_b32 v107, 0\n" \
    "v_max3_u32 v107, v107, v0, v1\n" \
    "v_max3_u32 v107, v107, v2, v3\n" \
    "v_max3_u32 v107, v107, v4, v5\n" \
    "v_max3_u32 v107, v107, v6, v7\n" \
    "v_max3_u32 v107, v107, v8, v9\n" \
    "v_max3_u32 v107, v107, v10, v11\n" \
    "v_max3_u32 v107, v107, v12, v13\n" \
    "v_max3_u32 v107, v107, v14, v15\n" \
    "v_max3_u32 v107, v107, v16, v17\n" \
    "v_max3_u32 v107, v107, v18, v19\n" \
    "v_max3_u32 v107, v107, v24, v25\n" \
    "v_max3_u32 v107, v107, v26, v27\n" \
    "v_max3_u32 v107, v107, v28, v29\n" \
    "v_max3_u32 v107, v107, v30, v31\n" \
    "v_max3_u32 v107, v107, v32, v33\n" \
    "v_max3_u32 v107, v107, v34, v35\n" \
    "v_max3_u32 v107, v107, v36, v37\n" \
    "v_max3_u32 v107, v107, v38, v39\n" \
    "v_cmp_eq_u32 vcc, -1, v107\n" \
    "s_nop 4\n" \
    "s_cbranch_vccz 6b\n" \
    "s_cmp_eq_u32 s36, 0\n" \
    "s_cbranch_scc1 6b\n" \
    "s_add_i32 s39, s39, -1\n" \
    "s_cmp_lt_i32 s39, 0\n" \
    "s_cbranch_scc1 6b\n" \
    "s_add_i32 s30, s30, 1\n" \
    "s_bitset1_b32 s30, 15\n" \
    "s_and_b32 s80, s31, 0xff\n" \
    "s_add_i32 s42, s42, s80\n" \
    "s_bfe_u32 s80, s31, 0x80008\n" \
    "s_add_i32 s40, s40, s80\n" \
    "s_waitcnt lgkmcnt(0)\n" \
    "s_mov_b32 s37, s29\n" \
    "s_nop 7\n" \
    "s_barrier\n" \
    "s_branch 0b\n" \
    "15:\n" \
    "s_add_i32 s30, s30, 0x10000\n" \
    "s_add_i32 s39, s39, -1\n" \
    "s_cmp_lt_i32 s39, 0\n" \
    "s_cbranch_scc1 10b\n" \
    "s_sleep 9\n" \
    "buffer_load_dwordx2 v[98:99], v113, %[rs], s78 offen sc1\n" \
    "s_waitcnt vmcnt(0)\n" \
    "v_cmp_ne_u16 vcc, s91, v99\n" \
    "s_nop 1\n" \
    "s_cbranch_vccnz 15b\n" \
    "s_memtime s[88:89]\n" \
    "s_memrealtime s[44:45]\n" \
    "s_nop 1\n" \
    "s_memrealtime s[46:47]\n" \
    "s_nop 1\n" \
    "s_memrealtime s[48:49]\n" \
    "s_nop 1\n" \
    "s_memrealtime s[50:51]\n" \
    "s_nop 1\n" \
    "s_memrealtime s[52:53]\n" \
    "s_nop 1\n" \
    "s_memrealtime s[54:55]\n" \
    "s_nop 1\n" \
    "s_memrealtime s[56:57]\n" \
    "s_nop 1\n" \
    "s_memrealtime vcc\n" \
    "s_waitcnt lgkmcnt(0)\n" \
    "s_add_i32 s44, s44, s46\n" \
    "s_add_i32 s48, s48, s50\n" \
    "s_add_i32 s52, s52, s54\n" \
    "s_add_i32 s44, s44, s48\n" \
    "s_add_i32 s52, s52, s56\n" \
    "s_add_i32 s44, s44, s52\n" \
    "s_add_i32 s29, s44, vcc_lo\n" \
    "s_mov_b32 s24, s88\n" \
    "s_mov_b32 s45, -1\n" \
    "s_branch 10b\n" \
    "18:\n" \
    "v_cmp_gt_i32 vcc, -320, v20\n" \
    "s_bitset0_b32 s30, 15\n" \
    "s_lshr_b32 s80, s31, 16\n" \
    "s_cmp_lt_u32 s36, 4\n" \
    "s_cselect_b32 s80, 0, s80\n" \
    "s_min_u32 s76, s80, 1\n" \
    "s_lshl_b32 s76, s76, 16\n" \
    "s_sub_i32 s31, s31, s76\n" \
    "s_mul_i32 s76, s80, 768\n" \
    "s_cmp_eq_u64 vcc, 0\n" \
    "s_cselect_b32 s80, 0, s80\n" \
    "s_cselect_b32 s76, s76, 0\n" \
    "s_add_i32 s33, s33, s80\n" \
    "s_add_i32 s32, s32, s76\n" \
    "s_branch 19b\n" \
    "40:\n" \
    "s_waitcnt vmcnt(2)\n" \
    "v_readfirstlane_b32 s80, v100\n" \
    "s_max_i32 s38, s38, s80\n" \
    "s_cmp_lt_i32 s38, s91\n" \
    "s_cbranch_scc0 41b\n" \
    "s_mov_b32 s94, 8\n" \
    "v_mov_b32 v18, 0x7ffffff0\n" \
    "v_mov_b32 v19, 0x7ffffff0\n" \
    "s_branch 41b\n" \
    "20:\n" \
    "s_mov_b32 s91, 63\n" \
    "s_mov_b32 s78, 0x1a8000\n" \
    "buffer_load_dwordx2 v[98:99], v113, %[rs], s78 offen sc1\n" \
    "buffer_load_dword v100, off, %[rw], s62 sc1\n" \
    "s_waitcnt vmcnt(0)\n" \
    "16:\n" \
    "v_and_b32 v112, 0x300, v127\n" \
    "v_add_u32 v112, v112, v127\n" \
    "s_add_i32 s44, %[qb], 0\n" \
    "s_add_i32 s45, s44, 4096\n" \
    "buffer_load_dwordx4 v[136:139], v112, %[rw], s44 offen offset:0 sc1\n" \
    "v_lshrrev_b32 v103, 1, v127\n" \
    "s_lshr_b32 s76, s74, 1\n" \
    "v_add_u32 v101, s76, v103\n" \
    "ds_write_b64 v101, v[96:97] offset:9216\n" \
    "ds_read_b128 v[0:3], v127 offset:16384\n" \
    "v_lshrrev_b32 v42, 8, v127\n" \
    "v_bfe_u32 v40, v127, 4, 4\n" \
    "ds_read_b128 v[4:7], v127 offset:17408\n" \
    "v_and_b32 v41, 2, v42\n" \
    "v_add_u32 v43, v41, v42\n" \
    "ds_read_b128 v[8:11], v127 offset:18432\n" \
    "s_lshl_b32 s76, s60, 4\n" \
    "s_and_b32 s76, s76, 16\n" \
    "v_add_u32 v40, s76, v40\n" \
    "v_mad_i32_i24 v40, v43, -4, v40\n" \
    "buffer_load_dwordx4 v[140:143], v112, %[rw], s44 offen offset:2048 sc1\n" \
    "ds_read_b128 v[12:15], v127 offset:19456\n" \
    "v_mov_b32 v43, 0xff61b1e6\n" \
    "v_cmp_gt_i32 vcc, 0, v40\n" \
    "ds_read_b128 v[16:19], v127 offset:20480\n" \
    "v_cndmask_b32 v88, 0, v43, vcc\n" \
    "v_cmp_gt_i32 vcc, 1, v40\n" \
    "ds_read_b128 v[20:23], v127 offset:21504\n" \
    "v_cndmask_b32 v89, 0, v43, vcc\n" \
    "v_cmp_gt_i32 vcc, 2, v40\n" \
    "ds_read_b128 v[24:27], v127 offset:22528\n" \
    "v_cndmask_b32 v90, 0, v43, vcc\n" \
    "v_cmp_gt_i32 vcc, 3, v40\n" \
    "buffer_load_dwordx4 v[144:147], v112, %[rw], s45 offen offset:0 sc1\n" \
    "ds_read_b128 v[28:31], v127 offset:23552\n" \
    "v_cndmask_b32 v91, 0, v43, vcc\n" \
    "v_cmp_gt_i32 vcc, 8, v40\n" \
    "ds_read_b128 v[32:35], v131 offset:0\n" \
    "v_cndmask_b32 v92, 0, v43, vcc\n" \
    "v_cmp_gt_i32 vcc, 9, v40\n" \
    "ds_read_b128 v[36:39], v131 offset:128\n" \
    "v_cndmask_b32 v93, 0, v43, vcc\n" \
    "v_cmp_gt_i32 vcc, 10, v40\n" \
    "ds_read_b128 v[56:59], v127 offset:24576\n" \
    "v_cndmask_b32 v94, 0, v43, vcc\n" \
    "v_cmp_gt_i32 vcc, 11, v40\n" \
    "buffer_load_dwordx4 v[148:151], v112, %[rw], s45 offen offset:2048 sc1\n" \
    "ds_read_b128 v[60:63], v127 offset:25600\n" \
    "v_cndmask_b32 v95, 0, v43, vcc\n" \
    "v_mad_u32_u24 v44, v41, 3, v42\n" \
    "ds_read_b128 v[64:67], v127 offset:26624\n" \
    "v_bfe_u32 v45, v127, 6, 1\n" \
    "v_bfe_u32 v46, v127, 4, 2\n" \
    "s_waitcnt lgkmcnt(8)\n" \
    "ds_read_b128 v[68:71], v127 offset:27648\n" \
    "v_lshl_add_u32 v44, v46, 1, v44\n" \
    "v_bfe_u32 v46, v127, 7, 1\n" \
    "ds_read_b128 a[120:123], v127 offset:28672\n" \
    "v_lshl_add_u32 v44, v44, 1, v46\n" \
    "v_mul_u32_u24 v44, 4352, v44\n" \
    "ds_read_b128 a[124:127], v127 offset:29696\n" \
    "v_lshl_add_u32 v44, v45, 1, v44\n" \
    "v_lshlrev_b32 v45, 2, v45\n" \
    "ds_read_b128 v[80:83], v127 offset:30720\n" \
    "s_lshr_b32 s79, %[u], 4\n" \
    "s_mul_i32 s79, s79, 32 * 4352\n" \
    "s_and_b32 s94, %[u], 15\n" \
    "s_lshl2_add_u32 s79, s94, s79\n" \
    "s_add_i32 s79, s79, 0x3e00fc0\n" \
    "s_lshl3_add_u32 s94, s94, 0x4065f80\n" \
    "s_lshr_b32 s80, s60, 1\n" \
    "s_mul_i32 s76, s80, 49152\n" \
    "s_add_i32 s76, s76, 98304\n" \
    "v_add_u32 v128, s76, v127\n" \
    "v_xor_b32 v130, 0x38000, v128\n" \
    "ds_read_b128 v[84:87], v127 offset:31744\n" \
    "s_lshl_b32 s76, s80, 12\n" \
    "s_add_i32 s76, s76, 83968\n" \
    "v_add_u32 v132, s76, v127\n" \
    "s_add_i32 s92, s64, 16515072\n" \
    "s_add_i32 s93, s92, 0x1000000\n" \
    "s_mov_b32 m0, s95\n" \
    "v_and_b32 v97, 0x380, v127\n" \
    "v_add_u32 v97, v97, v127\n" \
    "s_waitcnt vmcnt(6)\n" \
    "v_readfirstlane_b32 s80, v100\n" \
    "v_cmp_ne_u16 vcc, s91, v99\n" \
    "s_max_i32 s38, s38, s80\n" \
    "s_cmp_lt_i32 s38, s91\n" \
    "s_cbranch_scc1 21f\n" \
    "22:\n" \
    "s_cbranch_vccnz 23f\n" \
    "24:\n" \
    "buffer_load_ushort v113, v44, %[rw], s79 offen sc1\n" \
    "buffer_load_dword v114, v45, %[rw], s94 offen sc1\n" \
    "v_subrev_u32 v96, s29, v98\n" \
    "v_min_i32 v96, 0x4000, v96\n" \
    "v_max_i32 v96, 0, v96\n" \
    "s_waitcnt lgkmcnt(0)\n" \
    "s_barrier\n" \
    "v_max_i32_dpp v96, v96, v96 row_shr:1 row_mask:0xf bank_mask:0xf\n" \
    "ds_read_b64 v[52:53], v103 offset:9216\n" \
    "s_and_b32 s75, s91, 3\n" \
    "v_max_i32_dpp v96, v96, v96 row_shr:2 row_mask:0xf bank_mask:0xf\n" \
    "ds_read_b64 v[54:55], v103 offset:9728\n" \
    "s_lshl_b32 s75, s75, 16\n" \
    "v_max_i32_dpp v96, v96, v96 row_shr:4 row_mask:0xf bank_mask:0xf\n" \
    "ds_read_b64 v[100:101], v103 offset:10240\n" \
    "s_add_i32 s69, s75, s26\n" \
    "v_max_i32_dpp v96, v96, v96 row_shr:8 row_mask:0xf bank_mask:0xf\n" \
    "ds_read_b64 v[102:103], v103 offset:10752\n" \
    "s_xor_b32 s70, s69, 2048\n" \
    "v_max_i32_dpp v96, v96, v96 row_bcast:15 row_mask:0xa bank_mask:0xf\n" \
    "ds_read_b128 a[0:3], v128 offset:0\n" \
    "s_andn2_b32 s71, s69, 0x1fff\n" \
    "v_max_i32_dpp v96, v96, v96 row_bcast:31 row_mask:0xc bank_mask:0xf\n" \
    "ds_read_b128 a[16:19], v128 offset:2048\n" \
    "s_add_i32 s71, s71, 32768\n" \
    "v_readlane_b32 s90, v96, 63\n" \
    "s_add_i32 s90, s90, s42\n" \
    "s_mul_i32 s90, s90, s25\n" \
    "s_ashr_i32 s90, s90, 12\n" \
    "s_add_i32 s90, s90, s24\n" \
    "buffer_load_dwordx4 v97, %[rw], s92 offen sc1 lds\n" \
    "s_add_i32 m0, s95, 896\n" \
    "v_mov_b32 v99, 0\n" \
    "buffer_load_dwordx4 v97, %[rw], s92 offen offset:128 sc1 lds\n" \
    "s_add_i32 m0, s95, 8192\n" \
    "v_mov_b32 v96, 0\n" \
    "25:\n" \
    "v_mov_b64 v[48:49], 0\n" \
    "v_mov_b64 v[50:51], 0\n" \
    "s_waitcnt vmcnt(7)\n" \
    "v_mfma_f32_16x16x32_bf16 v[40:43], v[0:3], v[136:139], v[88:91]\n" \
    "ds_read_b128 a[4:7], v128 offset:1024\n" \
    "v_max3_u32 v98, v136, v137, v136\n" \
    "v_mfma_f32_16x16x32_bf16 v[44:47], v[4:7], v[136:139], v[92:95]\n" \
    "ds_read_b128 a[20:23], v128 offset:3072\n" \
    "v_max3_u32 v98, v138, v139, v98\n" \
    "s_waitcnt vmcnt(6)\n" \
    "v_mfma_f32_16x16x32_bf16 v[40:43], v[8:11], v[140:143], v[40:43]\n" \
    "ds_read_b128 a[8:11], v130 offset:0\n" \
    "v_max3_u32 v98, v140, v141, v98\n" \
    "v_mfma_f32_16x16x32_bf16 v[44:47], v[12:15], v[140:143], v[44:47]\n" \
    "ds_read_b128 a[24:27], v130 offset:2048\n" \
    "v_max3_u32 v98, v142, v143, v98\n" \
    "v_smfmac_f32_16x16x64_bf16 v[48:51], v[32:35], v[136:143], v124\n" \
    "ds_read_b128 a[12:15], v130 offset:1024\n" \
    "s_waitcnt vmcnt(5)\n" \
    "v_mfma_f32_16x16x32_bf16 v[40:43], v[16:19], v[144:147], v[40:43]\n" \
    "ds_read_b128 a[28:31], v130 offset:3072\n" \
    "v_max3_u32 v98, v144, v145, v98\n" \
    "v_mfma_f32_16x16x32_bf16 v[44:47], v[20:23], v[144:147], v[44:47]\n" \
    "ds_read_b128 a[32:35], v128 offset:4096\n" \
    "v_max3_u32 v98, v146, v147, v98\n" \
    "s_waitcnt vmcnt(4)\n" \
    "v_smfmac_f32_16x16x64_bf16 v[48:51], v[36:39], v[144:151], v124\n" \
    "ds_read_b128 a[48:51], v128 offset:6144\n" \
    "v_mfma_f32_16x16x32_bf16 v[40:43], v[24:27], v[148:151], v[40:43]\n" \
    "ds_read_b128 a[36:39], v128 offset:5120\n" \
    "v_max3_u32 v98, v148, v149, v98\n" \
    "v_max3_u32 v98, v150, v151, v98\n" \
    "v_mfma_f32_16x16x32_bf16 v[44:47], v[28:31], v[148:151], v[44:47]\n" \
    "v_cmp_eq_u32 vcc, -1, v98\n" \
    "s_add_i32 s44, %[qb], 262144\n" \
    "s_add_i32 s45, s44, 4096\n" \
    "s_waitcnt lgkmcnt(3)\n" \
    "v_add_f32 v48, v48, v50\n" \
    "v_add_f32 v49, v49, v51\n" \
    "s_nop 0\n" \
    "s_cbranch_vccnz 26f\n" \
    "27:\n" \
    "v_max3_f32 v0, v40, v41, v42\n" \
    "v_max3_f32 v1, v43, v44, v45\n" \
    "v_max3_f32 v0, v0, v46, v47\n" \
    "v_max3_f32 v1, v1, v48, v49\n" \
    "v_max_f32 v0, v0, v1\n" \
    "v_max_f32 v1, v0, v1\n" \
    "buffer_load_dwordx4 v112, %[rw], s93 offen sc1 lds\n" \
    "s_add_i32 m0, s95, 8960\n" \
    "s_nop 0\n" \
    "v_permlane16_swap_b32 v0, v1\n" \
    "v_max_f32 v0, v0, v1\n" \
    "v_max_f32 v1, v0, v1\n" \
    "buffer_load_dwordx4 v112, %[rw], s93 offen offset:256 sc1 lds\n" \
    "s_nop 0\n" \
    "v_permlane32_swap_b32 v0, v1\n" \
    "v_max_f32 v0, v0, v1\n" \
    "s_nop 0\n" \
    "v_sub_f32 v40, v40, v0\n" \
    "v_sub_f32 v41, v41, v0\n" \
    "ds_read_b128 v[136:139], v132 offset:0\n" \
    "v_exp_f32 v40, v40\n" \
    "v_exp_f32 v41, v41\n" \
    "v_sub_f32 v42, v42, v0\n" \
    "ds_read_b128 v[140:143], v132 offset:1024\n" \
    "v_sub_f32 v43, v43, v0\n" \
    "v_exp_f32 v42, v42\n" \
    "v_exp_f32 v43, v43\n" \
    "ds_read_b128 v[144:147], v132 offset:8192\n" \
    "v_cvt_pk_bf16_f32 v32, v40, v41\n" \
    "v_sub_f32 v44, v44, v0\n" \
    "v_sub_f32 v45, v45, v0\n" \
    "ds_read_b128 v[148:151], v132 offset:9216\n" \
    "v_exp_f32 v44, v44\n" \
    "v_exp_f32 v45, v45\n" \
    "v_cvt_pk_bf16_f32 v33, v42, v43\n" \
    "ds_read_b128 a[52:55], v128 offset:7168\n" \
    "v_sub_f32 v46, v46, v0\n" \
    "v_sub_f32 v47, v47, v0\n" \
    "v_exp_f32 v46, v46\n" \
    "s_memtime vcc\n" \
    "v_exp_f32 v47, v47\n" \
    "v_cvt_pk_bf16_f32 v34, v44, v45\n" \
    "v_sub_f32 v48, v48, v0\n" \
    "v_sub_f32 v49, v49, v0\n" \
    "v_exp_f32 v48, v48\n" \
    "v_exp_f32 v49, v49\n" \
    "v_cvt_pk_bf16_f32 v35, v46, v47\n" \
    "v_cvt_pk_bf16_f32 v98, v48, v49\n" \
    "v_cvt_pk_bf16_f32 v97, v48, v49\n" \
    "s_waitcnt lgkmcnt(0)\n" \
    "v_mfma_f32_16x16x32_bf16 v[36:39], v[120:123], v[32:35], 0\n" \
    "s_sub_i32 s75, s90, vcc_lo\n" \
    "s_cmp_gt_i32 s75, 100\n" \
    "v_mfma_f32_16x16x32_bf16 v[0:3], v[56:59], v[32:35], 0\n" \
    "s_cbranch_scc1 30f\n" \
    "31:\n" \
    "ds_read_b128 a[40:43], v130 offset:4096\n" \
    "v_and_b32 v44, 0x200, v127\n" \
    "s_and_b32 s80, s26, 0x800\n" \
    "v_mad_u32_u24 v44, v44, 3, v127\n" \
    "v_mfma_f32_16x16x32_bf16 v[4:7], v[60:63], v[32:35], 0\n" \
    "s_mov_b32 m0, s74\n" \
    "s_add_i32 s76, s74, 4096\n" \
    "buffer_load_dwordx4 v125, %[rs], s69 offen sc1 lds\n" \
    "s_mov_b32 m0, s76\n" \
    "v_mfma_f32_16x16x32_bf16 v[8:11], v[64:67], v[32:35], 0\n" \
    "ds_read_b128 a[56:59], v130 offset:6144\n" \
    "v_xor_b32 v44, s80, v44\n" \
    "v_bfe_u32 v40, v127, 6, 2\n" \
    "v_mfma_f32_16x16x32_bf16 v[12:15], v[68:71], v[32:35], 0\n" \
    "buffer_load_dwordx4 v125, %[rs], s70 offen sc1 lds\n" \
    "s_movk_i32 m0, 0x2000\n" \
    "v_mfma_f32_16x16x32_bf16 v[16:19], a[120:123], v[32:35], 0\n" \
    "ds_read_b128 a[44:47], v130 offset:5120\n" \
    "v_bfe_u32 v41, v127, 5, 1\n" \
    "v_bfe_u32 v42, v127, 4, 1\n" \
    "v_mfma_f32_16x16x32_bf16 v[20:23], a[124:127], v[32:35], 0\n" \
    "s_mov_b32 exec_lo, s27\n" \
    "s_mov_b32 exec_hi, s27\n" \
    "buffer_load_dwordx4 v44, %[rs], s71 offen sc1 lds\n" \
    "s_mov_b64 exec, -1\n" \
    "v_mfma_f32_16x16x32_bf16 v[24:27], v[80:83], v[32:35], 0\n" \
    "ds_read_b128 a[60:63], v130 offset:7168\n" \
    "v_lshrrev_b32 v43, 8, v127\n" \
    "v_lshlrev_b32 v134, 14, v40\n" \
    "v_mfma_f32_16x16x32_bf16 v[28:31], v[84:87], v[32:35], 0\n" \
    "ds_read_b128 a[64:67], v128 offset:8192\n" \
    "v_lshl_add_u32 v134, v41, 12, v134\n" \
    "v_lshl_add_u32 v134, v42, 6, v134\n" \
    "v_mfma_f32_16x16x16_bf16 v[36:39], v[120:121], v[98:99], v[36:39]\n" \
    "ds_read_b128 a[80:83], v128 offset:10240\n" \
    "v_lshl_add_u32 v134, v43, 4, v134\n" \
    "v_and_b32 v41, 2, v40\n" \
    "v_mfma_f32_16x16x16_bf16 v[0:3], v[52:53], v[98:99], v[0:3]\n" \
    "buffer_load_dwordx4 v[72:75], v112, %[rw], s44 offen offset:0 sc1\n" \
    "v_mfma_f32_16x16x16_bf16 v[4:7], v[52:53], v[96:97], v[4:7]\n" \
    "ds_read_b128 a[68:71], v128 offset:9216\n" \
    "v_add_lshl_u32 v135, v40, v41, 12\n" \
    "v_bfe_u32 v42, v127, 4, 2\n" \
    "v_mfma_f32_16x16x16_bf16 v[8:11], v[54:55], v[98:99], v[8:11]\n" \
    "buffer_load_dwordx4 v[76:79], v112, %[rw], s44 offen offset:2048 sc1\n" \
    "v_mfma_f32_16x16x16_bf16 v[12:15], v[54:55], v[96:97], v[12:15]\n" \
    "ds_read_b128 a[84:87], v128 offset:11264\n" \
    "v_lshl_add_u32 v135, v42, 5, v135\n" \
    "v_lshl_add_u32 v135, v43, 3, v135\n" \
    "v_mfma_f32_16x16x16_bf16 v[16:19], v[100:101], v[98:99], v[16:19]\n" \
    "buffer_load_dwordx4 v[104:107], v112, %[rw], s45 offen offset:0 sc1\n" \
    "v_mfma_f32_16x16x16_bf16 v[20:23], v[100:101], v[96:97], v[20:23]\n" \
    "ds_read_b128 a[72:75], v130 offset:8192\n" \
    "v_mfma_f32_16x16x16_bf16 v[24:27], v[102:103], v[98:99], v[24:27]\n" \
    "buffer_load_dwordx4 v[108:111], v112, %[rw], s45 offen offset:2048 sc1\n" \
    "v_mfma_f32_16x16x16_bf16 v[28:31], v[102:103], v[96:97], v[28:31]\n" \
    "ds_read_b128 a[88:91], v130 offset:10240\n" \
    "v_cvt_pk_bf16_f32 v40, v0, v4\n" \
    "v_cvt_pk_bf16_f32 v41, v1, v5\n" \
    "v_cvt_pk_bf16_f32 v42, v2, v6\n" \
    "v_cvt_pk_bf16_f32 v43, v3, v7\n" \
    "v_cvt_pk_bf16_f32 v44, v8, v12\n" \
    "v_cvt_pk_bf16_f32 v45, v9, v13\n" \
    "v_mfma_f32_16x16x32_bf16 v[64:67], v[136:139], v[40:43], 0\n" \
    "s_mov_b32 s46, %[tp]\n" \
    "ds_read_b128 a[76:79], v130 offset:9216\n" \
    "v_cvt_pk_bf16_f32 v46, v10, v14\n" \
    "v_cvt_pk_bf16_f32 v47, v11, v15\n" \
    "v_mfma_f32_16x16x32_bf16 v[56:59], a[0:3], v[40:43], 0\n" \
    "s_mov_b32 s47, %[tx]\n" \
    "ds_read_b128 a[92:95], v130 offset:11264\n" \
    "v_cvt_pk_bf16_f32 v48, v16, v20\n" \
    "v_cvt_pk_bf16_f32 v49, v17, v21\n" \
    "v_mfma_f32_16x16x32_bf16 v[64:67], v[140:143], v[44:47], v[64:67]\n" \
    "ds_read_b128 a[96:99], v128 offset:12288\n" \
    "v_cvt_pk_bf16_f32 v50, v18, v22\n" \
    "v_cvt_pk_bf16_f32 v51, v19, v23\n" \
    "v_mfma_f32_16x16x32_bf16 v[60:63], a[16:19], v[40:43], 0\n" \
    "s_waitcnt lgkmcnt(5)\n" \
    "ds_read_b128 a[112:115], v128 offset:14336\n" \
    "v_cvt_pk_bf16_f32 v52, v24, v28\n" \
    "v_cvt_pk_bf16_f32 v53, v25, v29\n" \
    "v_mfma_f32_16x16x32_bf16 v[64:67], v[144:147], v[48:51], v[64:67]\n" \
    "ds_read_b128 a[100:103], v128 offset:13312\n" \
    "v_cvt_pk_bf16_f32 v54, v26, v30\n" \
    "v_cvt_pk_bf16_f32 v55, v27, v31\n" \
    "v_mfma_f32_16x16x32_bf16 v[56:59], a[4:7], v[44:47], v[56:59]\n" \
    "ds_read_b128 a[116:119], v128 offset:15360\n" \
    "v_rcp_f32 v36, v36\n" \
    "v_mfma_f32_16x16x32_bf16 v[64:67], v[148:151], v[52:55], v[64:67]\n" \
    "ds_read_b128 a[104:107], v130 offset:12288\n" \
    "v_add_u32 v37, s74, v127\n" \
    "v_xor_b32 v38, 0x400, v37\n" \
    "v_mfma_f32_16x16x32_bf16 v[60:63], a[20:23], v[44:47], v[60:63]\n" \
    "ds_read_b128 a[120:123], v130 offset:14336\n" \
    "v_xor_b32 v39, 0x800, v37\n" \
    "v_xor_b32 v132, 0xc00, v37\n" \
    "v_mfma_f32_16x16x32_bf16 v[56:59], a[8:11], v[48:51], v[56:59]\n" \
    "ds_read_b128 a[108:111], v130 offset:13312\n" \
    "v_mul_f32 v64, v64, v36\n" \
    "v_mul_f32 v65, v65, v36\n" \
    "v_mfma_f32_16x16x32_bf16 v[60:63], a[24:27], v[48:51], v[60:63]\n" \
    "ds_read_b128 a[124:127], v130 offset:15360\n" \
    "v_mul_f32 v66, v66, v36\n" \
    "v_mul_f32 v67, v67, v36\n" \
    "v_mfma_f32_16x16x32_bf16 v[56:59], a[12:15], v[52:55], v[56:59]\n" \
    "v_cvt_pk_bf16_f32 v100, v64, v65\n" \
    "v_cvt_pk_bf16_f32 v101, v66, v67\n" \
    "v_mfma_f32_16x16x32_bf16 v[60:63], a[28:31], v[52:55], v[60:63]\n" \
    "buffer_store_dwordx2 v[100:101], v135, %[rw], s47 offen sc1\n" \
    "v_mfma_f32_16x16x32_bf16 v[64:67], a[32:35], v[40:43], 0\n" \
    "v_mul_f32 v56, v56, v36\n" \
    "v_mul_f32 v57, v57, v36\n" \
    "v_mfma_f32_16x16x32_bf16 v[68:71], a[48:51], v[40:43], 0\n" \
    "v_mul_f32 v58, v58, v36\n" \
    "v_mul_f32 v59, v59, v36\n" \
    "v_mfma_f32_16x16x32_bf16 v[64:67], a[36:39], v[44:47], v[64:67]\n" \
    "v_mul_f32 v60, v60, v36\n" \
    "v_mul_f32 v61, v61, v36\n" \
    "v_mfma_f32_16x16x32_bf16 v[68:71], a[52:55], v[44:47], v[68:71]\n" \
    "v_mul_f32 v62, v62, v36\n" \
    "v_mul_f32 v63, v63, v36\n" \
    "v_mfma_f32_16x16x32_bf16 v[64:67], a[40:43], v[48:51], v[64:67]\n" \
    "v_cvt_pk_bf16_f32 v96, v56, v57\n" \
    "v_cvt_pk_bf16_f32 v97, v58, v59\n" \
    "v_mfma_f32_16x16x32_bf16 v[68:71], a[56:59], v[48:51], v[68:71]\n" \
    "v_cvt_pk_bf16_f32 v98, v60, v61\n" \
    "v_cvt_pk_bf16_f32 v99, v62, v63\n" \
    "v_mfma_f32_16x16x32_bf16 v[64:67], a[44:47], v[52:55], v[64:67]\n" \
    "buffer_store_dwordx4 v[96:99], v134, %[rw], s46 offen sc1\n" \
    "s_add_i32 s46, s46, 0x20000\n" \
    "v_mfma_f32_16x16x32_bf16 v[68:71], a[60:63], v[52:55], v[68:71]\n" \
    "v_mov_b64 v[80:81], 0\n" \
    "v_mov_b64 v[82:83], 0\n" \
    "v_mfma_f32_16x16x32_bf16 v[56:59], a[64:67], v[40:43], 0\n" \
    "v_mul_f32 v64, v64, v36\n" \
    "v_mul_f32 v65, v65, v36\n" \
    "v_mfma_f32_16x16x32_bf16 v[60:63], a[80:83], v[40:43], 0\n" \
    "v_mul_f32 v66, v66, v36\n" \
    "v_mul_f32 v67, v67, v36\n" \
    "v_mfma_f32_16x16x32_bf16 v[56:59], a[68:71], v[44:47], v[56:59]\n" \
    "v_mul_f32 v68, v68, v36\n" \
    "v_mul_f32 v69, v69, v36\n" \
    "v_mfma_f32_16x16x32_bf16 v[60:63], a[84:87], v[44:47], v[60:63]\n" \
    "v_mul_f32 v70, v70, v36\n" \
    "v_mul_f32 v71, v71, v36\n" \
    "s_waitcnt lgkmcnt(11)\n" \
    "v_mfma_f32_16x16x32_bf16 v[56:59], a[72:75], v[48:51], v[56:59]\n" \
    "v_cvt_pk_bf16_f32 v100, v64, v65\n" \
    "v_cvt_pk_bf16_f32 v101, v66, v67\n" \
    "s_waitcnt lgkmcnt(10)\n" \
    "v_mfma_f32_16x16x32_bf16 v[60:63], a[88:91], v[48:51], v[60:63]\n" \
    "s_waitcnt vmcnt(6) lgkmcnt(0)\n" \
    "s_barrier\n" \
    "v_cvt_pk_bf16_f32 v102, v68, v69\n" \
    "v_cvt_pk_bf16_f32 v103, v70, v71\n" \
    "v_mfma_f32_16x16x32_bf16 v[56:59], a[76:79], v[52:55], v[56:59]\n" \
    "ds_read_b128 v[0:3], v37 offset:0\n" \
    "buffer_store_dwordx4 v[100:103], v134, %[rw], s46 offen sc1\n" \
    "s_add_i32 s46, s46, 0x20000\n" \
    "v_mfma_f32_16x16x32_bf16 v[60:63], a[92:95], v[52:55], v[60:63]\n" \
    "ds_read_b128 v[4:7], v38 offset:0\n" \
    "v_mov_b64 v[84:85], 0\n" \
    "v_mov_b64 v[86:87], 0\n" \
    "v_mfma_f32_16x16x32_bf16 v[64:67], a[96:99], v[40:43], 0\n" \
    "ds_read_b128 v[8:11], v39 offset:0\n" \
    "v_mul_f32 v56, v56, v36\n" \
    "v_mul_f32 v57, v57, v36\n" \
    "v_mfma_f32_16x16x32_bf16 v[68:71], a[112:115], v[40:43], 0\n" \
    "ds_read_b128 v[12:15], v132 offset:0\n" \
    "v_mul_f32 v58, v58, v36\n" \
    "v_mul_f32 v59, v59, v36\n" \
    "v_mfma_f32_16x16x32_bf16 v[64:67], a[100:103], v[44:47], v[64:67]\n" \
    "ds_read_b128 v[16:19], v37 offset:4096\n" \
    "v_mul_f32 v60, v60, v36\n" \
    "v_mul_f32 v61, v61, v36\n" \
    "v_mfma_f32_16x16x32_bf16 v[68:71], a[116:119], v[44:47], v[68:71]\n" \
    "ds_read_b128 v[20:23], v38 offset:4096\n" \
    "v_mul_f32 v62, v62, v36\n" \
    "v_mul_f32 v63, v63, v36\n" \
    "v_mfma_f32_16x16x32_bf16 v[64:67], a[104:107], v[48:51], v[64:67]\n" \
    "ds_read_b128 v[24:27], v39 offset:4096\n" \
    "v_cvt_pk_bf16_f32 v96, v56, v57\n" \
    "v_cvt_pk_bf16_f32 v97, v58, v59\n" \
    "v_mfma_f32_16x16x32_bf16 v[68:71], a[120:123], v[48:51], v[68:71]\n" \
    "ds_read_b128 v[28:31], v132 offset:4096\n" \
    "v_cvt_pk_bf16_f32 v98, v60, v61\n" \
    "v_cvt_pk_bf16_f32 v99, v62, v63\n" \
    "v_mfma_f32_16x16x32_bf16 v[64:67], a[108:111], v[52:55], v[64:67]\n" \
    "ds_read_b128 v[32:35], v126 offset:0\n" \
    "buffer_store_dwordx4 v[96:99], v134, %[rw], s46 offen sc1\n" \
    "s_add_i32 s46, s46, 0x20000\n" \
    "v_mfma_f32_16x16x32_bf16 v[68:71], a[124:127], v[52:55], v[68:71]\n" \
    "s_waitcnt lgkmcnt(3)\n" \
    "33:\n" \
    "v_smfmac_f32_16x16x64_bf16 v[80:83], v[0:3], a[128:135], v124\n" \
    "v_mul_f32 v64, v64, v36\n" \
    "v_smfmac_f32_16x16x64_bf16 v[80:83], v[4:7], a[144:151], v124\n" \
    "v_mul_f32 v65, v65, v36\n" \
    "v_smfmac_f32_16x16x64_bf16 v[80:83], v[8:11], a[160:167], v124\n" \
    "s_waitcnt lgkmcnt(0)\n" \
    "v_mul_f32 v66, v66, v36\n" \
    "v_smfmac_f32_16x16x64_bf16 v[80:83], v[12:15], a[176:183], v124\n" \
    "ds_read_b128 v[40:43], v127 offset:16384\n" \
    "v_mul_f32 v67, v67, v36\n" \
    "v_smfmac_f32_16x16x64_bf16 v[80:83], v[16:19], a[192:199], v124\n" \
    "ds_read_b128 v[44:47], v127 offset:17408\n" \
    "v_mul_f32 v68, v68, v36\n" \
    "v_smfmac_f32_16x16x64_bf16 v[80:83], v[20:23], a[208:215], v124\n" \
    "ds_read_b128 v[48:51], v127 offset:18432\n" \
    "v_mul_f32 v69, v69, v36\n" \
    "v_smfmac_f32_16x16x64_bf16 v[80:83], v[24:27], a[224:231], v124\n" \
    "ds_read_b128 v[52:55], v127 offset:19456\n" \
    "v_mul_f32 v70, v70, v36\n" \
    "v_mul_f32 v71, v71, v36\n" \
    "v_smfmac_f32_16x16x64_bf16 v[80:83], v[28:31], a[240:247], v124\n" \
    "ds_read_b128 v[56:59], v127 offset:20480\n" \
    "v_cvt_pk_bf16_f32 v100, v64, v65\n" \
    "v_cvt_pk_bf16_f32 v101, v66, v67\n" \
    "v_smfmac_f32_16x16x64_bf16 v[80:83], v[32:35], v[152:159], v124\n" \
    "ds_read_b128 v[60:63], v127 offset:21504\n" \
    "v_cvt_pk_bf16_f32 v102, v68, v69\n" \
    "v_cvt_pk_bf16_f32 v103, v70, v71\n" \
    "v_smfmac_f32_16x16x64_bf16 v[84:87], v[0:3], a[136:143], v124\n" \
    "ds_read_b128 v[64:67], v127 offset:22528\n" \
    "buffer_store_dwordx4 v[100:103], v134, %[rw], s46 offen sc1\n" \
    "v_smfmac_f32_16x16x64_bf16 v[84:87], v[4:7], a[152:159], v124\n" \
    "ds_read_b128 v[68:71], v127 offset:23552\n" \
    "v_add_f32 v80, v80, v82\n" \
    "v_add_f32 v81, v81, v83\n" \
    "v_smfmac_f32_16x16x64_bf16 v[84:87], v[8:11], a[168:175], v124\n" \
    "s_waitcnt vmcnt(5)\n" \
    "v_cmp_u_f32 vcc, v80, v81\n" \
    "v_max3_u32 v128, v72, v73, v72\n" \
    "v_smfmac_f32_16x16x64_bf16 v[84:87], v[12:15], a[184:191], v124\n" \
    "v_max3_u32 v128, v74, v75, v128\n" \
    "v_max3_u32 v128, v76, v77, v128\n" \
    "v_smfmac_f32_16x16x64_bf16 v[84:87], v[16:19], a[200:207], v124\n" \
    "s_cbranch_vccnz 32f\n" \
    "34:\n" \
    "v_max3_u32 v128, v78, v79, v128\n" \
    "v_max3_u32 v128, v104, v105, v128\n" \
    "v_smfmac_f32_16x16x64_bf16 v[84:87], v[20:23], a[216:223], v124\n" \
    "v_max3_u32 v128, v106, v107, v128\n" \
    "v_max3_u32 v128, v108, v109, v128\n" \
    "v_smfmac_f32_16x16x64_bf16 v[84:87], v[24:27], a[232:239], v124\n" \
    "v_max3_u32 v128, v110, v111, v128\n" \
    "v_cmp_eq_u32 vcc, -1, v128\n" \
    "v_smfmac_f32_16x16x64_bf16 v[84:87], v[28:31], a[248:255], v124\n" \
    "s_waitcnt lgkmcnt(0)\n" \
    "v_smfmac_f32_16x16x64_bf16 v[84:87], v[32:35], v[160:167], v124\n" \
    "s_cbranch_vccnz 28f\n" \
    "29:\n" \
    "v_mfma_f32_16x16x32_bf16 v[152:155], v[40:43], v[72:75], v[88:91]\n" \
    "ds_read_b128 a[128:131], v133 offset:0\n" \
    "v_mov_b64 v[96:97], 0\n" \
    "v_mov_b64 v[98:99], 0\n" \
    "v_mfma_f32_16x16x32_bf16 v[156:159], v[44:47], v[72:75], v[92:95]\n" \
    "ds_read_b128 a[132:135], v133 offset:1024\n" \
    "v_add_f32 v84, v84, v86\n" \
    "v_add_f32 v85, v85, v87\n" \
    "v_mfma_f32_16x16x32_bf16 v[152:155], v[48:51], v[76:79], v[152:155]\n" \
    "ds_read_b128 a[136:139], v133 offset:2048\n" \
    "v_cvt_pk_bf16_f32 v80, v80, v84\n" \
    "v_cvt_pk_bf16_f32 v81, v81, v85\n" \
    "v_mfma_f32_16x16x32_bf16 v[156:159], v[52:55], v[76:79], v[156:159]\n" \
    "ds_write2_b32 v129, v80, v81 offset0:0 offset1:144\n" \
    "v_mfma_f32_16x16x32_bf16 v[152:155], v[56:59], v[104:107], v[152:155]\n" \
    "ds_read_b128 a[140:143], v133 offset:3072\n" \
    "v_mov_b64 v[160:161], 0\n" \
    "v_mov_b64 v[162:163], 0\n" \
    "v_mfma_f32_16x16x32_bf16 v[156:159], v[60:63], v[104:107], v[156:159]\n" \
    "ds_read_b128 a[144:147], v133 offset:16384\n" \
    "v_mov_b64 v[100:101], 0\n" \
    "v_mov_b64 v[102:103], 0\n" \
    "v_mfma_f32_16x16x32_bf16 v[152:155], v[64:67], v[108:111], v[152:155]\n" \
    "ds_read_b128 a[148:151], v133 offset:17408\n" \
    "v_lshrrev_b32 v42, 1, v37\n" \
    "v_lshrrev_b32 v43, 1, v127\n" \
    "v_mfma_f32_16x16x32_bf16 v[156:159], v[68:71], v[108:111], v[156:159]\n" \
    "s_waitcnt lgkmcnt(3)\n" \
    "s_barrier\n" \
    "ds_read_b128 v[80:83], v131 offset:0\n" \
    "ds_read_b128 v[84:87], v131 offset:128\n" \
    "v_smfmac_f32_16x16x64_bf16 v[96:99], v[16:19], v[168:175], v124\n" \
    "ds_read_b128 a[152:155], v133 offset:18432\n" \
    "v_mov_b32 v61, 0\n" \
    "v_smfmac_f32_16x16x64_bf16 v[96:99], v[20:23], v[184:191], v124\n" \
    "ds_read_b128 a[156:159], v133 offset:19456\n" \
    "v_max3_f32 v40, v152, v153, v154\n" \
    "v_smfmac_f32_16x16x64_bf16 v[96:99], v[24:27], v[200:207], v124\n" \
    "ds_read_b128 a[160:163], v133 offset:32768\n" \
    "s_waitcnt lgkmcnt(3)\n" \
    "v_max3_f32 v41, v155, v156, v157\n" \
    "v_smfmac_f32_16x16x64_bf16 v[160:163], v[80:83], v[72:79], v124\n" \
    "ds_read_b128 a[164:167], v133 offset:33792\n" \
    "v_max3_f32 v40, v40, v158, v159\n" \
    "v_smfmac_f32_16x16x64_bf16 v[160:163], v[84:87], v[104:111], v124\n" \
    "ds_read_b128 a[168:171], v133 offset:34816\n" \
    "v_mov_b32 v62, 0\n" \
    "v_smfmac_f32_16x16x64_bf16 v[96:99], v[28:31], v[216:223], v124\n" \
    "ds_read_b128 a[172:175], v133 offset:35840\n" \
    "v_smfmac_f32_16x16x64_bf16 v[96:99], v[32:35], v[232:239], v124\n" \
    "ds_read_b128 a[176:179], v133 offset:49152\n" \
    "v_add_f32 v160, v160, v162\n" \
    "v_add_f32 v161, v161, v163\n" \
    "v_max3_f32 v41, v41, v160, v161\n" \
    "v_smfmac_f32_16x16x64_bf16 v[100:103], v[16:19], v[176:183], v124\n" \
    "ds_read_b128 a[180:183], v133 offset:50176\n" \
    "v_max_f32 v40, v40, v41\n" \
    "v_max_f32 v41, v40, v41\n" \
    "v_smfmac_f32_16x16x64_bf16 v[100:103], v[20:23], v[192:199], v124\n" \
    "ds_read_b128 a[192:195], v127 offset:24576\n" \
    "v_permlane16_swap_b32 v40, v41\n" \
    "v_max_f32 v40, v40, v41\n" \
    "v_max_f32 v41, v40, v41\n" \
    "v_smfmac_f32_16x16x64_bf16 v[96:99], v[0:3], a[128:135], v124\n" \
    "ds_read_b128 a[196:199], v127 offset:25600\n" \
    "v_permlane32_swap_b32 v40, v41\n" \
    "v_max_f32 v40, v40, v41\n" \
    "s_nop 0\n" \
    "v_smfmac_f32_16x16x64_bf16 v[96:99], v[4:7], a[144:151], v124\n" \
    "ds_read_b128 a[200:203], v127 offset:26624\n" \
    "s_waitcnt lgkmcnt(3)\n" \
    "v_sub_f32 v152, v152, v40\n" \
    "v_sub_f32 v153, v153, v40\n" \
    "v_exp_f32 v152, v152\n" \
    "v_smfmac_f32_16x16x64_bf16 v[96:99], v[8:11], a[160:167], v124\n" \
    "ds_read_b128 a[204:207], v127 offset:27648\n" \
    "v_exp_f32 v153, v153\n" \
    "v_sub_f32 v154, v154, v40\n" \
    "v_sub_f32 v155, v155, v40\n" \
    "v_smfmac_f32_16x16x64_bf16 v[96:99], v[12:15], a[176:183], v124\n" \
    "ds_read_b128 a[208:211], v127 offset:28672\n" \
    "v_exp_f32 v154, v154\n" \
    "v_exp_f32 v155, v155\n" \
    "v_cvt_pk_bf16_f32 v56, v152, v153\n" \
    "v_smfmac_f32_16x16x64_bf16 v[100:103], v[24:27], v[208:215], v124\n" \
    "ds_read_b128 a[212:215], v127 offset:29696\n" \
    "v_sub_f32 v156, v156, v40\n" \
    "v_sub_f32 v157, v157, v40\n" \
    "v_exp_f32 v156, v156\n" \
    "v_smfmac_f32_16x16x64_bf16 v[100:103], v[28:31], v[224:231], v124\n" \
    "ds_read_b128 a[216:219], v127 offset:30720\n" \
    "v_exp_f32 v157, v157\n" \
    "v_cvt_pk_bf16_f32 v57, v154, v155\n" \
    "v_sub_f32 v158, v158, v40\n" \
    "v_smfmac_f32_16x16x64_bf16 v[100:103], v[32:35], v[240:247], v124\n" \
    "ds_read_b128 a[220:223], v127 offset:31744\n" \
    "v_sub_f32 v159, v159, v40\n" \
    "v_exp_f32 v158, v158\n" \
    "v_exp_f32 v159, v159\n" \
    "v_smfmac_f32_16x16x64_bf16 v[100:103], v[12:15], v[248:255], v124\n" \
    "v_cvt_pk_bf16_f32 v58, v156, v157\n" \
    "v_sub_f32 v160, v160, v40\n" \
    "v_sub_f32 v161, v161, v40\n" \
    "v_smfmac_f32_16x16x64_bf16 v[100:103], v[0:3], a[136:143], v124\n" \
    "v_exp_f32 v160, v160\n" \
    "v_exp_f32 v161, v161\n" \
    "v_cvt_pk_bf16_f32 v59, v158, v159\n" \
    "v_smfmac_f32_16x16x64_bf16 v[100:103], v[4:7], a[152:159], v124\n" \
    "v_cvt_pk_bf16_f32 v60, v160, v161\n" \
    "v_cvt_pk_bf16_f32 v63, v160, v161\n" \
    "v_smfmac_f32_16x16x64_bf16 v[100:103], v[8:11], a[168:175], v124\n" \
    "s_waitcnt lgkmcnt(0)\n" \
    "v_mfma_f32_16x16x32_bf16 v[36:39], v[120:123], v[56:59], 0\n" \
    "v_add_f32 v96, v96, v98\n" \
    "v_add_f32 v97, v97, v99\n" \
    "v_mfma_f32_16x16x32_bf16 v[0:3], a[192:195], v[56:59], 0\n" \
    "v_add_f32 v100, v100, v102\n" \
    "v_add_f32 v101, v101, v103\n" \
    "v_mfma_f32_16x16x32_bf16 v[4:7], a[196:199], v[56:59], 0\n" \
    "v_cvt_pk_bf16_f32 v96, v96, v97\n" \
    "v_cvt_pk_bf16_f32 v97, v100, v101\n" \
    "v_mfma_f32_16x16x32_bf16 v[8:11], a[200:203], v[56:59], 0\n" \
    "ds_write_b64 v42, v[96:97] offset:9216\n" \
    "v_mfma_f32_16x16x32_bf16 v[12:15], a[204:207], v[56:59], 0\n" \
    "v_lshrrev_b32 v169, 8, v127\n" \
    "v_and_b32 v170, 2, v169\n" \
    "v_mfma_f32_16x16x32_bf16 v[16:19], a[208:211], v[56:59], 0\n" \
    "v_mad_u32_u24 v169, v170, 3, v169\n" \
    "v_bfe_u32 v168, v127, 6, 1\n" \
    "v_mfma_f32_16x16x32_bf16 v[20:23], a[212:215], v[56:59], 0\n" \
    "v_bfe_u32 v170, v127, 4, 2\n" \
    "v_lshl_add_u32 v169, v170, 1, v169\n" \
    "v_mfma_f32_16x16x32_bf16 v[24:27], a[216:219], v[56:59], 0\n" \
    "s_waitcnt lgkmcnt(0)\n" \
    "s_barrier\n" \
    "ds_read_b64 v[64:65], v43 offset:9216\n" \
    "ds_read_b64 v[66:67], v43 offset:9728\n" \
    "ds_read_b64 v[68:69], v43 offset:10240\n" \
    "ds_read_b64 v[70:71], v43 offset:10752\n" \
    "v_mfma_f32_16x16x32_bf16 v[28:31], a[220:223], v[56:59], 0\n" \
    "v_mul_u32_u24 v168, 1152, v168\n" \
    "v_lshl_add_u32 v168, v169, 2, v168\n" \
    "v_mfma_f32_16x16x16_bf16 v[36:39], v[120:121], v[60:61], v[36:39]\n" \
    "s_waitcnt lgkmcnt(2)\n" \
    "v_mfma_f32_16x16x16_bf16 v[0:3], v[64:65], v[60:61], v[0:3]\n" \
    "v_sub_f32 v169, 1.0, v114\n" \
    "v_lshlrev_b32 v172, 16, v113\n" \
    "v_mfma_f32_16x16x16_bf16 v[4:7], v[64:65], v[62:63], v[4:7]\n" \
    "s_waitcnt lgkmcnt(0)\n" \
    "v_mul_f32 v172, v172, v169\n" \
    "v_and_b32 v170, 16, v127\n" \
    "v_mfma_f32_16x16x16_bf16 v[8:11], v[66:67], v[60:61], v[8:11]\n" \
    "v_mov_b32 v169, 0x3f80\n" \
    "v_lshlrev_b32 v170, v170, v169\n" \
    "v_mfma_f32_16x16x16_bf16 v[12:15], v[66:67], v[62:63], v[12:15]\n" \
    "v_mov_b32_dpp v173, v172 row_ror:8 row_mask:0xf bank_mask:0xf\n" \
    "v_mov_b64 v[176:177], 0\n" \
    "v_mfma_f32_16x16x16_bf16 v[16:19], v[68:69], v[60:61], v[16:19]\n" \
    "v_mov_b64 v[178:179], 0\n" \
    "v_mov_b32_dpp v176, v170 quad_perm:[0,1,2,3] row_mask:0xf bank_mask:0x1\n" \
    "v_mfma_f32_16x16x16_bf16 v[20:23], v[68:69], v[62:63], v[20:23]\n" \
    "v_mov_b32_dpp v177, v170 quad_perm:[0,1,2,3] row_mask:0xf bank_mask:0x2\n" \
    "v_mov_b32_dpp v178, v170 quad_perm:[0,1,2,3] row_mask:0xf bank_mask:0x4\n" \
    "v_mfma_f32_16x16x16_bf16 v[24:27], v[70:71], v[60:61], v[24:27]\n" \
    "v_mov_b32_dpp v179, v170 quad_perm:[0,1,2,3] row_mask:0xf bank_mask:0x8\n" \
    "v_mfma_f32_16x16x16_bf16 v[28:31], v[70:71], v[62:63], v[28:31]\n" \
    "v_cvt_pk_bf16_f32 v40, v0, v4\n" \
    "v_cvt_pk_bf16_f32 v41, v1, v5\n" \
    "v_cvt_pk_bf16_f32 v42, v2, v6\n" \
    "v_cvt_pk_bf16_f32 v43, v3, v7\n" \
    "v_cvt_pk_bf16_f32 v44, v8, v12\n" \
    "v_cvt_pk_bf16_f32 v45, v9, v13\n" \
    "v_mfma_f32_16x16x32_bf16 v[80:83], v[136:139], v[40:43], 0\n" \
    "s_mov_b32 s46, %[tq]\n" \
    "v_cvt_pk_bf16_f32 v46, v10, v14\n" \
    "v_cvt_pk_bf16_f32 v47, v11, v15\n" \
    "v_mfma_f32_16x16x32_bf16 v[72:75], a[0:3], v[40:43], 0\n" \
    "s_add_i32 s47, %[tx], 0x40000\n" \
    "v_cvt_pk_bf16_f32 v48, v16, v20\n" \
    "v_cvt_pk_bf16_f32 v49, v17, v21\n" \
    "v_mfma_f32_16x16x32_bf16 v[80:83], v[140:143], v[44:47], v[80:83]\n" \
    "v_cvt_pk_bf16_f32 v50, v18, v22\n" \
    "v_cvt_pk_bf16_f32 v51, v19, v23\n" \
    "v_mfma_f32_16x16x32_bf16 v[76:79], a[16:19], v[40:43], 0\n" \
    "v_cvt_pk_bf16_f32 v52, v24, v28\n" \
    "v_cvt_pk_bf16_f32 v53, v25, v29\n" \
    "v_mfma_f32_16x16x32_bf16 v[80:83], v[144:147], v[48:51], v[80:83]\n" \
    "v_cvt_pk_bf16_f32 v54, v26, v30\n" \
    "v_cvt_pk_bf16_f32 v55, v27, v31\n" \
    "v_mfma_f32_16x16x32_bf16 v[72:75], a[4:7], v[44:47], v[72:75]\n" \
    "v_rcp_f32 v36, v36\n" \
    "v_mfma_f32_16x16x32_bf16 v[80:83], v[148:151], v[52:55], v[80:83]\n" \
    "s_and_b32 s94, %[u], 15\n" \
    "s_mul_i32 s94, s94, 2304\n" \
    "v_mfma_f32_16x16x32_bf16 v[76:79], a[20:23], v[44:47], v[76:79]\n" \
    "s_andn2_b32 s77, %[u], 15\n" \
    "s_lshl2_add_u32 s94, s77, s94\n" \
    "v_mfma_f32_16x16x32_bf16 v[72:75], a[8:11], v[48:51], v[72:75]\n" \
    "v_mul_f32 v80, v80, v36\n" \
    "v_mul_f32 v81, v81, v36\n" \
    "v_mfma_f32_16x16x32_bf16 v[76:79], a[24:27], v[48:51], v[76:79]\n" \
    "v_mul_f32 v82, v82, v36\n" \
    "v_mul_f32 v83, v83, v36\n" \
    "v_mfma_f32_16x16x32_bf16 v[72:75], a[12:15], v[52:55], v[72:75]\n" \
    "v_cvt_pk_bf16_f32 v100, v80, v81\n" \
    "v_cvt_pk_bf16_f32 v101, v82, v83\n" \
    "v_mfma_f32_16x16x32_bf16 v[76:79], a[28:31], v[52:55], v[76:79]\n" \
    "buffer_store_dwordx2 v[100:101], v135, %[rw], s47 offen sc1\n" \
    "v_mfma_f32_16x16x32_bf16 v[80:83], a[32:35], v[40:43], 0\n" \
    "v_mul_f32 v72, v72, v36\n" \
    "v_mul_f32 v73, v73, v36\n" \
    "v_mfma_f32_16x16x32_bf16 v[84:87], a[48:51], v[40:43], 0\n" \
    "v_mul_f32 v74, v74, v36\n" \
    "v_mul_f32 v75, v75, v36\n" \
    "v_mfma_f32_16x16x32_bf16 v[80:83], a[36:39], v[44:47], v[80:83]\n" \
    "v_mul_f32 v76, v76, v36\n" \
    "v_mul_f32 v77, v77, v36\n" \
    "v_mfma_f32_16x16x32_bf16 v[84:87], a[52:55], v[44:47], v[84:87]\n" \
    "v_mul_f32 v78, v78, v36\n" \
    "v_mul_f32 v79, v79, v36\n" \
    "v_mfma_f32_16x16x32_bf16 v[80:83], a[40:43], v[48:51], v[80:83]\n" \
    "v_cvt_pk_bf16_f32 v96, v72, v73\n" \
    "v_cvt_pk_bf16_f32 v97, v74, v75\n" \
    "v_mfma_f32_16x16x32_bf16 v[84:87], a[56:59], v[48:51], v[84:87]\n" \
    "v_cvt_pk_bf16_f32 v98, v76, v77\n" \
    "v_cvt_pk_bf16_f32 v99, v78, v79\n" \
    "v_mfma_f32_16x16x32_bf16 v[80:83], a[44:47], v[52:55], v[80:83]\n" \
    "buffer_store_dwordx4 v[96:99], v134, %[rw], s46 offen sc1\n" \
    "s_add_i32 s46, s46, 0x20000\n" \
    "v_mfma_f32_16x16x32_bf16 v[84:87], a[60:63], v[52:55], v[84:87]\n" \
    "s_add_i32 s94, s94, 2322432\n" \
    "v_mfma_f32_16x16x32_bf16 v[72:75], a[64:67], v[40:43], 0\n" \
    "v_mul_f32 v80, v80, v36\n" \
    "v_mul_f32 v81, v81, v36\n" \
    "v_mfma_f32_16x16x32_bf16 v[76:79], a[80:83], v[40:43], 0\n" \
    "v_mul_f32 v82, v82, v36\n" \
    "v_mul_f32 v83, v83, v36\n" \
    "v_mfma_f32_16x16x32_bf16 v[72:75], a[68:71], v[44:47], v[72:75]\n" \
    "v_mul_f32 v84, v84, v36\n" \
    "v_mul_f32 v85, v85, v36\n" \
    "v_mfma_f32_16x16x32_bf16 v[76:79], a[84:87], v[44:47], v[76:79]\n" \
    "v_mul_f32 v86, v86, v36\n" \
    "v_mul_f32 v87, v87, v36\n" \
    "v_mfma_f32_16x16x32_bf16 v[72:75], a[72:75], v[48:51], v[72:75]\n" \
    "v_cvt_pk_bf16_f32 v100, v80, v81\n" \
    "v_cvt_pk_bf16_f32 v101, v82, v83\n" \
    "v_mfma_f32_16x16x32_bf16 v[76:79], a[88:91], v[48:51], v[76:79]\n" \
    "v_cvt_pk_bf16_f32 v102, v84, v85\n" \
    "v_cvt_pk_bf16_f32 v103, v86, v87\n" \
    "v_mfma_f32_16x16x32_bf16 v[72:75], a[76:79], v[52:55], v[72:75]\n" \
    "buffer_store_dwordx4 v[100:103], v134, %[rw], s46 offen sc1\n" \
    "s_add_i32 s46, s46, 0x20000\n" \
    "v_mfma_f32_16x16x32_bf16 v[76:79], a[92:95], v[52:55], v[76:79]\n" \
    "v_mfma_f32_16x16x32_bf16 v[80:83], a[96:99], v[40:43], 0\n" \
    "v_mul_f32 v72, v72, v36\n" \
    "v_mul_f32 v73, v73, v36\n" \
    "v_mfma_f32_16x16x32_bf16 v[84:87], a[112:115], v[40:43], 0\n" \
    "v_mul_f32 v74, v74, v36\n" \
    "v_mul_f32 v75, v75, v36\n" \
    "v_mfma_f32_16x16x32_bf16 v[80:83], a[100:103], v[44:47], v[80:83]\n" \
    "v_mul_f32 v76, v76, v36\n" \
    "v_mul_f32 v77, v77, v36\n" \
    "v_mfma_f32_16x16x32_bf16 v[84:87], a[116:119], v[44:47], v[84:87]\n" \
    "v_mul_f32 v78, v78, v36\n" \
    "v_mul_f32 v79, v79, v36\n" \
    "v_mfma_f32_16x16x32_bf16 v[80:83], a[104:107], v[48:51], v[80:83]\n" \
    "v_cvt_pk_bf16_f32 v96, v72, v73\n" \
    "v_cvt_pk_bf16_f32 v97, v74, v75\n" \
    "v_mfma_f32_16x16x32_bf16 v[84:87], a[120:123], v[48:51], v[84:87]\n" \
    "v_cvt_pk_bf16_f32 v98, v76, v77\n" \
    "v_cvt_pk_bf16_f32 v99, v78, v79\n" \
    "v_mfma_f32_16x16x32_bf16 v[80:83], a[108:111], v[52:55], v[80:83]\n" \
    "buffer_store_dwordx4 v[96:99], v134, %[rw], s46 offen sc1\n" \
    "s_add_i32 s46, s46, 0x20000\n" \
    "v_mfma_f32_16x16x32_bf16 v[84:87], a[124:127], v[52:55], v[84:87]\n" \
    "s_memtime vcc\n" \
    "s_nop 2\n" \
    "v_mul_f32 v80, v80, v36\n" \
    "v_mul_f32 v81, v81, v36\n" \
    "v_mul_f32 v82, v82, v36\n" \
    "v_mul_f32 v83, v83, v36\n" \
    "v_mul_f32 v84, v84, v36\n" \
    "v_mul_f32 v85, v85, v36\n" \
    "v_mul_f32 v86, v86, v36\n" \
    "v_mul_f32 v87, v87, v36\n" \
    "v_cvt_pk_bf16_f32 v100, v80, v81\n" \
    "v_cvt_pk_bf16_f32 v101, v82, v83\n" \
    "v_cvt_pk_bf16_f32 v102, v84, v85\n" \
    "v_cvt_pk_bf16_f32 v103, v86, v87\n" \
    "buffer_store_dwordx4 v[100:103], v134, %[rw], s46 offen sc1\n" \
    "s_waitcnt lgkmcnt(0)\n" \
    "s_add_i32 s90, vcc_lo, %[fw]\n" \
    "35:\n" \
    "s_memtime vcc\n" \
    "s_waitcnt lgkmcnt(0)\n" \
    "s_sub_i32 s75, s90, vcc_lo\n" \
    "s_cmp_gt_i32 s75, 0\n" \
    "s_cbranch_scc1 35b\n" \
    "36:\n" \
    "buffer_load_dwordx4 v[0:3], v127, %[rw], %[ua] offen offset:0 sc1\n" \
    "buffer_load_dwordx4 v[4:7], v127, %[rw], %[ua] offen offset:1024 sc1\n" \
    "buffer_load_dwordx4 v[8:11], v127, %[rw], %[ua] offen offset:2048 sc1\n" \
    "buffer_load_dwordx4 v[12:15], v127, %[rw], %[ua] offen offset:3072 sc1\n" \
    "s_waitcnt vmcnt(3)\n" \
    "v_mfma_f32_16x16x32_bf16 v[32:35], v[176:179], v[0:3], 0\n" \
    "v_max3_u32 v40, v0, v1, v0\n" \
    "v_max3_u32 v40, v2, v3, v40\n" \
    "s_waitcnt vmcnt(2)\n" \
    "v_mfma_f32_16x16x32_bf16 v[32:35], v[176:179], v[4:7], v[32:35]\n" \
    "v_max3_u32 v40, v4, v5, v40\n" \
    "v_max3_u32 v40, v6, v7, v40\n" \
    "s_waitcnt vmcnt(1)\n" \
    "v_mfma_f32_16x16x32_bf16 v[32:35], v[176:179], v[8:11], v[32:35]\n" \
    "v_max3_u32 v40, v8, v9, v40\n" \
    "v_max3_u32 v40, v10, v11, v40\n" \
    "s_waitcnt vmcnt(0)\n" \
    "v_mfma_f32_16x16x32_bf16 v[32:35], v[176:179], v[12:15], v[32:35]\n" \
    "v_max3_u32 v40, v12, v13, v40\n" \
    "v_max3_u32 v40, v14, v15, v40\n" \
    "v_cmp_eq_u32 vcc, -1, v40\n" \
    "s_nop 3\n" \
    "s_cbranch_vccnz 38f\n" \
    "39:\n" \
    "v_add_f32_dpp v32, v32, v32 row_ror:8 row_mask:0xf bank_mask:0xf\n" \
    "v_add_f32_dpp v33, v33, v33 row_ror:8 row_mask:0xf bank_mask:0xf\n" \
    "s_mov_b32 exec_lo, 0x00ff00ff\n" \
    "s_mov_b32 exec_hi, 0x00ff00ff\n" \
    "v_fma_f32 v32, v32, v114, v172\n" \
    "v_fma_f32 v33, v33, v114, v173\n" \
    "v_cvt_pk_bf16_f32 v32, v32, v33\n" \
    "buffer_store_dword v32, v168, %[ru], s94 offen sc1\n" \
    "s_mov_b64 exec, -1\n" \
    "s_mov_b32 s36, 63\n" \
    "s_branch 9f\n" \
    "38:\n" \
    "s_add_i32 s39, s39, -1\n" \
    "s_cmp_lt_i32 s39, 0\n" \
    "s_cbranch_scc1 39b\n" \
    "s_add_i32 s30, s30, 0x10000\n" \
    "s_sleep 2\n" \
    "s_branch 36b\n" \
    "21:\n" \
    "s_add_i32 s39, s39, -1\n" \
    "s_cmp_lt_i32 s39, 0\n" \
    "s_cbranch_scc1 22b\n" \
    "buffer_load_dword v100, off, %[rw], s62 sc1\n" \
    "s_waitcnt vmcnt(0)\n" \
    "v_readfirstlane_b32 s38, v100\n" \
    "s_cmp_lt_i32 s38, s91\n" \
    "s_cbranch_scc1 21b\n" \
    "s_branch 22b\n" \
    "23:\n" \
    "s_add_i32 s39, s39, -1\n" \
    "s_cmp_lt_i32 s39, 0\n" \
    "s_cbranch_scc1 24b\n" \
    "s_sleep 4\n" \
    "buffer_load_dwordx2 v[98:99], v113, %[rs], s78 offen sc1\n" \
    "s_waitcnt vmcnt(0)\n" \
    "v_cmp_ne_u16 vcc, s91, v99\n" \
    "s_nop 1\n" \
    "s_cbranch_vccnz 23b\n" \
    "s_branch 24b\n" \
    "26:\n" \
    "s_add_i32 s39, s39, -1\n" \
    "s_cmp_lt_i32 s39, 0\n" \
    "s_cbranch_scc1 27b\n" \
    "s_sleep 4\n" \
    "s_add_i32 s44, %[qb], 0\n" \
    "s_add_i32 s45, s44, 4096\n" \
    "buffer_load_dwordx4 v[136:139], v112, %[rw], s44 offen offset:0 sc1\n" \
    "buffer_load_dwordx4 v[140:143], v112, %[rw], s44 offen offset:2048 sc1\n" \
    "buffer_load_dwordx4 v[144:147], v112, %[rw], s45 offen offset:0 sc1\n" \
    "buffer_load_dwordx4 v[148:151], v112, %[rw], s45 offen offset:2048 sc1\n" \
    "s_waitcnt vmcnt(0)\n" \
    "s_branch 25b\n" \
    "28:\n" \
    "s_add_i32 s39, s39, -1\n" \
    "s_cmp_lt_i32 s39, 0\n" \
    "s_cbranch_scc1 29b\n" \
    "s_sleep 4\n" \
    "s_add_i32 s44, %[qb], 262144\n" \
    "s_add_i32 s45, s44, 4096\n" \
    "buffer_load_dwordx4 v[72:75], v112, %[rw], s44 offen offset:0 sc1\n" \
    "buffer_load_dwordx4 v[76:79], v112, %[rw], s44 offen offset:2048 sc1\n" \
    "buffer_load_dwordx4 v[104:107], v112, %[rw], s45 offen offset:0 sc1\n" \
    "buffer_load_dwordx4 v[108:111], v112, %[rw], s45 offen offset:2048 sc1\n" \
    "s_waitcnt vmcnt(0)\n" \
    "v_max3_u32 v128, v72, v73, v72\n" \
    "v_max3_u32 v128, v74, v75, v128\n" \
    "v_max3_u32 v128, v76, v77, v128\n" \
    "v_max3_u32 v128, v78, v79, v128\n" \
    "v_max3_u32 v128, v104, v105, v128\n" \
    "v_max3_u32 v128, v106, v107, v128\n" \
    "v_max3_u32 v128, v108, v109, v128\n" \
    "v_max3_u32 v128, v110, v111, v128\n" \
    "v_cmp_eq_u32 vcc, -1, v128\n" \
    "s_nop 1\n" \
    "s_cbranch_vccnz 28b\n" \
    "s_branch 29b\n" \
    "30:\n" \
    "s_memtime vcc\n" \
    "s_waitcnt lgkmcnt(0)\n" \
    "s_sub_i32 s75, s90, vcc_lo\n" \
    "s_cmp_gt_i32 s75, 100\n" \
    "s_cbranch_scc1 30b\n" \
    "s_branch 31b\n" \
    "32:\n" \
    "v_max3_u32 v82, v0, v1, v0\n" \
    "v_max3_u32 v82, v2, v3, v82\n" \
    "v_max3_u32 v82, v4, v5, v82\n" \
    "v_max3_u32 v82, v6, v7, v82\n" \
    "v_max3_u32 v82, v8, v9, v82\n" \
    "v_max3_u32 v82, v10, v11, v82\n" \
    "v_max3_u32 v82, v12, v13, v82\n" \
    "v_max3_u32 v82, v14, v15, v82\n" \
    "v_max3_u32 v82, v16, v17, v82\n" \
    "v_max3_u32 v82, v18, v19, v82\n" \
    "v_max3_u32 v82, v20, v21, v82\n" \
    "v_max3_u32 v82, v22, v23, v82\n" \
    "v_max3_u32 v82, v24, v25, v82\n" \
    "v_max3_u32 v82, v26, v27, v82\n" \
    "v_max3_u32 v82, v28, v29, v82\n" \
    "v_max3_u32 v82, v30, v31, v82\n" \
    "v_max3_u32 v82, v32, v33, v82\n" \
    "v_max3_u32 v82, v34, v35, v82\n" \
    "v_cmp_eq_u32 vcc, -1, v82\n" \
    "s_nop 4\n" \
    "s_cbranch_vccz 34b\n" \
    "s_add_i32 s39, s39, -1\n" \
    "s_cmp_lt_i32 s39, 0\n" \
    "s_cbranch_scc1 34b\n" \
    "s_add_i32 s30, s30, 1\n" \
    "s_waitcnt lgkmcnt(0)\n" \
    "s_barrier\n" \
    "v_and_b32 v96, 0x200, v127\n" \
    "s_and_b32 s80, s26, 0x800\n" \
    "v_mad_u32_u24 v96, v96, 3, v127\n" \
    "v_xor_b32 v96, s80, v96\n" \
    "s_mov_b32 m0, s74\n" \
    "s_add_i32 s76, s74, 4096\n" \
    "buffer_load_dwordx4 v125, %[rs], s69 offen sc1 lds\n" \
    "s_mov_b32 m0, s76\n" \
    "v_mov_b64 v[80:81], 0\n" \
    "v_mov_b64 v[82:83], 0\n" \
    "buffer_load_dwordx4 v125, %[rs], s70 offen sc1 lds\n" \
    "s_movk_i32 m0, 0x2000\n" \
    "s_mov_b32 exec_lo, s27\n" \
    "s_mov_b32 exec_hi, s27\n" \
    "buffer_load_dwordx4 v96, %[rs], s71 offen sc1 lds\n" \
    "s_mov_b64 exec, -1\n" \
    "v_mov_b64 v[84:85], 0\n" \
    "v_mov_b64 v[86:87], 0\n" \
    "s_waitcnt vmcnt(0)\n" \
    "s_barrier\n" \
    "ds_read_b128 v[0:3], v37 offset:0\n" \
    "ds_read_b128 v[4:7], v38 offset:0\n" \
    "ds_read_b128 v[8:11], v39 offset:0\n" \
    "ds_read_b128 v[12:15], v132 offset:0\n" \
    "ds_read_b128 v[16:19], v37 offset:4096\n" \
    "ds_read_b128 v[20:23], v38 offset:4096\n" \
    "ds_read_b128 v[24:27], v39 offset:4096\n" \
    "ds_read_b128 v[28:31], v132 offset:4096\n" \
    "ds_read_b128 v[32:35], v126 offset:0\n" \
    "s_waitcnt lgkmcnt(8)\n" \
    "v_smfmac_f32_16x16x64_bf16 v[80:83], v[0:3], a[128:135], v124\n" \
    "s_waitcnt lgkmcnt(7)\n" \
    "v_smfmac_f32_16x16x64_bf16 v[80:83], v[4:7], a[144:151], v124\n" \
    "s_waitcnt lgkmcnt(6)\n" \
    "v_smfmac_f32_16x16x64_bf16 v[80:83], v[8:11], a[160:167], v124\n" \
    "s_waitcnt lgkmcnt(5)\n" \
    "v_smfmac_f32_16x16x64_bf16 v[80:83], v[12:15], a[176:183], v124\n" \
    "s_waitcnt lgkmcnt(4)\n" \
    "v_smfmac_f32_16x16x64_bf16 v[80:83], v[16:19], a[192:199], v124\n" \
    "s_waitcnt lgkmcnt(3)\n" \
    "v_smfmac_f32_16x16x64_bf16 v[80:83], v[20:23], a[208:215], v124\n" \
    "s_waitcnt lgkmcnt(2)\n" \
    "v_smfmac_f32_16x16x64_bf16 v[80:83], v[24:27], a[224:231], v124\n" \
    "s_waitcnt lgkmcnt(1)\n" \
    "v_smfmac_f32_16x16x64_bf16 v[80:83], v[28:31], a[240:247], v124\n" \
    "s_waitcnt lgkmcnt(0)\n" \
    "v_smfmac_f32_16x16x64_bf16 v[80:83], v[32:35], v[152:159], v124\n" \
    "v_smfmac_f32_16x16x64_bf16 v[84:87], v[0:3], a[136:143], v124\n" \
    "v_smfmac_f32_16x16x64_bf16 v[84:87], v[4:7], a[152:159], v124\n" \
    "v_smfmac_f32_16x16x64_bf16 v[84:87], v[8:11], a[168:175], v124\n" \
    "v_smfmac_f32_16x16x64_bf16 v[84:87], v[12:15], a[184:191], v124\n" \
    "v_smfmac_f32_16x16x64_bf16 v[84:87], v[16:19], a[200:207], v124\n" \
    "v_add_f32 v80, v80, v82\n" \
    "v_add_f32 v81, v81, v83\n" \
    "s_branch 32b\n" \
    "7:\n" \
    "s_mov_b32 s43, 2\n" \
    "s_nop 7\n" \
    "s_waitcnt vmcnt(0)\n" \
    "9:\n" \
    "s_mov_b32 m0, s68\n" \
    "s_add_i32 s37, s37, s29\n" \
    "s_waitcnt lgkmcnt(0)\n" \
    "v_mov_b32 v0, s36\n" \
    "v_mov_b32 v1, s37\n" \
    "v_mov_b32 v2, s38\n" \
    "v_mov_b32 v3, s39\n" \
    "v_mov_b32 v4, s40\n" \
    "v_mov_b32 v5, s41\n" \
    "v_mov_b32 v6, s42\n" \
    "v_mov_b32 v7, s43\n" \
    "v_mov_b32 v8, s28\n" \
    "v_mov_b32 v9, s29\n" \
    "v_mov_b32 v10, s30\n" \
    "v_mov_b32 v11, s31\n" \
    "v_mov_b32 v12, s32\n" \
    "v_mov_b32 v13, s33\n" \
    "v_mov_b32 v14, s34\n" \
    "v_mov_b32 v15, s35\n" \
    "v_mov_b32 v16, s24\n" \
    "v_mov_b32 v17, s25\n" \
    "v_mov_b32 v18, s26\n" \
    "v_mov_b32 v19, s27\n" \
    "v_mov_b32 v20, s84\n" \
    "v_mov_b32 v21, s86\n" \
    "v_mov_b32 v22, s88\n" \
    "v_mov_b32 v23, s84\n" \
    "v_mov_b32 v24, s60\n" \
    "v_mov_b32 v25, s61\n" \
    "v_mov_b32 v26, s62\n" \
    "v_mov_b32 v27, s63\n" \
    "v_mov_b32 v28, s64\n" \
    "v_mov_b32 v29, s65\n" \
    "v_mov_b32 v30, s66\n" \
    "v_mov_b32 v31, s67\n" \
    ".Lstep_end:\n" \
    ""
static_assert(2 * PART_STEP == 32768 && 2 * OFF_CAN == 1703936 && F_ERR == 2 * OFF_CAN + 65536 && MEMB_STRIDE == 32768 && OFF_VF - OFF_KF == 2048 * 4096 * 2 && OFF_BO == 65011712 && OFF_GT == 67518464 && BO_ROW == 4352, "constants of the step block");
#define STEP_CLOBBER "v32", "v33", "v34", "v35", "v36", "v37", "v38", "v39", "v40", "v41", "v42", "v43", "v44", "v45", "v46", "v47", "v48", "v49", "v50", "v51", "v52", "v53", "v54", "v55", "v56", "v57", "v58", "v59", "v60", "v61", "v62", "v63", "v64", "v65", "v66", "v67", "v68", "v69", "v70", "v71", "v72", "v73", "v74", "v75", "v76", "v77", "v78", "v79", "v80", "v81", "v82", "v83", "v84", "v85", "v86", "v87", "v88", "v89", "v90", "v91", "v92", "v93", "v94", "v95", "v98", "v99", "v100", "v101", "v102", "v103", "v104", "v105", "v106", "v107", "v108", "v109", "v110", "v111", "v112", "v113", "v114", "s24", "s25", "s26", "s27", "s28", "s29", "s30", "s31", "s32", "s33", "s34", "s35", "s36", "s37", "s38", "s39", "s40", "s41", "s42", "s43", "s44", "s45", "s46", "s47", "s48", "s49", "s50", "s51", "s52", "s53", "s54", "s55", "s56", "s57", "s58", "s59", "s60", "s61", "s62", "s63", "s64", "s65", "s66", "s67", "s68", "s69", "s70", "s71", "s72", "s73", "s74", "s75", "s76", "s77", "s78", "s79", "s80", "s81", "s82", "s83", "s84", "s85", "s86", "s87", "s88", "s89", "s90", "s91", "s92", "s93", "s94", "s95", "s96", "s97", "vcc", "scc", "memory"
#define OUT16_ASM \
    "s_mov_b32 exec_lo, %[tm]\n" \
    "s_mov_b32 exec_hi, 0\n" \
    "buffer_load_dword v79, %[tv], %[rw], %[so] offen sc1\n" \
    "s_mov_b64 exec, -1\n" \
    "s_add_i32 s44, %[so], 4096\n" \
    "s_add_i32 s45, %[so], 8192\n" \
    "s_add_i32 s46, %[so], 12288\n" \
    "buffer_load_dwordx4 v[16:19], %[vo], %[rw], %[so] offen offset:0 sc1\n" \
    "ds_read_b128 v[80:83], %[wl] offset:0\n" \
    "ds_read_b128 v[84:87], %[wl] offset:1024\n" \
    "s_nop 7\n" \
    "v_mov_b32 v104, 0\n" \
    "buffer_load_dwordx4 v[20:23], %[vo], %[rw], %[so] offen offset:256 sc1\n" \
    "s_nop 7\n" \
    "s_nop 3\n" \
    "buffer_load_dwordx4 v[24:27], %[vo], %[rw], %[so] offen offset:1024 sc1\n" \
    "s_nop 7\n" \
    "s_nop 3\n" \
    "buffer_load_dwordx4 v[28:31], %[vo], %[rw], %[so] offen offset:1280 sc1\n" \
    "s_nop 7\n" \
    "s_nop 3\n" \
    "buffer_load_dwordx4 v[32:35], %[vo], %[rw], %[so] offen offset:2048 sc1\n" \
    "s_nop 7\n" \
    "s_nop 3\n" \
    "buffer_load_dwordx4 v[36:39], %[vo], %[rw], %[so] offen offset:2304 sc1\n" \
    "s_nop 7\n" \
    "s_nop 3\n" \
    "buffer_load_dwordx4 v[40:43], %[vo], %[rw], %[so] offen offset:3072 sc1\n" \
    "s_nop 7\n" \
    "s_nop 3\n" \
    "buffer_load_dwordx4 v[44:47], %[vo], %[rw], %[so] offen offset:3328 sc1\n" \
    "s_nop 7\n" \
    "s_nop 3\n" \
    "buffer_load_dwordx4 v[48:51], %[vo], %[rw], s44 offen offset:0 sc1\n" \
    "s_nop 7\n" \
    "s_nop 3\n" \
    "buffer_load_dwordx4 v[52:55], %[vo], %[rw], s44 offen offset:256 sc1\n" \
    "s_nop 7\n" \
    "s_nop 3\n" \
    "buffer_load_dwordx4 v[56:59], %[vo], %[rw], s44 offen offset:1024 sc1\n" \
    "s_nop 7\n" \
    "s_nop 3\n" \
    "buffer_load_dwordx4 v[60:63], %[vo], %[rw], s44 offen offset:1280 sc1\n" \
    "ds_read_b128 v[88:91], %[wl] offset:2048\n" \
    "s_waitcnt vmcnt(11)\n" \
    "v_mfma_f32_16x16x32_bf16 v[0:3], a[0:3], v[16:19], 0\n" \
    "v_max3_u32 v104, v104, v16, v17\n" \
    "v_mfma_f32_16x16x32_bf16 v[4:7], a[128:131], v[16:19], 0\n" \
    "v_max3_u32 v104, v104, v18, v19\n" \
    "v_mfma_f32_16x16x32_bf16 v[8:11], v[128:131], v[16:19], 0\n" \
    "s_waitcnt lgkmcnt(2)\n" \
    "v_mfma_f32_16x16x32_bf16 v[12:15], v[80:83], v[16:19], 0\n" \
    "buffer_load_dwordx4 v[64:67], %[vo], %[rw], s44 offen offset:2048 sc1\n" \
    "ds_read_b128 v[80:83], %[wl] offset:3072\n" \
    "s_waitcnt vmcnt(11)\n" \
    "v_mfma_f32_16x16x32_bf16 v[0:3], a[4:7], v[20:23], v[0:3]\n" \
    "v_max3_u32 v104, v104, v20, v21\n" \
    "v_mfma_f32_16x16x32_bf16 v[4:7], a[132:135], v[20:23], v[4:7]\n" \
    "v_max3_u32 v104, v104, v22, v23\n" \
    "v_mfma_f32_16x16x32_bf16 v[8:11], v[132:135], v[20:23], v[8:11]\n" \
    "s_waitcnt lgkmcnt(2)\n" \
    "v_mfma_f32_16x16x32_bf16 v[12:15], v[84:87], v[20:23], v[12:15]\n" \
    "buffer_load_dwordx4 v[68:71], %[vo], %[rw], s44 offen offset:2304 sc1\n" \
    "ds_read_b128 v[84:87], %[wl] offset:4096\n" \
    "s_waitcnt vmcnt(11)\n" \
    "v_mfma_f32_16x16x32_bf16 v[0:3], a[8:11], v[24:27], v[0:3]\n" \
    "v_max3_u32 v104, v104, v24, v25\n" \
    "v_mfma_f32_16x16x32_bf16 v[4:7], a[136:139], v[24:27], v[4:7]\n" \
    "v_max3_u32 v104, v104, v26, v27\n" \
    "v_mfma_f32_16x16x32_bf16 v[8:11], v[136:139], v[24:27], v[8:11]\n" \
    "s_waitcnt lgkmcnt(2)\n" \
    "v_mfma_f32_16x16x32_bf16 v[12:15], v[88:91], v[24:27], v[12:15]\n" \
    "buffer_load_dwordx4 v[72:75], %[vo], %[rw], s44 offen offset:3072 sc1\n" \
    "ds_read_b128 v[88:91], %[wl] offset:5120\n" \
    "s_waitcnt vmcnt(11)\n" \
    "v_mfma_f32_16x16x32_bf16 v[0:3], a[12:15], v[28:31], v[0:3]\n" \
    "v_max3_u32 v104, v104, v28, v29\n" \
    "v_mfma_f32_16x16x32_bf16 v[4:7], a[140:143], v[28:31], v[4:7]\n" \
    "v_max3_u32 v104, v104, v30, v31\n" \
    "v_mfma_f32_16x16x32_bf16 v[8:11], v[140:143], v[28:31], v[8:11]\n" \
    "s_waitcnt lgkmcnt(2)\n" \
    "v_mfma_f32_16x16x32_bf16 v[12:15], v[80:83], v[28:31], v[12:15]\n" \
    "buffer_load_dwordx4 v[76:79], %[vo], %[rw], s44 offen offset:3328 sc1\n" \
    "ds_read_b128 v[80:83], %[wl] offset:6144\n" \
    "s_waitcnt vmcnt(11)\n" \
    "v_mfma_f32_16x16x32_bf16 v[0:3], a[16:19], v[32:35], v[0:3]\n" \
    "v_max3_u32 v104, v104, v32, v33\n" \
    "v_mfma_f32_16x16x32_bf16 v[4:7], a[144:147], v[32:35], v[4:7]\n" \
    "v_max3_u32 v104, v104, v34, v35\n" \
    "v_mfma_f32_16x16x32_bf16 v[8:11], v[144:147], v[32:35], v[8:11]\n" \
    "s_waitcnt lgkmcnt(2)\n" \
    "v_mfma_f32_16x16x32_bf16 v[12:15], v[84:87], v[32:35], v[12:15]\n" \
    "buffer_load_dwordx4 v[16:19], %[vo], %[rw], s45 offen offset:0 sc1\n" \
    "ds_read_b128 v[84:87], %[wl] offset:7168\n" \
    "s_waitcnt vmcnt(11)\n" \
    "v_mfma_f32_16x16x32_bf16 v[0:3], a[20:23], v[36:39], v[0:3]\n" \
    "v_max3_u32 v104, v104, v36, v37\n" \
    "v_mfma_f32_16x16x32_bf16 v[4:7], a[148:151], v[36:39], v[4:7]\n" \
    "v_max3_u32 v104, v104, v38, v39\n" \
    "v_mfma_f32_16x16x32_bf16 v[8:11], v[148:151], v[36:39], v[8:11]\n" \
    "s_waitcnt lgkmcnt(2)\n" \
    "v_mfma_f32_16x16x32_bf16 v[12:15], v[88:91], v[36:39], v[12:15]\n" \
    "buffer_load_dwordx4 v[20:23], %[vo], %[rw], s45 offen offset:256 sc1\n" \
    "ds_read_b128 v[88:91], %[wl] offset:8192\n" \
    "s_waitcnt vmcnt(11)\n" \
    "v_mfma_f32_16x16x32_bf16 v[0:3], a[24:27], v[40:43], v[0:3]\n" \
    "v_max3_u32 v104, v104, v40, v41\n" \
    "v_mfma_f32_16x16x32_bf16 v[4:7], a[152:155], v[40:43], v[4:7]\n" \
    "v_max3_u32 v104, v104, v42, v43\n" \
    "v_mfma_f32_16x16x32_bf16 v[8:11], v[152:155], v[40:43], v[8:11]\n" \
    "s_waitcnt lgkmcnt(2)\n" \
    "v_mfma_f32_16x16x32_bf16 v[12:15], v[80:83], v[40:43], v[12:15]\n" \
    "buffer_load_dwordx4 v[24:27], %[vo], %[rw], s45 offen offset:1024 sc1\n" \
    "ds_read_b128 v[80:83], %[wl] offset:9216\n" \
    "s_waitcnt vmcnt(11)\n" \
    "v_mfma_f32_16x16x32_bf16 v[0:3], a[28:31], v[44:47], v[0:3]\n" \
    "v_max3_u32 v104, v104, v44, v45\n" \
    "v_mfma_f32_16x16x32_bf16 v[4:7], a[156:159], v[44:47], v[4:7]\n" \
    "v_max3_u32 v104, v104, v46, v47\n" \
    "v_mfma_f32_16x16x32_bf16 v[8:11], v[156:159], v[44:47], v[8:11]\n" \
    "s_waitcnt lgkmcnt(2)\n" \
    "v_mfma_f32_16x16x32_bf16 v[12:15], v[84:87], v[44:47], v[12:15]\n" \
    "buffer_load_dwordx4 v[28:31], %[vo], %[rw], s45 offen offset:1280 sc1\n" \
    "ds_read_b128 v[84:87], %[wl] offset:10240\n" \
    "s_waitcnt vmcnt(11)\n" \
    "v_mfma_f32_16x16x32_bf16 v[0:3], a[32:35], v[48:51], v[0:3]\n" \
    "v_max3_u32 v104, v104, v48, v49\n" \
    "v_mfma_f32_16x16x32_bf16 v[4:7], a[160:163], v[48:51], v[4:7]\n" \
    "v_max3_u32 v104, v104, v50, v51\n" \
    "v_mfma_f32_16x16x32_bf16 v[8:11], v[160:163], v[48:51], v[8:11]\n" \
    "s_waitcnt lgkmcnt(2)\n" \
    "v_mfma_f32_16x16x32_bf16 v[12:15], v[88:91], v[48:51], v[12:15]\n" \
    "buffer_load_dwordx4 v[32:35], %[vo], %[rw], s45 offen offset:2048 sc1\n" \
    "ds_read_b128 v[88:91], %[wl] offset:11264\n" \
    "s_waitcnt vmcnt(11)\n" \
    "v_mfma_f32_16x16x32_bf16 v[0:3], a[36:39], v[52:55], v[0:3]\n" \
    "v_max3_u32 v104, v104, v52, v53\n" \
    "v_mfma_f32_16x16x32_bf16 v[4:7], a[164:167], v[52:55], v[4:7]\n" \
    "v_max3_u32 v104, v104, v54, v55\n" \
    "v_mfma_f32_16x16x32_bf16 v[8:11], v[164:167], v[52:55], v[8:11]\n" \
    "s_waitcnt lgkmcnt(2)\n" \
    "v_mfma_f32_16x16x32_bf16 v[12:15], v[80:83], v[52:55], v[12:15]\n" \
    "buffer_load_dwordx4 v[36:39], %[vo], %[rw], s45 offen offset:2304 sc1\n" \
    "ds_read_b128 v[80:83], %[wl] offset:12288\n" \
    "s_waitcnt vmcnt(11)\n" \
    "v_mfma_f32_16x16x32_bf16 v[0:3], a[40:43], v[56:59], v[0:3]\n" \
    "v_max3_u32 v104, v104, v56, v57\n" \
    "v_mfma_f32_16x16x32_bf16 v[4:7], a[168:171], v[56:59], v[4:7]\n" \
    "v_max3_u32 v104, v104, v58, v59\n" \
    "v_mfma_f32_16x16x32_bf16 v[8:11], v[168:171], v[56:59], v[8:11]\n" \
    "s_waitcnt lgkmcnt(2)\n" \
    "v_mfma_f32_16x16x32_bf16 v[12:15], v[84:87], v[56:59], v[12:15]\n" \
    "buffer_load_dwordx4 v[40:43], %[vo], %[rw], s45 offen offset:3072 sc1\n" \
    "ds_read_b128 v[84:87], %[wl] offset:13312\n" \
    "s_waitcnt vmcnt(11)\n" \
    "v_mfma_f32_16x16x32_bf16 v[0:3], a[44:47], v[60:63], v[0:3]\n" \
    "v_max3_u32 v104, v104, v60, v61\n" \
    "v_mfma_f32_16x16x32_bf16 v[4:7], a[172:175], v[60:63], v[4:7]\n" \
    "v_max3_u32 v104, v104, v62, v63\n" \
    "v_mfma_f32_16x16x32_bf16 v[8:11], v[172:175], v[60:63], v[8:11]\n" \
    "s_waitcnt lgkmcnt(2)\n" \
    "v_mfma_f32_16x16x32_bf16 v[12:15], v[88:91], v[60:63], v[12:15]\n" \
    "buffer_load_dwordx4 v[44:47], %[vo], %[rw], s45 offen offset:3328 sc1\n" \
    "ds_read_b128 v[88:91], %[wl] offset:14336\n" \
    "s_waitcnt vmcnt(11)\n" \
    "v_mfma_f32_16x16x32_bf16 v[0:3], a[48:51], v[64:67], v[0:3]\n" \
    "v_max3_u32 v104, v104, v64, v65\n" \
    "v_mfma_f32_16x16x32_bf16 v[4:7], a[176:179], v[64:67], v[4:7]\n" \
    "v_max3_u32 v104, v104, v66, v67\n" \
    "v_mfma_f32_16x16x32_bf16 v[8:11], v[176:179], v[64:67], v[8:11]\n" \
    "s_waitcnt lgkmcnt(2)\n" \
    "v_mfma_f32_16x16x32_bf16 v[12:15], v[80:83], v[64:67], v[12:15]\n" \
    "buffer_load_dwordx4 v[48:51], %[vo], %[rw], s46 offen offset:0 sc1\n" \
    "ds_read_b128 v[80:83], %[wl] offset:15360\n" \
    "s_waitcnt vmcnt(11)\n" \
    "v_mfma_f32_16x16x32_bf16 v[0:3], a[52:55], v[68:71], v[0:3]\n" \
    "v_max3_u32 v104, v104, v68, v69\n" \
    "v_mfma_f32_16x16x32_bf16 v[4:7], a[180:183], v[68:71], v[4:7]\n" \
    "v_max3_u32 v104, v104, v70, v71\n" \
    "v_mfma_f32_16x16x32_bf16 v[8:11], v[180:183], v[68:71], v[8:11]\n" \
    "s_waitcnt lgkmcnt(2)\n" \
    "v_mfma_f32_16x16x32_bf16 v[12:15], v[84:87], v[68:71], v[12:15]\n" \
    "buffer_load_dwordx4 v[52:55], %[vo], %[rw], s46 offen offset:256 sc1\n" \
    "ds_read_b128 v[84:87], %[wl] offset:16384\n" \
    "s_waitcnt vmcnt(11)\n" \
    "v_mfma_f32_16x16x32_bf16 v[0:3], a[56:59], v[72:75], v[0:3]\n" \
    "v_max3_u32 v104, v104, v72, v73\n" \
    "v_mfma_f32_16x16x32_bf16 v[4:7], a[184:187], v[72:75], v[4:7]\n" \
    "v_max3_u32 v104, v104, v74, v75\n" \
    "v_mfma_f32_16x16x32_bf16 v[8:11], v[184:187], v[72:75], v[8:11]\n" \
    "s_waitcnt lgkmcnt(2)\n" \
    "v_mfma_f32_16x16x32_bf16 v[12:15], v[88:91], v[72:75], v[12:15]\n" \
    "buffer_load_dwordx4 v[56:59], %[vo], %[rw], s46 offen offset:1024 sc1\n" \
    "ds_read_b128 v[88:91], %[wl] offset:17408\n" \
    "s_waitcnt vmcnt(11)\n" \
    "v_mfma_f32_16x16x32_bf16 v[0:3], a[60:63], v[76:79], v[0:3]\n" \
    "v_max3_u32 v104, v104, v76, v77\n" \
    "v_mfma_f32_16x16x32_bf16 v[4:7], a[188:191], v[76:79], v[4:7]\n" \
    "v_max3_u32 v104, v104, v78, v79\n" \
    "v_mfma_f32_16x16x32_bf16 v[8:11], v[188:191], v[76:79], v[8:11]\n" \
    "s_waitcnt lgkmcnt(2)\n" \
    "v_mfma_f32_16x16x32_bf16 v[12:15], v[80:83], v[76:79], v[12:15]\n" \
    "buffer_load_dwordx4 v[60:63], %[vo], %[rw], s46 offen offset:1280 sc1\n" \
    "ds_read_b128 v[80:83], %[wl] offset:18432\n" \
    "s_waitcnt vmcnt(11)\n" \
    "v_mfma_f32_16x16x32_bf16 v[0:3], a[64:67], v[16:19], v[0:3]\n" \
    "v_max3_u32 v104, v104, v16, v17\n" \
    "v_mfma_f32_16x16x32_bf16 v[4:7], a[192:195], v[16:19], v[4:7]\n" \
    "v_max3_u32 v104, v104, v18, v19\n" \
    "v_mfma_f32_16x16x32_bf16 v[8:11], v[192:195], v[16:19], v[8:11]\n" \
    "s_waitcnt lgkmcnt(2)\n" \
    "v_mfma_f32_16x16x32_bf16 v[12:15], v[84:87], v[16:19], v[12:15]\n" \
    "buffer_load_dwordx4 v[64:67], %[vo], %[rw], s46 offen offset:2048 sc1\n" \
    "ds_read_b128 v[84:87], %[wl] offset:19456\n" \
    "s_waitcnt vmcnt(11)\n" \
    "v_mfma_f32_16x16x32_bf16 v[0:3], a[68:71], v[20:23], v[0:3]\n" \
    "v_max3_u32 v104, v104, v20, v21\n" \
    "v_mfma_f32_16x16x32_bf16 v[4:7], a[196:199], v[20:23], v[4:7]\n" \
    "v_max3_u32 v104, v104, v22, v23\n" \
    "v_mfma_f32_16x16x32_bf16 v[8:11], v[196:199], v[20:23], v[8:11]\n" \
    "s_waitcnt lgkmcnt(2)\n" \
    "v_mfma_f32_16x16x32_bf16 v[12:15], v[88:91], v[20:23], v[12:15]\n" \
    "buffer_load_dwordx4 v[68:71], %[vo], %[rw], s46 offen offset:2304 sc1\n" \
    "ds_read_b128 v[88:91], %[wl] offset:20480\n" \
    "s_waitcnt vmcnt(11)\n" \
    "v_mfma_f32_16x16x32_bf16 v[0:3], a[72:75], v[24:27], v[0:3]\n" \
    "v_max3_u32 v104, v104, v24, v25\n" \
    "v_mfma_f32_16x16x32_bf16 v[4:7], a[200:203], v[24:27], v[4:7]\n" \
    "v_max3_u32 v104, v104, v26, v27\n" \
    "v_mfma_f32_16x16x32_bf16 v[8:11], v[200:203], v[24:27], v[8:11]\n" \
    "s_waitcnt lgkmcnt(2)\n" \
    "v_mfma_f32_16x16x32_bf16 v[12:15], v[80:83], v[24:27], v[12:15]\n" \
    "buffer_load_dwordx4 v[72:75], %[vo], %[rw], s46 offen offset:3072 sc1\n" \
    "ds_read_b128 v[80:83], %[wl] offset:21504\n" \
    "s_waitcnt vmcnt(11)\n" \
    "v_mfma_f32_16x16x32_bf16 v[0:3], a[76:79], v[28:31], v[0:3]\n" \
    "v_max3_u32 v104, v104, v28, v29\n" \
    "v_mfma_f32_16x16x32_bf16 v[4:7], a[204:207], v[28:31], v[4:7]\n" \
    "v_max3_u32 v104, v104, v30, v31\n" \
    "v_mfma_f32_16x16x32_bf16 v[8:11], v[204:207], v[28:31], v[8:11]\n" \
    "s_waitcnt lgkmcnt(2)\n" \
    "v_mfma_f32_16x16x32_bf16 v[12:15], v[84:87], v[28:31], v[12:15]\n" \
    "buffer_load_dwordx4 v[76:79], %[vo], %[rw], s46 offen offset:3328 sc1\n" \
    "ds_read_b128 v[84:87], %[wl] offset:22528\n" \
    "s_waitcnt vmcnt(11)\n" \
    "v_mfma_f32_16x16x32_bf16 v[0:3], a[80:83], v[32:35], v[0:3]\n" \
    "v_max3_u32 v104, v104, v32, v33\n" \
    "v_mfma_f32_16x16x32_bf16 v[4:7], a[208:211], v[32:35], v[4:7]\n" \
    "v_max3_u32 v104, v104, v34, v35\n" \
    "v_mfma_f32_16x16x32_bf16 v[8:11], v[208:211], v[32:35], v[8:11]\n" \
    "s_waitcnt lgkmcnt(2)\n" \
    "v_mfma_f32_16x16x32_bf16 v[12:15], v[88:91], v[32:35], v[12:15]\n" \
    "buffer_load_dwordx2 v[106:107], %[bv], %[rw], %[bs] offen sc1\n" \
    "ds_read_b128 v[88:91], %[wl] offset:23552\n" \
    "s_waitcnt vmcnt(11)\n" \
    "v_mfma_f32_16x16x32_bf16 v[0:3], a[84:87], v[36:39], v[0:3]\n" \
    "v_max3_u32 v104, v104, v36, v37\n" \
    "v_mfma_f32_16x16x32_bf16 v[4:7], a[212:215], v[36:39], v[4:7]\n" \
    "v_max3_u32 v104, v104, v38, v39\n" \
    "v_mfma_f32_16x16x32_bf16 v[8:11], v[212:215], v[36:39], v[8:11]\n" \
    "s_waitcnt lgkmcnt(2)\n" \
    "v_mfma_f32_16x16x32_bf16 v[12:15], v[80:83], v[36:39], v[12:15]\n" \
    "buffer_load_dword v108, %[gv], %[rw], %[gs] offen sc1\n" \
    "ds_read_b128 v[80:83], %[wl] offset:24576\n" \
    "s_waitcnt vmcnt(11)\n" \
    "v_mfma_f32_16x16x32_bf16 v[0:3], a[88:91], v[40:43], v[0:3]\n" \
    "v_max3_u32 v104, v104, v40, v41\n" \
    "v_mfma_f32_16x16x32_bf16 v[4:7], a[216:219], v[40:43], v[4:7]\n" \
    "v_max3_u32 v104, v104, v42, v43\n" \
    "v_mfma_f32_16x16x32_bf16 v[8:11], v[216:219], v[40:43], v[8:11]\n" \
    "s_waitcnt lgkmcnt(2)\n" \
    "v_mfma_f32_16x16x32_bf16 v[12:15], v[84:87], v[40:43], v[12:15]\n" \
    "buffer_load_dword v105, %[sv], %[rw], %[ss] offen sc1\n" \
    "ds_read_b128 v[84:87], %[wl] offset:25600\n" \
    "s_waitcnt vmcnt(11)\n" \
    "v_mfma_f32_16x16x32_bf16 v[0:3], a[92:95], v[44:47], v[0:3]\n" \
    "v_max3_u32 v104, v104, v44, v45\n" \
    "v_mfma_f32_16x16x32_bf16 v[4:7], a[220:223], v[44:47], v[4:7]\n" \
    "v_max3_u32 v104, v104, v46, v47\n" \
    "v_mfma_f32_16x16x32_bf16 v[8:11], v[220:223], v[44:47], v[8:11]\n" \
    "s_waitcnt lgkmcnt(2)\n" \
    "v_mfma_f32_16x16x32_bf16 v[12:15], v[88:91], v[44:47], v[12:15]\n" \
    "ds_read_b128 v[88:91], %[wl] offset:26624\n" \
    "s_waitcnt vmcnt(10)\n" \
    "v_mfma_f32_16x16x32_bf16 v[0:3], a[96:99], v[48:51], v[0:3]\n" \
    "v_max3_u32 v104, v104, v48, v49\n" \
    "v_mfma_f32_16x16x32_bf16 v[4:7], a[224:227], v[48:51], v[4:7]\n" \
    "v_max3_u32 v104, v104, v50, v51\n" \
    "v_mfma_f32_16x16x32_bf16 v[8:11], v[224:227], v[48:51], v[8:11]\n" \
    "s_waitcnt lgkmcnt(2)\n" \
    "v_mfma_f32_16x16x32_bf16 v[12:15], v[80:83], v[48:51], v[12:15]\n" \
    "ds_read_b128 v[80:83], %[wl] offset:27648\n" \
    "s_waitcnt vmcnt(9)\n" \
    "v_mfma_f32_16x16x32_bf16 v[0:3], a[100:103], v[52:55], v[0:3]\n" \
    "v_max3_u32 v104, v104, v52, v53\n" \
    "v_mfma_f32_16x16x32_bf16 v[4:7], a[228:231], v[52:55], v[4:7]\n" \
    "v_max3_u32 v104, v104, v54, v55\n" \
    "v_mfma_f32_16x16x32_bf16 v[8:11], v[228:231], v[52:55], v[8:11]\n" \
    "s_waitcnt lgkmcnt(2)\n" \
    "v_mfma_f32_16x16x32_bf16 v[12:15], v[84:87], v[52:55], v[12:15]\n" \
    "ds_read_b128 v[84:87], %[wl] offset:28672\n" \
    "s_waitcnt vmcnt(8)\n" \
    "v_mfma_f32_16x16x32_bf16 v[0:3], a[104:107], v[56:59], v[0:3]\n" \
    "v_max3_u32 v104, v104, v56, v57\n" \
    "v_mfma_f32_16x16x32_bf16 v[4:7], a[232:235], v[56:59], v[4:7]\n" \
    "v_max3_u32 v104, v104, v58, v59\n" \
    "v_mfma_f32_16x16x32_bf16 v[8:11], v[232:235], v[56:59], v[8:11]\n" \
    "s_waitcnt lgkmcnt(2)\n" \
    "v_mfma_f32_16x16x32_bf16 v[12:15], v[88:91], v[56:59], v[12:15]\n" \
    "ds_read_b128 v[88:91], %[wl] offset:29696\n" \
    "s_waitcnt vmcnt(7)\n" \
    "v_mfma_f32_16x16x32_bf16 v[0:3], a[108:111], v[60:63], v[0:3]\n" \
    "v_max3_u32 v104, v104, v60, v61\n" \
    "v_mfma_f32_16x16x32_bf16 v[4:7], a[236:239], v[60:63], v[4:7]\n" \
    "v_max3_u32 v104, v104, v62, v63\n" \
    "v_mfma_f32_16x16x32_bf16 v[8:11], v[236:239], v[60:63], v[8:11]\n" \
    "s_waitcnt lgkmcnt(2)\n" \
    "v_mfma_f32_16x16x32_bf16 v[12:15], v[80:83], v[60:63], v[12:15]\n" \
    "ds_read_b128 v[80:83], %[wl] offset:30720\n" \
    "s_waitcnt vmcnt(6)\n" \
    "v_mfma_f32_16x16x32_bf16 v[0:3], a[112:115], v[64:67], v[0:3]\n" \
    "v_max3_u32 v104, v104, v64, v65\n" \
    "v_mfma_f32_16x16x32_bf16 v[4:7], a[240:243], v[64:67], v[4:7]\n" \
    "v_max3_u32 v104, v104, v66, v67\n" \
    "v_mfma_f32_16x16x32_bf16 v[8:11], v[240:243], v[64:67], v[8:11]\n" \
    "s_waitcnt lgkmcnt(2)\n" \
    "v_mfma_f32_16x16x32_bf16 v[12:15], v[84:87], v[64:67], v[12:15]\n" \
    "ds_read_b128 v[84:87], %[wl] offset:31744\n" \
    "s_waitcnt vmcnt(5)\n" \
    "v_mfma_f32_16x16x32_bf16 v[0:3], a[116:119], v[68:71], v[0:3]\n" \
    "v_max3_u32 v104, v104, v68, v69\n" \
    "v_mfma_f32_16x16x32_bf16 v[4:7], a[244:247], v[68:71], v[4:7]\n" \
    "v_max3_u32 v104, v104, v70, v71\n" \
    "v_mfma_f32_16x16x32_bf16 v[8:11], v[244:247], v[68:71], v[8:11]\n" \
    "s_waitcnt lgkmcnt(2)\n" \
    "v_mfma_f32_16x16x32_bf16 v[12:15], v[88:91], v[68:71], v[12:15]\n" \
    "s_waitcnt vmcnt(4)\n" \
    "v_mfma_f32_16x16x32_bf16 v[0:3], a[120:123], v[72:75], v[0:3]\n" \
    "v_max3_u32 v104, v104, v72, v73\n" \
    "v_mfma_f32_16x16x32_bf16 v[4:7], a[248:251], v[72:75], v[4:7]\n" \
    "v_max3_u32 v104, v104, v74, v75\n" \
    "v_mfma_f32_16x16x32_bf16 v[8:11], v[248:251], v[72:75], v[8:11]\n" \
    "s_waitcnt lgkmcnt(1)\n" \
    "v_mfma_f32_16x16x32_bf16 v[12:15], v[80:83], v[72:75], v[12:15]\n" \
    "s_waitcnt vmcnt(3)\n" \
    "v_mfma_f32_16x16x32_bf16 v[0:3], a[124:127], v[76:79], v[0:3]\n" \
    "v_max3_u32 v104, v104, v76, v77\n" \
    "v_mfma_f32_16x16x32_bf16 v[4:7], a[252:255], v[76:79], v[4:7]\n" \
    "v_max3_u32 v104, v104, v78, v79\n" \
    "v_mfma_f32_16x16x32_bf16 v[8:11], v[252:255], v[76:79], v[8:11]\n" \
    "s_waitcnt lgkmcnt(0)\n" \
    "v_mfma_f32_16x16x32_bf16 v[12:15], v[84:87], v[76:79], v[12:15]\n" \
    "s_waitcnt vmcnt(0)\n" \
    "s_nop 7\n" \
    ""
#define OUT8_ASM \
    "s_mov_b32 exec_lo, %[tm]\n" \
    "s_mov_b32 exec_hi, 0\n" \
    "buffer_load_dword v79, %[tv], %[rw], %[so] offen sc1\n" \
    "s_mov_b64 exec, -1\n" \
    "s_add_i32 s44, %[so], 4096\n" \
    "s_add_i32 s45, %[so], 8192\n" \
    "s_add_i32 s46, %[so], 12288\n" \
    "buffer_load_dwordx4 v[16:19], %[vo], %[rw], %[so] offen offset:0 sc1\n" \
    "ds_read_b128 v[80:83], %[wl] offset:0\n" \
    "ds_read_b128 v[84:87], %[wl] offset:1024\n" \
    "ds_read_b128 v[88:91], %[wl] offset:2048\n" \
    "ds_read_b128 v[92:95], %[wl] offset:3072\n" \
    "v_mov_b64 v[0:1], 0\n" \
    "v_mov_b64 v[2:3], 0\n" \
    "v_mov_b64 v[4:5], 0\n" \
    "v_mov_b64 v[6:7], 0\n" \
    "v_mov_b64 v[8:9], 0\n" \
    "v_mov_b64 v[10:11], 0\n" \
    "v_mov_b64 v[12:13], 0\n" \
    "v_mov_b64 v[14:15], 0\n" \
    "v_mov_b32 v104, 0\n" \
    "buffer_load_dwordx4 v[20:23], %[vo], %[rw], %[so] offen offset:1024 sc1\n" \
    "s_nop 7\n" \
    "s_nop 3\n" \
    "buffer_load_dwordx4 v[24:27], %[vo], %[rw], %[so] offen offset:2048 sc1\n" \
    "s_nop 7\n" \
    "s_nop 3\n" \
    "buffer_load_dwordx4 v[28:31], %[vo], %[rw], %[so] offen offset:3072 sc1\n" \
    "s_nop 7\n" \
    "s_nop 3\n" \
    "buffer_load_dwordx4 v[32:35], %[vo], %[rw], s44 offen offset:0 sc1\n" \
    "s_nop 7\n" \
    "s_nop 3\n" \
    "buffer_load_dwordx4 v[36:39], %[vo], %[rw], s44 offen offset:1024 sc1\n" \
    "s_nop 7\n" \
    "s_nop 3\n" \
    "buffer_load_dwordx4 v[40:43], %[vo], %[rw], s44 offen offset:2048 sc1\n" \
    "s_nop 7\n" \
    "s_nop 3\n" \
    "buffer_load_dwordx4 v[44:47], %[vo], %[rw], s44 offen offset:3072 sc1\n" \
    "s_nop 7\n" \
    "s_nop 3\n" \
    "buffer_load_dwordx4 v[48:51], %[vo], %[rw], s45 offen offset:0 sc1\n" \
    "s_nop 7\n" \
    "s_nop 3\n" \
    "buffer_load_dwordx4 v[52:55], %[vo], %[rw], s45 offen offset:1024 sc1\n" \
    "ds_read_b128 v[96:99], %[wl] offset:4096\n" \
    "ds_read_b128 v[100:103], %[wl] offset:5120\n" \
    "s_waitcnt vmcnt(9)\n" \
    "v_smfmac_f32_16x16x64_bf16 v[0:3], v[16:19], a[0:7], %[idx]\n" \
    "v_max3_u32 v104, v104, v16, v17\n" \
    "v_smfmac_f32_16x16x64_bf16 v[4:7], v[16:19], a[128:135], %[idx]\n" \
    "v_max3_u32 v104, v104, v18, v19\n" \
    "v_smfmac_f32_16x16x64_bf16 v[8:11], v[16:19], v[128:135], %[idx]\n" \
    "s_waitcnt lgkmcnt(4)\n" \
    "v_smfmac_f32_16x16x64_bf16 v[12:15], v[16:19], v[80:87], %[idx]\n" \
    "buffer_load_dwordx4 v[56:59], %[vo], %[rw], s45 offen offset:2048 sc1\n" \
    "ds_read_b128 v[80:83], %[wl] offset:6144\n" \
    "ds_read_b128 v[84:87], %[wl] offset:7168\n" \
    "s_waitcnt vmcnt(9)\n" \
    "v_smfmac_f32_16x16x64_bf16 v[0:3], v[20:23], a[8:15], %[idx]\n" \
    "v_max3_u32 v104, v104, v20, v21\n" \
    "v_smfmac_f32_16x16x64_bf16 v[4:7], v[20:23], a[136:143], %[idx]\n" \
    "v_max3_u32 v104, v104, v22, v23\n" \
    "v_smfmac_f32_16x16x64_bf16 v[8:11], v[20:23], v[136:143], %[idx]\n" \
    "s_waitcnt lgkmcnt(4)\n" \
    "v_smfmac_f32_16x16x64_bf16 v[12:15], v[20:23], v[88:95], %[idx]\n" \
    "buffer_load_dwordx4 v[60:63], %[vo], %[rw], s45 offen offset:3072 sc1\n" \
    "ds_read_b128 v[88:91], %[wl] offset:8192\n" \
    "ds_read_b128 v[92:95], %[wl] offset:9216\n" \
    "s_waitcnt vmcnt(9)\n" \
    "v_smfmac_f32_16x16x64_bf16 v[0:3], v[24:27], a[16:23], %[idx]\n" \
    "v_max3_u32 v104, v104, v24, v25\n" \
    "v_smfmac_f32_16x16x64_bf16 v[4:7], v[24:27], a[144:151], %[idx]\n" \
    "v_max3_u32 v104, v104, v26, v27\n" \
    "v_smfmac_f32_16x16x64_bf16 v[8:11], v[24:27], v[144:151], %[idx]\n" \
    "s_waitcnt lgkmcnt(4)\n" \
    "v_smfmac_f32_16x16x64_bf16 v[12:15], v[24:27], v[96:103], %[idx]\n" \
    "buffer_load_dwordx4 v[64:67], %[vo], %[rw], s46 offen offset:0 sc1\n" \
    "ds_read_b128 v[96:99], %[wl] offset:10240\n" \
    "ds_read_b128 v[100:103], %[wl] offset:11264\n" \
    "s_waitcnt vmcnt(9)\n" \
    "v_smfmac_f32_16x16x64_bf16 v[0:3], v[28:31], a[24:31], %[idx]\n" \
    "v_max3_u32 v104, v104, v28, v29\n" \
    "v_smfmac_f32_16x16x64_bf16 v[4:7], v[28:31], a[152:159], %[idx]\n" \
    "v_max3_u32 v104, v104, v30, v31\n" \
    "v_smfmac_f32_16x16x64_bf16 v[8:11], v[28:31], v[152:159], %[idx]\n" \
    "s_waitcnt lgkmcnt(4)\n" \
    "v_smfmac_f32_16x16x64_bf16 v[12:15], v[28:31], v[80:87], %[idx]\n" \
    "buffer_load_dwordx4 v[68:71], %[vo], %[rw], s46 offen offset:1024 sc1\n" \
    "ds_read_b128 v[80:83], %[wl] offset:12288\n" \
    "ds_read_b128 v[84:87], %[wl] offset:13312\n" \
    "s_waitcnt vmcnt(9)\n" \
    "v_smfmac_f32_16x16x64_bf16 v[0:3], v[32:35], a[32:39], %[idx]\n" \
    "v_max3_u32 v104, v104, v32, v33\n" \
    "v_smfmac_f32_16x16x64_bf16 v[4:7], v[32:35], a[160:167], %[idx]\n" \
    "v_max3_u32 v104, v104, v34, v35\n" \
    "v_smfmac_f32_16x16x64_bf16 v[8:11], v[32:35], v[160:167], %[idx]\n" \
    "s_waitcnt lgkmcnt(4)\n" \
    "v_smfmac_f32_16x16x64_bf16 v[12:15], v[32:35], v[88:95], %[idx]\n" \
    "buffer_load_dwordx4 v[72:75], %[vo], %[rw], s46 offen offset:2048 sc1\n" \
    "ds_read_b128 v[88:91], %[wl] offset:14336\n" \
    "ds_read_b128 v[92:95], %[wl] offset:15360\n" \
    "s_waitcnt vmcnt(9)\n" \
    "v_smfmac_f32_16x16x64_bf16 v[0:3], v[36:39], a[40:47], %[idx]\n" \
    "v_max3_u32 v104, v104, v36, v37\n" \
    "v_smfmac_f32_16x16x64_bf16 v[4:7], v[36:39], a[168:175], %[idx]\n" \
    "v_max3_u32 v104, v104, v38, v39\n" \
    "v_smfmac_f32_16x16x64_bf16 v[8:11], v[36:39], v[168:175], %[idx]\n" \
    "s_waitcnt lgkmcnt(4)\n" \
    "v_smfmac_f32_16x16x64_bf16 v[12:15], v[36:39], v[96:103], %[idx]\n" \
    "buffer_load_dwordx4 v[76:79], %[vo], %[rw], s46 offen offset:3072 sc1\n" \
    "ds_read_b128 v[96:99], %[wl] offset:16384\n" \
    "ds_read_b128 v[100:103], %[wl] offset:17408\n" \
    "s_waitcnt vmcnt(9)\n" \
    "v_smfmac_f32_16x16x64_bf16 v[0:3], v[40:43], a[48:55], %[idx]\n" \
    "v_max3_u32 v104, v104, v40, v41\n" \
    "v_smfmac_f32_16x16x64_bf16 v[4:7], v[40:43], a[176:183], %[idx]\n" \
    "v_max3_u32 v104, v104, v42, v43\n" \
    "v_smfmac_f32_16x16x64_bf16 v[8:11], v[40:43], v[176:183], %[idx]\n" \
    "s_waitcnt lgkmcnt(4)\n" \
    "v_smfmac_f32_16x16x64_bf16 v[12:15], v[40:43], v[80:87], %[idx]\n" \
    "buffer_load_dwordx2 v[106:107], %[bv], %[rw], %[bs] offen sc1\n" \
    "ds_read_b128 v[80:83], %[wl] offset:18432\n" \
    "ds_read_b128 v[84:87], %[wl] offset:19456\n" \
    "s_waitcnt vmcnt(9)\n" \
    "v_smfmac_f32_16x16x64_bf16 v[0:3], v[44:47], a[56:63], %[idx]\n" \
    "v_max3_u32 v104, v104, v44, v45\n" \
    "v_smfmac_f32_16x16x64_bf16 v[4:7], v[44:47], a[184:191], %[idx]\n" \
    "v_max3_u32 v104, v104, v46, v47\n" \
    "v_smfmac_f32_16x16x64_bf16 v[8:11], v[44:47], v[184:191], %[idx]\n" \
    "s_waitcnt lgkmcnt(4)\n" \
    "v_smfmac_f32_16x16x64_bf16 v[12:15], v[44:47], v[88:95], %[idx]\n" \
    "buffer_load_dword v108, %[gv], %[rw], %[gs] offen sc1\n" \
    "ds_read_b128 v[88:91], %[wl] offset:20480\n" \
    "ds_read_b128 v[92:95], %[wl] offset:21504\n" \
    "s_waitcnt vmcnt(9)\n" \
    "v_smfmac_f32_16x16x64_bf16 v[0:3], v[48:51], a[64:71], %[idx]\n" \
    "v_max3_u32 v104, v104, v48, v49\n" \
    "v_smfmac_f32_16x16x64_bf16 v[4:7], v[48:51], a[192:199], %[idx]\n" \
    "v_max3_u32 v104, v104, v50, v51\n" \
    "v_smfmac_f32_16x16x64_bf16 v[8:11], v[48:51], v[192:199], %[idx]\n" \
    "s_waitcnt lgkmcnt(4)\n" \
    "v_smfmac_f32_16x16x64_bf16 v[12:15], v[48:51], v[96:103], %[idx]\n" \
    "buffer_load_dword v105, %[sv], %[rw], %[ss] offen sc1\n" \
    "ds_read_b128 v[96:99], %[wl] offset:22528\n" \
    "ds_read_b128 v[100:103], %[wl] offset:23552\n" \
    "s_waitcnt vmcnt(9)\n" \
    "v_smfmac_f32_16x16x64_bf16 v[0:3], v[52:55], a[72:79], %[idx]\n" \
    "v_max3_u32 v104, v104, v52, v53\n" \
    "v_smfmac_f32_16x16x64_bf16 v[4:7], v[52:55], a[200:207], %[idx]\n" \
    "v_max3_u32 v104, v104, v54, v55\n" \
    "v_smfmac_f32_16x16x64_bf16 v[8:11], v[52:55], v[200:207], %[idx]\n" \
    "s_waitcnt lgkmcnt(4)\n" \
    "v_smfmac_f32_16x16x64_bf16 v[12:15], v[52:55], v[80:87], %[idx]\n" \
    "ds_read_b128 v[80:83], %[wl] offset:24576\n" \
    "ds_read_b128 v[84:87], %[wl] offset:25600\n" \
    "s_waitcnt vmcnt(8)\n" \
    "v_smfmac_f32_16x16x64_bf16 v[0:3], v[56:59], a[80:87], %[idx]\n" \
    "v_max3_u32 v104, v104, v56, v57\n" \
    "v_smfmac_f32_16x16x64_bf16 v[4:7], v[56:59], a[208:215], %[idx]\n" \
    "v_max3_u32 v104, v104, v58, v59\n" \
    "v_smfmac_f32_16x16x64_bf16 v[8:11], v[56:59], v[208:215], %[idx]\n" \
    "s_waitcnt lgkmcnt(4)\n" \
    "v_smfmac_f32_16x16x64_bf16 v[12:15], v[56:59], v[88:95], %[idx]\n" \
    "ds_read_b128 v[88:91], %[wl] offset:26624\n" \
    "ds_read_b128 v[92:95], %[wl] offset:27648\n" \
    "s_waitcnt vmcnt(7)\n" \
    "v_smfmac_f32_16x16x64_bf16 v[0:3], v[60:63], a[88:95], %[idx]\n" \
    "v_max3_u32 v104, v104, v60, v61\n" \
    "v_smfmac_f32_16x16x64_bf16 v[4:7], v[60:63], a[216:223], %[idx]\n" \
    "v_max3_u32 v104, v104, v62, v63\n" \
    "v_smfmac_f32_16x16x64_bf16 v[8:11], v[60:63], v[216:223], %[idx]\n" \
    "s_waitcnt lgkmcnt(4)\n" \
    "v_smfmac_f32_16x16x64_bf16 v[12:15], v[60:63], v[96:103], %[idx]\n" \
    "ds_read_b128 v[96:99], %[wl] offset:28672\n" \
    "ds_read_b128 v[100:103], %[wl] offset:29696\n" \
    "s_waitcnt vmcnt(6)\n" \
    "v_smfmac_f32_16x16x64_bf16 v[0:3], v[64:67], a[96:103], %[idx]\n" \
    "v_max3_u32 v104, v104, v64, v65\n" \
    "v_smfmac_f32_16x16x64_bf16 v[4:7], v[64:67], a[224:231], %[idx]\n" \
    "v_max3_u32 v104, v104, v66, v67\n" \
    "v_smfmac_f32_16x16x64_bf16 v[8:11], v[64:67], v[224:231], %[idx]\n" \
    "s_waitcnt lgkmcnt(4)\n" \
    "v_smfmac_f32_16x16x64_bf16 v[12:15], v[64:67], v[80:87], %[idx]\n" \
    "ds_read_b128 v[80:83], %[wl] offset:30720\n" \
    "ds_read_b128 v[84:87], %[wl] offset:31744\n" \
    "s_waitcnt vmcnt(5)\n" \
    "v_smfmac_f32_16x16x64_bf16 v[0:3], v[68:71], a[104:111], %[idx]\n" \
    "v_max3_u32 v104, v104, v68, v69\n" \
    "v_smfmac_f32_16x16x64_bf16 v[4:7], v[68:71], a[232:239], %[idx]\n" \
    "v_max3_u32 v104, v104, v70, v71\n" \
    "v_smfmac_f32_16x16x64_bf16 v[8:11], v[68:71], v[232:239], %[idx]\n" \
    "s_waitcnt lgkmcnt(4)\n" \
    "v_smfmac_f32_16x16x64_bf16 v[12:15], v[68:71], v[88:95], %[idx]\n" \
    "s_waitcnt vmcnt(4)\n" \
    "v_smfmac_f32_16x16x64_bf16 v[0:3], v[72:75], a[112:119], %[idx]\n" \
    "v_max3_u32 v104, v104, v72, v73\n" \
    "v_smfmac_f32_16x16x64_bf16 v[4:7], v[72:75], a[240:247], %[idx]\n" \
    "v_max3_u32 v104, v104, v74, v75\n" \
    "v_smfmac_f32_16x16x64_bf16 v[8:11], v[72:75], v[240:247], %[idx]\n" \
    "s_waitcnt lgkmcnt(2)\n" \
    "v_smfmac_f32_16x16x64_bf16 v[12:15], v[72:75], v[96:103], %[idx]\n" \
    "s_waitcnt vmcnt(3)\n" \
    "v_smfmac_f32_16x16x64_bf16 v[0:3], v[76:79], a[120:127], %[idx]\n" \
    "v_max3_u32 v104, v104, v76, v77\n" \
    "v_smfmac_f32_16x16x64_bf16 v[4:7], v[76:79], a[248:255], %[idx]\n" \
    "v_max3_u32 v104, v104, v78, v79\n" \
    "v_smfmac_f32_16x16x64_bf16 v[8:11], v[76:79], v[248:255], %[idx]\n" \
    "s_waitcnt lgkmcnt(0)\n" \
    "v_smfmac_f32_16x16x64_bf16 v[12:15], v[76:79], v[80:87], %[idx]\n" \
    "s_waitcnt vmcnt(0)\n" \
    "s_nop 5\n" \
    "v_permlane32_swap_b32 v0, v8\n" \
    "v_add_f32 v0, v0, v8\n" \
    "v_permlane32_swap_b32 v1, v9\n" \
    "v_add_f32 v1, v1, v9\n" \
    "v_permlane32_swap_b32 v2, v10\n" \
    "v_add_f32 v2, v2, v10\n" \
    "v_permlane32_swap_b32 v3, v11\n" \
    "v_add_f32 v3, v3, v11\n" \
    "v_permlane32_swap_b32 v4, v12\n" \
    "v_add_f32 v4, v4, v12\n" \
    "v_permlane32_swap_b32 v5, v13\n" \
    "v_add_f32 v5, v5, v13\n" \
    "v_permlane32_swap_b32 v6, v14\n" \
    "v_add_f32 v6, v6, v14\n" \
    "v_permlane32_swap_b32 v7, v15\n" \
    "v_add_f32 v7, v7, v15\n" \
    ""
#define OUT_CLOBBER "v16", "v17", "v18", "v19", "v20", "v21", "v22", "v23", "v24", "v25", "v26", "v27", "v28", "v29", "v30", "v31", "v32", "v33", "v34", "v35", "v36", "v37", "v38", "v39", "v40", "v41", "v42", "v43", "v44", "v45", "v46", "v47", "v48", "v49", "v50", "v51", "v52", "v53", "v54", "v55", "v56", "v57", "v58", "v59", "v60", "v61", "v62", "v63", "v64", "v65", "v66", "v67", "v68", "v69", "v70", "v71", "v72", "v73", "v74", "v75", "v76", "v77", "v78", "v79", "v80", "v81", "v82", "v83", "v84", "v85", "v86", "v87", "v88", "v89", "v90", "v91", "v92", "v93", "v94", "v95", "v96", "v97", "v98", "v99", "v100", "v101", "v102", "v103", "s44", "s45", "s46", "scc", "memory"
constexpr int NREP = 2, NCP = 4;
#define OWNER_ASM \
    "s_getpc_b64 s[96:97]\n" \
    "14:\n" \
    "s_add_u32 s96, s96, 2f-14b\n" \
    "s_addc_u32 s97, s97, 0\n" \
    "s_sub_u32 s68, s96, 2f-11f\n" \
    "s_subb_u32 s69, s97, 0\n" \
    "s_mov_b32 %[np], 0\n" \
    "s_add_i32 s72, %[pb], 0x10000\n" \
    "13:\n" \
    "s_memtime vcc\n" \
    "s_waitcnt lgkmcnt(0)\n" \
    "s_sub_i32 s70, %[lk], vcc_lo\n" \
    "s_cmp_gt_i32 s70, %[rd]\n" \
    "s_cbranch_scc1 13b\n" \
    "s_add_i32 s70, %[pq], 0x10000\n" \
    "buffer_store_dwordx4 %[sv], %[ro], %[rs], %[pq] offen offset:0 sc1\n" \
    "buffer_store_dwordx4 %[sv], %[ro], %[rs], %[pq] offen offset:1024 sc1\n" \
    "buffer_store_dwordx4 %[sv], %[ro], %[rs], s70 offen offset:0 sc1\n" \
    "buffer_store_dwordx4 %[sv], %[ro], %[rs], s70 offen offset:1024 sc1\n" \
    "v_mov_b32 v2, -1\n" \
    "buffer_store_dword v2, %[rm], %[rs], %[mq] offen sc1\n" \
    "buffer_store_dwordx4 %[sv], %[rt], %[rw], %[tq] offen sc1\n" \
    "s_mov_b32 s71, 4\n" \
    "10:\n" \
    "s_memtime vcc\n" \
    "s_waitcnt lgkmcnt(0)\n" \
    "s_sub_i32 s70, %[lk], vcc_lo\n" \
    "s_cmp_gt_i32 s70, 112\n" \
    "s_cbranch_scc1 10b\n" \
    "s_max_i32 s70, s70, 0\n" \
    "s_memtime s[58:59]\n" \
    "s_memrealtime s[44:45]\n" \
    "s_nop 1\n" \
    "s_memrealtime s[46:47]\n" \
    "s_nop 1\n" \
    "s_memrealtime s[48:49]\n" \
    "s_nop 1\n" \
    "s_memrealtime s[50:51]\n" \
    "s_nop 1\n" \
    "s_memrealtime s[52:53]\n" \
    "s_nop 1\n" \
    "s_memrealtime s[54:55]\n" \
    "s_nop 1\n" \
    "s_memrealtime s[56:57]\n" \
    "s_nop 1\n" \
    "s_memrealtime vcc\n" \
    "s_waitcnt lgkmcnt(0)\n" \
    "s_add_i32 s44, s44, s46\n" \
    "s_add_i32 s48, s48, s50\n" \
    "s_add_i32 s52, s52, s54\n" \
    "s_add_i32 s44, s44, s48\n" \
    "s_add_i32 s52, s52, s56\n" \
    "s_add_i32 s44, s44, s52\n" \
    "s_add_i32 s59, s44, vcc_lo\n" \
    "s_sub_i32 s44, s59, %[oT]\n" \
    "s_mov_b32 %[oT], s59\n" \
    "s_sub_i32 s45, s58, %[oC]\n" \
    "s_mov_b32 %[oC], s58\n" \
    "s_mul_i32 s46, s44, %[oR]\n" \
    "s_lshr_b32 s46, s46, 12\n" \
    "s_sub_i32 s46, s45, s46\n" \
    "s_sub_i32 s44, s44, 1000\n" \
    "s_cmp_lt_u32 s44, 2000\n" \
    "s_cselect_b32 s46, s46, 0\n" \
    "s_add_i32 %[oR], %[oR], s46\n" \
    "s_sub_i32 s45, %[oT], %[c8]\n" \
    "s_and_b32 s70, s70, -4\n" \
    "s_sub_u32 vcc_lo, s68, s70\n" \
    "s_subb_u32 vcc_hi, s69, 0\n" \
    "s_setpc_b64 vcc\n" \
    "s_nop 0\n" \
    "s_nop 0\n" \
    "s_nop 0\n" \
    "s_nop 0\n" \
    "s_nop 0\n" \
    "s_nop 0\n" \
    "s_nop 0\n" \
    "s_nop 0\n" \
    "s_nop 0\n" \
    "s_nop 0\n" \
    "s_nop 0\n" \
    "s_nop 0\n" \
    "s_nop 0\n" \
    "s_nop 0\n" \
    "s_nop 0\n" \
    "s_nop 0\n" \
    "s_nop 0\n" \
    "s_nop 0\n" \
    "s_nop 0\n" \
    "s_nop 0\n" \
    "s_nop 0\n" \
    "s_nop 0\n" \
    "s_nop 0\n" \
    "s_nop 0\n" \
    "s_nop 0\n" \
    "s_nop 0\n" \
    "s_nop 0\n" \
    "s_nop 0\n" \
    "11:\n" \
    "buffer_load_dwordx2 v[0:1], %[ca], %[rs], %[cs] offen sc1\n" \
    "s_waitcnt vmcnt(0)\n" \
    "6:\n" \
    "s_memtime s[46:47]\n" \
    "v_subrev_u32 v0, s45, v0\n" \
    "v_cmp_ne_u16 vcc, %[tag], v1\n" \
    "s_add_i32 s48, %[oC], -136\n" \
    "v_max_i32_dpp v0, v0, v0 row_shr:1 row_mask:0xf bank_mask:0xf\n" \
    "v_max_u32_dpp v1, v1, v1 row_shr:1 row_mask:0xf bank_mask:0xf\n" \
    "s_cbranch_vccnz 7f\n" \
    "8:\n" \
    "v_max_i32_dpp v0, v0, v0 row_shr:2 row_mask:0xf bank_mask:0xf\n" \
    "v_max_u32_dpp v1, v1, v1 row_shr:2 row_mask:0xf bank_mask:0xf\n" \
    "s_nop 0\n" \
    "v_max_i32_dpp v0, v0, v0 row_shr:4 row_mask:0xf bank_mask:0xf\n" \
    "v_max_u32_dpp v1, v1, v1 row_shr:4 row_mask:0xf bank_mask:0xf\n" \
    "s_nop 0\n" \
    "v_max_i32_dpp v0, v0, v0 row_shr:8 row_mask:0xf bank_mask:0xf\n" \
    "v_max_u32_dpp v1, v1, v1 row_shr:8 row_mask:0xf bank_mask:0xf\n" \
    "s_waitcnt lgkmcnt(0)\n" \
    "v_max_i32_dpp v0, v0, v0 row_bcast:15 row_mask:0xa bank_mask:0xf\n" \
    "v_max_u32_dpp v1, v1, v1 row_bcast:15 row_mask:0xa bank_mask:0xf\n" \
    "s_add_i32 s46, s46, 44\n" \
    "v_max_i32_dpp v0, v0, v0 row_bcast:31 row_mask:0xc bank_mask:0xf\n" \
    "v_max_u32_dpp v1, v1, v1 row_bcast:31 row_mask:0xc bank_mask:0xf\n" \
    "v_readlane_b32 s44, v0, 63\n" \
    "v_readlane_b32 s49, v1, 63\n" \
    "s_mul_i32 s47, s44, %[oR]\n" \
    "s_ashr_i32 s47, s47, 12\n" \
    "s_add_i32 s47, s47, s48\n" \
    "s_sub_i32 s58, s47, s46\n" \
    "s_cmp_gt_i32 s58, 112\n" \
    "s_cbranch_scc1 1f\n" \
    "12:\n" \
    "s_max_i32 s58, s58, 0\n" \
    "s_and_b32 s58, s58, -4\n" \
    "s_sub_u32 vcc_lo, s96, s58\n" \
    "s_subb_u32 vcc_hi, s97, 0\n" \
    "s_setpc_b64 vcc\n" \
    "s_nop 0\n" \
    "s_nop 0\n" \
    "s_nop 0\n" \
    "s_nop 0\n" \
    "s_nop 0\n" \
    "s_nop 0\n" \
    "s_nop 0\n" \
    "s_nop 0\n" \
    "s_nop 0\n" \
    "s_nop 0\n" \
    "s_nop 0\n" \
    "s_nop 0\n" \
    "s_nop 0\n" \
    "s_nop 0\n" \
    "s_nop 0\n" \
    "s_nop 0\n" \
    "s_nop 0\n" \
    "s_nop 0\n" \
    "s_nop 0\n" \
    "s_nop 0\n" \
    "s_nop 0\n" \
    "s_nop 0\n" \
    "s_nop 0\n" \
    "s_nop 0\n" \
    "s_nop 0\n" \
    "s_nop 0\n" \
    "s_nop 0\n" \
    "s_nop 0\n" \
    "2:\n" \
    "buffer_load_dwordx4 v[0:3], %[o], %[rs], %[pb] offen offset:0 sc1\n" \
    "buffer_load_dwordx4 v[4:7], %[o], %[rs], %[pb] offen offset:1024 sc1\n" \
    "buffer_load_dwordx4 v[8:11], %[o], %[rs], s72 offen offset:0 sc1\n" \
    "buffer_load_dwordx4 v[12:15], %[o], %[rs], s72 offen offset:1024 sc1\n" \
    "s_add_i32 %[np], %[np], 1\n" \
    "s_add_i32 %[tm], s44, s45\n" \
    "s_lshr_b32 s50, s49, 16\n" \
    "s_add_i32 s50, s50, s44\n" \
    "s_add_i32 s50, s50, %[cw]\n" \
    "s_mul_i32 s50, s50, %[oR]\n" \
    "s_ashr_i32 s50, s50, 12\n" \
    "s_add_i32 %[lk], s50, %[oC]\n" \
    "s_waitcnt vmcnt(3)\n" \
    "v_mfma_f32_16x16x32_bf16 v[16:19], %[sel], v[0:3], 0\n" \
    "v_max3_u32 v20, v0, v1, v1\n" \
    "v_max3_u32 v20, v2, v3, v20\n" \
    "s_waitcnt vmcnt(2)\n" \
    "v_mfma_f32_16x16x32_bf16 v[16:19], %[sel], v[4:7], v[16:19]\n" \
    "v_max3_u32 v20, v4, v5, v20\n" \
    "v_max3_u32 v20, v6, v7, v20\n" \
    "s_waitcnt vmcnt(1)\n" \
    "v_mfma_f32_16x16x32_bf16 v[16:19], %[sel], v[8:11], v[16:19]\n" \
    "v_max3_u32 v20, v8, v9, v20\n" \
    "v_max3_u32 v20, v10, v11, v20\n" \
    "s_waitcnt vmcnt(0)\n" \
    "v_mfma_f32_16x16x32_bf16 v[16:19], %[sel], v[12:15], v[16:19]\n" \
    "v_max3_u32 v20, v12, v13, v20\n" \
    "v_max3_u32 v20, v14, v15, v20\n" \
    "v_cmp_eq_u32 vcc, -1, v20\n" \
    "s_nop 3\n" \
    "s_cbranch_vccnz 4f\n" \
    "3:\n" \
    "v_add_f32_dpp v16, v16, v16 row_ror:8 row_mask:0xf bank_mask:0xf\n" \
    "v_add_f32_dpp v17, v17, v17 row_ror:8 row_mask:0xf bank_mask:0xf\n" \
    "v_cvt_pk_bf16_f32 v16, v16, v17\n" \
    "buffer_store_dword v16, %[so], %[rs], %[mb0] offen sc1\n" \
    "s_branch 5f\n" \
    "1:\n" \
    "s_memtime vcc\n" \
    "s_waitcnt lgkmcnt(0)\n" \
    "s_sub_i32 s58, s47, vcc_lo\n" \
    "s_cmp_gt_i32 s58, 112\n" \
    "s_cbranch_scc1 1b\n" \
    "s_branch 12b\n" \
    "4:\n" \
    "s_add_i32 %[bud], %[bud], -1\n" \
    "s_cmp_lt_i32 %[bud], 0\n" \
    "s_cbranch_scc0 2b\n" \
    "s_branch 3b\n" \
    "7:\n" \
    "s_bcnt1_i32_b64 s44, vcc\n" \
    "s_cmp_le_u32 s44, s71\n" \
    "s_mov_b32 s71, 0\n" \
    "s_cbranch_scc1 15f\n" \
    "s_cmp_eq_u32 %[tag], 1\n" \
    "s_cbranch_scc1 9f\n" \
    "s_sleep 15\n" \
    "9:\n" \
    "s_sleep 6\n" \
    "15:\n" \
    "s_add_i32 %[bud], %[bud], -1\n" \
    "s_cmp_lt_i32 %[bud], 0\n" \
    "s_cbranch_scc1 8b\n" \
    "buffer_load_dwordx2 v[0:1], %[ca], %[rs], %[cs] offen sc1\n" \
    "s_memtime s[58:59]\n" \
    "s_memrealtime s[44:45]\n" \
    "s_nop 1\n" \
    "s_memrealtime s[46:47]\n" \
    "s_nop 1\n" \
    "s_memrealtime s[48:49]\n" \
    "s_nop 1\n" \
    "s_memrealtime s[50:51]\n" \
    "s_nop 1\n" \
    "s_memrealtime s[52:53]\n" \
    "s_nop 1\n" \
    "s_memrealtime s[54:55]\n" \
    "s_nop 1\n" \
    "s_memrealtime s[56:57]\n" \
    "s_nop 1\n" \
    "s_memrealtime vcc\n" \
    "s_waitcnt lgkmcnt(0)\n" \
    "s_add_i32 s44, s44, s46\n" \
    "s_add_i32 s48, s48, s50\n" \
    "s_add_i32 s52, s52, s54\n" \
    "s_add_i32 s44, s44, s48\n" \
    "s_add_i32 s52, s52, s56\n" \
    "s_add_i32 s44, s44, s52\n" \
    "s_add_i32 s59, s44, vcc_lo\n" \
    "s_sub_i32 s44, s59, %[oT]\n" \
    "s_mov_b32 %[oT], s59\n" \
    "s_sub_i32 s45, s58, %[oC]\n" \
    "s_mov_b32 %[oC], s58\n" \
    "s_mul_i32 s46, s44, %[oR]\n" \
    "s_lshr_b32 s46, s46, 12\n" \
    "s_sub_i32 s46, s45, s46\n" \
    "s_sub_i32 s44, s44, 1000\n" \
    "s_cmp_lt_u32 s44, 2000\n" \
    "s_cselect_b32 s46, s46, 0\n" \
    "s_add_i32 %[oR], %[oR], s46\n" \
    "s_sub_i32 s45, %[oT], %[c8]\n" \
    "s_waitcnt vmcnt(0)\n" \
    "s_branch 6b\n" \
    "5:\n" \
    ""
#define OWNER_CLOBBER "v0", "v1", "v2", "v3", "v4", "v5", "v6", "v7", "v8", "v9", "v10", "v11", "v12", "v13", "v14", "v15", "v16", "v17", "v18", "v19", "v20", "s44", "s45", "s46", "s47", "s48", "s49", "s50", "s51", "s52", "s53", "s54", "s55", "s56", "s57", "s58", "s59", "s68", "s69", "s70", "s71", "s72", "s96", "s97", "vcc", "scc", "memory"
constexpr int O_W = 0;
constexpr int O_STG = 131072;
constexpr int O_B = O_STG + 20480;
constexpr int OUT_REP = 7;
#define OUT_AUX 16
template <int O, class V> DI void put4(V& d, u32x4 f) {
#pragma unroll
    for (int j = 0; j < 4; ++j) d[O + j] = f[j];
}
DI void role_outp(const Args& a, LAS char* lds, unsigned lds_base, int nb, int rep, rsrc_t rw, int& budget) {
    const int tid = threadIdx.x, lane = tid & 63, w = __builtin_amdgcn_readfirstlane(tid >> 6);
    WG_BARRIER();
    const int n = lane & 15, g = lane >> 4;
    const int wl = O_W + w * 32768 + lane * 16;
    u32x32 A[8], V[4];
    {
        const int stg = O_STG + w * 5120;
        const rsrc_t ro = __builtin_amdgcn_make_buffer_rsrc((void*)a.Wo, 0, HD * DM * 2, 0x00020000);
        const int so = (1024 * w + (lane >> 3)) * (DM * 2) + nb * 128 + (lane & 7) * 16, sw = stg + (lane >> 3) * 160 + (lane & 7) * 16;
        const int ra = stg + (16 * (g & 1) + 4 * (g >> 1) + (n >> 2)) * 160 + (n & 3) * 8;
        constexpr int PB = 4;
        u32x4 b[PB][4];
        auto fetch = [&](int ks) {
#pragma unroll
            for (int i = 0; i < 4; ++i) b[ks % PB][i] = __builtin_amdgcn_raw_buffer_load_b128(ro, so, (64 * (ks >> 1) + 16 * (ks & 1) + 8 * (i & 1) + 32 * (i >> 1)) * (DM * 2), 0);
        };
        unsigned tp = now();
        const int pc = a.d1 >> 16;
#pragma unroll
        for (int ks = 0; ks < PB; ++ks) { fetch(ks); FENCE(); tp += pc; while ((int)(now() - tp) < 0) {} }
        FENCE();
        static_for<32>([&](auto ic) {
            constexpr int ks = decltype(ic)::value;
#pragma unroll
            for (int i = 0; i < 4; ++i) *(LAS u32x4*)(lds + sw + i * 1280) = b[ks % PB][i];
            if (ks + PB < 32) { fetch(ks + PB); FENCE(); tp += pc; }
            u32x4 f[4];
#pragma unroll
            for (int t = 0; t < 4; ++t) { const u32x2 lo = tr_read(lds, ra + t * 32), hi = tr_read(lds, ra + t * 32 + 1280); f[t] = (u32x4){lo.x, lo.y, hi.x, hi.y}; }
            put4<4 * (ks & 7)>(A[ks >> 3], f[0]); put4<4 * (ks & 7)>(A[4 + (ks >> 3)], f[1]); put4<4 * (ks & 7)>(V[ks >> 3], f[2]);
            *(LAS u32x4*)(lds + wl + ks * 1024) = f[3];
            if constexpr (ks == 7) asm volatile("" : "+{a[0:31]}"(A[0]), "+{a[128:159]}"(A[4]), "+{v[128:159]}"(V[0]));
            if constexpr (ks == 15) asm volatile("" : "+{a[32:63]}"(A[1]), "+{a[160:191]}"(A[5]), "+{v[160:191]}"(V[1]));
            if constexpr (ks == 23) asm volatile("" : "+{a[64:95]}"(A[2]), "+{a[192:223]}"(A[6]), "+{v[192:223]}"(V[2]));
            if constexpr (ks == 31) asm volatile("" : "+{a[96:127]}"(A[3]), "+{a[224:255]}"(A[7]), "+{v[224:255]}"(V[3]));
            FENCE();
            if (ks + PB < 31) while ((int)(now() - tp) < 0) {}
        });
    }
    wait_prog(rw, F_S4 + lane * 4, true, 1, budget);
    const rsrc_t ru = __builtin_amdgcn_make_buffer_rsrc(a.out, 0, SEQ * DM * 2, 0x00020000);
    constexpr int SP = 2 * LATE - 2;
    auto first = [&](int u) { return u >= SP ? 8 * u + 8 * SP : 16 * u; };
    auto st_s = [&](int u) { return (u >= 2 * FAST ? F_H + ((first(u) >> 4) - 2 * FAST) * 4096 : F_O + (u & 3) * 4) + w * 1024; };
    auto bn_s = [&](int u) { return OFF_BO + (64 * nb + 16 * w) * BO_ROW + first(u) * 2; };
    auto gn_s = [&](int u) { return OFF_GT + (first(u) + 4 * w) * 4; };
    int st = -1;
    u32x2 bn = ld64(rw, (lane >> 2) * BO_ROW + (lane & 3) * 8, bn_s(rep));
    float gn = __uint_as_float(ld32(rw, g * 4, gn_s(rep)));
    __builtin_amdgcn_s_waitcnt(0);
#define OUT_OPS "=&{v[0:15]}"(acc), "=&{v104}"(top), "=&{v105}"(st), "=&{v[106:107]}"(bn), "=&{v108}"(gn) \
        : "{a[0:31]}"(A[0]), "{a[32:63]}"(A[1]), "{a[64:95]}"(A[2]), "{a[96:127]}"(A[3]), "{a[128:159]}"(A[4]), "{a[160:191]}"(A[5]), "{a[192:223]}"(A[6]), "{a[224:255]}"(A[7]), \
          "{v[128:159]}"(V[0]), "{v[160:191]}"(V[1]), "{v[192:223]}"(V[2]), "{v[224:255]}"(V[3]), [rw] "s"(rw), [so] "s"(t0 * 8192), [wl] "v"(lds_base + O_W + w * 32768 + lane * 16), \
          [tv] "v"(tv), [tm] "s"(tm), [vo] "v"(vo), [sv] "v"(sv), [bv] "v"((lane >> 2) * BO_ROW + (lane & 3) * 8), [gv] "v"(g * 4), \
          [ss] "s"(st_s(u + OUT_REP)), [bs] "s"(bn_s(u + OUT_REP)), [gs] "s"(gn_s(u + OUT_REP))
    for (int u = rep; u < SP + 4; u += OUT_REP) {
        const bool fast = u >= 2 * FAST, sp = u >= SP;
        const int t0 = first(u);
        WG_BARRIER();
        int lane = opaque_lane(), n = lane & 15, g = lane >> 4;
        if (u == 3 * OUT_REP && nb == 0) __builtin_amdgcn_raw_buffer_store_b64(launch_tag(a), rw, w + lane == 0 ? F_EPOCH : OOB, 0, 16);
        S4S(0)
        TT(u >= 119, 0)
        *(LAS u32x2*)(lds + O_B + (16 * w + (lane >> 2)) * 40 + (lane & 3) * 8) = bn;
        const float gt = gn;
        const int tv = OFF_OF + w * 16384 + nb * 2048 + g * 65536 + n * 128, tm = __builtin_amdgcn_readfirstlane(nb < 8 ? sp ? 0xFFFF : -1 : 0);
        const int vo = OFF_OF + w * 16384 + (n & 7) * 16 + (sp ? (n >> 3) * 512 + g * 128 : (n >> 3) * 65536 + (g & 1) * 512 + (g >> 1) * 128);
        const int thr = fast ? 0 : u >> 1, sv = (lane & 7) * 128;
        if (!__all(st >= thr)) do { __builtin_amdgcn_s_sleep(1); st = (int)ld32(rw, sv, st_s(u)); asm volatile("" ::: "memory"); } while (!__all(st >= thr) && --budget >= 0);
        if (fast) { const unsigned t = now(); (void)wait_until(t + wave_max32(min((int)(2 * (st - t)) >> 1, 500))); }
        S4S(1)
        TT(u >= 119, 1)
        f32x16 acc; unsigned top;
        if (sp) {
            do asm volatile(OUT8_ASM : OUT_OPS, [idx] "v"(n & 8 ? 0xEEEE : 0x4444) : OUT_CLOBBER); while (__any(top == SENT) && --budget >= 0);
            TT(u >= 119, 2)
            lane = opaque_lane(); n = lane & 15; g = lane >> 4;
#pragma unroll
            for (int i = 0; i < 8; ++i) *(LAS float*)(lds + O_STG + (w * 16 + 4 * (g & 1) + (i & 3)) * 272 + (g >> 1) * 128 + (i >> 2) * 64 + n * 4) = acc[i];
        } else {
            do asm volatile(OUT16_ASM : OUT_OPS : OUT_CLOBBER); while (__any(top == SENT) && --budget >= 0);
            S4S(2)
            TT(u >= 119, 2)
            lane = opaque_lane(); n = lane & 15; g = lane >> 4;
#pragma unroll
            for (int t = 0; t < 4; ++t) *(LAS f32x4*)(lds + O_STG + (w * 16 + n) * 272 + t * 64 + g * 16) = (f32x4){acc[4 * t], acc[4 * t + 1], acc[4 * t + 2], acc[4 * t + 3]};
        }
        WG_BARRIER();
        S4S(3)
        const int tok = 4 * w + g, c4 = 4 * n;
        f32x4 r[4]; u16 bv[4];
#pragma unroll
        for (int ww = 0; ww < 4; ++ww) { r[ww] = *(LAS f32x4*)(lds + O_STG + (ww * 16 + tok) * 272 + c4 * 4); bv[ww] = *(LAS u16*)(lds + O_B + (c4 + ww) * 40 + tok * 2); }
        FENCE();
        const f32x4 v = gt * ((r[0] + r[1]) + (r[2] + r[3])) + (1.0f - gt) * (f32x4){bf2f(bv[0]), bf2f(bv[1]), bf2f(bv[2]), bf2f(bv[3])};
        __builtin_amdgcn_raw_buffer_store_b64((u32x2){pack2(v[0], v[1]), pack2(v[2], v[3])}, ru, sp && tok >= 8 ? OOB : (tok * DM + 64 * nb + c4) * 2, t0 * (DM * 2), OUT_AUX);
        S4S(4)
        TT(u >= 119, 3)
        TT(u >= 105 && u < 112, 4)
    }
}

constexpr int L_MEMS = 0;
constexpr int L_Q = 9216;
constexpr int L_KO = L_Q + 2560;
constexpr int L_KX = 16384;
constexpr int L_W = 32768;
constexpr int C_RING = 8;
static_assert(S_TOTAL <= L_SINK && L_W + C_RING * 16384 <= L_TOTAL && L_KO + 8 * 576 <= L_KX, "LDS budget");
static_assert(P_STG + P_RING * 16384 <= L_TOTAL, "LDS budget");

DI int pos2col(int p) { return p < 512 ? (p & ~63) + 16 * (p & 3) + ((p & 63) >> 2) : p; }
DI void unit_rc(int u, int& i, int& p0) {
    if (u < 512) { i = 2 * ((u >> 2) & 3) + (u & 1); p0 = (u & ~15) + 4 * (u & 2); }
    else { const int v = u - 512; i = (v >> 1) & 7; p0 = 512 + 16 * (v >> 4) + 8 * (v & 1); }
}
DI void mem0_arrange(LAS char* lds, int raw, int tid) {
#pragma unroll
    for (int k = 0; k < 2; ++k) {
        int i, p0; unit_rc(tid + 256 * k, i, p0);
        unsigned r[4];
#pragma unroll
        for (int j = 0; j < 4; ++j) r[j] = *(LAS unsigned*)(lds + raw + (i * DM + pos2col(p0)) * 2 + 32 * j);
        *(LAS u32x4*)(lds + L_MEMS + (tid + 256 * k) * 16) = (u32x4){__builtin_amdgcn_perm(r[1], r[0], 0x05040100), __builtin_amdgcn_perm(r[3], r[2], 0x05040100),
                                                                     __builtin_amdgcn_perm(r[1], r[0], 0x07060302), __builtin_amdgcn_perm(r[3], r[2], 0x07060302)};
    }
    int i, p0; unit_rc(512 + (tid & 63), i, p0);
    *(LAS u32x4*)(lds + L_MEMS + 8192 + (tid & 63) * 16) = *(LAS u32x4*)(lds + raw + (i * DM + p0) * 2);
}
DI unsigned read_canaries(int s, rsrc_t rs, int lane, int& budget) {
    const int o = 2 * OFF_CAN + (s & 3) * 16384 + (lane >> 2) * 512 + (lane & 3) * 16;
    u32x2 v;
    for (;;) {
        v = ld64(rs, o, 0);
        asm volatile("" ::: "memory");
        if (__all((v.y & 0xffff) == (unsigned)(s + 1))) break;
        if (--budget < 0) break;
    }
    const unsigned t = 8 * now();
    return t + (unsigned)wave_max32(min(max((int)(v.x - t), -0x4000), 0x4000));
}
DI u32x8 sm_frag(LAS char* lds, int sl, int r0, int sa, int sH, int cb, int lo8) {
    auto rd = [&](int r) { return tr_read(lds, sl + r * 256 + ((cb ^ (2 * swz(r))) * 16) + lo8); };
    const u32x2 a = rd(r0), b = rd(r0 + sa), c = rd(r0 + sH), d = rd(r0 + sa + sH);
    return (u32x8){a.x, a.y, b.x, b.y, c.x, c.y, d.x, d.y};
}
#define GS(slot, i)
#define GV(slot, i, v)
DI int tp_unit(int U, int sel) { return OFF_TP + cmap((U & 511) * 4096) + ((sel >> (4 + (U >> 8 & 1)) ^ U >> 9) & 1) * 8192; }
template <int N> DI void finish(const Args& a, rsrc_t rw, int U, int sel, unsigned t, int& budget) {
    const int lane = opaque_lane(), n = lane & 7, g = lane >> 4;
    const rsrc_t ru = __builtin_amdgcn_make_buffer_rsrc(a.out, 0, SEQ * DM * 2, 0x00020000);
    constexpr int M = (N + 1) / 2;
    int at[M]; f32x2 bv[M]; float gt[M];
#pragma unroll
    for (int i = 0; i < M; ++i) {
        const int u = U + 2 * i + (lane >> 3 & 1 & (N - 1));
        const int tok = LATE * SEG + (U < 512 ? 2 * (u & 15) + (n >> 2) : (u & 32) + 4 * (u & 7) + (n >> 1));
        const int col = U < 512 ? 32 * (u >> 4) + 16 * (g >> 1) + 4 * (n & 3) + 2 * (g & 1) : 512 + 2 * (u & 24) + 8 * (n & 1) + 2 * g;
#pragma unroll
        for (int r = 0; r < 2; ++r) bv[i][r] = bf2f(__builtin_amdgcn_raw_buffer_load_b16(rw, col * BO_ROW + tok * 2, OFF_BO + r * BO_ROW, 16));
        gt[i] = __uint_as_float(ld32(rw, tok * 4, OFF_GT));
        at[i] = (tok * DM + col) * 2;
    }
    u32x4 one;
#pragma unroll
    for (int j = 0; j < 4; ++j) one[j] = (lane >> 2 & 3) == j ? 0x3F80u << 16 * (lane & 1) : 0u;
#pragma unroll
    for (int i = 0; i < M; ++i) { bv[i] *= 1.0f - gt[i]; asm volatile("" : "+v"(bv[i]), "+v"(one)); }
    (void)wait_until(t);
    f32x4 v[N];
    for (;;) {
        u32x4 d[4 * N];
#pragma unroll
        for (int k = 0; k < 4 * N; ++k) d[k] = ld128(rw, lane * 16 + (k & 3) * 1024, tp_unit(U + (k >> 2), sel));
        unsigned top = 0;
#pragma unroll
        for (int i = 0; i < N; ++i) {
            asm volatile(M16 "%0, %1, %2, 0\n\t" M16 "%0, %1, %3, %0\n\t" M16 "%0, %1, %4, %0\n\t" M16 "%0, %1, %5, %0\n\ts_nop 7" : "=&v"(v[i]) : "v"(one), "v"(d[4 * i]), "v"(d[4 * i + 1]), "v"(d[4 * i + 2]), "v"(d[4 * i + 3]));
            top = mx4(mx4(mx4(mx4(top, d[4 * i]), d[4 * i + 1]), d[4 * i + 2]), d[4 * i + 3]);
        }
        if (!__any(top == SENT) || --budget < 0) break;
        __builtin_amdgcn_s_sleep(4);
    }
#pragma unroll
    for (int i = 0; i < M; ++i) {
        f32x2 x;
#pragma unroll
        for (int r = 0; r < 2; ++r) {
            auto both = [](float y) { return y + __int_as_float(__builtin_amdgcn_update_dpp(0, __float_as_int(y), 0x128, 0xf, 0xf, true)); };
            const float p = both(v[2 * i][r]), q = both(v[(2 * i + 1) & (N - 1)][r]);
            x[r] = gt[i] * (lane & 8 ? q : p) + bv[i][r];
        }
        __builtin_amdgcn_raw_buffer_store_b32(pack2(x[0], x[1]), ru, at[i], 0, OUT_AUX);
    }
}
#define TOUCH_M 15
DI void owner_loop(const Args& a, unsigned lds_base, int G, rsrc_t rs, rsrc_t rw, int sel, unsigned tbase, unsigned cbase, int& budget) {
    const int lane = threadIdx.x & 63, n = lane & 15, g = lane >> 4;
    const bool ex = G >= 64;
    const u32x4 sent4 = {SENT, SENT, SENT, SENT};
    const int js = (n & 2) ? 8 : ex ? (n & 4) + 2 * (n & 1) + (n >> 3) : 2 * (n >> 2) + (n & 1);
    u32x4 selu;
#pragma unroll
    for (int j = 0; j < 4; ++j) selu[j] = (js == 2 * j ? 0x3F80u : 0u) | (js == 2 * j + 1 ? 0x3F800000u : 0u);
    const int o = (part_unit(G, 0) ^ (sel & 8) * 256) + lane * 16;
    const int so = (n >> 3) * 16384 + ((ex ? 32768 + (G & 4) * 512 + (G & 3) * 128 : (G & 16) * 256 + (G & 32) * 64 + (G & 15) * 128) ^ (~sel & 8) * 256) + (g & 1) * 4 +
                   (ex ? (n & 4) * 16 + (g >> 1) * 32 + (n & 3) * 8 : (n & 6) * 16 + (g >> 1) * 16 + (n & 1) * 8);
    unsigned fails = 0;
    const int c8 = G >= 64 ? a.c1 >> 16 & 255 : (G & 28) == 0 ? a.c1 >> 8 & 255 : a.c1 & 255;
    const bool gv_on = true; (void)gv_on;
    unsigned oT = now(), oC = (unsigned)__builtin_readcyclecounter(), oR;
    { const unsigned r = (unsigned)(512.0f * (float)(oC - cbase) * __builtin_amdgcn_rcpf((float)(oT - tbase))); asm volatile("s_nop 0\n\tv_readfirstlane_b32 %0, %1" : "=s"(oR) : "v"(r)); }
    oT = 8 * oT + 16;
    unsigned lk = oC;
    budget = __builtin_amdgcn_readfirstlane(budget);
    const int ca = 2 * OFF_CAN + (lane >> 2) * 512 + (blockIdx.x & (NCP - 1)) * 128 + (lane & 3) * 16;
    unsigned tc;
    const unsigned oR0 = oR;
    oR = 0;
#pragma unroll 1
    for (int s = -1; s < NSEG - 1; ++s) {
        asm volatile("" : "+s"(s));
        const int pb = (s & 1) * 2 * PART_STEP;
        int np;
        asm volatile(OWNER_ASM : [np] "=&s"(np), [tm] "=&s"(tc), [bud] "+s"(budget), [oT] "+s"(oT), [oC] "+s"(oC), [oR] "+s"(oR), [lk] "+s"(lk)
            : [c8] "s"(c8), [ca] "v"(s < 0 ? OOB : ca), [cs] "s"((s & 3) * 16384), [tag] "s"(s + 1), [o] "v"(s < 0 ? OOB : o), [so] "v"(s < 0 ? OOB : so), [sel] "v"(selu), [rs] "s"(rs), [pb] "s"(pb), [mb0] "s"(2 * (OFF_MEMB + ((s + 1) & 3) * MEMB_STRIDE)),
              [cw] "s"(a.cw - c8), [rd] "s"(a.rd), [sv] "v"(sent4), [ro] "v"(s < 0 ? OOB : o), [pq] "s"(2 * PART_STEP - pb), [rm] "v"(s < 0 ? OOB : so), [mq] "s"(2 * (OFF_MEMB + ((s + 2) & 3) * MEMB_STRIDE)),
              [rw] "s"(rw), [rt] "v"(s >= 24 && s < 56 ? lane * 16 : OOB), [tq] "s"(tp_unit(8 * G + (s >> 2 & 7), sel) + (s & 3) * 1024) : OWNER_CLOBBER);
        if (s < 0) { oR = oR0; continue; }
        if ((s & TOUCH_M) == 8) {
            const char* pc; int d, len;
            asm volatile("s_getpc_b64 %0\n1:\ts_mov_b32 %1, .Lcold-1b+2048\n\ts_mov_b32 %2, .Lk_end-1b+2048" : "=s"(pc), "=s"(d), "=s"(len));
            touch(pc - 2048, max(min((G & 4 ? d + (G & 8) * 1024 : 0) + lane * 128, len), 0), lds_base);
        }
        fails += np > 1;
    }
    wait_prog(rw, F_S4 + lane * 4, true, 1, budget);
    const unsigned t = now(), t0 = t + min((int)(tc - 8 * t) >> 3, 100);
    finish<4>(a, rw, 4 * G + 4 * (G & 64), sel, t0 + (a.xa >> 8 & 255), budget);
    if (G >= 32 && G < 64) finish<1>(a, rw, 512 + G, sel, t0 + (a.xb >> 24), budget);
}
#define CORNER 5
DI void role_owner(const Args& a, unsigned lds_base, int ow, rsrc_t rw, int& budget) {
    const int tid = threadIdx.x, lane = tid & 63, w = __builtin_amdgcn_readfirstlane(tid >> 6);
    const unsigned tbase = now(), cbase = (unsigned)__builtin_readcyclecounter();
    if (ow < 56 && w) { prefetch(a, lds_base, ow & 3, (ow >> 2) * 3 + w - 1, 42); VM_DRAIN(); }
    if (w >= 2 || (w == 1 && (ow & ~11))) return;
    const u32x2 tag = launch_tag(a);
    tell_side(rw, tag);
    if (ow >= 56) {
        auto rtt = [&](int o) {
            unsigned long long t0, t1;
            asm volatile("s_memtime %0\n\ts_waitcnt lgkmcnt(0)" : "=s"(t0) :: "memory");
            st32(SENT, rw, lane == 0 ? 0 : OOB, o);
            asm volatile("s_waitcnt vmcnt(0)\n\ts_memtime %0\n\ts_waitcnt lgkmcnt(0)" : "=s"(t1) :: "memory");
            return (unsigned)(t1 - t0);
        };
        const int p = ow - 56, o = p == 0 ? 0 : p == 1 ? 32768 : p < 4 ? OFF_TP + (p - 2) * (2 << 20) : (p & 2) * 4096 + 1024 + (p & 1) * 1024;
        const int d = p == 1 ? 2048 : p < 4 ? 8192 : p & 1 ? 16384 : 4096;
        unsigned t0 = ~0u, t1 = ~0u;
        for (int r = 0; r < CORNER; ++r) { t0 = min(t0, rtt(o + r * 128)); t1 = min(t1, rtt(o + d + r * 128)); }
        const unsigned near = (t1 < t0) ^ (p == 1 ? die_side() : p > 4);
        __builtin_amdgcn_raw_buffer_store_b64((u32x2){(tag.x & ~7u) | near, tag.y}, rw, lane == 0 ? F_INIT + (105 + ow) * 128 : OOB, 0, 16);
    }
    const int sel = wait_init(rw, tag, budget, 161) ^ a.c1 >> 24;
    const rsrc_t rs = __builtin_amdgcn_make_buffer_rsrc(a.ws + (sel & 1) * 8192, 0, COMM_BYTES - 8192, 0x00020000);
    const int side = agree_side(rw, tag, ~tag, budget), cc = sel >> 1;
    const int G = (w ? 64 + 4 * side : 32 * side + (ow >> 1 & 24) + (ow & 4)) + ((cc ^ ow >> 1 ^ w) & 1) + ((cc ^ 2 * ow ^ ow >> 4) & 2);
    owner_loop(a, lds_base, G, rs, rw, sel, tbase, cbase, budget);
}

template <int O, class V> DI void put8(V& d, u32x8 f) {
#pragma unroll
    for (int j = 0; j < 8; ++j) d[O + j] = f[j];
}
DI f32x4 SMF64(u32x4 a, u32x8 b, f32x4 c, int i) { asm volatile("s_nop 1\n\tv_smfmac_f32_16x16x64_bf16 %0, %1, %2, %3\n\ts_nop 7" : "+v"(c) : "v"(a), "v"(b), "v"(i)); return c; }
DI f32x4 SMF32(u32x2 a, u32x4 b, f32x4 c, int i) { asm volatile("s_nop 1\n\tv_smfmac_f32_16x16x32_bf16 %0, %1, %2, %3\n\ts_nop 7" : "+v"(c) : "v"(a), "v"(b), "v"(i)); return c; }
DI f32x4 MFV(u32x4 a, u32x4 b, f32x4 c) { asm volatile("s_nop 1\n\tv_mfma_f32_16x16x32_bf16 %0, %1, %2, %0\n\ts_nop 7" : "+v"(c) : "v"(a), "v"(b)); return c; }

DI void role_head(const Args& a, LAS char* lds, unsigned lds_base, int h, rsrc_t rw, int& budget) {
    const int tid = threadIdx.x, w = __builtin_amdgcn_readfirstlane(tid >> 6);
    char* ws = a.ws;
    const unsigned tbase = now(), cbase = (unsigned)__builtin_readcyclecounter();
    int tw = 0;
    u32x32 A0, A1, A2, A3, A4, A5, A6, A7, R0, R1, R2, R3; u32x8 R4 = {};
    u32x2 vm;
    auto dma = [&](int voff, int soff, unsigned dst) { bdma(rw, voff, soff, dst); };
    auto fetch_kv = [&](int s, int lane) {
        const int n = lane & 15, g = lane >> 4;
        const int kxs = (g >> 1) * 1024 + ((g & 1) * 32 + 16 * (n >> 3) + (n & 7)) * 16;
        const int vxs = (g >> 1) * 1024 + ((g & 1) * 32 + n) * 16;
        const int so_ = (s * 32 + h) * 8192 + w * 2048;
        const unsigned xdst = lds_base + L_KX + w * 2048;
        dma(kxs, OFF_KF + so_, xdst); dma(kxs, OFF_KF + so_ + 128, xdst + 1024);
        dma(vxs, OFF_VF + so_, xdst + 8192); dma(vxs, OFF_VF + so_ + 256, xdst + 8192 + 1024);
    };
    u32x2 tag, in;
    unsigned wo_at;
    u32x8 S1, S2, S3, scon;
    int sel, kv_rdy = 0, tp, tq_, tx, U1, ua;
    auto setup = [&]() {
        sel = (init_ok(in, tag) ? init_sel(in) : wait_init(rw, tag, budget, 33 + 4 * h + w)) ^ a.c1 >> 24;
        R0[14] ^= (sel & 8) * 256; R0[15] ^= (sel & 8) * 256;
        scon[0] = w; scon[1] = 2 * OFF_CAN + (h >> 1) * 512 + (2 * (h & 1) + tw) * 16; scon[2] = F_KV + h * 512 + w * 128; scon[3] = 65536 + w * 4096;
        scon[4] = OFF_KF + h * 8192 + w * 2048; scon[5] = F_KVM + h * 256 + tw * 128; scon[6] = tw - 1; scon[7] = (tw ? OFF_VM : OFF_KM) + (h - NH) * 2048 + w * 512;
        const unsigned t = now(), c = (unsigned)__builtin_readcyclecounter();
        const float r = (float)(c - cbase) * __builtin_amdgcn_rcpf(8.0f * (float)(t - tbase));
        S1 = (u32x8){0u, (unsigned)a.e0, 0u, (unsigned)budget, (unsigned)a.d0, (unsigned)(4294967296.0f * __builtin_amdgcn_rcpf(r)), (unsigned)(a.c2 + a.b0), 0u};
        S2 = (u32x8){0u, 8 * t + 16, 0u, (unsigned)a.up, (unsigned)a.dn, (unsigned)a.c2, 0u, 1u};
        S3 = (u32x8){c, (unsigned)(4096.0f * r), 2u * OFF_MEMB + ((h >> 3 ^ sel >> 2) & 1) * 16384 + (w & 2) * 2048 + (~sel & 8) * 256 + (w & 1) * 1024, w == 3 ? ~0u : 0u};
        const int U0 = (2 * tw + (w >> 1)) * 64 + 8 * (w & 1);
        U1 = 4 * (2 * h + tw) + w;
        tp = tp_unit(U0, sel) + h * 128; tq_ = tp_unit(256 + U0, sel) + h * 128; tx = tp_unit(512 + (U0 >> 3) + 3 * (w & 1), sel) + h * 128; ua = tp_unit(256 + U1, sel);
        asm volatile("" : "+{v[0:7]}"(S1), "+{v[8:15]}"(S2), "+{v[16:23]}"(S3), "+{v[24:31]}"(scon), "+{v[120:151]}"(R0), "+s"(tp), "+s"(tq_), "+s"(tx), "+s"(U1), "+s"(ua), "+s"(sel));
    };
    {
        const int lane = tid & 63, n = lane & 15, g = lane >> 4;
#pragma unroll
        for (int j = 0; j < 3; ++j) glds16(a.mem0, min(4 * j + w, 8) * 1024 + lane * 16, lds_base + L_KX + min(4 * j + w, 8) * 1024);
        const int tq = n >> 2, tc = (n & 3) >> 1, lo8 = (n & 1) * 8;
        const int r_nat = 16 * (g >> 1) + 4 * (g & 1) + (tq & 1) + 8 * (tq >> 1);
        const int r_sig = 16 * (tq & 1) + 4 * (g >> 1) + (g & 1) + 2 * (tq >> 1);
        const int r_o = 16 * (tq & 1) + 8 * (g >> 1) + 4 * (g & 1) + (tq >> 1);
#define WFRAG(sl, kb, t) ((kb) < 8 ? sm_frag(lds, sl, r_sig, 32, 8, 4 * w + 2 * (t) + tc, lo8) : sm_frag(lds, sl, r_nat, 2, 32, 4 * w + 2 * (t) + tc, lo8))
        const u32x4 ro = ring_offs(w, lane, HD);
        auto issue = [&](int i) {
            const unsigned slot = lds_base + L_W + (i % C_RING) * 16384 + w * 4096;
            if (i == 23) ring_issue(a.Wo + (size_t)h * DH * DM + 512, ring_offs(w, lane, [](int r, int c) { return (r + 64 * (c >> 3)) * (DM * 2) + (c & 7) * 16; }), slot);
            else if (i < 28) ring_issue((i < 8 || i == 20 ? a.Wq : i < 16 || i == 21 ? a.Wk : a.Wv) + (size_t)(64 * (i < 16 ? i & 7 : i < 20 ? i - 12 : i < 24 ? 8 : i - 24)) * HD + h * DH, ro, slot);
            else ring_issue(a.Wo + (size_t)(h * DH + 64 * ((i & 1) ^ ((i - 28) >> 1))) * DM + 256 * tw + 128 * (i & 1), ring_offs(w, lane, DM), slot);
        };
        auto keep = [&](int at, u32x8 f) {
            *(LAS u32x4*)(lds + at + lane * 16) = (u32x4){f[0], f[1], f[2], f[3]};
            *(LAS u32x4*)(lds + at + 1024 + lane * 16) = (u32x4){f[4], f[5], f[6], f[7]};
        };
#pragma unroll
        for (int i = 0; i < C_RING; ++i) issue(i);
        tag = launch_tag(a);
        tell_side(rw, tag);
        const int ai = 2 * (n >> 2) + (n & 1), ahf = (n >> 1) & 1;
        R0[0] = R0[1] = R0[2] = R0[3] = 0x3F803F80u;
        R0[4] = ahf ? 0xEEEE : 0x4444;
        R0[5] = (g >> 1) * 512 + (n >> 2) * 128 + (g & 1) * 64 + (n & 3) * 16;
        R0[6] = lds_base + L_MEMS + 8192 + g * 256 + ai * 32 + ahf * 16;
        R0[7] = lane * 16;
        R0[8] = lds_base + L_Q + 2 * g * 320 + (16 * w + n) * 4;
        R0[9] = lds_base + L_KO + 2 * g * 576 + (16 * w + (n & 8) + 4 * (n & 1) + ((n >> 1) & 3)) * 4;
        R0[10] = lds_base + L_Q + ai * 320 + g * 16;
        R0[11] = lds_base + L_KO + ai * 576 + g * 32 + ahf * 16;
        R0[12] = lds_base + L_KX + 8192 + w * 2048 + (g >> 1) * 256 + n * 16 + (g & 1) * 8;
        R0[13] = lds_base + L_W + w * 4096 + lane * 16;
        wo_at = lds_base + L_W + (4 + (w >> 1)) * 16384 + r_o * 256 + ((8 * (w & 1) + tc) ^ (2 * swz(r_o))) * 16 + lo8;
        vm.x = vm.y = (w << 14) + r_sig * 256 + ((4 * w + tc) ^ (2 * swz(r_sig))) * 16 + lo8;
        asm volatile("" : "+{v[120:151]}"(R0), "+{v119}"(wo_at), "+{v[96:97]}"(vm), "+{v[248:255]}"(R4));
        u32x2 other;
        ring_wait<4 * C_RING + 1>();
        mem0_arrange(lds, L_KX, tid);
        static_for<6>([&](auto ic) {
            constexpr int G = decltype(ic)::value;
            ring_wait<(G == 2 || G == 5 ? 16 : 17)>();
            if constexpr (G == 5) { asm volatile("" : "+v"(other)); tw = agree_side(rw, tag, other, budget); }
            const int sl = L_W + (G & 1) * 65536;
            auto fr = [&](int j, int t) { return WFRAG(sl + ((j ^ w) << 14), 0, t); };
            if constexpr (G < 4) {
                u32x32& A = G == 0 ? A0 : G == 1 ? A2 : G == 2 ? A4 : A6;
                u32x32& B = G == 0 ? A1 : G == 1 ? A3 : G == 2 ? A5 : A7;
                put8<0>(A, fr(0, 0)); put8<8>(A, fr(0, 1)); put8<16>(A, fr(1, 0)); put8<24>(A, fr(1, 1));
                put8<0>(B, fr(2, 0)); put8<8>(B, fr(2, 1)); put8<16>(B, fr(3, 0)); put8<24>(B, fr(3, 1));
                if constexpr (G == 0) asm volatile("" : "+{a[0:31]}"(A0), "+{a[32:63]}"(A1));
                if constexpr (G == 1) asm volatile("" : "+{a[64:95]}"(A2), "+{a[96:127]}"(A3));
                if constexpr (G == 2) asm volatile("" : "+{a[128:159]}"(A4), "+{a[160:191]}"(A5));
                if constexpr (G == 3) asm volatile("" : "+{a[192:223]}"(A6), "+{a[224:255]}"(A7));
            } else if constexpr (G == 4) {
                put8<16>(R1, fr(0, 0)); put8<24>(R1, fr(0, 1)); put8<0>(R2, fr(1, 0)); put8<8>(R2, fr(1, 1));
                put8<16>(R2, fr(2, 0)); put8<24>(R2, fr(2, 1)); put8<0>(R3, fr(3, 0)); put8<8>(R3, fr(3, 1));
            } else if constexpr (G == 5) {
                put8<16>(R0, WFRAG(sl, 8, 0)); put8<24>(R0, WFRAG(sl, 8, 1)); put8<0>(R1, WFRAG(sl + 16384, 8, 0)); put8<8>(R1, WFRAG(sl + 16384, 8, 1));
                put8<16>(R3, WFRAG(sl + 32768, 8, 0)); put8<24>(R3, WFRAG(sl + 32768, 8, 1));
                keep(L_KX + w * 2048, sm_frag(lds, sl + 49152, r_o, 2, 32, 8 * (w >> 1) + 4 * tw + 2 * (w & 1) + tc, lo8));
                R0[14] = part_unit((2 * (4 * tw + w) + (n >> 3)) * 4 + g, h) + (n & 7) * 16;
                R0[15] = (g >> 1) == (w >> 1) ? part_unit(64 + 2 * (2 * tw + (w & 1)) + (g >> 1), h) + (g & 1) * 64 + n * 4 : OOB;
                asm volatile("" : "+{v[120:151]}"(R0));
            }
            WG_BARRIER();
            if constexpr (G == 4) asm volatile("buffer_load_dwordx2 %0, %1, %2, 0 offen sc1" : "=v"(in) : "v"(init_at(33 + 4 * h + w)), "s"(rw) : "memory");
#pragma unroll
            for (int i = 0; i < 4; ++i) issue(4 * G + 8 + i);
            if constexpr (G == 2) asm volatile("buffer_load_dwordx2 %0, off, %1, %2 sc1" : "=v"(other) : "s"(rw), "s"(side_at(16)) : "memory");
            if constexpr (G == 5) { asm volatile("" : "+v"(in)); setup(); }
        });
        asm volatile("" : "+{v[120:151]}"(R0), "+{v[152:183]}"(R1), "+{v[184:215]}"(R2), "+{v[216:247]}"(R3), "+{v[248:255]}"(R4));
    }
    const bool gv_on = tw == 0; (void)gv_on;
    const rsrc_t rs = __builtin_amdgcn_make_buffer_rsrc(ws + (sel & 1) * 8192, 0, COMM_BYTES - 8192, 0x00020000);
    const int fo = F_KVM + h * 256 + tw * 128;
    for (;;) {
        asm volatile(STEP_ASM
            : "+{v[96:97]}"(vm), "+{v[0:7]}"(S1), "+{v[8:15]}"(S2), "+{v[16:23]}"(S3), "+{v[24:31]}"(scon)
              , "+{a[0:31]}"(A0), "+{a[32:63]}"(A1), "+{a[64:95]}"(A2), "+{a[96:127]}"(A3), "+{a[128:159]}"(A4), "+{a[160:191]}"(A5), "+{a[192:223]}"(A6), "+{v[120:151]}"(R0), "+{v[152:183]}"(R1), "+{v[248:255]}"(R4)
            : "{a[224:255]}"(A7), "{v[184:215]}"(R2), "{v[216:247]}"(R3), "{v119}"(wo_at),
              [rs] "s"(rs), [rw] "s"(rw), [qb] "s"(OFF_OF + LATE * 262144 + h * 8192 + (w & 1) * 256),
              [tp] "s"(tp), [tq] "s"(tq_), [tx] "s"(tx), [u] "s"(U1), [ua] "s"(ua), [fw] "s"(8 * (a.xb >> 16 & 255)), [ru] "s"(__builtin_amdgcn_make_buffer_rsrc(a.out, 0, SEQ * DM * 2, 0x00020000))
            : STEP_CLOBBER);
        const int s = __builtin_amdgcn_readfirstlane(S1[0]);
        const unsigned st = __builtin_amdgcn_readfirstlane(S1[7]);
        kv_rdy = __builtin_amdgcn_readfirstlane(S1[2]); budget = __builtin_amdgcn_readfirstlane(S1[3]);
        if (s == NSEG - 1) break;
        if (__builtin_expect(st & 2, 0)) {
            const int lane = opaque_lane();
            LAS char* z = (LAS char*)0;
            const f32x4 zero = {0, 0, 0, 0};
            const u32x4 ones = __builtin_shufflevector(R0, R0, 0, 1, 2, 3);
            const unsigned zr = lane >> 6;
            const int idx = R0[4];
            u32x4 qf[4];
            f32x4 s1 = zero, s2 = zero;
#pragma unroll
            for (int ks = 0; ks < 4; ++ks) {
                qf[ks] = *(LAS u32x4*)(z + R0[10] + ks * 64);
                s1 = MFV(*(LAS u32x4*)(lds + L_KX + lane * 16 + ks * 2048), qf[ks], s1);
                s2 = MFV(*(LAS u32x4*)(lds + L_KX + lane * 16 + ks * 2048 + 1024), qf[ks], s2);
            }
            f32x4 s0 = SMF64(*(LAS u32x4*)(z + R0[11]), __builtin_shufflevector(qf[0], qf[1], 0, 1, 2, 3, 4, 5, 6, 7), zero, idx);
            s0 = SMF64(*(LAS u32x4*)(z + R0[11] + 128), __builtin_shufflevector(qf[2], qf[3], 0, 1, 2, 3, 4, 5, 6, 7), s0, idx);
            const float sa = s0[0] + s0[2], sb = s0[1] + s0[3];
            float mx = fmaxf(fmaxf(fmaxf(fmaxf(s1[0], s1[1]), fmaxf(s1[2], s1[3])), fmaxf(fmaxf(s2[0], s2[1]), fmaxf(s2[2], s2[3]))), fmaxf(sa, sb));
            mx = fmaxf(mx, __shfl_xor(mx, 16)); mx = fmaxf(mx, __shfl_xor(mx, 32));
            auto ex2 = [&](float x, float y) { return pack2(__builtin_amdgcn_exp2f(x - mx), __builtin_amdgcn_exp2f(y - mx)); };
            const u32x4 px = {ex2(s1[0], s1[1]), ex2(s1[2], s1[3]), ex2(s2[0], s2[1]), ex2(s2[2], s2[3])};
            const unsigned pm = ex2(sa, sb);
            const f32x4 l = MFV(((u32x4){pm, zr, zr, zr}), ones, MFV(px, ones, zero));
            const u32x2 x0 = *(LAS u32x2*)(z + R0[12]), y0 = *(LAS u32x2*)(z + R0[12] + 1024), x1 = *(LAS u32x2*)(z + R0[12] + 512), y1 = *(LAS u32x2*)(z + R0[12] + 1536);
            const f32x4 o = SMF64(px, ((u32x8){x0.x, y0.x, x0.y, y0.y, x1.x, y1.x, x1.y, y1.y}), zero, idx) + SMF32(((u32x2){pm, zr}), ((u32x4){vm.x, vm.y, zr, zr}), zero, idx);
            const float i0 = __builtin_amdgcn_rcpf(l[0]), i1 = __builtin_amdgcn_rcpf(l[1]);
            *(LAS unsigned*)(z + R0[9] + 256) = pack2(o[0] * i0, o[2] * i0);
            *(LAS unsigned*)(z + R0[9] + 256 + 576) = pack2(o[1] * i1, o[3] * i1);
            WG_BARRIER();
            const u32x4 oa = *(LAS u32x4*)(z + R0[11] + 256 + (w >> 1) * 128), ob = *(LAS u32x4*)(z + R0[11] + 384 - (w >> 1) * 128);
            auto frag = [&](int at) { return __builtin_shufflevector(*(LAS u32x4*)(z + at), *(LAS u32x4*)(z + at + 1024), 0, 1, 2, 3, 4, 5, 6, 7); };
            f32x2 f[4];
#pragma unroll
            for (int T = 0; T < 4; ++T) f[T] = fold(SMF64(ob, frag(R0[13] + scon[3] + 32768 + T * 2048), SMF64(oa, frag(R0[13] + scon[3] + T * 2048), zero, idx), idx));
            const f32x2 fx = fold(SMF64(ob, frag((R0[13] ^ 8192) + 49152 + 2048), SMF64(oa, frag(R0[13] + 49152 + 2048), zero, idx), idx));
            const int pb = (s & 1) * 2 * PART_STEP;
            st128((u32x4){pack2(f[0].x, f[1].x), pack2(f[2].x, f[3].x), pack2(f[0].y, f[1].y), pack2(f[2].y, f[3].y)}, rs, R0[14], pb);
            st32(pack2(fx.x, fx.y), rs, R0[15], pb);
            if (s == LATE) { S1[7] = 4; continue; }
        }
        if (__builtin_expect((st & 2) || kv_rdy <= s, 0)) {
            while (kv_rdy <= s) { kv_rdy = prog_min(rw, F_KV + h * 512 + w * 128); if (--budget < 0) break; }
            fetch_kv(s + 1, opaque_lane());
        }
        if (st & 2) { S1[1] = read_canaries(s, rs, opaque_lane(), budget) + S1[6]; S2[2] += 0x10000; }
        if ((int)(S1[1] - S2[1]) > 0x8000) { S2[1] = 8 * now() + 16; S3[0] = (unsigned)__builtin_readcyclecounter(); }
        S1[0] = s + 1; S1[2] = kv_rdy; S1[3] = budget;
    }
    TT(true, 0)
    st32((unsigned)(NSEG - 2), rw, opaque_lane() == 0 && w == 0 ? fo : OOB, 0);
    TT(true, 2)
}

__global__ void __launch_bounds__(256, 1) fused_kernel(Args a) {
    extern __shared__ __attribute__((aligned(16))) char smem[];
    LAS char* lds = (LAS char*)smem;
    const unsigned lds_base = (unsigned)(uintptr_t)lds;
    const int w = __builtin_amdgcn_readfirstlane(threadIdx.x >> 6);
    const int b = blockIdx.x, bx = b & 7, bi = b >> 3;
    if (bi >= 28) {
        const void* pc; unsigned len;
        asm volatile("s_getpc_b64 %0\n1:\ts_mov_b32 %1, .Lk_end-1b" : "=s"(pc), "=s"(len));
        touch(pc, min((bi - 28) * 32768 + threadIdx.x * 128, len), lds_base);
    }
    asm volatile("" :: "s"(a.Wq), "s"(a.cw));
    const rsrc_t rw = __builtin_amdgcn_make_buffer_rsrc(a.ws, 0, WS_USED, 0x00020000);
    int budget = SPIN_MAX;
#define RT(i)
    if (bx < 4) {
        if (bi < 16) role_head(a, lds, lds_base, 8 * bx + (bi >> 2) * 2 + (bi & 1), rw, budget);
        else role_owner(a, lds_base, 4 * (bi - 16) + bx, rw, budget);
    } else {
        const int fx = bx - 4, no = fx < 3 ? 14 : 22, j = 14 * fx + bi;
        if (bi >= no || (j & 2)) {
            const unsigned t = now() + (a.d1 & 0xffff);
            if (bi >= no) { if (w) prefetch(a, lds_base, bx, (bi - no) * 3 + w - 1, 3 * (32 - no)); }
            else {
                const int sh = (j >> 2) * 2 + (j & 1);
#pragma unroll
                for (int i = 0; i < 6; ++i) {
                    const int u = 6 * sh + i, c = u >> 3;
                    st128((u32x4){SENT, SENT, SENT, SENT}, rw, threadIdx.x * 16, (c < 18 ? c * 4 * PART_STEP : (c < 20 ? 2 * (OFF_MEMB + MEMB_STRIDE) - 18 * 32768 : 2 * OFF_CAN - 20 * 32768) + c * 32768) + (u & 7) * 4096);
                }
                const u32x2 tag = launch_tag(a);
                VM_DRAIN();
                WG_BARRIER();
                __builtin_amdgcn_raw_buffer_store_b64(tag, rw, threadIdx.x == 0 ? F_INIT + sh * 128 : OOB, 0, 16);
                RT(3)
            }
            (void)wait_until(t);
        }
        if (bi >= no) {
            const int t = 18 * fx + bi - no;
            role_s4d(a, lds, lds_base, t, rw, budget);
            RT(1)
            TT(true, 6)
            if (t < 9 * OUT_REP) role_outp(a, lds, lds_base, t % 9, t / 9, rw, budget);
        }
        else {
            if (!(j & 2)) role_kv(a, lds, lds_base, (j >> 2) * 2 + (j & 1), rw, budget);
            RT(1)
            TT(true, 6)
            role_qa(a, lds, lds_base, (j >> 2) * 2, (j & 2 ? 0 : FAST + 2) + (j & 1), (j & 2 ? FAST : FAST + 2) + (j & 1), rw, budget);
        }
    }
    RT(2)
    TT(true, 5)
    if (budget < 0 && threadIdx.x == 0) st32(0x1000u + b, rw, F_ERR, 0);
    asm volatile(".Lk_end:");
}

static int g_c1 = 34, g_c1m = 30, g_c1x = 46, g_c2 = 316, g_b0 = 32, g_up = 8, g_fl = 8, g_dn = 2, g_d0 = 1680, g_da = 200, g_ua = 48, g_d1 = 300, g_rd = 34, g_cw = 69, g_e0 = 112, g_x1 = 40, g_x2 = 140, g_x3 = 150, g_x4 = 75, g_x5 = 135, g_x6 = 157, g_x7 = 35, g_x8 = 210, g_pc = 45, g_cc = 0;
extern "C" void kernel_launch(void* const* d_in, const int* in_sizes, int n_in, void* d_out, int out_size, void* d_ws, size_t ws_size, hipStream_t stream) {
    Args a;
    a.x = (const u16*)d_in[0]; a.mem0 = (const u16*)d_in[1];
    a.Wq = (const u16*)d_in[2]; a.Wk = (const u16*)d_in[3]; a.Wv = (const u16*)d_in[4]; a.Wo = (const u16*)d_in[5];
    a.lAr = (const u16*)d_in[8]; a.Aim = (const u16*)d_in[9]; a.Cr = (const u16*)d_in[10]; a.Ci = (const u16*)d_in[11];
    a.ldt = (const u16*)d_in[12]; a.Dsk = (const u16*)d_in[13]; a.gw = (const u16*)d_in[14];
    a.out = (u16*)d_out;
    a.ws = (char*)(((uintptr_t)d_ws + ((2u << 20) - 1)) & ~(uintptr_t)((2u << 20) - 1));
    a.c1 = g_c1 | g_c1m << 8 | g_c1x << 16 | g_cc << 24; a.c2 = g_c2; a.d0 = g_d0 + g_da; a.b0 = g_b0; a.d1 = g_d1 | g_pc << 16; a.up = g_up | g_ua << 8 | g_fl << 16; a.dn = g_dn | g_d0 << 8; a.rd = 23 * g_rd - 300; a.cw = 8 * g_cw - 104; a.e0 = 8 * g_e0;
    a.xa = g_x1 | g_x2 << 8 | g_x5 << 16 | g_x7 << 24; a.xb = g_x6 | g_x3 << 8 | g_x4 << 16 | g_x8 << 24;
    (void)hipFuncSetAttribute((const void*)fused_kernel, hipFuncAttributeMaxDynamicSharedMemorySize, L_TOTAL);
    fused_kernel<<<256, 256, L_TOTAL, stream>>>(a);
}
```

```cpp
#include <hip/hip_runtime.h>
#include <hip/hip_bf16.h>
#include <stdint.h>

typedef unsigned short u16;
typedef __bf16 bf16x8 __attribute__((ext_vector_type(8)));
typedef __bf16 bf16x16 __attribute__((ext_vector_type(16)));
typedef _Float16 f16x2 __attribute__((ext_vector_type(2)));
typedef _Float16 f16x8 __attribute__((ext_vector_type(8)));
typedef short s16x4 __attribute__((ext_vector_type(4)));
typedef float f32x2 __attribute__((ext_vector_type(2)));
typedef float f32x4 __attribute__((ext_vector_type(4)));
typedef float f32x16 __attribute__((ext_vector_type(16)));
typedef unsigned u32x2 __attribute__((ext_vector_type(2)));
typedef unsigned u32x4 __attribute__((ext_vector_type(4)));
typedef unsigned u32x8 __attribute__((ext_vector_type(8)));
typedef unsigned u32x16 __attribute__((ext_vector_type(16)));
typedef unsigned u32x32 __attribute__((ext_vector_type(32)));
typedef short s16x8 __attribute__((ext_vector_type(8)));
#define LAS __attribute__((address_space(3)))
#define DI __device__ __forceinline__
typedef __amdgpu_buffer_rsrc_t rsrc_t;

constexpr int SEQ = 2048, DM = 576, NH = 32, DH = 128, HD = NH * DH, NM = 8, SEG = 32, NSEG = 64, NS = 128;
constexpr unsigned SENT = 0xFFFFFFFFu;
constexpr unsigned OOB = 0x7FFFFFF0u;
constexpr int SPIN_MAX = 1 << 20;

constexpr int PART_STEP = 16384;
constexpr int OFF_MEMB = 40 * PART_STEP;
constexpr int MEMB_STRIDE = 32768;
constexpr int OFF_CAN = 52 * PART_STEP;
constexpr int COMM_LOGICAL = OFF_CAN + 32768;
constexpr int COMM_BYTES = 2 * COMM_LOGICAL;
constexpr int F_ERR = COMM_BYTES;
constexpr int F_KV = F_ERR + 128;
constexpr int F_KVM = F_KV + 16384;
constexpr int F_O = F_KVM + 8192;
constexpr int F_H = F_O + 4096;
constexpr int F_S4 = F_H + 32768;
constexpr int F_DONE = F_S4 + 512;
constexpr int INIT_BYTES = 2 * OFF_CAN + 131072;
constexpr int F_INIT = INIT_BYTES;
constexpr int F_CL = F_INIT + 24576;
constexpr int F_EPOCH = F_CL + 16384;
static_assert(F_DONE + 128 <= INIT_BYTES, "");
static_assert(F_EPOCH + 128 <= (2 << 20), "hand-off region");
constexpr int OFF_TP = 2 << 20;
constexpr int OFF_KF = 6 << 20;
constexpr int OFF_VF = OFF_KF + SEQ * HD * 2;
constexpr int OFF_OF = OFF_VF + SEQ * HD * 2;
constexpr int OFF_KM = OFF_OF + SEQ * HD * 2;
constexpr int OFF_VM = OFF_KM + NSEG * NH * NM * DH * 2;
constexpr int FAST = NSEG - 6;
constexpr int LATE = NSEG - 2;
constexpr int OFF_BO = OFF_VM + NSEG * NH * NM * DH * 2;
constexpr int BO_ROW = 4352;
constexpr int OFF_GT = OFF_BO + DM * BO_ROW;
constexpr int OFF_DBG = OFF_GT + SEQ * 4;
constexpr int WS_USED = OFF_DBG + (4 << 20);

struct Args {
    const u16 *x, *mem0, *Wq, *Wk, *Wv, *Wo, *lAr, *Aim, *Cr, *Ci, *ldt, *Dsk, *gw;
    u16* out; char* ws;
    int c1, c2, d0, b0, d1, up, dn, rd, cw, e0;
    unsigned xa, xb;
};

DI float bf2f(u16 b) { return __uint_as_float((unsigned)b << 16); }
DI float bflo(unsigned w) { return __uint_as_float(w << 16); }
DI float bfhi(unsigned w) { return __uint_as_float(w & 0xFFFF0000u); }
DI u16 f2bf(float f) { return __builtin_bit_cast(u16, (__bf16)f); }
DI unsigned pack2(float lo, float hi) {
    typedef __bf16 bf16x2 __attribute__((ext_vector_type(2)));
    bf16x2 v = {(__bf16)lo, (__bf16)hi};
    return __builtin_bit_cast(unsigned, v);
}
#define WG_BARRIER() asm volatile("s_waitcnt lgkmcnt(0)\n\ts_barrier" ::: "memory")
#define VM_DRAIN() asm volatile("s_waitcnt vmcnt(0)" ::: "memory")
DI int opaque_lane() { int l; asm volatile("v_mbcnt_lo_u32_b32 %0, -1, 0\n\tv_mbcnt_hi_u32_b32 %0, -1, %0" : "=v"(l)); return l; }
DI int cmap(int o) { return (o & 0x1FFF) | ((o >> 13) << 14); }
DI int part_unit(int G, int h) { const int p = G < 64 ? G & 31 : 32 + (G & 3); return (p >> 2) * 131072 + cmap((p & 3) * 4096) + ((G >> (G < 64 ? 5 : 2) ^ p >> 3) & 1) * 2048 + (h >> 4) * 65536 + (h & 15) * 128; }
DI unsigned mx4(unsigned a, u32x4 v) { a = max(max(a, v.x), v.y); return max(max(a, v.z), v.w); }
DI unsigned now() { return (unsigned)__builtin_amdgcn_s_memrealtime(); }
DI unsigned wait_until(unsigned t) {
    unsigned v; int guard = 8192;
    do { v = now(); } while ((int)(t - v) >= 4 && --guard > 0);
    const int r = __builtin_amdgcn_readfirstlane((int)(t - v));
    asm volatile("s_cmp_lt_i32 %0, 1\n\ts_cbranch_scc1 1f\n\ts_nop 3\n\ts_cmp_lt_i32 %0, 2\n\ts_cbranch_scc1 1f\n\ts_nop 3\n\ts_cmp_lt_i32 %0, 3\n\ts_cbranch_scc1 1f\n\ts_nop 7\n1:" :: "s"(r) : "scc");
    return r > 0 ? t : v + 2;
}
DI void st128(u32x4 v, rsrc_t rs, int off, int soff) {
    __builtin_amdgcn_raw_buffer_store_b128(v, rs, off, soff, 16);
    asm volatile("s_nop 1" :: "v"(v) : "memory");
}
DI void st32(unsigned v, rsrc_t rs, int off, int soff) { __builtin_amdgcn_raw_buffer_store_b32(v, rs, off, soff, 16); }
DI u32x4 ld128(rsrc_t rs, int off, int soff) { return __builtin_amdgcn_raw_buffer_load_b128(rs, off, soff, 16); }
DI u32x2 ld64(rsrc_t rs, int off, int soff) { return __builtin_amdgcn_raw_buffer_load_b64(rs, off, soff, 16); }
DI unsigned ld32(rsrc_t rs, int off, int soff) { return __builtin_amdgcn_raw_buffer_load_b32(rs, off, soff, 16); }
DI f32x2 fold(f32x4 a) { return (f32x2){a[0], a[1]} + (f32x2){a[2], a[3]}; }
DI float dpp_xor1(float v) { return __int_as_float(__builtin_amdgcn_update_dpp(0, __float_as_int(v), 0xB1, 0xf, 0xf, true)); }
DI float red_sum_g(float v) {
    auto a = __builtin_amdgcn_permlane16_swap(__float_as_uint(v), __float_as_uint(v), false, false);
    v = __uint_as_float(a[0]) + __uint_as_float(a[1]);
    auto b = __builtin_amdgcn_permlane32_swap(__float_as_uint(v), __float_as_uint(v), false, false);
    return __uint_as_float(b[0]) + __uint_as_float(b[1]);
}
DI int wave_max32(int d) {
    d = max(d, __builtin_amdgcn_update_dpp(d, d, 0x121, 0xf, 0xf, false));
    d = max(d, __builtin_amdgcn_update_dpp(d, d, 0x122, 0xf, 0xf, false));
    d = max(d, __builtin_amdgcn_update_dpp(d, d, 0x124, 0xf, 0xf, false));
    d = max(d, __builtin_amdgcn_update_dpp(d, d, 0x128, 0xf, 0xf, false));
    return __builtin_amdgcn_readfirstlane(max(max(__builtin_amdgcn_readlane(d, 0), __builtin_amdgcn_readlane(d, 16)), max(__builtin_amdgcn_readlane(d, 32), __builtin_amdgcn_readlane(d, 48))));
}
DI u32x2 tr_read(LAS char* lds, int addr) { return __builtin_bit_cast(u32x2, __builtin_amdgcn_ds_read_tr16_b64_v4i16((LAS s16x4*)(lds + addr))); }
DI void wait_prog(rsrc_t rw, int off, bool act, int thr, int& budget) {
    for (;;) {
        const int v = (int)ld32(rw, off, 0);
        asm volatile("" ::: "memory");
        if (__all(!act || v >= thr)) break;
        if (--budget < 0) break;
        __builtin_amdgcn_s_sleep(1);
    }
}
DI u32x2 launch_tag(const Args& a) {
    unsigned long long z;
    asm volatile("s_load_dwordx2 %0, %1, 0x0\n\ts_waitcnt lgkmcnt(0)" : "=s"(z) : "s"(a.ws + F_EPOCH) : "memory");
    z += 0x9E3779B97F4A7C15ull;
    z = (z ^ (z >> 30)) * 0xBF58476D1CE4E5B9ull; z = (z ^ (z >> 27)) * 0x94D049BB133111EBull; z ^= z >> 31;
    return (u32x2){(unsigned)z, (unsigned)(z >> 32)};
}
DI int init_at(int more) { const int l = threadIdx.x & 63; return F_INIT + (l < 32 ? l : l < 56 || !more ? more : l + 105) * 128; }
DI bool init_ok(u32x2 v, u32x2 tag) { return __all(((v.x ^ tag.x) >> 3) == 0 && v.y == tag.y); }
DI int init_sel(u32x2 v) { const int m = __ballot(v.x & 1) >> 56; return (m & 1) + 2 * (m >> (4 + 2 * (m & 1)) & 3) + 4 * (m & 14); }
DI int wait_init(rsrc_t rw, u32x2 tag, int& budget, int more = 0) {
    u32x2 v;
    for (;;) {
        v = ld64(rw, init_at(more), 0);
        asm volatile("" ::: "memory");
        if (init_ok(v, tag) || --budget < 0) break;
        __builtin_amdgcn_s_sleep(2);
    }
    return init_sel(v);
}
DI unsigned die_side() { return (__builtin_amdgcn_s_getreg(4 | 14 << 6) ^ blockIdx.x >> 1) & 1; }
DI int side_at(int x) { const int b = blockIdx.x ^ x; return F_CL + ((b >> 3) * 4 + (b & 3)) * 128; }
DI void tell_side(rsrc_t rw, u32x2 tag) { __builtin_amdgcn_raw_buffer_store_b64((u32x2){(tag.x & ~1u) | die_side(), tag.y}, rw, threadIdx.x == 0 ? 0 : OOB, side_at(0), 16); }
DI int agree_side(rsrc_t rw, u32x2 tag, u32x2 v, int& budget) {
    while (!__all(((v.x ^ tag.x) >> 1) == 0 && v.y == tag.y) && --budget >= 0) { v = ld64(rw, 0, side_at(16)); asm volatile("" ::: "memory"); }
    const unsigned me = die_side();
    return (__builtin_amdgcn_readfirstlane(v.x) ^ me) & 1 ? me : blockIdx.x >> 4 & 1;
}
DI void glds16(const void* base, int voff, unsigned lds_dst) {
    unsigned keep;
    asm volatile("s_mov_b32 %0, m0\n\ts_mov_b32 m0, %3\n\ts_nop 0\n\tglobal_load_lds_dwordx4 %1, %2\n\ts_mov_b32 m0, %0" : "=&s"(keep) : "v"(voff), "s"(base), "s"(lds_dst) : "memory");
}
DI void bdma(rsrc_t rs, int voff, int soff, unsigned lds_dst) {
    unsigned keep;
    asm volatile("s_mov_b32 %0, m0\n\ts_mov_b32 m0, %4\n\ts_nop 0\n\tbuffer_load_dwordx4 %1, %2, %3 offen sc1 lds\n\ts_mov_b32 m0, %0" : "=&s"(keep) : "v"(voff), "s"(rs), "s"(soff), "s"(lds_dst) : "memory");
}
DI int swz(int r) { return ((r & 3) ^ (((r >> 2) & 3) << 1) ^ (((r >> 4) & 1) << 2)) & 7; }
template <class F> DI u32x4 ring_offs(int w, int lane, F at) {
    u32x4 o;
#pragma unroll
    for (int j = 0; j < 4; ++j) { const int r = 16 * w + 4 * j + (lane >> 4); o[j] = at(r, (lane & 15) ^ (2 * swz(r))) - 1024 * j; }
    return o;
}
DI u32x4 ring_offs(int w, int lane, int ld) { return ring_offs(w, lane, [&](int r, int c) { return r * ld * 2 + c * 16; }); }
DI void ring_issue(const u16* src, u32x4 o, unsigned slot) {
    unsigned keep;
    asm volatile("s_mov_b32 %0, m0\n\ts_mov_b32 m0, %6\n\ts_nop 0\n\tglobal_load_lds_dwordx4 %1, %5\n\tglobal_load_lds_dwordx4 %2, %5 offset:1024\n\t"
                 "global_load_lds_dwordx4 %3, %5 offset:2048\n\tglobal_load_lds_dwordx4 %4, %5 offset:3072\n\ts_mov_b32 m0, %0"
                 : "=&s"(keep) : "v"(o.x), "v"(o.y), "v"(o.z), "v"(o.w), "s"(src), "s"(slot) : "memory");
}
#define PF_FIRST 2
constexpr int L_TOTAL = 163840, L_SINK = L_TOTAL - 256;
DI void touch(const void* base, int voff, unsigned lds_base) {
    unsigned keep;
    asm volatile("s_mov_b32 %0, m0\n\ts_mov_b32 m0, %3\n\ts_nop 0\n\tglobal_load_lds_dword %1, %2\n\ts_mov_b32 m0, %0" : "=&s"(keep) : "v"(voff), "s"(base), "s"(lds_base + L_SINK) : "memory");
}
DI void prefetch(const Args& a, unsigned lds_base, int x, int id, int n) {
    const int lane = threadIdx.x & 63;
    const int h0 = x < 4 ? 8 * x : (0x160E0800 >> (8 * (x - 4))) & 255, lpr = x == 5 ? 12 : x == 7 ? 20 : 16, per = 9 * lpr;
    const int first = PF_FIRST * lpr, end = (x < 4 ? 4 : 2) * per;
    const float rl = __builtin_amdgcn_rcpf((float)lpr);
    if (x >= 4 && id < 18) touch(a.x, (id * 64 + lane) * 128, lds_base);
    for (int i = first + id; i < end; i += n) {
        int m = (i >= per) + (i >= 2 * per) + (i >= 3 * per), j = i - m * per;
        if (x < 4 && m < 3) { const int b = i >> 4; m = b < 20 ? b >> 3 : min(b - 20, 2); j = (b < 16 ? b & 7 : b < 20 ? b - 12 : b < 23 ? 8 : b - 23) * 16 + (i & 15); }
        const int mm = m + (x >= 4);
        const int L = j * 64 + lane, row = (int)((L + 0.5f) * rl);
        touch(mm == 0 ? a.Wq : mm == 1 ? a.Wk : mm == 2 ? a.Wv : a.Wo, m == 3 ? h0 * (DH * DM * 2) + L * 128 : row * (HD * 2) + (L - row * lpr) * 128 + h0 * 256, lds_base);
    }
}
template <int N> DI void ring_wait() { asm volatile("s_waitcnt vmcnt(%0) lgkmcnt(0)\n\ts_barrier" :: "i"(N) : "memory"); }
template <int I> struct ic_t { static constexpr int value = I; };
template <int N, int I = 0, class F> DI void static_for(F&& f) { if constexpr (I < N) { f(ic_t<I>{}); static_for<N, I + 1>(f); } }
#define MFMA32(a, b, c) __builtin_amdgcn_mfma_f32_32x32x16_bf16(__builtin_bit_cast(bf16x8, a), __builtin_bit_cast(bf16x8, b), c, 0, 0, 0)
#define MFMA16(a, b, c) __builtin_amdgcn_mfma_f32_16x16x32_bf16(__builtin_bit_cast(bf16x8, a), __builtin_bit_cast(bf16x8, b), c, 0, 0, 0)

constexpr int S_CH = 9;
constexpr int S_XRS = 4096 + 32;
constexpr int S_XR = 0;
constexpr int S_RC = S_CH * S_XRS;
constexpr int S_RCS = 4192;
constexpr int S_KD = S_RC + 4 * S_RCS;
constexpr int S_K = S_KD + 48 * 128;
constexpr int S_KROW = 96;
constexpr int S_TAB = S_K + 79 * S_KROW;
constexpr int S_TROW = 544;
constexpr int S_RED = S_TAB + 57 * S_TROW;
constexpr int S_TOTAL = S_RED + 4 * 64 * 144;
static_assert(S_RC % 16 == 0 && S_TAB % 16 == 0 && S_RED % 16 == 0, "");
DI f32x2 crot(f32x2 z) { return (f32x2){-z.y, z.x}; }
DI f32x2 cmul(f32x2 a, f32x2 z) { return __builtin_elementwise_fma(a.yy, crot(z), a.xx * z); }
DI f32x2 cexp_p(float ar, float ai, float p) {
    const float x = ai * p;
    const float hi = x * 0.15915494309189535f;
    const float lo = __builtin_fmaf(x, 0.15915494309189535f, -hi) + x * 6.420638316725915e-9f;
    const float rev = (hi - __builtin_rintf(hi)) + lo;
    const float m = __builtin_amdgcn_exp2f(ar * p * 1.4426950408889634f);
    return (f32x2){m * __builtin_amdgcn_cosf(rev), m * __builtin_amdgcn_sinf(rev)};
}
#define FENCE() __builtin_amdgcn_sched_barrier(0)
#define TT(c, i)
DI void role_s4d(const Args& a, LAS char* lds, unsigned lds_base, int task, rsrc_t rw, int& budget) {
    const int tid = threadIdx.x, lane = tid & 63, w = __builtin_amdgcn_readfirstlane(tid >> 6);
    const int d0 = task * S_CH;
    touch(w == 0 ? a.lAr : w == 1 ? a.Aim : w == 2 ? a.Cr : a.Ci, min(d0 * (NS * 2) + min(lane, 2 * S_CH + 1) * 128, DM * NS * 2 - 4), lds_base);
    touch(w & 1 ? a.ldt : a.Dsk, min(lane * 128, DM * 2 - 4), lds_base);
#define S4S(i)
    {
        const rsrc_t rx = __builtin_amdgcn_make_buffer_rsrc((void*)a.x, 0, SEQ * DM * 2, 0x00020000);
        const unsigned sh = (d0 & 1) * 16;
        u32x4 v[8]; unsigned e[8];
#pragma unroll
        for (int i = 0; i < 8; ++i) {
            const int o = (tid + 256 * i) * (DM * 2) + (d0 & ~1) * 2;
            v[i] = __builtin_amdgcn_raw_buffer_load_b128(rx, o, 0, 0); e[i] = __builtin_amdgcn_raw_buffer_load_b32(rx, o, 16, 0);
        }
#pragma unroll
        for (int p = 0; p < 2; ++p) {
            const int tok = 32 * task + 8 * w + 4 * p + (lane >> 4);
            float acc = 0.f;
#pragma unroll
            for (int i = 0; i < 5; ++i) {
                const int ch = (lane & 15) + 16 * i;
                const u32x4 gv = ch < 72 ? *(const u32x4*)(a.gw + ch * 8) : (u32x4){0u, 0u, 0u, 0u};
                const u32x4 xv = *(const u32x4*)(a.x + (size_t)tok * DM + min(ch, 71) * 8);
#pragma unroll
                for (int j = 0; j < 4; ++j) acc += bflo(xv[j]) * bflo(gv[j]) + bfhi(xv[j]) * bfhi(gv[j]);
            }
            acc += __shfl_xor(acc, 1); acc += __shfl_xor(acc, 2); acc += __shfl_xor(acc, 4); acc += __shfl_xor(acc, 8);
            st32(__float_as_uint(1.0f / (1.0f + __expf(-acc))), rw, (lane & 15) == 0 ? OFF_GT + tok * 4 : OOB, 0);
        }
#pragma unroll
        for (int i = 0; i < 8; ++i) {
            const unsigned x5[5] = {__builtin_amdgcn_alignbit(v[i].y, v[i].x, sh), __builtin_amdgcn_alignbit(v[i].z, v[i].y, sh), __builtin_amdgcn_alignbit(v[i].w, v[i].z, sh),
                                    __builtin_amdgcn_alignbit(e[i], v[i].w, sh), e[i] >> sh};
#pragma unroll
            for (int c = 0; c < S_CH; ++c) *(LAS u16*)(lds + S_XR + c * S_XRS + (2047 - tid - 256 * i) * 2) = (u16)(x5[c >> 1] >> (16 * (c & 1)));
        }
    }
    if (tid < 8 * S_CH) *(LAS unsigned*)(lds + S_XR + (tid >> 3) * S_XRS + 4096 + (tid & 7) * 4) = 0;
    if (tid < 64) *(LAS unsigned*)(lds + S_RC + (tid >> 4) * S_RCS + 4096 + (tid & 15) * 4) = 0;
    for (int i = tid; i < 15 * S_KROW / 4; i += 256) *(LAS unsigned*)(lds + S_K + i * 4) = 0;
    int kd[8];
#pragma unroll
    for (int j = 0; j < 8; ++j) {
        const int p = (int)bf2f(f2bf((float)(8 * tid + j))), k = p >> 7, r = p & 7;
        const int row = p < 256 ? 8 * k + r : (p < 512 ? 8 + 4 * k + (r >> 1) : (p < 1024 ? 16 + 2 * k + (r >> 2) : 24 + k));
        kd[j] = S_KD + (row * 16 + ((p >> 3) & 15)) * 8;
    }
    const int n = tid & 127, nn = lane & 15, g = lane >> 4;
    u16 pA = a.lAr[d0 * NS + n], pI = a.Aim[d0 * NS + n], pCr = a.Cr[d0 * NS + n], pCi = a.Ci[d0 * NS + n], pT = a.ldt[d0], pD = a.Dsk[d0];
    WG_BARRIER();
    for (int ch = 0; ch < S_CH; ++ch) {
        S4S(0)
        const float dsk = bf2f(pD);
        {
            const u32x4 v = *(LAS u32x4*)(lds + S_XR + ch * S_XRS + tid * 16);
            const u32x2 e = *(LAS u32x2*)(lds + S_XR + ch * S_XRS + tid * 16 + 16);
            const unsigned s01 = __builtin_amdgcn_alignbit(v.y, v.x, 16), s12 = __builtin_amdgcn_alignbit(v.z, v.y, 16), s23 = __builtin_amdgcn_alignbit(v.w, v.z, 16);
            const unsigned s34 = __builtin_amdgcn_alignbit(e.x, v.w, 16), s45 = __builtin_amdgcn_alignbit(e.y, e.x, 16);
            *(LAS u32x4*)(lds + S_RC + tid * 16) = v;
            *(LAS u32x4*)(lds + S_RC + S_RCS + tid * 16) = (u32x4){s01, s12, s23, s34};
            *(LAS u32x4*)(lds + S_RC + 2 * S_RCS + tid * 16) = (u32x4){v.y, v.z, v.w, e.x};
            *(LAS u32x4*)(lds + S_RC + 3 * S_RCS + tid * 16) = (u32x4){s12, s23, s34, s45};
        }
        S4S(1)
        const float dt = bf2f(f2bf(expf(bf2f(pT))));
        const int sc = 140 - (wave_max32(max(pCr & 0x7FFF, pCi & 0x7FFF)) >> 7);
        {
            const float ar = -bf2f(f2bf(expf(bf2f(pA)))) * dt, ai = bf2f(pI) * dt;
            const f32x2 C = {__builtin_amdgcn_ldexpf(bf2f(pCr), sc), __builtin_amdgcn_ldexpf(bf2f(pCi), sc)};
            const int dn = min(d0 + ch + 1, DM - 1);
            pA = a.lAr[dn * NS + n]; pI = a.Aim[dn * NS + n]; pCr = a.Cr[dn * NS + n]; pCi = a.Ci[dn * NS + n]; pT = a.ldt[dn]; pD = a.Dsk[dn];
            const int to = S_TAB + n * 4;
            auto put = [&](int row, f32x2 v) { *(LAS f16x2*)(lds + to + row * S_TROW) = __builtin_convertvector(v, f16x2); };
            const f32x2 z128 = cexp_p(ar, ai, 128.f);
            if (w < 2) {
                f32x2 E[8], G[4];
                E[1] = cexp_p(ar, ai, 1.f); G[0] = C;
#pragma unroll
                for (int r = 2; r < 8; ++r) E[r] = cmul(E[r >> 1], E[r - (r >> 1)]);
#pragma unroll
                for (int k = 1; k < 4; ++k) G[k] = cmul(G[k - 1], z128);
#pragma unroll
                for (int k = 0; k < 4; ++k)
#pragma unroll
                    for (int r = 0; r < 8; r += 1 + (k >> 1)) put(k < 2 ? 8 * k + r : 8 + 4 * k + (r >> 1), r ? cmul(E[r], G[k]) : G[k]);
            } else {
                const f32x2 z4 = cexp_p(ar, ai, 4.f), z8 = cexp_p(ar, -ai, 8.f);
                f32x2 G = cmul(C, cexp_p(ar, ai, 512.f)), B = z8;
                put(41, (f32x2){1.f, 0.f});
#pragma unroll
                for (int k = 1; k < 16; ++k) {
                    put(41 + k, B); B = cmul(B, z8);
                    if (k < 5) { put(22 + 2 * k, G); put(23 + 2 * k, cmul(G, z4)); }
                    else if (k < 14) put(27 + k, G);
                    G = cmul(G, z128);
                }
            }
        }
        WG_BARRIER();
        S4S(2)
        {
            const int ko = (w & 1) * 256 + g * 16, t0 = w & 2;
            u32x4 av[2][4], bv[4];
#pragma unroll
            for (int j = 0; j < 4; ++j) {
                bv[j] = *(LAS u32x4*)(lds + S_TAB + (41 + nn) * S_TROW + ko + j * 64);
                av[0][j] = *(LAS u32x4*)(lds + S_TAB + min(16 * t0 + nn, 40) * S_TROW + ko + j * 64);
                av[1][j] = *(LAS u32x4*)(lds + S_TAB + (16 + nn) * S_TROW + ko + j * 64);
            }
            f32x4 acc[2] = {};
#pragma unroll
            for (int j = 0; j < 8; ++j) acc[j & 1] = __builtin_amdgcn_mfma_f32_16x16x32_f16(__builtin_bit_cast(f16x8, av[j & 1][j >> 1]), __builtin_bit_cast(f16x8, bv[j >> 1]), acc[j & 1], 0, 0, 0);
#pragma unroll
            for (int r = 0; r < 8; ++r) *(LAS float*)(lds + S_KD + (((r < 4 ? 16 * t0 : 16) + 4 * g + (r & 3)) * 16 + nn) * 8 + (w & 1) * 4) = __builtin_amdgcn_ldexpf(acc[r >> 2][r & 3] * dt, 1 - sc);
        }
        WG_BARRIER();
        S4S(3)
        {
            float kv[8];
#pragma unroll
            for (int j = 0; j < 8; ++j) { const f32x2 t = *(LAS f32x2*)(lds + kd[j]); kv[j] = t.x + t.y; }
            *(LAS u32x4*)(lds + S_K + (15 + (tid >> 2)) * S_KROW + (tid & 3) * 16) = (u32x4){pack2(kv[0], kv[1]), pack2(kv[2], kv[3]), pack2(kv[4], kv[5]), pack2(kv[6], kv[7])};
        }
        WG_BARRIER();
        S4S(4)
        {
            const int st0 = 2047 - nn + 8 * g - 32 * w, cc = st0 & 3;
            const int ub = S_RC + cc * S_RCS + (st0 - cc) * 2 - 3872, kb = S_K + (3 + nn - w) * S_KROW + g * 16;
            f32x4 y[4][2] = {};
            u32x2 U[2][16]; u32x4 F[2][4];
            auto rd = [&](int i, int f) {
                if (f < 4) F[i & 1][f] = *(LAS u32x4*)(lds + kb + (12 - 4 * i + 16 * f) * S_KROW);
                else U[i & 1][f - 4] = *(LAS u32x2*)(lds + ub + 3872 - 256 * i - 1024 * ((f - 4) >> 2) - 32 * ((f >> 1) & 1) + 8 * (f & 1));
            };
#pragma unroll
            for (int f = 0; f < 20; ++f) { rd(0, f); FENCE(); }
#pragma unroll
            for (int i = 0; i < 4; ++i) {
                int c = 0;
#pragma unroll
                for (int q = 0; q < 4; ++q)
#pragma unroll
                    for (int T = q; T < 4; ++T)
#pragma unroll
                        for (int bt = 0; bt < 2; ++bt) {
                            y[T][bt] = MFMA16(__builtin_shufflevector(U[i & 1][4 * q + 2 * bt], U[i & 1][4 * q + 2 * bt + 1], 0, 1, 2, 3), F[i & 1][T - q], y[T][bt]);
                            if (i < 3) rd(i + 1, c);
                            ++c;
                            FENCE();
                        }
            }
#pragma unroll
            for (int T = 0; T < 4; ++T)
#pragma unroll
                for (int bt = 0; bt < 2; ++bt) *(LAS f32x4*)(lds + S_RED + (w * 64 + 16 * T + nn) * 144 + bt * 64 + g * 16) = y[T][bt];
        }
        WG_BARRIER();
        S4S(5)
        {
            f32x4 y0 = {0, 0, 0, 0}, y1 = y0;
#pragma unroll
            for (int ww = 0; ww < 4; ++ww) {
                const int o = S_RED + (ww * 64 + (tid >> 2)) * 144 + (tid & 3) * 32;
                y0 += *(LAS f32x4*)(lds + o); y1 += *(LAS f32x4*)(lds + o + 16);
            }
            const u32x4 u = *(LAS u32x4*)(lds + S_XR + ch * S_XRS + (255 - tid) * 16);
            st128((u32x4){pack2(y0[0] + bfhi(u.w) * dsk, y0[1] + bflo(u.w) * dsk), pack2(y0[2] + bfhi(u.z) * dsk, y0[3] + bflo(u.z) * dsk),
                          pack2(y1[0] + bfhi(u.y) * dsk, y1[1] + bflo(u.y) * dsk), pack2(y1[2] + bfhi(u.x) * dsk, y1[3] + bflo(u.x) * dsk)}, rw, tid * 16, OFF_BO + (d0 + ch) * BO_ROW);
        }
        S4S(6)
    }
    wait_init(rw, launch_tag(a), budget);
    VM_DRAIN();
    WG_BARRIER();
    if (tid == 0) st32(1u, rw, F_S4 + task * 4, 0);
}

constexpr int XT_BYTES = 32 * 1152;
constexpr int P_XT = 0;
constexpr int P_STG = 2 * XT_BYTES;
struct WSlice { u32x4 f[36]; };
constexpr int P_RING = 5;
template <class E, class F> DI void load_wslices(WSlice& W0, WSlice& W1, const u16* s0, const u16* s1, int w, int lane, LAS char* lds, unsigned lds_base, E also, F got) {
    constexpr int NB = 18;
    const int r0 = 8 * (lane >> 5) + ((lane & 15) >> 2);
    const int cb = (4 * w + 2 * ((lane >> 4) & 1) + ((lane & 3) >> 1)), lo8 = (lane & 1) * 8;
    auto src = [&](int i) { return (i < 9 ? s0 : s1) + (size_t)(64 * (i % 9)) * HD; };
    const u32x4 ro = ring_offs(w, lane, HD);
#pragma unroll
    for (int i = 0; i < P_RING - 1; ++i) { ring_issue(src(i), ro, lds_base + P_STG + i * 16384 + w * 4096); also(i); }
    static_for<NB>([&](auto ic) {
        constexpr int i = decltype(ic)::value, rem = NB - 1 - i < P_RING - 2 ? NB - 1 - i : P_RING - 2;
        ring_wait<4 * rem + (i < 2) * 9 + (i < 4) * 9 + (i > 0 && i < 5) * 4>();
        got(ic, ic_t<0>{});
        if constexpr (i + P_RING - 1 < NB) ring_issue(src(i + P_RING - 1), ro, lds_base + P_STG + ((i + P_RING - 1) % P_RING) * 16384 + w * 4096);
        got(ic, ic_t<1>{});
        const int sl = P_STG + (i % P_RING) * 16384;
#pragma unroll
        for (int kk = 0; kk < 4; ++kk) {
            const int ra = r0 + 16 * kk, rb = ra + 4;
            const u32x2 lo = tr_read(lds, sl + ra * 256 + ((cb ^ (2 * swz(ra))) * 16) + lo8), hi = tr_read(lds, sl + rb * 256 + ((cb ^ (2 * swz(rb))) * 16) + lo8);
            (i < 9 ? W0 : W1).f[(i % 9) * 4 + kk] = (u32x4){lo.x, lo.y, hi.x, hi.y};
        }
        got(ic, ic_t<2>{});
        u32x4* f = (i < 9 ? W0 : W1).f + (i % 9) * 4;
        if constexpr (i < 16) asm volatile("" : "+a"(f[0]), "+a"(f[1]), "+a"(f[2]), "+a"(f[3]));
    });
    static_for<3>([&](auto jc) { got(ic_t<NB>{}, jc); });
}
DI u32x4 piece_pi(const f32x16& a, int qq) {
    return (u32x4){pack2(a[4 * qq], a[4 * qq + 8]), pack2(a[4 * qq + 1], a[4 * qq + 9]), pack2(a[4 * qq + 2], a[4 * qq + 10]), pack2(a[4 * qq + 3], a[4 * qq + 11])};
}
DI u32x4 piece_seq(const f32x16& a, int s2) {
    return (u32x4){pack2(a[8 * s2], a[8 * s2 + 1]), pack2(a[8 * s2 + 2], a[8 * s2 + 3]), pack2(a[8 * s2 + 4], a[8 * s2 + 5]), pack2(a[8 * s2 + 6], a[8 * s2 + 7])};
}
struct XTile {
    int xs[9], xa[4];
    DI void init(int w, int lane) {
#pragma unroll
        for (int i = 0; i < 9; ++i) { const int L = (i * 4 + w) * 64 + lane, r = L / 72, cs = L - 72 * r; xs[i] = r * 1152 + (cs ^ ((r >> 1) & 7)) * 16; }
        const int c32 = lane & 31, hf = lane >> 5;
#pragma unroll
        for (int j = 0; j < 4; ++j) xa[j] = P_XT + c32 * 1152 + (((2 * j + hf) ^ ((c32 >> 1) & 7)) * 16);
    }
    DI void dma(const Args& a, unsigned lds_base, int w, int s, int buf, int i) const {
        glds16((const char*)a.x + (size_t)min(s, NSEG - 1) * XT_BYTES, xs[i], lds_base + P_XT + buf * XT_BYTES + (i * 4 + w) * 1024);
    }
    DI void first(const Args& a, unsigned lds_base, int w, int lane, int s) {
        init(w, lane);
#pragma unroll
        for (int i = 0; i < 9; ++i) dma(a, lds_base, w, s, 0, i);
    }
};
template <class F> DI void project(f32x16& acc0, f32x16& acc1, const WSlice& W0, const WSlice& W1, const XTile& X, const Args& a, LAS char* lds, unsigned lds_base, int w, int buf, int sn, F gap) {
    const int bo = buf * XT_BYTES;
    constexpr int PD = 5;
    u32x4 xf[PD];
#pragma unroll
    for (int i = 0; i < PD; ++i) xf[i] = *(LAS u32x4*)(lds + X.xa[i & 3] + bo + (i >> 2) * 128);
    static_for<36>([&](auto ic) {
        constexpr int ks = decltype(ic)::value;
        if (ks == 0) asm volatile("v_mfma_f32_32x32x16_bf16 %0, %1, %2, 0" : "=&v"(acc0) : "a"(W0.f[0]), "v"(xf[0]) : "memory");
        else asm volatile("v_mfma_f32_32x32x16_bf16 %0, %1, %2, %0" : "+v"(acc0) : "a"(W0.f[ks]), "v"(xf[ks % PD]) : "memory");
        if (ks < 9) X.dma(a, lds_base, w, sn, buf ^ 1, ks);
        gap(ic_t<2 * ks>{});
        if (ks == 0) asm volatile("v_mfma_f32_32x32x16_bf16 %0, %1, %2, 0" : "=&v"(acc1) : "a"(W1.f[0]), "v"(xf[0]) : "memory");
        else if (ks < 28) asm volatile("v_mfma_f32_32x32x16_bf16 %0, %1, %2, %0" : "+v"(acc1) : "a"(W1.f[ks]), "v"(xf[ks % PD]) : "memory");
        else asm volatile("v_mfma_f32_32x32x16_bf16 %0, %1, %2, %0" : "+v"(acc1) : "v"(W1.f[ks]), "v"(xf[ks % PD]) : "memory");
        if (ks + PD < 36) xf[ks % PD] = *(LAS u32x4*)(lds + X.xa[(ks + PD) & 3] + bo + ((ks + PD) >> 2) * 128);
        gap(ic_t<2 * ks + 1>{});
    });
    asm volatile("s_nop 7\n\ts_nop 3" : "+v"(acc0), "+v"(acc1) :: "memory");
}
#define KV_T 12
#define KV_F 22
DI void role_kv(const Args& a, LAS char* lds, unsigned lds_base, int h, rsrc_t rw, int& budget) {
    const int tid = threadIdx.x, lane = tid & 63, w = __builtin_amdgcn_readfirstlane(tid >> 6);
    WSlice W0, W1;
    XTile X;
    const int oo = h * 8192 + w * 2048 + lane * 16;
    const int fo = lane == 0 ? F_KV + h * 512 + w * 128 : OOB;
    u32x4 xf[6], P[4] = {};
    auto frag = [&](int buf, int ks) { xf[ks % 6] = *(LAS u32x4*)(lds + X.xa[ks & 3] + buf * XT_BYTES + (ks >> 2) * 128); };
    auto cvt = [&](const f32x16& k, const f32x16& v, int i) {
#pragma unroll
        for (int j = 0; j < 4; ++j) asm volatile("v_cvt_pk_bf16_f32 %0, %1, %2" : "=v"(P[i][j]) : "v"(i < 2 ? k[4 * i + j] : v[8 * i - 16 + 2 * j]), "v"(i < 2 ? k[4 * i + j + 8] : v[8 * i - 15 + 2 * j]));
    };
    auto put = [&](int o, int s, int i) { st128(P[i], rw, o + (i & 1) * 1024, (i < 2 ? OFF_KF : OFF_VF) + s * (NH * 8192)); };
    auto now_ = [&](f32x16& k, f32x16& v, int s) {
        asm volatile("s_nop 7\n\ts_nop 3" : "+v"(k), "+v"(v) :: "memory");
#pragma unroll
        for (int i = 0; i < 4; ++i) { cvt(k, v, i); put(oo, s, i); }
    };
    auto seg = [&](f32x16& k, f32x16& v, const f32x16& pk, const f32x16& pv, int s, int buf) {
        const int o = s > 2 ? oo : OOB;
#pragma unroll
        for (int ks = 0; ks < 36; ++ks) {
            if (ks == 0) asm volatile("v_mfma_f32_32x32x16_bf16 %0, %1, %2, 0" : "=&v"(k) : "a"(W0.f[0]), "v"(xf[0]) : "memory");
            else asm volatile("v_mfma_f32_32x32x16_bf16 %0, %1, %2, %0" : "+v"(k) : "a"(W0.f[ks]), "v"(xf[ks % 6]) : "memory");
            if (ks >= 1 && ks < 5) cvt(pk, pv, ks - 1);
            if (ks >= 5 && ks < 13 && (ks & 1)) put(o, s - 1, (ks - 5) >> 1);
            if (ks == KV_T && !buf && s == 2) { asm volatile("s_waitcnt vmcnt(8)" ::: "memory"); st32(1u, rw, fo, 0); }
            if (ks == 14 && (unsigned)(s - 4) < 8) {
                const int i = s - 4, t = opaque_lane() + 64 * w;
                st128((u32x4){SENT, SENT, SENT, SENT}, rw, i < 4 ? (t >> 7) * 65536 + (t & 127) * 16 : t * 16,
                      i < 4 ? (i & 2 ? OFF_VM : OFF_KM) + (FAST + 2 * (i & 1)) * 65536 + h * 2048 : OFF_OF + (LATE - 2 + (i >> 1)) * 262144 + h * 8192 + (i & 1) * 4096);
            }
            if (ks == KV_F) { VM_DRAIN(); st32((unsigned)(s - 1), rw, fo, 0); }
            if (ks == KV_F + 2 && (unsigned)(s - 52) < 10) {
                const int l = opaque_lane();
                if (s < 61) touch(a.Wq + (h & ~1) * DH, ((s - 52) * 64 + w * 16 + (l >> 2)) * (HD * 2) + (l & 3) * 128, lds_base);
                else bdma(rw, (l & 15) * 16, F_S4, lds_base + P_STG + w * 4096);
            }
            if (ks == 0) asm volatile("v_mfma_f32_32x32x16_bf16 %0, %2, %1, 0" : "=&v"(v) : "a"(W1.f[0]), "v"(xf[0]) : "memory");
            else if (ks < 28) asm volatile("v_mfma_f32_32x32x16_bf16 %0, %2, %1, %0" : "+v"(v) : "a"(W1.f[ks]), "v"(xf[ks % 6]) : "memory");
            else asm volatile("v_mfma_f32_32x32x16_bf16 %0, %2, %1, %0" : "+v"(v) : "v"(W1.f[ks]), "v"(xf[ks % 6]) : "memory");
            if (ks == 30) WG_BARRIER();
            frag(buf ^ (ks >= 30), (ks + 6) % 36);
            if (ks < 4) X.dma(a, lds_base, w, s + 1, buf ^ 1, ks + 5);
            if (ks > 30) X.dma(a, lds_base, w, s + 2, buf, ks - 31);
        }
    };
    f32x16 k0 = {}, v0 = {}, k1 = {}, v1 = {};
    u32x2 tag, in;
    u32x4 xa[2][4], xb[2][4];
    auto mm = [&](f32x16& k, f32x16& v, int t, int i, int j) {
        if (t) {}
        else if (i >= 16) asm volatile("s_nop 1\n\tv_mfma_f32_32x32x16_bf16 %0, %2, %1, %0" : "+v"(v) : "v"(W1.f[4 * i % 36 + j]), "v"(xa[t][j]) : "memory");
        else if (i >= 9) asm volatile("s_nop 1\n\tv_mfma_f32_32x32x16_bf16 %0, %2, %1, %0" : "+v"(v) : "a"(W1.f[4 * i % 36 + j]), "v"(xa[t][j]) : "memory");
        if (i > 1 + 2 * t && i < 11 + 2 * t) asm volatile("s_nop 1\n\tv_mfma_f32_32x32x16_bf16 %0, %1, %2, %0" : "+v"(k) : "a"(W0.f[4 * i % 36 + j]), "v"(xa[t][j]) : "memory");
    };
    load_wslices(W0, W1, a.Wk + h * DH, a.Wv + h * DH, w, lane, lds, lds_base, [&](int i) {
        if (i == 1) X.first(a, lds_base, w, lane, 0);
        if (i == 3)
#pragma unroll
            for (int j = 0; j < 9; ++j) X.dma(a, lds_base, w, 1, 1, j);
    }, [&](auto ic, auto jc) {
        constexpr int i = decltype(ic)::value - 1, part = decltype(jc)::value;
        mm(k0, v0, 0, i, part); mm(k1, v1, 1, i, part);
        if (part < 2) return;
        if (i < 0)
#pragma unroll
            for (int j = 0; j < 4; ++j) { P[j] = (u32x4){SENT, SENT, SENT, SENT}; put(oo, 0, j); }
#pragma unroll
        for (int j = 0; j < 8; ++j) if (i > (j >> 2) * 2 && i < 17 - 5 * (j >> 2)) xb[j >> 2][j & 3] = *(LAS u32x4*)(lds + X.xa[j & 3] + (j >> 2) * XT_BYTES + ((i + 1) % 9) * 128);
        mm(k0, v0, 0, i, 3); mm(k1, v1, 1, i, 3);
#pragma unroll
        for (int j = 0; j < 8; ++j) if (i > (j >> 2) * 2) xa[j >> 2][j & 3] = xb[j >> 2][j & 3];
        if (i == 4) { tag = launch_tag(a); __builtin_amdgcn_raw_buffer_store_b64(tag, rw, lane == 0 ? F_INIT + (33 + 4 * h + w) * 128 : OOB, 0, 16); }
        if (i == 10) asm volatile("buffer_load_dwordx2 %0, %1, %2, 0 offen sc1" : "=v"(in) : "v"(init_at(0)), "s"(rw) : "memory");
        if (i == 11) { cvt(k0, v0, 0); put(oo, 0, 0); cvt(k0, v0, 1); put(oo, 0, 1); }
    });
#pragma unroll
    for (int i = 0; i < 6; ++i) frag(1, i);
    asm volatile("s_nop 7\n\ts_nop 3" : "+v"(v0) :: "memory");
    cvt(k0, v0, 2); put(oo, 0, 2); cvt(k0, v0, 3); put(oo, 0, 3);
    WG_BARRIER();
#pragma unroll
    for (int ks = 0; ks < 36; ++ks) {
        if (ks == 0) asm volatile("v_mfma_f32_32x32x16_bf16 %0, %2, %1, 0" : "=&v"(v1) : "a"(W1.f[0]), "v"(xf[0]) : "memory");
        else if (ks < 28) asm volatile("v_mfma_f32_32x32x16_bf16 %0, %2, %1, %0" : "+v"(v1) : "a"(W1.f[ks]), "v"(xf[ks % 6]) : "memory");
        else asm volatile("v_mfma_f32_32x32x16_bf16 %0, %2, %1, %0" : "+v"(v1) : "v"(W1.f[ks]), "v"(xf[ks % 6]) : "memory");
        if (ks < 18 && !(ks & 1)) X.dma(a, lds_base, w, 2, 0, ks >> 1);
        if (ks == 19 || ks == 22) { cvt(k1, v1, ks == 22); put(oo, 1, ks == 22); }
        if (ks == 30) ring_wait<2>();
        frag(ks < 30, (ks + 6) % 36);
        if (ks > 30) X.dma(a, lds_base, w, 3, 1, ks - 31);
    }
    asm volatile("s_nop 7\n\ts_nop 3" : "+v"(v1), "+v"(in) :: "memory");
    cvt(k1, v1, 2); put(oo, 1, 2); cvt(k1, v1, 3); put(oo, 1, 3);
    if (!init_ok(in, tag)) wait_init(rw, tag, budget);
    for (int s = 2; s < NSEG; s += 2) { seg(k0, v0, k1, v1, s, 0); seg(k1, v1, k0, v0, s + 1, 1); }
    now_(k1, v1, NSEG - 1);
    if (!__all(*(volatile LAS int*)(lds + P_STG + w * 4096 + lane * 4) >= 1)) wait_prog(rw, F_S4 + lane * 4, true, 1, budget);
}

DI int prog_min(rsrc_t rw, int off) {
    const int v = (int)ld32(rw, off, 0);
    asm volatile("" ::: "memory");
    return -wave_max32(-v);
}
DI float red_max_g(float v) {
    auto a = __builtin_amdgcn_permlane16_swap(__float_as_uint(v), __float_as_uint(v), false, false);
    v = fmaxf(__uint_as_float(a[0]), __uint_as_float(a[1]));
    auto b = __builtin_amdgcn_permlane32_swap(__float_as_uint(v), __float_as_uint(v), false, false);
    return fmaxf(__uint_as_float(b[0]), __uint_as_float(b[1]));
}
#define M16 "v_mfma_f32_16x16x32_bf16 "
DI void role_qa(const Args& a, LAS char* lds, unsigned lds_base, int h0, int s0, int s1, rsrc_t rw, int& budget) {
    const int tid = threadIdx.x, lane = tid & 63, w = __builtin_amdgcn_readfirstlane(tid >> 6);
    const int n = lane & 15, g = lane >> 4, h = h0 + (w >> 1), tt = w & 1;
    WSlice W0, W1;
    XTile X;
    const int oo = OFF_OF + (2 * tt + (n >> 3)) * 65536 + h * 2048 + g * 128 + (n & 7) * 16;
    const int fk = F_KV + h * 512 + (lane & 3) * 128, fm = F_KVM + h * 256 + (lane & 1) * 128;
    int pm_, m_rdy = -1, kv_rdy = -1;
    auto look = [&]() { pm_ = (int)ld32(rw, lane & 4 ? fk : fm, 0); };
    auto seen = [&]() { auto rl = [&](int i) { return __builtin_amdgcn_readlane(pm_, i); }; m_rdy = min(rl(0), rl(1)); kv_rdy = min(min(rl(4), rl(5)), min(rl(6), rl(7))); };
    f32x16 q0 = {}, q1 = {};
    u32x4 xa[4], xb[4];
    auto mm = [&](int i, int j) {
        if (i >= 16) asm volatile("s_nop 1\n\tv_mfma_f32_32x32x16_bf16 %0, %1, %2, %0" : "+v"(q1) : "v"(W1.f[4 * i % 36 + j]), "v"(xa[j]) : "memory");
        else if (i >= 9) asm volatile("s_nop 1\n\tv_mfma_f32_32x32x16_bf16 %0, %1, %2, %0" : "+v"(q1) : "a"(W1.f[4 * i % 36 + j]), "v"(xa[j]) : "memory");
        if (i > 1 && i < 11) asm volatile("s_nop 1\n\tv_mfma_f32_32x32x16_bf16 %0, %1, %2, %0" : "+v"(q0) : "a"(W0.f[4 * i % 36 + j]), "v"(xa[j]) : "memory");
    };
    load_wslices(W0, W1, a.Wq + h0 * DH, a.Wq + (h0 + 1) * DH, w, lane, lds, lds_base, [&](int i) {
        if (i == 1) X.first(a, lds_base, w, lane, s0);
        if (i == 3)
#pragma unroll
            for (int j = 0; j < 9; ++j) X.dma(a, lds_base, w, s0 + 2, 1, j);
    }, [&](auto ic, auto jc) {
        constexpr int i = decltype(ic)::value - 1, part = decltype(jc)::value;
        mm(i, part);
        if (i < 0 && part == 0) st32(NSEG, rw, lane == 0 && s0 > FAST ? F_KV + (h0 + (s0 & 1)) * 512 + w * 128 : OOB, 0);
        if (part < 2) return;
#pragma unroll
        for (int j = 0; j < 4; ++j) {
            if (i < 0) st128((u32x4){SENT, SENT, SENT, SENT}, rw, oo + j * 512, s1 * 262144);
            if (i > 0 && i < 17) xb[j] = *(LAS u32x4*)(lds + X.xa[j] + ((i + 1) % 9) * 128);
        }
        mm(i, 3);
        if (i == 13) look();
#pragma unroll
        for (int j = 0; j < 4; ++j) if (i > 0) xa[j] = xb[j];
    });
    asm volatile("s_nop 7\n\ts_nop 3\n\ts_barrier" : "+v"(q0), "+v"(q1) :: "memory");
    constexpr int Q_Q = P_STG, Q_KV = Q_Q + 16384, Q_M = Q_KV + 32768;
    static_assert(Q_M + 16384 <= P_STG + P_RING * 16384, "");
    auto wait_m = [&](int thr, unsigned& t) {
        for (;;) {
            const u32x2 v = ld64(rw, fm, 0);
            asm volatile("" ::: "memory");
            t = now();
            const int p0 = __builtin_amdgcn_readlane(v.x, 0), p1 = __builtin_amdgcn_readlane(v.x, 1);
            const int d0 = (int)(__builtin_amdgcn_readlane(v.y, 0) - 8 * t) >> 3, d1 = (int)(__builtin_amdgcn_readlane(v.y, 1) - 8 * t) >> 3;
            if (min(p0, p1) >= thr || --budget < 0) return max(p0 == thr ? d0 : -1000, p1 == thr ? d1 : -1000);
            (void)wait_until(t + min(max(min(d0, d1) + (int)(a.xa >> 16 & 255), 10), 200));
        }
    };
    auto keep_q = [&](const f32x16& acc0, const f32x16& acc1) {
        const int o = Q_Q + w * 2048 + lane * 16;
        *(LAS u32x4*)(lds + o) = piece_pi(acc0, 0); *(LAS u32x4*)(lds + o + 1024) = piece_pi(acc0, 1);
        *(LAS u32x4*)(lds + o + 8192) = piece_pi(acc1, 0); *(LAS u32x4*)(lds + o + 8192 + 1024) = piece_pi(acc1, 1);
    };
    auto proj = [&](int s, auto gap) {
        f32x16 acc0, acc1;
        project(acc0, acc1, W0, W1, X, a, lds, lds_base, w, ((s - s0) >> 1) & 1, s + 2, [&](auto ic) { gap(ic); if (decltype(ic)::value == 55) look(); });
        if (s >= LATE) {
            const int o = OFF_OF + h0 * 8192 + w * 2048 + lane * 16;
            acc0 *= __uint_as_float(0x3e0293eeu); acc1 *= __uint_as_float(0x3e0293eeu);
            st128(piece_pi(acc0, 0), rw, o, s * 262144); st128(piece_pi(acc0, 1), rw, o + 1024, s * 262144);
            st128(piece_pi(acc1, 0), rw, o + 8192, s * 262144); st128(piece_pi(acc1, 1), rw, o + 8192 + 1024, s * 262144);
            return;
        }
        keep_q(acc0, acc1);
    };
    const int fs = (tt ? OFF_VF : OFF_KF) + (g >> 1) * 1024 + ((g & 1) * 32 + 16 * (n >> 3) + (n & 7)) * 16;
    auto fetch = [&](int s) {
        const int so = (min(s, NSEG - 1) * NH + h) * 8192;
#pragma unroll
        for (int f = 0; f < 8; ++f) bdma(rw, fs, so + (f >> 1) * 2048 + (f & 1) * 128, lds_base + Q_KV + w * 8192 + f * 1024);
    };
    const int ms = OFF_KM + (lane & 7) * 256 + (lane >> 3) * 16;
    auto fetch_m = [&](int s) {
        const int so = (s * NH + h) * 2048;
        const unsigned d = lds_base + Q_M + w * 4096;
        bdma(rw, ms, so, d); bdma(rw, ms, so + 128, d + 1024); bdma(rw, lane * 16, OFF_VM + so, d + 2048); bdma(rw, lane * 16, OFF_VM + so + 1024, d + 3072);
    };
    const float c = 0.08838834764831845f * 1.4426950408889634f;
    u32x4 qf[4];
    f32x4 o[8]; float mx, l;
    auto xpart = [&]() {
        const int kb = Q_KV + (w >> 1) * 16384 + lane * 16;
        u32x4 kn[8], vn[8];
#pragma unroll
        for (int ks = 0; ks < 4; ++ks) qf[ks] = *(LAS u32x4*)(lds + Q_Q + (w >> 1) * 8192 + (2 * ks + (g >> 1)) * 1024 + ((g & 1) * 32 + 16 * tt + n) * 16);
#pragma unroll
        for (int f = 0; f < 8; ++f) kn[f] = *(LAS u32x4*)(lds + kb + f * 1024);
        FENCE();
#pragma unroll
        for (int f = 0; f < 8; ++f) vn[f] = *(LAS u32x4*)(lds + kb + 8192 + f * 1024);
        f32x4 s0, s1;
        asm volatile(M16 "%0, %2, %10, 0\n\t" M16 "%1, %3, %10, 0\n\t" M16 "%0, %4, %11, %0\n\t" M16 "%1, %5, %11, %1\n\t"
                     M16 "%0, %6, %12, %0\n\t" M16 "%1, %7, %12, %1\n\t" M16 "%0, %8, %13, %0\n\t" M16 "%1, %9, %13, %1\n\ts_nop 7"
                     : "=&v"(s0), "=&v"(s1) : "v"(kn[0]), "v"(kn[1]), "v"(kn[2]), "v"(kn[3]), "v"(kn[4]), "v"(kn[5]), "v"(kn[6]), "v"(kn[7]), "v"(qf[0]), "v"(qf[1]), "v"(qf[2]), "v"(qf[3]));
        float p[8];
        mx = -3.0e38f; l = 0.f;
#pragma unroll
        for (int r = 0; r < 8; ++r) { const int key = 16 * (g >> 1) + 4 * (g & 1) + (r & 3) + 8 * (r >> 2); p[r] = key <= 16 * tt + n ? (r < 4 ? s0[r & 3] : s1[r & 3]) : -3.0e38f; mx = fmaxf(mx, p[r]); }
        mx = red_max_g(mx);
#pragma unroll
        for (int r = 0; r < 8; ++r) { p[r] = __builtin_amdgcn_exp2f((p[r] - mx) * c); l += p[r]; }
        const u32x4 pf = {pack2(p[0], p[1]), pack2(p[2], p[3]), pack2(p[4], p[5]), pack2(p[6], p[7])};
        asm volatile("s_nop 1\n\t" M16 "%0, %8, %16, 0\n\t" M16 "%1, %9, %16, 0\n\t" M16 "%2, %10, %16, 0\n\t" M16 "%3, %11, %16, 0\n\t"
                     M16 "%4, %12, %16, 0\n\t" M16 "%5, %13, %16, 0\n\t" M16 "%6, %14, %16, 0\n\t" M16 "%7, %15, %16, 0\n\ts_nop 7"
                     : "=&v"(o[0]), "=&v"(o[1]), "=&v"(o[2]), "=&v"(o[3]), "=&v"(o[4]), "=&v"(o[5]), "=&v"(o[6]), "=&v"(o[7])
                     : "v"(vn[0]), "v"(vn[1]), "v"(vn[2]), "v"(vn[3]), "v"(vn[4]), "v"(vn[5]), "v"(vn[6]), "v"(vn[7]), "v"(pf));
    };
    const int fo = lane == 0 ? F_O + h * 128 + (2 * s0 + tt) * 4 : OOB;
    bool pre = false;
    auto both = [&](int s) {
        const int kb = Q_KV + (w >> 1) * 16384 + lane * 16;
        u32x4 kn[8], vn[8], pf;
        f32x4 sc[2];
        float p[8];
        int v0, v1, v2;
        asm volatile("buffer_load_dword %0, %1, %2, 0 offen sc1" : "=v"(pm_) : "v"(lane & 4 ? fk : fm), "s"(rw) : "memory");
#define PIN(...) asm volatile("" : __VA_ARGS__)
        proj(s + 4, [&](auto ic) {
            constexpr int i = decltype(ic)::value;
            if constexpr (i < 8) {
                if (!(i & 1)) qf[i >> 1] = *(LAS u32x4*)(lds + Q_Q + (w >> 1) * 8192 + (i + (g >> 1)) * 1024 + ((g & 1) * 32 + 16 * tt + n) * 16);
                kn[i] = *(LAS u32x4*)(lds + kb + i * 1024);
            }
            if constexpr (i >= 16 && i < 32 && !(i & 1)) vn[(i - 16) >> 1] = *(LAS u32x4*)(lds + kb + 8192 + (i - 16) * 512);
            if constexpr (i == 5 || i == 6) asm volatile(M16 "%0, %1, %2, 0" : "=&v"(sc[i - 5]) : "v"(kn[i - 5]), "v"(qf[0]));
            if constexpr (i > 6 && i < 13) asm volatile(M16 "%0, %1, %2, %0" : "+v"(sc[(i - 5) & 1]) : "v"(kn[i - 5]), "v"(qf[(i - 5) >> 1]));
            if constexpr (i == 15 || i == 16) {
                constexpr int r = 4 * (i - 15);
                PIN("+v"(sc[i - 15]));
#pragma unroll
                for (int j = 0; j < 4; ++j) p[r + j] = 16 * (g >> 1) + 4 * (g & 1) + j + 2 * r <= 16 * tt + n ? sc[i - 15][j] : -3.0e38f;
                PIN("+v"(p[r]), "+v"(p[r + 1]), "+v"(p[r + 2]), "+v"(p[r + 3]));
            }
            if constexpr (i == 17) { mx = fmaxf(fmaxf(fmaxf(fmaxf(p[0], p[1]), p[2]), fmaxf(fmaxf(p[3], p[4]), p[5])), fmaxf(p[6], p[7])); PIN("+v"(mx)); }
            if constexpr (i == 18) { auto x = __builtin_amdgcn_permlane16_swap(__float_as_uint(mx), __float_as_uint(mx), false, false); mx = fmaxf(__uint_as_float(x[0]), __uint_as_float(x[1])); PIN("+v"(mx)); }
            if constexpr (i == 19) { auto x = __builtin_amdgcn_permlane32_swap(__float_as_uint(mx), __float_as_uint(mx), false, false); mx = fmaxf(__uint_as_float(x[0]), __uint_as_float(x[1])); PIN("+v"(mx)); }
            if constexpr (i >= 20 && i < 24) {
                constexpr int r = 2 * (i - 20);
                PIN("+v"(p[r]), "+v"(p[r + 1]));
                p[r] = __builtin_amdgcn_exp2f((p[r] - mx) * c); p[r + 1] = __builtin_amdgcn_exp2f((p[r + 1] - mx) * c);
                PIN("+v"(p[r]), "+v"(p[r + 1]));
            }
            if constexpr (i == 24) { pf = (u32x4){pack2(p[0], p[1]), pack2(p[2], p[3]), pack2(p[4], p[5]), pack2(p[6], p[7])}; PIN("+v"(pf)); }
            if constexpr (i == 25) { l = ((p[0] + p[1]) + p[2]) + p[3]; PIN("+v"(l)); }
            if constexpr (i == 26) { l = (((l + p[4]) + p[5]) + p[6]) + p[7]; PIN("+v"(l)); }
            if constexpr (i == 27) { asm volatile("s_waitcnt vmcnt(9)" : "+v"(pm_) :: "memory"); st32((unsigned)s, rw, fo, 0); }
            if constexpr (i == 29) { seen(); pre = m_rdy >= s + 2 && kv_rdy >= s + 4; }
            if constexpr (i == 31) { v0 = pre ? ms : OOB; v1 = pre ? lane * 16 : OOB; v2 = pre ? fs : OOB; PIN("+v"(v0), "+v"(v1), "+v"(v2)); }
            if constexpr (i >= 28 && i < 36) asm volatile(M16 "%0, %1, %2, 0" : "=&v"(o[i - 28]) : "v"(vn[i - 28]), "v"(pf));
            if constexpr (i == 36) asm volatile("s_barrier" ::: "memory");
            if constexpr (i >= 38 && i < 42) bdma(rw, i < 40 ? v0 : v1, ((s + 2) * NH + h) * 2048 + (i < 40 ? (i & 1) * 128 : OFF_VM + (i & 1) * 1024), lds_base + Q_M + w * 4096 + (i - 38) * 1024);
            if constexpr (i >= 42 && i < 50) bdma(rw, v2, ((s + 4) * NH + h) * 8192 + ((i - 42) >> 1) * 2048 + (i & 1) * 128, lds_base + Q_KV + w * 8192 + (i - 42) * 1024);
        });
    };
    auto none = [](auto) {};
    keep_q(q0, q1);
    if (s0 < FAST) wait_init(rw, launch_tag(a), budget); else seen();
    if (kv_rdy < s0) wait_prog(rw, fk, true, s0, budget);
    fetch(s0);
#pragma unroll 1
    for (int p = 0; p < 2; ++p) {
        const bool x = p == (s0 > FAST);
        if (x) VM_DRAIN();
        WG_BARRIER();
        if (x) xpart(); else proj(s0 + 2, none);
    }
    for (int s = s0; s <= s1; s += 2) {
        S4S(0)
        const int mb = Q_M + w * 4096;
        u32x4 kf[4]; u32x2 vm[4];
        auto frags = [&]() {
#pragma unroll
            for (int ks = 0; ks < 4; ++ks) kf[ks] = *(LAS u32x4*)(lds + mb + (4 * ks + g) * 128 + (2 * (n >> 2) + (n & 1)) * 16);
#pragma unroll
            for (int t = 0; t < 4; ++t) { const int vo = mb + 2048 + t * 512 + (n >> 3) * 256 + g * 64 + (n & 7) * 4; vm[t] = (u32x2){*(LAS unsigned*)(lds + vo), *(LAS unsigned*)(lds + vo + 32)}; }
        };
        if (s == s1) {
            TT(true, 3)
            unsigned t;
            const int d = min(wait_m(s - 1, t) + (int)(a.xa & 255), 500);
            st32((t + max(d, 0) + (a.xb >> 8 * ((s - FAST) >> 1) & 255)) & 0x7FFFFFFFu, rw, lane == 0 ? F_H + ((2 * (s - FAST) + tt) * NH + h) * 128 : OOB, 0);
            (void)wait_until(t + d);
            TT(true, 0)
            for (;;) {
                fetch_m(s);
                VM_DRAIN();
                frags();
                unsigned c = 0;
#pragma unroll
                for (int i = 0; i < 4; ++i) c = max(max(mx4(c, kf[i]), vm[i].x), vm[i].y);
                if (!__any(c == SENT) || --budget < 0) break;
            }
        } else {
            if (!pre) {
                seen();
                unsigned t;
                if (m_rdy < s) { const int d = wait_m(s, t); (void)wait_until(t + min(d + (int)(a.xa >> 24), 300)); }
                fetch_m(s);
                if (kv_rdy < s + 2) wait_prog(rw, fk, true, s + 2, budget);
                fetch(s + 2);
            }
            S4S(1)
            asm volatile("s_waitcnt vmcnt(8)" ::: "memory");
            frags();
        }
        TT(s == s1, 1)
        S4S(2)
        f32x4 sm;
        asm volatile(M16 "%0, %1, %5, 0\n\t" M16 "%0, %2, %6, %0\n\t" M16 "%0, %3, %7, %0\n\t" M16 "%0, %4, %8, %0\n\ts_nop 7"
                     : "=&v"(sm) : "v"(kf[0]), "v"(kf[1]), "v"(kf[2]), "v"(kf[3]), "v"(qf[0]), "v"(qf[1]), "v"(qf[2]), "v"(qf[3]));
        const float m2 = red_max_g(fmaxf(mx, fmaxf(sm[0], sm[1])));
        const float al = __builtin_amdgcn_exp2f((mx - m2) * c), p0 = __builtin_amdgcn_exp2f((sm[0] - m2) * c), p1 = __builtin_amdgcn_exp2f((sm[1] - m2) * c);
        const float inv = __builtin_amdgcn_rcpf(red_sum_g(l * al + p0 + p1)), ai = al * inv;
#pragma unroll
        for (int t = 0; t < 8; ++t) o[t] *= ai;
        const unsigned pm = pack2(p0 * inv, p1 * inv);
#define M1K "v_mfma_f32_16x16x16_bf16 "
        asm volatile("s_nop 1\n\t" M1K "%0, %8, %12, %0\n\t" M1K "%1, %8, %13, %1\n\t" M1K "%2, %9, %12, %2\n\t" M1K "%3, %9, %13, %3\n\t"
                     M1K "%4, %10, %12, %4\n\t" M1K "%5, %10, %13, %5\n\t" M1K "%6, %11, %12, %6\n\t" M1K "%7, %11, %13, %7\n\ts_nop 7"
                     : "+v"(o[0]), "+v"(o[1]), "+v"(o[2]), "+v"(o[3]), "+v"(o[4]), "+v"(o[5]), "+v"(o[6]), "+v"(o[7])
                     : "v"(vm[0]), "v"(vm[1]), "v"(vm[2]), "v"(vm[3]), "v"((u32x2){pm, 0u}), "v"((u32x2){0u, pm}));
#pragma unroll
        for (int b = 0; b < 2; ++b) {
            u32x4 x, y;
#pragma unroll
            for (int j = 0; j < 4; ++j) { x[j] = pack2(o[4 * b + (j >> 1)][2 * (j & 1)], o[4 * b + (j >> 1)][2 * (j & 1) + 1]); y[j] = pack2(o[4 * b + 2 + (j >> 1)][2 * (j & 1)], o[4 * b + 2 + (j >> 1)][2 * (j & 1) + 1]); }
            if (s == LATE - 1) { const u32x4 z = {x.x, x.z, y.x, y.z}; y = (u32x4){x.y, x.w, y.y, y.w}; x = z; }
            st128(x, rw, oo + b * 1024, s * 262144); st128(y, rw, oo + b * 1024 + 512, s * 262144);
        }
        TT(s == s1, 2)
        S4S(3)
        if (s == s1) break;
        asm volatile("s_waitcnt vmcnt(4)" ::: "memory");
        WG_BARRIER();
        S4S(4)
        if (s + 4 <= s1) { both(s); S4S(5) continue; }
        xpart();
        FENCE();
        VM_DRAIN();
        st32((unsigned)s, rw, fo, 0);
        TT(true, 4)
    }
}

#define STEP_ASM \
    "v_readfirstlane_b32 s36, v0\n" \
    "v_readfirstlane_b32 s37, v1\n" \
    "v_readfirstlane_b32 s38, v2\n" \
    "v_readfirstlane_b32 s39, v3\n" \
    "v_readfirstlane_b32 s40, v4\n" \
    "v_readfirstlane_b32 s41, v5\n" \
    "v_readfirstlane_b32 s42, v6\n" \
    "v_readfirstlane_b32 s43, v7\n" \
    "v_readfirstlane_b32 s28, v8\n" \
    "v_readfirstlane_b32 s29, v9\n" \
    "v_readfirstlane_b32 s30, v10\n" \
    "v_readfirstlane_b32 s31, v11\n" \
    "v_readfirstlane_b32 s32, v12\n" \
    "v_readfirstlane_b32 s33, v13\n" \
    "v_readfirstlane_b32 s34, v14\n" \
    "v_readfirstlane_b32 s35, v15\n" \
    "v_readfirstlane_b32 s24, v16\n" \
    "v_readfirstlane_b32 s25, v17\n" \
    "v_readfirstlane_b32 s26, v18\n" \
    "v_readfirstlane_b32 s27, v19\n" \
    "v_readfirstlane_b32 s60, v24\n" \
    "v_readfirstlane_b32 s61, v25\n" \
    "v_readfirstlane_b32 s62, v26\n" \
    "v_readfirstlane_b32 s63, v27\n" \
    "v_readfirstlane_b32 s64, v28\n" \
    "v_readfirstlane_b32 s65, v29\n" \
    "v_readfirstlane_b32 s66, v30\n" \
    "v_readfirstlane_b32 s67, v31\n" \
    "s_mov_b32 s68, m0\n" \
    "s_getpc_b64 s[96:97]\n" \
    "14:\n" \
    "s_add_u32 s96, s96, 2f-14b\n" \
    "s_addc_u32 s97, s97, 0\n" \
    "s_lshl_b32 s74, s60, 10\n" \
    "s_lshl_b32 s95, s74, 1\n" \
    "s_add_i32 s95, s95, 16384\n" \
    "s_mov_b32 s81, 0x3e0293ee\n" \
    "s_mov_b32 s82, 0x53800000\n" \
    "s_lshl_b32 s83, s60, 6\n" \
    "s_and_b32 s83, s83, 128\n" \
    "v_add_u32 v109, s83, v131\n" \
    "v_sub_u32 v111, v131, s83\n" \
    "v_xor_b32 v112, 0x2000, v133\n" \
    "v_and_b32 v113, 0x3c0, v127\n" \
    "v_mad_u32_u24 v113, v113, 7, v127\n" \
    "s_bfe_u32 s83, s61, 0x2000b\n" \
    "s_and_b32 s83, s83, 3\n" \
    "v_lshl_add_u32 v113, s83, 7, v113\n" \
    "v_lshlrev_b32 v114, 1, v127\n" \
    "v_add_u32 v106, s63, v133\n" \
    "s_bfe_u32 s83, s61, 0x40009\n" \
    "s_bfe_u32 s80, s61, 0x20004\n" \
    "s_lshl2_add_u32 s83, s83, s80\n" \
    "s_mov_b64 s[72:73], 0\n" \
    "s_cmp_eq_u32 s43, 4\n" \
    "s_cbranch_scc1 20f\n" \
    "0:\n" \
    "s_and_b32 s75, s36, 3\n" \
    "s_lshl_b32 s75, s75, 16\n" \
    "s_add_i32 s69, s75, s26\n" \
    "s_xor_b32 s70, s69, 2048\n" \
    "s_andn2_b32 s71, s69, 0x1fff\n" \
    "s_add_i32 s71, s71, 32768\n" \
    "ds_read_b128 v[72:75], v133 offset:0\n" \
    "ds_read_b128 v[76:79], v133 offset:1024\n" \
    "ds_read_b128 v[80:83], v133 offset:2048\n" \
    "ds_read_b128 v[84:87], v133 offset:3072\n" \
    "ds_read_b128 v[88:91], v133 offset:16384\n" \
    "ds_read_b128 v[92:95], v133 offset:17408\n" \
    "v_mov_b64 v[40:41], 0\n" \
    "v_mov_b64 v[42:43], 0\n" \
    "v_mov_b64 v[44:45], 0\n" \
    "v_mov_b64 v[46:47], 0\n" \
    "v_mov_b64 v[48:49], 0\n" \
    "v_mov_b64 v[50:51], 0\n" \
    "v_mov_b64 v[52:53], 0\n" \
    "v_mov_b64 v[54:55], 0\n" \
    "v_mov_b64 v[56:57], 0\n" \
    "v_mov_b64 v[58:59], 0\n" \
    "v_mov_b64 v[60:61], 0\n" \
    "v_mov_b64 v[62:63], 0\n" \
    "v_mov_b64 v[64:65], 0\n" \
    "v_mov_b64 v[66:67], 0\n" \
    "v_mov_b64 v[68:69], 0\n" \
    "v_mov_b64 v[70:71], 0\n" \
    "v_add_u32 v108, s74, v127\n" \
    "v_xor_b32 v24, 0x400, v108\n" \
    "v_xor_b32 v28, 0x800, v108\n" \
    "v_xor_b32 v32, 0xc00, v108\n" \
    "v_and_b32 v22, 0x200, v127\n" \
    "s_and_b32 s76, s26, 0x800\n" \
    "v_mad_u32_u24 v22, v22, 3, v127\n" \
    "v_xor_b32 v22, s76, v22\n" \
    "s_movk_i32 m0, 0x2000\n" \
    "s_cmp_eq_u32 s36, 0\n" \
    "s_cbranch_scc0 17f\n" \
    "v_and_b32 v36, 0x380, v127\n" \
    "v_add_u32 v36, v36, v127\n" \
    "v_and_b32 v37, 0x300, v127\n" \
    "v_add_u32 v37, v37, v127\n" \
    "s_add_i32 s93, s64, 0x1000000\n" \
    "v_add_u32 v21, s95, v127\n" \
    "v_xor_b32 v17, 32, v119\n" \
    "v_xor_b32 v18, 64, v119\n" \
    "v_xor_b32 v19, 96, v119\n" \
    "v_xor_b32 v0, 0x0, v96\n" \
    "v_xor_b32 v1, 0x80, v96\n" \
    "v_xor_b32 v2, 0x20, v96\n" \
    "v_xor_b32 v3, 0xa0, v96\n" \
    "v_xor_b32 v4, 0x4000, v96\n" \
    "v_xor_b32 v5, 0x4080, v96\n" \
    "v_xor_b32 v6, 0x4020, v96\n" \
    "v_xor_b32 v7, 0x40a0, v96\n" \
    "v_xor_b32 v8, 0x8000, v96\n" \
    "v_xor_b32 v9, 0x8080, v96\n" \
    "v_xor_b32 v10, 0x8020, v96\n" \
    "v_xor_b32 v11, 0x80a0, v96\n" \
    "v_xor_b32 v12, 0xc000, v96\n" \
    "v_xor_b32 v13, 0xc080, v96\n" \
    "v_xor_b32 v14, 0xc020, v96\n" \
    "v_xor_b32 v15, 0xc0a0, v96\n" \
    "s_waitcnt vmcnt(16) lgkmcnt(0)\n" \
    "s_barrier\n" \
    "ds_read_b64_tr_b16 v[72:73], v0 offset:32768\n" \
    "ds_read_b64_tr_b16 v[74:75], v0 offset:40960\n" \
    "ds_read_b64_tr_b16 v[76:77], v1 offset:34816\n" \
    "ds_read_b64_tr_b16 v[78:79], v1 offset:43008\n" \
    "ds_read_b64_tr_b16 v[80:81], v2 offset:32768\n" \
    "ds_read_b64_tr_b16 v[82:83], v2 offset:40960\n" \
    "ds_read_b64_tr_b16 v[84:85], v3 offset:34816\n" \
    "ds_read_b64_tr_b16 v[86:87], v3 offset:43008\n" \
    "ds_read_b64_tr_b16 v[88:89], v4 offset:32768\n" \
    "ds_read_b64_tr_b16 v[90:91], v4 offset:40960\n" \
    "ds_read_b64_tr_b16 v[92:93], v5 offset:34816\n" \
    "ds_read_b64_tr_b16 v[94:95], v5 offset:43008\n" \
    "s_waitcnt lgkmcnt(8)\n" \
    "ds_read_b64_tr_b16 v[40:41], v6 offset:32768\n" \
    "ds_read_b64_tr_b16 v[42:43], v6 offset:40960\n" \
    "ds_read_b64_tr_b16 v[44:45], v7 offset:34816\n" \
    "ds_read_b64_tr_b16 v[46:47], v7 offset:43008\n" \
    "s_waitcnt lgkmcnt(8)\n" \
    "ds_read_b64_tr_b16 v[48:49], v8 offset:32768\n" \
    "ds_read_b64_tr_b16 v[50:51], v8 offset:40960\n" \
    "ds_read_b64_tr_b16 v[52:53], v9 offset:34816\n" \
    "ds_read_b64_tr_b16 v[54:55], v9 offset:43008\n" \
    "s_waitcnt lgkmcnt(8)\n" \
    "ds_read_b64_tr_b16 v[56:57], v10 offset:32768\n" \
    "ds_read_b64_tr_b16 v[58:59], v10 offset:40960\n" \
    "ds_read_b64_tr_b16 v[60:61], v11 offset:34816\n" \
    "ds_read_b64_tr_b16 v[62:63], v11 offset:43008\n" \
    "s_waitcnt lgkmcnt(8)\n" \
    "ds_read_b64_tr_b16 v[64:65], v12 offset:32768\n" \
    "ds_read_b64_tr_b16 v[66:67], v12 offset:40960\n" \
    "ds_read_b64_tr_b16 v[68:69], v13 offset:34816\n" \
    "ds_read_b64_tr_b16 v[70:71], v13 offset:43008\n" \
    "s_waitcnt lgkmcnt(8)\n" \
    "ds_read_b64_tr_b16 v[248:249], v14 offset:32768\n" \
    "ds_read_b64_tr_b16 v[250:251], v14 offset:40960\n" \
    "ds_read_b64_tr_b16 v[252:253], v15 offset:34816\n" \
    "ds_read_b64_tr_b16 v[254:255], v15 offset:43008\n" \
    "ds_read_b128 v[98:101], v21 offset:0\n" \
    "ds_read_b128 v[102:105], v21 offset:1024\n" \
    "s_waitcnt lgkmcnt(0)\n" \
    "s_barrier\n" \
    "ds_write_b128 v133, v[72:75] offset:0\n" \
    "ds_write_b128 v133, v[76:79] offset:1024\n" \
    "ds_write_b128 v133, v[80:83] offset:2048\n" \
    "ds_write_b128 v133, v[84:87] offset:3072\n" \
    "ds_write_b128 v133, v[88:91] offset:16384\n" \
    "ds_write_b128 v133, v[92:95] offset:17408\n" \
    "ds_write_b128 v133, v[40:43] offset:18432\n" \
    "ds_write_b128 v133, v[44:47] offset:19456\n" \
    "ds_write_b128 v133, v[48:51] offset:32768\n" \
    "ds_write_b128 v133, v[52:55] offset:33792\n" \
    "ds_write_b128 v133, v[56:59] offset:34816\n" \
    "ds_write_b128 v133, v[60:63] offset:35840\n" \
    "ds_write_b128 v133, v[64:67] offset:49152\n" \
    "ds_write_b128 v133, v[68:71] offset:50176\n" \
    "ds_write_b128 v133, v[98:101] offset:51200\n" \
    "s_waitcnt lgkmcnt(14)\n" \
    "ds_write_b128 v133, v[102:105] offset:52224\n" \
    "s_waitcnt vmcnt(8) lgkmcnt(0)\n" \
    "s_barrier\n" \
    "ds_read_b64_tr_b16 v[40:41], v119 offset:0\n" \
    "ds_read_b64_tr_b16 v[42:43], v18 offset:512\n" \
    "ds_read_b64_tr_b16 v[44:45], v119 offset:8192\n" \
    "ds_read_b64_tr_b16 v[46:47], v18 offset:8704\n" \
    "ds_read_b64_tr_b16 v[48:49], v17 offset:0\n" \
    "ds_read_b64_tr_b16 v[50:51], v19 offset:512\n" \
    "ds_read_b64_tr_b16 v[52:53], v17 offset:8192\n" \
    "ds_read_b64_tr_b16 v[54:55], v19 offset:8704\n" \
    "ds_read_b64_tr_b16 v[56:57], v18 offset:0\n" \
    "ds_read_b64_tr_b16 v[58:59], v119 offset:512\n" \
    "ds_read_b64_tr_b16 v[60:61], v18 offset:8192\n" \
    "ds_read_b64_tr_b16 v[62:63], v119 offset:8704\n" \
    "s_add_i32 m0, s95, 0\n" \
    "s_nop 0\n" \
    "buffer_load_dwordx4 v36, %[rw], s64 offen sc1 lds\n" \
    "s_add_i32 m0, s95, 896\n" \
    "s_nop 0\n" \
    "buffer_load_dwordx4 v36, %[rw], s64 offen offset:128 sc1 lds\n" \
    "s_add_i32 m0, s95, 8192\n" \
    "s_nop 0\n" \
    "buffer_load_dwordx4 v37, %[rw], s93 offen sc1 lds\n" \
    "s_add_i32 m0, s95, 8960\n" \
    "s_nop 0\n" \
    "buffer_load_dwordx4 v37, %[rw], s93 offen offset:256 sc1 lds\n" \
    "s_waitcnt lgkmcnt(8)\n" \
    "ds_read_b64_tr_b16 v[64:65], v19 offset:0\n" \
    "ds_read_b64_tr_b16 v[66:67], v17 offset:512\n" \
    "ds_read_b64_tr_b16 v[68:69], v19 offset:8192\n" \
    "ds_read_b64_tr_b16 v[70:71], v17 offset:8704\n" \
    "s_waitcnt vmcnt(4) lgkmcnt(0)\n" \
    "s_barrier\n" \
    "ds_read_b64_tr_b16 v[0:1], v119 offset:32768\n" \
    "ds_read_b64_tr_b16 v[2:3], v18 offset:33280\n" \
    "ds_read_b64_tr_b16 v[4:5], v119 offset:40960\n" \
    "ds_read_b64_tr_b16 v[6:7], v18 offset:41472\n" \
    "ds_write_b128 v106, v[40:43] offset:0\n" \
    "ds_write_b128 v106, v[44:47] offset:1024\n" \
    "ds_read_b64_tr_b16 v[8:9], v17 offset:32768\n" \
    "ds_read_b64_tr_b16 v[10:11], v19 offset:33280\n" \
    "ds_read_b64_tr_b16 v[12:13], v17 offset:40960\n" \
    "ds_read_b64_tr_b16 v[14:15], v19 offset:41472\n" \
    "ds_write_b128 v106, v[48:51] offset:2048\n" \
    "ds_write_b128 v106, v[52:55] offset:3072\n" \
    "s_waitcnt lgkmcnt(8)\n" \
    "ds_read_b64_tr_b16 v[40:41], v18 offset:32768\n" \
    "ds_read_b64_tr_b16 v[42:43], v119 offset:33280\n" \
    "ds_read_b64_tr_b16 v[44:45], v18 offset:40960\n" \
    "ds_read_b64_tr_b16 v[46:47], v119 offset:41472\n" \
    "s_waitcnt lgkmcnt(6)\n" \
    "ds_read_b64_tr_b16 v[48:49], v19 offset:32768\n" \
    "ds_read_b64_tr_b16 v[50:51], v17 offset:33280\n" \
    "ds_read_b64_tr_b16 v[52:53], v19 offset:40960\n" \
    "ds_read_b64_tr_b16 v[54:55], v17 offset:41472\n" \
    "ds_write_b128 v106, v[56:59] offset:4096\n" \
    "ds_write_b128 v106, v[60:63] offset:5120\n" \
    "ds_write_b128 v106, v[64:67] offset:6144\n" \
    "ds_write_b128 v106, v[68:71] offset:7168\n" \
    "s_waitcnt lgkmcnt(4)\n" \
    "s_barrier\n" \
    "ds_write_b128 v106, v[0:3] offset:32768\n" \
    "ds_write_b128 v106, v[4:7] offset:33792\n" \
    "ds_write_b128 v106, v[8:11] offset:34816\n" \
    "ds_write_b128 v106, v[12:15] offset:35840\n" \
    "ds_write_b128 v106, v[40:43] offset:36864\n" \
    "ds_write_b128 v106, v[44:47] offset:37888\n" \
    "ds_write_b128 v106, v[48:51] offset:38912\n" \
    "ds_write_b128 v106, v[52:55] offset:39936\n" \
    "v_mov_b64 v[56:57], 0\n" \
    "v_mov_b64 v[58:59], 0\n" \
    "v_mov_b64 v[60:61], 0\n" \
    "v_mov_b64 v[62:63], 0\n" \
    "v_mov_b64 v[64:65], 0\n" \
    "v_mov_b64 v[66:67], 0\n" \
    "v_mov_b64 v[68:69], 0\n" \
    "v_mov_b64 v[70:71], 0\n" \
    "s_waitcnt lgkmcnt(6)\n" \
    "ds_read_b128 v[0:3], v108 offset:0\n" \
    "ds_read_b128 v[16:19], v108 offset:4096\n" \
    "s_waitcnt lgkmcnt(2)\n" \
    "v_mov_b64 v[40:41], 0\n" \
    "v_mov_b64 v[42:43], 0\n" \
    "v_mov_b64 v[44:45], 0\n" \
    "v_mov_b64 v[46:47], 0\n" \
    "v_mov_b64 v[48:49], 0\n" \
    "v_mov_b64 v[50:51], 0\n" \
    "v_mov_b64 v[52:53], 0\n" \
    "v_mov_b64 v[54:55], 0\n" \
    "73:\n" \
    "s_waitcnt vmcnt(0) lgkmcnt(0)\n" \
    "ds_read_b128 v[4:7], v21 offset:0\n" \
    "ds_read_b128 v[8:11], v21 offset:1024\n" \
    "ds_read_b128 v[12:15], v21 offset:8192\n" \
    "ds_read_b128 v[102:105], v21 offset:9216\n" \
    "v_mov_b32 v107, 0\n" \
    "s_waitcnt lgkmcnt(3)\n" \
    "v_max3_u32 v107, v107, v4, v5\n" \
    "v_max3_u32 v107, v107, v6, v7\n" \
    "s_waitcnt lgkmcnt(2)\n" \
    "v_max3_u32 v107, v107, v8, v9\n" \
    "v_max3_u32 v107, v107, v10, v11\n" \
    "s_waitcnt lgkmcnt(1)\n" \
    "v_max3_u32 v107, v107, v12, v13\n" \
    "v_max3_u32 v107, v107, v14, v15\n" \
    "s_waitcnt lgkmcnt(0)\n" \
    "v_max3_u32 v107, v107, v102, v103\n" \
    "v_max3_u32 v107, v107, v104, v105\n" \
    "v_cmp_eq_u32 vcc, -1, v107\n" \
    "s_add_i32 s39, s39, -1\n" \
    "s_cmp_lt_i32 s39, 0\n" \
    "s_nop 2\n" \
    "s_cbranch_vccnz 75f\n" \
    "74:\n" \
    "s_barrier\n" \
    "s_memrealtime s[88:89]\n" \
    "s_pack_ll_b32_b16 s80, 1, s42\n" \
    "v_mov_b32 v5, s80\n" \
    "s_cmp_eq_u32 s60, 0\n" \
    "s_cselect_b32 s75, 0x1010101, 0\n" \
    "s_waitcnt lgkmcnt(0)\n" \
    "s_lshl3_add_u32 s80, s88, s37\n" \
    "v_mov_b32 v4, s80\n" \
    "s_mov_b32 exec_lo, s75\n" \
    "s_mov_b32 exec_hi, 0\n" \
    "buffer_store_dwordx2 v[4:5], v127, %[rs], s61 offen sc1\n" \
    "s_mov_b64 exec, -1\n" \
    "s_branch 3f\n" \
    "75:\n" \
    "s_cbranch_scc1 74b\n" \
    "s_add_i32 m0, s95, 0\n" \
    "s_nop 0\n" \
    "buffer_load_dwordx4 v36, %[rw], s64 offen sc1 lds\n" \
    "s_add_i32 m0, s95, 896\n" \
    "s_nop 0\n" \
    "buffer_load_dwordx4 v36, %[rw], s64 offen offset:128 sc1 lds\n" \
    "s_add_i32 m0, s95, 8192\n" \
    "s_nop 0\n" \
    "buffer_load_dwordx4 v37, %[rw], s93 offen sc1 lds\n" \
    "s_add_i32 m0, s95, 8960\n" \
    "s_nop 0\n" \
    "buffer_load_dwordx4 v37, %[rw], s93 offen offset:256 sc1 lds\n" \
    "s_branch 73b\n" \
    "17:\n" \
    "s_sub_i32 s37, s37, s29\n" \
    "12:\n" \
    "1:\n" \
    "s_memtime vcc\n" \
    "s_mul_i32 s90, s37, s25\n" \
    "s_ashr_i32 s90, s90, 12\n" \
    "s_add_i32 s90, s90, s24\n" \
    "s_add_i32 s90, s90, -116\n" \
    "s_waitcnt lgkmcnt(0)\n" \
    "s_sub_i32 s75, s90, vcc_lo\n" \
    "s_cmp_gt_i32 s75, 112\n" \
    "s_cbranch_scc1 1b\n" \
    "s_max_i32 s75, s75, 0\n" \
    "s_and_b32 s75, s75, -4\n" \
    "s_sub_u32 vcc_lo, s96, s75\n" \
    "s_subb_u32 vcc_hi, s97, 0\n" \
    "s_setpc_b64 vcc\n" \
    "s_nop 0\n" \
    "s_nop 0\n" \
    "s_nop 0\n" \
    "s_nop 0\n" \
    "s_nop 0\n" \
    "s_nop 0\n" \
    "s_nop 0\n" \
    "s_nop 0\n" \
    "s_nop 0\n" \
    "s_nop 0\n" \
    "s_nop 0\n" \
    "s_nop 0\n" \
    "s_nop 0\n" \
    "s_nop 0\n" \
    "s_nop 0\n" \
    "s_nop 0\n" \
    "s_nop 0\n" \
    "s_nop 0\n" \
    "s_nop 0\n" \
    "s_nop 0\n" \
    "s_nop 0\n" \
    "s_nop 0\n" \
    "s_nop 0\n" \
    "s_nop 0\n" \
    "s_nop 0\n" \
    "s_nop 0\n" \
    "s_nop 0\n" \
    "s_nop 0\n" \
    "2:\n" \
    "buffer_load_dwordx4 v[0:3], v125, %[rs], s69 offen sc1\n" \
    "s_nop 3\n" \
    "s_nop 3\n" \
    "buffer_load_dwordx4 v[16:19], v125, %[rs], s70 offen sc1\n" \
    "s_mov_b32 exec_lo, s27\n" \
    "s_mov_b32 exec_hi, s27\n" \
    "buffer_load_dwordx4 v22, %[rs], s71 offen sc1 lds\n" \
    "s_mov_b64 exec, -1\n" \
    "v_bfe_u32 v20, v127, 8, 2\n" \
    "v_subrev_u32 v20, s60, v20\n" \
    "v_mul_i32_i24 v20, -1088, v20\n" \
    "v_add_u32 v20, v20, v129\n" \
    "v_lshrrev_b32 v21, 2, v127\n" \
    "s_mov_b32 exec_lo, s66\n" \
    "s_mov_b32 exec_hi, s66\n" \
    "ds_read2_b32 v[96:97], v20 offset0:0 offset1:144\n" \
    "s_mov_b64 exec, -1\n" \
    "s_lshl_b32 s80, s36, 16\n" \
    "s_add_i32 s80, s80, s67\n" \
    "s_sub_i32 s75, s37, s79\n" \
    "s_cmp_gt_i32 s75, 24\n" \
    "s_cselect_b32 s76, 2, 0\n" \
    "s_cmp_lt_i32 s75, 10\n" \
    "s_cselect_b32 s76, -6, s76\n" \
    "s_add_i32 s28, s28, s76\n" \
    "s_max_i32 s28, s28, 0\n" \
    "s_min_i32 s28, s28, 20\n" \
    "s_cmp_eq_u32 s60, 0\n" \
    "s_cselect_b32 s72, 0x1010101, 0\n" \
    "s_waitcnt lgkmcnt(0)\n" \
    "buffer_store_dword v96, v21, %[rw], s80 offen sc1\n" \
    "buffer_store_dword v97, v21, %[rw], s80 offen offset:256 sc1\n" \
    "3:\n" \
    "s_mov_b32 m0, s95\n" \
    "s_add_i32 s91, s36, 1\n" \
    "s_and_b32 s78, s36, 3\n" \
    "s_lshl_b32 s78, s78, 14\n" \
    "s_add_i32 s77, s78, s61\n" \
    "s_bitcmp1_b32 s36, 0\n" \
    "s_cselect_b32 s79, 0x8000, 0\n" \
    "s_lshl_b32 s92, s91, 18\n" \
    "s_add_i32 s92, s92, s64\n" \
    "s_add_i32 s78, s78, 0x1a0000\n" \
    "s_add_i32 s93, s92, 0x1000000\n" \
    "s_sub_i32 s76, s33, s42\n" \
    "s_ashr_i32 s76, s76, s32\n" \
    "s_add_i32 s42, s42, s76\n" \
    "s_lshr_b32 s76, s32, 8\n" \
    "s_sub_i32 s76, s76, s40\n" \
    "s_ashr_i32 s76, s76, 2\n" \
    "s_add_i32 s40, s40, s76\n" \
    "s_sub_i32 s76, s40, s24\n" \
    "s_min_i32 s94, s91, s38\n" \
    "s_lshl_b32 s94, s94, 18\n" \
    "s_andn2_b32 s80, 0x1000000, s66\n" \
    "s_add_i32 s94, s94, s64\n" \
    "s_add_i32 s94, s94, s80\n" \
    "s_add_i32 s75, s36, 2\n" \
    "s_cmp_lt_i32 s38, s75\n" \
    "s_cselect_b32 s75, s62, 0x1b0040\n" \
    "v_mov_b64 v[98:99], 0\n" \
    "v_mov_b32 v101, 0\n" \
    "s_pack_ll_b32_b16 s80, s91, s42\n" \
    "v_mov_b32 v103, s80\n" \
    "s_waitcnt vmcnt(4)\n" \
    "v_smfmac_f32_16x16x64_bf16 v[40:43], v[0:3], a[0:7], v124\n" \
    "ds_write_b128 v108, v[0:3] offset:0\n" \
    "v_smfmac_f32_16x16x64_bf16 v[44:47], v[0:3], a[8:15], v124\n" \
    "v_smfmac_f32_16x16x64_bf16 v[48:51], v[0:3], a[128:135], v124\n" \
    "v_smfmac_f32_16x16x64_bf16 v[52:55], v[0:3], a[136:143], v124\n" \
    "s_waitcnt vmcnt(3)\n" \
    "v_smfmac_f32_16x16x64_bf16 v[40:43], v[16:19], a[64:71], v124\n" \
    "ds_write_b128 v108, v[16:19] offset:4096\n" \
    "v_smfmac_f32_16x16x64_bf16 v[44:47], v[16:19], a[72:79], v124\n" \
    "s_waitcnt lgkmcnt(1)\n" \
    "s_barrier\n" \
    "v_smfmac_f32_16x16x64_bf16 v[48:51], v[16:19], a[192:199], v124\n" \
    "ds_read_b128 v[4:7], v24 offset:0\n" \
    "v_smfmac_f32_16x16x64_bf16 v[52:55], v[16:19], a[200:207], v124\n" \
    "ds_read_b128 v[8:11], v28 offset:0\n" \
    "v_smfmac_f32_16x16x64_bf16 v[56:59], v[0:3], v[72:79], v124\n" \
    "ds_read_b128 v[12:15], v32 offset:0\n" \
    "v_smfmac_f32_16x16x64_bf16 v[60:63], v[0:3], v[80:87], v124\n" \
    "s_waitcnt vmcnt(2) lgkmcnt(3)\n" \
    "s_barrier\n" \
    "s_memtime s[86:87]\n" \
    "v_smfmac_f32_16x16x64_bf16 v[56:59], v[16:19], v[168:175], v124\n" \
    "ds_read_b128 v[24:27], v24 offset:4096\n" \
    "v_smfmac_f32_16x16x64_bf16 v[60:63], v[16:19], v[176:183], v124\n" \
    "ds_read_b128 v[28:31], v28 offset:4096\n" \
    "s_waitcnt lgkmcnt(3)\n" \
    "v_smfmac_f32_16x16x64_bf16 v[40:43], v[4:7], a[16:23], v124\n" \
    "ds_read_b128 v[32:35], v32 offset:4096\n" \
    "v_smfmac_f32_16x16x64_bf16 v[40:43], v[8:11], a[32:39], v124\n" \
    "ds_read_b128 v[36:39], v126 offset:0\n" \
    "s_waitcnt lgkmcnt(4)\n" \
    "v_smfmac_f32_16x16x64_bf16 v[40:43], v[12:15], a[48:55], v124\n" \
    "buffer_load_dword v110, v114, %[rw], s94 offen sc1\n" \
    "v_smfmac_f32_16x16x64_bf16 v[44:47], v[4:7], a[24:31], v124\n" \
    "s_waitcnt lgkmcnt(2)\n" \
    "v_smfmac_f32_16x16x64_bf16 v[40:43], v[24:27], a[80:87], v124\n" \
    "v_smfmac_f32_16x16x64_bf16 v[40:43], v[28:31], a[96:103], v124\n" \
    "s_waitcnt lgkmcnt(0)\n" \
    "v_smfmac_f32_16x16x64_bf16 v[40:43], v[32:35], a[112:119], v124\n" \
    "ds_read_b128 v[72:75], v133 offset:18432\n" \
    "s_add_i32 s80, s86, s76\n" \
    "v_smfmac_f32_16x16x64_bf16 v[40:43], v[36:39], v[136:143], v124\n" \
    "ds_read_b128 v[76:79], v133 offset:19456\n" \
    "s_mul_hi_u32 s80, s80, s41\n" \
    "v_smfmac_f32_16x16x64_bf16 v[44:47], v[8:11], a[40:47], v124\n" \
    "ds_read_b128 v[80:83], v133 offset:32768\n" \
    "s_add_i32 s80, s80, s29\n" \
    "v_mov_b32 v102, s80\n" \
    "v_smfmac_f32_16x16x64_bf16 v[44:47], v[12:15], a[56:63], v124\n" \
    "ds_read_b128 v[84:87], v133 offset:33792\n" \
    "v_add_f32 v40, v40, v42\n" \
    "v_add_f32 v41, v41, v43\n" \
    "v_smfmac_f32_16x16x64_bf16 v[44:47], v[24:27], a[88:95], v124\n" \
    "ds_read_b128 v[20:23], v127 offset:19456\n" \
    "v_cmp_u_f32 vcc, v40, v41\n" \
    "v_smfmac_f32_16x16x64_bf16 v[44:47], v[28:31], a[104:111], v124\n" \
    "v_mul_f32 v40, s81, v40\n" \
    "v_smfmac_f32_16x16x64_bf16 v[44:47], v[32:35], a[120:127], v124\n" \
    "s_cbranch_vccnz 5f\n" \
    "6:\n" \
    "s_mov_b64 exec, s[72:73]\n" \
    "v_smfmac_f32_16x16x64_bf16 v[44:47], v[36:39], v[144:151], v124\n" \
    "buffer_store_dwordx2 v[102:103], v127, %[rs], s77 offen sc1\n" \
    "v_smfmac_f32_16x16x64_bf16 v[56:59], v[4:7], v[88:95], v124\n" \
    "s_mov_b64 exec, -1\n" \
    "s_waitcnt lgkmcnt(1)\n" \
    "v_mul_f32 v41, s81, v41\n" \
    "v_smfmac_f32_16x16x64_bf16 v[60:63], v[4:7], v[72:79], v124\n" \
    "ds_read_b128 v[88:91], v133 offset:34816\n" \
    "v_add_f32 v44, v44, v46\n" \
    "v_smfmac_f32_16x16x64_bf16 v[56:59], v[8:11], v[80:87], v124\n" \
    "ds_read_b128 v[92:95], v133 offset:35840\n" \
    "v_add_f32 v45, v45, v47\n" \
    "v_smfmac_f32_16x16x64_bf16 v[48:51], v[4:7], a[144:151], v124\n" \
    "ds_read_b128 v[72:75], v133 offset:49152\n" \
    "v_mul_f32 v44, s81, v44\n" \
    "v_smfmac_f32_16x16x64_bf16 v[52:55], v[4:7], a[152:159], v124\n" \
    "ds_read_b128 v[76:79], v133 offset:50176\n" \
    "v_mul_f32 v45, s81, v45\n" \
    "v_smfmac_f32_16x16x64_bf16 v[48:51], v[8:11], a[160:167], v124\n" \
    "ds_read_b128 v[0:3], v127 offset:16384\n" \
    "v_cvt_pk_bf16_f32 v107, v40, v44\n" \
    "v_smfmac_f32_16x16x64_bf16 v[52:55], v[8:11], a[168:175], v124\n" \
    "ds_read_b128 v[4:7], v127 offset:18432\n" \
    "s_waitcnt lgkmcnt(4)\n" \
    "v_cvt_pk_bf16_f32 v108, v41, v45\n" \
    "v_smfmac_f32_16x16x64_bf16 v[60:63], v[8:11], v[88:95], v124\n" \
    "ds_write2_b32 v128, v107, v108 offset0:0 offset1:80\n" \
    "v_smfmac_f32_16x16x64_bf16 v[60:63], v[12:15], v[248:255], v124\n" \
    "ds_read_b128 v[16:19], v127 offset:17408\n" \
    "s_waitcnt lgkmcnt(4)\n" \
    "v_smfmac_f32_16x16x64_bf16 v[56:59], v[12:15], v[72:79], v124\n" \
    "ds_read_b128 v[8:11], v127 offset:20480\n" \
    "v_smfmac_f32_16x16x64_bf16 v[48:51], v[12:15], a[176:183], v124\n" \
    "ds_read_b128 v[40:43], v127 offset:21504\n" \
    "v_smfmac_f32_16x16x64_bf16 v[52:55], v[12:15], a[184:191], v124\n" \
    "ds_read_b128 v[44:47], v127 offset:23552\n" \
    "v_smfmac_f32_16x16x64_bf16 v[48:51], v[24:27], a[208:215], v124\n" \
    "s_waitcnt lgkmcnt(4)\n" \
    "s_barrier\n" \
    "v_smfmac_f32_16x16x64_bf16 v[52:55], v[24:27], a[216:223], v124\n" \
    "ds_read_b128 v[72:75], v130 offset:0\n" \
    "v_smfmac_f32_16x16x64_bf16 v[48:51], v[28:31], a[224:231], v124\n" \
    "ds_read_b128 v[76:79], v130 offset:64\n" \
    "v_smfmac_f32_16x16x64_bf16 v[48:51], v[32:35], a[240:247], v124\n" \
    "ds_read_b128 v[80:83], v130 offset:128\n" \
    "v_smfmac_f32_16x16x64_bf16 v[48:51], v[36:39], v[152:159], v124\n" \
    "ds_read_b128 v[84:87], v130 offset:192\n" \
    "v_smfmac_f32_16x16x64_bf16 v[52:55], v[28:31], a[232:239], v124\n" \
    "ds_read_b128 v[12:15], v127 offset:22528\n" \
    "v_smfmac_f32_16x16x64_bf16 v[52:55], v[32:35], a[248:255], v124\n" \
    "v_add_f32 v48, v48, v50\n" \
    "v_smfmac_f32_16x16x64_bf16 v[52:55], v[36:39], v[160:167], v124\n" \
    "s_waitcnt lgkmcnt(3)\n" \
    "v_add_f32 v49, v49, v51\n" \
    "v_mfma_f32_16x16x32_bf16 v[88:91], v[0:3], v[72:75], 0\n" \
    "ds_read_b64 v[0:1], v132 offset:0\n" \
    "ds_read_b64 v[2:3], v132 offset:1024\n" \
    "v_mfma_f32_16x16x32_bf16 v[88:91], v[4:7], v[76:79], v[88:91]\n" \
    "s_waitcnt lgkmcnt(2)\n" \
    "v_add_f32 v52, v52, v54\n" \
    "v_add_f32 v53, v53, v55\n" \
    "v_mfma_f32_16x16x32_bf16 v[88:91], v[8:11], v[80:83], v[88:91]\n" \
    "v_cvt_pk_bf16_f32 v107, v48, v52\n" \
    "v_cvt_pk_bf16_f32 v108, v49, v53\n" \
    "v_mfma_f32_16x16x32_bf16 v[88:91], v[12:15], v[84:87], v[88:91]\n" \
    "ds_write2_b32 v129, v107, v108 offset0:0 offset1:144\n" \
    "v_mfma_f32_16x16x32_bf16 v[92:95], v[16:19], v[72:75], 0\n" \
    "ds_read_b64 v[4:5], v132 offset:512\n" \
    "ds_read_b64 v[6:7], v132 offset:1536\n" \
    "v_mfma_f32_16x16x32_bf16 v[92:95], v[20:23], v[76:79], v[92:95]\n" \
    "ds_read_b128 v[8:11], v106 offset:0\n" \
    "v_exp_f32 v88, v88\n" \
    "v_mfma_f32_16x16x32_bf16 v[92:95], v[40:43], v[80:83], v[92:95]\n" \
    "ds_read_b128 v[12:15], v106 offset:1024\n" \
    "v_exp_f32 v89, v89\n" \
    "v_mfma_f32_16x16x32_bf16 v[92:95], v[44:47], v[84:87], v[92:95]\n" \
    "s_waitcnt lgkmcnt(4)\n" \
    "s_barrier\n" \
    "v_smfmac_f32_16x16x64_bf16 v[56:59], v[24:27], v[184:191], v124\n" \
    "ds_read_b128 v[48:51], v131 offset:0\n" \
    "ds_read_b128 v[52:55], v131 offset:128\n" \
    "v_cvt_pk_bf16_f32 v88, v88, v89\n" \
    "v_smfmac_f32_16x16x64_bf16 v[56:59], v[28:31], v[200:207], v124\n" \
    "ds_read_b128 v[16:19], v106 offset:32768\n" \
    "v_exp_f32 v90, v90\n" \
    "v_smfmac_f32_16x16x64_bf16 v[56:59], v[32:35], v[216:223], v124\n" \
    "ds_read_b128 v[20:23], v106 offset:33792\n" \
    "v_exp_f32 v91, v91\n" \
    "v_smfmac_f32_16x16x64_bf16 v[56:59], v[36:39], v[232:239], v124\n" \
    "ds_read_b128 v[40:43], v106 offset:2048\n" \
    "s_waitcnt lgkmcnt(3)\n" \
    "v_exp_f32 v92, v92\n" \
    "v_cvt_pk_bf16_f32 v89, v90, v91\n" \
    "v_smfmac_f32_16x16x64_bf16 v[64:67], v[48:51], v[72:79], v124\n" \
    "ds_read_b128 v[44:47], v106 offset:3072\n" \
    "v_exp_f32 v93, v93\n" \
    "v_smfmac_f32_16x16x64_bf16 v[64:67], v[52:55], v[80:87], v124\n" \
    "ds_read_b128 v[72:75], v106 offset:34816\n" \
    "v_exp_f32 v94, v94\n" \
    "v_cvt_pk_bf16_f32 v90, v92, v93\n" \
    "v_smfmac_f32_16x16x64_bf16 v[60:63], v[24:27], v[192:199], v124\n" \
    "ds_read_b128 v[76:79], v106 offset:35840\n" \
    "v_exp_f32 v95, v95\n" \
    "v_smfmac_f32_16x16x64_bf16 v[60:63], v[28:31], v[208:215], v124\n" \
    "ds_read_b128 v[80:83], v106 offset:4096\n" \
    "s_cmp_eq_u32 s36, 62\n" \
    "v_add_f32 v64, v64, v66\n" \
    "v_add_f32 v65, v65, v67\n" \
    "v_smfmac_f32_16x16x64_bf16 v[60:63], v[32:35], v[224:231], v124\n" \
    "ds_read_b128 v[84:87], v106 offset:5120\n" \
    "v_exp_f32 v64, v64\n" \
    "v_cvt_pk_bf16_f32 v91, v94, v95\n" \
    "v_smfmac_f32_16x16x64_bf16 v[60:63], v[36:39], v[240:247], v124\n" \
    "ds_read_b128 v[48:51], v106 offset:36864\n" \
    "v_exp_f32 v65, v65\n" \
    "v_swap_b32 v1, v2\n" \
    "v_swap_b32 v5, v6\n" \
    "v_mfma_f32_16x16x32_bf16 v[102:105], v[88:91], v[120:123], 0\n" \
    "ds_read_b128 v[52:55], v106 offset:37888\n" \
    "v_cvt_pk_bf16_f32 v100, v64, v65\n" \
    "v_add_f32 v56, v56, v58\n" \
    "v_add_f32 v57, v57, v59\n" \
    "v_smfmac_f32_16x16x64_bf16 v[68:71], v[88:91], v[0:7], v124\n" \
    "ds_read_b128 v[24:27], v106 offset:6144\n" \
    "v_add_f32 v60, v60, v62\n" \
    "v_add_f32 v61, v61, v63\n" \
    "v_mfma_f32_16x16x16_bf16 v[102:105], v[100:101], v[120:121], v[102:105]\n" \
    "ds_read_b128 v[28:31], v106 offset:7168\n" \
    "v_cvt_pk_bf16_f32 v96, v56, v57\n" \
    "v_cvt_pk_bf16_f32 v97, v60, v61\n" \
    "s_nop 1\n" \
    "v_smfmac_f32_16x16x32_bf16 v[68:71], v[100:101], v[96:99], v124\n" \
    "ds_read_b128 v[32:35], v106 offset:38912\n" \
    "v_mov_b64 v[56:57], 0\n" \
    "v_mov_b64 v[58:59], 0\n" \
    "v_rcp_f32 v104, v102\n" \
    "v_rcp_f32 v105, v103\n" \
    "v_mul_f32 v68, v68, v104\n" \
    "v_mul_f32 v70, v70, v104\n" \
    "v_mul_f32 v69, v69, v105\n" \
    "v_mul_f32 v71, v71, v105\n" \
    "v_cvt_pk_bf16_f32 v107, v68, v70\n" \
    "v_cvt_pk_bf16_f32 v108, v69, v71\n" \
    "ds_write2_b32 v129, v107, v108 offset0:64 offset1:208\n" \
    "v_max3_f32 v102, v102, v103, v104\n" \
    "v_max_f32 v102, v102, v105\n" \
    "v_cmp_nge_f32 vcc, s82, v102\n" \
    "v_mov_b64 v[60:61], 0\n" \
    "v_mov_b64 v[62:63], 0\n" \
    "v_mov_b64 v[88:89], 0\n" \
    "v_mov_b64 v[90:91], 0\n" \
    "v_mov_b64 v[92:93], 0\n" \
    "v_mov_b64 v[94:95], 0\n" \
    "s_waitcnt vmcnt(2) lgkmcnt(0)\n" \
    "s_barrier\n" \
    "ds_read_b128 v[68:71], v109 offset:256\n" \
    "ds_read_b128 v[102:105], v111 offset:384\n" \
    "s_cbranch_vccnz 7f\n" \
    "buffer_load_dwordx2 v[98:99], v113, %[rs], s78 offen sc1\n" \
    "buffer_load_dword v100, off, %[rw], s75 sc1\n" \
    "v_mov_b64 v[64:65], 0\n" \
    "v_mov_b64 v[66:67], 0\n" \
    "s_waitcnt lgkmcnt(1)\n" \
    "v_smfmac_f32_16x16x64_bf16 v[56:59], v[68:71], v[8:15], v124\n" \
    "s_waitcnt lgkmcnt(0)\n" \
    "ds_read_b128 v[36:39], v106 offset:39936\n" \
    "v_smfmac_f32_16x16x64_bf16 v[56:59], v[102:105], v[16:23], v124\n" \
    "ds_read_b128 v[0:3], v133 offset:51200\n" \
    "v_smfmac_f32_16x16x64_bf16 v[60:63], v[68:71], v[40:47], v124\n" \
    "ds_read_b128 v[4:7], v133 offset:52224\n" \
    "v_smfmac_f32_16x16x64_bf16 v[60:63], v[102:105], v[72:79], v124\n" \
    "ds_read_b128 v[16:19], v112 offset:51200\n" \
    "v_add_f32 v56, v56, v58\n" \
    "v_smfmac_f32_16x16x64_bf16 v[88:91], v[68:71], v[80:87], v124\n" \
    "ds_read_b128 v[20:23], v112 offset:52224\n" \
    "v_add_f32 v57, v57, v59\n" \
    "v_smfmac_f32_16x16x64_bf16 v[88:91], v[102:105], v[48:55], v124\n" \
    "v_add_f32 v60, v60, v62\n" \
    "v_smfmac_f32_16x16x64_bf16 v[92:95], v[68:71], v[24:31], v124\n" \
    "s_waitcnt lgkmcnt(4)\n" \
    "v_add_f32 v61, v61, v63\n" \
    "v_smfmac_f32_16x16x64_bf16 v[92:95], v[102:105], v[32:39], v124\n" \
    "s_waitcnt lgkmcnt(2)\n" \
    "v_add_f32 v88, v88, v90\n" \
    "v_smfmac_f32_16x16x64_bf16 v[64:67], v[68:71], v[0:7], v124\n" \
    "s_waitcnt lgkmcnt(0)\n" \
    "v_add_f32 v89, v89, v91\n" \
    "v_smfmac_f32_16x16x64_bf16 v[64:67], v[102:105], v[16:23], v124\n" \
    "v_add_f32 v92, v92, v94\n" \
    "v_add_f32 v93, v93, v95\n" \
    "v_cvt_pk_bf16_f32 v8, v56, v60\n" \
    "v_cvt_pk_bf16_f32 v10, v57, v61\n" \
    "v_cvt_pk_bf16_f32 v9, v88, v92\n" \
    "v_cvt_pk_bf16_f32 v11, v89, v93\n" \
    "buffer_store_dwordx4 v[8:11], v134, %[rs], s79 offen sc1\n" \
    "v_add_f32 v64, v64, v66\n" \
    "v_add_f32 v65, v65, v67\n" \
    "v_cvt_pk_bf16_f32 v64, v64, v65\n" \
    "buffer_store_dword v64, v135, %[rs], s79 offen sc1\n" \
    "s_cbranch_scc1 16f\n" \
    "s_memtime s[88:89]\n" \
    "s_memrealtime s[44:45]\n" \
    "s_and_b32 s75, s91, 3\n" \
    "s_lshl_b32 s75, s75, 16\n" \
    "s_memrealtime s[46:47]\n" \
    "s_add_i32 s69, s75, s26\n" \
    "s_xor_b32 s70, s69, 2048\n" \
    "s_memrealtime s[48:49]\n" \
    "s_andn2_b32 s71, s69, 0x1fff\n" \
    "s_add_i32 s71, s71, 32768\n" \
    "s_memrealtime s[50:51]\n" \
    "s_mov_b32 s94, 0\n" \
    "s_mov_b32 s43, 0\n" \
    "s_memrealtime s[52:53]\n" \
    "s_cmp_lt_i32 s38, s91\n" \
    "s_nop 0\n" \
    "s_memrealtime s[54:55]\n" \
    "s_nop 1\n" \
    "s_memrealtime s[56:57]\n" \
    "s_nop 1\n" \
    "s_memrealtime vcc\n" \
    "v_and_b32 v19, 0x380, v127\n" \
    "v_add_u32 v19, v19, v127\n" \
    "v_and_b32 v18, 0x300, v127\n" \
    "v_add_u32 v18, v18, v127\n" \
    "s_cbranch_scc1 40f\n" \
    "41:\n" \
    "buffer_load_dwordx4 v19, %[rw], s92 offen sc1 lds\n" \
    "s_add_i32 m0, s95, 896\n" \
    "s_waitcnt lgkmcnt(0)\n" \
    "s_add_i32 s44, s44, s46\n" \
    "s_add_i32 s48, s48, s50\n" \
    "s_add_i32 s52, s52, s54\n" \
    "s_add_i32 s44, s44, s48\n" \
    "s_add_i32 s52, s52, s56\n" \
    "s_add_i32 s44, s44, s52\n" \
    "s_add_i32 s80, s44, vcc_lo\n" \
    "s_sub_i32 s45, s80, s29\n" \
    "s_mov_b32 s29, s80\n" \
    "s_sub_i32 s47, s88, s24\n" \
    "s_mov_b32 s24, s88\n" \
    "buffer_load_dwordx4 v19, %[rw], s92 offen offset:128 sc1 lds\n" \
    "s_add_i32 m0, s95, 8192\n" \
    "s_add_i32 s76, s29, s42\n" \
    "v_add_u32 v16, -1, s36\n" \
    "v_mov_b32 v17, s76\n" \
    "s_mov_b64 exec, s[72:73]\n" \
    "buffer_store_dwordx2 v[16:17], off, %[rw], s65 sc1\n" \
    "s_mov_b64 exec, -1\n" \
    "ds_read_b128 v[72:75], v133 offset:0\n" \
    "ds_read_b128 v[76:79], v133 offset:1024\n" \
    "ds_read_b128 v[80:83], v133 offset:2048\n" \
    "ds_read_b128 v[84:87], v133 offset:3072\n" \
    "ds_read_b128 v[88:91], v133 offset:16384\n" \
    "ds_read_b128 v[92:95], v133 offset:17408\n" \
    "buffer_load_dwordx4 v18, %[rw], s93 offen sc1 lds\n" \
    "s_add_i32 m0, s95, 8960\n" \
    "v_mov_b64 v[40:41], 0\n" \
    "v_mov_b64 v[42:43], 0\n" \
    "v_mov_b64 v[44:45], 0\n" \
    "v_mov_b64 v[46:47], 0\n" \
    "v_mov_b64 v[48:49], 0\n" \
    "v_mov_b64 v[50:51], 0\n" \
    "v_mov_b64 v[52:53], 0\n" \
    "v_mov_b64 v[54:55], 0\n" \
    "buffer_load_dwordx4 v18, %[rw], s93 offen offset:256 sc1 lds\n" \
    "s_movk_i32 m0, 0x2000\n" \
    "v_mov_b64 v[56:57], 0\n" \
    "v_mov_b64 v[58:59], 0\n" \
    "v_mov_b64 v[60:61], 0\n" \
    "v_mov_b64 v[62:63], 0\n" \
    "v_mov_b64 v[64:65], 0\n" \
    "v_mov_b64 v[66:67], 0\n" \
    "v_mov_b64 v[68:69], 0\n" \
    "v_mov_b64 v[70:71], 0\n" \
    "v_add_u32 v108, s74, v127\n" \
    "v_xor_b32 v24, 0x400, v108\n" \
    "v_xor_b32 v28, 0x800, v108\n" \
    "v_xor_b32 v32, 0xc00, v108\n" \
    "v_and_b32 v22, 0x200, v127\n" \
    "s_and_b32 s76, s26, 0x800\n" \
    "v_mad_u32_u24 v22, v22, 3, v127\n" \
    "v_xor_b32 v22, s76, v22\n" \
    "s_waitcnt vmcnt(7)\n" \
    "v_cmp_ne_u16 vcc, s91, v99\n" \
    "v_readfirstlane_b32 s80, v100\n" \
    "s_max_i32 s38, s38, s80\n" \
    "s_cbranch_vccnz 15f\n" \
    "10:\n" \
    "v_subrev_u32 v20, s29, v98\n" \
    "s_bitcmp1_b32 s30, 15\n" \
    "v_min_i32 v20, 0x4000, v20\n" \
    "v_max_i32 v20, 0xffffc000, v20\n" \
    "s_cbranch_scc1 18f\n" \
    "19:\n" \
    "v_readlane_b32 s79, v20, s83\n" \
    "v_max_i32_dpp v20, v20, v20 row_shr:1 row_mask:0xf bank_mask:0xf\n" \
    "s_mul_i32 s49, s45, s25\n" \
    "s_lshr_b32 s49, s49, 12\n" \
    "v_max_i32_dpp v20, v20, v20 row_shr:2 row_mask:0xf bank_mask:0xf\n" \
    "s_sub_i32 s49, s47, s49\n" \
    "s_mul_hi_u32 s47, s47, s41\n" \
    "v_max_i32_dpp v20, v20, v20 row_shr:4 row_mask:0xf bank_mask:0xf\n" \
    "s_sub_i32 s47, s45, s47\n" \
    "s_lshl_b32 s47, s47, 19\n" \
    "v_max_i32_dpp v20, v20, v20 row_shr:8 row_mask:0xf bank_mask:0xf\n" \
    "s_cmp_lt_u32 s45, 3000\n" \
    "s_cselect_b32 s49, s49, 0\n" \
    "v_max_i32_dpp v20, v20, v20 row_bcast:15 row_mask:0xa bank_mask:0xf\n" \
    "s_cselect_b32 s47, s47, 0\n" \
    "s_lshl1_add_u32 s25, s49, s25\n" \
    "v_max_i32_dpp v20, v20, v20 row_bcast:31 row_mask:0xc bank_mask:0xf\n" \
    "s_add_i32 s41, s41, s47\n" \
    "s_add_i32 s76, s42, s28\n" \
    "s_add_i32 s79, s79, s76\n" \
    "v_readlane_b32 s37, v20, 63\n" \
    "s_add_i32 s37, s37, s76\n" \
    "s_cmp_lg_u32 s94, 0\n" \
    "s_cselect_b32 s36, s36, s91\n" \
    "s_cbranch_scc0 12b\n" \
    "s_branch 9f\n" \
    ".Lcold:\n" \
    "5:\n" \
    "v_mov_b32 v107, 0\n" \
    "v_max3_u32 v107, v107, v0, v1\n" \
    "v_max3_u32 v107, v107, v2, v3\n" \
    "v_max3_u32 v107, v107, v4, v5\n" \
    "v_max3_u32 v107, v107, v6, v7\n" \
    "v_max3_u32 v107, v107, v8, v9\n" \
    "v_max3_u32 v107, v107, v10, v11\n" \
    "v_max3_u32 v107, v107, v12, v13\n" \
    "v_max3_u32 v107, v107, v14, v15\n" \
    "v_max3_u32 v107, v107, v16, v17\n" \
    "v_max3_u32 v107, v107, v18, v19\n" \
    "v_max3_u32 v107, v107, v24, v25\n" \
    "v_max3_u32 v107, v107, v26, v27\n" \
    "v_max3_u32 v107, v107, v28, v29\n" \
    "v_max3_u32 v107, v107, v30, v31\n" \
    "v_max3_u32 v107, v107, v32, v33\n" \
    "v_max3_u32 v107, v107, v34, v35\n" \
    "v_max3_u32 v107, v107, v36, v37\n" \
    "v_max3_u32 v107, v107, v38, v39\n" \
    "v_cmp_eq_u32 vcc, -1, v107\n" \
    "s_nop 4\n" \
    "s_cbranch_vccz 6b\n" \
    "s_cmp_eq_u32 s36, 0\n" \
    "s_cbranch_scc1 6b\n" \
    "s_add_i32 s39, s39, -1\n" \
    "s_cmp_lt_i32 s39, 0\n" \
    "s_cbranch_scc1 6b\n" \
    "s_add_i32 s30, s30, 1\n" \
    "s_bitset1_b32 s30, 15\n" \
    "s_and_b32 s80, s31, 0xff\n" \
    "s_add_i32 s42, s42, s80\n" \
    "s_bfe_u32 s80, s31, 0x80008\n" \
    "s_add_i32 s40, s40, s80\n" \
    "s_waitcnt lgkmcnt(0)\n" \
    "s_mov_b32 s37, s29\n" \
    "s_nop 7\n" \
    "s_barrier\n" \
    "s_branch 0b\n" \
    "15:\n" \
    "s_add_i32 s30, s30, 0x10000\n" \
    "s_add_i32 s39, s39, -1\n" \
    "s_cmp_lt_i32 s39, 0\n" \
    "s_cbranch_scc1 10b\n" \
    "s_sleep 9\n" \
    "buffer_load_dwordx2 v[98:99], v113, %[rs], s78 offen sc1\n" \
    "s_waitcnt vmcnt(0)\n" \
    "v_cmp_ne_u16 vcc, s91, v99\n" \
    "s_nop 1\n" \
    "s_cbranch_vccnz 15b\n" \
    "s_memtime s[88:89]\n" \
    "s_memrealtime s[44:45]\n" \
    "s_nop 1\n" \
    "s_memrealtime s[46:47]\n" \
    "s_nop 1\n" \
    "s_memrealtime s[48:49]\n" \
    "s_nop 1\n" \
    "s_memrealtime s[50:51]\n" \
    "s_nop 1\n" \
    "s_memrealtime s[52:53]\n" \
    "s_nop 1\n" \
    "s_memrealtime s[54:55]\n" \
    "s_nop 1\n" \
    "s_memrealtime s[56:57]\n" \
    "s_nop 1\n" \
    "s_memrealtime vcc\n" \
    "s_waitcnt lgkmcnt(0)\n" \
    "s_add_i32 s44, s44, s46\n" \
    "s_add_i32 s48, s48, s50\n" \
    "s_add_i32 s52, s52, s54\n" \
    "s_add_i32 s44, s44, s48\n" \
    "s_add_i32 s52, s52, s56\n" \
    "s_add_i32 s44, s44, s52\n" \
    "s_add_i32 s29, s44, vcc_lo\n" \
    "s_mov_b32 s24, s88\n" \
    "s_mov_b32 s45, -1\n" \
    "s_branch 10b\n" \
    "18:\n" \
    "v_cmp_gt_i32 vcc, -320, v20\n" \
    "s_bitset0_b32 s30, 15\n" \
    "s_lshr_b32 s80, s31, 16\n" \
    "s_cmp_lt_u32 s36, 4\n" \
    "s_cselect_b32 s80, 0, s80\n" \
    "s_min_u32 s76, s80, 1\n" \
    "s_lshl_b32 s76, s76, 16\n" \
    "s_sub_i32 s31, s31, s76\n" \
    "s_mul_i32 s76, s80, 768\n" \
    "s_cmp_eq_u64 vcc, 0\n" \
    "s_cselect_b32 s80, 0, s80\n" \
    "s_cselect_b32 s76, s76, 0\n" \
    "s_add_i32 s33, s33, s80\n" \
    "s_add_i32 s32, s32, s76\n" \
    "s_branch 19b\n" \
    "40:\n" \
    "s_waitcnt vmcnt(2)\n" \
    "v_readfirstlane_b32 s80, v100\n" \
    "s_max_i32 s38, s38, s80\n" \
    "s_cmp_lt_i32 s38, s91\n" \
    "s_cbranch_scc0 41b\n" \
    "s_mov_b32 s94, 8\n" \
    "v_mov_b32 v18, 0x7ffffff0\n" \
    "v_mov_b32 v19, 0x7ffffff0\n" \
    "s_branch 41b\n" \
    "20:\n" \
    "s_mov_b32 s91, 63\n" \
    "s_mov_b32 s78, 0x1a8000\n" \
    "buffer_load_dwordx2 v[98:99], v113, %[rs], s78 offen sc1\n" \
    "buffer_load_dword v100, off, %[rw], s62 sc1\n" \
    "s_waitcnt vmcnt(0)\n" \
    "16:\n" \
    "v_and_b32 v112, 0x300, v127\n" \
    "v_add_u32 v112, v112, v127\n" \
    "s_add_i32 s44, %[qb], 0\n" \
    "s_add_i32 s45, s44, 4096\n" \
    "buffer_load_dwordx4 v[136:139], v112, %[rw], s44 offen offset:0 sc1\n" \
    "v_lshrrev_b32 v103, 1, v127\n" \
    "s_lshr_b32 s76, s74, 1\n" \
    "v_add_u32 v101, s76, v103\n" \
    "ds_write_b64 v101, v[96:97] offset:9216\n" \
    "ds_read_b128 v[0:3], v127 offset:16384\n" \
    "v_lshrrev_b32 v42, 8, v127\n" \
    "v_bfe_u32 v40, v127, 4, 4\n" \
    "ds_read_b128 v[4:7], v127 offset:17408\n" \
    "v_and_b32 v41, 2, v42\n" \
    "v_add_u32 v43, v41, v42\n" \
    "ds_read_b128 v[8:11], v127 offset:18432\n" \
    "s_lshl_b32 s76, s60, 4\n" \
    "s_and_b32 s76, s76, 16\n" \
    "v_add_u32 v40, s76, v40\n" \
    "v_mad_i32_i24 v40, v43, -4, v40\n" \
    "buffer_load_dwordx4 v[140:143], v112, %[rw], s44 offen offset:2048 sc1\n" \
    "ds_read_b128 v[12:15], v127 offset:19456\n" \
    "v_mov_b32 v43, 0xff61b1e6\n" \
    "v_cmp_gt_i32 vcc, 0, v40\n" \
    "ds_read_b128 v[16:19], v127 offset:20480\n" \
    "v_cndmask_b32 v88, 0, v43, vcc\n" \
    "v_cmp_gt_i32 vcc, 1, v40\n" \
    "ds_read_b128 v[20:23], v127 offset:21504\n" \
    "v_cndmask_b32 v89, 0, v43, vcc\n" \
    "v_cmp_gt_i32 vcc, 2, v40\n" \
    "ds_read_b128 v[24:27], v127 offset:22528\n" \
    "v_cndmask_b32 v90, 0, v43, vcc\n" \
    "v_cmp_gt_i32 vcc, 3, v40\n" \
    "buffer_load_dwordx4 v[144:147], v112, %[rw], s45 offen offset:0 sc1\n" \
    "ds_read_b128 v[28:31], v127 offset:23552\n" \
    "v_cndmask_b32 v91, 0, v43, vcc\n" \
    "v_cmp_gt_i32 vcc, 8, v40\n" \
    "ds_read_b128 v[32:35], v131 offset:0\n" \
    "v_cndmask_b32 v92, 0, v43, vcc\n" \
    "v_cmp_gt_i32 vcc, 9, v40\n" \
    "ds_read_b128 v[36:39], v131 offset:128\n" \
    "v_cndmask_b32 v93, 0, v43, vcc\n" \
    "v_cmp_gt_i32 vcc, 10, v40\n" \
    "ds_read_b128 v[56:59], v127 offset:24576\n" \
    "v_cndmask_b32 v94, 0, v43, vcc\n" \
    "v_cmp_gt_i32 vcc, 11, v40\n" \
    "buffer_load_dwordx4 v[148:151], v112, %[rw], s45 offen offset:2048 sc1\n" \
    "ds_read_b128 v[60:63], v127 offset:25600\n" \
    "v_cndmask_b32 v95, 0, v43, vcc\n" \
    "v_mad_u32_u24 v44, v41, 3, v42\n" \
    "ds_read_b128 v[64:67], v127 offset:26624\n" \
    "v_bfe_u32 v45, v127, 6, 1\n" \
    "v_bfe_u32 v46, v127, 4, 2\n" \
    "s_waitcnt lgkmcnt(8)\n" \
    "ds_read_b128 v[68:71], v127 offset:27648\n" \
    "v_lshl_add_u32 v44, v46, 1, v44\n" \
    "v_bfe_u32 v46, v127, 7, 1\n" \
    "ds_read_b128 a[120:123], v127 offset:28672\n" \
    "v_lshl_add_u32 v44, v44, 1, v46\n" \
    "v_mul_u32_u24 v44, 4352, v44\n" \
    "ds_read_b128 a[124:127], v127 offset:29696\n" \
    "v_lshl_add_u32 v44, v45, 1, v44\n" \
    "v_lshlrev_b32 v45, 2, v45\n" \
    "ds_read_b128 v[80:83], v127 offset:30720\n" \
    "s_lshr_b32 s79, %[u], 4\n" \
    "s_mul_i32 s79, s79, 32 * 4352\n" \
    "s_and_b32 s94, %[u], 15\n" \
    "s_lshl2_add_u32 s79, s94, s79\n" \
    "s_add_i32 s79, s79, 0x3e00fc0\n" \
    "s_lshl3_add_u32 s94, s94, 0x4065f80\n" \
    "s_lshr_b32 s80, s60, 1\n" \
    "s_mul_i32 s76, s80, 49152\n" \
    "s_add_i32 s76, s76, 98304\n" \
    "v_add_u32 v128, s76, v127\n" \
    "v_xor_b32 v130, 0x38000, v128\n" \
    "ds_read_b128 v[84:87], v127 offset:31744\n" \
    "s_lshl_b32 s76, s80, 12\n" \
    "s_add_i32 s76, s76, 83968\n" \
    "v_add_u32 v132, s76, v127\n" \
    "s_add_i32 s92, s64, 16515072\n" \
    "s_add_i32 s93, s92, 0x1000000\n" \
    "s_mov_b32 m0, s95\n" \
    "v_and_b32 v97, 0x380, v127\n" \
    "v_add_u32 v97, v97, v127\n" \
    "s_waitcnt vmcnt(6)\n" \
    "v_readfirstlane_b32 s80, v100\n" \
    "v_cmp_ne_u16 vcc, s91, v99\n" \
    "s_max_i32 s38, s38, s80\n" \
    "s_cmp_lt_i32 s38, s91\n" \
    "s_cbranch_scc1 21f\n" \
    "22:\n" \
    "s_cbranch_vccnz 23f\n" \
    "24:\n" \
    "buffer_load_ushort v113, v44, %[rw], s79 offen sc1\n" \
    "buffer_load_dword v114, v45, %[rw], s94 offen sc1\n" \
    "v_subrev_u32 v96, s29, v98\n" \
    "v_min_i32 v96, 0x4000, v96\n" \
    "v_max_i32 v96, 0, v96\n" \
    "s_waitcnt lgkmcnt(0)\n" \
    "s_barrier\n" \
    "v_max_i32_dpp v96, v96, v96 row_shr:1 row_mask:0xf bank_mask:0xf\n" \
    "ds_read_b64 v[52:53], v103 offset:9216\n" \
    "s_and_b32 s75, s91, 3\n" \
    "v_max_i32_dpp v96, v96, v96 row_shr:2 row_mask:0xf bank_mask:0xf\n" \
    "ds_read_b64 v[54:55], v103 offset:9728\n" \
    "s_lshl_b32 s75, s75, 16\n" \
    "v_max_i32_dpp v96, v96, v96 row_shr:4 row_mask:0xf bank_mask:0xf\n" \
    "ds_read_b64 v[100:101], v103 offset:10240\n" \
    "s_add_i32 s69, s75, s26\n" \
    "v_max_i32_dpp v96, v96, v96 row_shr:8 row_mask:0xf bank_mask:0xf\n" \
    "ds_read_b64 v[102:103], v103 offset:10752\n" \
    "s_xor_b32 s70, s69, 2048\n" \
    "v_max_i32_dpp v96, v96, v96 row_bcast:15 row_mask:0xa bank_mask:0xf\n" \
    "ds_read_b128 a[0:3], v128 offset:0\n" \
    "s_andn2_b32 s71, s69, 0x1fff\n" \
    "v_max_i32_dpp v96, v96, v96 row_bcast:31 row_mask:0xc bank_mask:0xf\n" \
    "ds_read_b128 a[16:19], v128 offset:2048\n" \
    "s_add_i32 s71, s71, 32768\n" \
    "v_readlane_b32 s90, v96, 63\n" \
    "s_add_i32 s90, s90, s42\n" \
    "s_mul_i32 s90, s90, s25\n" \
    "s_ashr_i32 s90, s90, 12\n" \
    "s_add_i32 s90, s90, s24\n" \
    "buffer_load_dwordx4 v97, %[rw], s92 offen sc1 lds\n" \
    "s_add_i32 m0, s95, 896\n" \
    "v_mov_b32 v99, 0\n" \
    "buffer_load_dwordx4 v97, %[rw], s92 offen offset:128 sc1 lds\n" \
    "s_add_i32 m0, s95, 8192\n" \
    "v_mov_b32 v96, 0\n" \
    "25:\n" \
    "v_mov_b64 v[48:49], 0\n" \
    "v_mov_b64 v[50:51], 0\n" \
    "s_waitcnt vmcnt(7)\n" \
    "v_mfma_f32_16x16x32_bf16 v[40:43], v[0:3], v[136:139], v[88:91]\n" \
    "ds_read_b128 a[4:7], v128 offset:1024\n" \
    "v_max3_u32 v98, v136, v137, v136\n" \
    "v_mfma_f32_16x16x32_bf16 v[44:47], v[4:7], v[136:139], v[92:95]\n" \
    "ds_read_b128 a[20:23], v128 offset:3072\n" \
    "v_max3_u32 v98, v138, v139, v98\n" \
    "s_waitcnt vmcnt(6)\n" \
    "v_mfma_f32_16x16x32_bf16 v[40:43], v[8:11], v[140:143], v[40:43]\n" \
    "ds_read_b128 a[8:11], v130 offset:0\n" \
    "v_max3_u32 v98, v140, v141, v98\n" \
    "v_mfma_f32_16x16x32_bf16 v[44:47], v[12:15], v[140:143], v[44:47]\n" \
    "ds_read_b128 a[24:27], v130 offset:2048\n" \
    "v_max3_u32 v98, v142, v143, v98\n" \
    "v_smfmac_f32_16x16x64_bf16 v[48:51], v[32:35], v[136:143], v124\n" \
    "ds_read_b128 a[12:15], v130 offset:1024\n" \
    "s_waitcnt vmcnt(5)\n" \
    "v_mfma_f32_16x16x32_bf16 v[40:43], v[16:19], v[144:147], v[40:43]\n" \
    "ds_read_b128 a[28:31], v130 offset:3072\n" \
    "v_max3_u32 v98, v144, v145, v98\n" \
    "v_mfma_f32_16x16x32_bf16 v[44:47], v[20:23], v[144:147], v[44:47]\n" \
    "ds_read_b128 a[32:35], v128 offset:4096\n" \
    "v_max3_u32 v98, v146, v147, v98\n" \
    "s_waitcnt vmcnt(4)\n" \
    "v_smfmac_f32_16x16x64_bf16 v[48:51], v[36:39], v[144:151], v124\n" \
    "ds_read_b128 a[48:51], v128 offset:6144\n" \
    "v_mfma_f32_16x16x32_bf16 v[40:43], v[24:27], v[148:151], v[40:43]\n" \
    "ds_read_b128 a[36:39], v128 offset:5120\n" \
    "v_max3_u32 v98, v148, v149, v98\n" \
    "v_max3_u32 v98, v150, v151, v98\n" \
    "v_mfma_f32_16x16x32_bf16 v[44:47], v[28:31], v[148:151], v[44:47]\n" \
    "v_cmp_eq_u32 vcc, -1, v98\n" \
    "s_add_i32 s44, %[qb], 262144\n" \
    "s_add_i32 s45, s44, 4096\n" \
    "s_waitcnt lgkmcnt(3)\n" \
    "v_add_f32 v48, v48, v50\n" \
    "v_add_f32 v49, v49, v51\n" \
    "s_nop 0\n" \
    "s_cbranch_vccnz 26f\n" \
    "27:\n" \
    "v_max3_f32 v0, v40, v41, v42\n" \
    "v_max3_f32 v1, v43, v44, v45\n" \
    "v_max3_f32 v0, v0, v46, v47\n" \
    "v_max3_f32 v1, v1, v48, v49\n" \
    "v_max_f32 v0, v0, v1\n" \
    "v_max_f32 v1, v0, v1\n" \
    "buffer_load_dwordx4 v112, %[rw], s93 offen sc1 lds\n" \
    "s_add_i32 m0, s95, 8960\n" \
    "s_nop 0\n" \
    "v_permlane16_swap_b32 v0, v1\n" \
    "v_max_f32 v0, v0, v1\n" \
    "v_max_f32 v1, v0, v1\n" \
    "buffer_load_dwordx4 v112, %[rw], s93 offen offset:256 sc1 lds\n" \
    "s_nop 0\n" \
    "v_permlane32_swap_b32 v0, v1\n" \
    "v_max_f32 v0, v0, v1\n" \
    "s_nop 0\n" \
    "v_sub_f32 v40, v40, v0\n" \
    "v_sub_f32 v41, v41, v0\n" \
    "ds_read_b128 v[136:139], v132 offset:0\n" \
    "v_exp_f32 v40, v40\n" \
    "v_exp_f32 v41, v41\n" \
    "v_sub_f32 v42, v42, v0\n" \
    "ds_read_b128 v[140:143], v132 offset:1024\n" \
    "v_sub_f32 v43, v43, v0\n" \
    "v_exp_f32 v42, v42\n" \
    "v_exp_f32 v43, v43\n" \
    "ds_read_b128 v[144:147], v132 offset:8192\n" \
    "v_cvt_pk_bf16_f32 v32, v40, v41\n" \
    "v_sub_f32 v44, v44, v0\n" \
    "v_sub_f32 v45, v45, v0\n" \
    "ds_read_b128 v[148:151], v132 offset:9216\n" \
    "v_exp_f32 v44, v44\n" \
    "v_exp_f32 v45, v45\n" \
    "v_cvt_pk_bf16_f32 v33, v42, v43\n" \
    "ds_read_b128 a[52:55], v128 offset:7168\n" \
    "v_sub_f32 v46, v46, v0\n" \
    "v_sub_f32 v47, v47, v0\n" \
    "v_exp_f32 v46, v46\n" \
    "s_memtime vcc\n" \
    "v_exp_f32 v47, v47\n" \
    "v_cvt_pk_bf16_f32 v34, v44, v45\n" \
    "v_sub_f32 v48, v48, v0\n" \
    "v_sub_f32 v49, v49, v0\n" \
    "v_exp_f32 v48, v48\n" \
    "v_exp_f32 v49, v49\n" \
    "v_cvt_pk_bf16_f32 v35, v46, v47\n" \
    "v_cvt_pk_bf16_f32 v98, v48, v49\n" \
    "v_cvt_pk_bf16_f32 v97, v48, v49\n" \
    "s_waitcnt lgkmcnt(0)\n" \
    "v_mfma_f32_16x16x32_bf16 v[36:39], v[120:123], v[32:35], 0\n" \
    "s_sub_i32 s75, s90, vcc_lo\n" \
    "s_cmp_gt_i32 s75, 100\n" \
    "v_mfma_f32_16x16x32_bf16 v[0:3], v[56:59], v[32:35], 0\n" \
    "s_cbranch_scc1 30f\n" \
    "31:\n" \
    "ds_read_b128 a[40:43], v130 offset:4096\n" \
    "v_and_b32 v44, 0x200, v127\n" \
    "s_and_b32 s80, s26, 0x800\n" \
    "v_mad_u32_u24 v44, v44, 3, v127\n" \
    "v_mfma_f32_16x16x32_bf16 v[4:7], v[60:63], v[32:35], 0\n" \
    "s_mov_b32 m0, s74\n" \
    "s_add_i32 s76, s74, 4096\n" \
    "buffer_load_dwordx4 v125, %[rs], s69 offen sc1 lds\n" \
    "s_mov_b32 m0, s76\n" \
    "v_mfma_f32_16x16x32_bf16 v[8:11], v[64:67], v[32:35], 0\n" \
    "ds_read_b128 a[56:59], v130 offset:6144\n" \
    "v_xor_b32 v44, s80, v44\n" \
    "v_bfe_u32 v40, v127, 6, 2\n" \
    "v_mfma_f32_16x16x32_bf16 v[12:15], v[68:71], v[32:35], 0\n" \
    "buffer_load_dwordx4 v125, %[rs], s70 offen sc1 lds\n" \
    "s_movk_i32 m0, 0x2000\n" \
    "v_mfma_f32_16x16x32_bf16 v[16:19], a[120:123], v[32:35], 0\n" \
    "ds_read_b128 a[44:47], v130 offset:5120\n" \
    "v_bfe_u32 v41, v127, 5, 1\n" \
    "v_bfe_u32 v42, v127, 4, 1\n" \
    "v_mfma_f32_16x16x32_bf16 v[20:23], a[124:127], v[32:35], 0\n" \
    "s_mov_b32 exec_lo, s27\n" \
    "s_mov_b32 exec_hi, s27\n" \
    "buffer_load_dwordx4 v44, %[rs], s71 offen sc1 lds\n" \
    "s_mov_b64 exec, -1\n" \
    "v_mfma_f32_16x16x32_bf16 v[24:27], v[80:83], v[32:35], 0\n" \
    "ds_read_b128 a[60:63], v130 offset:7168\n" \
    "v_lshrrev_b32 v43, 8, v127\n" \
    "v_lshlrev_b32 v134, 14, v40\n" \
    "v_mfma_f32_16x16x32_bf16 v[28:31], v[84:87], v[32:35], 0\n" \
    "ds_read_b128 a[64:67], v128 offset:8192\n" \
    "v_lshl_add_u32 v134, v41, 12, v134\n" \
    "v_lshl_add_u32 v134, v42, 6, v134\n" \
    "v_mfma_f32_16x16x16_bf16 v[36:39], v[120:121], v[98:99], v[36:39]\n" \
    "ds_read_b128 a[80:83], v128 offset:10240\n" \
    "v_lshl_add_u32 v134, v43, 4, v134\n" \
    "v_and_b32 v41, 2, v40\n" \
    "v_mfma_f32_16x16x16_bf16 v[0:3], v[52:53], v[98:99], v[0:3]\n" \
    "buffer_load_dwordx4 v[72:75], v112, %[rw], s44 offen offset:0 sc1\n" \
    "v_mfma_f32_16x16x16_bf16 v[4:7], v[52:53], v[96:97], v[4:7]\n" \
    "ds_read_b128 a[68:71], v128 offset:9216\n" \
    "v_add_lshl_u32 v135, v40, v41, 12\n" \
    "v_bfe_u32 v42, v127, 4, 2\n" \
    "v_mfma_f32_16x16x16_bf16 v[8:11], v[54:55], v[98:99], v[8:11]\n" \
    "buffer_load_dwordx4 v[76:79], v112, %[rw], s44 offen offset:2048 sc1\n" \
    "v_mfma_f32_16x16x16_bf16 v[12:15], v[54:55], v[96:97], v[12:15]\n" \
    "ds_read_b128 a[84:87], v128 offset:11264\n" \
    "v_lshl_add_u32 v135, v42, 5, v135\n" \
    "v_lshl_add_u32 v135, v43, 3, v135\n" \
    "v_mfma_f32_16x16x16_bf16 v[16:19], v[100:101], v[98:99], v[16:19]\n" \
    "buffer_load_dwordx4 v[104:107], v112, %[rw], s45 offen offset:0 sc1\n" \
    "v_mfma_f32_16x16x16_bf16 v[20:23], v[100:101], v[96:97], v[20:23]\n" \
    "ds_read_b128 a[72:75], v130 offset:8192\n" \
    "v_mfma_f32_16x16x16_bf16 v[24:27], v[102:103], v[98:99], v[24:27]\n" \
    "buffer_load_dwordx4 v[108:111], v112, %[rw], s45 offen offset:2048 sc1\n" \
    "v_mfma_f32_16x16x16_bf16 v[28:31], v[102:103], v[96:97], v[28:31]\n" \
    "ds_read_b128 a[88:91], v130 offset:10240\n" \
    "v_cvt_pk_bf16_f32 v40, v0, v4\n" \
    "v_cvt_pk_bf16_f32 v41, v1, v5\n" \
    "v_cvt_pk_bf16_f32 v42, v2, v6\n" \
    "v_cvt_pk_bf16_f32 v43, v3, v7\n" \
    "v_cvt_pk_bf16_f32 v44, v8, v12\n" \
    "v_cvt_pk_bf16_f32 v45, v9, v13\n" \
    "v_mfma_f32_16x16x32_bf16 v[64:67], v[136:139], v[40:43], 0\n" \
    "s_mov_b32 s46, %[tp]\n" \
    "ds_read_b128 a[76:79], v130 offset:9216\n" \
    "v_cvt_pk_bf16_f32 v46, v10, v14\n" \
    "v_cvt_pk_bf16_f32 v47, v11, v15\n" \
    "v_mfma_f32_16x16x32_bf16 v[56:59], a[0:3], v[40:43], 0\n" \
    "s_mov_b32 s47, %[tx]\n" \
    "ds_read_b128 a[92:95], v130 offset:11264\n" \
    "v_cvt_pk_bf16_f32 v48, v16, v20\n" \
    "v_cvt_pk_bf16_f32 v49, v17, v21\n" \
    "v_mfma_f32_16x16x32_bf16 v[64:67], v[140:143], v[44:47], v[64:67]\n" \
    "ds_read_b128 a[96:99], v128 offset:12288\n" \
    "v_cvt_pk_bf16_f32 v50, v18, v22\n" \
    "v_cvt_pk_bf16_f32 v51, v19, v23\n" \
    "v_mfma_f32_16x16x32_bf16 v[60:63], a[16:19], v[40:43], 0\n" \
    "s_waitcnt lgkmcnt(5)\n" \
    "ds_read_b128 a[112:115], v128 offset:14336\n" \
    "v_cvt_pk_bf16_f32 v52, v24, v28\n" \
    "v_cvt_pk_bf16_f32 v53, v25, v29\n" \
    "v_mfma_f32_16x16x32_bf16 v[64:67], v[144:147], v[48:51], v[64:67]\n" \
    "ds_read_b128 a[100:103], v128 offset:13312\n" \
    "v_cvt_pk_bf16_f32 v54, v26, v30\n" \
    "v_cvt_pk_bf16_f32 v55, v27, v31\n" \
    "v_mfma_f32_16x16x32_bf16 v[56:59], a[4:7], v[44:47], v[56:59]\n" \
    "ds_read_b128 a[116:119], v128 offset:15360\n" \
    "v_rcp_f32 v36, v36\n" \
    "v_mfma_f32_16x16x32_bf16 v[64:67], v[148:151], v[52:55], v[64:67]\n" \
    "ds_read_b128 a[104:107], v130 offset:12288\n" \
    "v_add_u32 v37, s74, v127\n" \
    "v_xor_b32 v38, 0x400, v37\n" \
    "v_mfma_f32_16x16x32_bf16 v[60:63], a[20:23], v[44:47], v[60:63]\n" \
    "ds_read_b128 a[120:123], v130 offset:14336\n" \
    "v_xor_b32 v39, 0x800, v37\n" \
    "v_xor_b32 v132, 0xc00, v37\n" \
    "v_mfma_f32_16x16x32_bf16 v[56:59], a[8:11], v[48:51], v[56:59]\n" \
    "ds_read_b128 a[108:111], v130 offset:13312\n" \
    "v_mul_f32 v64, v64, v36\n" \
    "v_mul_f32 v65, v65, v36\n" \
    "v_mfma_f32_16x16x32_bf16 v[60:63], a[24:27], v[48:51], v[60:63]\n" \
    "ds_read_b128 a[124:127], v130 offset:15360\n" \
    "v_mul_f32 v66, v66, v36\n" \
    "v_mul_f32 v67, v67, v36\n" \
    "v_mfma_f32_16x16x32_bf16 v[56:59], a[12:15], v[52:55], v[56:59]\n" \
    "v_cvt_pk_bf16_f32 v100, v64, v65\n" \
    "v_cvt_pk_bf16_f32 v101, v66, v67\n" \
    "v_mfma_f32_16x16x32_bf16 v[60:63], a[28:31], v[52:55], v[60:63]\n" \
    "buffer_store_dwordx2 v[100:101], v135, %[rw], s47 offen sc1\n" \
    "v_mfma_f32_16x16x32_bf16 v[64:67], a[32:35], v[40:43], 0\n" \
    "v_mul_f32 v56, v56, v36\n" \
    "v_mul_f32 v57, v57, v36\n" \
    "v_mfma_f32_16x16x32_bf16 v[68:71], a[48:51], v[40:43], 0\n" \
    "v_mul_f32 v58, v58, v36\n" \
    "v_mul_f32 v59, v59, v36\n" \
    "v_mfma_f32_16x16x32_bf16 v[64:67], a[36:39], v[44:47], v[64:67]\n" \
    "v_mul_f32 v60, v60, v36\n" \
    "v_mul_f32 v61, v61, v36\n" \
    "v_mfma_f32_16x16x32_bf16 v[68:71], a[52:55], v[44:47], v[68:71]\n" \
    "v_mul_f32 v62, v62, v36\n" \
    "v_mul_f32 v63, v63, v36\n" \
    "v_mfma_f32_16x16x32_bf16 v[64:67], a[40:43], v[48:51], v[64:67]\n" \
    "v_cvt_pk_bf16_f32 v96, v56, v57\n" \
    "v_cvt_pk_bf16_f32 v97, v58, v59\n" \
    "v_mfma_f32_16x16x32_bf16 v[68:71], a[56:59], v[48:51], v[68:71]\n" \
    "v_cvt_pk_bf16_f32 v98, v60, v61\n" \
    "v_cvt_pk_bf16_f32 v99, v62, v63\n" \
    "v_mfma_f32_16x16x32_bf16 v[64:67], a[44:47], v[52:55], v[64:67]\n" \
    "buffer_store_dwordx4 v[96:99], v134, %[rw], s46 offen sc1\n" \
    "s_add_i32 s46, s46, 0x20000\n" \
    "v_mfma_f32_16x16x32_bf16 v[68:71], a[60:63], v[52:55], v[68:71]\n" \
    "v_mov_b64 v[80:81], 0\n" \
    "v_mov_b64 v[82:83], 0\n" \
    "v_mfma_f32_16x16x32_bf16 v[56:59], a[64:67], v[40:43], 0\n" \
    "v_mul_f32 v64, v64, v36\n" \
    "v_mul_f32 v65, v65, v36\n" \
    "v_mfma_f32_16x16x32_bf16 v[60:63], a[80:83], v[40:43], 0\n" \
    "v_mul_f32 v66, v66, v36\n" \
    "v_mul_f32 v67, v67, v36\n" \
    "v_mfma_f32_16x16x32_bf16 v[56:59], a[68:71], v[44:47], v[56:59]\n" \
    "v_mul_f32 v68, v68, v36\n" \
    "v_mul_f32 v69, v69, v36\n" \
    "v_mfma_f32_16x16x32_bf16 v[60:63], a[84:87], v[44:47], v[60:63]\n" \
    "v_mul_f32 v70, v70, v36\n" \
    "v_mul_f32 v71, v71, v36\n" \
    "s_waitcnt lgkmcnt(11)\n" \
    "v_mfma_f32_16x16x32_bf16 v[56:59], a[72:75], v[48:51], v[56:59]\n" \
    "v_cvt_pk_bf16_f32 v100, v64, v65\n" \
    "v_cvt_pk_bf16_f32 v101, v66, v67\n" \
    "s_waitcnt lgkmcnt(10)\n" \
    "v_mfma_f32_16x16x32_bf16 v[60:63], a[88:91], v[48:51], v[60:63]\n" \
    "s_waitcnt vmcnt(6) lgkmcnt(0)\n" \
    "s_barrier\n" \
    "v_cvt_pk_bf16_f32 v102, v68, v69\n" \
    "v_cvt_pk_bf16_f32 v103, v70, v71\n" \
    "v_mfma_f32_16x16x32_bf16 v[56:59], a[76:79], v[52:55], v[56:59]\n" \
    "ds_read_b128 v[0:3], v37 offset:0\n" \
    "buffer_store_dwordx4 v[100:103], v134, %[rw], s46 offen sc1\n" \
    "s_add_i32 s46, s46, 0x20000\n" \
    "v_mfma_f32_16x16x32_bf16 v[60:63], a[92:95], v[52:55], v[60:63]\n" \
    "ds_read_b128 v[4:7], v38 offset:0\n" \
    "v_mov_b64 v[84:85], 0\n" \
    "v_mov_b64 v[86:87], 0\n" \
    "v_mfma_f32_16x16x32_bf16 v[64:67], a[96:99], v[40:43], 0\n" \
    "ds_read_b128 v[8:11], v39 offset:0\n" \
    "v_mul_f32 v56, v56, v36\n" \
    "v_mul_f32 v57, v57, v36\n" \
    "v_mfma_f32_16x16x32_bf16 v[68:71], a[112:115], v[40:43], 0\n" \
    "ds_read_b128 v[12:15], v132 offset:0\n" \
    "v_mul_f32 v58, v58, v36\n" \
    "v_mul_f32 v59, v59, v36\n" \
    "v_mfma_f32_16x16x32_bf16 v[64:67], a[100:103], v[44:47], v[64:67]\n" \
    "ds_read_b128 v[16:19], v37 offset:4096\n" \
    "v_mul_f32 v60, v60, v36\n" \
    "v_mul_f32 v61, v61, v36\n" \
    "v_mfma_f32_16x16x32_bf16 v[68:71], a[116:119], v[44:47], v[68:71]\n" \
    "ds_read_b128 v[20:23], v38 offset:4096\n" \
    "v_mul_f32 v62, v62, v36\n" \
    "v_mul_f32 v63, v63, v36\n" \
    "v_mfma_f32_16x16x32_bf16 v[64:67], a[104:107], v[48:51], v[64:67]\n" \
    "ds_read_b128 v[24:27], v39 offset:4096\n" \
    "v_cvt_pk_bf16_f32 v96, v56, v57\n" \
    "v_cvt_pk_bf16_f32 v97, v58, v59\n" \
    "v_mfma_f32_16x16x32_bf16 v[68:71], a[120:123], v[48:51], v[68:71]\n" \
    "ds_read_b128 v[28:31], v132 offset:4096\n" \
    "v_cvt_pk_bf16_f32 v98, v60, v61\n" \
    "v_cvt_pk_bf16_f32 v99, v62, v63\n" \
    "v_mfma_f32_16x16x32_bf16 v[64:67], a[108:111], v[52:55], v[64:67]\n" \
    "ds_read_b128 v[32:35], v126 offset:0\n" \
    "buffer_store_dwordx4 v[96:99], v134, %[rw], s46 offen sc1\n" \
    "s_add_i32 s46, s46, 0x20000\n" \
    "v_mfma_f32_16x16x32_bf16 v[68:71], a[124:127], v[52:55], v[68:71]\n" \
    "s_waitcnt lgkmcnt(3)\n" \
    "33:\n" \
    "v_smfmac_f32_16x16x64_bf16 v[80:83], v[0:3], a[128:135], v124\n" \
    "v_mul_f32 v64, v64, v36\n" \
    "v_smfmac_f32_16x16x64_bf16 v[80:83], v[4:7], a[144:151], v124\n" \
    "v_mul_f32 v65, v65, v36\n" \
    "v_smfmac_f32_16x16x64_bf16 v[80:83], v[8:11], a[160:167], v124\n" \
    "s_waitcnt lgkmcnt(0)\n" \
    "v_mul_f32 v66, v66, v36\n" \
    "v_smfmac_f32_16x16x64_bf16 v[80:83], v[12:15], a[176:183], v124\n" \
    "ds_read_b128 v[40:43], v127 offset:16384\n" \
    "v_mul_f32 v67, v67, v36\n" \
    "v_smfmac_f32_16x16x64_bf16 v[80:83], v[16:19], a[192:199], v124\n" \
    "ds_read_b128 v[44:47], v127 offset:17408\n" \
    "v_mul_f32 v68, v68, v36\n" \
    "v_smfmac_f32_16x16x64_bf16 v[80:83], v[20:23], a[208:215], v124\n" \
    "ds_read_b128 v[48:51], v127 offset:18432\n" \
    "v_mul_f32 v69, v69, v36\n" \
    "v_smfmac_f32_16x16x64_bf16 v[80:83], v[24:27], a[224:231], v124\n" \
    "ds_read_b128 v[52:55], v127 offset:19456\n" \
    "v_mul_f32 v70, v70, v36\n" \
    "v_mul_f32 v71, v71, v36\n" \
    "v_smfmac_f32_16x16x64_bf16 v[80:83], v[28:31], a[240:247], v124\n" \
    "ds_read_b128 v[56:59], v127 offset:20480\n" \
    "v_cvt_pk_bf16_f32 v100, v64, v65\n" \
    "v_cvt_pk_bf16_f32 v101, v66, v67\n" \
    "v_smfmac_f32_16x16x64_bf16 v[80:83], v[32:35], v[152:159], v124\n" \
    "ds_read_b128 v[60:63], v127 offset:21504\n" \
    "v_cvt_pk_bf16_f32 v102, v68, v69\n" \
    "v_cvt_pk_bf16_f32 v103, v70, v71\n" \
    "v_smfmac_f32_16x16x64_bf16 v[84:87], v[0:3], a[136:143], v124\n" \
    "ds_read_b128 v[64:67], v127 offset:22528\n" \
    "buffer_store_dwordx4 v[100:103], v134, %[rw], s46 offen sc1\n" \
    "v_smfmac_f32_16x16x64_bf16 v[84:87], v[4:7], a[152:159], v124\n" \
    "ds_read_b128 v[68:71], v127 offset:23552\n" \
    "v_add_f32 v80, v80, v82\n" \
    "v_add_f32 v81, v81, v83\n" \
    "v_smfmac_f32_16x16x64_bf16 v[84:87], v[8:11], a[168:175], v124\n" \
    "s_waitcnt vmcnt(5)\n" \
    "v_cmp_u_f32 vcc, v80, v81\n" \
    "v_max3_u32 v128, v72, v73, v72\n" \
    "v_smfmac_f32_16x16x64_bf16 v[84:87], v[12:15], a[184:191], v124\n" \
    "v_max3_u32 v128, v74, v75, v128\n" \
    "v_max3_u32 v128, v76, v77, v128\n" \
    "v_smfmac_f32_16x16x64_bf16 v[84:87], v[16:19], a[200:207], v124\n" \
    "s_cbranch_vccnz 32f\n" \
    "34:\n" \
    "v_max3_u32 v128, v78, v79, v128\n" \
    "v_max3_u32 v128, v104, v105, v128\n" \
    "v_smfmac_f32_16x16x64_bf16 v[84:87], v[20:23], a[216:223], v124\n" \
    "v_max3_u32 v128, v106, v107, v128\n" \
    "v_max3_u32 v128, v108, v109, v128\n" \
    "v_smfmac_f32_16x16x64_bf16 v[84:87], v[24:27], a[232:239], v124\n" \
    "v_max3_u32 v128, v110, v111, v128\n" \
    "v_cmp_eq_u32 vcc, -1, v128\n" \
    "v_smfmac_f32_16x16x64_bf16 v[84:87], v[28:31], a[248:255], v124\n" \
    "s_waitcnt lgkmcnt(0)\n" \
    "v_smfmac_f32_16x16x64_bf16 v[84:87], v[32:35], v[160:167], v124\n" \
    "s_cbranch_vccnz 28f\n" \
    "29:\n" \
    "v_mfma_f32_16x16x32_bf16 v[152:155], v[40:43], v[72:75], v[88:91]\n" \
    "ds_read_b128 a[128:131], v133 offset:0\n" \
    "v_mov_b64 v[96:97], 0\n" \
    "v_mov_b64 v[98:99], 0\n" \
    "v_mfma_f32_16x16x32_bf16 v[156:159], v[44:47], v[72:75], v[92:95]\n" \
    "ds_read_b128 a[132:135], v133 offset:1024\n" \
    "v_add_f32 v84, v84, v86\n" \
    "v_add_f32 v85, v85, v87\n" \
    "v_mfma_f32_16x16x32_bf16 v[152:155], v[48:51], v[76:79], v[152:155]\n" \
    "ds_read_b128 a[136:139], v133 offset:2048\n" \
    "v_cvt_pk_bf16_f32 v80, v80, v84\n" \
    "v_cvt_pk_bf16_f32 v81, v81, v85\n" \
    "v_mfma_f32_16x16x32_bf16 v[156:159], v[52:55], v[76:79], v[156:159]\n" \
    "ds_write2_b32 v129, v80, v81 offset0:0 offset1:144\n" \
    "v_mfma_f32_16x16x32_bf16 v[152:155], v[56:59], v[104:107], v[152:155]\n" \
    "ds_read_b128 a[140:143], v133 offset:3072\n" \
    "v_mov_b64 v[160:161], 0\n" \
    "v_mov_b64 v[162:163], 0\n" \
    "v_mfma_f32_16x16x32_bf16 v[156:159], v[60:63], v[104:107], v[156:159]\n" \
    "ds_read_b128 a[144:147], v133 offset:16384\n" \
    "v_mov_b64 v[100:101], 0\n" \
    "v_mov_b64 v[102:103], 0\n" \
    "v_mfma_f32_16x16x32_bf16 v[152:155], v[64:67], v[108:111], v[152:155]\n" \
    "ds_read_b128 a[148:151], v133 offset:17408\n" \
    "v_lshrrev_b32 v42, 1, v37\n" \
    "v_lshrrev_b32 v43, 1, v127\n" \
    "v_mfma_f32_16x16x32_bf16 v[156:159], v[68:71], v[108:111], v[156:159]\n" \
    "s_waitcnt lgkmcnt(3)\n" \
    "s_barrier\n" \
    "ds_read_b128 v[80:83], v131 offset:0\n" \
    "ds_read_b128 v[84:87], v131 offset:128\n" \
    "v_smfmac_f32_16x16x64_bf16 v[96:99], v[16:19], v[168:175], v124\n" \
    "ds_read_b128 a[152:155], v133 offset:18432\n" \
    "v_mov_b32 v61, 0\n" \
    "v_smfmac_f32_16x16x64_bf16 v[96:99], v[20:23], v[184:191], v124\n" \
    "ds_read_b128 a[156:159], v133 offset:19456\n" \
    "v_max3_f32 v40, v152, v153, v154\n" \
    "v_smfmac_f32_16x16x64_bf16 v[96:99], v[24:27], v[200:207], v124\n" \
    "ds_read_b128 a[160:163], v133 offset:32768\n" \
    "s_waitcnt lgkmcnt(3)\n" \
    "v_max3_f32 v41, v155, v156, v157\n" \
    "v_smfmac_f32_16x16x64_bf16 v[160:163], v[80:83], v[72:79], v124\n" \
    "ds_read_b128 a[164:167], v133 offset:33792\n" \
    "v_max3_f32 v40, v40, v158, v159\n" \
    "v_smfmac_f32_16x16x64_bf16 v[160:163], v[84:87], v[104:111], v124\n" \
    "ds_read_b128 a[168:171], v133 offset:34816\n" \
    "v_mov_b32 v62, 0\n" \
    "v_smfmac_f32_16x16x64_bf16 v[96:99], v[28:31], v[216:223], v124\n" \
    "ds_read_b128 a[172:175], v133 offset:35840\n" \
    "v_smfmac_f32_16x16x64_bf16 v[96:99], v[32:35], v[232:239], v124\n" \
    "ds_read_b128 a[176:179], v133 offset:49152\n" \
    "v_add_f32 v160, v160, v162\n" \
    "v_add_f32 v161, v161, v163\n" \
    "v_max3_f32 v41, v41, v160, v161\n" \
    "v_smfmac_f32_16x16x64_bf16 v[100:103], v[16:19], v[176:183], v124\n" \
    "ds_read_b128 a[180:183], v133 offset:50176\n" \
    "v_max_f32 v40, v40, v41\n" \
    "v_max_f32 v41, v40, v41\n" \
    "v_smfmac_f32_16x16x64_bf16 v[100:103], v[20:23], v[192:199], v124\n" \
    "ds_read_b128 a[192:195], v127 offset:24576\n" \
    "v_permlane16_swap_b32 v40, v41\n" \
    "v_max_f32 v40, v40, v41\n" \
    "v_max_f32 v41, v40, v41\n" \
    "v_smfmac_f32_16x16x64_bf16 v[96:99], v[0:3], a[128:135], v124\n" \
    "ds_read_b128 a[196:199], v127 offset:25600\n" \
    "v_permlane32_swap_b32 v40, v41\n" \
    "v_max_f32 v40, v40, v41\n" \
    "s_nop 0\n" \
    "v_smfmac_f32_16x16x64_bf16 v[96:99], v[4:7], a[144:151], v124\n" \
    "ds_read_b128 a[200:203], v127 offset:26624\n" \
    "s_waitcnt lgkmcnt(3)\n" \
    "v_sub_f32 v152, v152, v40\n" \
    "v_sub_f32 v153, v153, v40\n" \
    "v_exp_f32 v152, v152\n" \
    "v_smfmac_f32_16x16x64_bf16 v[96:99], v[8:11], a[160:167], v124\n" \
    "ds_read_b128 a[204:207], v127 offset:27648\n" \
    "v_exp_f32 v153, v153\n" \
    "v_sub_f32 v154, v154, v40\n" \
    "v_sub_f32 v155, v155, v40\n" \
    "v_smfmac_f32_16x16x64_bf16 v[96:99], v[12:15], a[176:183], v124\n" \
    "ds_read_b128 a[208:211], v127 offset:28672\n" \
    "v_exp_f32 v154, v154\n" \
    "v_exp_f32 v155, v155\n" \
    "v_cvt_pk_bf16_f32 v56, v152, v153\n" \
    "v_smfmac_f32_16x16x64_bf16 v[100:103], v[24:27], v[208:215], v124\n" \
    "ds_read_b128 a[212:215], v127 offset:29696\n" \
    "v_sub_f32 v156, v156, v40\n" \
    "v_sub_f32 v157, v157, v40\n" \
    "v_exp_f32 v156, v156\n" \
    "v_smfmac_f32_16x16x64_bf16 v[100:103], v[28:31], v[224:231], v124\n" \
    "ds_read_b128 a[216:219], v127 offset:30720\n" \
    "v_exp_f32 v157, v157\n" \
    "v_cvt_pk_bf16_f32 v57, v154, v155\n" \
    "v_sub_f32 v158, v158, v40\n" \
    "v_smfmac_f32_16x16x64_bf16 v[100:103], v[32:35], v[240:247], v124\n" \
    "ds_read_b128 a[220:223], v127 offset:31744\n" \
    "v_sub_f32 v159, v159, v40\n" \
    "v_exp_f32 v158, v158\n" \
    "v_exp_f32 v159, v159\n" \
    "v_smfmac_f32_16x16x64_bf16 v[100:103], v[12:15], v[248:255], v124\n" \
    "v_cvt_pk_bf16_f32 v58, v156, v157\n" \
    "v_sub_f32 v160, v160, v40\n" \
    "v_sub_f32 v161, v161, v40\n" \
    "v_smfmac_f32_16x16x64_bf16 v[100:103], v[0:3], a[136:143], v124\n" \
    "v_exp_f32 v160, v160\n" \
    "v_exp_f32 v161, v161\n" \
    "v_cvt_pk_bf16_f32 v59, v158, v159\n" \
    "v_smfmac_f32_16x16x64_bf16 v[100:103], v[4:7], a[152:159], v124\n" \
    "v_cvt_pk_bf16_f32 v60, v160, v161\n" \
    "v_cvt_pk_bf16_f32 v63, v160, v161\n" \
    "v_smfmac_f32_16x16x64_bf16 v[100:103], v[8:11], a[168:175], v124\n" \
    "s_waitcnt lgkmcnt(0)\n" \
    "v_mfma_f32_16x16x32_bf16 v[36:39], v[120:123], v[56:59], 0\n" \
    "v_add_f32 v96, v96, v98\n" \
    "v_add_f32 v97, v97, v99\n" \
    "v_mfma_f32_16x16x32_bf16 v[0:3], a[192:195], v[56:59], 0\n" \
    "v_add_f32 v100, v100, v102\n" \
    "v_add_f32 v101, v101, v103\n" \
    "v_mfma_f32_16x16x32_bf16 v[4:7], a[196:199], v[56:59], 0\n" \
    "v_cvt_pk_bf16_f32 v96, v96, v97\n" \
    "v_cvt_pk_bf16_f32 v97, v100, v101\n" \
    "v_mfma_f32_16x16x32_bf16 v[8:11], a[200:203], v[56:59], 0\n" \
    "ds_write_b64 v42, v[96:97] offset:9216\n" \
    "v_mfma_f32_16x16x32_bf16 v[12:15], a[204:207], v[56:59], 0\n" \
    "v_lshrrev_b32 v169, 8, v127\n" \
    "v_and_b32 v170, 2, v169\n" \
    "v_mfma_f32_16x16x32_bf16 v[16:19], a[208:211], v[56:59], 0\n" \
    "v_mad_u32_u24 v169, v170, 3, v169\n" \
    "v_bfe_u32 v168, v127, 6, 1\n" \
    "v_mfma_f32_16x16x32_bf16 v[20:23], a[212:215], v[56:59], 0\n" \
    "v_bfe_u32 v170, v127, 4, 2\n" \
    "v_lshl_add_u32 v169, v170, 1, v169\n" \
    "v_mfma_f32_16x16x32_bf16 v[24:27], a[216:219], v[56:59], 0\n" \
    "s_waitcnt lgkmcnt(0)\n" \
    "s_barrier\n" \
    "ds_read_b64 v[64:65], v43 offset:9216\n" \
    "ds_read_b64 v[66:67], v43 offset:9728\n" \
    "ds_read_b64 v[68:69], v43 offset:10240\n" \
    "ds_read_b64 v[70:71], v43 offset:10752\n" \
    "v_mfma_f32_16x16x32_bf16 v[28:31], a[220:223], v[56:59], 0\n" \
    "v_mul_u32_u24 v168, 1152, v168\n" \
    "v_lshl_add_u32 v168, v169, 2, v168\n" \
    "v_mfma_f32_16x16x16_bf16 v[36:39], v[120:121], v[60:61], v[36:39]\n" \
    "s_waitcnt lgkmcnt(2)\n" \
    "v_mfma_f32_16x16x16_bf16 v[0:3], v[64:65], v[60:61], v[0:3]\n" \
    "v_sub_f32 v169, 1.0, v114\n" \
    "v_lshlrev_b32 v172, 16, v113\n" \
    "v_mfma_f32_16x16x16_bf16 v[4:7], v[64:65], v[62:63], v[4:7]\n" \
    "s_waitcnt lgkmcnt(0)\n" \
    "v_mul_f32 v172, v172, v169\n" \
    "v_and_b32 v170, 16, v127\n" \
    "v_mfma_f32_16x16x16_bf16 v[8:11], v[66:67], v[60:61], v[8:11]\n" \
    "v_mov_b32 v169, 0x3f80\n" \
    "v_lshlrev_b32 v170, v170, v169\n" \
    "v_mfma_f32_16x16x16_bf16 v[12:15], v[66:67], v[62:63], v[12:15]\n" \
    "v_mov_b32_dpp v173, v172 row_ror:8 row_mask:0xf bank_mask:0xf\n" \
    "v_mov_b64 v[176:177], 0\n" \
    "v_mfma_f32_16x16x16_bf16 v[16:19], v[68:69], v[60:61], v[16:19]\n" \
    "v_mov_b64 v[178:179], 0\n" \
    "v_mov_b32_dpp v176, v170 quad_perm:[0,1,2,3] row_mask:0xf bank_mask:0x1\n" \
    "v_mfma_f32_16x16x16_bf16 v[20:23], v[68:69], v[62:63], v[20:23]\n" \
    "v_mov_b32_dpp v177, v170 quad_perm:[0,1,2,3] row_mask:0xf bank_mask:0x2\n" \
    "v_mov_b32_dpp v178, v170 quad_perm:[0,1,2,3] row_mask:0xf bank_mask:0x4\n" \
    "v_mfma_f32_16x16x16_bf16 v[24:27], v[70:71], v[60:61], v[24:27]\n" \
    "v_mov_b32_dpp v179, v170 quad_perm:[0,1,2,3] row_mask:0xf bank_mask:0x8\n" \
    "v_mfma_f32_16x16x16_bf16 v[28:31], v[70:71], v[62:63], v[28:31]\n" \
    "v_cvt_pk_bf16_f32 v40, v0, v4\n" \
    "v_cvt_pk_bf16_f32 v41, v1, v5\n" \
    "v_cvt_pk_bf16_f32 v42, v2, v6\n" \
    "v_cvt_pk_bf16_f32 v43, v3, v7\n" \
    "v_cvt_pk_bf16_f32 v44, v8, v12\n" \
    "v_cvt_pk_bf16_f32 v45, v9, v13\n" \
    "v_mfma_f32_16x16x32_bf16 v[80:83], v[136:139], v[40:43], 0\n" \
    "s_mov_b32 s46, %[tq]\n" \
    "v_cvt_pk_bf16_f32 v46, v10, v14\n" \
    "v_cvt_pk_bf16_f32 v47, v11, v15\n" \
    "v_mfma_f32_16x16x32_bf16 v[72:75], a[0:3], v[40:43], 0\n" \
    "s_add_i32 s47, %[tx], 0x40000\n" \
    "v_cvt_pk_bf16_f32 v48, v16, v20\n" \
    "v_cvt_pk_bf16_f32 v49, v17, v21\n" \
    "v_mfma_f32_16x16x32_bf16 v[80:83], v[140:143], v[44:47], v[80:83]\n" \
    "v_cvt_pk_bf16_f32 v50, v18, v22\n" \
    "v_cvt_pk_bf16_f32 v51, v19, v23\n" \
    "v_mfma_f32_16x16x32_bf16 v[76:79], a[16:19], v[40:43], 0\n" \
    "v_cvt_pk_bf16_f32 v52, v24, v28\n" \
    "v_cvt_pk_bf16_f32 v53, v25, v29\n" \
    "v_mfma_f32_16x16x32_bf16 v[80:83], v[144:147], v[48:51], v[80:83]\n" \
    "v_cvt_pk_bf16_f32 v54, v26, v30\n" \
    "v_cvt_pk_bf16_f32 v55, v27, v31\n" \
    "v_mfma_f32_16x16x32_bf16 v[72:75], a[4:7], v[44:47], v[72:75]\n" \
    "v_rcp_f32 v36, v36\n" \
    "v_mfma_f32_16x16x32_bf16 v[80:83], v[148:151], v[52:55], v[80:83]\n" \
    "s_and_b32 s94, %[u], 15\n" \
    "s_mul_i32 s94, s94, 2304\n" \
    "v_mfma_f32_16x16x32_bf16 v[76:79], a[20:23], v[44:47], v[76:79]\n" \
    "s_andn2_b32 s77, %[u], 15\n" \
    "s_lshl2_add_u32 s94, s77, s94\n" \
    "v_mfma_f32_16x16x32_bf16 v[72:75], a[8:11], v[48:51], v[72:75]\n" \
    "v_mul_f32 v80, v80, v36\n" \
    "v_mul_f32 v81, v81, v36\n" \
    "v_mfma_f32_16x16x32_bf16 v[76:79], a[24:27], v[48:51], v[76:79]\n" \
    "v_mul_f32 v82, v82, v36\n" \
    "v_mul_f32 v83, v83, v36\n" \
    "v_mfma_f32_16x16x32_bf16 v[72:75], a[12:15], v[52:55], v[72:75]\n" \
    "v_cvt_pk_bf16_f32 v100, v80, v81\n" \
    "v_cvt_pk_bf16_f32 v101, v82, v83\n" \
    "v_mfma_f32_16x16x32_bf16 v[76:79], a[28:31], v[52:55], v[76:79]\n" \
    "buffer_store_dwordx2 v[100:101], v135, %[rw], s47 offen sc1\n" \
    "v_mfma_f32_16x16x32_bf16 v[80:83], a[32:35], v[40:43], 0\n" \
    "v_mul_f32 v72, v72, v36\n" \
    "v_mul_f32 v73, v73, v36\n" \
    "v_mfma_f32_16x16x32_bf16 v[84:87], a[48:51], v[40:43], 0\n" \
    "v_mul_f32 v74, v74, v36\n" \
    "v_mul_f32 v75, v75, v36\n" \
    "v_mfma_f32_16x16x32_bf16 v[80:83], a[36:39], v[44:47], v[80:83]\n" \
    "v_mul_f32 v76, v76, v36\n" \
    "v_mul_f32 v77, v77, v36\n" \
    "v_mfma_f32_16x16x32_bf16 v[84:87], a[52:55], v[44:47], v[84:87]\n" \
    "v_mul_f32 v78, v78, v36\n" \
    "v_mul_f32 v79, v79, v36\n" \
    "v_mfma_f32_16x16x32_bf16 v[80:83], a[40:43], v[48:51], v[80:83]\n" \
    "v_cvt_pk_bf16_f32 v96, v72, v73\n" \
    "v_cvt_pk_bf16_f32 v97, v74, v75\n" \
    "v_mfma_f32_16x16x32_bf16 v[84:87], a[56:59], v[48:51], v[84:87]\n" \
    "v_cvt_pk_bf16_f32 v98, v76, v77\n" \
    "v_cvt_pk_bf16_f32 v99, v78, v79\n" \
    "v_mfma_f32_16x16x32_bf16 v[80:83], a[44:47], v[52:55], v[80:83]\n" \
    "buffer_store_dwordx4 v[96:99], v134, %[rw], s46 offen sc1\n" \
    "s_add_i32 s46, s46, 0x20000\n" \
    "v_mfma_f32_16x16x32_bf16 v[84:87], a[60:63], v[52:55], v[84:87]\n" \
    "s_add_i32 s94, s94, 2322432\n" \
    "v_mfma_f32_16x16x32_bf16 v[72:75], a[64:67], v[40:43], 0\n" \
    "v_mul_f32 v80, v80, v36\n" \
    "v_mul_f32 v81, v81, v36\n" \
    "v_mfma_f32_16x16x32_bf16 v[76:79], a[80:83], v[40:43], 0\n" \
    "v_mul_f32 v82, v82, v36\n" \
    "v_mul_f32 v83, v83, v36\n" \
    "v_mfma_f32_16x16x32_bf16 v[72:75], a[68:71], v[44:47], v[72:75]\n" \
    "v_mul_f32 v84, v84, v36\n" \
    "v_mul_f32 v85, v85, v36\n" \
    "v_mfma_f32_16x16x32_bf16 v[76:79], a[84:87], v[44:47], v[76:79]\n" \
    "v_mul_f32 v86, v86, v36\n" \
    "v_mul_f32 v87, v87, v36\n" \
    "v_mfma_f32_16x16x32_bf16 v[72:75], a[72:75], v[48:51], v[72:75]\n" \
    "v_cvt_pk_bf16_f32 v100, v80, v81\n" \
    "v_cvt_pk_bf16_f32 v101, v82, v83\n" \
    "v_mfma_f32_16x16x32_bf16 v[76:79], a[88:91], v[48:51], v[76:79]\n" \
    "v_cvt_pk_bf16_f32 v102, v84, v85\n" \
    "v_cvt_pk_bf16_f32 v103, v86, v87\n" \
    "v_mfma_f32_16x16x32_bf16 v[72:75], a[76:79], v[52:55], v[72:75]\n" \
    "buffer_store_dwordx4 v[100:103], v134, %[rw], s46 offen sc1\n" \
    "s_add_i32 s46, s46, 0x20000\n" \
    "v_mfma_f32_16x16x32_bf16 v[76:79], a[92:95], v[52:55], v[76:79]\n" \
    "v_mfma_f32_16x16x32_bf16 v[80:83], a[96:99], v[40:43], 0\n" \
    "v_mul_f32 v72, v72, v36\n" \
    "v_mul_f32 v73, v73, v36\n" \
    "v_mfma_f32_16x16x32_bf16 v[84:87], a[112:115], v[40:43], 0\n" \
    "v_mul_f32 v74, v74, v36\n" \
    "v_mul_f32 v75, v75, v36\n" \
    "v_mfma_f32_16x16x32_bf16 v[80:83], a[100:103], v[44:47], v[80:83]\n" \
    "v_mul_f32 v76, v76, v36\n" \
    "v_mul_f32 v77, v77, v36\n" \
    "v_mfma_f32_16x16x32_bf16 v[84:87], a[116:119], v[44:47], v[84:87]\n" \
    "v_mul_f32 v78, v78, v36\n" \
    "v_mul_f32 v79, v79, v36\n" \
    "v_mfma_f32_16x16x32_bf16 v[80:83], a[104:107], v[48:51], v[80:83]\n" \
    "v_cvt_pk_bf16_f32 v96, v72, v73\n" \
    "v_cvt_pk_bf16_f32 v97, v74, v75\n" \
    "v_mfma_f32_16x16x32_bf16 v[84:87], a[120:123], v[48:51], v[84:87]\n" \
    "v_cvt_pk_bf16_f32 v98, v76, v77\n" \
    "v_cvt_pk_bf16_f32 v99, v78, v79\n" \
    "v_mfma_f32_16x16x32_bf16 v[80:83], a[108:111], v[52:55], v[80:83]\n" \
    "buffer_store_dwordx4 v[96:99], v134, %[rw], s46 offen sc1\n" \
    "s_add_i32 s46, s46, 0x20000\n" \
    "v_mfma_f32_16x16x32_bf16 v[84:87], a[124:127], v[52:55], v[84:87]\n" \
    "s_memtime vcc\n" \
    "s_nop 2\n" \
    "v_mul_f32 v80, v80, v36\n" \
    "v_mul_f32 v81, v81, v36\n" \
    "v_mul_f32 v82, v82, v36\n" \
    "v_mul_f32 v83, v83, v36\n" \
    "v_mul_f32 v84, v84, v36\n" \
    "v_mul_f32 v85, v85, v36\n" \
    "v_mul_f32 v86, v86, v36\n" \
    "v_mul_f32 v87, v87, v36\n" \
    "v_cvt_pk_bf16_f32 v100, v80, v81\n" \
    "v_cvt_pk_bf16_f32 v101, v82, v83\n" \
    "v_cvt_pk_bf16_f32 v102, v84, v85\n" \
    "v_cvt_pk_bf16_f32 v103, v86, v87\n" \
    "buffer_store_dwordx4 v[100:103], v134, %[rw], s46 offen sc1\n" \
    "s_waitcnt lgkmcnt(0)\n" \
    "s_add_i32 s90, vcc_lo, %[fw]\n" \
    "35:\n" \
    "s_memtime vcc\n" \
    "s_waitcnt lgkmcnt(0)\n" \
    "s_sub_i32 s75, s90, vcc_lo\n" \
    "s_cmp_gt_i32 s75, 0\n" \
    "s_cbranch_scc1 35b\n" \
    "36:\n" \
    "buffer_load_dwordx4 v[0:3], v127, %[rw], %[ua] offen offset:0 sc1\n" \
    "buffer_load_dwordx4 v[4:7], v127, %[rw], %[ua] offen offset:1024 sc1\n" \
    "buffer_load_dwordx4 v[8:11], v127, %[rw], %[ua] offen offset:2048 sc1\n" \
    "buffer_load_dwordx4 v[12:15], v127, %[rw], %[ua] offen offset:3072 sc1\n" \
    "s_waitcnt vmcnt(3)\n" \
    "v_mfma_f32_16x16x32_bf16 v[32:35], v[176:179], v[0:3], 0\n" \
    "v_max3_u32 v40, v0, v1, v0\n" \
    "v_max3_u32 v40, v2, v3, v40\n" \
    "s_waitcnt vmcnt(2)\n" \
    "v_mfma_f32_16x16x32_bf16 v[32:35], v[176:179], v[4:7], v[32:35]\n" \
    "v_max3_u32 v40, v4, v5, v40\n" \
    "v_max3_u32 v40, v6, v7, v40\n" \
    "s_waitcnt vmcnt(1)\n" \
    "v_mfma_f32_16x16x32_bf16 v[32:35], v[176:179], v[8:11], v[32:35]\n" \
    "v_max3_u32 v40, v8, v9, v40\n" \
    "v_max3_u32 v40, v10, v11, v40\n" \
    "s_waitcnt vmcnt(0)\n" \
    "v_mfma_f32_16x16x32_bf16 v[32:35], v[176:179], v[12:15], v[32:35]\n" \
    "v_max3_u32 v40, v12, v13, v40\n" \
    "v_max3_u32 v40, v14, v15, v40\n" \
    "v_cmp_eq_u32 vcc, -1, v40\n" \
    "s_nop 3\n" \
    "s_cbranch_vccnz 38f\n" \
    "39:\n" \
    "v_add_f32_dpp v32, v32, v32 row_ror:8 row_mask:0xf bank_mask:0xf\n" \
    "v_add_f32_dpp v33, v33, v33 row_ror:8 row_mask:0xf bank_mask:0xf\n" \
    "s_mov_b32 exec_lo, 0x00ff00ff\n" \
    "s_mov_b32 exec_hi, 0x00ff00ff\n" \
    "v_fma_f32 v32, v32, v114, v172\n" \
    "v_fma_f32 v33, v33, v114, v173\n" \
    "v_cvt_pk_bf16_f32 v32, v32, v33\n" \
    "buffer_store_dword v32, v168, %[ru], s94 offen sc1\n" \
    "s_mov_b64 exec, -1\n" \
    "s_mov_b32 s36, 63\n" \
    "s_branch 9f\n" \
    "38:\n" \
    "s_add_i32 s39, s39, -1\n" \
    "s_cmp_lt_i32 s39, 0\n" \
    "s_cbranch_scc1 39b\n" \
    "s_add_i32 s30, s30, 0x10000\n" \
    "s_sleep 2\n" \
    "s_branch 36b\n" \
    "21:\n" \
    "s_add_i32 s39, s39, -1\n" \
    "s_cmp_lt_i32 s39, 0\n" \
    "s_cbranch_scc1 22b\n" \
    "buffer_load_dword v100, off, %[rw], s62 sc1\n" \
    "s_waitcnt vmcnt(0)\n" \
    "v_readfirstlane_b32 s38, v100\n" \
    "s_cmp_lt_i32 s38, s91\n" \
    "s_cbranch_scc1 21b\n" \
    "s_branch 22b\n" \
    "23:\n" \
    "s_add_i32 s39, s39, -1\n" \
    "s_cmp_lt_i32 s39, 0\n" \
    "s_cbranch_scc1 24b\n" \
    "s_sleep 4\n" \
    "buffer_load_dwordx2 v[98:99], v113, %[rs], s78 offen sc1\n" \
    "s_waitcnt vmcnt(0)\n" \
    "v_cmp_ne_u16 vcc, s91, v99\n" \
    "s_nop 1\n" \
    "s_cbranch_vccnz 23b\n" \
    "s_branch 24b\n" \
    "26:\n" \
    "s_add_i32 s39, s39, -1\n" \
    "s_cmp_lt_i32 s39, 0\n" \
    "s_cbranch_scc1 27b\n" \
    "s_sleep 4\n" \
    "s_add_i32 s44, %[qb], 0\n" \
    "s_add_i32 s45, s44, 4096\n" \
    "buffer_load_dwordx4 v[136:139], v112, %[rw], s44 offen offset:0 sc1\n" \
    "buffer_load_dwordx4 v[140:143], v112, %[rw], s44 offen offset:2048 sc1\n" \
    "buffer_load_dwordx4 v[144:147], v112, %[rw], s45 offen offset:0 sc1\n" \
    "buffer_load_dwordx4 v[148:151], v112, %[rw], s45 offen offset:2048 sc1\n" \
    "s_waitcnt vmcnt(0)\n" \
    "s_branch 25b\n" \
    "28:\n" \
    "s_add_i32 s39, s39, -1\n" \
    "s_cmp_lt_i32 s39, 0\n" \
    "s_cbranch_scc1 29b\n" \
    "s_sleep 4\n" \
    "s_add_i32 s44, %[qb], 262144\n" \
    "s_add_i32 s45, s44, 4096\n" \
    "buffer_load_dwordx4 v[72:75], v112, %[rw], s44 offen offset:0 sc1\n" \
    "buffer_load_dwordx4 v[76:79], v112, %[rw], s44 offen offset:2048 sc1\n" \
    "buffer_load_dwordx4 v[104:107], v112, %[rw], s45 offen offset:0 sc1\n" \
    "buffer_load_dwordx4 v[108:111], v112, %[rw], s45 offen offset:2048 sc1\n" \
    "s_waitcnt vmcnt(0)\n" \
    "v_max3_u32 v128, v72, v73, v72\n" \
    "v_max3_u32 v128, v74, v75, v128\n" \
    "v_max3_u32 v128, v76, v77, v128\n" \
    "v_max3_u32 v128, v78, v79, v128\n" \
    "v_max3_u32 v128, v104, v105, v128\n" \
    "v_max3_u32 v128, v106, v107, v128\n" \
    "v_max3_u32 v128, v108, v109, v128\n" \
    "v_max3_u32 v128, v110, v111, v128\n" \
    "v_cmp_eq_u32 vcc, -1, v128\n" \
    "s_nop 1\n" \
    "s_cbranch_vccnz 28b\n" \
    "s_branch 29b\n" \
    "30:\n" \
    "s_memtime vcc\n" \
    "s_waitcnt lgkmcnt(0)\n" \
    "s_sub_i32 s75, s90, vcc_lo\n" \
    "s_cmp_gt_i32 s75, 100\n" \
    "s_cbranch_scc1 30b\n" \
    "s_branch 31b\n" \
    "32:\n" \
    "v_max3_u32 v82, v0, v1, v0\n" \
    "v_max3_u32 v82, v2, v3, v82\n" \
    "v_max3_u32 v82, v4, v5, v82\n" \
    "v_max3_u32 v82, v6, v7, v82\n" \
    "v_max3_u32 v82, v8, v9, v82\n" \
    "v_max3_u32 v82, v10, v11, v82\n" \
    "v_max3_u32 v82, v12, v13, v82\n" \
    "v_max3_u32 v82, v14, v15, v82\n" \
    "v_max3_u32 v82, v16, v17, v82\n" \
    "v_max3_u32 v82, v18, v19, v82\n" \
    "v_max3_u32 v82, v20, v21, v82\n" \
    "v_max3_u32 v82, v22, v23, v82\n" \
    "v_max3_u32 v82, v24, v25, v82\n" \
    "v_max3_u32 v82, v26, v27, v82\n" \
    "v_max3_u32 v82, v28, v29, v82\n" \
    "v_max3_u32 v82, v30, v31, v82\n" \
    "v_max3_u32 v82, v32, v33, v82\n" \
    "v_max3_u32 v82, v34, v35, v82\n" \
    "v_cmp_eq_u32 vcc, -1, v82\n" \
    "s_nop 4\n" \
    "s_cbranch_vccz 34b\n" \
    "s_add_i32 s39, s39, -1\n" \
    "s_cmp_lt_i32 s39, 0\n" \
    "s_cbranch_scc1 34b\n" \
    "s_add_i32 s30, s30, 1\n" \
    "s_waitcnt lgkmcnt(0)\n" \
    "s_barrier\n" \
    "v_and_b32 v96, 0x200, v127\n" \
    "s_and_b32 s80, s26, 0x800\n" \
    "v_mad_u32_u24 v96, v96, 3, v127\n" \
    "v_xor_b32 v96, s80, v96\n" \
    "s_mov_b32 m0, s74\n" \
    "s_add_i32 s76, s74, 4096\n" \
    "buffer_load_dwordx4 v125, %[rs], s69 offen sc1 lds\n" \
    "s_mov_b32 m0, s76\n" \
    "v_mov_b64 v[80:81], 0\n" \
    "v_mov_b64 v[82:83], 0\n" \
    "buffer_load_dwordx4 v125, %[rs], s70 offen sc1 lds\n" \
    "s_movk_i32 m0, 0x2000\n" \
    "s_mov_b32 exec_lo, s27\n" \
    "s_mov_b32 exec_hi, s27\n" \
    "buffer_load_dwordx4 v96, %[rs], s71 offen sc1 lds\n" \
    "s_mov_b64 exec, -1\n" \
    "v_mov_b64 v[84:85], 0\n" \
    "v_mov_b64 v[86:87], 0\n" \
    "s_waitcnt vmcnt(0)\n" \
    "s_barrier\n" \
    "ds_read_b128 v[0:3], v37 offset:0\n" \
    "ds_read_b128 v[4:7], v38 offset:0\n" \
    "ds_read_b128 v[8:11], v39 offset:0\n" \
    "ds_read_b128 v[12:15], v132 offset:0\n" \
    "ds_read_b128 v[16:19], v37 offset:4096\n" \
    "ds_read_b128 v[20:23], v38 offset:4096\n" \
    "ds_read_b128 v[24:27], v39 offset:4096\n" \
    "ds_read_b128 v[28:31], v132 offset:4096\n" \
    "ds_read_b128 v[32:35], v126 offset:0\n" \
    "s_waitcnt lgkmcnt(8)\n" \
    "v_smfmac_f32_16x16x64_bf16 v[80:83], v[0:3], a[128:135], v124\n" \
    "s_waitcnt lgkmcnt(7)\n" \
    "v_smfmac_f32_16x16x64_bf16 v[80:83], v[4:7], a[144:151], v124\n" \
    "s_waitcnt lgkmcnt(6)\n" \
    "v_smfmac_f32_16x16x64_bf16 v[80:83], v[8:11], a[160:167], v124\n" \
    "s_waitcnt lgkmcnt(5)\n" \
    "v_smfmac_f32_16x16x64_bf16 v[80:83], v[12:15], a[176:183], v124\n" \
    "s_waitcnt lgkmcnt(4)\n" \
    "v_smfmac_f32_16x16x64_bf16 v[80:83], v[16:19], a[192:199], v124\n" \
    "s_waitcnt lgkmcnt(3)\n" \
    "v_smfmac_f32_16x16x64_bf16 v[80:83], v[20:23], a[208:215], v124\n" \
    "s_waitcnt lgkmcnt(2)\n" \
    "v_smfmac_f32_16x16x64_bf16 v[80:83], v[24:27], a[224:231], v124\n" \
    "s_waitcnt lgkmcnt(1)\n" \
    "v_smfmac_f32_16x16x64_bf16 v[80:83], v[28:31], a[240:247], v124\n" \
    "s_waitcnt lgkmcnt(0)\n" \
    "v_smfmac_f32_16x16x64_bf16 v[80:83], v[32:35], v[152:159], v124\n" \
    "v_smfmac_f32_16x16x64_bf16 v[84:87], v[0:3], a[136:143], v124\n" \
    "v_smfmac_f32_16x16x64_bf16 v[84:87], v[4:7], a[152:159], v124\n" \
    "v_smfmac_f32_16x16x64_bf16 v[84:87], v[8:11], a[168:175], v124\n" \
    "v_smfmac_f32_16x16x64_bf16 v[84:87], v[12:15], a[184:191], v124\n" \
    "v_smfmac_f32_16x16x64_bf16 v[84:87], v[16:19], a[200:207], v124\n" \
    "v_add_f32 v80, v80, v82\n" \
    "v_add_f32 v81, v81, v83\n" \
    "s_branch 32b\n" \
    "7:\n" \
    "s_mov_b32 s43, 2\n" \
    "s_nop 7\n" \
    "s_waitcnt vmcnt(0)\n" \
    "9:\n" \
    "s_mov_b32 m0, s68\n" \
    "s_add_i32 s37, s37, s29\n" \
    "s_waitcnt lgkmcnt(0)\n" \
    "v_mov_b32 v0, s36\n" \
    "v_mov_b32 v1, s37\n" \
    "v_mov_b32 v2, s38\n" \
    "v_mov_b32 v3, s39\n" \
    "v_mov_b32 v4, s40\n" \
    "v_mov_b32 v5, s41\n" \
    "v_mov_b32 v6, s42\n" \
    "v_mov_b32 v7, s43\n" \
    "v_mov_b32 v8, s28\n" \
    "v_mov_b32 v9, s29\n" \
    "v_mov_b32 v10, s30\n" \
    "v_mov_b32 v11, s31\n" \
    "v_mov_b32 v12, s32\n" \
    "v_mov_b32 v13, s33\n" \
    "v_mov_b32 v14, s34\n" \
    "v_mov_b32 v15, s35\n" \
    "v_mov_b32 v16, s24\n" \
    "v_mov_b32 v17, s25\n" \
    "v_mov_b32 v18, s26\n" \
    "v_mov_b32 v19, s27\n" \
    "v_mov_b32 v20, s84\n" \
    "v_mov_b32 v21, s86\n" \
    "v_mov_b32 v22, s88\n" \
    "v_mov_b32 v23, s84\n" \
    "v_mov_b32 v24, s60\n" \
    "v_mov_b32 v25, s61\n" \
    "v_mov_b32 v26, s62\n" \
    "v_mov_b32 v27, s63\n" \
    "v_mov_b32 v28, s64\n" \
    "v_mov_b32 v29, s65\n" \
    "v_mov_b32 v30, s66\n" \
    "v_mov_b32 v31, s67\n" \
    ".Lstep_end:\n" \
    ""
static_assert(2 * PART_STEP == 32768 && 2 * OFF_CAN == 1703936 && F_ERR == 2 * OFF_CAN + 65536 && MEMB_STRIDE == 32768 && OFF_VF - OFF_KF == 2048 * 4096 * 2 && OFF_BO == 65011712 && OFF_GT == 67518464 && BO_ROW == 4352, "constants of the step block");
#define STEP_CLOBBER "v32", "v33", "v34", "v35", "v36", "v37", "v38", "v39", "v40", "v41", "v42", "v43", "v44", "v45", "v46", "v47", "v48", "v49", "v50", "v51", "v52", "v53", "v54", "v55", "v56", "v57", "v58", "v59", "v60", "v61", "v62", "v63", "v64", "v65", "v66", "v67", "v68", "v69", "v70", "v71", "v72", "v73", "v74", "v75", "v76", "v77", "v78", "v79", "v80", "v81", "v82", "v83", "v84", "v85", "v86", "v87", "v88", "v89", "v90", "v91", "v92", "v93", "v94", "v95", "v98", "v99", "v100", "v101", "v102", "v103", "v104", "v105", "v106", "v107", "v108", "v109", "v110", "v111", "v112", "v113", "v114", "s24", "s25", "s26", "s27", "s28", "s29", "s30", "s31", "s32", "s33", "s34", "s35", "s36", "s37", "s38", "s39", "s40", "s41", "s42", "s43", "s44", "s45", "s46", "s47", "s48", "s49", "s50", "s51", "s52", "s53", "s54", "s55", "s56", "s57", "s58", "s59", "s60", "s61", "s62", "s63", "s64", "s65", "s66", "s67", "s68", "s69", "s70", "s71", "s72", "s73", "s74", "s75", "s76", "s77", "s78", "s79", "s80", "s81", "s82", "s83", "s84", "s85", "s86", "s87", "s88", "s89", "s90", "s91", "s92", "s93", "s94", "s95", "s96", "s97", "vcc", "scc", "memory"
#define OUT16_ASM \
    "s_mov_b32 exec_lo, %[tm]\n" \
    "s_mov_b32 exec_hi, 0\n" \
    "buffer_load_dword v79, %[tv], %[rw], %[so] offen sc1\n" \
    "s_mov_b64 exec, -1\n" \
    "s_add_i32 s44, %[so], 4096\n" \
    "s_add_i32 s45, %[so], 8192\n" \
    "s_add_i32 s46, %[so], 12288\n" \
    "buffer_load_dwordx4 v[16:19], %[vo], %[rw], %[so] offen offset:0 sc1\n" \
    "ds_read_b128 v[80:83], %[wl] offset:0\n" \
    "ds_read_b128 v[84:87], %[wl] offset:1024\n" \
    "s_nop 7\n" \
    "v_mov_b32 v104, 0\n" \
    "buffer_load_dwordx4 v[20:23], %[vo], %[rw], %[so] offen offset:256 sc1\n" \
    "s_nop 7\n" \
    "s_nop 3\n" \
    "buffer_load_dwordx4 v[24:27], %[vo], %[rw], %[so] offen offset:1024 sc1\n" \
    "s_nop 7\n" \
    "s_nop 3\n" \
    "buffer_load_dwordx4 v[28:31], %[vo], %[rw], %[so] offen offset:1280 sc1\n" \
    "s_nop 7\n" \
    "s_nop 3\n" \
    "buffer_load_dwordx4 v[32:35], %[vo], %[rw], %[so] offen offset:2048 sc1\n" \
    "s_nop 7\n" \
    "s_nop 3\n" \
    "buffer_load_dwordx4 v[36:39], %[vo], %[rw], %[so] offen offset:2304 sc1\n" \
    "s_nop 7\n" \
    "s_nop 3\n" \
    "buffer_load_dwordx4 v[40:43], %[vo], %[rw], %[so] offen offset:3072 sc1\n" \
    "s_nop 7\n" \
    "s_nop 3\n" \
    "buffer_load_dwordx4 v[44:47], %[vo], %[rw], %[so] offen offset:3328 sc1\n" \
    "s_nop 7\n" \
    "s_nop 3\n" \
    "buffer_load_dwordx4 v[48:51], %[vo], %[rw], s44 offen offset:0 sc1\n" \
    "s_nop 7\n" \
    "s_nop 3\n" \
    "buffer_load_dwordx4 v[52:55], %[vo], %[rw], s44 offen offset:256 sc1\n" \
    "s_nop 7\n" \
    "s_nop 3\n" \
    "buffer_load_dwordx4 v[56:59], %[vo], %[rw], s44 offen offset:1024 sc1\n" \
    "s_nop 7\n" \
    "s_nop 3\n" \
    "buffer_load_dwordx4 v[60:63], %[vo], %[rw], s44 offen offset:1280 sc1\n" \
    "ds_read_b128 v[88:91], %[wl] offset:2048\n" \
    "s_waitcnt vmcnt(11)\n" \
    "v_mfma_f32_16x16x32_bf16 v[0:3], a[0:3], v[16:19], 0\n" \
    "v_max3_u32 v104, v104, v16, v17\n" \
    "v_mfma_f32_16x16x32_bf16 v[4:7], a[128:131], v[16:19], 0\n" \
    "v_max3_u32 v104, v104, v18, v19\n" \
    "v_mfma_f32_16x16x32_bf16 v[8:11], v[128:131], v[16:19], 0\n" \
    "s_waitcnt lgkmcnt(2)\n" \
    "v_mfma_f32_16x16x32_bf16 v[12:15], v[80:83], v[16:19], 0\n" \
    "buffer_load_dwordx4 v[64:67], %[vo], %[rw], s44 offen offset:2048 sc1\n" \
    "ds_read_b128 v[80:83], %[wl] offset:3072\n" \
    "s_waitcnt vmcnt(11)\n" \
    "v_mfma_f32_16x16x32_bf16 v[0:3], a[4:7], v[20:23], v[0:3]\n" \
    "v_max3_u32 v104, v104, v20, v21\n" \
    "v_mfma_f32_16x16x32_bf16 v[4:7], a[132:135], v[20:23], v[4:7]\n" \
    "v_max3_u32 v104, v104, v22, v23\n" \
    "v_mfma_f32_16x16x32_bf16 v[8:11], v[132:135], v[20:23], v[8:11]\n" \
    "s_waitcnt lgkmcnt(2)\n" \
    "v_mfma_f32_16x16x32_bf16 v[12:15], v[84:87], v[20:23], v[12:15]\n" \
    "buffer_load_dwordx4 v[68:71], %[vo], %[rw], s44 offen offset:2304 sc1\n" \
    "ds_read_b128 v[84:87], %[wl] offset:4096\n" \
    "s_waitcnt vmcnt(11)\n" \
    "v_mfma_f32_16x16x32_bf16 v[0:3], a[8:11], v[24:27], v[0:3]\n" \
    "v_max3_u32 v104, v104, v24, v25\n" \
    "v_mfma_f32_16x16x32_bf16 v[4:7], a[136:139], v[24:27], v[4:7]\n" \
    "v_max3_u32 v104, v104, v26, v27\n" \
    "v_mfma_f32_16x16x32_bf16 v[8:11], v[136:139], v[24:27], v[8:11]\n" \
    "s_waitcnt lgkmcnt(2)\n" \
    "v_mfma_f32_16x16x32_bf16 v[12:15], v[88:91], v[24:27], v[12:15]\n" \
    "buffer_load_dwordx4 v[72:75], %[vo], %[rw], s44 offen offset:3072 sc1\n" \
    "ds_read_b128 v[88:91], %[wl] offset:5120\n" \
    "s_waitcnt vmcnt(11)\n" \
    "v_mfma_f32_16x16x32_bf16 v[0:3], a[12:15], v[28:31], v[0:3]\n" \
    "v_max3_u32 v104, v104, v28, v29\n" \
    "v_mfma_f32_16x16x32_bf16 v[4:7], a[140:143], v[28:31], v[4:7]\n" \
    "v_max3_u32 v104, v104, v30, v31\n" \
    "v_mfma_f32_16x16x32_bf16 v[8:11], v[140:143], v[28:31], v[8:11]\n" \
    "s_waitcnt lgkmcnt(2)\n" \
    "v_mfma_f32_16x16x32_bf16 v[12:15], v[80:83], v[28:31], v[12:15]\n" \
    "buffer_load_dwordx4 v[76:79], %[vo], %[rw], s44 offen offset:3328 sc1\n" \
    "ds_read_b128 v[80:83], %[wl] offset:6144\n" \
    "s_waitcnt vmcnt(11)\n" \
    "v_mfma_f32_16x16x32_bf16 v[0:3], a[16:19], v[32:35], v[0:3]\n" \
    "v_max3_u32 v104, v104, v32, v33\n" \
    "v_mfma_f32_16x16x32_bf16 v[4:7], a[144:147], v[32:35], v[4:7]\n" \
    "v_max3_u32 v104, v104, v34, v35\n" \
    "v_mfma_f32_16x16x32_bf16 v[8:11], v[144:147], v[32:35], v[8:11]\n" \
    "s_waitcnt lgkmcnt(2)\n" \
    "v_mfma_f32_16x16x32_bf16 v[12:15], v[84:87], v[32:35], v[12:15]\n" \
    "buffer_load_dwordx4 v[16:19], %[vo], %[rw], s45 offen offset:0 sc1\n" \
    "ds_read_b128 v[84:87], %[wl] offset:7168\n" \
    "s_waitcnt vmcnt(11)\n" \
    "v_mfma_f32_16x16x32_bf16 v[0:3], a[20:23], v[36:39], v[0:3]\n" \
    "v_max3_u32 v104, v104, v36, v37\n" \
    "v_mfma_f32_16x16x32_bf16 v[4:7], a[148:151], v[36:39], v[4:7]\n" \
    "v_max3_u32 v104, v104, v38, v39\n" \
    "v_mfma_f32_16x16x32_bf16 v[8:11], v[148:151], v[36:39], v[8:11]\n" \
    "s_waitcnt lgkmcnt(2)\n" \
    "v_mfma_f32_16x16x32_bf16 v[12:15], v[88:91], v[36:39], v[12:15]\n" \
    "buffer_load_dwordx4 v[20:23], %[vo], %[rw], s45 offen offset:256 sc1\n" \
    "ds_read_b128 v[88:91], %[wl] offset:8192\n" \
    "s_waitcnt vmcnt(11)\n" \
    "v_mfma_f32_16x16x32_bf16 v[0:3], a[24:27], v[40:43], v[0:3]\n" \
    "v_max3_u32 v104, v104, v40, v41\n" \
    "v_mfma_f32_16x16x32_bf16 v[4:7], a[152:155], v[40:43], v[4:7]\n" \
    "v_max3_u32 v104, v104, v42, v43\n" \
    "v_mfma_f32_16x16x32_bf16 v[8:11], v[152:155], v[40:43], v[8:11]\n" \
    "s_waitcnt lgkmcnt(2)\n" \
    "v_mfma_f32_16x16x32_bf16 v[12:15], v[80:83], v[40:43], v[12:15]\n" \
    "buffer_load_dwordx4 v[24:27], %[vo], %[rw], s45 offen offset:1024 sc1\n" \
    "ds_read_b128 v[80:83], %[wl] offset:9216\n" \
    "s_waitcnt vmcnt(11)\n" \
    "v_mfma_f32_16x16x32_bf16 v[0:3], a[28:31], v[44:47], v[0:3]\n" \
    "v_max3_u32 v104, v104, v44, v45\n" \
    "v_mfma_f32_16x16x32_bf16 v[4:7], a[156:159], v[44:47], v[4:7]\n" \
    "v_max3_u32 v104, v104, v46, v47\n" \
    "v_mfma_f32_16x16x32_bf16 v[8:11], v[156:159], v[44:47], v[8:11]\n" \
    "s_waitcnt lgkmcnt(2)\n" \
    "v_mfma_f32_16x16x32_bf16 v[12:15], v[84:87], v[44:47], v[12:15]\n" \
    "buffer_load_dwordx4 v[28:31], %[vo], %[rw], s45 offen offset:1280 sc1\n" \
    "ds_read_b128 v[84:87], %[wl] offset:10240\n" \
    "s_waitcnt vmcnt(11)\n" \
    "v_mfma_f32_16x16x32_bf16 v[0:3], a[32:35], v[48:51], v[0:3]\n" \
    "v_max3_u32 v104, v104, v48, v49\n" \
    "v_mfma_f32_16x16x32_bf16 v[4:7], a[160:163], v[48:51], v[4:7]\n" \
    "v_max3_u32 v104, v104, v50, v51\n" \
    "v_mfma_f32_16x16x32_bf16 v[8:11], v[160:163], v[48:51], v[8:11]\n" \
    "s_waitcnt lgkmcnt(2)\n" \
    "v_mfma_f32_16x16x32_bf16 v[12:15], v[88:91], v[48:51], v[12:15]\n" \
    "buffer_load_dwordx4 v[32:35], %[vo], %[rw], s45 offen offset:2048 sc1\n" \
    "ds_read_b128 v[88:91], %[wl] offset:11264\n" \
    "s_waitcnt vmcnt(11)\n" \
    "v_mfma_f32_16x16x32_bf16 v[0:3], a[36:39], v[52:55], v[0:3]\n" \
    "v_max3_u32 v104, v104, v52, v53\n" \
    "v_mfma_f32_16x16x32_bf16 v[4:7], a[164:167], v[52:55], v[4:7]\n" \
    "v_max3_u32 v104, v104, v54, v55\n" \
    "v_mfma_f32_16x16x32_bf16 v[8:11], v[164:167], v[52:55], v[8:11]\n" \
    "s_waitcnt lgkmcnt(2)\n" \
    "v_mfma_f32_16x16x32_bf16 v[12:15], v[80:83], v[52:55], v[12:15]\n" \
    "buffer_load_dwordx4 v[36:39], %[vo], %[rw], s45 offen offset:2304 sc1\n" \
    "ds_read_b128 v[80:83], %[wl] offset:12288\n" \
    "s_waitcnt vmcnt(11)\n" \
    "v_mfma_f32_16x16x32_bf16 v[0:3], a[40:43], v[56:59], v[0:3]\n" \
    "v_max3_u32 v104, v104, v56, v57\n" \
    "v_mfma_f32_16x16x32_bf16 v[4:7], a[168:171], v[56:59], v[4:7]\n" \
    "v_max3_u32 v104, v104, v58, v59\n" \
    "v_mfma_f32_16x16x32_bf16 v[8:11], v[168:171], v[56:59], v[8:11]\n" \
    "s_waitcnt lgkmcnt(2)\n" \
    "v_mfma_f32_16x16x32_bf16 v[12:15], v[84:87], v[56:59], v[12:15]\n" \
    "buffer_load_dwordx4 v[40:43], %[vo], %[rw], s45 offen offset:3072 sc1\n" \
    "ds_read_b128 v[84:87], %[wl] offset:13312\n" \
    "s_waitcnt vmcnt(11)\n" \
    "v_mfma_f32_16x16x32_bf16 v[0:3], a[44:47], v[60:63], v[0:3]\n" \
    "v_max3_u32 v104, v104, v60, v61\n" \
    "v_mfma_f32_16x16x32_bf16 v[4:7], a[172:175], v[60:63], v[4:7]\n" \
    "v_max3_u32 v104, v104, v62, v63\n" \
    "v_mfma_f32_16x16x32_bf16 v[8:11], v[172:175], v[60:63], v[8:11]\n" \
    "s_waitcnt lgkmcnt(2)\n" \
    "v_mfma_f32_16x16x32_bf16 v[12:15], v[88:91], v[60:63], v[12:15]\n" \
    "buffer_load_dwordx4 v[44:47], %[vo], %[rw], s45 offen offset:3328 sc1\n" \
    "ds_read_b128 v[88:91], %[wl] offset:14336\n" \
    "s_waitcnt vmcnt(11)\n" \
    "v_mfma_f32_16x16x32_bf16 v[0:3], a[48:51], v[64:67], v[0:3]\n" \
    "v_max3_u32 v104, v104, v64, v65\n" \
    "v_mfma_f32_16x16x32_bf16 v[4:7], a[176:179], v[64:67], v[4:7]\n" \
    "v_max3_u32 v104, v104, v66, v67\n" \
    "v_mfma_f32_16x16x32_bf16 v[8:11], v[176:179], v[64:67], v[8:11]\n" \
    "s_waitcnt lgkmcnt(2)\n" \
    "v_mfma_f32_16x16x32_bf16 v[12:15], v[80:83], v[64:67], v[12:15]\n" \
    "buffer_load_dwordx4 v[48:51], %[vo], %[rw], s46 offen offset:0 sc1\n" \
    "ds_read_b128 v[80:83], %[wl] offset:15360\n" \
    "s_waitcnt vmcnt(11)\n" \
    "v_mfma_f32_16x16x32_bf16 v[0:3], a[52:55], v[68:71], v[0:3]\n" \
    "v_max3_u32 v104, v104, v68, v69\n" \
    "v_mfma_f32_16x16x32_bf16 v[4:7], a[180:183], v[68:71], v[4:7]\n" \
    "v_max3_u32 v104, v104, v70, v71\n" \
    "v_mfma_f32_16x16x32_bf16 v[8:11], v[180:183], v[68:71], v[8:11]\n" \
    "s_waitcnt lgkmcnt(2)\n" \
    "v_mfma_f32_16x16x32_bf16 v[12:15], v[84:87], v[68:71], v[12:15]\n" \
    "buffer_load_dwordx4 v[52:55], %[vo], %[rw], s46 offen offset:256 sc1\n" \
    "ds_read_b128 v[84:87], %[wl] offset:16384\n" \
    "s_waitcnt vmcnt(11)\n" \
    "v_mfma_f32_16x16x32_bf16 v[0:3], a[56:59], v[72:75], v[0:3]\n" \
    "v_max3_u32 v104, v104, v72, v73\n" \
    "v_mfma_f32_16x16x32_bf16 v[4:7], a[184:187], v[72:75], v[4:7]\n" \
    "v_max3_u32 v104, v104, v74, v75\n" \
    "v_mfma_f32_16x16x32_bf16 v[8:11], v[184:187], v[72:75], v[8:11]\n" \
    "s_waitcnt lgkmcnt(2)\n" \
    "v_mfma_f32_16x16x32_bf16 v[12:15], v[88:91], v[72:75], v[12:15]\n" \
    "buffer_load_dwordx4 v[56:59], %[vo], %[rw], s46 offen offset:1024 sc1\n" \
    "ds_read_b128 v[88:91], %[wl] offset:17408\n" \
    "s_waitcnt vmcnt(11)\n" \
    "v_mfma_f32_16x16x32_bf16 v[0:3], a[60:63], v[76:79], v[0:3]\n" \
    "v_max3_u32 v104, v104, v76, v77\n" \
    "v_mfma_f32_16x16x32_bf16 v[4:7], a[188:191], v[76:79], v[4:7]\n" \
    "v_max3_u32 v104, v104, v78, v79\n" \
    "v_mfma_f32_16x16x32_bf16 v[8:11], v[188:191], v[76:79], v[8:11]\n" \
    "s_waitcnt lgkmcnt(2)\n" \
    "v_mfma_f32_16x16x32_bf16 v[12:15], v[80:83], v[76:79], v[12:15]\n" \
    "buffer_load_dwordx4 v[60:63], %[vo], %[rw], s46 offen offset:1280 sc1\n" \
    "ds_read_b128 v[80:83], %[wl] offset:18432\n" \
    "s_waitcnt vmcnt(11)\n" \
    "v_mfma_f32_16x16x32_bf16 v[0:3], a[64:67], v[16:19], v[0:3]\n" \
    "v_max3_u32 v104, v104, v16, v17\n" \
    "v_mfma_f32_16x16x32_bf16 v[4:7], a[192:195], v[16:19], v[4:7]\n" \
    "v_max3_u32 v104, v104, v18, v19\n" \
    "v_mfma_f32_16x16x32_bf16 v[8:11], v[192:195], v[16:19], v[8:11]\n" \
    "s_waitcnt lgkmcnt(2)\n" \
    "v_mfma_f32_16x16x32_bf16 v[12:15], v[84:87], v[16:19], v[12:15]\n" \
    "buffer_load_dwordx4 v[64:67], %[vo], %[rw], s46 offen offset:2048 sc1\n" \
    "ds_read_b128 v[84:87], %[wl] offset:19456\n" \
    "s_waitcnt vmcnt(11)\n" \
    "v_mfma_f32_16x16x32_bf16 v[0:3], a[68:71], v[20:23], v[0:3]\n" \
    "v_max3_u32 v104, v104, v20, v21\n" \
    "v_mfma_f32_16x16x32_bf16 v[4:7], a[196:199], v[20:23], v[4:7]\n" \
    "v_max3_u32 v104, v104, v22, v23\n" \
    "v_mfma_f32_16x16x32_bf16 v[8:11], v[196:199], v[20:23], v[8:11]\n" \
    "s_waitcnt lgkmcnt(2)\n" \
    "v_mfma_f32_16x16x32_bf16 v[12:15], v[88:91], v[20:23], v[12:15]\n" \
    "buffer_load_dwordx4 v[68:71], %[vo], %[rw], s46 offen offset:2304 sc1\n" \
    "ds_read_b128 v[88:91], %[wl] offset:20480\n" \
    "s_waitcnt vmcnt(11)\n" \
    "v_mfma_f32_16x16x32_bf16 v[0:3], a[72:75], v[24:27], v[0:3]\n" \
    "v_max3_u32 v104, v104, v24, v25\n" \
    "v_mfma_f32_16x16x32_bf16 v[4:7], a[200:203], v[24:27], v[4:7]\n" \
    "v_max3_u32 v104, v104, v26, v27\n" \
    "v_mfma_f32_16x16x32_bf16 v[8:11], v[200:203], v[24:27], v[8:11]\n" \
    "s_waitcnt lgkmcnt(2)\n" \
    "v_mfma_f32_16x16x32_bf16 v[12:15], v[80:83], v[24:27], v[12:15]\n" \
    "buffer_load_dwordx4 v[72:75], %[vo], %[rw], s46 offen offset:3072 sc1\n" \
    "ds_read_b128 v[80:83], %[wl] offset:21504\n" \
    "s_waitcnt vmcnt(11)\n" \
    "v_mfma_f32_16x16x32_bf16 v[0:3], a[76:79], v[28:31], v[0:3]\n" \
    "v_max3_u32 v104, v104, v28, v29\n" \
    "v_mfma_f32_16x16x32_bf16 v[4:7], a[204:207], v[28:31], v[4:7]\n" \
    "v_max3_u32 v104, v104, v30, v31\n" \
    "v_mfma_f32_16x16x32_bf16 v[8:11], v[204:207], v[28:31], v[8:11]\n" \
    "s_waitcnt lgkmcnt(2)\n" \
    "v_mfma_f32_16x16x32_bf16 v[12:15], v[84:87], v[28:31], v[12:15]\n" \
    "buffer_load_dwordx4 v[76:79], %[vo], %[rw], s46 offen offset:3328 sc1\n" \
    "ds_read_b128 v[84:87], %[wl] offset:22528\n" \
    "s_waitcnt vmcnt(11)\n" \
    "v_mfma_f32_16x16x32_bf16 v[0:3], a[80:83], v[32:35], v[0:3]\n" \
    "v_max3_u32 v104, v104, v32, v33\n" \
    "v_mfma_f32_16x16x32_bf16 v[4:7], a[208:211], v[32:35], v[4:7]\n" \
    "v_max3_u32 v104, v104, v34, v35\n" \
    "v_mfma_f32_16x16x32_bf16 v[8:11], v[208:211], v[32:35], v[8:11]\n" \
    "s_waitcnt lgkmcnt(2)\n" \
    "v_mfma_f32_16x16x32_bf16 v[12:15], v[88:91], v[32:35], v[12:15]\n" \
    "buffer_load_dwordx2 v[106:107], %[bv], %[rw], %[bs] offen sc1\n" \
    "ds_read_b128 v[88:91], %[wl] offset:23552\n" \
    "s_waitcnt vmcnt(11)\n" \
    "v_mfma_f32_16x16x32_bf16 v[0:3], a[84:87], v[36:39], v[0:3]\n" \
    "v_max3_u32 v104, v104, v36, v37\n" \
    "v_mfma_f32_16x16x32_bf16 v[4:7], a[212:215], v[36:39], v[4:7]\n" \
    "v_max3_u32 v104, v104, v38, v39\n" \
    "v_mfma_f32_16x16x32_bf16 v[8:11], v[212:215], v[36:39], v[8:11]\n" \
    "s_waitcnt lgkmcnt(2)\n" \
    "v_mfma_f32_16x16x32_bf16 v[12:15], v[80:83], v[36:39], v[12:15]\n" \
    "buffer_load_dword v108, %[gv], %[rw], %[gs] offen sc1\n" \
    "ds_read_b128 v[80:83], %[wl] offset:24576\n" \
    "s_waitcnt vmcnt(11)\n" \
    "v_mfma_f32_16x16x32_bf16 v[0:3], a[88:91], v[40:43], v[0:3]\n" \
    "v_max3_u32 v104, v104, v40, v41\n" \
    "v_mfma_f32_16x16x32_bf16 v[4:7], a[216:219], v[40:43], v[4:7]\n" \
    "v_max3_u32 v104, v104, v42, v43\n" \
    "v_mfma_f32_16x16x32_bf16 v[8:11], v[216:219], v[40:43], v[8:11]\n" \
    "s_waitcnt lgkmcnt(2)\n" \
    "v_mfma_f32_16x16x32_bf16 v[12:15], v[84:87], v[40:43], v[12:15]\n" \
    "buffer_load_dword v105, %[sv], %[rw], %[ss] offen sc1\n" \
    "ds_read_b128 v[84:87], %[wl] offset:25600\n" \
    "s_waitcnt vmcnt(11)\n" \
    "v_mfma_f32_16x16x32_bf16 v[0:3], a[92:95], v[44:47], v[0:3]\n" \
    "v_max3_u32 v104, v104, v44, v45\n" \
    "v_mfma_f32_16x16x32_bf16 v[4:7], a[220:223], v[44:47], v[4:7]\n" \
    "v_max3_u32 v104, v104, v46, v47\n" \
    "v_mfma_f32_16x16x32_bf16 v[8:11], v[220:223], v[44:47], v[8:11]\n" \
    "s_waitcnt lgkmcnt(2)\n" \
    "v_mfma_f32_16x16x32_bf16 v[12:15], v[88:91], v[44:47], v[12:15]\n" \
    "ds_read_b128 v[88:91], %[wl] offset:26624\n" \
    "s_waitcnt vmcnt(10)\n" \
    "v_mfma_f32_16x16x32_bf16 v[0:3], a[96:99], v[48:51], v[0:3]\n" \
    "v_max3_u32 v104, v104, v48, v49\n" \
    "v_mfma_f32_16x16x32_bf16 v[4:7], a[224:227], v[48:51], v[4:7]\n" \
    "v_max3_u32 v104, v104, v50, v51\n" \
    "v_mfma_f32_16x16x32_bf16 v[8:11], v[224:227], v[48:51], v[8:11]\n" \
    "s_waitcnt lgkmcnt(2)\n" \
    "v_mfma_f32_16x16x32_bf16 v[12:15], v[80:83], v[48:51], v[12:15]\n" \
    "ds_read_b128 v[80:83], %[wl] offset:27648\n" \
    "s_waitcnt vmcnt(9)\n" \
    "v_mfma_f32_16x16x32_bf16 v[0:3], a[100:103], v[52:55], v[0:3]\n" \
    "v_max3_u32 v104, v104, v52, v53\n" \
    "v_mfma_f32_16x16x32_bf16 v[4:7], a[228:231], v[52:55], v[4:7]\n" \
    "v_max3_u32 v104, v104, v54, v55\n" \
    "v_mfma_f32_16x16x32_bf16 v[8:11], v[228:231], v[52:55], v[8:11]\n" \
    "s_waitcnt lgkmcnt(2)\n" \
    "v_mfma_f32_16x16x32_bf16 v[12:15], v[84:87], v[52:55], v[12:15]\n" \
    "ds_read_b128 v[84:87], %[wl] offset:28672\n" \
    "s_waitcnt vmcnt(8)\n" \
    "v_mfma_f32_16x16x32_bf16 v[0:3], a[104:107], v[56:59], v[0:3]\n" \
    "v_max3_u32 v104, v104, v56, v57\n" \
    "v_mfma_f32_16x16x32_bf16 v[4:7], a[232:235], v[56:59], v[4:7]\n" \
    "v_max3_u32 v104, v104, v58, v59\n" \
    "v_mfma_f32_16x16x32_bf16 v[8:11], v[232:235], v[56:59], v[8:11]\n" \
    "s_waitcnt lgkmcnt(2)\n" \
    "v_mfma_f32_16x16x32_bf16 v[12:15], v[88:91], v[56:59], v[12:15]\n" \
    "ds_read_b128 v[88:91], %[wl] offset:29696\n" \
    "s_waitcnt vmcnt(7)\n" \
    "v_mfma_f32_16x16x32_bf16 v[0:3], a[108:111], v[60:63], v[0:3]\n" \
    "v_max3_u32 v104, v104, v60, v61\n" \
    "v_mfma_f32_16x16x32_bf16 v[4:7], a[236:239], v[60:63], v[4:7]\n" \
    "v_max3_u32 v104, v104, v62, v63\n" \
    "v_mfma_f32_16x16x32_bf16 v[8:11], v[236:239], v[60:63], v[8:11]\n" \
    "s_waitcnt lgkmcnt(2)\n" \
    "v_mfma_f32_16x16x32_bf16 v[12:15], v[80:83], v[60:63], v[12:15]\n" \
    "ds_read_b128 v[80:83], %[wl] offset:30720\n" \
    "s_waitcnt vmcnt(6)\n" \
    "v_mfma_f32_16x16x32_bf16 v[0:3], a[112:115], v[64:67], v[0:3]\n" \
    "v_max3_u32 v104, v104, v64, v65\n" \
    "v_mfma_f32_16x16x32_bf16 v[4:7], a[240:243], v[64:67], v[4:7]\n" \
    "v_max3_u32 v104, v104, v66, v67\n" \
    "v_mfma_f32_16x16x32_bf16 v[8:11], v[240:243], v[64:67], v[8:11]\n" \
    "s_waitcnt lgkmcnt(2)\n" \
    "v_mfma_f32_16x16x32_bf16 v[12:15], v[84:87], v[64:67], v[12:15]\n" \
    "ds_read_b128 v[84:87], %[wl] offset:31744\n" \
    "s_waitcnt vmcnt(5)\n" \
    "v_mfma_f32_16x16x32_bf16 v[0:3], a[116:119], v[68:71], v[0:3]\n" \
    "v_max3_u32 v104, v104, v68, v69\n" \
    "v_mfma_f32_16x16x32_bf16 v[4:7], a[244:247], v[68:71], v[4:7]\n" \
    "v_max3_u32 v104, v104, v70, v71\n" \
    "v_mfma_f32_16x16x32_bf16 v[8:11], v[244:247], v[68:71], v[8:11]\n" \
    "s_waitcnt lgkmcnt(2)\n" \
    "v_mfma_f32_16x16x32_bf16 v[12:15], v[88:91], v[68:71], v[12:15]\n" \
    "s_waitcnt vmcnt(4)\n" \
    "v_mfma_f32_16x16x32_bf16 v[0:3], a[120:123], v[72:75], v[0:3]\n" \
    "v_max3_u32 v104, v104, v72, v73\n" \
    "v_mfma_f32_16x16x32_bf16 v[4:7], a[248:251], v[72:75], v[4:7]\n" \
    "v_max3_u32 v104, v104, v74, v75\n" \
    "v_mfma_f32_16x16x32_bf16 v[8:11], v[248:251], v[72:75], v[8:11]\n" \
    "s_waitcnt lgkmcnt(1)\n" \
    "v_mfma_f32_16x16x32_bf16 v[12:15], v[80:83], v[72:75], v[12:15]\n" \
    "s_waitcnt vmcnt(3)\n" \
    "v_mfma_f32_16x16x32_bf16 v[0:3], a[124:127], v[76:79], v[0:3]\n" \
    "v_max3_u32 v104, v104, v76, v77\n" \
    "v_mfma_f32_16x16x32_bf16 v[4:7], a[252:255], v[76:79], v[4:7]\n" \
    "v_max3_u32 v104, v104, v78, v79\n" \
    "v_mfma_f32_16x16x32_bf16 v[8:11], v[252:255], v[76:79], v[8:11]\n" \
    "s_waitcnt lgkmcnt(0)\n" \
    "v_mfma_f32_16x16x32_bf16 v[12:15], v[84:87], v[76:79], v[12:15]\n" \
    "s_waitcnt vmcnt(0)\n" \
    "s_nop 7\n" \
    ""
#define OUT8_ASM \
    "s_mov_b32 exec_lo, %[tm]\n" \
    "s_mov_b32 exec_hi, 0\n" \
    "buffer_load_dword v79, %[tv], %[rw], %[so] offen sc1\n" \
    "s_mov_b64 exec, -1\n" \
    "s_add_i32 s44, %[so], 4096\n" \
    "s_add_i32 s45, %[so], 8192\n" \
    "s_add_i32 s46, %[so], 12288\n" \
    "buffer_load_dwordx4 v[16:19], %[vo], %[rw], %[so] offen offset:0 sc1\n" \
    "ds_read_b128 v[80:83], %[wl] offset:0\n" \
    "ds_read_b128 v[84:87], %[wl] offset:1024\n" \
    "ds_read_b128 v[88:91], %[wl] offset:2048\n" \
    "ds_read_b128 v[92:95], %[wl] offset:3072\n" \
    "v_mov_b64 v[0:1], 0\n" \
    "v_mov_b64 v[2:3], 0\n" \
    "v_mov_b64 v[4:5], 0\n" \
    "v_mov_b64 v[6:7], 0\n" \
    "v_mov_b64 v[8:9], 0\n" \
    "v_mov_b64 v[10:11], 0\n" \
    "v_mov_b64 v[12:13], 0\n" \
    "v_mov_b64 v[14:15], 0\n" \
    "v_mov_b32 v104, 0\n" \
    "buffer_load_dwordx4 v[20:23], %[vo], %[rw], %[so] offen offset:1024 sc1\n" \
    "s_nop 7\n" \
    "s_nop 3\n" \
    "buffer_load_dwordx4 v[24:27], %[vo], %[rw], %[so] offen offset:2048 sc1\n" \
    "s_nop 7\n" \
    "s_nop 3\n" \
    "buffer_load_dwordx4 v[28:31], %[vo], %[rw], %[so] offen offset:3072 sc1\n" \
    "s_nop 7\n" \
    "s_nop 3\n" \
    "buffer_load_dwordx4 v[32:35], %[vo], %[rw], s44 offen offset:0 sc1\n" \
    "s_nop 7\n" \
    "s_nop 3\n" \
    "buffer_load_dwordx4 v[36:39], %[vo], %[rw], s44 offen offset:1024 sc1\n" \
    "s_nop 7\n" \
    "s_nop 3\n" \
    "buffer_load_dwordx4 v[40:43], %[vo], %[rw], s44 offen offset:2048 sc1\n" \
    "s_nop 7\n" \
    "s_nop 3\n" \
    "buffer_load_dwordx4 v[44:47], %[vo], %[rw], s44 offen offset:3072 sc1\n" \
    "s_nop 7\n" \
    "s_nop 3\n" \
    "buffer_load_dwordx4 v[48:51], %[vo], %[rw], s45 offen offset:0 sc1\n" \
    "s_nop 7\n" \
    "s_nop 3\n" \
    "buffer_load_dwordx4 v[52:55], %[vo], %[rw], s45 offen offset:1024 sc1\n" \
    "ds_read_b128 v[96:99], %[wl] offset:4096\n" \
    "ds_read_b128 v[100:103], %[wl] offset:5120\n" \
    "s_waitcnt vmcnt(9)\n" \
    "v_smfmac_f32_16x16x64_bf16 v[0:3], v[16:19], a[0:7], %[idx]\n" \
    "v_max3_u32 v104, v104, v16, v17\n" \
    "v_smfmac_f32_16x16x64_bf16 v[4:7], v[16:19], a[128:135], %[idx]\n" \
    "v_max3_u32 v104, v104, v18, v19\n" \
    "v_smfmac_f32_16x16x64_bf16 v[8:11], v[16:19], v[128:135], %[idx]\n" \
    "s_waitcnt lgkmcnt(4)\n" \
    "v_smfmac_f32_16x16x64_bf16 v[12:15], v[16:19], v[80:87], %[idx]\n" \
    "buffer_load_dwordx4 v[56:59], %[vo], %[rw], s45 offen offset:2048 sc1\n" \
    "ds_read_b128 v[80:83], %[wl] offset:6144\n" \
    "ds_read_b128 v[84:87], %[wl] offset:7168\n" \
    "s_waitcnt vmcnt(9)\n" \
    "v_smfmac_f32_16x16x64_bf16 v[0:3], v[20:23], a[8:15], %[idx]\n" \
    "v_max3_u32 v104, v104, v20, v21\n" \
    "v_smfmac_f32_16x16x64_bf16 v[4:7], v[20:23], a[136:143], %[idx]\n" \
    "v_max3_u32 v104, v104, v22, v23\n" \
    "v_smfmac_f32_16x16x64_bf16 v[8:11], v[20:23], v[136:143], %[idx]\n" \
    "s_waitcnt lgkmcnt(4)\n" \
    "v_smfmac_f32_16x16x64_bf16 v[12:15], v[20:23], v[88:95], %[idx]\n" \
    "buffer_load_dwordx4 v[60:63], %[vo], %[rw], s45 offen offset:3072 sc1\n" \
    "ds_read_b128 v[88:91], %[wl] offset:8192\n" \
    "ds_read_b128 v[92:95], %[wl] offset:9216\n" \
    "s_waitcnt vmcnt(9)\n" \
    "v_smfmac_f32_16x16x64_bf16 v[0:3], v[24:27], a[16:23], %[idx]\n" \
    "v_max3_u32 v104, v104, v24, v25\n" \
    "v_smfmac_f32_16x16x64_bf16 v[4:7], v[24:27], a[144:151], %[idx]\n" \
    "v_max3_u32 v104, v104, v26, v27\n" \
    "v_smfmac_f32_16x16x64_bf16 v[8:11], v[24:27], v[144:151], %[idx]\n" \
    "s_waitcnt lgkmcnt(4)\n" \
    "v_smfmac_f32_16x16x64_bf16 v[12:15], v[24:27], v[96:103], %[idx]\n" \
    "buffer_load_dwordx4 v[64:67], %[vo], %[rw], s46 offen offset:0 sc1\n" \
    "ds_read_b128 v[96:99], %[wl] offset:10240\n" \
    "ds_read_b128 v[100:103], %[wl] offset:11264\n" \
    "s_waitcnt vmcnt(9)\n" \
    "v_smfmac_f32_16x16x64_bf16 v[0:3], v[28:31], a[24:31], %[idx]\n" \
    "v_max3_u32 v104, v104, v28, v29\n" \
    "v_smfmac_f32_16x16x64_bf16 v[4:7], v[28:31], a[152:159], %[idx]\n" \
    "v_max3_u32 v104, v104, v30, v31\n" \
    "v_smfmac_f32_16x16x64_bf16 v[8:11], v[28:31], v[152:159], %[idx]\n" \
    "s_waitcnt lgkmcnt(4)\n" \
    "v_smfmac_f32_16x16x64_bf16 v[12:15], v[28:31], v[80:87], %[idx]\n" \
    "buffer_load_dwordx4 v[68:71], %[vo], %[rw], s46 offen offset:1024 sc1\n" \
    "ds_read_b128 v[80:83], %[wl] offset:12288\n" \
    "ds_read_b128 v[84:87], %[wl] offset:13312\n" \
    "s_waitcnt vmcnt(9)\n" \
    "v_smfmac_f32_16x16x64_bf16 v[0:3], v[32:35], a[32:39], %[idx]\n" \
    "v_max3_u32 v104, v104, v32, v33\n" \
    "v_smfmac_f32_16x16x64_bf16 v[4:7], v[32:35], a[160:167], %[idx]\n" \
    "v_max3_u32 v104, v104, v34, v35\n" \
    "v_smfmac_f32_16x16x64_bf16 v[8:11], v[32:35], v[160:167], %[idx]\n" \
    "s_waitcnt lgkmcnt(4)\n" \
    "v_smfmac_f32_16x16x64_bf16 v[12:15], v[32:35], v[88:95], %[idx]\n" \
    "buffer_load_dwordx4 v[72:75], %[vo], %[rw], s46 offen offset:2048 sc1\n" \
    "ds_read_b128 v[88:91], %[wl] offset:14336\n" \
    "ds_read_b128 v[92:95], %[wl] offset:15360\n" \
    "s_waitcnt vmcnt(9)\n" \
    "v_smfmac_f32_16x16x64_bf16 v[0:3], v[36:39], a[40:47], %[idx]\n" \
    "v_max3_u32 v104, v104, v36, v37\n" \
    "v_smfmac_f32_16x16x64_bf16 v[4:7], v[36:39], a[168:175], %[idx]\n" \
    "v_max3_u32 v104, v104, v38, v39\n" \
    "v_smfmac_f32_16x16x64_bf16 v[8:11], v[36:39], v[168:175], %[idx]\n" \
    "s_waitcnt lgkmcnt(4)\n" \
    "v_smfmac_f32_16x16x64_bf16 v[12:15], v[36:39], v[96:103], %[idx]\n" \
    "buffer_load_dwordx4 v[76:79], %[vo], %[rw], s46 offen offset:3072 sc1\n" \
    "ds_read_b128 v[96:99], %[wl] offset:16384\n" \
    "ds_read_b128 v[100:103], %[wl] offset:17408\n" \
    "s_waitcnt vmcnt(9)\n" \
    "v_smfmac_f32_16x16x64_bf16 v[0:3], v[40:43], a[48:55], %[idx]\n" \
    "v_max3_u32 v104, v104, v40, v41\n" \
    "v_smfmac_f32_16x16x64_bf16 v[4:7], v[40:43], a[176:183], %[idx]\n" \
    "v_max3_u32 v104, v104, v42, v43\n" \
    "v_smfmac_f32_16x16x64_bf16 v[8:11], v[40:43], v[176:183], %[idx]\n" \
    "s_waitcnt lgkmcnt(4)\n" \
    "v_smfmac_f32_16x16x64_bf16 v[12:15], v[40:43], v[80:87], %[idx]\n" \
    "buffer_load_dwordx2 v[106:107], %[bv], %[rw], %[bs] offen sc1\n" \
    "ds_read_b128 v[80:83], %[wl] offset:18432\n" \
    "ds_read_b128 v[84:87], %[wl] offset:19456\n" \
    "s_waitcnt vmcnt(9)\n" \
    "v_smfmac_f32_16x16x64_bf16 v[0:3], v[44:47], a[56:63], %[idx]\n" \
    "v_max3_u32 v104, v104, v44, v45\n" \
    "v_smfmac_f32_16x16x64_bf16 v[4:7], v[44:47], a[184:191], %[idx]\n" \
    "v_max3_u32 v104, v104, v46, v47\n" \
    "v_smfmac_f32_16x16x64_bf16 v[8:11], v[44:47], v[184:191], %[idx]\n" \
    "s_waitcnt lgkmcnt(4)\n" \
    "v_smfmac_f32_16x16x64_bf16 v[12:15], v[44:47], v[88:95], %[idx]\n" \
    "buffer_load_dword v108, %[gv], %[rw], %[gs] offen sc1\n" \
    "ds_read_b128 v[88:91], %[wl] offset:20480\n" \
    "ds_read_b128 v[92:95], %[wl] offset:21504\n" \
    "s_waitcnt vmcnt(9)\n" \
    "v_smfmac_f32_16x16x64_bf16 v[0:3], v[48:51], a[64:71], %[idx]\n" \
    "v_max3_u32 v104, v104, v48, v49\n" \
    "v_smfmac_f32_16x16x64_bf16 v[4:7], v[48:51], a[192:199], %[idx]\n" \
    "v_max3_u32 v104, v104, v50, v51\n" \
    "v_smfmac_f32_16x16x64_bf16 v[8:11], v[48:51], v[192:199], %[idx]\n" \
    "s_waitcnt lgkmcnt(4)\n" \
    "v_smfmac_f32_16x16x64_bf16 v[12:15], v[48:51], v[96:103], %[idx]\n" \
    "buffer_load_dword v105, %[sv], %[rw], %[ss] offen sc1\n" \
    "ds_read_b128 v[96:99], %[wl] offset:22528\n" \
    "ds_read_b128 v[100:103], %[wl] offset:23552\n" \
    "s_waitcnt vmcnt(9)\n" \
    "v_smfmac_f32_16x16x64_bf16 v[0:3], v[52:55], a[72:79], %[idx]\n" \
    "v_max3_u32 v104, v104, v52, v53\n" \
    "v_smfmac_f32_16x16x64_bf16 v[4:7], v[52:55], a[200:207], %[idx]\n" \
    "v_max3_u32 v104, v104, v54, v55\n" \
    "v_smfmac_f32_16x16x64_bf16 v[8:11], v[52:55], v[200:207], %[idx]\n" \
    "s_waitcnt lgkmcnt(4)\n" \
    "v_smfmac_f32_16x16x64_bf16 v[12:15], v[52:55], v[80:87], %[idx]\n" \
    "ds_read_b128 v[80:83], %[wl] offset:24576\n" \
    "ds_read_b128 v[84:87], %[wl] offset:25600\n" \
    "s_waitcnt vmcnt(8)\n" \
    "v_smfmac_f32_16x16x64_bf16 v[0:3], v[56:59], a[80:87], %[idx]\n" \
    "v_max3_u32 v104, v104, v56, v57\n" \
    "v_smfmac_f32_16x16x64_bf16 v[4:7], v[56:59], a[208:215], %[idx]\n" \
    "v_max3_u32 v104, v104, v58, v59\n" \
    "v_smfmac_f32_16x16x64_bf16 v[8:11], v[56:59], v[208:215], %[idx]\n" \
    "s_waitcnt lgkmcnt(4)\n" \
    "v_smfmac_f32_16x16x64_bf16 v[12:15], v[56:59], v[88:95], %[idx]\n" \
    "ds_read_b128 v[88:91], %[wl] offset:26624\n" \
    "ds_read_b128 v[92:95], %[wl] offset:27648\n" \
    "s_waitcnt vmcnt(7)\n" \
    "v_smfmac_f32_16x16x64_bf16 v[0:3], v[60:63], a[88:95], %[idx]\n" \
    "v_max3_u32 v104, v104, v60, v61\n" \
    "v_smfmac_f32_16x16x64_bf16 v[4:7], v[60:63], a[216:223], %[idx]\n" \
    "v_max3_u32 v104, v104, v62, v63\n" \
    "v_smfmac_f32_16x16x64_bf16 v[8:11], v[60:63], v[216:223], %[idx]\n" \
    "s_waitcnt lgkmcnt(4)\n" \
    "v_smfmac_f32_16x16x64_bf16 v[12:15], v[60:63], v[96:103], %[idx]\n" \
    "ds_read_b128 v[96:99], %[wl] offset:28672\n" \
    "ds_read_b128 v[100:103], %[wl] offset:29696\n" \
    "s_waitcnt vmcnt(6)\n" \
    "v_smfmac_f32_16x16x64_bf16 v[0:3], v[64:67], a[96:103], %[idx]\n" \
    "v_max3_u32 v104, v104, v64, v65\n" \
    "v_smfmac_f32_16x16x64_bf16 v[4:7], v[64:67], a[224:231], %[idx]\n" \
    "v_max3_u32 v104, v104, v66, v67\n" \
    "v_smfmac_f32_16x16x64_bf16 v[8:11], v[64:67], v[224:231], %[idx]\n" \
    "s_waitcnt lgkmcnt(4)\n" \
    "v_smfmac_f32_16x16x64_bf16 v[12:15], v[64:67], v[80:87], %[idx]\n" \
    "ds_read_b128 v[80:83], %[wl] offset:30720\n" \
    "ds_read_b128 v[84:87], %[wl] offset:31744\n" \
    "s_waitcnt vmcnt(5)\n" \
    "v_smfmac_f32_16x16x64_bf16 v[0:3], v[68:71], a[104:111], %[idx]\n" \
    "v_max3_u32 v104, v104, v68, v69\n" \
    "v_smfmac_f32_16x16x64_bf16 v[4:7], v[68:71], a[232:239], %[idx]\n" \
    "v_max3_u32 v104, v104, v70, v71\n" \
    "v_smfmac_f32_16x16x64_bf16 v[8:11], v[68:71], v[232:239], %[idx]\n" \
    "s_waitcnt lgkmcnt(4)\n" \
    "v_smfmac_f32_16x16x64_bf16 v[12:15], v[68:71], v[88:95], %[idx]\n" \
    "s_waitcnt vmcnt(4)\n" \
    "v_smfmac_f32_16x16x64_bf16 v[0:3], v[72:75], a[112:119], %[idx]\n" \
    "v_max3_u32 v104, v104, v72, v73\n" \
    "v_smfmac_f32_16x16x64_bf16 v[4:7], v[72:75], a[240:247], %[idx]\n" \
    "v_max3_u32 v104, v104, v74, v75\n" \
    "v_smfmac_f32_16x16x64_bf16 v[8:11], v[72:75], v[240:247], %[idx]\n" \
    "s_waitcnt lgkmcnt(2)\n" \
    "v_smfmac_f32_16x16x64_bf16 v[12:15], v[72:75], v[96:103], %[idx]\n" \
    "s_waitcnt vmcnt(3)\n" \
    "v_smfmac_f32_16x16x64_bf16 v[0:3], v[76:79], a[120:127], %[idx]\n" \
    "v_max3_u32 v104, v104, v76, v77\n" \
    "v_smfmac_f32_16x16x64_bf16 v[4:7], v[76:79], a[248:255], %[idx]\n" \
    "v_max3_u32 v104, v104, v78, v79\n" \
    "v_smfmac_f32_16x16x64_bf16 v[8:11], v[76:79], v[248:255], %[idx]\n" \
    "s_waitcnt lgkmcnt(0)\n" \
    "v_smfmac_f32_16x16x64_bf16 v[12:15], v[76:79], v[80:87], %[idx]\n" \
    "s_waitcnt vmcnt(0)\n" \
    "s_nop 5\n" \
    "v_permlane32_swap_b32 v0, v8\n" \
    "v_add_f32 v0, v0, v8\n" \
    "v_permlane32_swap_b32 v1, v9\n" \
    "v_add_f32 v1, v1, v9\n" \
    "v_permlane32_swap_b32 v2, v10\n" \
    "v_add_f32 v2, v2, v10\n" \
    "v_permlane32_swap_b32 v3, v11\n" \
    "v_add_f32 v3, v3, v11\n" \
    "v_permlane32_swap_b32 v4, v12\n" \
    "v_add_f32 v4, v4, v12\n" \
    "v_permlane32_swap_b32 v5, v13\n" \
    "v_add_f32 v5, v5, v13\n" \
    "v_permlane32_swap_b32 v6, v14\n" \
    "v_add_f32 v6, v6, v14\n" \
    "v_permlane32_swap_b32 v7, v15\n" \
    "v_add_f32 v7, v7, v15\n" \
    ""
#define OUT_CLOBBER "v16", "v17", "v18", "v19", "v20", "v21", "v22", "v23", "v24", "v25", "v26", "v27", "v28", "v29", "v30", "v31", "v32", "v33", "v34", "v35", "v36", "v37", "v38", "v39", "v40", "v41", "v42", "v43", "v44", "v45", "v46", "v47", "v48", "v49", "v50", "v51", "v52", "v53", "v54", "v55", "v56", "v57", "v58", "v59", "v60", "v61", "v62", "v63", "v64", "v65", "v66", "v67", "v68", "v69", "v70", "v71", "v72", "v73", "v74", "v75", "v76", "v77", "v78", "v79", "v80", "v81", "v82", "v83", "v84", "v85", "v86", "v87", "v88", "v89", "v90", "v91", "v92", "v93", "v94", "v95", "v96", "v97", "v98", "v99", "v100", "v101", "v102", "v103", "s44", "s45", "s46", "scc", "memory"
constexpr int NREP = 2, NCP = 4;
#define OWNER_ASM \
    "s_getpc_b64 s[96:97]\n" \
    "14:\n" \
    "s_add_u32 s96, s96, 2f-14b\n" \
    "s_addc_u32 s97, s97, 0\n" \
    "s_sub_u32 s68, s96, 2f-11f\n" \
    "s_subb_u32 s69, s97, 0\n" \
    "s_mov_b32 %[np], 0\n" \
    "s_add_i32 s72, %[pb], 0x10000\n" \
    "13:\n" \
    "s_memtime vcc\n" \
    "s_waitcnt lgkmcnt(0)\n" \
    "s_sub_i32 s70, %[lk], vcc_lo\n" \
    "s_cmp_gt_i32 s70, %[rd]\n" \
    "s_cbranch_scc1 13b\n" \
    "s_add_i32 s70, %[pq], 0x10000\n" \
    "buffer_store_dwordx4 %[sv], %[ro], %[rs], %[pq] offen offset:0 sc1\n" \
    "buffer_store_dwordx4 %[sv], %[ro], %[rs], %[pq] offen offset:1024 sc1\n" \
    "buffer_store_dwordx4 %[sv], %[ro], %[rs], s70 offen offset:0 sc1\n" \
    "buffer_store_dwordx4 %[sv], %[ro], %[rs], s70 offen offset:1024 sc1\n" \
    "v_mov_b32 v2, -1\n" \
    "buffer_store_dword v2, %[rm], %[rs], %[mq] offen sc1\n" \
    "buffer_store_dwordx4 %[sv], %[rt], %[rw], %[tq] offen sc1\n" \
    "s_mov_b32 s71, 4\n" \
    "10:\n" \
    "s_memtime vcc\n" \
    "s_waitcnt lgkmcnt(0)\n" \
    "s_sub_i32 s70, %[lk], vcc_lo\n" \
    "s_cmp_gt_i32 s70, 112\n" \
    "s_cbranch_scc1 10b\n" \
    "s_max_i32 s70, s70, 0\n" \
    "s_memtime s[58:59]\n" \
    "s_memrealtime s[44:45]\n" \
    "s_nop 1\n" \
    "s_memrealtime s[46:47]\n" \
    "s_nop 1\n" \
    "s_memrealtime s[48:49]\n" \
    "s_nop 1\n" \
    "s_memrealtime s[50:51]\n" \
    "s_nop 1\n" \
    "s_memrealtime s[52:53]\n" \
    "s_nop 1\n" \
    "s_memrealtime s[54:55]\n" \
    "s_nop 1\n" \
    "s_memrealtime s[56:57]\n" \
    "s_nop 1\n" \
    "s_memrealtime vcc\n" \
    "s_waitcnt lgkmcnt(0)\n" \
    "s_add_i32 s44, s44, s46\n" \
    "s_add_i32 s48, s48, s50\n" \
    "s_add_i32 s52, s52, s54\n" \
    "s_add_i32 s44, s44, s48\n" \
    "s_add_i32 s52, s52, s56\n" \
    "s_add_i32 s44, s44, s52\n" \
    "s_add_i32 s59, s44, vcc_lo\n" \
    "s_sub_i32 s44, s59, %[oT]\n" \
    "s_mov_b32 %[oT], s59\n" \
    "s_sub_i32 s45, s58, %[oC]\n" \
    "s_mov_b32 %[oC], s58\n" \
    "s_mul_i32 s46, s44, %[oR]\n" \
    "s_lshr_b32 s46, s46, 12\n" \
    "s_sub_i32 s46, s45, s46\n" \
    "s_sub_i32 s44, s44, 1000\n" \
    "s_cmp_lt_u32 s44, 2000\n" \
    "s_cselect_b32 s46, s46, 0\n" \
    "s_add_i32 %[oR], %[oR], s46\n" \
    "s_sub_i32 s45, %[oT], %[c8]\n" \
    "s_and_b32 s70, s70, -4\n" \
    "s_sub_u32 vcc_lo, s68, s70\n" \
    "s_subb_u32 vcc_hi, s69, 0\n" \
    "s_setpc_b64 vcc\n" \
    "s_nop 0\n" \
    "s_nop 0\n" \
    "s_nop 0\n" \
    "s_nop 0\n" \
    "s_nop 0\n" \
    "s_nop 0\n" \
    "s_nop 0\n" \
    "s_nop 0\n" \
    "s_nop 0\n" \
    "s_nop 0\n" \
    "s_nop 0\n" \
    "s_nop 0\n" \
    "s_nop 0\n" \
    "s_nop 0\n" \
    "s_nop 0\n" \
    "s_nop 0\n" \
    "s_nop 0\n" \
    "s_nop 0\n" \
    "s_nop 0\n" \
    "s_nop 0\n" \
    "s_nop 0\n" \
    "s_nop 0\n" \
    "s_nop 0\n" \
    "s_nop 0\n" \
    "s_nop 0\n" \
    "s_nop 0\n" \
    "s_nop 0\n" \
    "s_nop 0\n" \
    "11:\n" \
    "buffer_load_dwordx2 v[0:1], %[ca], %[rs], %[cs] offen sc1\n" \
    "s_waitcnt vmcnt(0)\n" \
    "6:\n" \
    "s_memtime s[46:47]\n" \
    "v_subrev_u32 v0, s45, v0\n" \
    "v_cmp_ne_u16 vcc, %[tag], v1\n" \
    "s_add_i32 s48, %[oC], -136\n" \
    "v_max_i32_dpp v0, v0, v0 row_shr:1 row_mask:0xf bank_mask:0xf\n" \
    "v_max_u32_dpp v1, v1, v1 row_shr:1 row_mask:0xf bank_mask:0xf\n" \
    "s_cbranch_vccnz 7f\n" \
    "8:\n" \
    "v_max_i32_dpp v0, v0, v0 row_shr:2 row_mask:0xf bank_mask:0xf\n" \
    "v_max_u32_dpp v1, v1, v1 row_shr:2 row_mask:0xf bank_mask:0xf\n" \
    "s_nop 0\n" \
    "v_max_i32_dpp v0, v0, v0 row_shr:4 row_mask:0xf bank_mask:0xf\n" \
    "v_max_u32_dpp v1, v1, v1 row_shr:4 row_mask:0xf bank_mask:0xf\n" \
    "s_nop 0\n" \
    "v_max_i32_dpp v0, v0, v0 row_shr:8 row_mask:0xf bank_mask:0xf\n" \
    "v_max_u32_dpp v1, v1, v1 row_shr:8 row_mask:0xf bank_mask:0xf\n" \
    "s_waitcnt lgkmcnt(0)\n" \
    "v_max_i32_dpp v0, v0, v0 row_bcast:15 row_mask:0xa bank_mask:0xf\n" \
    "v_max_u32_dpp v1, v1, v1 row_bcast:15 row_mask:0xa bank_mask:0xf\n" \
    "s_add_i32 s46, s46, 44\n" \
    "v_max_i32_dpp v0, v0, v0 row_bcast:31 row_mask:0xc bank_mask:0xf\n" \
    "v_max_u32_dpp v1, v1, v1 row_bcast:31 row_mask:0xc bank_mask:0xf\n" \
    "v_readlane_b32 s44, v0, 63\n" \
    "v_readlane_b32 s49, v1, 63\n" \
    "s_mul_i32 s47, s44, %[oR]\n" \
    "s_ashr_i32 s47, s47, 12\n" \
    "s_add_i32 s47, s47, s48\n" \
    "s_sub_i32 s58, s47, s46\n" \
    "s_cmp_gt_i32 s58, 112\n" \
    "s_cbranch_scc1 1f\n" \
    "12:\n" \
    "s_max_i32 s58, s58, 0\n" \
    "s_and_b32 s58, s58, -4\n" \
    "s_sub_u32 vcc_lo, s96, s58\n" \
    "s_subb_u32 vcc_hi, s97, 0\n" \
    "s_setpc_b64 vcc\n" \
    "s_nop 0\n" \
    "s_nop 0\n" \
    "s_nop 0\n" \
    "s_nop 0\n" \
    "s_nop 0\n" \
    "s_nop 0\n" \
    "s_nop 0\n" \
    "s_nop 0\n" \
    "s_nop 0\n" \
    "s_nop 0\n" \
    "s_nop 0\n" \
    "s_nop 0\n" \
    "s_nop 0\n" \
    "s_nop 0\n" \
    "s_nop 0\n" \
    "s_nop 0\n" \
    "s_nop 0\n" \
    "s_nop 0\n" \
    "s_nop 0\n" \
    "s_nop 0\n" \
    "s_nop 0\n" \
    "s_nop 0\n" \
    "s_nop 0\n" \
    "s_nop 0\n" \
    "s_nop 0\n" \
    "s_nop 0\n" \
    "s_nop 0\n" \
    "s_nop 0\n" \
    "2:\n" \
    "buffer_load_dwordx4 v[0:3], %[o], %[rs], %[pb] offen offset:0 sc1\n" \
    "buffer_load_dwordx4 v[4:7], %[o], %[rs], %[pb] offen offset:1024 sc1\n" \
    "buffer_load_dwordx4 v[8:11], %[o], %[rs], s72 offen offset:0 sc1\n" \
    "buffer_load_dwordx4 v[12:15], %[o], %[rs], s72 offen offset:1024 sc1\n" \
    "s_add_i32 %[np], %[np], 1\n" \
    "s_add_i32 %[tm], s44, s45\n" \
    "s_lshr_b32 s50, s49, 16\n" \
    "s_add_i32 s50, s50, s44\n" \
    "s_add_i32 s50, s50, %[cw]\n" \
    "s_mul_i32 s50, s50, %[oR]\n" \
    "s_ashr_i32 s50, s50, 12\n" \
    "s_add_i32 %[lk], s50, %[oC]\n" \
    "s_waitcnt vmcnt(3)\n" \
    "v_mfma_f32_16x16x32_bf16 v[16:19], %[sel], v[0:3], 0\n" \
    "v_max3_u32 v20, v0, v1, v1\n" \
    "v_max3_u32 v20, v2, v3, v20\n" \
    "s_waitcnt vmcnt(2)\n" \
    "v_mfma_f32_16x16x32_bf16 v[16:19], %[sel], v[4:7], v[16:19]\n" \
    "v_max3_u32 v20, v4, v5, v20\n" \
    "v_max3_u32 v20, v6, v7, v20\n" \
    "s_waitcnt vmcnt(1)\n" \
    "v_mfma_f32_16x16x32_bf16 v[16:19], %[sel], v[8:11], v[16:19]\n" \
    "v_max3_u32 v20, v8, v9, v20\n" \
    "v_max3_u32 v20, v10, v11, v20\n" \
    "s_waitcnt vmcnt(0)\n" \
    "v_mfma_f32_16x16x32_bf16 v[16:19], %[sel], v[12:15], v[16:19]\n" \
    "v_max3_u32 v20, v12, v13, v20\n" \
    "v_max3_u32 v20, v14, v15, v20\n" \
    "v_cmp_eq_u32 vcc, -1, v20\n" \
    "s_nop 3\n" \
    "s_cbranch_vccnz 4f\n" \
    "3:\n" \
    "v_add_f32_dpp v16, v16, v16 row_ror:8 row_mask:0xf bank_mask:0xf\n" \
    "v_add_f32_dpp v17, v17, v17 row_ror:8 row_mask:0xf bank_mask:0xf\n" \
    "v_cvt_pk_bf16_f32 v16, v16, v17\n" \
    "buffer_store_dword v16, %[so], %[rs], %[mb0] offen sc1\n" \
    "s_branch 5f\n" \
    "1:\n" \
    "s_memtime vcc\n" \
    "s_waitcnt lgkmcnt(0)\n" \
    "s_sub_i32 s58, s47, vcc_lo\n" \
    "s_cmp_gt_i32 s58, 112\n" \
    "s_cbranch_scc1 1b\n" \
    "s_branch 12b\n" \
    "4:\n" \
    "s_add_i32 %[bud], %[bud], -1\n" \
    "s_cmp_lt_i32 %[bud], 0\n" \
    "s_cbranch_scc0 2b\n" \
    "s_branch 3b\n" \
    "7:\n" \
    "s_bcnt1_i32_b64 s44, vcc\n" \
    "s_cmp_le_u32 s44, s71\n" \
    "s_mov_b32 s71, 0\n" \
    "s_cbranch_scc1 15f\n" \
    "s_cmp_eq_u32 %[tag], 1\n" \
    "s_cbranch_scc1 9f\n" \
    "s_sleep 15\n" \
    "9:\n" \
    "s_sleep 6\n" \
    "15:\n" \
    "s_add_i32 %[bud], %[bud], -1\n" \
    "s_cmp_lt_i32 %[bud], 0\n" \
    "s_cbranch_scc1 8b\n" \
    "buffer_load_dwordx2 v[0:1], %[ca], %[rs], %[cs] offen sc1\n" \
    "s_memtime s[58:59]\n" \
    "s_memrealtime s[44:45]\n" \
    "s_nop 1\n" \
    "s_memrealtime s[46:47]\n" \
    "s_nop 1\n" \
    "s_memrealtime s[48:49]\n" \
    "s_nop 1\n" \
    "s_memrealtime s[50:51]\n" \
    "s_nop 1\n" \
    "s_memrealtime s[52:53]\n" \
    "s_nop 1\n" \
    "s_memrealtime s[54:55]\n" \
    "s_nop 1\n" \
    "s_memrealtime s[56:57]\n" \
    "s_nop 1\n" \
    "s_memrealtime vcc\n" \
    "s_waitcnt lgkmcnt(0)\n" \
    "s_add_i32 s44, s44, s46\n" \
    "s_add_i32 s48, s48, s50\n" \
    "s_add_i32 s52, s52, s54\n" \
    "s_add_i32 s44, s44, s48\n" \
    "s_add_i32 s52, s52, s56\n" \
    "s_add_i32 s44, s44, s52\n" \
    "s_add_i32 s59, s44, vcc_lo\n" \
    "s_sub_i32 s44, s59, %[oT]\n" \
    "s_mov_b32 %[oT], s59\n" \
    "s_sub_i32 s45, s58, %[oC]\n" \
    "s_mov_b32 %[oC], s58\n" \
    "s_mul_i32 s46, s44, %[oR]\n" \
    "s_lshr_b32 s46, s46, 12\n" \
    "s_sub_i32 s46, s45, s46\n" \
    "s_sub_i32 s44, s44, 1000\n" \
    "s_cmp_lt_u32 s44, 2000\n" \
    "s_cselect_b32 s46, s46, 0\n" \
    "s_add_i32 %[oR], %[oR], s46\n" \
    "s_sub_i32 s45, %[oT], %[c8]\n" \
    "s_waitcnt vmcnt(0)\n" \
    "s_branch 6b\n" \
    "5:\n" \
    ""
#define OWNER_CLOBBER "v0", "v1", "v2", "v3", "v4", "v5", "v6", "v7", "v8", "v9", "v10", "v11", "v12", "v13", "v14", "v15", "v16", "v17", "v18", "v19", "v20", "s44", "s45", "s46", "s47", "s48", "s49", "s50", "s51", "s52", "s53", "s54", "s55", "s56", "s57", "s58", "s59", "s68", "s69", "s70", "s71", "s72", "s96", "s97", "vcc", "scc", "memory"
constexpr int O_W = 0;
constexpr int O_STG = 131072;
constexpr int O_B = O_STG + 20480;
constexpr int OUT_REP = 7;
#define OUT_AUX 16
template <int O, class V> DI void put4(V& d, u32x4 f) {
#pragma unroll
    for (int j = 0; j < 4; ++j) d[O + j] = f[j];
}
DI void role_outp(const Args& a, LAS char* lds, unsigned lds_base, int nb, int rep, rsrc_t rw, int& budget) {
    const int tid = threadIdx.x, lane = tid & 63, w = __builtin_amdgcn_readfirstlane(tid >> 6);
    WG_BARRIER();
    const int n = lane & 15, g = lane >> 4;
    const int wl = O_W + w * 32768 + lane * 16;
    u32x32 A[8], V[4];
    {
        const int stg = O_STG + w * 5120;
        const rsrc_t ro = __builtin_amdgcn_make_buffer_rsrc((void*)a.Wo, 0, HD * DM * 2, 0x00020000);
        const int so = (1024 * w + (lane >> 3)) * (DM * 2) + nb * 128 + (lane & 7) * 16, sw = stg + (lane >> 3) * 160 + (lane & 7) * 16;
        const int ra = stg + (16 * (g & 1) + 4 * (g >> 1) + (n >> 2)) * 160 + (n & 3) * 8;
        constexpr int PB = 4;
        u32x4 b[PB][4];
        auto fetch = [&](int ks) {
#pragma unroll
            for (int i = 0; i < 4; ++i) b[ks % PB][i] = __builtin_amdgcn_raw_buffer_load_b128(ro, so, (64 * (ks >> 1) + 16 * (ks & 1) + 8 * (i & 1) + 32 * (i >> 1)) * (DM * 2), 0);
        };
        unsigned tp = now();
        const int pc = a.d1 >> 16;
#pragma unroll
        for (int ks = 0; ks < PB; ++ks) { fetch(ks); FENCE(); tp += pc; while ((int)(now() - tp) < 0) {} }
        FENCE();
        static_for<32>([&](auto ic) {
            constexpr int ks = decltype(ic)::value;
#pragma unroll
            for (int i = 0; i < 4; ++i) *(LAS u32x4*)(lds + sw + i * 1280) = b[ks % PB][i];
            if (ks + PB < 32) { fetch(ks + PB); FENCE(); tp += pc; }
            u32x4 f[4];
#pragma unroll
            for (int t = 0; t < 4; ++t) { const u32x2 lo = tr_read(lds, ra + t * 32), hi = tr_read(lds, ra + t * 32 + 1280); f[t] = (u32x4){lo.x, lo.y, hi.x, hi.y}; }
            put4<4 * (ks & 7)>(A[ks >> 3], f[0]); put4<4 * (ks & 7)>(A[4 + (ks >> 3)], f[1]); put4<4 * (ks & 7)>(V[ks >> 3], f[2]);
            *(LAS u32x4*)(lds + wl + ks * 1024) = f[3];
            if constexpr (ks == 7) asm volatile("" : "+{a[0:31]}"(A[0]), "+{a[128:159]}"(A[4]), "+{v[128:159]}"(V[0]));
            if constexpr (ks == 15) asm volatile("" : "+{a[32:63]}"(A[1]), "+{a[160:191]}"(A[5]), "+{v[160:191]}"(V[1]));
            if constexpr (ks == 23) asm volatile("" : "+{a[64:95]}"(A[2]), "+{a[192:223]}"(A[6]), "+{v[192:223]}"(V[2]));
            if constexpr (ks == 31) asm volatile("" : "+{a[96:127]}"(A[3]), "+{a[224:255]}"(A[7]), "+{v[224:255]}"(V[3]));
            FENCE();
            if (ks + PB < 31) while ((int)(now() - tp) < 0) {}
        });
    }
    wait_prog(rw, F_S4 + lane * 4, true, 1, budget);
    const rsrc_t ru = __builtin_amdgcn_make_buffer_rsrc(a.out, 0, SEQ * DM * 2, 0x00020000);
    constexpr int SP = 2 * LATE - 2;
    auto first = [&](int u) { return u >= SP ? 8 * u + 8 * SP : 16 * u; };
    auto st_s = [&](int u) { return (u >= 2 * FAST ? F_H + ((first(u) >> 4) - 2 * FAST) * 4096 : F_O + (u & 3) * 4) + w * 1024; };
    auto bn_s = [&](int u) { return OFF_BO + (64 * nb + 16 * w) * BO_ROW + first(u) * 2; };
    auto gn_s = [&](int u) { return OFF_GT + (first(u) + 4 * w) * 4; };
    int st = -1;
    u32x2 bn = ld64(rw, (lane >> 2) * BO_ROW + (lane & 3) * 8, bn_s(rep));
    float gn = __uint_as_float(ld32(rw, g * 4, gn_s(rep)));
    __builtin_amdgcn_s_waitcnt(0);
#define OUT_OPS "=&{v[0:15]}"(acc), "=&{v104}"(top), "=&{v105}"(st), "=&{v[106:107]}"(bn), "=&{v108}"(gn) \
        : "{a[0:31]}"(A[0]), "{a[32:63]}"(A[1]), "{a[64:95]}"(A[2]), "{a[96:127]}"(A[3]), "{a[128:159]}"(A[4]), "{a[160:191]}"(A[5]), "{a[192:223]}"(A[6]), "{a[224:255]}"(A[7]), \
          "{v[128:159]}"(V[0]), "{v[160:191]}"(V[1]), "{v[192:223]}"(V[2]), "{v[224:255]}"(V[3]), [rw] "s"(rw), [so] "s"(t0 * 8192), [wl] "v"(lds_base + O_W + w * 32768 + lane * 16), \
          [tv] "v"(tv), [tm] "s"(tm), [vo] "v"(vo), [sv] "v"(sv), [bv] "v"((lane >> 2) * BO_ROW + (lane & 3) * 8), [gv] "v"(g * 4), \
          [ss] "s"(st_s(u + OUT_REP)), [bs] "s"(bn_s(u + OUT_REP)), [gs] "s"(gn_s(u + OUT_REP))
    for (int u = rep; u < SP + 4; u += OUT_REP) {
        const bool fast = u >= 2 * FAST, sp = u >= SP;
        const int t0 = first(u);
        WG_BARRIER();
        int lane = opaque_lane(), n = lane & 15, g = lane >> 4;
        if (u == 3 * OUT_REP && nb == 0) __builtin_amdgcn_raw_buffer_store_b64(launch_tag(a), rw, w + lane == 0 ? F_EPOCH : OOB, 0, 16);
        S4S(0)
        TT(u >= 119, 0)
        *(LAS u32x2*)(lds + O_B + (16 * w + (lane >> 2)) * 40 + (lane & 3) * 8) = bn;
        const float gt = gn;
        const int tv = OFF_OF + w * 16384 + nb * 2048 + g * 65536 + n * 128, tm = __builtin_amdgcn_readfirstlane(nb < 8 ? sp ? 0xFFFF : -1 : 0);
        const int vo = OFF_OF + w * 16384 + (n & 7) * 16 + (sp ? (n >> 3) * 512 + g * 128 : (n >> 3) * 65536 + (g & 1) * 512 + (g >> 1) * 128);
        const int thr = fast ? 0 : u >> 1, sv = (lane & 7) * 128;
        if (!__all(st >= thr)) do { __builtin_amdgcn_s_sleep(1); st = (int)ld32(rw, sv, st_s(u)); asm volatile("" ::: "memory"); } while (!__all(st >= thr) && --budget >= 0);
        if (fast) { const unsigned t = now(); (void)wait_until(t + wave_max32(min((int)(2 * (st - t)) >> 1, 500))); }
        S4S(1)
        TT(u >= 119, 1)
        f32x16 acc; unsigned top;
        if (sp) {
            do asm volatile(OUT8_ASM : OUT_OPS, [idx] "v"(n & 8 ? 0xEEEE : 0x4444) : OUT_CLOBBER); while (__any(top == SENT) && --budget >= 0);
            TT(u >= 119, 2)
            lane = opaque_lane(); n = lane & 15; g = lane >> 4;
#pragma unroll
            for (int i = 0; i < 8; ++i) *(LAS float*)(lds + O_STG + (w * 16 + 4 * (g & 1) + (i & 3)) * 272 + (g >> 1) * 128 + (i >> 2) * 64 + n * 4) = acc[i];
        } else {
            do asm volatile(OUT16_ASM : OUT_OPS : OUT_CLOBBER); while (__any(top == SENT) && --budget >= 0);
            S4S(2)
            TT(u >= 119, 2)
            lane = opaque_lane(); n = lane & 15; g = lane >> 4;
#pragma unroll
            for (int t = 0; t < 4; ++t) *(LAS f32x4*)(lds + O_STG + (w * 16 + n) * 272 + t * 64 + g * 16) = (f32x4){acc[4 * t], acc[4 * t + 1], acc[4 * t + 2], acc[4 * t + 3]};
        }
        WG_BARRIER();
        S4S(3)
        const int tok = 4 * w + g, c4 = 4 * n;
        f32x4 r[4]; u16 bv[4];
#pragma unroll
        for (int ww = 0; ww < 4; ++ww) { r[ww] = *(LAS f32x4*)(lds + O_STG + (ww * 16 + tok) * 272 + c4 * 4); bv[ww] = *(LAS u16*)(lds + O_B + (c4 + ww) * 40 + tok * 2); }
        FENCE();
        const f32x4 v = gt * ((r[0] + r[1]) + (r[2] + r[3])) + (1.0f - gt) * (f32x4){bf2f(bv[0]), bf2f(bv[1]), bf2f(bv[2]), bf2f(bv[3])};
        __builtin_amdgcn_raw_buffer_store_b64((u32x2){pack2(v[0], v[1]), pack2(v[2], v[3])}, ru, sp && tok >= 8 ? OOB : (tok * DM + 64 * nb + c4) * 2, t0 * (DM * 2), OUT_AUX);
        S4S(4)
        TT(u >= 119, 3)
        TT(u >= 105 && u < 112, 4)
    }
}

constexpr int L_MEMS = 0;
constexpr int L_Q = 9216;
constexpr int L_KO = L_Q + 2560;
constexpr int L_KX = 16384;
constexpr int L_W = 32768;
constexpr int C_RING = 8;
static_assert(S_TOTAL <= L_SINK && L_W + C_RING * 16384 <= L_TOTAL && L_KO + 8 * 576 <= L_KX, "LDS budget");
static_assert(P_STG + P_RING * 16384 <= L_TOTAL, "LDS budget");

DI int pos2col(int p) { return p < 512 ? (p & ~63) + 16 * (p & 3) + ((p & 63) >> 2) : p; }
DI void unit_rc(int u, int& i, int& p0) {
    if (u < 512) { i = 2 * ((u >> 2) & 3) + (u & 1); p0 = (u & ~15) + 4 * (u & 2); }
    else { const int v = u - 512; i = (v >> 1) & 7; p0 = 512 + 16 * (v >> 4) + 8 * (v & 1); }
}
DI void mem0_arrange(LAS char* lds, int raw, int tid) {
#pragma unroll
    for (int k = 0; k < 2; ++k) {
        int i, p0; unit_rc(tid + 256 * k, i, p0);
        unsigned r[4];
#pragma unroll
        for (int j = 0; j < 4; ++j) r[j] = *(LAS unsigned*)(lds + raw + (i * DM + pos2col(p0)) * 2 + 32 * j);
        *(LAS u32x4*)(lds + L_MEMS + (tid + 256 * k) * 16) = (u32x4){__builtin_amdgcn_perm(r[1], r[0], 0x05040100), __builtin_amdgcn_perm(r[3], r[2], 0x05040100),
                                                                     __builtin_amdgcn_perm(r[1], r[0], 0x07060302), __builtin_amdgcn_perm(r[3], r[2], 0x07060302)};
    }
    int i, p0; unit_rc(512 + (tid & 63), i, p0);
    *(LAS u32x4*)(lds + L_MEMS + 8192 + (tid & 63) * 16) = *(LAS u32x4*)(lds + raw + (i * DM + p0) * 2);
}
DI unsigned read_canaries(int s, rsrc_t rs, int lane, int& budget) {
    const int o = 2 * OFF_CAN + (s & 3) * 16384 + (lane >> 2) * 512 + (lane & 3) * 16;
    u32x2 v;
    for (;;) {
        v = ld64(rs, o, 0);
        asm volatile("" ::: "memory");
        if (__all((v.y & 0xffff) == (unsigned)(s + 1))) break;
        if (--budget < 0) break;
    }
    const unsigned t = 8 * now();
    return t + (unsigned)wave_max32(min(max((int)(v.x - t), -0x4000), 0x4000));
}
DI u32x8 sm_frag(LAS char* lds, int sl, int r0, int sa, int sH, int cb, int lo8) {
    auto rd = [&](int r) { return tr_read(lds, sl + r * 256 + ((cb ^ (2 * swz(r))) * 16) + lo8); };
    const u32x2 a = rd(r0), b = rd(r0 + sa), c = rd(r0 + sH), d = rd(r0 + sa + sH);
    return (u32x8){a.x, a.y, b.x, b.y, c.x, c.y, d.x, d.y};
}
#define GS(slot, i)
#define GV(slot, i, v)
DI int tp_unit(int U, int sel) { return OFF_TP + cmap((U & 511) * 4096) + ((sel >> (4 + (U >> 8 & 1)) ^ U >> 9) & 1) * 8192; }
template <int N> DI void finish(const Args& a, rsrc_t rw, int U, int sel, unsigned t, int& budget) {
    const int lane = opaque_lane(), n = lane & 7, g = lane >> 4;
    const rsrc_t ru = __builtin_amdgcn_make_buffer_rsrc(a.out, 0, SEQ * DM * 2, 0x00020000);
    constexpr int M = (N + 1) / 2;
    int at[M]; f32x2 bv[M]; float gt[M];
#pragma unroll
    for (int i = 0; i < M; ++i) {
        const int u = U + 2 * i + (lane >> 3 & 1 & (N - 1));
        const int tok = LATE * SEG + (U < 512 ? 2 * (u & 15) + (n >> 2) : (u & 32) + 4 * (u & 7) + (n >> 1));
        const int col = U < 512 ? 32 * (u >> 4) + 16 * (g >> 1) + 4 * (n & 3) + 2 * (g & 1) : 512 + 2 * (u & 24) + 8 * (n & 1) + 2 * g;
#pragma unroll
        for (int r = 0; r < 2; ++r) bv[i][r] = bf2f(__builtin_amdgcn_raw_buffer_load_b16(rw, col * BO_ROW + tok * 2, OFF_BO + r * BO_ROW, 16));
        gt[i] = __uint_as_float(ld32(rw, tok * 4, OFF_GT));
        at[i] = (tok * DM + col) * 2;
    }
    u32x4 one;
#pragma unroll
    for (int j = 0; j < 4; ++j) one[j] = (lane >> 2 & 3) == j ? 0x3F80u << 16 * (lane & 1) : 0u;
#pragma unroll
    for (int i = 0; i < M; ++i) { bv[i] *= 1.0f - gt[i]; asm volatile("" : "+v"(bv[i]), "+v"(one)); }
    (void)wait_until(t);
    f32x4 v[N];
    for (;;) {
        u32x4 d[4 * N];
#pragma unroll
        for (int k = 0; k < 4 * N; ++k) d[k] = ld128(rw, lane * 16 + (k & 3) * 1024, tp_unit(U + (k >> 2), sel));
        unsigned top = 0;
#pragma unroll
        for (int i = 0; i < N; ++i) {
            asm volatile(M16 "%0, %1, %2, 0\n\t" M16 "%0, %1, %3, %0\n\t" M16 "%0, %1, %4, %0\n\t" M16 "%0, %1, %5, %0\n\ts_nop 7" : "=&v"(v[i]) : "v"(one), "v"(d[4 * i]), "v"(d[4 * i + 1]), "v"(d[4 * i + 2]), "v"(d[4 * i + 3]));
            top = mx4(mx4(mx4(mx4(top, d[4 * i]), d[4 * i + 1]), d[4 * i + 2]), d[4 * i + 3]);
        }
        if (!__any(top == SENT) || --budget < 0) break;
        __builtin_amdgcn_s_sleep(4);
    }
#pragma unroll
    for (int i = 0; i < M; ++i) {
        f32x2 x;
#pragma unroll
        for (int r = 0; r < 2; ++r) {
            auto both = [](float y) { return y + __int_as_float(__builtin_amdgcn_update_dpp(0, __float_as_int(y), 0x128, 0xf, 0xf, true)); };
            const float p = both(v[2 * i][r]), q = both(v[(2 * i + 1) & (N - 1)][r]);
            x[r] = gt[i] * (lane & 8 ? q : p) + bv[i][r];
        }
        __builtin_amdgcn_raw_buffer_store_b32(pack2(x[0], x[1]), ru, at[i], 0, OUT_AUX);
    }
}
#define TOUCH_M 15
DI void owner_loop(const Args& a, unsigned lds_base, int G, rsrc_t rs, rsrc_t rw, int sel, unsigned tbase, unsigned cbase, int& budget) {
    const int lane = threadIdx.x & 63, n = lane & 15, g = lane >> 4;
    const bool ex = G >= 64;
    const u32x4 sent4 = {SENT, SENT, SENT, SENT};
    const int js = (n & 2) ? 8 : ex ? (n & 4) + 2 * (n & 1) + (n >> 3) : 2 * (n >> 2) + (n & 1);
    u32x4 selu;
#pragma unroll
    for (int j = 0; j < 4; ++j) selu[j] = (js == 2 * j ? 0x3F80u : 0u) | (js == 2 * j + 1 ? 0x3F800000u : 0u);
    const int o = (part_unit(G, 0) ^ (sel & 8) * 256) + lane * 16;
    const int so = (n >> 3) * 16384 + ((ex ? 32768 + (G & 4) * 512 + (G & 3) * 128 : (G & 16) * 256 + (G & 32) * 64 + (G & 15) * 128) ^ (~sel & 8) * 256) + (g & 1) * 4 +
                   (ex ? (n & 4) * 16 + (g >> 1) * 32 + (n & 3) * 8 : (n & 6) * 16 + (g >> 1) * 16 + (n & 1) * 8);
    unsigned fails = 0;
    const int c8 = G >= 64 ? a.c1 >> 16 & 255 : (G & 28) == 0 ? a.c1 >> 8 & 255 : a.c1 & 255;
    const bool gv_on = true; (void)gv_on;
    unsigned oT = now(), oC = (unsigned)__builtin_readcyclecounter(), oR;
    { const unsigned r = (unsigned)(512.0f * (float)(oC - cbase) * __builtin_amdgcn_rcpf((float)(oT - tbase))); asm volatile("s_nop 0\n\tv_readfirstlane_b32 %0, %1" : "=s"(oR) : "v"(r)); }
    oT = 8 * oT + 16;
    unsigned lk = oC;
    budget = __builtin_amdgcn_readfirstlane(budget);
    const int ca = 2 * OFF_CAN + (lane >> 2) * 512 + (blockIdx.x & (NCP - 1)) * 128 + (lane & 3) * 16;
    unsigned tc;
    const unsigned oR0 = oR;
    oR = 0;
#pragma unroll 1
    for (int s = -1; s < NSEG - 1; ++s) {
        asm volatile("" : "+s"(s));
        const int pb = (s & 1) * 2 * PART_STEP;
        int np;
        asm volatile(OWNER_ASM : [np] "=&s"(np), [tm] "=&s"(tc), [bud] "+s"(budget), [oT] "+s"(oT), [oC] "+s"(oC), [oR] "+s"(oR), [lk] "+s"(lk)
            : [c8] "s"(c8), [ca] "v"(s < 0 ? OOB : ca), [cs] "s"((s & 3) * 16384), [tag] "s"(s + 1), [o] "v"(s < 0 ? OOB : o), [so] "v"(s < 0 ? OOB : so), [sel] "v"(selu), [rs] "s"(rs), [pb] "s"(pb), [mb0] "s"(2 * (OFF_MEMB + ((s + 1) & 3) * MEMB_STRIDE)),
              [cw] "s"(a.cw - c8), [rd] "s"(a.rd), [sv] "v"(sent4), [ro] "v"(s < 0 ? OOB : o), [pq] "s"(2 * PART_STEP - pb), [rm] "v"(s < 0 ? OOB : so), [mq] "s"(2 * (OFF_MEMB + ((s + 2) & 3) * MEMB_STRIDE)),
              [rw] "s"(rw), [rt] "v"(s >= 24 && s < 56 ? lane * 16 : OOB), [tq] "s"(tp_unit(8 * G + (s >> 2 & 7), sel) + (s & 3) * 1024) : OWNER_CLOBBER);
        if (s < 0) { oR = oR0; continue; }
        if ((s & TOUCH_M) == 8) {
            const char* pc; int d, len;
            asm volatile("s_getpc_b64 %0\n1:\ts_mov_b32 %1, .Lcold-1b+2048\n\ts_mov_b32 %2, .Lk_end-1b+2048" : "=s"(pc), "=s"(d), "=s"(len));
            touch(pc - 2048, max(min((G & 4 ? d + (G & 8) * 1024 : 0) + lane * 128, len), 0), lds_base);
        }
        fails += np > 1;
    }
    wait_prog(rw, F_S4 + lane * 4, true, 1, budget);
    const unsigned t = now(), t0 = t + min((int)(tc - 8 * t) >> 3, 100);
    finish<4>(a, rw, 4 * G + 4 * (G & 64), sel, t0 + (a.xa >> 8 & 255), budget);
    if (G >= 32 && G < 64) finish<1>(a, rw, 512 + G, sel, t0 + (a.xb >> 24), budget);
}
#define CORNER 5
DI void role_owner(const Args& a, unsigned lds_base, int ow, rsrc_t rw, int& budget) {
    const int tid = threadIdx.x, lane = tid & 63, w = __builtin_amdgcn_readfirstlane(tid >> 6);
    const unsigned tbase = now(), cbase = (unsigned)__builtin_readcyclecounter();
    if (ow < 56 && w) { prefetch(a, lds_base, ow & 3, (ow >> 2) * 3 + w - 1, 42); VM_DRAIN(); }
    if (w >= 2 || (w == 1 && (ow & ~11))) return;
    const u32x2 tag = launch_tag(a);
    tell_side(rw, tag);
    if (ow >= 56) {
        auto rtt = [&](int o) {
            unsigned long long t0, t1;
            asm volatile("s_memtime %0\n\ts_waitcnt lgkmcnt(0)" : "=s"(t0) :: "memory");
            st32(SENT, rw, lane == 0 ? 0 : OOB, o);
            asm volatile("s_waitcnt vmcnt(0)\n\ts_memtime %0\n\ts_waitcnt lgkmcnt(0)" : "=s"(t1) :: "memory");
            return (unsigned)(t1 - t0);
        };
        const int p = ow - 56, o = p == 0 ? 0 : p == 1 ? 32768 : p < 4 ? OFF_TP + (p - 2) * (2 << 20) : (p & 2) * 4096 + 1024 + (p & 1) * 1024;
        const int d = p == 1 ? 2048 : p < 4 ? 8192 : p & 1 ? 16384 : 4096;
        unsigned t0 = ~0u, t1 = ~0u;
        for (int r = 0; r < CORNER; ++r) { t0 = min(t0, rtt(o + r * 128)); t1 = min(t1, rtt(o + d + r * 128)); }
        const unsigned near = (t1 < t0) ^ (p == 1 ? die_side() : p > 4);
        __builtin_amdgcn_raw_buffer_store_b64((u32x2){(tag.x & ~7u) | near, tag.y}, rw, lane == 0 ? F_INIT + (105 + ow) * 128 : OOB, 0, 16);
    }
    const int sel = wait_init(rw, tag, budget, 161) ^ a.c1 >> 24;
    const rsrc_t rs = __builtin_amdgcn_make_buffer_rsrc(a.ws + (sel & 1) * 8192, 0, COMM_BYTES - 8192, 0x00020000);
    const int side = agree_side(rw, tag, ~tag, budget), cc = sel >> 1;
    const int G = (w ? 64 + 4 * side : 32 * side + (ow >> 1 & 24) + (ow & 4)) + ((cc ^ ow >> 1 ^ w) & 1) + ((cc ^ 2 * ow ^ ow >> 4) & 2);
    owner_loop(a, lds_base, G, rs, rw, sel, tbase, cbase, budget);
}

template <int O, class V> DI void put8(V& d, u32x8 f) {
#pragma unroll
    for (int j = 0; j < 8; ++j) d[O + j] = f[j];
}
DI f32x4 SMF64(u32x4 a, u32x8 b, f32x4 c, int i) { asm volatile("s_nop 1\n\tv_smfmac_f32_16x16x64_bf16 %0, %1, %2, %3\n\ts_nop 7" : "+v"(c) : "v"(a), "v"(b), "v"(i)); return c; }
DI f32x4 SMF32(u32x2 a, u32x4 b, f32x4 c, int i) { asm volatile("s_nop 1\n\tv_smfmac_f32_16x16x32_bf16 %0, %1, %2, %3\n\ts_nop 7" : "+v"(c) : "v"(a), "v"(b), "v"(i)); return c; }
DI f32x4 MFV(u32x4 a, u32x4 b, f32x4 c) { asm volatile("s_nop 1\n\tv_mfma_f32_16x16x32_bf16 %0, %1, %2, %0\n\ts_nop 7" : "+v"(c) : "v"(a), "v"(b)); return c; }

DI void role_head(const Args& a, LAS char* lds, unsigned lds_base, int h, rsrc_t rw, int& budget) {
    const int tid = threadIdx.x, w = __builtin_amdgcn_readfirstlane(tid >> 6);
    char* ws = a.ws;
    const unsigned tbase = now(), cbase = (unsigned)__builtin_readcyclecounter();
    int tw = 0;
    u32x32 A0, A1, A2, A3, A4, A5, A6, A7, R0, R1, R2, R3; u32x8 R4 = {};
    u32x2 vm;
    auto dma = [&](int voff, int soff, unsigned dst) { bdma(rw, voff, soff, dst); };
    auto fetch_kv = [&](int s, int lane) {
        const int n = lane & 15, g = lane >> 4;
        const int kxs = (g >> 1) * 1024 + ((g & 1) * 32 + 16 * (n >> 3) + (n & 7)) * 16;
        const int vxs = (g >> 1) * 1024 + ((g & 1) * 32 + n) * 16;
        const int so_ = (s * 32 + h) * 8192 + w * 2048;
        const unsigned xdst = lds_base + L_KX + w * 2048;
        dma(kxs, OFF_KF + so_, xdst); dma(kxs, OFF_KF + so_ + 128, xdst + 1024);
        dma(vxs, OFF_VF + so_, xdst + 8192); dma(vxs, OFF_VF + so_ + 256, xdst + 8192 + 1024);
    };
    u32x2 tag, in;
    unsigned wo_at;
    u32x8 S1, S2, S3, scon;
    int sel, kv_rdy = 0, tp, tq_, tx, U1, ua;
    constexpr int SETUP_G = 4;
    auto setup = [&]() {
        scon[0] = w; scon[1] = 2 * OFF_CAN + (h >> 1) * 512 + (2 * (h & 1) + tw) * 16; scon[2] = F_KV + h * 512 + w * 128; scon[3] = 65536 + w * 4096;
        scon[4] = OFF_KF + h * 8192 + w * 2048; scon[5] = F_KVM + h * 256 + tw * 128; scon[6] = tw - 1; scon[7] = (tw ? OFF_VM : OFF_KM) + (h - NH) * 2048 + w * 512;
        const unsigned t = now(), c = (unsigned)__builtin_readcyclecounter();
        const float r = (float)(c - cbase) * __builtin_amdgcn_rcpf(8.0f * (float)(t - tbase));
        S1 = (u32x8){0u, (unsigned)a.e0, 0u, (unsigned)budget, (unsigned)a.d0, (unsigned)(4294967296.0f * __builtin_amdgcn_rcpf(r)), (unsigned)(a.c2 + a.b0), 0u};
        S2 = (u32x8){0u, 8 * t + 16, 0u, (unsigned)a.up, (unsigned)a.dn, (unsigned)a.c2, 0u, 1u};
        S3 = (u32x8){c, (unsigned)(4096.0f * r), 2u * OFF_MEMB + ((h >> 3 ^ sel >> 2) & 1) * 16384 + (w & 2) * 2048 + (~sel & 8) * 256 + (w & 1) * 1024, w == 3 ? ~0u : 0u};
        const int U0 = (2 * tw + (w >> 1)) * 64 + 8 * (w & 1);
        U1 = 4 * (2 * h + tw) + w;
        tp = tp_unit(U0, sel) + h * 128; tq_ = tp_unit(256 + U0, sel) + h * 128; tx = tp_unit(512 + (U0 >> 3) + 3 * (w & 1), sel) + h * 128; ua = tp_unit(256 + U1, sel);
        asm volatile("" : "+{v[0:7]}"(S1), "+{v[8:15]}"(S2), "+{v[16:23]}"(S3), "+{v[24:31]}"(scon), "+{v[120:151]}"(R0), "+s"(tp), "+s"(tq_), "+s"(tx), "+s"(U1), "+s"(ua), "+s"(sel));
    };
    {
        const int lane = tid & 63, n = lane & 15, g = lane >> 4;
#pragma unroll
        for (int j = 0; j < 3; ++j) glds16(a.mem0, min(4 * j + w, 8) * 1024 + lane * 16, lds_base + L_KX + min(4 * j + w, 8) * 1024);
        const int tq = n >> 2, tc = (n & 3) >> 1, lo8 = (n & 1) * 8;
        const int r_nat = 16 * (g >> 1) + 4 * (g & 1) + (tq & 1) + 8 * (tq >> 1);
        const int r_sig = 16 * (tq & 1) + 4 * (g >> 1) + (g & 1) + 2 * (tq >> 1);
        const int r_o = 16 * (tq & 1) + 8 * (g >> 1) + 4 * (g & 1) + (tq >> 1);
#define WFRAG(sl, kb, t) ((kb) < 8 ? sm_frag(lds, sl, r_sig, 32, 8, 4 * w + 2 * (t) + tc, lo8) : sm_frag(lds, sl, r_nat, 2, 32, 4 * w + 2 * (t) + tc, lo8))
        const u32x4 ro = ring_offs(w, lane, HD);
        auto issue = [&](int i) {
            const unsigned slot = lds_base + L_W + (i % C_RING) * 16384 + w * 4096;
            if (i == 23) ring_issue(a.Wo + (size_t)h * DH * DM + 512, ring_offs(w, lane, [](int r, int c) { return (r + 64 * (c >> 3)) * (DM * 2) + (c & 7) * 16; }), slot);
            else if (i < 28) ring_issue((i < 8 || i == 20 ? a.Wq : i < 16 || i == 21 ? a.Wk : a.Wv) + (size_t)(64 * (i < 16 ? i & 7 : i < 20 ? i - 12 : i < 24 ? 8 : i - 24)) * HD + h * DH, ro, slot);
            else ring_issue(a.Wo + (size_t)(h * DH + 64 * ((i & 1) ^ ((i - 28) >> 1))) * DM + 256 * tw + 128 * (i & 1), ring_offs(w, lane, DM), slot);
        };
        auto keep = [&](int at, u32x8 f) {
            *(LAS u32x4*)(lds + at + lane * 16) = (u32x4){f[0], f[1], f[2], f[3]};
            *(LAS u32x4*)(lds + at + 1024 + lane * 16) = (u32x4){f[4], f[5], f[6], f[7]};
        };
#pragma unroll
        for (int i = 0; i < C_RING; ++i) issue(i);
        tag = launch_tag(a);
        tell_side(rw, tag);
        const int ai = 2 * (n >> 2) + (n & 1), ahf = (n >> 1) & 1;
        R0[0] = R0[1] = R0[2] = R0[3] = 0x3F803F80u;
        R0[4] = ahf ? 0xEEEE : 0x4444;
        R0[5] = (g >> 1) * 512 + (n >> 2) * 128 + (g & 1) * 64 + (n & 3) * 16;
        R0[6] = lds_base + L_MEMS + 8192 + g * 256 + ai * 32 + ahf * 16;
        R0[7] = lane * 16;
        R0[8] = lds_base + L_Q + 2 * g * 320 + (16 * w + n) * 4;
        R0[9] = lds_base + L_KO + 2 * g * 576 + (16 * w + (n & 8) + 4 * (n & 1) + ((n >> 1) & 3)) * 4;
        R0[10] = lds_base + L_Q + ai * 320 + g * 16;
        R0[11] = lds_base + L_KO + ai * 576 + g * 32 + ahf * 16;
        R0[12] = lds_base + L_KX + 8192 + w * 2048 + (g >> 1) * 256 + n * 16 + (g & 1) * 8;
        R0[13] = lds_base + L_W + w * 4096 + lane * 16;
        wo_at = lds_base + L_W + (4 + (w >> 1)) * 16384 + r_o * 256 + ((8 * (w & 1) + tc) ^ (2 * swz(r_o))) * 16 + lo8;
        vm.x = vm.y = (w << 14) + r_sig * 256 + ((4 * w + tc) ^ (2 * swz(r_sig))) * 16 + lo8;
        asm volatile("" : "+{v[120:151]}"(R0), "+{v119}"(wo_at), "+{v[96:97]}"(vm), "+{v[248:255]}"(R4));
        u32x2 other;
        ring_wait<4 * C_RING + 1>();
        mem0_arrange(lds, L_KX, tid);
        static_for<6>([&](auto ic) {
            constexpr int G = decltype(ic)::value;
            ring_wait<(G == 2 || G >= SETUP_G ? 16 : 17)>();
            if constexpr (G == SETUP_G) { asm volatile("" : "+v"(other)); tw = agree_side(rw, tag, other, budget); }
            const int sl = L_W + (G & 1) * 65536;
            auto fr = [&](int j, int t) { return WFRAG(sl + ((j ^ w) << 14), 0, t); };
            if constexpr (G < 4) {
                u32x32& A = G == 0 ? A0 : G == 1 ? A2 : G == 2 ? A4 : A6;
                u32x32& B = G == 0 ? A1 : G == 1 ? A3 : G == 2 ? A5 : A7;
                put8<0>(A, fr(0, 0)); put8<8>(A, fr(0, 1)); put8<16>(A, fr(1, 0)); put8<24>(A, fr(1, 1));
                put8<0>(B, fr(2, 0)); put8<8>(B, fr(2, 1)); put8<16>(B, fr(3, 0)); put8<24>(B, fr(3, 1));
                if constexpr (G == 0) asm volatile("" : "+{a[0:31]}"(A0), "+{a[32:63]}"(A1));
                if constexpr (G == 1) asm volatile("" : "+{a[64:95]}"(A2), "+{a[96:127]}"(A3));
                if constexpr (G == 2) asm volatile("" : "+{a[128:159]}"(A4), "+{a[160:191]}"(A5));
                if constexpr (G == 3) asm volatile("" : "+{a[192:223]}"(A6), "+{a[224:255]}"(A7));
            } else if constexpr (G == 4) {
                put8<16>(R1, fr(0, 0)); put8<24>(R1, fr(0, 1)); put8<0>(R2, fr(1, 0)); put8<8>(R2, fr(1, 1));
                put8<16>(R2, fr(2, 0)); put8<24>(R2, fr(2, 1)); put8<0>(R3, fr(3, 0)); put8<8>(R3, fr(3, 1));
            } else if constexpr (G == 5) {
                put8<16>(R0, WFRAG(sl, 8, 0)); put8<24>(R0, WFRAG(sl, 8, 1)); put8<0>(R1, WFRAG(sl + 16384, 8, 0)); put8<8>(R1, WFRAG(sl + 16384, 8, 1));
                put8<16>(R3, WFRAG(sl + 32768, 8, 0)); put8<24>(R3, WFRAG(sl + 32768, 8, 1));
                keep(L_KX + w * 2048, sm_frag(lds, sl + 49152, r_o, 2, 32, 8 * (w >> 1) + 4 * tw + 2 * (w & 1) + tc, lo8));
                R0[14] = part_unit((2 * (4 * tw + w) + (n >> 3)) * 4 + g, h) + (n & 7) * 16;
                R0[15] = (g >> 1) == (w >> 1) ? part_unit(64 + 2 * (2 * tw + (w & 1)) + (g >> 1), h) + (g & 1) * 64 + n * 4 : OOB;
                asm volatile("" : "+{v[120:151]}"(R0));
            }
            WG_BARRIER();
            if constexpr (G == SETUP_G - 1) asm volatile("buffer_load_dwordx2 %0, %1, %2, 0 offen sc1" : "=v"(in) : "v"(init_at(33 + 4 * h + w)), "s"(rw) : "memory");
#pragma unroll
            for (int i = 0; i < 4; ++i) issue(4 * G + 8 + i);
            if constexpr (G == 2) asm volatile("buffer_load_dwordx2 %0, off, %1, %2 sc1" : "=v"(other) : "s"(rw), "s"(side_at(16)) : "memory");
            if constexpr (G == SETUP_G) { asm volatile("" : "+v"(in)); sel = (init_ok(in, tag) ? init_sel(in) : wait_init(rw, tag, budget, 33 + 4 * h + w)) ^ a.c1 >> 24; }
            if constexpr (G == 5) { R0[14] ^= (sel & 8) * 256; R0[15] ^= (sel & 8) * 256; setup(); }
        });
        asm volatile("" : "+{v[120:151]}"(R0), "+{v[152:183]}"(R1), "+{v[184:215]}"(R2), "+{v[216:247]}"(R3), "+{v[248:255]}"(R4));
    }
    const bool gv_on = tw == 0; (void)gv_on;
    const rsrc_t rs = __builtin_amdgcn_make_buffer_rsrc(ws + (sel & 1) * 8192, 0, COMM_BYTES - 8192, 0x00020000);
    const int fo = F_KVM + h * 256 + tw * 128;
    for (;;) {
        asm volatile(STEP_ASM
            : "+{v[96:97]}"(vm), "+{v[0:7]}"(S1), "+{v[8:15]}"(S2), "+{v[16:23]}"(S3), "+{v[24:31]}"(scon)
              , "+{a[0:31]}"(A0), "+{a[32:63]}"(A1), "+{a[64:95]}"(A2), "+{a[96:127]}"(A3), "+{a[128:159]}"(A4), "+{a[160:191]}"(A5), "+{a[192:223]}"(A6), "+{v[120:151]}"(R0), "+{v[152:183]}"(R1), "+{v[248:255]}"(R4)
            : "{a[224:255]}"(A7), "{v[184:215]}"(R2), "{v[216:247]}"(R3), "{v119}"(wo_at),
              [rs] "s"(rs), [rw] "s"(rw), [qb] "s"(OFF_OF + LATE * 262144 + h * 8192 + (w & 1) * 256),
              [tp] "s"(tp), [tq] "s"(tq_), [tx] "s"(tx), [u] "s"(U1), [ua] "s"(ua), [fw] "s"(8 * (a.xb >> 16 & 255)), [ru] "s"(__builtin_amdgcn_make_buffer_rsrc(a.out, 0, SEQ * DM * 2, 0x00020000))
            : STEP_CLOBBER);
        const int s = __builtin_amdgcn_readfirstlane(S1[0]);
        const unsigned st = __builtin_amdgcn_readfirstlane(S1[7]);
        kv_rdy = __builtin_amdgcn_readfirstlane(S1[2]); budget = __builtin_amdgcn_readfirstlane(S1[3]);
        if (s == NSEG - 1) break;
        if (__builtin_expect(st & 2, 0)) {
            const int lane = opaque_lane();
            LAS char* z = (LAS char*)0;
            const f32x4 zero = {0, 0, 0, 0};
            const u32x4 ones = __builtin_shufflevector(R0, R0, 0, 1, 2, 3);
            const unsigned zr = lane >> 6;
            const int idx = R0[4];
            u32x4 qf[4];
            f32x4 s1 = zero, s2 = zero;
#pragma unroll
            for (int ks = 0; ks < 4; ++ks) {
                qf[ks] = *(LAS u32x4*)(z + R0[10] + ks * 64);
                s1 = MFV(*(LAS u32x4*)(lds + L_KX + lane * 16 + ks * 2048), qf[ks], s1);
                s2 = MFV(*(LAS u32x4*)(lds + L_KX + lane * 16 + ks * 2048 + 1024), qf[ks], s2);
            }
            f32x4 s0 = SMF64(*(LAS u32x4*)(z + R0[11]), __builtin_shufflevector(qf[0], qf[1], 0, 1, 2, 3, 4, 5, 6, 7), zero, idx);
            s0 = SMF64(*(LAS u32x4*)(z + R0[11] + 128), __builtin_shufflevector(qf[2], qf[3], 0, 1, 2, 3, 4, 5, 6, 7), s0, idx);
            const float sa = s0[0] + s0[2], sb = s0[1] + s0[3];
            float mx = fmaxf(fmaxf(fmaxf(fmaxf(s1[0], s1[1]), fmaxf(s1[2], s1[3])), fmaxf(fmaxf(s2[0], s2[1]), fmaxf(s2[2], s2[3]))), fmaxf(sa, sb));
            mx = fmaxf(mx, __shfl_xor(mx, 16)); mx = fmaxf(mx, __shfl_xor(mx, 32));
            auto ex2 = [&](float x, float y) { return pack2(__builtin_amdgcn_exp2f(x - mx), __builtin_amdgcn_exp2f(y - mx)); };
            const u32x4 px = {ex2(s1[0], s1[1]), ex2(s1[2], s1[3]), ex2(s2[0], s2[1]), ex2(s2[2], s2[3])};
            const unsigned pm = ex2(sa, sb);
            const f32x4 l = MFV(((u32x4){pm, zr, zr, zr}), ones, MFV(px, ones, zero));
            const u32x2 x0 = *(LAS u32x2*)(z + R0[12]), y0 = *(LAS u32x2*)(z + R0[12] + 1024), x1 = *(LAS u32x2*)(z + R0[12] + 512), y1 = *(LAS u32x2*)(z + R0[12] + 1536);
            const f32x4 o = SMF64(px, ((u32x8){x0.x, y0.x, x0.y, y0.y, x1.x, y1.x, x1.y, y1.y}), zero, idx) + SMF32(((u32x2){pm, zr}), ((u32x4){vm.x, vm.y, zr, zr}), zero, idx);
            const float i0 = __builtin_amdgcn_rcpf(l[0]), i1 = __builtin_amdgcn_rcpf(l[1]);
            *(LAS unsigned*)(z + R0[9] + 256) = pack2(o[0] * i0, o[2] * i0);
            *(LAS unsigned*)(z + R0[9] + 256 + 576) = pack2(o[1] * i1, o[3] * i1);
            WG_BARRIER();
            const u32x4 oa = *(LAS u32x4*)(z + R0[11] + 256 + (w >> 1) * 128), ob = *(LAS u32x4*)(z + R0[11] + 384 - (w >> 1) * 128);
            auto frag = [&](int at) { return __builtin_shufflevector(*(LAS u32x4*)(z + at), *(LAS u32x4*)(z + at + 1024), 0, 1, 2, 3, 4, 5, 6, 7); };
            f32x2 f[4];
#pragma unroll
            for (int T = 0; T < 4; ++T) f[T] = fold(SMF64(ob, frag(R0[13] + scon[3] + 32768 + T * 2048), SMF64(oa, frag(R0[13] + scon[3] + T * 2048), zero, idx), idx));
            const f32x2 fx = fold(SMF64(ob, frag((R0[13] ^ 8192) + 49152 + 2048), SMF64(oa, frag(R0[13] + 49152 + 2048), zero, idx), idx));
            const int pb = (s & 1) * 2 * PART_STEP;
            st128((u32x4){pack2(f[0].x, f[1].x), pack2(f[2].x, f[3].x), pack2(f[0].y, f[1].y), pack2(f[2].y, f[3].y)}, rs, R0[14], pb);
            st32(pack2(fx.x, fx.y), rs, R0[15], pb);
            if (s == LATE) { S1[7] = 4; continue; }
        }
        if (__builtin_expect((st & 2) || kv_rdy <= s, 0)) {
            while (kv_rdy <= s) { kv_rdy = prog_min(rw, F_KV + h * 512 + w * 128); if (--budget < 0) break; }
            fetch_kv(s + 1, opaque_lane());
        }
        if (st & 2) { S1[1] = read_canaries(s, rs, opaque_lane(), budget) + S1[6]; S2[2] += 0x10000; }
        if ((int)(S1[1] - S2[1]) > 0x8000) { S2[1] = 8 * now() + 16; S3[0] = (unsigned)__builtin_readcyclecounter(); }
        S1[0] = s + 1; S1[2] = kv_rdy; S1[3] = budget;
    }
    TT(true, 0)
    st32((unsigned)(NSEG - 2), rw, opaque_lane() == 0 && w == 0 ? fo : OOB, 0);
    TT(true, 2)
}

__global__ void __launch_bounds__(256, 1) fused_kernel(Args a) {
    extern __shared__ __attribute__((aligned(16))) char smem[];
    LAS char* lds = (LAS char*)smem;
    const unsigned lds_base = (unsigned)(uintptr_t)lds;
    const int w = __builtin_amdgcn_readfirstlane(threadIdx.x >> 6);
    const int b = blockIdx.x, bx = b & 7, bi = b >> 3;
    if (bi >= 28) {
        const void* pc; unsigned len;
        asm volatile("s_getpc_b64 %0\n1:\ts_mov_b32 %1, .Lk_end-1b" : "=s"(pc), "=s"(len));
        touch(pc, min((bi - 28) * 32768 + threadIdx.x * 128, len), lds_base);
    }
    asm volatile("" :: "s"(a.Wq), "s"(a.cw));
    const rsrc_t rw = __builtin_amdgcn_make_buffer_rsrc(a.ws, 0, WS_USED, 0x00020000);
    int budget = SPIN_MAX;
#define RT(i)
    if (bx < 4) {
        if (bi < 16) role_head(a, lds, lds_base, 8 * bx + (bi >> 2) * 2 + (bi & 1), rw, budget);
        else role_owner(a, lds_base, 4 * (bi - 16) + bx, rw, budget);
    } else {
        const int fx = bx - 4, no = fx < 3 ? 14 : 22, j = 14 * fx + bi;
        if (bi >= no || (j & 2)) {
            const unsigned t = now() + (a.d1 & 0xffff);
            if (bi >= no) { if (w) prefetch(a, lds_base, bx, (bi - no) * 3 + w - 1, 3 * (32 - no)); }
            else {
                const int sh = (j >> 2) * 2 + (j & 1);
#pragma unroll
                for (int i = 0; i < 6; ++i) {
                    const int u = 6 * sh + i, c = u >> 3;
                    st128((u32x4){SENT, SENT, SENT, SENT}, rw, threadIdx.x * 16, (c < 18 ? c * 4 * PART_STEP : (c < 20 ? 2 * (OFF_MEMB + MEMB_STRIDE) - 18 * 32768 : 2 * OFF_CAN - 20 * 32768) + c * 32768) + (u & 7) * 4096);
                }
                const u32x2 tag = launch_tag(a);
                VM_DRAIN();
                WG_BARRIER();
                __builtin_amdgcn_raw_buffer_store_b64(tag, rw, threadIdx.x == 0 ? F_INIT + sh * 128 : OOB, 0, 16);
                RT(3)
            }
            (void)wait_until(t);
        }
        if (bi >= no) {
            const int t = 18 * fx + bi - no;
            role_s4d(a, lds, lds_base, t, rw, budget);
            RT(1)
            TT(true, 6)
            if (t < 9 * OUT_REP) role_outp(a, lds, lds_base, t % 9, t / 9, rw, budget);
        }
        else {
            if (!(j & 2)) role_kv(a, lds, lds_base, (j >> 2) * 2 + (j & 1), rw, budget);
            RT(1)
            TT(true, 6)
            role_qa(a, lds, lds_base, (j >> 2) * 2, (j & 2 ? 0 : FAST + 2) + (j & 1), (j & 2 ? FAST : FAST + 2) + (j & 1), rw, budget);
        }
    }
    RT(2)
    TT(true, 5)
    if (budget < 0 && threadIdx.x == 0) st32(0x1000u + b, rw, F_ERR, 0);
    asm volatile(".Lk_end:");
}

static int g_c1 = 34, g_c1m = 30, g_c1x = 46, g_c2 = 316, g_b0 = 32, g_up = 8, g_fl = 8, g_dn = 2, g_d0 = 1680, g_da = 200, g_ua = 48, g_d1 = 300, g_rd = 34, g_cw = 69, g_e0 = 112, g_x1 = 40, g_x2 = 140, g_x3 = 150, g_x4 = 75, g_x5 = 135, g_x6 = 157, g_x7 = 35, g_x8 = 210, g_pc = 45, g_cc = 0;
extern "C" void kernel_launch(void* const* d_in, const int* in_sizes, int n_in, void* d_out, int out_size, void* d_ws, size_t ws_size, hipStream_t stream) {
    Args a;
    a.x = (const u16*)d_in[0]; a.mem0 = (const u16*)d_in[1];
    a.Wq = (const u16*)d_in[2]; a.Wk = (const u16*)d_in[3]; a.Wv = (const u16*)d_in[4]; a.Wo = (const u16*)d_in[5];
    a.lAr = (const u16*)d_in[8]; a.Aim = (const u16*)d_in[9]; a.Cr = (const u16*)d_in[10]; a.Ci = (const u16*)d_in[11];
    a.ldt = (const u16*)d_in[12]; a.Dsk = (const u16*)d_in[13]; a.gw = (const u16*)d_in[14];
    a.out = (u16*)d_out;
    a.ws = (char*)(((uintptr_t)d_ws + ((2u << 20) - 1)) & ~(uintptr_t)((2u << 20) - 1));
    a.c1 = g_c1 | g_c1m << 8 | g_c1x << 16 | g_cc << 24; a.c2 = g_c2; a.d0 = g_d0 + g_da; a.b0 = g_b0; a.d1 = g_d1 | g_pc << 16; a.up = g_up | g_ua << 8 | g_fl << 16; a.dn = g_dn | g_d0 << 8; a.rd = 23 * g_rd - 300; a.cw = 8 * g_cw - 104; a.e0 = 8 * g_e0;
    a.xa = g_x1 | g_x2 << 8 | g_x5 << 16 | g_x7 << 24; a.xb = g_x6 | g_x3 << 8 | g_x4 << 16 | g_x8 << 24;
    (void)hipFuncSetAttribute((const void*)fused_kernel, hipFuncAttributeMaxDynamicSharedMemorySize, L_TOTAL);
    fused_kernel<<<256, 256, L_TOTAL, stream>>>(a);
}
```
